```python
import torch

def _softmax(x, axis=-1):
    return torch.softmax(x, dim=axis)

def _gelu(x):
    return torch.nn.functional.gelu(x, approximate='tanh')

def _silu(x):
    return torch.nn.functional.silu(x)

def _sigmoid(x):
    return torch.sigmoid(x)

def _layernorm(x, axis=-1, eps=1e-05):
    m = x.mean(dim=axis, keepdim=True)
    v = x.var(dim=axis, keepdim=True, unbiased=False)
    return (x - m) / torch.sqrt(v + eps)

def _scatter_add(tgt, idx, src, axis=0):
    return tgt.clone().index_add_(axis, idx, src)

def _segment_sum(src, seg, n):
    out = torch.zeros((n,) + src.shape[1:], dtype=src.dtype)
    return out.index_add_(0, seg, src)

def _pad(x, pw):
    flat = tuple((v for pair in reversed(pw) for v in pair))
    return torch.nn.functional.pad(x, flat)
kernel_name = 'interleave_layers__ssm_conv_s4d__memory_aug_recurrent_mem_tr__prompt'

def setup_inputs():

    _w = lambda a: (lambda t: t.to(torch.bfloat16) if t.is_floating_point() else t)(torch.as_tensor(a))

    def _A_setup():
        Bsz, L, D, N = (1, 2048, 512, 128)
        return {'u': _w((lambda *s: torch.randn(*s, dtype=torch.bfloat16) * 0.05)(Bsz, L, D)), 'log_A_real': _w((lambda *s: torch.randn(*s, dtype=torch.bfloat16) * 3.2)(D, N) * 0.5), 'A_imag': _w((lambda *s: torch.randn(*s, dtype=torch.bfloat16) * 0.05)(D, N) * 3.14), 'C_real': _w((lambda *s: torch.randn(*s, dtype=torch.bfloat16) * 0.05)(D, N) / N ** 0.5), 'C_imag': _w((lambda *s: torch.randn(*s, dtype=torch.bfloat16) * 0.05)(D, N) / N ** 0.5), 'log_dt': _w((lambda *s: torch.randn(*s, dtype=torch.bfloat16) * 0.05)(D) * 0.5 - 4.0), 'D_skip': _w((lambda *s: torch.randn(*s, dtype=torch.bfloat16) * 0.05)(D))}
    _A_in = _A_setup()

    def _B_setup():
        B, S, Dm = (1, 2048, 512)
        H, Dh = (32, 128)
        M, seg = (8, 32)
        seg = min(seg, S)
        S = S // seg * seg
        return {'x': _w((lambda *s: torch.randn(*s, dtype=torch.bfloat16) * 0.05)(B, S, Dm)), 'mem0': _w((lambda *s: torch.randn(*s, dtype=torch.bfloat16) * 0.8)(B, M, Dm)), 'Wq': _w((lambda *s: torch.randn(*s, dtype=torch.bfloat16) * 1.6)(Dm, H, Dh) * Dm ** (-0.5)), 'Wk': _w((lambda *s: torch.randn(*s, dtype=torch.bfloat16) * 1.6)(Dm, H, Dh) * Dm ** (-0.5)), 'Wv': _w((lambda *s: torch.randn(*s, dtype=torch.bfloat16) * 0.8)(Dm, H, Dh) * Dm ** (-0.5)), 'Wo': _w((lambda *s: torch.randn(*s, dtype=torch.bfloat16) * 0.8)(H, Dh, Dm) * (H * Dh) ** (-0.5)), 'seg_len': seg}
    _B_in = _B_setup()
    return {'A_u': _A_in['u'], 'A_log_A_real': _A_in['log_A_real'], 'A_A_imag': _A_in['A_imag'], 'A_C_real': _A_in['C_real'], 'A_C_imag': _A_in['C_imag'], 'A_log_dt': _A_in['log_dt'], 'A_D_skip': _A_in['D_skip'], 'B_x': _B_in['x'], 'B_mem0': _B_in['mem0'], 'B_Wq': _B_in['Wq'], 'B_Wk': _B_in['Wk'], 'B_Wv': _B_in['Wv'], 'B_Wo': _B_in['Wo'], 'B_seg_len': _B_in['seg_len']}

def reference(A_u, A_log_A_real, A_A_imag, A_C_real, A_C_imag, A_log_dt, A_D_skip, B_x, B_mem0, B_Wq, B_Wk, B_Wv, B_Wo, B_seg_len):

    def _A_ref(u, log_A_real, A_imag, C_real, C_imag, log_dt, D_skip):
        L = u.shape[1]
        dt = torch.exp(log_dt)
        A = -torch.exp(log_A_real) + 1j * A_imag
        C = (C_real + 1j * C_imag) * dt[:, None]
        pos = (lambda x, dt: x.to(dt))(torch.arange(L), log_dt.dtype)
        P = torch.exp((A * dt[:, None])[:, :, None] * pos[None, None, :])
        K = 2.0 * torch.einsum('dn,dnl->dl', C, P).real
        L2 = 2 * L
        u_t = (lambda x, *d: x.permute(*d))(u, 0, 2, 1)
        Kf = (lambda x, *a, **k: torch.fft.rfft(x.float(), *a, **k))(K, L2)
        Uf = (lambda x, *a, **k: torch.fft.rfft(x.float(), *a, **k))(u_t, L2)
        y = (lambda x, *a, **k: torch.fft.irfft(x, *a, **k).to(torch.bfloat16))(Uf * Kf[None, :, :], L2)[:, :, :L]
        y = (lambda x, *d: x.permute(*d))(y, 0, 2, 1)
        return y + u * D_skip[None, None, :]

    def _B_ref(x, mem0, Wq, Wk, Wv, Wo, seg_len):
        B, S, Dm = x.shape
        H, Dh = (Wq.shape[1], Wq.shape[2])
        M = mem0.shape[1]
        scale = Dh ** (-0.5)
        n_seg = S // seg_len
        L = M + seg_len
        idx = torch.arange(L)
        mask = (idx[:, None] < M) | (idx[:, None] >= idx[None, :])
        mem = mem0
        outs = []
        for s in range(n_seg):
            xs = x[:, s * seg_len:(s + 1) * seg_len, :].clone()
            h = torch.concatenate([mem, xs], axis=1)
            q = torch.einsum('btd,dhe->bhte', h, Wq)
            k = torch.einsum('btd,dhe->bhte', h, Wk)
            v = torch.einsum('btd,dhe->bhte', h, Wv)
            scores = torch.einsum('bhqe,bhke->bhqk', q, k) * scale
            scores = torch.where(mask[None, None], scores, -1000000000.0)
            p = _softmax(scores, axis=-1)
            o = torch.einsum('bhqk,bhke->bhqe', p, v)
            o = torch.einsum('bhte,hed->btd', o, Wo)
            mem = o[:, :M, :].clone()
            outs.append(o[:, M:, :])
        return torch.concatenate(outs, axis=1)
    x = A_u
    x = x + _A_ref(_layernorm(x, axis=-1), A_log_A_real, A_A_imag, A_C_real, A_C_imag, A_log_dt, A_D_skip)
    x = x + _B_ref(_layernorm(x, axis=-1), B_mem0, B_Wq, B_Wk, B_Wv, B_Wo, B_seg_len)
    x = x + _A_ref(_layernorm(x, axis=-1), A_log_A_real, A_A_imag, A_C_real, A_C_imag, A_log_dt, A_D_skip)
    x = x + _B_ref(_layernorm(x, axis=-1), B_mem0, B_Wq, B_Wk, B_Wv, B_Wo, B_seg_len)
    return x
```

```cpp
#include <hip/hip_runtime.h>
#include <hip/hip_bf16.h>
#include <stdint.h>

constexpr int L_SEQ = 2048;
constexpr int DM    = 512;
constexpr int NST   = 128;
constexpr int NH    = 32;
constexpr int DH    = 128;
constexpr int HD    = NH * DH;
constexpr int MM    = 8;
constexpr int SEG   = 32;
constexpr int NSEG  = L_SEQ / SEG;
constexpr int LK    = MM + SEG;

typedef unsigned short u16;
using bf16x8 = __attribute__((ext_vector_type(8))) short;
using f32x4  = __attribute__((ext_vector_type(4))) float;
using f32x16 = __attribute__((ext_vector_type(16))) float;

static __device__ __forceinline__ u16 f2bf(float f) {
  unsigned u = __float_as_uint(f);
  u += 0x7fffu + ((u >> 16) & 1u);
  return (u16)(u >> 16);
}
static __device__ __forceinline__ float bf2f(u16 h) { return __uint_as_float(((unsigned)h) << 16); }
static __device__ __forceinline__ float bfr(float f) { return bf2f(f2bf(f)); }

constexpr size_t SZ_X      = (size_t)L_SEQ * DM * 2;
constexpr size_t OFF_KF    = 0;
constexpr size_t OFF_UT    = OFF_KF + (size_t)DM * L_SEQ * 4;
constexpr size_t OFF_YT    = OFF_UT + SZ_X;
constexpr size_t OFF_XA    = OFF_YT + SZ_X;
constexpr size_t OFF_H     = OFF_XA + SZ_X;
constexpr size_t OFF_X2    = OFF_H + SZ_X;
constexpr size_t OFF_U2    = OFF_X2 + SZ_X;
constexpr size_t OFF_X3    = OFF_U2 + SZ_X;
constexpr size_t SZ_QK     = (size_t)L_SEQ * 2 * HD * 2;
constexpr size_t OFF_QK    = OFF_X3 + SZ_X;
constexpr size_t SZ_O      = (size_t)L_SEQ * HD * 2;
constexpr size_t OFF_VF    = OFF_QK + 2 * SZ_QK;
constexpr size_t SZ_PW     = (size_t)2 * NSEG * 16 * SEG * DM * 2;
constexpr size_t OFF_PW    = OFF_VF + 2 * SZ_O;
constexpr size_t SZ_MEM    = (size_t)NSEG * MM * HD * 2;
constexpr size_t OFF_MEMK  = OFF_PW + SZ_PW;
constexpr size_t OFF_MEMV  = OFF_MEMK + 2 * SZ_MEM;
constexpr size_t OFF_GT    = OFF_MEMV + 2 * SZ_MEM;
constexpr size_t SZ_ACC    = (size_t)2 * 32768;
constexpr size_t OFF_Z     = OFF_GT + (size_t)64 * DM * 4;
constexpr size_t OFF_ACC   = OFF_Z;
constexpr size_t OFF_FL    = OFF_ACC + 2 * SZ_ACC;
constexpr int FL_FK  = 0;
constexpr int FL_FW  = FL_FK + 2 * 64 * 32 * 32;
constexpr int FL_FR  = FL_FW + 2 * 64 * 32 * 32;
constexpr int FL_FC  = FL_FR + 64 * 32 * 32;
constexpr int FL_FQ  = FL_FC + 2 * 64 * 64 * 32;
constexpr int FL_FS  = FL_FQ + 2 * 64 * 32 * 32;
constexpr int FL_F0  = FL_FS + 8 * 32 * 32;
constexpr int FL_FZ  = FL_F0 + 8 * 8 * 32;
constexpr int FL_FN  = FL_FZ + 16 * 32;
constexpr int FL_KG  = FL_FN + 2 * 64 * 16 * 32;
constexpr int FL_G   = FL_KG + 64 * 32;
constexpr int FL_TMO = FL_G + 2 * 8 * 32;
constexpr int FL_END = FL_TMO + 64;
constexpr size_t Z_BYTES   = OFF_FL + (size_t)FL_END * 4 - OFF_Z;
constexpr size_t WS_TOTAL  = OFF_Z + Z_BYTES;
static_assert(Z_BYTES % 16 == 0 && OFF_Z % 256 == 0, "zero region");
static_assert(WS_TOTAL + (4 << 20) <= (size_t)256 * 1024 * 1024, "workspace too large");

static_assert(WS_TOTAL - OFF_XA <= (size_t)384 * 8 * 65536, "translation warm-up");
constexpr int CKN = 2208;
static_assert((size_t)DM * 2 * CKN * 2 <= (size_t)DM * L_SEQ * 4 + SZ_X, "K buffer");
__device__ __forceinline__ float pos_of_index(int p) {
  if (p < 256) return (float)p;
  if (p < 384) return (float)(256 + 2 * (p - 256));
  if (p < 512) return (float)(512 + 4 * (p - 384));
  if (p < 640) return (float)(1024 + 8 * (p - 512));
  return 2048.0f;
}
__device__ __forceinline__ int index_of_l(int l) {
  float pf = bfr((float)l);
  int p = (int)pf;
  if (p < 256) return p;
  if (p < 512) return 256 + ((p - 256) >> 1);
  if (p < 1024) return 384 + ((p - 512) >> 2);
  if (p < 2048) return 512 + ((p - 1024) >> 3);
  return 640;
}

template <int CTRL> __device__ __forceinline__ float dppr(float x) {
  return __builtin_bit_cast(float, __builtin_amdgcn_update_dpp(0, __builtin_bit_cast(int, x), CTRL, 0xf, 0xf, false));
}
__device__ __forceinline__ float wave_sum(float v) {
  v += dppr<0x121>(v);
  v += dppr<0x122>(v);
  v += dppr<0x124>(v);
  v += dppr<0x128>(v);
  {
    auto r = __builtin_amdgcn_permlane16_swap(__float_as_uint(v), __float_as_uint(v), false, false);
    v = __uint_as_float(r[0]) + __uint_as_float(r[1]);
  }
  {
    auto r = __builtin_amdgcn_permlane32_swap(__float_as_uint(v), __float_as_uint(v), false, false);
    v = __uint_as_float(r[0]) + __uint_as_float(r[1]);
  }
  return v;
}
__device__ __forceinline__ void ln_row8(float (&v)[8]) {
  float s = 0.f;
#pragma unroll
  for (int j = 0; j < 8; ++j) s += v[j];
  s = wave_sum(s);
  const float mean = s * (1.0f / DM);
  float q = 0.f;
#pragma unroll
  for (int j = 0; j < 8; ++j) { float t = v[j] - mean; q += t * t; }
  q = wave_sum(q);
  const float var = q * (1.0f / DM);
  const float mb = bfr(mean);
  const float sd = bfr(sqrtf(bfr(bfr(var) + 1e-5f)));
#pragma unroll
  for (int j = 0; j < 8; ++j) v[j] = bfr(bfr(v[j] - mb) / sd);
}

constexpr int ZBLK = 512;
__global__ __launch_bounds__(256) void zero_kernel(uint4* __restrict__ zero) {
  for (size_t i = (size_t)blockIdx.x * 256 + threadIdx.x; i < Z_BYTES / 16; i += (size_t)ZBLK * 256) zero[i] = uint4{0u, 0u, 0u, 0u};
}

#define RLX_AGENT __ATOMIC_RELAXED, __HIP_MEMORY_SCOPE_AGENT
typedef __attribute__((address_space(1))) unsigned gu32;
typedef unsigned v4u __attribute__((ext_vector_type(4)));
constexpr int AUX_SC1 = 16;
constexpr unsigned SPIN_CAP = 1u << 18;

__device__ __forceinline__ bool wait_flags(const unsigned* flags, int lane, int nfl, unsigned want, unsigned* tmo) {
  for (unsigned spins = 0;; ++spins) {
    unsigned v = want;
    if (lane < nfl) v = __hip_atomic_load((gu32*)flags + lane * 32, RLX_AGENT);
    if (__all(v >= want)) break;
    if (spins > SPIN_CAP) {
      if (lane == 0) __hip_atomic_store((gu32*)tmo, 1u, RLX_AGENT);
      return false;
    }
  }
  __builtin_amdgcn_fence(__ATOMIC_ACQUIRE, "wavefront");
  return true;
}

#define STAMP(i)
#define STAMP_DUMP()
#define WSTAMP(i)
template <int CTRL> __device__ __forceinline__ float dppf(float old, float x) {
  return __builtin_bit_cast(float, __builtin_amdgcn_update_dpp(__builtin_bit_cast(int, old), __builtin_bit_cast(int, x), CTRL, 0xf, 0xf, false));
}
__device__ __forceinline__ float wave_max_dpp(float x) {
  const float ninf = -3e38f;
  x = fmaxf(x, dppf<0x111>(ninf, x));
  x = fmaxf(x, dppf<0x112>(ninf, x));
  x = fmaxf(x, dppf<0x114>(ninf, x));
  x = fmaxf(x, dppf<0x118>(ninf, x));
  x = fmaxf(x, dppf<0x142>(ninf, x));
  x = fmaxf(x, dppf<0x143>(ninf, x));
  return __builtin_bit_cast(float, __builtin_amdgcn_readlane(__builtin_bit_cast(int, x), 63));
}
__device__ __forceinline__ float wave_sum_dpp(float x) {
  x += dppf<0x111>(0.f, x);
  x += dppf<0x112>(0.f, x);
  x += dppf<0x114>(0.f, x);
  x += dppf<0x118>(0.f, x);
  x += dppf<0x142>(0.f, x);
  x += dppf<0x143>(0.f, x);
  return __builtin_bit_cast(float, __builtin_amdgcn_readlane(__builtin_bit_cast(int, x), 63));
}
constexpr int SMS = 528;
constexpr int QS = 144;
constexpr int VS = 80;
constexpr int SOS = 144;
#define VAR_NREP 4
constexpr int NREP = VAR_NREP;
__device__ __forceinline__ float xsum16(float x) {
  auto r = __builtin_amdgcn_permlane16_swap(__float_as_uint(x), __float_as_uint(x), false, false);
  return __uint_as_float(r[0]) + __uint_as_float(r[1]);
}
__device__ __forceinline__ float xsum32(float x) {
  auto r = __builtin_amdgcn_permlane32_swap(__float_as_uint(x), __float_as_uint(x), false, false);
  return __uint_as_float(r[0]) + __uint_as_float(r[1]);
}
__device__ __forceinline__ float xmax16(float x) {
  auto r = __builtin_amdgcn_permlane16_swap(__float_as_uint(x), __float_as_uint(x), false, false);
  return fmaxf(__uint_as_float(r[0]), __uint_as_float(r[1]));
}
__device__ __forceinline__ float xmax32(float x) {
  auto r = __builtin_amdgcn_permlane32_swap(__float_as_uint(x), __float_as_uint(x), false, false);
  return fmaxf(__uint_as_float(r[0]), __uint_as_float(r[1]));
}
__device__ __forceinline__ float red_mod4(float x) {
  x += dppf<0x124>(0.f, x);
  x += dppf<0x128>(0.f, x);
  return xsum32(xsum16(x));
}
typedef __bf16 bf16x2_t __attribute__((ext_vector_type(2)));
typedef float f32x2_t __attribute__((ext_vector_type(2)));
__device__ __forceinline__ unsigned pk2(float a, float b) {
  f32x2_t v = {a, b};
  bf16x2_t r = __builtin_convertvector(v, bf16x2_t);
  return __builtin_bit_cast(unsigned, r);
}
__device__ __forceinline__ float one(float x) {
  asm("" : "+v"(x));
  return x;
}
__device__ __forceinline__ unsigned pk2s(float a, float b) { return pk2(one(a), one(b)); }
__device__ __forceinline__ float dot2sq(const unsigned x, const float acc) {
  return __builtin_amdgcn_fdot2_f32_bf16(__builtin_bit_cast(bf16x2_t, x), __builtin_bit_cast(bf16x2_t, x), acc, false);
}
__device__ __forceinline__ float lo16(unsigned u) { return __uint_as_float(u << 16); }
__device__ __forceinline__ float hi16(unsigned u) { return __uint_as_float(u & 0xffff0000u); }
__device__ __forceinline__ void scale_pair(float a, float b, float scale, float& oa, float& ob) {
  unsigned u = pk2(a, b);
  unsigned v = pk2s(lo16(u) * scale, hi16(u) * scale);
  oa = lo16(v);
  ob = hi16(v);
}

#define XMAP 2
__device__ __forceinline__ int vbid() {
  const int b = blockIdx.x, x = b & 7, t = b >> 3;
  if (XMAP == 0) {
    if (b < 64) return (b & 32) + 4 * x + (t & 3);
    if (b < 96) return (b & ~15) + 2 * x + (t & 1);
    if (b >= 104 && b < 168) return 104 + 8 * x + t - 13;
    if (b >= 168 && b < 232) return 168 + ((b - 168) & 32) + 4 * x + ((t - 21) & 3);
    return b;
  }
  if (XMAP == 2) {
    if (x < 4) return t < 16 ? 4 * (t & 8) + 8 * x + (t & 7) : 104 + 16 * x + t - 16;
    const int y = x - 4;
    if (t >= 24) return (y == 0 ? 96 : 224 + 8 * y) + t - 24;
    if (t < 16) return 168 + 4 * (t & 8) + 8 * y + (t & 7);
    return 64 + 4 * (t & 4) + 4 * y + (t & 3);
  }
  if (x < 2) return 2 * (t & 16) + 16 * x + (t & 15);
  const int i = 16 * (x - 2) + (t & 15);
  if (t < 16) {
    const int k = (i * 171) >> 9, r = i - 3 * k;
    return r == 0 ? 168 + k : r == 1 ? 200 + k : 64 + 16 * (k & 1) + (k >> 1);
  }
  const int g = i >> 3, e = i & 7;
  if (g == 3) return 96 + e;
  if (g == 7) return 232 + e;
  if (g == 9) return 240 + e;
  if (g == 11) return 248 + e;
  return 104 + 8 * (g - (g > 3) - (g > 7) - (g > 9)) + e;
}
#define MSTAMP()
#define WSTAMP2()
#define SST(k)
#define TS(k, ss, t)
#define LS(k, ss)
#define LS_DUMP()
struct MegaArgs {
  const u16* mem0;
  const u16* Dskip;
  char* ws;
  u16* out;
  const u16* x0;
  const u16 *logAre, *Aim, *Cre, *Cim, *logdt;
  const u16 *Wq, *Wk, *Wv, *Wo;
  int vb;
};

__device__ __forceinline__ int lane_id() {
  int l;
  asm volatile("v_mbcnt_lo_u32_b32 %0, -1, 0\n\tv_mbcnt_hi_u32_b32 %0, -1, %0" : "=v"(l));
  return l;
}
__device__ __forceinline__ bool spin_timeout(unsigned spins, unsigned* tmo) {
  if (spins > SPIN_CAP) {
    if (lane_id() == 0) __hip_atomic_store((gu32*)tmo, 1u, RLX_AGENT);
    return true;
  }
  return false;
}
#define CPRIO 1
#define P1PRIO 3
#define QLEAD 2
#define POLL_SLEEP 12
#define SLP_C POLL_SLEEP
#define SLP_A POLL_SLEEP
#define SLP_W POLL_SLEEP
#define SLP_R POLL_SLEEP
#define SLP_V POLL_SLEEP
#define SLP_Q POLL_SLEEP
#define SLP_M 4
#define AWPRIO 2
#define SLP_S 8
#define SPOS 4
#define CVPRE 2
#define QPD 3
#define QPRI 2
#define TLBW 1
#define QHV 0
#define QCHK 0
#define LEADN 99
#define GATE_V 200
#define GATE_R 440
#define GATE_N 440
#define AWAHEAD 1
#define GATE_M 150
#define PRI_A 3
#define PRI_WA 2
#define PRI_WB 1
#define AWSKIP 3
#define PRI_0 3
#define AWDEF 0
#define SLP_F 2
#define EXP_OFF 0
#define AWSPEC 1
#define CVTHR 48
#define NR_C 8
#define NR_R 8
#define NR_W 8
#define NR_N 2
#define QAHEAD 0
#define NS0 4
#define SLP_N POLL_SLEEP
template <int SLP, int WPL>
__device__ __forceinline__ bool wait_n(const unsigned* f, int n, unsigned* tmo) {
  const int l = lane_id();
  for (unsigned spins = 0;; ++spins) {
    unsigned v = 1u;
    if (l < n) v = __hip_atomic_load((gu32*)f + (l / WPL) * 32 + l % WPL, RLX_AGENT);
    if (__all(v != 0u)) break;
    if (spin_timeout(spins, tmo)) return false;
    __builtin_amdgcn_s_sleep(SLP);
  }
  asm volatile("" ::: "memory");
  return true;
}
__device__ __forceinline__ unsigned now10() { return (unsigned)__builtin_amdgcn_s_memrealtime(); }
__device__ __forceinline__ void gate(const unsigned t) {
  for (int i = 0; i < 64; ++i) {
    const int d = (int)(t - now10());
    if (d <= 0) break;
    if (d > 100) __builtin_amdgcn_s_sleep(32);
    else if (d > 25) __builtin_amdgcn_s_sleep(8);
    else __builtin_amdgcn_s_sleep(2);
  }
}
template <int SLP, int WPL, int GT>
__device__ __forceinline__ void grp_wait(const bool leader, volatile unsigned* sw, const unsigned val, const unsigned* f, int n,
                                         unsigned* tmo, unsigned& tl) {
  if (leader) {
    const int l = lane_id();
    for (unsigned spins = 0;; ++spins) {
      unsigned v = 1u;
      if (l < n) v = __hip_atomic_load((gu32*)f + (l / WPL) * 32 + l % WPL, RLX_AGENT);
      if (__all(v != 0u) || spin_timeout(spins, tmo)) break;
      if (GT && spins == 0) gate(tl);
      else __builtin_amdgcn_s_sleep(SLP);
    }
    if (GT) tl = now10() + GT;
    if ((threadIdx.x & 63) == 0) *sw = val;
  } else {
    for (unsigned spins = 0; *sw < val; ++spins) {
      if (spins > 64u * SPIN_CAP) break;
      if (GT && spins == 0) gate(tl);
      __builtin_amdgcn_s_sleep(SLP_F);
    }
    if (GT) tl = now10() + GT;
  }
  asm volatile("" ::: "memory");
}
template <int SLP, int WPL>
__device__ __forceinline__ bool wg_wait(const unsigned* f, int n, unsigned* tmo) {
  if (threadIdx.x < 64) (void)wait_n<SLP, WPL>(f, n, tmo);
  __syncthreads();
  asm volatile("" ::: "memory");
  return true;
}
__device__ __forceinline__ void set_flag(unsigned* f) { __hip_atomic_store((gu32*)f, 1u, RLX_AGENT); }
#define DRAIN() asm volatile("s_waitcnt vmcnt(0)" ::: "memory")
#define MKRS(ptr, bytes) __builtin_amdgcn_make_buffer_rsrc((void*)(ptr), 0, (int)(bytes), 0x00020000)
typedef unsigned v2u __attribute__((ext_vector_type(2)));
#define LD128(rs, voff, soff) __builtin_amdgcn_raw_buffer_load_b128(rs, voff, soff, AUX_SC1)
#define LD64(rs, voff, soff) __builtin_amdgcn_raw_buffer_load_b64(rs, voff, soff, AUX_SC1)
#define ST128(v, rs, voff, soff)                                              \
  do {                                                                        \
    const v4u st_d_ = (v);                                                    \
    __builtin_amdgcn_raw_buffer_store_b128(st_d_, rs, voff, soff, AUX_SC1);   \
    asm volatile("s_nop 1" ::"v"(st_d_) : "memory");                          \
  } while (0)
#define ST64(v, rs, voff, soff) __builtin_amdgcn_raw_buffer_store_b64(v, rs, voff, soff, AUX_SC1)
#define BF8(x) __builtin_bit_cast(bf16x8, x)
#define MFMA16(a, b, c) __builtin_amdgcn_mfma_f32_16x16x32_bf16(a, b, c, 0, 0, 0)
typedef short bf16x4 __attribute__((ext_vector_type(4)));
#define MFMA16K(a, b, c) \
  __builtin_amdgcn_mfma_f32_16x16x16bf16_1k(__builtin_bit_cast(bf16x4, a), __builtin_bit_cast(bf16x4, b), c, 0, 0, 0)
__device__ __forceinline__ float fmax_nn(float a, float b) { return __builtin_elementwise_maximum(a, b); }
typedef __bf16 bfv8 __attribute__((ext_vector_type(8)));
typedef __bf16 bfv16 __attribute__((ext_vector_type(16)));
typedef unsigned v8u __attribute__((ext_vector_type(8)));
#define SMF(a, b, c, idx) \
  __builtin_amdgcn_smfmac_f32_16x16x64_bf16(__builtin_bit_cast(bfv8, a), __builtin_bit_cast(bfv16, b), c, idx, 0, 0)
__device__ __forceinline__ float swap_add32(float a, float b) {
  auto r = __builtin_amdgcn_permlane32_swap(__float_as_uint(a), __float_as_uint(b), false, false);
  return __uint_as_float(r[0]) + __uint_as_float(r[1]);
}
__device__ __forceinline__ float swap_add16(float a, float b) {
  auto r = __builtin_amdgcn_permlane16_swap(__float_as_uint(a), __float_as_uint(b), false, false);
  return __uint_as_float(r[0]) + __uint_as_float(r[1]);
}
__device__ __forceinline__ v4u smf_fix(const v4u d) {
  auto x = __builtin_amdgcn_permlane16_swap(d.x, d.z, false, false);
  auto y = __builtin_amdgcn_permlane16_swap(d.y, d.w, false, false);
  return v4u{x[0], x[1], y[0], y[1]};
}
__device__ __forceinline__ v8u smf_bfrag(const u16* p) {
  const v2u a = *reinterpret_cast<const v2u*>(p), b = *reinterpret_cast<const v2u*>(p + 8);
  const v2u c = *reinterpret_cast<const v2u*>(p + 32), d = *reinterpret_cast<const v2u*>(p + 40);
  return v8u{a.x, b.x, a.y, b.y, c.x, d.x, c.y, d.y};
}

#define VAR_PPA 0
#define VAR_PPB 0
#define VAR_THR 0
#define VAR_THR2 0
#define CV_LEAD 100
#define VAR_EARLY 1
#define VAR_DRAIN 1
#define VAR_D1 0
#define VAR_D2 0
#define VAR_DR2 1
#define VAR_S1 0
#define VAR_S2 0
typedef __attribute__((address_space(1))) unsigned long long gu64;
typedef __attribute__((address_space(1))) char gch;
__device__ __forceinline__ int cvt_rpi(float x) {
  int r;
  asm("v_cvt_rpi_i32_f32 %0, %1" : "=v"(r) : "v"(x));
  return r;
}
typedef short v4s __attribute__((ext_vector_type(4)));
typedef __attribute__((address_space(3))) v4s* lp4s;
__device__ __forceinline__ void glds16(const void* sbase, const unsigned voff, const unsigned lds) {
  unsigned keep;
  asm volatile("s_nop 4\n\ts_mov_b32 %0, m0\n\ts_mov_b32 m0, %3\n\ts_nop 0\n\tglobal_load_lds_dwordx4 %1, %2\n\ts_mov_b32 m0, %0"
               : "=&s"(keep)
               : "v"(voff), "s"(sbase), "s"(lds)
               : "memory");
}
__device__ __forceinline__ void glds16c(const void* sbase, const unsigned voff, const unsigned lds) {
  unsigned keep;
  asm volatile("s_nop 4\n\ts_mov_b32 %0, m0\n\ts_mov_b32 m0, %3\n\ts_nop 0\n\tglobal_load_lds_dwordx4 %1, %2 sc1\n\ts_mov_b32 m0, %0"
               : "=&s"(keep)
               : "v"(voff), "s"(sbase), "s"(lds)
               : "memory");
}
__device__ __forceinline__ v2u trd(const unsigned a) {
  return __builtin_bit_cast(v2u, __builtin_amdgcn_ds_read_tr16_b64_v4i16((lp4s)(unsigned long)a));
}
__device__ __forceinline__ unsigned lds_base(char* smem) { return (unsigned)(unsigned long)(__attribute__((address_space(3))) char*)smem; }
#define RAW_BAR()                                           \
  do {                                                      \
    asm volatile("s_waitcnt lgkmcnt(0)" ::: "memory");      \
    __builtin_amdgcn_s_barrier();                           \
    asm volatile("" ::: "memory");                          \
  } while (0)
#define WAIT_VM(n) asm volatile("s_waitcnt vmcnt(" #n ")" ::: "memory")
template <bool SP>
__device__ __forceinline__ void wqkv_issue(const u16* Wm, const int hf, const int h, const int w, const int l, const unsigned sb) {
  const char* base = (const char*)Wm + (size_t)(256 * hf + 32 * w) * (HD * 2) + h * (DH * 2);
#pragma unroll
  for (int i = 0; i < 8; ++i) {
    const int r = l >> 4;
    const int f = SP ? ((r & 1) | ((i & 1) << 1) | (((i >> 1) & 1) << 2)) : (r | (((i >> 1) & 1) << 2));
    glds16(base, (unsigned)((4 * i + r) * (HD * 2) + (((l & 15) ^ (f << 1)) << 4)), sb + w * 16384 + hf * 8192 + i * 1024);
  }
}

__device__ __forceinline__ unsigned row_stats(const v4u raw) {
  float v[8];
#pragma unroll
  for (int e = 0; e < 4; ++e) { v[2 * e] = lo16(raw[e]); v[2 * e + 1] = hi16(raw[e]); }
  float s = 0.f;
#pragma unroll
  for (int e = 0; e < 8; ++e) s += v[e];
  const float mean = wave_sum(s) * (1.0f / DM);
  float q = 0.f;
#pragma unroll
  for (int e = 0; e < 8; ++e) { const float t = v[e] - mean; q += t * t; }
  const float var = wave_sum(q) * (1.0f / DM);
  const float sd = bfr(sqrtf(bfr(bfr(var) + 1e-5f)));
  return (unsigned)f2bf(mean) | ((unsigned)f2bf(sd) << 16);
}
__device__ __forceinline__ void start_kgen(const MegaArgs& A, char* smem, const int hi, const int xi) {
  const int tid = threadIdx.x, j = tid >> 7, u = tid & 127, d = 4 * hi + j;
  const int w = __builtin_amdgcn_readfirstlane(tid >> 6), l = tid & 63;
  float* prm = reinterpret_cast<float*>(smem) + j * 10 * NST;
  float* part = reinterpret_cast<float*>(smem) + 40 * NST + j * 8 * 400;
  float* kd = reinterpret_cast<float*>(smem) + 40 * NST + 32 * 400 + j * 392;
  v4u raw[8], ra, rb;
  const int r0 = 16 * xi + 2 * w;
  if (xi >= 0) {
    ra = *reinterpret_cast<const v4u*>(A.x0 + (size_t)r0 * DM + l * 8);
    rb = *reinterpret_cast<const v4u*>(A.x0 + (size_t)(r0 + 1) * DM + l * 8);
  }
  const u16 p0 = A.logdt[d], p1 = A.logAre[d * NST + u], p2 = A.Aim[d * NST + u], p3 = A.Cre[d * NST + u], p4 = A.Cim[d * NST + u];
  if (w >= 6) {
#pragma unroll
    for (int i = 0; i < 8; ++i) raw[i] = *reinterpret_cast<const v4u*>(A.x0 + (size_t)(16 * hi + 8 * (w - 6) + i) * DM + l * 8);
  }
  if (xi >= 0) {
    const unsigned sa = row_stats(ra), sb = row_stats(rb);
    if (l < 2) __hip_atomic_store((gu32*)(A.ws + OFF_GT) + r0 + l, l ? sb : sa, RLX_AGENT);
  }
  {
    const float dt = bfr(expf(bf2f(p0)));
    const float ar = -bfr(expf(bf2f(p1))) * dt, ai = bf2f(p2) * dt;
    prm[u] = ar;
    prm[NST + u] = ai;
    prm[2 * NST + u] = bf2f(p3) * dt;
    prm[3 * NST + u] = bf2f(p4) * dt;
#pragma unroll
    for (int cls = 1; cls < 4; ++cls) {
      const float st = (float)(1 << cls);
      const float e = expf(ar * st);
      float sn, cs;
      sincosf(ai * st, &sn, &cs);
      prm[(2 + 2 * cls) * NST + u] = e * cs;
      prm[(3 + 2 * cls) * NST + u] = e * sn;
    }
  }
  if (xi >= 0) {
    DRAIN();
    if (l < 8) set_flag((unsigned*)(A.ws + OFF_FL) + FL_F0 + (l * 8 + xi) * 32 + w);
    SST(11);
  }
  __syncthreads();
  SST(1);
  unsigned tw = 0u;
  if (TLBW && w < 6 && (blockIdx.x >> 6) == 1) {
    const unsigned o = (unsigned)OFF_XA + (unsigned)(tid * 8 + ((blockIdx.x >> 3) & 7)) * 65536u;
    if (o < WS_TOTAL) asm volatile("global_load_dword %0, %1, %2" : "=v"(tw) : "v"(o), "s"(A.ws) : "memory");
  }
  if (w >= 6) {
    unsigned stw = 0u;
#pragma unroll
    for (int i = 0; i < 8; ++i) {
      const unsigned t = row_stats(raw[i]);
      if (l == i) stw = t;
    }
    const int sx = 2 * hi + w - 6;
    if (l < 8) __hip_atomic_store((gu32*)(A.ws + OFF_GT) + 8 * sx + l, stw, RLX_AGENT);
    DRAIN();
    if (l < 8) set_flag((unsigned*)(A.ws + OFF_FL) + FL_FS + (l * 32 + (sx >> 3)) * 32 + (sx & 7));
    if (w == 6 && hi < 64) {
      unsigned* FL = (unsigned*)(A.ws + OFF_FL);
      const int s = hi >> 5, r = hi & 31;
      (void)wait_n<4, 8>(FL + FL_FC + (s * 8 + (blockIdx.x & (NR_C - 1))) * 8 * 32, 64, FL + FL_TMO);
      const unsigned st = row_stats(LD128(MKRS(A.ws + OFF_XA, SZ_X), l * 512, s * 32768 + r * 16));
      const unsigned long long v = ((unsigned long long)__float_as_uint(1.0f / hi16(st)) << 32) | ((st << 16) | 1u);
      if (l < NR_N) __hip_atomic_store((gu64*)(FL + FL_FN + (s * 8 + l) * 64) + r, v, RLX_AGENT);
    }
  } else {
    const int jm = tid / 96, um = tid % 96;
    const int r = um % 12, ng = um / 12;
    const float* prm = reinterpret_cast<float*>(smem) + jm * 10 * NST;
    float* part = reinterpret_cast<float*>(smem) + 40 * NST + jm * 8 * 400;
    const float* pw = prm + (4 + 2 * (r >> 2)) * NST;
    const float p0 = pos_of_index(256 + 32 * r);
    f32x2_t acc[33];
#pragma unroll
    for (int i = 0; i < 33; ++i) acc[i] = f32x2_t{0.f, 0.f};
#pragma unroll 1
    for (int bt = 0; bt < 4; ++bt) {
      const int n = 16 * ng + 4 * bt;
      const f32x4 ar = *reinterpret_cast<const f32x4*>(prm + n), ai = *reinterpret_cast<const f32x4*>(prm + NST + n);
      const f32x4 cr = *reinterpret_cast<const f32x4*>(prm + 2 * NST + n), ci = *reinterpret_cast<const f32x4*>(prm + 3 * NST + n);
      const f32x4 wr = *reinterpret_cast<const f32x4*>(pw + n), wi = *reinterpret_cast<const f32x4*>(pw + NST + n);
      float yr0[4], yi0[4];
#pragma unroll
      for (int k = 0; k < 4; ++k) {
        const float e = __expf(ar[k] * p0);
        float sn, cs;
        __sincosf(ai[k] * p0, &sn, &cs);
        const float zr = e * cs, zi = e * sn;
        yr0[k] = cr[k] * zr - ci[k] * zi;
        yi0[k] = cr[k] * zi + ci[k] * zr;
      }
      f32x2_t yra = {yr0[0], yr0[1]}, yia = {yi0[0], yi0[1]}, wra = {wr[0], wr[1]}, wia = {wi[0], wi[1]};
      f32x2_t yrb = {yr0[2], yr0[3]}, yib = {yi0[2], yi0[3]}, wrb = {wr[2], wr[3]}, wib = {wi[2], wi[3]};
#pragma unroll
      for (int i = 0; i < 33; ++i) {
        acc[i] += yra + yrb;
        const f32x2_t ta = yra * wra - yia * wia, tb = yrb * wrb - yib * wib;
        yia = yra * wia + yia * wra;
        yib = yrb * wib + yib * wrb;
        yra = ta;
        yrb = tb;
      }
    }
#pragma unroll
    for (int i = 0; i < 33; ++i) part[ng * 400 + 33 * r + i] = acc[i].x + acc[i].y;
  }
  __syncthreads();
  SST(2);
  for (int i = u; i < 385; i += 128) {
    const int o = i < 384 ? 33 * (i >> 5) + (i & 31) : 33 * 11 + 32;
    float v = 0.f;
#pragma unroll
    for (int g = 0; g < 8; ++g) v += part[g * 400 + o];
    kd[i] = 2.0f * v;
  }
  __syncthreads();
  const __amdgpu_buffer_rsrc_t rs = MKRS(A.ws + OFF_KF, DM * 2 * CKN * 2);
  for (int un = u; un < 229; un += 128) {
    v4u oh, ol;
#pragma unroll
    for (int e = 0; e < 4; ++e) {
      float kv[2];
#pragma unroll
      for (int k = 0; k < 2; ++k) {
        const int lag = 2080 - (8 * un + 2 * e + k);
        kv[k] = (lag >= 256 && lag < L_SEQ) ? kd[index_of_l(lag) - 256] : 0.f;
      }
      oh[e] = pk2(kv[0], kv[1]);
      ol[e] = pk2(kv[0] - lo16(oh[e]), kv[1] - hi16(oh[e]));
    }
    ST128(oh, rs, (d * 2 * CKN + 8 * un) * 2, 0);
    ST128(ol, rs, (d * 2 * CKN + CKN + 8 * un) * 2, 0);
  }
  DRAIN();
  asm volatile("" ::"v"(tw));
  __syncthreads();
  if (tid == 0) set_flag((unsigned*)(A.ws + OFF_FL) + FL_KG + (hi >> 1) * 32 + (hi & 1));
  SST(3);
}

constexpr int C_SM = 131072, C_SO = C_SM + 8192, C_KT = C_SO + 2048, C_X = C_KT + 16384, C_SQ = C_X + 1024;
constexpr int C_SK = C_SQ + (7 * QS + 128) * 2;
static_assert(C_SK + 8 * QS * 2 <= 163840, "lds");
#define TDSLOT 0
#define CHAIN_ASM_ATT \
  "s_mov_b32 s92, 1152\n" \
  "s_mov_b32 s93, 576\n" \
  "s_mov_b32 s94, 2272\n" \
  "s_mov_b32 s95, 272\n" \
  "s_mov_b32 s96, 288\n" \
  "s_mov_b32 s91, 0x40000001\n" \
  "s_mov_b32 s84, 0x4b400000\n" \
  "s_mov_b32 s85, 0x4b400000\n" \
  "s_mov_b32 s78, 0x3e0293ee\n" \
  "s_mov_b32 s79, 0x3fb8aa3b\n" \
  "s_mov_b32 s76, 0xff00ff00\n" \
  "s_mov_b32 s77, 0xff00ff00\n" \
  "s_add_u32 s58, s56, s60\n" \
  "s_addc_u32 s59, s57, 0\n" \
  "s_lshl_b32 s98, s73, 10\n" \
  "s_add_u32 s98, s98, s74\n" \
  "s_add_u32 s98, s98, 131072\n" \
  "s_lshl_b32 s97, s73, 5\n" \
  "s_add_u32 s97, s97, s74\n" \
  "s_add_u32 s97, s97, 158720\n" \
  "s_lshl_b32 s99, s73, 10\n" \
  "v_mbcnt_lo_u32_b32 v48, -1, 0\n" \
  "v_mbcnt_hi_u32_b32 v48, -1, v48\n" \
  "v_and_b32 v49, 15, v48\n" \
  "v_lshl_add_u32 v8, v49, 1, s97\n" \
  "v_bfe_u32 v49, v48, 4, 1\n" \
  "v_mad_u32_u24 v8, v49, s92, v8\n" \
  "v_lshrrev_b32 v49, 5, v48\n" \
  "v_mad_u32_u24 v9, v49, s93, v8\n" \
  "v_mad_u32_u24 v8, v49, s94, v8\n" \
  "v_add_u32 v10, s99, v1\n" \
  "s_lshr_b32 s88, s99, 2\n" \
  "s_add_u32 s88, s88, s74\n" \
  "s_add_u32 s88, s88, 139264\n" \
  "v_lshl_add_u32 v7, v49, 10, s88\n" \
  "v_and_b32 v49, 31, v48\n" \
  "v_lshl_add_u32 v7, v49, 3, v7\n" \
  "v_mov_b64 v[12:13], 0\n" \
  "v_mov_b64 v[14:15], 0\n" \
  "v_mov_b64 v[16:17], 0\n" \
  "v_mov_b64 v[18:19], 0\n" \
  "v_mov_b64 v[20:21], 0\n" \
  "v_mov_b64 v[22:23], 0\n" \
  "s_add_u32 s88, s74, 157760\n" \
  "v_mov_b32 v59, s88\n" \
  "v_mov_b32 v24, 0x44870000\n" \
  "v_mul_f32 v24, s72, v24\n" \
  "s_nop 0\n" \
  "v_readfirstlane_b32 s86, v24\n" \
  "s_lshr_b32 s86, s86, 23\n" \
  "s_addk_i32 s86, 0xff83\n" \
  "s_ashr_i32 s86, s86, 1\n" \
  "s_max_i32 s86, s86, -20\n" \
  "s_min_i32 s86, s86, 100\n" \
  "s_mov_b32 s82, s86\n" \
  "s_lshr_b32 s83, s72, 23\n" \
  "s_sub_i32 s83, s83, 242\n" \
  "s_lshl_b32 s88, s73, 10\n" \
  "v_add_u32 v24, s88, v1\n" \
  "ds_read_b128 v[32:35], v24 offset:0\n" \
  "s_waitcnt lgkmcnt(0)\n" \
  "v_mov_b32 v11, 0\n" \
  "v_dot2c_f32_bf16 v11, v32, v32\n" \
  "v_dot2c_f32_bf16 v11, v33, v33\n" \
  "v_dot2c_f32_bf16 v11, v34, v34\n" \
  "v_dot2c_f32_bf16 v11, v35, v35\n" \
  "s_nop 1\n" \
  "s_nop 0\n" \
  "v_cmpx_lt_f32 vcc, 2.0, v11\n" \
  "ds_max_u32 v59, v11\n" \
  "s_mov_b64 exec, -1\n" \
  ".LCA_TOP_%=:\n" \
  "s_waitcnt lgkmcnt(0)\n" \
  "s_barrier\n" \
  "ds_read_b128 v[40:43], v1 offset:0\n" \
  "ds_read_b128 v[44:47], v1 offset:1024\n" \
  "ds_read_b128 v[48:51], v1 offset:2048\n" \
  "ds_read_b128 v[52:55], v1 offset:3072\n" \
  "ds_read_b128 v[56:59], v1 offset:4096\n" \
  "ds_read_b128 v[60:63], v1 offset:5120\n" \
  "s_cmp_lg_u32 s73, 0\n" \
  "s_cbranch_scc1 .LCA_NOZ_%=\n" \
  "s_xor_b32 s88, s60, 0x8000\n" \
  "s_add_u32 s88, s88, s61\n" \
  "buffer_store_dwordx2 v[12:13], v5, s[40:43], s88 offen sc1\n" \
  ".LCA_NOZ_%=:\n" \
  "s_waitcnt lgkmcnt(5)\n" \
  "v_smfmac_f32_16x16x64_bf16 v[20:23], v[40:43], v[192:199], v0\n" \
  "v_smfmac_f32_16x16x64_bf16 v[12:15], v[40:43], v[64:71], v0\n" \
  "v_smfmac_f32_16x16x64_bf16 v[16:19], v[40:43], v[128:135], v0\n" \
  "ds_read_b128 v[40:43], v1 offset:6144\n" \
  "s_waitcnt lgkmcnt(5)\n" \
  "v_smfmac_f32_16x16x64_bf16 v[20:23], v[44:47], v[200:207], v0\n" \
  "v_smfmac_f32_16x16x64_bf16 v[12:15], v[44:47], v[72:79], v0\n" \
  "v_smfmac_f32_16x16x64_bf16 v[16:19], v[44:47], v[136:143], v0\n" \
  "ds_read_b128 v[44:47], v1 offset:7168\n" \
  "s_waitcnt lgkmcnt(5)\n" \
  "v_smfmac_f32_16x16x64_bf16 v[20:23], v[48:51], v[208:215], v0\n" \
  "v_smfmac_f32_16x16x64_bf16 v[12:15], v[48:51], v[80:87], v0\n" \
  "v_smfmac_f32_16x16x64_bf16 v[16:19], v[48:51], v[144:151], v0\n" \
  "ds_read_b128 v[24:27], v1 offset:10240\n" \
  "ds_read_b128 v[28:31], v1 offset:14336\n" \
  "s_waitcnt lgkmcnt(6)\n" \
  "v_smfmac_f32_16x16x64_bf16 v[20:23], v[52:55], v[216:223], v0\n" \
  "v_smfmac_f32_16x16x64_bf16 v[12:15], v[52:55], v[88:95], v0\n" \
  "v_smfmac_f32_16x16x64_bf16 v[16:19], v[52:55], v[152:159], v0\n" \
  "ds_read_b128 v[32:35], v1 offset:11264\n" \
  "ds_read_b128 v[36:39], v1 offset:15360\n" \
  "s_waitcnt lgkmcnt(7)\n" \
  "v_smfmac_f32_16x16x64_bf16 v[20:23], v[56:59], v[224:231], v0\n" \
  "v_smfmac_f32_16x16x64_bf16 v[12:15], v[56:59], v[96:103], v0\n" \
  "v_smfmac_f32_16x16x64_bf16 v[16:19], v[56:59], v[160:167], v0\n" \
  "s_waitcnt lgkmcnt(6)\n" \
  "v_smfmac_f32_16x16x64_bf16 v[20:23], v[60:63], v[232:239], v0\n" \
  "v_smfmac_f32_16x16x64_bf16 v[12:15], v[60:63], v[104:111], v0\n" \
  "v_smfmac_f32_16x16x64_bf16 v[16:19], v[60:63], v[168:175], v0\n" \
  "s_waitcnt lgkmcnt(5)\n" \
  "v_smfmac_f32_16x16x64_bf16 v[20:23], v[40:43], v[240:247], v0\n" \
  "v_smfmac_f32_16x16x64_bf16 v[12:15], v[40:43], v[112:119], v0\n" \
  "v_smfmac_f32_16x16x64_bf16 v[16:19], v[40:43], v[176:183], v0\n" \
  "s_waitcnt lgkmcnt(4)\n" \
  "v_smfmac_f32_16x16x64_bf16 v[20:23], v[44:47], v[248:255], v0\n" \
  "v_smfmac_f32_16x16x64_bf16 v[12:15], v[44:47], v[120:127], v0\n" \
  "v_smfmac_f32_16x16x64_bf16 v[16:19], v[44:47], v[184:191], v0\n" \
  "s_cmp_eq_u32 s71, 0\n" \
  "s_nop 4\n" \
  "v_permlane32_swap_b32 v20, v22\n" \
  "v_permlane32_swap_b32 v21, v23\n" \
  "v_pk_add_f32 v[20:21], v[20:21], v[22:23]\n" \
  "v_permlane32_swap_b32 v12, v16\n" \
  "v_permlane32_swap_b32 v13, v17\n" \
  "v_cvt_pk_bf16_f32 v20, v20, v21\n" \
  "v_pk_add_f32 v[12:13], v[12:13], v[16:17]\n" \
  "v_permlane32_swap_b32 v14, v18\n" \
  "v_permlane32_swap_b32 v15, v19\n" \
  "ds_write_b16 v9, v20 offset:128\n" \
  "v_cvt_pk_bf16_f32 v12, v12, v13\n" \
  "v_pk_add_f32 v[14:15], v[14:15], v[18:19]\n" \
  "ds_write_b16_d16_hi v9, v20 offset:416\n" \
  "ds_write_b16 v8, v12\n" \
  "v_cvt_pk_bf16_f32 v14, v14, v15\n" \
  "ds_write_b16_d16_hi v8, v12 offset:288\n" \
  "ds_write_b16 v8, v14 offset:576\n" \
  "ds_write_b16_d16_hi v8, v14 offset:864\n" \
  "s_setprio 3\n" \
  "s_waitcnt lgkmcnt(0)\n" \
  "s_barrier\n" \
  "s_cbranch_scc1 .LCA_LAST_%=\n" \
  "ds_read_b32 v11, v1 offset:26688\n" \
  "ds_read_b128 v[40:43], v2 offset:0\n" \
  "ds_read_b128 v[44:47], v3 offset:0\n" \
  "ds_read_b128 v[48:51], v2 offset:64\n" \
  "ds_read_b128 v[52:55], v3 offset:64\n" \
  "ds_read_b128 v[56:59], v2 offset:128\n" \
  "ds_read_b128 v[60:63], v3 offset:128\n" \
  "s_waitcnt lgkmcnt(5)\n" \
  "v_mfma_f32_16x16x32_bf16 v[12:15], v[24:27], v[40:43], 0\n" \
  "v_mfma_f32_16x16x32_bf16 v[16:19], v[28:31], v[40:43], 0\n" \
  "v_readfirstlane_b32 s86, v11\n" \
  "ds_read_b128 v[24:27], v1 offset:12288\n" \
  "ds_read_b128 v[28:31], v1 offset:16384\n" \
  "s_waitcnt lgkmcnt(6)\n" \
  "v_mfma_f32_16x16x32_bf16 v[20:23], v[44:47], v[40:43], 0\n" \
  "s_lshr_b32 s86, s86, 23\n" \
  "s_add_i32 s86, s86, s83\n" \
  "ds_read_b128 v[40:43], v2 offset:192\n" \
  "ds_read_b128 v[44:47], v3 offset:192\n" \
  "s_waitcnt lgkmcnt(7)\n" \
  "v_mfma_f32_16x16x32_bf16 v[12:15], v[32:35], v[48:51], v[12:15]\n" \
  "v_mfma_f32_16x16x32_bf16 v[16:19], v[36:39], v[48:51], v[16:19]\n" \
  "s_ashr_i32 s86, s86, 1\n" \
  "s_max_i32 s86, s86, s82\n" \
  "ds_read_b128 v[32:35], v1 offset:13312\n" \
  "ds_read_b128 v[36:39], v1 offset:17408\n" \
  "s_waitcnt lgkmcnt(8)\n" \
  "v_mfma_f32_16x16x32_bf16 v[20:23], v[52:55], v[48:51], v[20:23]\n" \
  "s_min_i32 s86, s86, 100\n" \
  "s_lshl_b32 s86, s86, 23\n" \
  "s_sub_i32 s84, 0x4a800000, s86\n" \
  "ds_read_b128 v[48:51], v10 offset:18432\n" \
  "ds_read_b128 v[52:55], v10 offset:22528\n" \
  "s_waitcnt lgkmcnt(8)\n" \
  "v_mfma_f32_16x16x32_bf16 v[20:23], v[60:63], v[56:59], v[20:23]\n" \
  "s_waitcnt lgkmcnt(6)\n" \
  "v_mfma_f32_16x16x32_bf16 v[12:15], v[24:27], v[56:59], v[12:15]\n" \
  "v_mfma_f32_16x16x32_bf16 v[16:19], v[28:31], v[56:59], v[16:19]\n" \
  "ds_read_b128 v[56:59], v6 offset:0\n" \
  "ds_read_b128 v[60:63], v6 offset:1024\n" \
  "s_waitcnt lgkmcnt(6)\n" \
  "v_mfma_f32_16x16x32_bf16 v[20:23], v[44:47], v[40:43], v[20:23]\n" \
  "ds_read_b128 v[24:27], v6 offset:2048\n" \
  "ds_read_b128 v[28:31], v6 offset:3072\n" \
  "s_waitcnt lgkmcnt(6)\n" \
  "v_mfma_f32_16x16x32_bf16 v[12:15], v[32:35], v[40:43], v[12:15]\n" \
  "v_mfma_f32_16x16x32_bf16 v[16:19], v[36:39], v[40:43], v[16:19]\n" \
  "ds_read_b128 v[32:35], v6 offset:8192\n" \
  "ds_read_b128 v[36:39], v6 offset:9216\n" \
  "s_waitcnt lgkmcnt(6)\n" \
  "v_cndmask_b32 v20, v20, v21, s[76:77]\n" \
  "v_mov_b32 v41, 0\n" \
  "v_mov_b32 v42, 0\n" \
  "s_nop 1\n" \
  "v_cndmask_b32 v12, v12, v16, s[76:77]\n" \
  "v_cndmask_b32 v13, v13, v17, s[76:77]\n" \
  "v_cndmask_b32 v14, v14, v18, s[76:77]\n" \
  "v_cndmask_b32 v15, v15, v19, s[76:77]\n" \
  "v_maximum3_f32 v16, v20, v12, v13\n" \
  "v_maximum3_f32 v16, v16, v14, v15\n" \
  "s_nop 1\n" \
  "v_max_f32_dpp v16, v16, v16 row_ror:8 row_mask:0xf bank_mask:0xf\n" \
  "v_mov_b32 v17, v16\n" \
  "s_nop 1\n" \
  "v_permlane16_swap_b32 v16, v17\n" \
  "v_max_f32 v16, v16, v17\n" \
  "v_mov_b32 v17, v16\n" \
  "s_nop 1\n" \
  "v_permlane32_swap_b32 v16, v17\n" \
  "v_max_f32 v16, v16, v17\n" \
  "v_mul_f32 v16, s78, v16\n" \
  "v_pk_fma_f32 v[12:13], v[12:13], s[78:79], v[16:17] op_sel_hi:[1,0,0] neg_lo:[0,0,1] neg_hi:[0,0,1]\n" \
  "v_pk_fma_f32 v[14:15], v[14:15], s[78:79], v[16:17] op_sel_hi:[1,0,0] neg_lo:[0,0,1] neg_hi:[0,0,1]\n" \
  "v_fma_f32 v20, v20, s78, -v16\n" \
  "v_exp_f32 v12, v12\n" \
  "v_exp_f32 v13, v13\n" \
  "v_exp_f32 v14, v14\n" \
  "v_exp_f32 v15, v15\n" \
  "v_exp_f32 v20, v20\n" \
  "s_barrier\n" \
  "ds_read_b64 v[22:23], v7 offset:0\n" \
  "v_cvt_pk_bf16_f32 v44, v12, v13\n" \
  "v_cvt_pk_bf16_f32 v45, v14, v15\n" \
  "v_pk_add_f32 v[8:9], v[12:13], v[14:15]\n" \
  "v_mov_b32_dpp v46, v44 row_ror:8 row_mask:0xf bank_mask:0xf\n" \
  "v_mov_b32_dpp v17, v20 row_ror:8 row_mask:0xf bank_mask:0xf\n" \
  "v_mov_b32_dpp v47, v45 row_ror:8 row_mask:0xf bank_mask:0xf\n" \
  "v_add_f32 v10, v8, v20\n" \
  "v_cvt_pk_bf16_f32 v40, v20, v17\n" \
  "v_add_f32 v10, v10, v9\n" \
  "v_mfma_f32_16x16x32_bf16 v[12:15], v[48:51], v[44:47], 0\n" \
  "v_cvt_pk_bf16_f32 v43, v20, v17\n" \
  "v_add_f32_dpp v10, v10, v10 row_ror:8 row_mask:0xf bank_mask:0xf\n" \
  "v_mfma_f32_16x16x32_bf16 v[16:19], v[52:55], v[44:47], 0\n" \
  "v_mov_b32 v11, v10\n" \
  "s_waitcnt lgkmcnt(0)\n" \
  "v_mfma_f32_16x16x16_bf16 v[12:15], v[22:23], v[40:41], v[12:15]\n" \
  "v_mfma_f32_16x16x16_bf16 v[16:19], v[22:23], v[42:43], v[16:19]\n" \
  "v_permlane16_swap_b32 v10, v11\n" \
  "v_add_f32 v10, v10, v11\n" \
  "ds_read_b128 v[48:51], v6 offset:10240\n" \
  "v_mov_b32 v11, v10\n" \
  "ds_read_b128 v[52:55], v6 offset:11264\n" \
  "v_mov_b64 v[20:21], 0\n" \
  "v_mov_b64 v[22:23], 0\n" \
  "v_permlane32_swap_b32 v10, v11\n" \
  "v_add_f32 v10, v10, v11\n" \
  "v_rcp_f32 v10, v10\n" \
  "s_nop 0\n" \
  "v_pk_mul_f32 v[12:13], v[12:13], v[10:11] op_sel_hi:[1,0]\n" \
  "v_pk_mul_f32 v[14:15], v[14:15], v[10:11] op_sel_hi:[1,0]\n" \
  "v_pk_mul_f32 v[16:17], v[16:17], v[10:11] op_sel_hi:[1,0]\n" \
  "v_pk_mul_f32 v[18:19], v[18:19], v[10:11] op_sel_hi:[1,0]\n" \
  "v_cvt_pk_bf16_f32 v40, v12, v13\n" \
  "v_cvt_pk_bf16_f32 v41, v14, v15\n" \
  "v_cvt_pk_bf16_f32 v42, v16, v17\n" \
  "ds_write_b64 v4, v[40:41]\n" \
  "v_cvt_pk_bf16_f32 v43, v18, v19\n" \
  "ds_write_b64 v4, v[42:43] offset:1024\n" \
  "v_mov_b64 v[12:13], 0\n" \
  "v_mov_b64 v[14:15], 0\n" \
  "v_mov_b64 v[16:17], 0\n" \
  "v_mov_b64 v[18:19], 0\n" \
  "v_mov_b64 v[8:9], 0\n" \
  "v_mov_b64 v[10:11], 0\n" \
  "s_setprio 0\n" \
  "s_waitcnt lgkmcnt(0)\n" \
  "s_barrier\n" \
  "ds_read_b128 v[40:43], v1 offset:8192\n" \
  "ds_read_b128 v[44:47], v1 offset:9216\n" \
  "s_waitcnt lgkmcnt(1)\n" \
  "v_smfmac_f32_16x16x64_bf16 v[12:15], v[40:43], v[56:63], v0\n" \
  "ds_read_b128 v[56:59], v6 offset:4096\n" \
  "ds_read_b128 v[60:63], v6 offset:5120\n" \
  "v_smfmac_f32_16x16x64_bf16 v[16:19], v[40:43], v[24:31], v0\n" \
  "ds_read_b128 v[24:27], v6 offset:6144\n" \
  "ds_read_b128 v[28:31], v6 offset:7168\n" \
  "s_waitcnt lgkmcnt(4)\n" \
  "v_smfmac_f32_16x16x64_bf16 v[12:15], v[44:47], v[32:39], v0\n" \
  "ds_read_b128 v[32:35], v6 offset:12288\n" \
  "ds_read_b128 v[36:39], v6 offset:13312\n" \
  "v_smfmac_f32_16x16x64_bf16 v[16:19], v[44:47], v[48:55], v0\n" \
  "ds_read_b128 v[48:51], v6 offset:14336\n" \
  "ds_read_b128 v[52:55], v6 offset:15360\n" \
  "s_nop 0\n" \
  "s_waitcnt lgkmcnt(6)\n" \
  "v_smfmac_f32_16x16x64_bf16 v[20:23], v[40:43], v[56:63], v0\n" \
  "s_nop 0\n" \
  "s_waitcnt lgkmcnt(4)\n" \
  "v_smfmac_f32_16x16x64_bf16 v[8:11], v[40:43], v[24:31], v0\n" \
  "v_permlane32_swap_b32 v12, v17\n" \
  "v_permlane32_swap_b32 v13, v16\n" \
  "v_permlane32_swap_b32 v14, v19\n" \
  "v_permlane32_swap_b32 v15, v18\n" \
  "s_waitcnt lgkmcnt(2)\n" \
  "v_smfmac_f32_16x16x64_bf16 v[20:23], v[44:47], v[32:39], v0\n" \
  "v_pk_add_f32 v[12:13], v[12:13], v[16:17] op_sel:[0,1] op_sel_hi:[1,0]\n" \
  "v_pk_add_f32 v[14:15], v[14:15], v[18:19] op_sel:[0,1] op_sel_hi:[1,0]\n" \
  "v_pk_fma_f32 v[12:13], v[12:13], s[84:85], s[84:85] op_sel:[0,0,1] op_sel_hi:[1,0,1]\n" \
  "v_pk_fma_f32 v[14:15], v[14:15], s[84:85], s[84:85] op_sel:[0,0,1] op_sel_hi:[1,0,1]\n" \
  "s_waitcnt lgkmcnt(0)\n" \
  "v_smfmac_f32_16x16x64_bf16 v[8:11], v[44:47], v[48:55], v0\n" \
  "v_lshl_add_u32 v12, v12, 6, s91\n" \
  "v_lshl_add_u32 v13, v13, 2, s75\n" \
  "global_atomic_add_x2 v5, v[12:13], s[58:59] offset:0\n" \
  "v_lshl_add_u32 v14, v14, 6, s91\n" \
  "v_lshl_add_u32 v15, v15, 2, s75\n" \
  "global_atomic_add_x2 v5, v[14:15], s[58:59] offset:2048\n" \
  "s_nop 1\n" \
  "v_permlane32_swap_b32 v20, v9\n" \
  "v_permlane32_swap_b32 v21, v8\n" \
  "v_permlane32_swap_b32 v22, v11\n" \
  "v_permlane32_swap_b32 v23, v10\n" \
  "v_pk_add_f32 v[20:21], v[20:21], v[8:9] op_sel:[0,1] op_sel_hi:[1,0]\n" \
  "v_pk_add_f32 v[22:23], v[22:23], v[10:11] op_sel:[0,1] op_sel_hi:[1,0]\n" \
  "v_pk_fma_f32 v[20:21], v[20:21], s[84:85], s[84:85] op_sel:[0,0,1] op_sel_hi:[1,0,1]\n" \
  "v_pk_fma_f32 v[22:23], v[22:23], s[84:85], s[84:85] op_sel:[0,0,1] op_sel_hi:[1,0,1]\n" \
  "v_lshl_add_u32 v20, v20, 6, s91\n" \
  "v_lshl_add_u32 v21, v21, 2, s75\n" \
  "global_atomic_add_x2 v5, v[20:21], s[58:59] offset:3072\n" \
  "v_lshl_add_u32 v22, v22, 6, s91\n" \
  "v_lshl_add_u32 v23, v23, 2, s75\n" \
  "global_atomic_add_x2 v5, v[22:23], s[58:59] offset:1024\n" \
  "v_mbcnt_lo_u32_b32 v48, -1, 0\n" \
  "v_mbcnt_hi_u32_b32 v48, -1, v48\n" \
  "v_and_b32 v49, 15, v48\n" \
  "v_lshl_add_u32 v8, v49, 1, s97\n" \
  "v_bfe_u32 v49, v48, 4, 1\n" \
  "v_mad_u32_u24 v8, v49, s92, v8\n" \
  "v_lshrrev_b32 v49, 5, v48\n" \
  "v_mad_u32_u24 v9, v49, s93, v8\n" \
  "v_mad_u32_u24 v8, v49, s94, v8\n" \
  "v_add_u32 v10, s99, v1\n" \
  "s_lshr_b32 s88, s99, 2\n" \
  "s_add_u32 s88, s88, s74\n" \
  "s_add_u32 s88, s88, 139264\n" \
  "v_lshl_add_u32 v7, v49, 10, s88\n" \
  "v_and_b32 v49, 31, v48\n" \
  "v_lshl_add_u32 v7, v49, 3, v7\n" \
  "v_mov_b64 v[12:13], 0\n" \
  "v_mov_b64 v[14:15], 0\n" \
  "v_mov_b64 v[16:17], 0\n" \
  "v_mov_b64 v[18:19], 0\n" \
  "v_mov_b64 v[20:21], 0\n" \
  "v_mov_b64 v[22:23], 0\n" \
  "s_add_u32 s88, s74, 157760\n" \
  "v_mov_b32 v59, s88\n" \
  "v_lshrrev_b32 v49, 3, v48\n" \
  "v_and_b32 v50, 7, v48\n" \
  "v_lshlrev_b32 v50, 4, v50\n" \
  "v_lshl_or_b32 v44, v49, 8, v50\n" \
  "v_and_b32 v50, 3, v48\n" \
  "v_lshl_add_u32 v40, v50, 2, s98\n" \
  "v_lshrrev_b32 v50, 4, v48\n" \
  "v_mad_u32_u24 v40, v50, s95, v40\n" \
  "v_and_b32 v49, 1, v49\n" \
  "v_lshl_add_u32 v40, v49, 6, v40\n" \
  "v_bfe_u32 v49, v48, 2, 1\n" \
  "v_lshl_add_u32 v40, v49, 7, v40\n" \
  "v_xor_b32 v41, 16, v40\n" \
  "v_xor_b32 v42, 32, v40\n" \
  "v_xor_b32 v43, 48, v40\n" \
  "s_add_i32 s80, s86, 0x32800000\n" \
  "s_add_i32 s81, s86, 0x33800000\n" \
  "s_add_i32 s88, s86, 0xb4000000\n" \
  "v_mov_b32 v46, s88\n" \
  "v_mov_b32 v47, 0\n" \
  "v_mov_b32 v11, 0\n" \
  "s_mov_b32 s89, s60\n" \
  "s_xor_b32 s60, s60, 0x80\n" \
  "s_bitcmp0_b32 s60, 7\n" \
  "s_cselect_b32 s88, 0x8000, 0\n" \
  "s_add_u32 s60, s60, s88\n" \
  "s_and_b32 s60, s60, 0xffff\n" \
  "s_add_u32 s58, s56, s60\n" \
  "s_addc_u32 s59, s57, 0\n" \
  "s_add_u32 s66, s66, 0x1000\n" \
  "s_addc_u32 s67, s67, 0\n" \
  "s_sub_u32 s71, s71, 1\n" \
  "s_mov_b32 s87, 0x40000\n" \
  "s_waitcnt vmcnt(0)\n" \
  "s_sleep 8\n" \
  ".LCA_POLL_%=:\n" \
  "buffer_load_dwordx4 v[24:27], v44, s[40:43], s89 offen sc1\n" \
  "buffer_load_dwordx4 v[28:31], v44, s[40:43], s89 offen offset:2048 sc1\n" \
  "s_sub_u32 s87, s87, 1\n" \
  "s_cbranch_scc1 .LCA_END_%=\n" \
  "s_waitcnt vmcnt(1)\n" \
  "v_bitop3_b32 v45, v24, v26, 32 bitop3:0x80\n" \
  "v_alignbit_b32 v32, v25, v24, 2\n" \
  "v_alignbit_b32 v34, v27, v26, 2\n" \
  "s_waitcnt vmcnt(0)\n" \
  "v_bitop3_b32 v45, v45, v28, v30 bitop3:0x80\n" \
  "v_cmp_eq_u32 vcc, 0, v45\n" \
  "v_cvt_f32_i32 v32, v32\n" \
  "v_cvt_f32_i32 v33, v25\n" \
  "s_cmp_eq_u64 vcc, 0\n" \
  "s_cbranch_scc0 .LCA_POLL_%=\n" \
  "v_cvt_f32_i32 v34, v34\n" \
  "v_cvt_f32_i32 v35, v27\n" \
  "v_pk_fma_f32 v[32:33], v[32:33], s[80:81], v[46:47]\n" \
  "v_alignbit_b32 v36, v29, v28, 2\n" \
  "v_pk_fma_f32 v[34:35], v[34:35], s[80:81], v[46:47]\n" \
  "v_alignbit_b32 v38, v31, v30, 2\n" \
  "v_cvt_pk_bf16_f32 v32, v32, v34\n" \
  "v_cvt_pk_bf16_f32 v33, v33, v35\n" \
  "v_cvt_f32_i32 v36, v36\n" \
  "ds_write_b32 v40, v32\n" \
  "v_cvt_f32_i32 v37, v29\n" \
  "ds_write_b32 v41, v33\n" \
  "v_cvt_f32_i32 v38, v38\n" \
  "v_cvt_f32_i32 v39, v31\n" \
  "v_pk_fma_f32 v[36:37], v[36:37], s[80:81], v[46:47]\n" \
  "v_pk_fma_f32 v[38:39], v[38:39], s[80:81], v[46:47]\n" \
  "v_dot2c_f32_bf16 v11, v32, v32\n" \
  "v_cvt_pk_bf16_f32 v36, v36, v38\n" \
  "v_cvt_pk_bf16_f32 v37, v37, v39\n" \
  "ds_write_b32 v42, v36\n" \
  "ds_write_b32 v43, v37\n" \
  "v_dot2c_f32_bf16 v11, v33, v33\n" \
  "v_dot2c_f32_bf16 v11, v36, v36\n" \
  "v_dot2c_f32_bf16 v11, v37, v37\n" \
  "s_nop 1\n" \
  "s_nop 0\n" \
  "v_cmpx_lt_f32 vcc, 2.0, v11\n" \
  "ds_max_u32 v59, v11\n" \
  "s_mov_b64 exec, -1\n" \
  "s_branch .LCA_TOP_%=\n" \
  ".LCA_LAST_%=:\n" \
  ".LCA_END_%=:\n" \
  "s_endpgm\n"
#define CHAIN_ASM_VO \
  "s_mov_b32 s92, 1152\n" \
  "s_mov_b32 s93, 576\n" \
  "s_mov_b32 s94, 2272\n" \
  "s_mov_b32 s95, 272\n" \
  "s_mov_b32 s96, 288\n" \
  "s_mov_b32 s91, 0x40000001\n" \
  "s_mov_b32 s84, 0x4b400000\n" \
  "s_mov_b32 s85, 0x4b400000\n" \
  "s_mov_b32 s78, 0x3e0293ee\n" \
  "s_mov_b32 s79, 0x3fb8aa3b\n" \
  "s_mov_b32 s76, 0xff00ff00\n" \
  "s_mov_b32 s77, 0xff00ff00\n" \
  "s_add_u32 s58, s56, s60\n" \
  "s_addc_u32 s59, s57, 0\n" \
  "s_lshl_b32 s98, s73, 10\n" \
  "s_add_u32 s98, s98, s74\n" \
  "s_add_u32 s98, s98, 131072\n" \
  "s_lshl_b32 s97, s73, 5\n" \
  "s_add_u32 s97, s97, s74\n" \
  "s_add_u32 s97, s97, 160992\n" \
  "s_lshl_b32 s99, s73, 11\n" \
  "s_add_u32 s99, s99, s74\n" \
  "s_add_u32 s99, s99, 133120\n" \
  "v_mbcnt_lo_u32_b32 v48, -1, 0\n" \
  "v_mbcnt_hi_u32_b32 v48, -1, v48\n" \
  "s_sub_u32 s88, s73, 4\n" \
  "v_and_b32 v51, 15, v48\n" \
  "v_lshl_add_u32 v4, v51, 1, s97\n" \
  "v_bfe_u32 v49, v48, 4, 1\n" \
  "v_mad_u32_u24 v4, v49, s92, v4\n" \
  "v_lshrrev_b32 v50, 5, v48\n" \
  "v_mad_u32_u24 v4, v50, s93, v4\n" \
  "v_lshlrev_b32 v8, 3, v49\n" \
  "v_lshl_add_u32 v8, v51, 4, v8\n" \
  "v_lshl_add_u32 v8, v50, 10, v8\n" \
  "s_lshl_b32 s89, s88, 8\n" \
  "s_add_u32 s89, s89, s74\n" \
  "s_add_u32 s89, s89, 139264\n" \
  "v_and_b32 v49, 31, v48\n" \
  "v_lshl_add_u32 v5, v49, 3, s89\n" \
  "v_lshl_add_u32 v5, v50, 10, v5\n" \
  "v_lshrrev_b32 v49, 2, v51\n" \
  "v_and_b32 v50, 5, v51\n" \
  "v_and_or_b32 v49, v49, 2, v50\n" \
  "s_lshl_b32 s89, s88, 6\n" \
  "s_add_u32 s89, s89, s74\n" \
  "s_add_u32 s89, s89, 160992\n" \
  "v_and_b32 v50, 48, v48\n" \
  "v_add_u32 v6, s89, v50\n" \
  "v_mad_u32_u24 v6, v49, s96, v6\n" \
  "v_lshlrev_b32 v7, 3, v50\n" \
  "v_lshl_add_u32 v7, v49, 4, v7\n" \
  "v_and_b32 v49, 2, v51\n" \
  "v_lshl_add_u32 v7, v49, 29, v7\n" \
  "v_mov_b32 v9, 0\n" \
  "v_lshlrev_b32 v10, 4, v48\n" \
  "v_mov_b64 v[12:13], 0\n" \
  "v_mov_b64 v[14:15], 0\n" \
  "v_mov_b64 v[16:17], 0\n" \
  "v_mov_b64 v[18:19], 0\n" \
  "v_mov_b64 v[20:21], 0\n" \
  "v_mov_b64 v[22:23], 0\n" \
  "s_add_u32 s88, s74, 157760\n" \
  "v_mov_b32 v59, s88\n" \
  "v_mov_b32 v24, 0x44870000\n" \
  "v_mul_f32 v24, s72, v24\n" \
  "s_nop 0\n" \
  "v_readfirstlane_b32 s86, v24\n" \
  "s_lshr_b32 s86, s86, 23\n" \
  "s_addk_i32 s86, 0xff83\n" \
  "s_ashr_i32 s86, s86, 1\n" \
  "s_max_i32 s86, s86, -20\n" \
  "s_min_i32 s86, s86, 100\n" \
  "s_mov_b32 s82, s86\n" \
  "s_lshr_b32 s83, s72, 23\n" \
  "s_sub_i32 s83, s83, 242\n" \
  "s_lshl_b32 s88, s73, 10\n" \
  "v_add_u32 v24, s88, v1\n" \
  "ds_read_b128 v[32:35], v24 offset:0\n" \
  "s_waitcnt lgkmcnt(0)\n" \
  "v_mov_b32 v11, 0\n" \
  "v_dot2c_f32_bf16 v11, v32, v32\n" \
  "v_dot2c_f32_bf16 v11, v33, v33\n" \
  "v_dot2c_f32_bf16 v11, v34, v34\n" \
  "v_dot2c_f32_bf16 v11, v35, v35\n" \
  "s_nop 1\n" \
  "s_nop 0\n" \
  "v_cmpx_lt_f32 vcc, 2.0, v11\n" \
  "ds_max_u32 v59, v11\n" \
  "s_mov_b64 exec, -1\n" \
  ".LCV_TOP_%=:\n" \
  "s_waitcnt lgkmcnt(0)\n" \
  "s_barrier\n" \
  "s_setprio 3\n" \
  "ds_read_b128 v[24:27], v1 offset:0\n" \
  "ds_read_b128 v[28:31], v1 offset:1024\n" \
  "ds_read_b128 v[32:35], v1 offset:2048\n" \
  "ds_read_b128 v[36:39], v1 offset:3072\n" \
  "ds_read_b128 v[40:43], v1 offset:4096\n" \
  "ds_read_b128 v[44:47], v1 offset:5120\n" \
  "ds_read_b128 v[48:51], v1 offset:6144\n" \
  "ds_read_b128 v[52:55], v1 offset:7168\n" \
  "buffer_load_dword v58, v9, s[44:47], s68 offen sc1\n" \
  "s_waitcnt lgkmcnt(7)\n" \
  "v_smfmac_f32_16x16x64_bf16 v[12:15], v[24:27], v[64:71], v0\n" \
  "s_waitcnt lgkmcnt(6)\n" \
  "v_smfmac_f32_16x16x64_bf16 v[12:15], v[28:31], v[72:79], v0\n" \
  "s_waitcnt lgkmcnt(5)\n" \
  "v_smfmac_f32_16x16x64_bf16 v[12:15], v[32:35], v[80:87], v0\n" \
  "s_waitcnt lgkmcnt(4)\n" \
  "v_smfmac_f32_16x16x64_bf16 v[12:15], v[36:39], v[88:95], v0\n" \
  "s_waitcnt lgkmcnt(3)\n" \
  "v_smfmac_f32_16x16x64_bf16 v[12:15], v[40:43], v[96:103], v0\n" \
  "s_waitcnt lgkmcnt(2)\n" \
  "v_smfmac_f32_16x16x64_bf16 v[12:15], v[44:47], v[104:111], v0\n" \
  "s_waitcnt lgkmcnt(1)\n" \
  "v_smfmac_f32_16x16x64_bf16 v[12:15], v[48:51], v[112:119], v0\n" \
  "s_waitcnt lgkmcnt(0)\n" \
  "v_smfmac_f32_16x16x64_bf16 v[12:15], v[52:55], v[120:127], v0\n" \
  "ds_read_b32 v11, v1 offset:26688\n" \
  "s_nop 6\n" \
  "v_permlane32_swap_b32 v12, v14\n" \
  "v_permlane32_swap_b32 v13, v15\n" \
  "v_pk_add_f32 v[12:13], v[12:13], v[14:15]\n" \
  "s_nop 0\n" \
  "v_cvt_pk_bf16_f32 v12, v12, v13\n" \
  "ds_write_b16 v4, v12\n" \
  "ds_write_b16_d16_hi v4, v12 offset:288\n" \
  "s_setprio 0\n" \
  "v_mov_b64 v[12:13], 0\n" \
  "v_mov_b64 v[14:15], 0\n" \
  "s_waitcnt lgkmcnt(2)\n" \
  "v_readfirstlane_b32 s86, v11\n" \
  "s_lshr_b32 s86, s86, 23\n" \
  "s_add_i32 s86, s86, s83\n" \
  "s_ashr_i32 s86, s86, 1\n" \
  "s_max_i32 s86, s86, s82\n" \
  "s_min_i32 s86, s86, 100\n" \
  "s_lshl_b32 s86, s86, 23\n" \
  "s_sub_i32 s84, 0x4a800000, s86\n" \
  "s_cmp_eq_u32 s71, 0\n" \
  "s_waitcnt lgkmcnt(0)\n" \
  "s_barrier\n" \
  "ds_read_b128 v[60:63], v6 offset:0\n" \
  "v_smfmac_f32_16x16x64_bf16 v[16:19], v[24:27], v[128:135], v0\n" \
  "v_smfmac_f32_16x16x64_bf16 v[20:23], v[24:27], v[192:199], v0\n" \
  "v_smfmac_f32_16x16x64_bf16 v[16:19], v[28:31], v[136:143], v0\n" \
  "v_smfmac_f32_16x16x64_bf16 v[20:23], v[28:31], v[200:207], v0\n" \
  "v_smfmac_f32_16x16x64_bf16 v[16:19], v[32:35], v[144:151], v0\n" \
  "v_smfmac_f32_16x16x64_bf16 v[20:23], v[32:35], v[208:215], v0\n" \
  "v_smfmac_f32_16x16x64_bf16 v[16:19], v[36:39], v[152:159], v0\n" \
  "v_smfmac_f32_16x16x64_bf16 v[20:23], v[36:39], v[216:223], v0\n" \
  "v_smfmac_f32_16x16x64_bf16 v[16:19], v[40:43], v[160:167], v0\n" \
  "v_smfmac_f32_16x16x64_bf16 v[20:23], v[40:43], v[224:231], v0\n" \
  "v_smfmac_f32_16x16x64_bf16 v[16:19], v[44:47], v[168:175], v0\n" \
  "v_smfmac_f32_16x16x64_bf16 v[20:23], v[44:47], v[232:239], v0\n" \
  "v_smfmac_f32_16x16x64_bf16 v[16:19], v[48:51], v[176:183], v0\n" \
  "v_smfmac_f32_16x16x64_bf16 v[20:23], v[48:51], v[240:247], v0\n" \
  "v_smfmac_f32_16x16x64_bf16 v[16:19], v[52:55], v[184:191], v0\n" \
  "v_smfmac_f32_16x16x64_bf16 v[20:23], v[52:55], v[248:255], v0\n" \
  "ds_read_b128 v[24:27], v3 offset:0\n" \
  "ds_read_b128 v[28:31], v3 offset:1024\n" \
  "ds_read_b128 v[32:35], v3 offset:2048\n" \
  "ds_read_b128 v[36:39], v3 offset:3072\n" \
  "ds_read_b128 v[40:43], v3 offset:8192\n" \
  "ds_read_b128 v[44:47], v3 offset:9216\n" \
  "s_nop 0\n" \
  "v_permlane32_swap_b32 v16, v18\n" \
  "v_permlane32_swap_b32 v17, v19\n" \
  "v_permlane32_swap_b32 v20, v22\n" \
  "v_permlane32_swap_b32 v21, v23\n" \
  "v_pk_add_f32 v[16:17], v[16:17], v[18:19]\n" \
  "v_pk_add_f32 v[20:21], v[20:21], v[22:23]\n" \
  "v_cvt_pk_bf16_f32 v16, v16, v17\n" \
  "v_cvt_pk_bf16_f32 v17, v20, v21\n" \
  "ds_write_b64 v5, v[16:17]\n" \
  "s_cbranch_scc1 .LCV_LAST_%=\n" \
  "s_waitcnt vmcnt(0)\n" \
  "v_cmp_eq_u32 vcc, 0, v58\n" \
  "s_cmp_eq_u64 vcc, 0\n" \
  "s_cselect_b32 s90, 1, 0\n" \
  "s_waitcnt lgkmcnt(0)\n" \
  "s_barrier\n" \
  "s_cbranch_scc0 .LCV_NOTOK_%=\n" \
  "s_mov_b32 m0, s99\n" \
  "s_add_u32 s88, s99, 8192\n" \
  "global_load_lds_dwordx4 v10, s[62:63] sc1\n" \
  "global_load_lds_dwordx4 v10, s[62:63] offset:1024 sc1\n" \
  "s_mov_b32 m0, s88\n" \
  "s_nop 0\n" \
  "global_load_lds_dwordx4 v10, s[64:65] sc1\n" \
  "global_load_lds_dwordx4 v10, s[64:65] offset:1024 sc1\n" \
  ".LCV_NOTOK_%=:\n" \
  "ds_write_b32 v59, v12\n" \
  "ds_read_b128 v[48:51], v3 offset:10240\n" \
  "ds_read_b128 v[52:55], v3 offset:11264\n" \
  "v_permlane32_swap_b32 v16, v17\n" \
  "v_max_u32 v60, 1, v60\n" \
  "v_max_u32 v61, 1, v61\n" \
  "v_max_u32 v16, 1, v16\n" \
  "v_max_u32 v17, 1, v17\n" \
  "buffer_store_dwordx2 v[16:17], v8, s[52:55], s70 offen sc1\n" \
  "v_max_u32 v62, 1, v62\n" \
  "v_max_u32 v63, 1, v63\n" \
  "buffer_store_dwordx4 v[60:63], v7, s[48:51], s69 offen sc1\n" \
  "v_mov_b64 v[18:19], 0\n" \
  "v_mov_b64 v[20:21], 0\n" \
  "v_mov_b64 v[22:23], 0\n" \
  "v_mov_b64 v[16:17], 0\n" \
  "ds_read_b128 v[56:59], v3 offset:4096\n" \
  "ds_read_b128 v[60:63], v3 offset:5120\n" \
  "s_waitcnt lgkmcnt(0)\n" \
  "s_barrier\n" \
  "ds_read_b128 v[4:7], v1 offset:8192\n" \
  "ds_read_b128 v[8:11], v1 offset:9216\n" \
  "s_waitcnt lgkmcnt(1)\n" \
  "v_smfmac_f32_16x16x64_bf16 v[12:15], v[4:7], v[24:31], v0\n" \
  "ds_read_b128 v[24:27], v3 offset:6144\n" \
  "ds_read_b128 v[28:31], v3 offset:7168\n" \
  "v_smfmac_f32_16x16x64_bf16 v[16:19], v[4:7], v[32:39], v0\n" \
  "ds_read_b128 v[32:35], v3 offset:12288\n" \
  "ds_read_b128 v[36:39], v3 offset:13312\n" \
  "s_waitcnt lgkmcnt(4)\n" \
  "v_smfmac_f32_16x16x64_bf16 v[12:15], v[8:11], v[40:47], v0\n" \
  "v_smfmac_f32_16x16x64_bf16 v[16:19], v[8:11], v[48:55], v0\n" \
  "v_smfmac_f32_16x16x64_bf16 v[20:23], v[4:7], v[56:63], v0\n" \
  "ds_read_b128 v[56:59], v3 offset:14336\n" \
  "ds_read_b128 v[60:63], v3 offset:15360\n" \
  "v_mov_b64 v[40:41], 0\n" \
  "v_mov_b64 v[42:43], 0\n" \
  "s_waitcnt lgkmcnt(2)\n" \
  "v_smfmac_f32_16x16x64_bf16 v[20:23], v[8:11], v[32:39], v0\n" \
  "s_nop 0\n" \
  "v_permlane32_swap_b32 v12, v17\n" \
  "v_permlane32_swap_b32 v13, v16\n" \
  "v_permlane32_swap_b32 v14, v19\n" \
  "v_permlane32_swap_b32 v15, v18\n" \
  "v_smfmac_f32_16x16x64_bf16 v[40:43], v[4:7], v[24:31], v0\n" \
  "v_pk_add_f32 v[12:13], v[12:13], v[16:17] op_sel:[0,1] op_sel_hi:[1,0]\n" \
  "v_pk_add_f32 v[14:15], v[14:15], v[18:19] op_sel:[0,1] op_sel_hi:[1,0]\n" \
  "v_pk_fma_f32 v[12:13], v[12:13], s[84:85], s[84:85] op_sel:[0,0,1] op_sel_hi:[1,0,1]\n" \
  "v_pk_fma_f32 v[14:15], v[14:15], s[84:85], s[84:85] op_sel:[0,0,1] op_sel_hi:[1,0,1]\n" \
  "s_waitcnt lgkmcnt(0)\n" \
  "v_smfmac_f32_16x16x64_bf16 v[40:43], v[8:11], v[56:63], v0\n" \
  "v_lshl_add_u32 v12, v12, 6, s91\n" \
  "v_lshl_add_u32 v13, v13, 2, s75\n" \
  "global_atomic_add_x2 v2, v[12:13], s[58:59] offset:0\n" \
  "v_lshl_add_u32 v14, v14, 6, s91\n" \
  "v_lshl_add_u32 v15, v15, 2, s75\n" \
  "global_atomic_add_x2 v2, v[14:15], s[58:59] offset:2048\n" \
  "s_nop 1\n" \
  "v_permlane32_swap_b32 v20, v41\n" \
  "v_permlane32_swap_b32 v21, v40\n" \
  "v_permlane32_swap_b32 v22, v43\n" \
  "v_permlane32_swap_b32 v23, v42\n" \
  "v_pk_add_f32 v[20:21], v[20:21], v[40:41] op_sel:[0,1] op_sel_hi:[1,0]\n" \
  "v_pk_add_f32 v[22:23], v[22:23], v[42:43] op_sel:[0,1] op_sel_hi:[1,0]\n" \
  "v_pk_fma_f32 v[20:21], v[20:21], s[84:85], s[84:85] op_sel:[0,0,1] op_sel_hi:[1,0,1]\n" \
  "v_pk_fma_f32 v[22:23], v[22:23], s[84:85], s[84:85] op_sel:[0,0,1] op_sel_hi:[1,0,1]\n" \
  "v_lshl_add_u32 v20, v20, 6, s91\n" \
  "v_lshl_add_u32 v21, v21, 2, s75\n" \
  "global_atomic_add_x2 v2, v[20:21], s[58:59] offset:3072\n" \
  "v_lshl_add_u32 v22, v22, 6, s91\n" \
  "v_lshl_add_u32 v23, v23, 2, s75\n" \
  "global_atomic_add_x2 v2, v[22:23], s[58:59] offset:1024\n" \
  "v_mbcnt_lo_u32_b32 v48, -1, 0\n" \
  "v_mbcnt_hi_u32_b32 v48, -1, v48\n" \
  "s_sub_u32 s88, s73, 4\n" \
  "v_and_b32 v51, 15, v48\n" \
  "v_lshl_add_u32 v4, v51, 1, s97\n" \
  "v_bfe_u32 v49, v48, 4, 1\n" \
  "v_mad_u32_u24 v4, v49, s92, v4\n" \
  "v_lshrrev_b32 v50, 5, v48\n" \
  "v_mad_u32_u24 v4, v50, s93, v4\n" \
  "v_lshlrev_b32 v8, 3, v49\n" \
  "v_lshl_add_u32 v8, v51, 4, v8\n" \
  "v_lshl_add_u32 v8, v50, 10, v8\n" \
  "s_lshl_b32 s89, s88, 8\n" \
  "s_add_u32 s89, s89, s74\n" \
  "s_add_u32 s89, s89, 139264\n" \
  "v_and_b32 v49, 31, v48\n" \
  "v_lshl_add_u32 v5, v49, 3, s89\n" \
  "v_lshl_add_u32 v5, v50, 10, v5\n" \
  "v_lshrrev_b32 v49, 2, v51\n" \
  "v_and_b32 v50, 5, v51\n" \
  "v_and_or_b32 v49, v49, 2, v50\n" \
  "s_lshl_b32 s89, s88, 6\n" \
  "s_add_u32 s89, s89, s74\n" \
  "s_add_u32 s89, s89, 160992\n" \
  "v_and_b32 v50, 48, v48\n" \
  "v_add_u32 v6, s89, v50\n" \
  "v_mad_u32_u24 v6, v49, s96, v6\n" \
  "v_lshlrev_b32 v7, 3, v50\n" \
  "v_lshl_add_u32 v7, v49, 4, v7\n" \
  "v_and_b32 v49, 2, v51\n" \
  "v_lshl_add_u32 v7, v49, 29, v7\n" \
  "v_mov_b32 v9, 0\n" \
  "v_lshlrev_b32 v10, 4, v48\n" \
  "v_mov_b64 v[12:13], 0\n" \
  "v_mov_b64 v[14:15], 0\n" \
  "v_mov_b64 v[16:17], 0\n" \
  "v_mov_b64 v[18:19], 0\n" \
  "v_mov_b64 v[20:21], 0\n" \
  "v_mov_b64 v[22:23], 0\n" \
  "s_add_u32 s88, s74, 157760\n" \
  "v_mov_b32 v59, s88\n" \
  "s_cmp_eq_u32 s90, 1\n" \
  "s_cbranch_scc1 .LCV_HAVE_%=\n" \
  "s_mov_b32 s87, 0x40000\n" \
  ".LCV_LATE_%=:\n" \
  "buffer_load_dword v58, v9, s[44:47], s68 offen sc1\n" \
  "s_sub_u32 s87, s87, 1\n" \
  "s_cbranch_scc1 .LCV_END_%=\n" \
  "s_waitcnt vmcnt(0)\n" \
  "v_cmp_eq_u32 vcc, 0, v58\n" \
  "s_cmp_eq_u64 vcc, 0\n" \
  "s_cbranch_scc1 .LCV_GOT_%=\n" \
  "s_sleep 12\n" \
  "s_branch .LCV_LATE_%=\n" \
  ".LCV_GOT_%=:\n" \
  "s_mov_b32 m0, s99\n" \
  "s_add_u32 s88, s99, 8192\n" \
  "global_load_lds_dwordx4 v10, s[62:63] sc1\n" \
  "global_load_lds_dwordx4 v10, s[62:63] offset:1024 sc1\n" \
  "s_mov_b32 m0, s88\n" \
  "s_nop 0\n" \
  "global_load_lds_dwordx4 v10, s[64:65] sc1\n" \
  "global_load_lds_dwordx4 v10, s[64:65] offset:1024 sc1\n" \
  ".LCV_HAVE_%=:\n" \
  "v_lshrrev_b32 v49, 3, v48\n" \
  "v_and_b32 v50, 7, v48\n" \
  "v_lshlrev_b32 v50, 4, v50\n" \
  "v_lshl_or_b32 v44, v49, 8, v50\n" \
  "v_and_b32 v50, 3, v48\n" \
  "v_lshl_add_u32 v40, v50, 2, s98\n" \
  "v_lshrrev_b32 v50, 4, v48\n" \
  "v_mad_u32_u24 v40, v50, s95, v40\n" \
  "v_and_b32 v49, 1, v49\n" \
  "v_lshl_add_u32 v40, v49, 6, v40\n" \
  "v_bfe_u32 v49, v48, 2, 1\n" \
  "v_lshl_add_u32 v40, v49, 7, v40\n" \
  "v_xor_b32 v41, 16, v40\n" \
  "v_xor_b32 v42, 32, v40\n" \
  "v_xor_b32 v43, 48, v40\n" \
  "s_add_u32 s62, s62, 0x80000\n" \
  "s_addc_u32 s63, s63, 0\n" \
  "s_add_u32 s64, s64, 0x40000\n" \
  "s_addc_u32 s65, s65, 0\n" \
  "s_add_u32 s68, s68, 0x1000\n" \
  "s_add_u32 s69, s69, 0x10000\n" \
  "s_add_u32 s70, s70, 0x10000\n" \
  "s_add_i32 s80, s86, 0x32800000\n" \
  "s_add_i32 s81, s86, 0x33800000\n" \
  "s_add_i32 s88, s86, 0xb4000000\n" \
  "v_mov_b32 v46, s88\n" \
  "v_mov_b32 v47, 0\n" \
  "v_mov_b32 v11, 0\n" \
  "s_mov_b32 s89, s60\n" \
  "s_xor_b32 s60, s60, 0x80\n" \
  "s_bitcmp0_b32 s60, 7\n" \
  "s_cselect_b32 s88, 0x8000, 0\n" \
  "s_add_u32 s60, s60, s88\n" \
  "s_and_b32 s60, s60, 0xffff\n" \
  "s_add_u32 s58, s56, s60\n" \
  "s_addc_u32 s59, s57, 0\n" \
  "s_add_u32 s66, s66, 0x1000\n" \
  "s_addc_u32 s67, s67, 0\n" \
  "s_sub_u32 s71, s71, 1\n" \
  "s_mov_b32 s87, 0x40000\n" \
  "s_cmp_eq_u32 s90, 1\n" \
  "s_cbranch_scc0 .LCV_POLL_%=\n" \
  "s_waitcnt vmcnt(0)\n" \
  "s_sleep 5\n" \
  ".LCV_POLL_%=:\n" \
  "buffer_load_dwordx4 v[24:27], v44, s[40:43], s89 offen sc1\n" \
  "buffer_load_dwordx4 v[28:31], v44, s[40:43], s89 offen offset:2048 sc1\n" \
  "s_sub_u32 s87, s87, 1\n" \
  "s_cbranch_scc1 .LCV_END_%=\n" \
  "s_waitcnt vmcnt(1)\n" \
  "v_bitop3_b32 v45, v24, v26, 32 bitop3:0x80\n" \
  "v_alignbit_b32 v32, v25, v24, 2\n" \
  "v_alignbit_b32 v34, v27, v26, 2\n" \
  "s_waitcnt vmcnt(0)\n" \
  "v_bitop3_b32 v45, v45, v28, v30 bitop3:0x80\n" \
  "v_cmp_eq_u32 vcc, 0, v45\n" \
  "v_cvt_f32_i32 v32, v32\n" \
  "v_cvt_f32_i32 v33, v25\n" \
  "s_cmp_eq_u64 vcc, 0\n" \
  "s_cbranch_scc0 .LCV_POLL_%=\n" \
  "v_cvt_f32_i32 v34, v34\n" \
  "v_cvt_f32_i32 v35, v27\n" \
  "v_pk_fma_f32 v[32:33], v[32:33], s[80:81], v[46:47]\n" \
  "v_alignbit_b32 v36, v29, v28, 2\n" \
  "v_pk_fma_f32 v[34:35], v[34:35], s[80:81], v[46:47]\n" \
  "v_alignbit_b32 v38, v31, v30, 2\n" \
  "v_cvt_pk_bf16_f32 v32, v32, v34\n" \
  "v_cvt_pk_bf16_f32 v33, v33, v35\n" \
  "v_cvt_f32_i32 v36, v36\n" \
  "ds_write_b32 v40, v32\n" \
  "v_cvt_f32_i32 v37, v29\n" \
  "ds_write_b32 v41, v33\n" \
  "v_cvt_f32_i32 v38, v38\n" \
  "v_cvt_f32_i32 v39, v31\n" \
  "v_pk_fma_f32 v[36:37], v[36:37], s[80:81], v[46:47]\n" \
  "v_pk_fma_f32 v[38:39], v[38:39], s[80:81], v[46:47]\n" \
  "v_dot2c_f32_bf16 v11, v32, v32\n" \
  "v_cvt_pk_bf16_f32 v36, v36, v38\n" \
  "v_cvt_pk_bf16_f32 v37, v37, v39\n" \
  "ds_write_b32 v42, v36\n" \
  "ds_write_b32 v43, v37\n" \
  "v_dot2c_f32_bf16 v11, v33, v33\n" \
  "v_dot2c_f32_bf16 v11, v36, v36\n" \
  "v_dot2c_f32_bf16 v11, v37, v37\n" \
  "s_nop 1\n" \
  "s_nop 0\n" \
  "v_cmpx_lt_f32 vcc, 2.0, v11\n" \
  "ds_max_u32 v59, v11\n" \
  "s_mov_b64 exec, -1\n" \
  "s_branch .LCV_TOP_%=\n" \
  ".LCV_LAST_%=:\n" \
  "s_waitcnt lgkmcnt(0)\n" \
  "s_nop 0\n" \
  "v_permlane32_swap_b32 v16, v17\n" \
  "v_max_u32 v60, 1, v60\n" \
  "v_max_u32 v61, 1, v61\n" \
  "v_max_u32 v16, 1, v16\n" \
  "v_max_u32 v17, 1, v17\n" \
  "buffer_store_dwordx2 v[16:17], v8, s[52:55], s70 offen sc1\n" \
  "v_max_u32 v62, 1, v62\n" \
  "v_max_u32 v63, 1, v63\n" \
  "buffer_store_dwordx4 v[60:63], v7, s[48:51], s69 offen sc1\n" \
  "s_waitcnt vmcnt(0)\n" \
  ".LCV_END_%=:\n" \
  "s_endpgm\n"
__device__ __forceinline__ v4u mk_srd(const void* p, const unsigned bytes) {
  const unsigned long long a = (unsigned long long)p;
  return v4u{(unsigned)a, (unsigned)(a >> 32) & 0xffffu, bytes, 0x00020000u};
}
template <bool ATT>
__device__ __forceinline__ void role_chain_t(const MegaArgs& A, char* smem, const int L, const int h, const int rep) {
  u16* swo = reinterpret_cast<u16*>(smem);
  const int w = __builtin_amdgcn_readfirstlane(threadIdx.x >> 6);
  char* ws = A.ws;
  unsigned* FL = (unsigned*)(ws + OFF_FL);
  unsigned* TMO = FL + FL_TMO;
#define LANE_IDS()                         \
  const int l = lane_id();                 \
  const int c = l & 15, g = l >> 4;        \
  (void)c; (void)g;
  float* sgm = reinterpret_cast<float*>(smem + C_X);
  float* sgf = sgm + 8;
  unsigned* FQ = FL + FL_FQ + L * 64 * 32 * 32;
  v8u w0[8], w1[8], w2[8];
  {
    LANE_IDS();
    const unsigned sb = lds_base(smem);
    const int q = (l >> 2) & 3, p = l & 3;
    const int f = (q & 1) | ((g & 1) << 1) | ((q >> 1) << 2);
    const unsigned ra = sb + (16 * (g >> 1) + 4 * (g & 1) + (q & 1) + 8 * (q >> 1)) * 256 + (w ^ f) * 32 + p * 8;
#define W_READ(W, hf, X)                                                       \
  {                                                                           \
    _Pragma("unroll") for (int t = 0; t < 4; ++t) {                           \
      const unsigned a_ = (ra ^ (X)) + (hf) * 8192 + t * 32768;               \
      const v2u r0 = trd(a_), r1 = trd(a_ + 512), r2 = trd(a_ + 16384), r3 = trd(a_ + 16384 + 512); \
      W[4 * (hf) + t] = v8u{r0.x, r0.y, r1.x, r1.y, r2.x, r2.y, r3.x, r3.y};  \
    }                                                                         \
    _Pragma("unroll") for (int t = 0; t < 4; ++t) asm volatile("" : "+v"(W[4 * (hf) + t])); \
  }
#define W_STAGE(M, hf, VM, NEXT)                                               \
  {                                                                           \
    WAIT_VM(VM);                                                              \
    RAW_BAR();                                                                \
    if ((M) == 0 && ATT) { W_READ(w0, hf, 0); W_READ(w2, hf, 128); }          \
    if ((M) == 1 && ATT) { W_READ(w1, hf, 0); }                               \
    if ((M) == 1 && !ATT) { W_READ(w0, hf, 0); }                              \
    if ((M) == 2 && !ATT) { W_READ(w1, hf, 128); W_READ(w2, hf, 0); }         \
    RAW_BAR();                                                                \
    NEXT;                                                                     \
  }
#define WO_ISSUE(hf)                                                          \
  {                                                                           \
    const char* base_ = (const char*)A.Wo + (size_t)(h * DH + 64 * (hf) + 8 * w) * (DM * 2); \
    _Pragma("unroll") for (int i = 0; i < 8; ++i) {                           \
      const int fd = (i & 1) | (((i >> 2) & 1) << 1) | ((w & 1) << 2);        \
      glds16(base_, (unsigned)(i * 1024 + ((l ^ (fd << 1)) << 4)), sb + w * 16384 + (hf) * 8192 + i * 1024); \
    }                                                                         \
  }
    wqkv_issue<true>(A.Wv, 0, h, w, l, sb);
    wqkv_issue<true>(A.Wv, 1, h, w, l, sb);
    v4u wr[16];
#define WR_LD(i, R) \
  asm volatile("global_load_dwordx4 %0, %1, %2" : "={" R "}"(wr[i]) : "v"(l * 16), "s"((const char*)A.Wo + (size_t)(h * DH + 16 * w + i) * (DM * 2)) : "memory")
    WR_LD(0, "v[64:67]");    WR_LD(1, "v[68:71]");    WR_LD(2, "v[72:75]");    WR_LD(3, "v[76:79]");
    WR_LD(4, "v[80:83]");    WR_LD(5, "v[84:87]");    WR_LD(6, "v[88:91]");    WR_LD(7, "v[92:95]");
    WR_LD(8, "v[96:99]");    WR_LD(9, "v[100:103]");    WR_LD(10, "v[104:107]");    WR_LD(11, "v[108:111]");
    WR_LD(12, "v[112:115]");    WR_LD(13, "v[116:119]");    WR_LD(14, "v[120:123]");    WR_LD(15, "v[124:127]");
#undef WR_LD
    W_STAGE(2, 0, 24, wqkv_issue<true>(A.Wk, 0, h, w, l, sb));
    SST(4);
    W_STAGE(2, 1, 24, wqkv_issue<true>(A.Wk, 1, h, w, l, sb));
    SST(5);
#define DOT2(x, acc) dot2sq(x, acc)
    float* cs = reinterpret_cast<float*>(smem + C_SM);
    {
      float f2 = 0.f;
      if (!ATT) {
#pragma unroll
        for (int t = 0; t < 8; ++t)
#pragma unroll
          for (int e = 0; e < 8; ++e) f2 = DOT2(w2[t][e], DOT2(w1[t][e], f2));
      }
      f2 = wave_sum_dpp(f2);
      if (l == 0) { sgf[w] = f2; sgm[16] = 0.f; }
      WAIT_VM(16);
      f32x4 ca = {}, cb = {};
#pragma unroll
      for (int i = 0; i < 16; ++i) {
        asm volatile("" : "+v"(wr[i]), "+v"(ca), "+v"(cb));
        const f32x4 x = {lo16(wr[i].x), hi16(wr[i].x), lo16(wr[i].y), hi16(wr[i].y)};
        const f32x4 y = {lo16(wr[i].z), hi16(wr[i].z), lo16(wr[i].w), hi16(wr[i].w)};
        ca += x * x;
        cb += y * y;
      }
      *reinterpret_cast<f32x4*>(cs + w * 512 + l * 8) = ca;
      *reinterpret_cast<f32x4*>(cs + w * 512 + l * 8 + 4) = cb;
    }
    W_STAGE(1, 0, 8, wqkv_issue<true>(A.Wq, 0, h, w, l, sb));
    {
      float m2 = 0.f;
#pragma unroll
      for (int i = 0; i < 8; ++i) m2 += cs[i * 512 + w * 64 + l];
      m2 = wave_max_dpp(m2);
      if (l == 0) sgm[w] = m2;
    }
    W_STAGE(1, 1, 8, wqkv_issue<true>(A.Wq, 1, h, w, l, sb));
    if (w == 0) {
      float m2 = 0.f, f2 = 0.f;
#pragma unroll
      for (int i = 0; i < 8; ++i) { m2 = fmaxf(m2, sgm[i]); f2 += sgf[i]; }
      __builtin_amdgcn_raw_buffer_store_b32(__float_as_uint(sqrtf(m2 * f2) * 1.001f) | 1u, MKRS(FL + FL_G + L * 8 * 32, 8 * 128), l * 128 + h * 4, 0, AUX_SC1);
    }
    SST(6);
    W_STAGE(0, 0, 8, WO_ISSUE(0));
    W_STAGE(0, 1, 8, WO_ISSUE(1));
    SST(7);
#undef W_STAGE
#undef W_READ
#undef WO_ISSUE
#undef DOT2
    const unsigned rb = sb + (2 * (g >> 1) + (q >> 1)) * 16384 + (4 * (g & 1) + (q & 1)) * 1024 + p * 8;
#pragma unroll
    for (int hf = 0; hf < 2; ++hf) {
      if (hf == 0) WAIT_VM(8); else WAIT_VM(0);
      RAW_BAR();
      v4u fr[8];
#pragma unroll
      for (int i = 0; i < 8; ++i) {
        const unsigned a_ = rb + hf * 8192 + (i & 1) * 65536 + ((4 * w + (i >> 1)) ^ f) * 32;
        const v2u r0 = trd(a_), r1 = trd(a_ + 2048);
        fr[i] = v4u{r0.x, r0.y, r1.x, r1.y};
      }
#pragma unroll
      for (int i = 0; i < 8; ++i) asm volatile("" : "+v"(fr[i]));
      RAW_BAR();
#pragma unroll
      for (int i = 0; i < 8; ++i) *reinterpret_cast<v4u*>(swo + w * 8192 + hf * 4096 + i * 512 + l * 8) = fr[i];
    }
  }
  WSTAMP2();
  v4u m0r;
  {
    LANE_IDS();
    m0r = *reinterpret_cast<const v4u*>(A.mem0 + (c & 7) * DM + 64 * w + 16 * g + 8 * (c >> 3));
  }
  const unsigned sb = lds_base(smem);
  const char* gK = ws + OFF_QK + L * SZ_QK + ((size_t)h * 16 + 2 * w) * 1024;
  const char* gV = ws + OFF_VF + L * SZ_O + ((size_t)h * 8 + 2 * w - 8) * 1024;
  float gw2;
  {
    const unsigned* gp = FL + FL_G + (L * 8 + (blockIdx.x & 7)) * 32;
    unsigned gv;
    for (unsigned spins = 0;; ++spins) {
      gv = __hip_atomic_load((gu32*)gp + (lane_id() & 31), RLX_AGENT);
      if (__all(gv != 0u)) break;
      if (spin_timeout(spins, TMO)) return;
      __builtin_amdgcn_s_sleep(4);
    }
    const float gm = wave_max_dpp(__uint_as_float(gv));
    asm volatile("s_nop 0\n\tv_readfirstlane_b32 %0, %1" : "=s"(gw2) : "v"(gm * gm * 1.06f));
  }
  v8u pv;
  {
    LANE_IDS();
    *reinterpret_cast<v4u*>(smem + C_SM + w * 1024 + l * 16) = m0r;
    const unsigned voff = g * 256 + c * 8, wp = sb + w * 16384 + l * 16;
    pv[0] = (c & 8) ? 0xEEEE : 0x4444;
    pv[1] = sb + C_SM + l * 16;
    pv[2] = ATT ? sb + C_SQ + ((c & 7) * QS + 8 * g) * 2 : voff;
    pv[3] = ATT ? sb + C_SK + ((4 * ((c >> 2) & 1) + 2 * (c >> 3) + (c & 1)) * QS + 8 * g) * 2 : wp;
    pv[4] = sb + (c < 8 ? C_SO : C_SQ) + w * 256 + ((c & 7) + 8 * (g >> 1)) * 16 + 8 * (g & 1);
    pv[5] = voff;
    pv[6] = wp;
    pv[7] = sb + C_SO + (l >> 5) * 1024 + w * 256 + (l & 31) * 8;
  }
  if (!ATT) {
    if (!wait_n<4, 1>(FQ + h * 32, 1, TMO)) return;
    const unsigned lo = lane_id() * 16, d = sb + C_KT + (2 * w - 8) * 1024;
    glds16c(gK, lo, d);
    glds16c(gK + 1024, lo, d + 1024);
    glds16c(gV, lo, d + 8192);
    glds16c(gV + 1024, lo, d + 9216);
  }
  DRAIN();
  SST(8);
  const v4u rsA = mk_srd(ws + OFF_ACC + L * SZ_ACC, SZ_ACC);
  const v4u rsF = mk_srd(FQ, 64 * 32 * 32 * 4);
  const v4u rsMK = mk_srd(ws + OFF_MEMK + L * SZ_MEM, SZ_MEM);
  const v4u rsMV = mk_srd(ws + OFF_MEMV + L * SZ_MEM, SZ_MEM);
  const char* pAB = ws + OFF_ACC + L * SZ_ACC;
  const char* pGK = gK + NH * 16 * 1024;
  const char* pGV = gV + NH * 8 * 1024;
  const unsigned* pFK = FL + FL_FK + (L * 64 * 32 + h) * 32 + w;
  unsigned sc[8];
  sc[0] = (32 + h) * 128;
  sc[1] = (h * 4 + w - 4) * 512;
  sc[2] = (h * DH + 16 * (w - 4)) * MM * 2;
  sc[3] = NSEG - 1;
  sc[4] = __float_as_uint(gw2);
  sc[5] = w;
  sc[6] = sb;
  sc[7] = h == 0 ? 0xd4000000u - 2u - (1u << 29) : 0xd4000000u;
  const char* dbg = ws;
#define CHAIN_OPS                                                                                                          \
  ::"{v[64:71]}"(w0[0]), "{v[72:79]}"(w0[1]), "{v[80:87]}"(w0[2]), "{v[88:95]}"(w0[3]), "{v[96:103]}"(w0[4]),              \
      "{v[104:111]}"(w0[5]), "{v[112:119]}"(w0[6]), "{v[120:127]}"(w0[7]), "{v[128:135]}"(w1[0]), "{v[136:143]}"(w1[1]),    \
      "{v[144:151]}"(w1[2]), "{v[152:159]}"(w1[3]), "{v[160:167]}"(w1[4]), "{v[168:175]}"(w1[5]), "{v[176:183]}"(w1[6]),    \
      "{v[184:191]}"(w1[7]), "{v[192:199]}"(w2[0]), "{v[200:207]}"(w2[1]), "{v[208:215]}"(w2[2]), "{v[216:223]}"(w2[3]),    \
      "{v[224:231]}"(w2[4]), "{v[232:239]}"(w2[5]), "{v[240:247]}"(w2[6]), "{v[248:255]}"(w2[7]), "{v[0:7]}"(pv),          \
      "{s[40:43]}"(rsA), "{s[44:47]}"(rsF), "{s[48:51]}"(rsMK), "{s[52:55]}"(rsMV), "{s[56:57]}"(pAB), "{s60}"(w * 4096), "{s61}"(h * 1024), \
      "{s[62:63]}"(pGK), "{s[64:65]}"(pGV), "{s[66:67]}"(pFK), "{s68}"(sc[0]), "{s69}"(sc[1]), "{s70}"(sc[2]),    \
      "{s71}"(sc[3]), "{s72}"(sc[4]), "{s73}"(sc[5]), "{s74}"(sc[6]), "{s75}"(sc[7]), "{s[36:37]}"(dbg)                    \
      : "memory", "vcc", "scc", "s99", "v63"
  if (ATT) asm volatile("s_mov_b64 exec, -1\ns_nop 4\n" CHAIN_ASM_ATT CHAIN_OPS);
  else asm volatile("s_mov_b64 exec, -1\ns_nop 4\n" CHAIN_ASM_VO CHAIN_OPS);
#undef CHAIN_OPS
#undef LANE_IDS
}

__device__ __forceinline__ void role_chain(const MegaArgs& A, char* smem, const int L, const int h, const int rep) {
  if (__builtin_amdgcn_readfirstlane(threadIdx.x >> 6) < 4) role_chain_t<true>(A, smem, L, h, rep);
  else role_chain_t<false>(A, smem, L, h, rep);
}

constexpr int AW_SO = 0, AW_M = 32768, AW_TK = AW_M + 8192, AW_CNT = AW_TK + 98304;
typedef __attribute__((address_space(3))) unsigned lu32;
__device__ __forceinline__ unsigned umin3(unsigned a, unsigned b, unsigned c) {
  return __builtin_elementwise_min(__builtin_elementwise_min(a, b), c);
}
__device__ __forceinline__ bool all_set(const unsigned r) {
  unsigned long long m;
  asm volatile("v_cmp_eq_u32 %0, 0, %1" : "=s"(m) : "v"(r) : "memory");
  return m == 0ull;
}
template <bool AT>
__device__ __forceinline__ void role_aw_t(const MegaArgs& A, char* smem, const int L, const int a) {
  const int w = __builtin_amdgcn_readfirstlane(threadIdx.x >> 6);
  const int l = threadIdx.x & 63, c = l & 15, g = l >> 4;
  const int qt = w & 1, hh = (w >> 1) & 1, dh = AT ? 0 : 1, h = 2 * a + hh;
  char* ws = A.ws;
  unsigned* FL = (unsigned*)(ws + OFF_FL);
  unsigned* TMO = FL + FL_TMO;
  const unsigned sb = lds_base(smem);
  constexpr int NP = AT ? 1 : 3;
  const int cp0 = AT ? w : 3 * w - 8;
  v4u wo[2 * NP][8];
  {
    const int q = (l >> 2) & 3, p = l & 3, f = q | ((g & 1) << 2);
    const unsigned ra = sb + (4 * g + q) * 256 + (p & 1) * 16;
#pragma unroll
    for (int r = 0; r < 2; ++r) {
#pragma unroll
      for (int k = 0; k < 2; ++k) {
        const char* base_ = (const char*)A.Wo + ((size_t)((2 * a + r) * DH + 64 * (w & 1) + 32 * k) * DM + 128 * (w >> 1)) * 2;
#pragma unroll
        for (int i = 0; i < 8; ++i) {
          const int fd = g | ((i & 1) << 2);
          glds16(base_, (unsigned)((4 * i + g) * (DM * 2) + ((c ^ (fd << 1)) << 4)), sb + w * 16384 + k * 8192 + i * 1024);
        }
      }
      WAIT_VM(0);
      RAW_BAR();
#pragma unroll
      for (int ks = 0; ks < 4; ++ks) {
#pragma unroll
        for (int ct = 0; ct < 2 * NP; ++ct) {
          const int cp = cp0 + (ct >> 1);
          const unsigned a_ = ra + (cp >> 2) * 32768 + ks * 8192 + ((2 * (cp & 3) + (p >> 1)) ^ f) * 32 + (ct & 1) * 8;
          const v2u lo = trd(a_), hi = trd(a_ + 4096);
          wo[ct][4 * r + ks] = v4u{lo.x, lo.y, hi.x, hi.y};
        }
#pragma unroll
        for (int ct = 0; ct < 2 * NP; ++ct) asm volatile("" : "+v"(wo[ct][4 * r + ks]));
      }
      RAW_BAR();
    }
  }
  lu32* cnt = (lu32*)(unsigned long)(sb + AW_CNT);
  if (threadIdx.x < 10) cnt[threadIdx.x] = 0u;
  WSTAMP2();
  const __amdgpu_buffer_rsrc_t rsW = MKRS(ws + OFF_PW, SZ_PW);
  const unsigned* FQ = FL + FL_FQ + (L * 64 * 32 + h) * 32;
  const v4u rsF = mk_srd(FQ, (64 * 32 - h) * 128);
  const char* gQ = ws + OFF_QK + L * SZ_QK + ((size_t)h * 16 + 4 * (2 * qt + dh)) * 1024;
  const char* gV = ws + OFF_VF + L * SZ_O + ((size_t)h * 8 + 2 * (2 * qt + dh)) * 1024;
  const char* gM = ws + (qt ? OFF_MEMV : OFF_MEMK) + L * SZ_MEM + h * 2048;
  const int mo = AW_M + hh * 4096;
  const unsigned tq0 = __builtin_amdgcn_readfirstlane(sb + AW_TK + (hh * 24 + 4 * (2 * qt + dh)) * 1024);
  const unsigned tv0 = __builtin_amdgcn_readfirstlane(sb + AW_TK + (hh * 24 + 16 + 2 * (2 * qt + dh)) * 1024);
  const float scale = 0.08838834764831845f;
#define T_DMA(ss)                                                                                                       \
  {                                                                                                                     \
    const unsigned s_ = __builtin_amdgcn_readfirstlane(ss);                                                             \
    const unsigned d_ = tq0 + ((s_ & 1) ? 49152u : 0u), e_ = tv0 + ((s_ & 1) ? 49152u : 0u);                             \
    unsigned keep_;                                                                                                     \
    asm volatile(                                                                                                       \
        "s_nop 4\n\ts_mov_b32 %0, m0\n\ts_mov_b32 m0, %4\n\ts_nop 0\n\t"                                               \
        "global_load_lds_dwordx4 %1, %2 sc1\n\tglobal_load_lds_dwordx4 %1, %2 offset:1024 sc1\n\t"                       \
        "global_load_lds_dwordx4 %1, %2 offset:2048 sc1\n\tglobal_load_lds_dwordx4 %1, %2 offset:3072 sc1\n\t"           \
        "s_mov_b32 m0, %5\n\ts_nop 0\n\t"                                                                               \
        "global_load_lds_dwordx4 %1, %3 sc1\n\tglobal_load_lds_dwordx4 %1, %3 offset:1024 sc1\n\ts_mov_b32 m0, %0"       \
        : "=&s"(keep_)                                                                                                  \
        : "v"(l * 16), "s"(gQ + (size_t)s_ * (NH * 16 * 1024)), "s"(gV + (size_t)s_ * (NH * 8 * 1024)), "s"(d_), "s"(e_) \
        : "memory");                                                                                                    \
  }
#define M_DMA(ss)                                                                                                       \
  {                                                                                                                     \
    unsigned keep_;                                                                                                     \
    asm volatile("s_nop 4\n\ts_mov_b32 %0, m0\n\ts_mov_b32 m0, %3\n\ts_nop 0\n\tglobal_load_lds_dwordx4 %1, %2 sc1\n\t"     \
                 "global_load_lds_dwordx4 %1, %2 offset:1024 sc1\n\ts_mov_b32 m0, %0"                                    \
                 : "=&s"(keep_)                                                                                         \
                 : "v"(l * 16), "s"(gM + (size_t)__builtin_amdgcn_readfirstlane(ss) * (NH * 2048)), "s"(sb + mo + qt * 2048)                           \
                 : "memory");                                                                                           \
  }
#define M_TEST()                                                                                                        \
  {                                                                                                                     \
    const v4u t_ = *reinterpret_cast<const v4u*>(smem + mo + qt * 2048 + l * 16);                                        \
    const v4u u_ = *reinterpret_cast<const v4u*>(smem + mo + qt * 2048 + 1024 + l * 16);                                 \
    ok = __all(umin3(umin3(t_.x, t_.y, t_.z), umin3(u_.x, u_.y, u_.z), umin3(t_.w, u_.w, 1u)) != 0u);                   \
    if (ok) asm volatile("s_mov_b64 exec, 1\n\tds_add_u32 %0, %1\n\ts_mov_b64 exec, -1" ::"v"(sb + AW_CNT + 12 + 4 * hh), "v"(1u) : "memory"); \
  }
#define F_LOAD(r, ss) \
  asm volatile("s_nop 4\n\tbuffer_load_dword %0, %1, %2, %3 offen sc1" : "=v"(r) : "v"(0), "s"(rsF), "s"(__builtin_amdgcn_readfirstlane(ss) * 4096) : "memory")
#define F_TEST(r) all_set(r)
#define W_FLAG(ss, THEN, ELSE)                                                                                          \
  {                                                                                                                     \
    unsigned old_;                                                                                                      \
    asm volatile("s_mov_b64 exec, 1\n\tds_add_rtn_u32 %0, %1, %2\n\ts_mov_b64 exec, -1\n\ts_waitcnt lgkmcnt(0)"           \
                 : "=&v"(old_) : "v"(sb + AW_CNT + ((ss) & 1) * 36), "v"(1u) : "memory");                                                 \
    if ((__builtin_amdgcn_readfirstlane(old_) & 7u) == 7u) {                                                            \
      TS(4, ss, threadIdx.x & ~63u);                                                                                    \
      if (l < NR_W) set_flag(FL + FL_FW + (((L * 64 + (ss)) * 8 + l) * 4 + (a >> 2)) * 32 + (a & 3));                       \
      THEN;                                                                                                             \
    } else {                                                                                                            \
      ELSE;                                                                                                             \
    }                                                                                                                   \
    pend = false;                                                                                                       \
  }
  __builtin_amdgcn_s_waitcnt(0x0F70);
  bool have = false, late = true, pend = false, ahead = false;
  unsigned nf = 0u, tl = now10();
  int asked = 0, lead = 0, skip = 0;
  for (int s = 0; s < NSEG; ++s) {
    const bool spec = have;
    if (late) {
      if (pend) {
        WAIT_VM(0);
        W_FLAG(s - 1, , );
      }
      if (!have) {
        (void)wait_n<SLP_A, 1>(FQ + s * 1024, 1, TMO);
        T_DMA(s);
        WAIT_VM(0);
      }
      RAW_BAR();
    }
    TS(0, s, 0);
    const char* tk = smem + AW_TK + ((s & 1) * 2 + hh) * 24 * 1024 + l * 16;
#define TKF(i) (*reinterpret_cast<const bf16x8*>(tk + (i) * 1024))
    bf16x8 q[4], vt[8];
    float x[12], mx;
    bool ok = false;
    if (AT) {
      f32x4 s0 = {}, s1 = {};
      bf16x8 kd[4], k0[4];
#pragma unroll
      for (int ks = 0; ks < 4; ++ks) {
        q[ks] = TKF(4 * qt + ks);
        kd[ks] = TKF(8 + 4 * qt + ks);
        k0[ks] = TKF(8 + ks);
      }
#pragma unroll
      for (int i = 0; i < 8; ++i) vt[i] = TKF(16 + i);
      __builtin_amdgcn_sched_barrier(0);
#pragma unroll
      for (int ks = 0; ks < 4; ++ks) {
        s0 = MFMA16(kd[ks], q[ks], s0);
        s1 = MFMA16(k0[ks], q[ks], s1);
      }
      scale_pair(s0[0], s0[1], scale, x[0], x[1]);
      scale_pair(s0[2], s0[3], scale, x[2], x[3]);
      scale_pair(s1[0], s1[1], scale, x[4], x[5]);
      scale_pair(s1[2], s1[3], scale, x[6], x[7]);
#pragma unroll
      for (int i = 0; i < 4; ++i) {
        if (4 * g + i > c) x[i] = -3e38f;
        if (!qt) x[4 + i] = -3e38f;
      }
      mx = fmaxf(fmaxf(fmaxf(x[0], x[1]), fmaxf(x[2], x[3])), fmaxf(fmaxf(x[4], x[5]), fmaxf(x[6], x[7])));
      asm volatile("" : "+v"(mx), "+v"(vt[0]), "+v"(vt[1]), "+v"(vt[2]), "+v"(vt[3]), "+v"(vt[4]), "+v"(vt[5]), "+v"(vt[6]), "+v"(vt[7]));
    }
#undef TKF
    have = ahead;
    if (spec) {
      if (AT && asked == 2) WAIT_VM(1); else if (AT) WAIT_VM(2); else WAIT_VM(6);
      if (!ahead) have = F_TEST(nf);
      if (AT && asked) M_TEST();
      skip = ok ? 0 : asked == 2 ? AWSKIP : skip - 1;
      lead = ok ? lead + 1 : 0;
      if (have && !ahead) T_DMA(s + 1);
    }
    unsigned n3;
    if (AT) F_LOAD(n3, s + 2);
    if (!AT) ok = have;
    if (!ok) {
      if (pend) {
        if (!AT) WAIT_VM(0); else if (have && !ahead) WAIT_VM(7); else WAIT_VM(1);
        W_FLAG(s - 1, , );
      }
      if (AT && GATE_M) gate(tl);
    }
    for (unsigned spins = 0; !ok; ++spins) {
      if (spin_timeout(spins, TMO)) break;
      if (spins) __builtin_amdgcn_s_sleep(SLP_M);
      unsigned n2;
      if (AT) M_DMA(s);
      F_LOAD(n2, have ? NSEG : s + 1);
      WAIT_VM(0);
      if (AT) M_TEST() else ok = true;
      if (F_TEST(n2)) {
        have = true;
        T_DMA(s + 1);
      }
    }
    char* so = smem + AW_SO + (s & 1) * 16384 + l * 16;
    if (AT) {
      __builtin_amdgcn_s_setprio(PRI_A);
      tl = now10() + GATE_M;
      asked = 0;
      for (unsigned spins = 0; (unsigned)__builtin_amdgcn_readfirstlane(*(volatile lu32*)(cnt + 3 + hh)) < 2u * (s + 1) && spins < 64u * SPIN_CAP; ++spins)
        __builtin_amdgcn_s_sleep(1);
      v4u km[4];
      v2u vm[8];
#pragma unroll
      for (int ks = 0; ks < 4; ++ks) km[ks] = *reinterpret_cast<const v4u*>(smem + mo + (ks * 32 + g * 8 + (c & 7)) * 16);
#pragma unroll
      for (int i = 0; i < 8; ++i) vm[i] = *reinterpret_cast<const v2u*>(smem + mo + 2048 + ((16 * i + c) * MM + 4 * (g & 1)) * 2);
      TS(1, s, 0);
      f32x4 m0 = {};
#pragma unroll
      for (int ks = 0; ks < 4; ++ks) m0 = MFMA16(BF8(km[ks]), q[ks], m0);
      scale_pair(m0[0], m0[1], scale, x[8], x[9]);
      scale_pair(m0[2], m0[3], scale, x[10], x[11]);
#pragma unroll
      for (int i = 0; i < 4; ++i) {
        if (g >= 2) x[8 + i] = -3e38f;
        mx = fmaxf(mx, x[8 + i]);
      }
      mx = xmax32(xmax16(mx));
      float sum = 0.f;
#pragma unroll
      for (int i = 0; i < 12; ++i) {
        x[i] = __expf(x[i] - mx);
        sum += x[i];
      }
      sum = xsum32(xsum16(sum));
      const float inv = 1.0f / sum;
      const v2u pa = {pk2s(x[0] * inv, x[1] * inv), pk2s(x[2] * inv, x[3] * inv)};
      const v2u pb = {pk2s(x[4] * inv, x[5] * inv), pk2s(x[6] * inv, x[7] * inv)};
      const v4u p0 = qt ? v4u{pb.x, pb.y, pa.x, pa.y} : v4u{pa.x, pa.y, pb.x, pb.y};
      const v2u p1 = {pk2s(x[8] * inv, x[9] * inv), pk2s(x[10] * inv, x[11] * inv)};
#pragma unroll
      for (int k = 0; k < 4; ++k) {
        v4u of;
#pragma unroll
        for (int hf = 0; hf < 2; ++hf) {
          f32x4 ao = {};
          ao = MFMA16(vt[2 * k + hf], BF8(p0), ao);
          ao = MFMA16K(vm[2 * k + hf], p1, ao);
          of[2 * hf] = pk2(ao[0], ao[1]);
          of[2 * hf + 1] = pk2(ao[2], ao[3]);
        }
        *reinterpret_cast<v4u*>(so + ((hh * 2 + qt) * 4 + k) * 1024) = of;
      }
    }
    if (!have && l == 0) *(volatile lu32*)(cnt + 1 + (s & 1)) = s + 1;
    if (AT) {
      WAIT_VM(0);
      ahead = F_TEST(n3);
      if (ahead && l == 0) *(volatile lu32*)(cnt + 5 + 2 * hh + (s & 1)) = s + 1;
      ahead = ahead && have;
      if (pend) W_FLAG(s - 1, , );
    } else if (!AWDEF || !ahead || (unsigned)__builtin_amdgcn_readfirstlane(*(volatile lu32*)(cnt + 3 + hh)) < 2u * (s + 1)) {
      WAIT_VM(0);
      if (pend) W_FLAG(s - 1, , );
    }
    RAW_BAR();
    __builtin_amdgcn_s_setprio(AT ? PRI_WA : PRI_WB);
    TS(2, s, 256);
    const int voff = (c >> 1) * 2048 + (c & 1) * 64 + g * 16, soff = ((L * 64 + s) * 16 + a) * 32768 + cp0 * 128;
    const unsigned lt = *(volatile lu32*)(cnt + 1 + (s & 1));
    if (!AT) ahead = have && (unsigned)__builtin_amdgcn_readfirstlane(*(volatile lu32*)(cnt + 5 + 2 * hh + (s & 1))) == (unsigned)(s + 1);
    unsigned pr;
#define OFRAG(i) (*reinterpret_cast<const bf16x8*>(so + (((((i) & 7) >> 2) * 2 + ((i) >> 3)) * 4 + ((i) & 3)) * 1024))
    constexpr int NO = AT ? 16 : 3;
    bf16x8 o[NO];
#pragma unroll
    for (int i = 0; i < (AT ? 16 : 2); ++i) o[i] = OFRAG(i);
    if (AT)
      asm volatile("s_nop 4\n\tglobal_load_dword %0, %1, %2 sc1"
                   : "=v"(pr)
                   : "v"(2044), "s"(gM + (size_t)(s < NSEG - 1 ? s + 1 : s) * (NH * 2048))
                   : "memory");
    if (have) {
      F_LOAD(nf, ahead ? NSEG : s + 2);
      if (ahead) T_DMA(s + 2);
    }
    __builtin_amdgcn_sched_barrier(0);
    late = __builtin_amdgcn_readfirstlane(lt) == (unsigned)(s + 1);
#pragma unroll
    for (int rt = 0; rt < 2; ++rt) {
      f32x4 acc[2 * NP] = {};
#pragma unroll
      for (int kk = 0; kk < 8; ++kk) {
        const int i = 8 * rt + kk;
        if (!AT && i + 2 < 16) o[(i + 2) % NO] = OFRAG(i + 2);
#pragma unroll
        for (int ct = 0; ct < 2 * NP; ++ct) acc[ct] = MFMA16(BF8(wo[ct][kk]), o[i % NO], acc[ct]);
        if (!AT) __builtin_amdgcn_sched_barrier(0);
      }
      if (AT && rt == 1) {
        if (ahead) WAIT_VM(8); else WAIT_VM(1);
        if (all_set(pr) && have) {
          M_DMA(s + 1);
          asked = 2;
        }
      }
      if (AWDEF && !AT && rt == 0 && pend) {
        if (ahead) WAIT_VM(7); else WAIT_VM(1);
        W_FLAG(s - 1, , );
      }
#pragma unroll
      for (int u = 0; u < NP; ++u)
        ST128((v4u{pk2(acc[2 * u][0], acc[2 * u][1]), pk2(acc[2 * u][2], acc[2 * u][3]), pk2(acc[2 * u + 1][0], acc[2 * u + 1][1]),
                   pk2(acc[2 * u + 1][2], acc[2 * u + 1][3])}),
              rsW, voff + u * 128, soff + rt * 16384);
    }
    __builtin_amdgcn_s_setprio(AT ? PRI_0 : 0);
    pend = true;
    TS(3, s, 256);
    TS(5, s, 0);
    MSTAMP();
  }
  WAIT_VM(0);
  W_FLAG(NSEG - 1, , );
#undef OFRAG
#undef T_DMA
#undef M_DMA
#undef M_TEST
#undef F_LOAD
#undef F_TEST
#undef W_FLAG
}
__device__ __forceinline__ void role_aw(const MegaArgs& A, char* smem, const int L, const int a) {
  if (__builtin_amdgcn_readfirstlane(threadIdx.x >> 6) < 4) role_aw_t<true>(A, smem, L, a);
  else role_aw_t<false>(A, smem, L, a);
}

constexpr int RG = 2, RW = 2;
__device__ __forceinline__ void role_red(const MegaArgs& A, char* smem, const int L, const int idx) {
  const int w = __builtin_amdgcn_readfirstlane(threadIdx.x >> 6);
  const int l = threadIdx.x & 63;
  const int gr = (w >> 1) & 1, r = idx * 2 + (w & 1);
  char* ws = A.ws;
  unsigned* FL = (unsigned*)(ws + OFF_FL);
  unsigned* TMO = FL + FL_TMO;
  const __amdgpu_buffer_rsrc_t rsW = MKRS(ws + OFF_PW, SZ_PW);
  const __amdgpu_buffer_rsrc_t rsX = MKRS(ws + (L ? OFF_X3 : OFF_XA), SZ_X);
  const __amdgpu_buffer_rsrc_t rsY = L ? MKRS(A.out, SZ_X) : MKRS(ws + OFF_X2, SZ_X);
  volatile unsigned* sw = reinterpret_cast<volatile unsigned*>(smem) + (w >> 1) * 32;
  if (threadIdx.x < 128) reinterpret_cast<volatile unsigned*>(smem)[threadIdx.x] = 0u;
  __syncthreads();
  if ((EXP_OFF & 2) && L == 0 && w < 4) return;
  if ((EXP_OFF & 64) && L == 1 && w < 4) return;
  if ((EXP_OFF & 8) && L == 1 && w >= 4) return;
  unsigned tl = now10();
  if (w >= 4) {
    for (int s = gr + (L ? 0 : 2); s < NSEG; s += RG) {
      grp_wait<SLP_N, 8, GATE_N>((w & 1) == 0, sw, s + 1, FL + FL_FC + ((L * 64 + s) * 8 + (blockIdx.x & (NR_C - 1))) * 8 * 32, 64, TMO, tl);
      if (L == 0) tl = now10();
      const unsigned st = row_stats(LD128(rsX, l * 512, s * 32768 + r * 16));
      const unsigned long long v = ((unsigned long long)__float_as_uint(1.0f / hi16(st)) << 32) | ((st << 16) | 1u);
      if (l < NR_N) __hip_atomic_store((gu64*)(FL + FL_FN + ((L * 64 + s) * 8 + l) * 64) + r, v, RLX_AGENT);
      TS(7, s, 256 + gr * 128);
    }
    return;
  }
  if (L == 0) {
    for (int s = gr; s < NSEG; s += RG) ST128((v4u{0u, 0u, 0u, 0u}), rsY, l * 16, (s * SEG + r) * DM * 2);
    DRAIN();
    if (l < 8) set_flag(FL + FL_FZ + (2 * l + (idx >> 3)) * 32 + (idx & 7) * 4 + w);
  }
  for (int s = gr; s < NSEG; s += RG) {
    grp_wait<SLP_R, 4, GATE_R>((w & 1) == 0, sw, s + 1, FL + FL_FW + ((L * 64 + s) * 8 + (blockIdx.x & (NR_W - 1))) * 4 * 32, 16, TMO, tl);
    if (s >= NSEG - 6) tl = now10();
    TS(0, s, gr * RW * 64);
    TS(4, s, gr * RW * 64 + (RW - 1) * 64);
    v4u p[16];
    const v4u xr = LD128(rsX, l * 512, s * 32768 + r * 16);
#pragma unroll
    for (int k = 0; k < 16; ++k)
      p[k] = LD128(rsW, (l >> 2) * 128 + (l & 3) * 16, ((L * 64 + s) * 16 + k) * 32768 + (r >> 1) * 2048 + (r & 1) * 64);
    v4u xo;
#pragma unroll
    for (int e = 0; e < 4; ++e) {
      f32x2_t t[4];
#pragma unroll
      for (int k = 0; k < 4; ++k)
        t[k] = (f32x2_t{lo16(p[4 * k][e]), hi16(p[4 * k][e])} + f32x2_t{lo16(p[4 * k + 1][e]), hi16(p[4 * k + 1][e])}) +
               (f32x2_t{lo16(p[4 * k + 2][e]), hi16(p[4 * k + 2][e])} + f32x2_t{lo16(p[4 * k + 3][e]), hi16(p[4 * k + 3][e])});
      const f32x2_t u = (t[0] + t[1]) + (t[2] + t[3]);
      const unsigned a0 = pk2(u.x, u.y);
      xo[e] = pk2(lo16(xr[e]) + lo16(a0), hi16(xr[e]) + hi16(a0));
      if (L == 0) xo[e] = xo[e] ? xo[e] : 0x8000u;
    }
    ST128(xo, rsY, l * 16, (s * SEG + r) * DM * 2);
    TS(1, s, gr * RW * 64);
    TS(5, s, gr * RW * 64 + (RW - 1) * 64);
    if (L == 0) {
      const unsigned st = row_stats(xo);
      if (l < NR_R) __hip_atomic_store((gu32*)(FL + FL_FR + ((s * 8 + l) * 4 + (r >> 3)) * 32 + (r & 7)), st, RLX_AGENT);
    }
    TS(6, s, gr * RW * 64 + (RW - 1) * 64);
    TS(3, s, gr * RW * 64);
  }
}

constexpr int CUN = 2144;
__device__ __forceinline__ bf16x8 conv_bfrag(const unsigned* up, const unsigned sh) {
  const unsigned d0 = up[0], d1 = up[1], d2 = up[2], d3 = up[3], d4 = up[4];
  const v4u r = {__builtin_amdgcn_alignbit(d1, d0, sh), __builtin_amdgcn_alignbit(d2, d1, sh),
                 __builtin_amdgcn_alignbit(d3, d2, sh), __builtin_amdgcn_alignbit(d4, d3, sh)};
  return BF8(r);
}
__device__ __forceinline__ void role_conv(const MegaArgs& A, char* smem, const int cv) {
  u16* Kr = reinterpret_cast<u16*>(smem);
  u16* Uc = Kr + 8 * 2 * CKN;
  u16* Xr = Uc + 8 * CUN;
  u16* To = Uc + 8 * 2 * CUN;
  u16* Su = To + 32 * 8;
  u16* Sx = Su + 32 * 8;
  const int tid = threadIdx.x;
  const int w = __builtin_amdgcn_readfirstlane(tid >> 6);
  const int l = tid & 63, c = l & 15, g = l >> 4;
  const int d = 8 * cv + w;
  char* ws = A.ws;
  unsigned* FL = (unsigned*)(ws + OFF_FL);
  unsigned* TMO = FL + FL_TMO;
  u16* kr = Kr + w * 2 * CKN;
  float* prm = reinterpret_cast<float*>(kr);
  v4u tx[4];
  {
    const unsigned pa = *reinterpret_cast<const unsigned*>(A.logAre + d * NST + 2 * l);
    const unsigned pb = *reinterpret_cast<const unsigned*>(A.Aim + d * NST + 2 * l);
    const unsigned pc = *reinterpret_cast<const unsigned*>(A.Cre + d * NST + 2 * l);
    const unsigned pd = *reinterpret_cast<const unsigned*>(A.Cim + d * NST + 2 * l);
    const float dt = bfr(expf(bf2f(A.logdt[d])));
    if (w < 2) tx[0] = *reinterpret_cast<const v4u*>(A.x0 + (size_t)tid * DM + 8 * cv);
    if (l < 16) reinterpret_cast<unsigned*>(Uc + w * CUN)[l] = 0u;
    if (l < 32) reinterpret_cast<unsigned*>(Uc + w * CUN + 32 + L_SEQ)[l] = 0u;
    if (l < 32) reinterpret_cast<unsigned*>(Uc + w * CUN + 32 + 128)[l] = 0u;
    if (l < 48) {
      reinterpret_cast<v4u*>(kr + 1824)[l] = v4u{0u, 0u, 0u, 0u};
      reinterpret_cast<v4u*>(kr + CKN + 1824)[l] = v4u{0u, 0u, 0u, 0u};
    }
#pragma unroll
    for (int e = 0; e < 2; ++e) {
      const float ar = -bfr(expf(e ? hi16(pa) : lo16(pa))) * dt, ai = (e ? hi16(pb) : lo16(pb)) * dt;
      const float ex = expf(ar);
      float sn, cs;
      sincosf(ai, &sn, &cs);
      prm[2 * l + e] = ar;
      prm[NST + 2 * l + e] = ai;
      prm[2 * NST + 2 * l + e] = (e ? hi16(pc) : lo16(pc)) * dt;
      prm[3 * NST + 2 * l + e] = (e ? hi16(pd) : lo16(pd)) * dt;
      prm[4 * NST + 2 * l + e] = ex * cs;
      prm[5 * NST + 2 * l + e] = ex * sn;
    }
  }
  const float dsk = bf2f(A.Dskip[d]);
  const u16* ka = kr + (c >> 3) * CKN + 2080 - 16 * (c & 7) + 8 * g;
  const unsigned ush = (c & 1) * 16;
#define KFRAG(D0) (*reinterpret_cast<const bf16x8*>(ka - (D0)))
  f32x4 acc[16], far[9];
  v4u xf[3];
  const unsigned* ub1 = reinterpret_cast<const unsigned*>(Uc + w * CUN + (c & ~1) + 8 * g + 32);
  const __amdgpu_buffer_rsrc_t rz1 = MKRS(ws + OFF_XA, SZ_X);
  const int t0 = 16 * (4 * (g & 1) + (g >> 1)) + c;
  {
    const u16* uc = Uc + w * CUN;
    const unsigned* ub = reinterpret_cast<const unsigned*>(uc + (c & ~1) + 8 * g + 32);
    const __amdgpu_buffer_rsrc_t rz = MKRS(ws + OFF_XA, SZ_X);
    const __amdgpu_buffer_rsrc_t rst = MKRS(ws + OFF_GT, L_SEQ * 4);
#define TILE_OUT(T)                                                                            \
  {                                                                                            \
    const float u0 = bf2f(uc[32 + 128 * (T) + t0]), u1 = bf2f(uc[64 + 128 * (T) + t0]);        \
    const float a0 = bfr(bfr(y0) + bfr(u0 * dsk)), a1 = bfr(bfr(y1) + bfr(u1 * dsk));          \
    u16* xp = Xr + (128 * (T) + t0) * 8 + w;                                                   \
    xp[0] = f2bf(bf2f(xp[0]) + a0);                                                            \
    xp[256] = f2bf(bf2f(xp[256]) + a1);                                                        \
  }
#define CHUNK_ST(ck)                                                                           \
  {                                                                                            \
    const v4u o = *reinterpret_cast<const v4u*>(Xr + (64 * (ck) + l) * 8);                     \
    ST128(o, rz, (l >> 5) * 32768 + (l & 31) * 16, (2 * (ck) * 64 + cv) * 512);                \
  }
#define U_ROW(row)                                                                             \
  {                                                                                            \
    const unsigned st_ = __builtin_amdgcn_raw_buffer_load_b32(rst, (row) * 4, 0, AUX_SC1);     \
    const v4u xr_ = *reinterpret_cast<const v4u*>(Xr + (row) * 8);                             \
    const float mb_ = lo16(st_), ri_ = 1.0f / hi16(st_);                                       \
    _Pragma("unroll") for (int e = 0; e < 4; ++e) {                                            \
      const unsigned t_ = pk2(lo16(xr_[e]) - mb_, hi16(xr_[e]) - mb_);                         \
      const unsigned q_ = pk2(lo16(t_) * ri_, hi16(t_) * ri_);                                 \
      Uc[(2 * e) * CUN + 32 + (row)] = (u16)q_;                                                \
      Uc[(2 * e + 1) * CUN + 32 + (row)] = (u16)(q_ >> 16);                                    \
    }                                                                                          \
  }
    const unsigned* FS = FL + FL_FS + (blockIdx.x & 7) * 32 * 32;
    const int gi = 4 * (g * 8 + (l & 7));
    const int tl = 32 * ((l >> 3) & 1) + 8 * g + (l & 7);
    unsigned khl = 0u;
#pragma unroll 1
    for (int ps = 0; ps < 4; ++ps) {
      float v[32];
      {
        const f32x4 ar = *reinterpret_cast<const f32x4*>(prm + gi), ai = *reinterpret_cast<const f32x4*>(prm + NST + gi);
        const f32x4 cr = *reinterpret_cast<const f32x4*>(prm + 2 * NST + gi), ci = *reinterpret_cast<const f32x4*>(prm + 3 * NST + gi);
        const f32x4 wr = *reinterpret_cast<const f32x4*>(prm + 4 * NST + gi), wi = *reinterpret_cast<const f32x4*>(prm + 5 * NST + gi);
        const float p0 = (float)(64 * ps + 32 * ((l >> 3) & 1));
        float yr0[4], yi0[4];
#pragma unroll
        for (int k = 0; k < 4; ++k) {
          const float e = __expf(ar[k] * p0);
          float sn, cs;
          __sincosf(ai[k] * p0, &sn, &cs);
          const float zr = e * cs, zi = e * sn;
          yr0[k] = cr[k] * zr - ci[k] * zi;
          yi0[k] = cr[k] * zi + ci[k] * zr;
        }
        f32x2_t yra = {yr0[0], yr0[1]}, yia = {yi0[0], yi0[1]}, wra = {wr[0], wr[1]}, wia = {wi[0], wi[1]};
        f32x2_t yrb = {yr0[2], yr0[3]}, yib = {yi0[2], yi0[3]}, wrb = {wr[2], wr[3]}, wib = {wi[2], wi[3]};
#pragma unroll
        for (int i = 0; i < 32; ++i) {
          const f32x2_t sm2 = yra + yrb;
          v[i] = sm2.x + sm2.y;
          const f32x2_t ta = yra * wra - yia * wia, tb = yrb * wrb - yib * wib;
          yia = yra * wia + yia * wra;
          yib = yrb * wib + yib * wrb;
          yra = ta;
          yrb = tb;
        }
      }
#pragma unroll
      for (int i = 0; i < 16; ++i) v[i] = swap_add32(v[i], v[i + 16]);
#pragma unroll
      for (int i = 0; i < 8; ++i) {
        v[i] = swap_add16(v[i], v[i + 8]);
        v[i] += dppf<0xB1>(0.f, v[i]);
        v[i] += dppf<0x4E>(0.f, v[i]);
        v[i] += dppf<0x141>(0.f, v[i]);
      }
      float s0 = v[0], s1 = v[1], s2 = v[2], s3 = v[3], s4 = v[4], s5 = v[5], s6 = v[6], s7 = v[7];
      asm volatile("" : "+v"(s0), "+v"(s1), "+v"(s2), "+v"(s3), "+v"(s4), "+v"(s5), "+v"(s6), "+v"(s7));
      s0 = (l & 4) ? s4 : s0; s1 = (l & 4) ? s5 : s1; s2 = (l & 4) ? s6 : s2; s3 = (l & 4) ? s7 : s3;
      s0 = (l & 2) ? s2 : s0; s1 = (l & 2) ? s3 : s1;
      const float kv = 2.0f * ((l & 1) ? s1 : s0);
      khl = pk2(kv, 0.f);
      khl = pk2(kv, kv - lo16(khl));
      kr[2080 - 64 * ps - tl] = (u16)khl;
      kr[CKN + 2080 - 64 * ps - tl] = (u16)(khl >> 16);
      SST(ps);
      if (ps < 2) {
        if (ps == 0) {
          if (w < 2) {
            reinterpret_cast<v4u*>(Xr)[tid] = tx[0];
            (void)wait_n<2, 8>(FL + FL_F0 + ((blockIdx.x & 7) * 8 + 4 * w) * 32, 32, TMO);
            U_ROW(tid);
          }
          __syncthreads();
        }
        f32x4 a0t = {};
#pragma unroll
        for (int b = -1; b < 4; ++b) a0t = MFMA16(KFRAG(-32 * b), conv_bfrag(ub + 16 * b, ush), a0t);
        const float y0 = swap_add32(a0t[0], a0t[1]), y1 = swap_add32(a0t[2], a0t[3]);
        if ((g & 1) == ps) TILE_OUT(0);
        __syncthreads();
        if (w == ps) {
          CHUNK_ST(w);
          DRAIN();
          if (l < 16) set_flag(FL + FL_FC + (((2 * w + (l >> 3)) * 8 + (l & 7)) * 8 + (cv >> 3)) * 32 + (cv & 7));
        }
        SST(4 + ps);
      }
      if (ps == 2) {
        if (w >= 2) tx[0] = *reinterpret_cast<const v4u*>(A.x0 + (size_t)tid * DM + 8 * cv);
#pragma unroll
        for (int i = 1; i < 4; ++i) tx[i] = *reinterpret_cast<const v4u*>(A.x0 + (size_t)(tid + 512 * i) * DM + 8 * cv);
      }
    }
    if (w >= 2) reinterpret_cast<v4u*>(Xr)[tid] = tx[0];
#pragma unroll
    for (int i = 1; i < 4; ++i) reinterpret_cast<v4u*>(Xr)[tid + 512 * i] = tx[i];
    {
      unsigned spins = 0;
      while (!__all(__hip_atomic_load((gu32*)FS + (w + 8 * ((l >> 3) & 3)) * 32 + (l & 7), RLX_AGENT) != 0u)) {
        if (spin_timeout(++spins, TMO)) break;
        __builtin_amdgcn_s_sleep(4);
      }
      asm volatile("" ::: "memory");
    }
    if (w >= 2) U_ROW(tid);
#pragma unroll
    for (int i = 1; i < 4; ++i) U_ROW(tid + 512 * i);
    SST(6);
    (void)wait_n<8, 1>(FL + FL_KG + cv * 32 + (w >> 2), 1, TMO);
    SST(7);
    {
      const __amdgpu_buffer_rsrc_t rk = MKRS(ws + OFF_KF + (size_t)d * 2 * CKN * 2, 2 * CKN * 2);
      v4u t[8];
#pragma unroll
      for (int i = 0; i < 4; ++i) {
        t[i] = LD128(rk, (64 * i + l) * 16, 0);
        t[4 + i] = LD128(rk, (64 * i + l) * 16, CKN * 2);
      }
#pragma unroll
      for (int i = 0; i < 4; ++i)
        if (64 * i + l < 229) {
          reinterpret_cast<v4u*>(kr)[64 * i + l] = t[i];
          reinterpret_cast<v4u*>(kr + CKN)[64 * i + l] = t[4 + i];
        }
      kr[2080 - 192 - tl] = (u16)khl;
      kr[CKN + 2080 - 192 - tl] = (u16)(khl >> 16);
    }
    __syncthreads();
#define BULK(TA, TB, HOOK)                                                                      \
  {                                                                                            \
    const bf16x8 bm = conv_bfrag(ub - 16, ush);                                                \
    _Pragma("unroll") for (int T = (TA); T < (TB); ++T) acc[T] = MFMA16(KFRAG(128 * T + 32), bm, (f32x4{})); \
    _Pragma("unroll 1") for (int r = 0; r < 4; ++r) {                                          \
      bf16x8 bf[TB];                                                                           \
      _Pragma("unroll") for (int j = 0; j < (TB); ++j) bf[j] = conv_bfrag(ub + 16 * r + 64 * j, ush); \
      _Pragma("unroll") for (int e = 0; e < (TB); ++e) {                                       \
        const bf16x8 a = KFRAG(128 * e - 32 * r);                                              \
        _Pragma("unroll") for (int T = (e > (TA) ? e : (TA)); T < (TB); ++T) acc[T] = MFMA16(a, bf[T - e], acc[T]); \
      }                                                                                        \
      if (r == 0) { HOOK }                                                                     \
    }                                                                                          \
  }
    BULK(1, 4, );
#pragma unroll
    for (int T = 1; T < 4; ++T) {
      const float y0 = swap_add32(acc[T][0], acc[T][1]), y1 = swap_add32(acc[T][2], acc[T][3]);
      TILE_OUT(T);
    }
    __syncthreads();
    if (w < 6) CHUNK_ST(2 + w);
    SST(8);
    BULK(4, 7, if (w < 6) {
      DRAIN();
      if (l < 16) set_flag(FL + FL_FC + (((4 + 2 * w + (l >> 3)) * 8 + (l & 7)) * 8 + (cv >> 3)) * 32 + (cv & 7));
      if (w == 1) (void)(wait_n<SLP_V, 32>(FL + FL_FZ + (blockIdx.x & 7) * 64, 64, TMO));
    });
#pragma unroll
    for (int T = 4; T < 7; ++T) {
      const float y0 = swap_add32(acc[T][0], acc[T][1]), y1 = swap_add32(acc[T][2], acc[T][3]);
      TILE_OUT(T);
    }
    __syncthreads();
    if (w >= 2) {
      CHUNK_ST(6 + w);
#pragma unroll
      for (int i = 0; i < 3; ++i) xf[i] = *reinterpret_cast<const v4u*>(Xr + (64 * (8 + 3 * w + i) + l) * 8);
    }
    SST(10);
    {
      const bf16x8 bm = conv_bfrag(ub - 16, ush);
#pragma unroll
      for (int T = 7; T < 16; ++T) far[T - 7] = MFMA16(KFRAG(128 * T + 32), bm, (f32x4{}));
    }
#define FAR_HALF(r, E0, E1)                                                                     \
  {                                                                                            \
    bf16x8 bf[16];                                                                             \
    _Pragma("unroll") for (int j = (8 - (E1) > 0 ? 8 - (E1) : 0); j < 16 - (E0); ++j) {        \
      bf[j] = conv_bfrag(ub1 + 16 * (r) + 64 * j, ush);                                        \
      if (j == 7 && (E1) - (E0) > 8) __builtin_amdgcn_sched_barrier(0);                        \
    }                                                                                          \
    __builtin_amdgcn_sched_barrier(0);                                                         \
    _Pragma("unroll") for (int e = (E0); e < (E1); ++e) {                                      \
      const bf16x8 a = KFRAG(128 * e - 32 * (r));                                              \
      _Pragma("unroll") for (int T = (e > 7 ? e : 7); T < 16; ++T) far[T - 7] = MFMA16(a, bf[T - e], far[T - 7]); \
    }                                                                                          \
  }
#define FAR_PIECE(p)                                                                            \
  {                                                                                            \
    if ((p) < 8) {                                                                             \
      const int r_ = 2 + ((p) >> 2);                                                           \
      if (((p) & 3) == 0) FAR_HALF(r_, 0, 3) else if (((p) & 3) == 1) FAR_HALF(r_, 3, 6)       \
      else if (((p) & 3) == 2) FAR_HALF(r_, 6, 9) else FAR_HALF(r_, 9, 16)                     \
    } else if ((p) == 8) {                                                                     \
                     \
      u16* ucw = Uc + w * CUN + 32 + t0;                                                       \
      _Pragma("unroll") for (int T = 7; T < 16; ++T) {                                         \
        const float y0 = swap_add32(far[T - 7][0], far[T - 7][1]), y1 = swap_add32(far[T - 7][2], far[T - 7][3]); \
        const float u0 = bf2f(ucw[128 * T]), u1 = bf2f(ucw[128 * T + 32]);                     \
        const unsigned yy = pk2(y0, y1), uu = pk2(u0 * dsk, u1 * dsk);                         \
        const unsigned aa = pk2(lo16(yy) + lo16(uu), hi16(yy) + hi16(uu));                     \
        ucw[128 * T] = (u16)aa;                                                                \
        ucw[128 * T + 32] = (u16)(aa >> 16);                                                   \
      }                                                                                        \
    } else if (w >= 2) {                                                                       \
      int l = tid & 63;                                                                        \
      asm volatile("" : "+v"(l), "+v"(xf[0]), "+v"(xf[1]), "+v"(xf[2]));                       \
      if ((p) == 9) {                                                                          \
        _Pragma("unroll") for (int i = 0; i < 3; ++i) {                                        \
          const int ck = 8 + 3 * w + i;                                                        \
          const u16* ur = Uc + 32 + 64 * ck + l;                                               \
          v4u o;                                                                               \
          _Pragma("unroll") for (int e = 0; e < 4; ++e)                                        \
            o[e] = pk2(lo16(xf[i][e]) + bf2f(ur[(2 * e) * CUN]), hi16(xf[i][e]) + bf2f(ur[(2 * e + 1) * CUN])); \
          ST128(o, rz1, (l >> 5) * 32768 + (l & 31) * 16, (2 * ck * 64 + cv) * 512);           \
        }                                                                                      \
      } else {                                                                                 \
        DRAIN();                                                                               \
        if (l < 48) set_flag(FL + FL_FC + (((16 + 6 * w + (l >> 3)) * 8 + (l & 7)) * 8 + (cv >> 3)) * 32 + (cv & 7)); \
      }                                                                                        \
    }                                                                                          \
  }
#pragma unroll 1
    for (int p = 0; p < 2; ++p) {
      FAR_HALF(p, 0, 16);
      if (p == 0 && w >= 2) {
        DRAIN();
        if (l < 16) set_flag(FL + FL_FC + (((12 + 2 * w + (l >> 3)) * 8 + (l & 7)) * 8 + (cv >> 3)) * 32 + (cv & 7));
      }
    }
#undef BULK
    SST(9);
#undef TILE_OUT
#undef CHUNK_ST
#undef U_ROW
  }
  if (EXP_OFF & 4) {
    for (int p = 0; p < 11; ++p) {
      __syncthreads();
      FAR_PIECE(p);
    }
    return;
  }
  u16* uc = Uc + (8 + w) * CUN;
  const unsigned* ub = reinterpret_cast<const unsigned*>(uc + (c & ~1) + 8 * g + 32);
#pragma unroll
  for (int j = 0; j < 16; ++j) acc[j] = f32x4{};
  const __amdgpu_buffer_rsrc_t rx = MKRS(ws + OFF_X2, SZ_X), rz = MKRS(ws + OFF_X3, SZ_X);
  const __amdgpu_buffer_rsrc_t rf = MKRS(FL + FL_FR, 64 * 32 * 128);
  unsigned st = 0u;
  v2u xx = {0u, 0u};
#define X2_LOAD(ss, far)                                                                                                      \
  {                                                                                                                      \
    xx = LD64(rx, (l >> 1) * (DM * 2) + (l & 1) * 8, (far) ? (int)SZ_X : ((ss) * SEG * DM + 8 * cv) * 2);                 \
    st = __builtin_amdgcn_raw_buffer_load_b32(rf, (l >> 4) * 128 + ((l >> 1) & 7) * 4, ((ss) * 8 + (blockIdx.x & (NR_R - 1))) * 512, AUX_SC1); \
  }
  unsigned tl = now10();
  for (int s = 0; s < NSEG; ++s) {
    if (w == 1) {
      for (unsigned spins = 0; !__all(umin3(xx.x, xx.y, st) != 0u); ++spins) {
        if (spin_timeout(spins, TMO)) break;
        if (GATE_V && spins == 0 && !__any(st != 0u)) gate(tl);
        if (spins && !__all(st != 0u)) __builtin_amdgcn_s_sleep(SLP_V);
        X2_LOAD(s, __builtin_popcountll(__ballot(st != 0u)) < CVTHR);
      }
      tl = now10() + GATE_V;
      TS(0, s, 64);
      const float mb = lo16(st), ri = 1.0f / hi16(st);
      const unsigned t0 = pk2(lo16(xx.x) - mb, hi16(xx.x) - mb), t1 = pk2(lo16(xx.y) - mb, hi16(xx.y) - mb);
      *reinterpret_cast<v2u*>(Su + l * 4) = v2u{pk2(lo16(t0) * ri, hi16(t0) * ri), pk2(lo16(t1) * ri, hi16(t1) * ri)};
      *reinterpret_cast<v2u*>(Sx + l * 4) = xx;
    }
    const int q = s & 3;
    const u16* kq = ka + 32 * q;
    const bf16x8 a0 = *reinterpret_cast<const bf16x8*>(kq - 32), a1 = *reinterpret_cast<const bf16x8*>(kq);
    __syncthreads();
    TS(1, s, 0);
    if (s == 0)
      for (int x = l; x < CUN / 2; x += 64) reinterpret_cast<unsigned*>(uc)[x] = 0u;
    if (l < 32) uc[32 + 32 * s + l] = Su[l * 8 + w];
    asm volatile("s_waitcnt lgkmcnt(0)" ::: "memory");
    const bf16x8 bp = conv_bfrag(ub + 16 * (s - 1), ush);
    acc[0] = MFMA16(a0, bp, (q == 0 ? acc[1] : acc[0]));
    const f32x4 tmp = MFMA16(a1, conv_bfrag(ub + 16 * s, ush), acc[0]);
    float y0 = (q & 1) ? tmp[2] : tmp[0], y1 = (q & 1) ? tmp[3] : tmp[1];
    y0 = xsum32(y0);
    y1 = xsum32(y1);
    if (g == (q >> 1)) {
      const float u0 = bf2f(Su[c * 8 + w]), u1 = bf2f(Su[(16 + c) * 8 + w]);
      const float a0 = bfr(bfr(y0) + bfr(u0 * dsk)), a1 = bfr(bfr(y1) + bfr(u1 * dsk));
      To[c * 8 + w] = f2bf(bf2f(Sx[c * 8 + w]) + a0);
      To[(16 + c) * 8 + w] = f2bf(bf2f(Sx[(16 + c) * 8 + w]) + a1);
    }
    __syncthreads();
    if (w == 0 && l < 32) {
      const v4u o = *reinterpret_cast<const v4u*>(To + l * 8);
      ST128(o, rz, l * 16, (s * 64 + cv) * 512);
    }
    if (w == 1) X2_LOAD(s + 1, CVTHR > 0);
    const int left = 15 - (s >> 2);
#define PUSH(J0, SH)                                                                                              \
  if (left >= (J0)) {                                                                                             \
    _Pragma("unroll") for (int j = (J0); j < (J0) + 4 && j < 16; ++j)                                             \
      acc[j] = MFMA16(*reinterpret_cast<const bf16x8*>(kq - 32 - 128 * j), bp, (j + (SH) < 16 ? acc[(j + (SH)) & 15] : f32x4{})); \
  }
    TS(2, s, 0);
    if (w == 0) {
      DRAIN();
      if (l < NR_C) set_flag(FL + FL_FC + (((64 + s) * 8 + l) * 8 + (cv >> 3)) * 32 + (cv & 7));
    }
    TS(3, s, 0);
    if (q == 0) { PUSH(1, 1) PUSH(5, 1) PUSH(9, 1) PUSH(13, 1) }
    else { PUSH(1, 0) PUSH(5, 0) PUSH(9, 0) PUSH(13, 0) }
#undef PUSH
    MSTAMP();
    if (s >= CVPRE && s < 11 + CVPRE) FAR_PIECE(s - CVPRE);
  }
#undef KFRAG
#undef X2_LOAD
#undef FAR_PIECE
#undef FAR_HALF
}

__device__ __forceinline__ v4u ln_row_packed(const v4u xr) {
  float v[8];
#pragma unroll
  for (int e = 0; e < 4; ++e) { v[2 * e] = lo16(xr[e]); v[2 * e + 1] = hi16(xr[e]); }
  float s = ((v[0] + v[1]) + (v[2] + v[3])) + ((v[4] + v[5]) + (v[6] + v[7]));
  s = wave_sum(s);
  const float mean = s * (1.0f / DM);
  float q = 0.f;
#pragma unroll
  for (int j = 0; j < 8; ++j) { float t = v[j] - mean; q += t * t; }
  q = wave_sum(q);
  const float var = q * (1.0f / DM);
  const float mb = bfr(mean);
  const float sd = bfr(sqrtf(bfr(bfr(var) + 1e-5f)));
  const float ri = 1.0f / sd;
  v4u o;
#pragma unroll
  for (int e = 0; e < 4; ++e) {
    const unsigned t = pk2(v[2 * e] - mb, v[2 * e + 1] - mb);
    const float x0 = lo16(t), x1 = hi16(t);
    float q0 = x0 * ri, q1 = x1 * ri;
    q0 = __builtin_fmaf(__builtin_fmaf(-q0, sd, x0), ri, q0);
    q1 = __builtin_fmaf(__builtin_fmaf(-q1, sd, x1), ri, q1);
    o[e] = pk2(q0, q1);
  }
  return o;
}

__device__ __forceinline__ float bfr1(float x) { return lo16(pk2(x, x)); }
__device__ __forceinline__ void ln4_packed(v4u (&x)[4]) {
  f32x2_t v[16];
#pragma unroll
  for (int e = 0; e < 16; ++e) v[e] = f32x2_t{lo16(x[e >> 2][e & 3]), hi16(x[e >> 2][e & 3])};
  const f32x2_t t = (((v[0] + v[1]) + (v[2] + v[3])) + ((v[4] + v[5]) + (v[6] + v[7]))) +
                    (((v[8] + v[9]) + (v[10] + v[11])) + ((v[12] + v[13]) + (v[14] + v[15])));
  const float mean = red_mod4(t.x + t.y) * (1.0f / DM);
  const float mb = bfr1(mean);
  f32x2_t q0 = {0.f, 0.f}, q1 = {0.f, 0.f};
#pragma unroll
  for (int e = 0; e < 16; e += 2) {
    v[e] -= mb;
    v[e + 1] -= mb;
    q0 += v[e] * v[e];
    q1 += v[e + 1] * v[e + 1];
  }
  q0 += q1;
  const float dm = mean - mb;
  const float var = fmaxf(red_mod4(q0.x + q0.y) * (1.0f / DM) - dm * dm, 0.f);
  const float sd = bfr1(sqrtf(bfr1(bfr1(var) + 1e-5f)));
  const float ri = 1.0f / sd;
#pragma unroll
  for (int e = 0; e < 16; ++e) {
    const unsigned t2 = pk2(v[e].x, v[e].y);
    const f32x2_t q = f32x2_t{lo16(t2), hi16(t2)} * ri;
    x[e >> 2][e & 3] = pk2(q.x, q.y);
  }
}

#define Q_ASM \
  "s_mov_b32 s74, 0\n" \
  "s_mov_b32 s76, 0\n" \
  "s_mov_b32 s78, 0x8000\n" \
  "s_mov_b32 s79, 512\n" \
  "s_branch .LCQ_POLL_%=\n" \
  ".LCQ_TOP_%=:\n" \
  "s_barrier\n" \
  "s_sub_i32 s78, 0, s78\n" \
  "v_add_u32 v0, s78, v0\n" \
  "ds_read_b128 v[18:21], v0 offset:0\n" \
  "ds_read_b128 v[22:25], v0 offset:2048\n" \
  "ds_read_b128 v[26:29], v0 offset:4096\n" \
  "ds_read2_b32 v[6:7], v3 offset0:16 offset1:48\n" \
  "ds_read_b128 v[10:13], v4 offset:256\n" \
  "ds_read_b128 v[14:17], v4 offset:320\n" \
  "ds_read_b32 v8, v3 offset:384\n" \
  "s_sub_i32 s79, 0, s79\n" \
  "s_add_u32 s69, s69, s79\n" \
  "s_add_u32 s62, s62, 0x800\n" \
  "s_addc_u32 s63, s63, 0\n" \
  "s_add_u32 s71, s71, 0x2000\n" \
  "s_mov_b32 s73, s75\n" \
  "v_lshrrev_b32 v9, 1, v2\n" \
  "v_lshrrev_b32 v5, 7, v2\n" \
  "v_and_b32 v9, 0xf8, v9\n" \
  "v_and_or_b32 v9, v5, 4, v9\n" \
  "s_setprio 3\n" \
  "ds_read_b128 v[30:33], v0 offset:6144\n" \
  "s_waitcnt lgkmcnt(7)\n" \
  "v_mfma_f32_16x16x32_bf16 v[34:37], v[64:67], v[18:21], 0\n" \
  "v_mfma_f32_16x16x32_bf16 v[38:41], v[128:131], v[18:21], 0\n" \
  "v_mfma_f32_16x16x32_bf16 v[42:45], v[18:21], v[192:195], 0\n" \
  "s_cmp_eq_u32 s73, 0\n" \
  "s_cbranch_scc1 .LCQ_X1_%=\n" \
  "s_mov_b32 m0, s69\n" \
  "s_nop 0\n" \
  "global_load_lds_dword v9, s[62:63] sc1\n" \
  ".LCQ_X1_%=:\n" \
  "ds_read_b128 v[18:21], v0 offset:8192\n" \
  "s_waitcnt lgkmcnt(7)\n" \
  "v_mfma_f32_16x16x32_bf16 v[34:37], v[68:71], v[22:25], v[34:37]\n" \
  "s_waitcnt lgkmcnt(4)\n" \
  "v_fma_f32 v48, -v6, v10, v48\n" \
  "v_fma_f32 v49, -v6, v11, v49\n" \
  "v_mfma_f32_16x16x32_bf16 v[38:41], v[132:135], v[22:25], v[38:41]\n" \
  "v_fma_f32 v50, -v6, v12, v50\n" \
  "v_fma_f32 v51, -v6, v13, v51\n" \
  "v_mfma_f32_16x16x32_bf16 v[42:45], v[22:25], v[196:199], v[42:45]\n" \
  "ds_read_b128 v[10:13], v4 offset:64\n" \
  "s_waitcnt lgkmcnt(4)\n" \
  "v_fma_f32 v54, -v6, v14, v54\n" \
  "v_fma_f32 v55, -v6, v15, v55\n" \
  "s_cmp_eq_u32 s73, 0\n" \
  "s_cbranch_scc1 .LCQ_X2_%=\n" \
  "s_mov_b32 m0, s68\n" \
  "s_xor_b32 s68, s68, 0x8000\n" \
  "global_load_lds_dwordx4 v2, s[60:61] offset:0 sc1\n" \
  "global_load_lds_dwordx4 v2, s[60:61] offset:1024 sc1\n" \
  ".LCQ_X2_%=:\n" \
  "ds_read_b128 v[22:25], v0 offset:10240\n" \
  "v_mfma_f32_16x16x32_bf16 v[34:37], v[72:75], v[26:29], v[34:37]\n" \
  "v_fma_f32 v56, -v6, v16, v56\n" \
  "v_fma_f32 v57, -v6, v17, v57\n" \
  "v_mfma_f32_16x16x32_bf16 v[38:41], v[136:139], v[26:29], v[38:41]\n" \
  "ds_read_b128 v[14:17], v4 offset:192\n" \
  "v_mul_f32 v48, v48, v7\n" \
  "v_mul_f32 v49, v49, v7\n" \
  "v_mfma_f32_16x16x32_bf16 v[42:45], v[26:29], v[200:203], v[42:45]\n" \
  "v_mul_f32 v50, v50, v7\n" \
  "v_mul_f32 v51, v51, v7\n" \
  "s_cmp_eq_u32 s73, 0\n" \
  "s_cbranch_scc1 .LCQ_X3_%=\n" \
  "global_load_lds_dwordx4 v2, s[60:61] offset:2048 sc1\n" \
  "global_load_lds_dwordx4 v2, s[60:61] offset:3072 sc1\n" \
  "s_add_u32 s60, s60, 0x8000\n" \
  "s_addc_u32 s61, s61, 0\n" \
  ".LCQ_X3_%=:\n" \
  "ds_read_b128 v[26:29], v0 offset:12288\n" \
  "s_waitcnt lgkmcnt(5)\n" \
  "v_mfma_f32_16x16x32_bf16 v[34:37], v[76:79], v[30:33], v[34:37]\n" \
  "v_cvt_pk_bf16_f32 v48, v48, v49\n" \
  "v_cvt_pk_bf16_f32 v49, v50, v51\n" \
  "v_mfma_f32_16x16x32_bf16 v[38:41], v[140:143], v[30:33], v[38:41]\n" \
  "v_mul_f32 v54, v54, v7\n" \
  "v_mul_f32 v55, v55, v7\n" \
  "v_mfma_f32_16x16x32_bf16 v[42:45], v[30:33], v[204:207], v[42:45]\n" \
  "v_mul_f32 v56, v56, v7\n" \
  "v_mul_f32 v57, v57, v7\n" \
  "s_cmp_eq_u32 s73, 0\n" \
  "s_cbranch_scc1 .LCQ_X4_%=\n" \
  "s_add_u32 s83, s71, 0x2000\n" \
  "v_bfe_u32 v5, v2, 2, 5\n" \
  "buffer_load_dword v5, v5, s[52:55], s83 offen sc1\n" \
  ".LCQ_X4_%=:\n" \
  "s_cmp_eq_u32 s74, 0\n" \
  "s_cbranch_scc1 .LCQ_NPA_%=\n" \
  "s_waitcnt vmcnt(6)\n" \
  "s_cmp_lg_u32 s73, 0\n" \
  "s_cbranch_scc1 .LCQ_PA_%=\n" \
  "s_waitcnt vmcnt(0)\n" \
  ".LCQ_PA_%=:\n" \
  "v_mov_b32 v46, s81\n" \
  "v_mov_b32 v47, 1\n" \
  "s_mov_b64 exec, 1\n" \
  "ds_add_rtn_u32 v46, v46, v47\n" \
  "s_mov_b64 exec, -1\n" \
  "s_waitcnt lgkmcnt(0)\n" \
  "v_readfirstlane_b32 s83, v46\n" \
  "s_mov_b32 s74, 0\n" \
  "s_and_b32 s83, s83, 7\n" \
  "s_cmp_eq_u32 s83, 7\n" \
  "s_cbranch_scc0 .LCQ_F5_%=\n" \
  "s_mov_b64 exec, 1\n" \
  "buffer_store_dword v47, off, s[56:59], s80 sc1\n" \
  "s_mov_b64 exec, -1\n" \
  ".LCQ_F5_%=:\n" \
  ".LCQ_NPA_%=:\n" \
  "ds_read_b128 v[30:33], v0 offset:14336\n" \
  "s_waitcnt lgkmcnt(5)\n" \
  "v_mfma_f32_16x16x32_bf16 v[34:37], v[80:83], v[18:21], v[34:37]\n" \
  "v_cvt_pk_bf16_f32 v54, v54, v55\n" \
  "v_cvt_pk_bf16_f32 v55, v56, v57\n" \
  "v_mfma_f32_16x16x32_bf16 v[38:41], v[144:147], v[18:21], v[38:41]\n" \
  "s_waitcnt lgkmcnt(4)\n" \
  "v_fma_f32 v60, -v10, v8, v60\n" \
  "v_fma_f32 v61, -v11, v8, v61\n" \
  "v_mfma_f32_16x16x32_bf16 v[42:45], v[18:21], v[208:211], v[42:45]\n" \
  "v_fma_f32 v62, -v12, v8, v62\n" \
  "v_fma_f32 v63, -v13, v8, v63\n" \
  "ds_read_b128 v[18:21], v0 offset:16384\n" \
  "s_waitcnt lgkmcnt(4)\n" \
  "v_mfma_f32_16x16x32_bf16 v[34:37], v[84:87], v[22:25], v[34:37]\n" \
  "v_permlane16_swap_b32 v46, v48\n" \
  "v_mfma_f32_16x16x32_bf16 v[38:41], v[148:151], v[22:25], v[38:41]\n" \
  "v_permlane16_swap_b32 v47, v49\n" \
  "v_mfma_f32_16x16x32_bf16 v[42:45], v[22:25], v[212:215], v[42:45]\n" \
  "v_permlane16_swap_b32 v52, v54\n" \
  "ds_read_b128 v[22:25], v0 offset:18432\n" \
  "s_waitcnt lgkmcnt(3)\n" \
  "v_mfma_f32_16x16x32_bf16 v[34:37], v[88:91], v[26:29], v[34:37]\n" \
  "v_permlane16_swap_b32 v53, v55\n" \
  "v_mfma_f32_16x16x32_bf16 v[38:41], v[152:155], v[26:29], v[38:41]\n" \
  "v_mul_f32 v60, v60, v14\n" \
  "v_mul_f32 v61, v61, v15\n" \
  "v_mfma_f32_16x16x32_bf16 v[42:45], v[26:29], v[216:219], v[42:45]\n" \
  "v_mul_f32 v62, v62, v16\n" \
  "v_mul_f32 v63, v63, v17\n" \
  "ds_read_b128 v[26:29], v0 offset:20480\n" \
  "s_waitcnt lgkmcnt(3)\n" \
  "v_mfma_f32_16x16x32_bf16 v[34:37], v[92:95], v[30:33], v[34:37]\n" \
  "v_cvt_pk_bf16_f32 v60, v60, v61\n" \
  "v_cvt_pk_bf16_f32 v61, v62, v63\n" \
  "v_mfma_f32_16x16x32_bf16 v[38:41], v[156:159], v[30:33], v[38:41]\n" \
  "v_mov_b64 v[10:11], 0\n" \
  "v_lshrrev_b32 v9, 1, v2\n" \
  "v_mfma_f32_16x16x32_bf16 v[42:45], v[30:33], v[220:223], v[42:45]\n" \
  "s_cmp_eq_u32 s76, 0\n" \
  "s_cbranch_scc1 .LCQ_NST_%=\n" \
  "s_add_u32 s83, s65, 0x2000\n" \
  "buffer_store_dwordx4 v[52:55], v1, s[40:43], s83 offen sc1\n" \
  "buffer_store_dwordx4 v[58:61], v2, s[44:47], s66 offen sc1\n" \
  "buffer_store_dwordx4 v[46:49], v1, s[40:43], s65 offen sc1\n" \
  "buffer_store_dwordx2 v[10:11], v9, s[48:51], s67 offen sc1\n" \
  "s_add_u32 s65, s65, 0x80000\n" \
  "s_add_u32 s66, s66, 0x40000\n" \
  "s_add_u32 s67, s67, 0x10000\n" \
  "s_mov_b32 s80, s72\n" \
  "s_add_u32 s72, s72, 0x1000\n" \
  "s_mov_b32 s81, s70\n" \
  "s_xor_b32 s70, s70, 4\n" \
  "s_mov_b32 s74, 1\n" \
  ".LCQ_NST_%=:\n" \
  "s_setprio 2\n" \
  "ds_read_b128 v[30:33], v0 offset:22528\n" \
  "s_waitcnt lgkmcnt(3)\n" \
  "v_mfma_f32_16x16x32_bf16 v[34:37], v[96:99], v[18:21], v[34:37]\n" \
  "v_mfma_f32_16x16x32_bf16 v[38:41], v[160:163], v[18:21], v[38:41]\n" \
  "v_mfma_f32_16x16x32_bf16 v[42:45], v[18:21], v[224:227], v[42:45]\n" \
  "ds_read_b128 v[18:21], v0 offset:24576\n" \
  "s_waitcnt lgkmcnt(3)\n" \
  "v_mfma_f32_16x16x32_bf16 v[34:37], v[100:103], v[22:25], v[34:37]\n" \
  "v_lshrrev_b32 v10, 1, v2\n" \
  "v_lshrrev_b32 v11, 7, v2\n" \
  "v_mfma_f32_16x16x32_bf16 v[38:41], v[164:167], v[22:25], v[38:41]\n" \
  "v_and_b32 v10, 0xf8, v10\n" \
  "v_and_or_b32 v10, v11, 4, v10\n" \
  "v_mfma_f32_16x16x32_bf16 v[42:45], v[22:25], v[228:231], v[42:45]\n" \
  "v_add_u32 v3, s79, v3\n" \
  "v_add_u32 v4, s79, v4\n" \
  "s_cmp_lg_u32 s73, 0\n" \
  "s_cbranch_scc1 .LCQ_NSD_%=\n" \
  "s_mov_b32 m0, s69\n" \
  "s_nop 0\n" \
  "global_load_lds_dword v10, s[62:63] sc1\n" \
  ".LCQ_NSD_%=:\n" \
  "ds_read_b128 v[22:25], v0 offset:26624\n" \
  "s_waitcnt lgkmcnt(3)\n" \
  "v_mfma_f32_16x16x32_bf16 v[34:37], v[104:107], v[26:29], v[34:37]\n" \
  "v_mfma_f32_16x16x32_bf16 v[38:41], v[168:171], v[26:29], v[38:41]\n" \
  "v_mfma_f32_16x16x32_bf16 v[42:45], v[26:29], v[232:235], v[42:45]\n" \
  "ds_read_b128 v[26:29], v0 offset:28672\n" \
  "s_waitcnt lgkmcnt(3)\n" \
  "v_mfma_f32_16x16x32_bf16 v[34:37], v[108:111], v[30:33], v[34:37]\n" \
  "v_mfma_f32_16x16x32_bf16 v[38:41], v[172:175], v[30:33], v[38:41]\n" \
  "v_mfma_f32_16x16x32_bf16 v[42:45], v[30:33], v[236:239], v[42:45]\n" \
  "ds_read_b128 v[30:33], v0 offset:30720\n" \
  "s_waitcnt lgkmcnt(3)\n" \
  "v_mfma_f32_16x16x32_bf16 v[34:37], v[112:115], v[18:21], v[34:37]\n" \
  "v_mfma_f32_16x16x32_bf16 v[38:41], v[176:179], v[18:21], v[38:41]\n" \
  "v_mfma_f32_16x16x32_bf16 v[42:45], v[18:21], v[240:243], v[42:45]\n" \
  "ds_read_b128 v[18:21], v0 offset:256\n" \
  "s_waitcnt lgkmcnt(3)\n" \
  "v_mfma_f32_16x16x32_bf16 v[34:37], v[116:119], v[22:25], v[34:37]\n" \
  "v_mfma_f32_16x16x32_bf16 v[38:41], v[180:183], v[22:25], v[38:41]\n" \
  "v_mfma_f32_16x16x32_bf16 v[42:45], v[22:25], v[244:247], v[42:45]\n" \
  "ds_read_b128 v[22:25], v0 offset:2304\n" \
  "s_waitcnt lgkmcnt(3)\n" \
  "v_mfma_f32_16x16x32_bf16 v[34:37], v[120:123], v[26:29], v[34:37]\n" \
  "v_mfma_f32_16x16x32_bf16 v[38:41], v[184:187], v[26:29], v[38:41]\n" \
  "v_mfma_f32_16x16x32_bf16 v[42:45], v[26:29], v[248:251], v[42:45]\n" \
  "ds_read_b128 v[26:29], v0 offset:4352\n" \
  "s_waitcnt lgkmcnt(3)\n" \
  "v_mfma_f32_16x16x32_bf16 v[34:37], v[124:127], v[30:33], v[34:37]\n" \
  "v_mfma_f32_16x16x32_bf16 v[38:41], v[188:191], v[30:33], v[38:41]\n" \
  "v_mfma_f32_16x16x32_bf16 v[42:45], v[30:33], v[252:255], v[42:45]\n" \
  "s_setprio 1\n" \
  "ds_read_b128 v[30:33], v0 offset:6400\n" \
  "s_waitcnt lgkmcnt(3)\n" \
  "v_mfma_f32_16x16x32_bf16 v[48:51], v[64:67], v[18:21], 0\n" \
  "v_mfma_f32_16x16x32_bf16 v[54:57], v[128:131], v[18:21], 0\n" \
  "v_mfma_f32_16x16x32_bf16 v[60:63], v[18:21], v[192:195], 0\n" \
  "ds_read_b128 v[18:21], v0 offset:8448\n" \
  "s_waitcnt lgkmcnt(3)\n" \
  "v_mfma_f32_16x16x32_bf16 v[48:51], v[68:71], v[22:25], v[48:51]\n" \
  "v_mfma_f32_16x16x32_bf16 v[54:57], v[132:135], v[22:25], v[54:57]\n" \
  "v_mfma_f32_16x16x32_bf16 v[60:63], v[22:25], v[196:199], v[60:63]\n" \
  "ds_read_b128 v[22:25], v0 offset:10496\n" \
  "s_waitcnt lgkmcnt(3)\n" \
  "v_mfma_f32_16x16x32_bf16 v[48:51], v[72:75], v[26:29], v[48:51]\n" \
  "v_mfma_f32_16x16x32_bf16 v[54:57], v[136:139], v[26:29], v[54:57]\n" \
  "v_mfma_f32_16x16x32_bf16 v[60:63], v[26:29], v[200:203], v[60:63]\n" \
  "v_lshrrev_b32 v9, 2, v2\n" \
  "v_add_u32 v9, s69, v9\n" \
  "s_waitcnt vmcnt(4)\n" \
  "s_and_b32 s76, s76, s73\n" \
  "s_cbranch_scc1 .LCQ_NPB_%=\n" \
  "s_waitcnt vmcnt(0)\n" \
  "s_cmp_eq_u32 s74, 0\n" \
  "s_cbranch_scc1 .LCQ_PB_%=\n" \
  "v_mov_b32 v10, s81\n" \
  "v_mov_b32 v11, 1\n" \
  "s_mov_b64 exec, 1\n" \
  "ds_add_rtn_u32 v10, v10, v11\n" \
  "s_mov_b64 exec, -1\n" \
  ".LCQ_PB_%=:\n" \
  "s_cmp_lg_u32 s73, 0\n" \
  "s_cbranch_scc1 .LCQ_NPB_%=\n" \
  "v_bfe_u32 v5, v2, 2, 5\n" \
  "buffer_load_dword v5, v5, s[52:55], s71 offen sc1\n" \
  ".LCQ_NPB_%=:\n" \
  "ds_read_b32 v9, v9\n" \
  "ds_read_b128 v[26:29], v0 offset:12544\n" \
  "s_waitcnt lgkmcnt(4)\n" \
  "v_mfma_f32_16x16x32_bf16 v[48:51], v[76:79], v[30:33], v[48:51]\n" \
  "v_mfma_f32_16x16x32_bf16 v[54:57], v[140:143], v[30:33], v[54:57]\n" \
  "v_mfma_f32_16x16x32_bf16 v[60:63], v[30:33], v[204:207], v[60:63]\n" \
  "ds_read_b128 v[30:33], v0 offset:14592\n" \
  "s_waitcnt lgkmcnt(4)\n" \
  "v_mfma_f32_16x16x32_bf16 v[48:51], v[80:83], v[18:21], v[48:51]\n" \
  "v_mfma_f32_16x16x32_bf16 v[54:57], v[144:147], v[18:21], v[54:57]\n" \
  "v_mfma_f32_16x16x32_bf16 v[60:63], v[18:21], v[208:211], v[60:63]\n" \
  "s_waitcnt lgkmcnt(2)\n" \
  "s_andn2_b32 s83, s74, s76\n" \
  "s_cbranch_scc0 .LCQ_NPC_%=\n" \
  "v_readfirstlane_b32 s83, v10\n" \
  "s_mov_b32 s74, 0\n" \
  "s_and_b32 s83, s83, 7\n" \
  "s_cmp_eq_u32 s83, 7\n" \
  "s_cbranch_scc0 .LCQ_F6_%=\n" \
  "s_mov_b64 exec, 1\n" \
  "buffer_store_dword v11, off, s[56:59], s80 sc1\n" \
  "s_mov_b64 exec, -1\n" \
  ".LCQ_F6_%=:\n" \
  ".LCQ_NPC_%=:\n" \
  "v_cmp_eq_u32 vcc, 0, v9\n" \
  "s_cmp_eq_u64 vcc, 0\n" \
  "s_cselect_b32 s77, 1, 0\n" \
  "s_cbranch_scc0 .LCQ_NOE_%=\n" \
  "ds_read2_b32 v[6:7], v3 offset0:0 offset1:32\n" \
  "ds_read_b128 v[10:13], v4 offset:256\n" \
  "ds_read_b128 v[14:17], v4 offset:320\n" \
  "ds_read_b32 v8, v3 offset:384\n" \
  "ds_read_b128 v[18:21], v0 offset:16640\n" \
  "v_mfma_f32_16x16x32_bf16 v[48:51], v[84:87], v[22:25], v[48:51]\n" \
  "v_mfma_f32_16x16x32_bf16 v[54:57], v[148:151], v[22:25], v[54:57]\n" \
  "v_mfma_f32_16x16x32_bf16 v[60:63], v[22:25], v[212:215], v[60:63]\n" \
  "ds_read_b128 v[22:25], v0 offset:18688\n" \
  "s_waitcnt lgkmcnt(7)\n" \
  "v_mfma_f32_16x16x32_bf16 v[48:51], v[88:91], v[26:29], v[48:51]\n" \
  "s_waitcnt lgkmcnt(4)\n" \
  "v_fma_f32 v34, -v6, v10, v34\n" \
  "v_fma_f32 v35, -v6, v11, v35\n" \
  "v_mfma_f32_16x16x32_bf16 v[54:57], v[152:155], v[26:29], v[54:57]\n" \
  "v_fma_f32 v36, -v6, v12, v36\n" \
  "v_fma_f32 v37, -v6, v13, v37\n" \
  "v_mfma_f32_16x16x32_bf16 v[60:63], v[26:29], v[216:219], v[60:63]\n" \
  "ds_read_b128 v[10:13], v4 offset:0\n" \
  "s_waitcnt lgkmcnt(4)\n" \
  "v_fma_f32 v38, -v6, v14, v38\n" \
  "v_fma_f32 v39, -v6, v15, v39\n" \
  "ds_read_b128 v[26:29], v0 offset:20736\n" \
  "v_mfma_f32_16x16x32_bf16 v[48:51], v[92:95], v[30:33], v[48:51]\n" \
  "v_fma_f32 v40, -v6, v16, v40\n" \
  "v_fma_f32 v41, -v6, v17, v41\n" \
  "v_mfma_f32_16x16x32_bf16 v[54:57], v[156:159], v[30:33], v[54:57]\n" \
  "ds_read_b128 v[14:17], v4 offset:128\n" \
  "v_mul_f32 v34, v34, v7\n" \
  "v_mul_f32 v35, v35, v7\n" \
  "v_mfma_f32_16x16x32_bf16 v[60:63], v[30:33], v[220:223], v[60:63]\n" \
  "v_mul_f32 v36, v36, v7\n" \
  "v_mul_f32 v37, v37, v7\n" \
  "s_setprio 0\n" \
  "ds_read_b128 v[30:33], v0 offset:22784\n" \
  "s_waitcnt lgkmcnt(5)\n" \
  "v_mfma_f32_16x16x32_bf16 v[48:51], v[96:99], v[18:21], v[48:51]\n" \
  "v_cvt_pk_bf16_f32 v46, v34, v35\n" \
  "v_cvt_pk_bf16_f32 v47, v36, v37\n" \
  "v_mfma_f32_16x16x32_bf16 v[54:57], v[160:163], v[18:21], v[54:57]\n" \
  "v_mul_f32 v38, v38, v7\n" \
  "v_mul_f32 v39, v39, v7\n" \
  "v_mfma_f32_16x16x32_bf16 v[60:63], v[18:21], v[224:227], v[60:63]\n" \
  "v_mul_f32 v40, v40, v7\n" \
  "v_mul_f32 v41, v41, v7\n" \
  "ds_read_b128 v[18:21], v0 offset:24832\n" \
  "s_waitcnt lgkmcnt(5)\n" \
  "v_mfma_f32_16x16x32_bf16 v[48:51], v[100:103], v[22:25], v[48:51]\n" \
  "v_cvt_pk_bf16_f32 v52, v38, v39\n" \
  "v_cvt_pk_bf16_f32 v53, v40, v41\n" \
  "v_mfma_f32_16x16x32_bf16 v[54:57], v[164:167], v[22:25], v[54:57]\n" \
  "s_waitcnt lgkmcnt(4)\n" \
  "v_fma_f32 v42, -v10, v8, v42\n" \
  "v_fma_f32 v43, -v11, v8, v43\n" \
  "v_mfma_f32_16x16x32_bf16 v[60:63], v[22:25], v[228:231], v[60:63]\n" \
  "v_fma_f32 v44, -v12, v8, v44\n" \
  "v_fma_f32 v45, -v13, v8, v45\n" \
  "ds_read_b128 v[22:25], v0 offset:26880\n" \
  "s_waitcnt lgkmcnt(4)\n" \
  "v_mfma_f32_16x16x32_bf16 v[48:51], v[104:107], v[26:29], v[48:51]\n" \
  "s_waitcnt lgkmcnt(3)\n" \
  "v_mul_f32 v42, v42, v14\n" \
  "v_mul_f32 v43, v43, v15\n" \
  "v_mfma_f32_16x16x32_bf16 v[54:57], v[168:171], v[26:29], v[54:57]\n" \
  "v_mul_f32 v44, v44, v16\n" \
  "v_mul_f32 v45, v45, v17\n" \
  "v_mfma_f32_16x16x32_bf16 v[60:63], v[26:29], v[232:235], v[60:63]\n" \
  "v_cvt_pk_bf16_f32 v58, v42, v43\n" \
  "v_cvt_pk_bf16_f32 v59, v44, v45\n" \
  "ds_read_b128 v[26:29], v0 offset:28928\n" \
  "s_waitcnt lgkmcnt(3)\n" \
  "v_mfma_f32_16x16x32_bf16 v[48:51], v[108:111], v[30:33], v[48:51]\n" \
  "v_mfma_f32_16x16x32_bf16 v[54:57], v[172:175], v[30:33], v[54:57]\n" \
  "v_mfma_f32_16x16x32_bf16 v[60:63], v[30:33], v[236:239], v[60:63]\n" \
  "ds_read_b128 v[30:33], v0 offset:30976\n" \
  "s_waitcnt lgkmcnt(3)\n" \
  "v_mfma_f32_16x16x32_bf16 v[48:51], v[112:115], v[18:21], v[48:51]\n" \
  "v_mfma_f32_16x16x32_bf16 v[54:57], v[176:179], v[18:21], v[54:57]\n" \
  "v_mfma_f32_16x16x32_bf16 v[60:63], v[18:21], v[240:243], v[60:63]\n" \
  "s_waitcnt vmcnt(0)\n" \
  "s_cmp_eq_u32 s74, 0\n" \
  "s_cbranch_scc1 .LCQ_G7_%=\n" \
  "v_mov_b32 v6, s81\n" \
  "v_mov_b32 v7, 1\n" \
  "s_mov_b64 exec, 1\n" \
  "ds_add_rtn_u32 v6, v6, v7\n" \
  "s_mov_b64 exec, -1\n" \
  ".LCQ_G7_%=:\n" \
  "s_waitcnt lgkmcnt(2)\n" \
  "v_mfma_f32_16x16x32_bf16 v[48:51], v[116:119], v[22:25], v[48:51]\n" \
  "v_mfma_f32_16x16x32_bf16 v[54:57], v[180:183], v[22:25], v[54:57]\n" \
  "v_mfma_f32_16x16x32_bf16 v[60:63], v[22:25], v[244:247], v[60:63]\n" \
  "s_waitcnt lgkmcnt(1)\n" \
  "v_mfma_f32_16x16x32_bf16 v[48:51], v[120:123], v[26:29], v[48:51]\n" \
  "v_mfma_f32_16x16x32_bf16 v[54:57], v[184:187], v[26:29], v[54:57]\n" \
  "v_mfma_f32_16x16x32_bf16 v[60:63], v[26:29], v[248:251], v[60:63]\n" \
  "s_waitcnt lgkmcnt(0)\n" \
  "v_mfma_f32_16x16x32_bf16 v[48:51], v[124:127], v[30:33], v[48:51]\n" \
  "v_mfma_f32_16x16x32_bf16 v[54:57], v[188:191], v[30:33], v[54:57]\n" \
  "v_mfma_f32_16x16x32_bf16 v[60:63], v[30:33], v[252:255], v[60:63]\n" \
  "s_branch .LCQ_END2_%=\n" \
  ".LCQ_NOE_%=:\n" \
  "ds_read_b128 v[18:21], v0 offset:16640\n" \
  "v_mfma_f32_16x16x32_bf16 v[48:51], v[84:87], v[22:25], v[48:51]\n" \
  "v_mfma_f32_16x16x32_bf16 v[54:57], v[148:151], v[22:25], v[54:57]\n" \
  "v_mfma_f32_16x16x32_bf16 v[60:63], v[22:25], v[212:215], v[60:63]\n" \
  "ds_read_b128 v[22:25], v0 offset:18688\n" \
  "s_waitcnt lgkmcnt(3)\n" \
  "v_mfma_f32_16x16x32_bf16 v[48:51], v[88:91], v[26:29], v[48:51]\n" \
  "v_mfma_f32_16x16x32_bf16 v[54:57], v[152:155], v[26:29], v[54:57]\n" \
  "v_mfma_f32_16x16x32_bf16 v[60:63], v[26:29], v[216:219], v[60:63]\n" \
  "ds_read_b128 v[26:29], v0 offset:20736\n" \
  "s_waitcnt lgkmcnt(3)\n" \
  "v_mfma_f32_16x16x32_bf16 v[48:51], v[92:95], v[30:33], v[48:51]\n" \
  "v_mfma_f32_16x16x32_bf16 v[54:57], v[156:159], v[30:33], v[54:57]\n" \
  "v_mfma_f32_16x16x32_bf16 v[60:63], v[30:33], v[220:223], v[60:63]\n" \
  "s_setprio 0\n" \
  "ds_read_b128 v[30:33], v0 offset:22784\n" \
  "s_waitcnt lgkmcnt(3)\n" \
  "v_mfma_f32_16x16x32_bf16 v[48:51], v[96:99], v[18:21], v[48:51]\n" \
  "v_mfma_f32_16x16x32_bf16 v[54:57], v[160:163], v[18:21], v[54:57]\n" \
  "v_mfma_f32_16x16x32_bf16 v[60:63], v[18:21], v[224:227], v[60:63]\n" \
  "ds_read_b128 v[18:21], v0 offset:24832\n" \
  "s_waitcnt lgkmcnt(3)\n" \
  "v_mfma_f32_16x16x32_bf16 v[48:51], v[100:103], v[22:25], v[48:51]\n" \
  "v_mfma_f32_16x16x32_bf16 v[54:57], v[164:167], v[22:25], v[54:57]\n" \
  "v_mfma_f32_16x16x32_bf16 v[60:63], v[22:25], v[228:231], v[60:63]\n" \
  "ds_read_b128 v[22:25], v0 offset:26880\n" \
  "s_waitcnt lgkmcnt(3)\n" \
  "v_mfma_f32_16x16x32_bf16 v[48:51], v[104:107], v[26:29], v[48:51]\n" \
  "v_mfma_f32_16x16x32_bf16 v[54:57], v[168:171], v[26:29], v[54:57]\n" \
  "v_mfma_f32_16x16x32_bf16 v[60:63], v[26:29], v[232:235], v[60:63]\n" \
  "ds_read_b128 v[26:29], v0 offset:28928\n" \
  "s_waitcnt lgkmcnt(3)\n" \
  "v_mfma_f32_16x16x32_bf16 v[48:51], v[108:111], v[30:33], v[48:51]\n" \
  "v_mfma_f32_16x16x32_bf16 v[54:57], v[172:175], v[30:33], v[54:57]\n" \
  "v_mfma_f32_16x16x32_bf16 v[60:63], v[30:33], v[236:239], v[60:63]\n" \
  "ds_read_b128 v[30:33], v0 offset:30976\n" \
  "s_waitcnt lgkmcnt(3)\n" \
  "v_mfma_f32_16x16x32_bf16 v[48:51], v[112:115], v[18:21], v[48:51]\n" \
  "v_mfma_f32_16x16x32_bf16 v[54:57], v[176:179], v[18:21], v[54:57]\n" \
  "v_mfma_f32_16x16x32_bf16 v[60:63], v[18:21], v[240:243], v[60:63]\n" \
  "s_waitcnt vmcnt(0)\n" \
  "s_cmp_eq_u32 s74, 0\n" \
  "s_cbranch_scc1 .LCQ_G8_%=\n" \
  "v_mov_b32 v6, s81\n" \
  "v_mov_b32 v7, 1\n" \
  "s_mov_b64 exec, 1\n" \
  "ds_add_rtn_u32 v6, v6, v7\n" \
  "s_mov_b64 exec, -1\n" \
  ".LCQ_G8_%=:\n" \
  "s_waitcnt lgkmcnt(2)\n" \
  "v_mfma_f32_16x16x32_bf16 v[48:51], v[116:119], v[22:25], v[48:51]\n" \
  "v_mfma_f32_16x16x32_bf16 v[54:57], v[180:183], v[22:25], v[54:57]\n" \
  "v_mfma_f32_16x16x32_bf16 v[60:63], v[22:25], v[244:247], v[60:63]\n" \
  "s_waitcnt lgkmcnt(1)\n" \
  "v_mfma_f32_16x16x32_bf16 v[48:51], v[120:123], v[26:29], v[48:51]\n" \
  "v_mfma_f32_16x16x32_bf16 v[54:57], v[184:187], v[26:29], v[54:57]\n" \
  "v_mfma_f32_16x16x32_bf16 v[60:63], v[26:29], v[248:251], v[60:63]\n" \
  "s_waitcnt lgkmcnt(0)\n" \
  "v_mfma_f32_16x16x32_bf16 v[48:51], v[124:127], v[30:33], v[48:51]\n" \
  "v_mfma_f32_16x16x32_bf16 v[54:57], v[188:191], v[30:33], v[54:57]\n" \
  "v_mfma_f32_16x16x32_bf16 v[60:63], v[30:33], v[252:255], v[60:63]\n" \
  ".LCQ_END2_%=:\n" \
  "s_cmp_eq_u32 s74, 0\n" \
  "s_cbranch_scc1 .LCQ_NPE_%=\n" \
  "s_waitcnt lgkmcnt(0)\n" \
  "v_readfirstlane_b32 s83, v6\n" \
  "s_mov_b32 s74, 0\n" \
  "s_and_b32 s83, s83, 7\n" \
  "s_cmp_eq_u32 s83, 7\n" \
  "s_cbranch_scc0 .LCQ_F9_%=\n" \
  "s_mov_b64 exec, 1\n" \
  "buffer_store_dword v7, off, s[56:59], s80 sc1\n" \
  "s_mov_b64 exec, -1\n" \
  ".LCQ_F9_%=:\n" \
  ".LCQ_NPE_%=:\n" \
  "v_cmp_eq_u32 vcc, 0, v5\n" \
  "s_cmp_eq_u64 vcc, 0\n" \
  "s_cselect_b32 s75, 1, 0\n" \
  "s_sub_u32 s64, s64, 1\n" \
  "s_and_b32 s76, s73, s77\n" \
  "s_cbranch_scc1 .LCQ_TOP_%=\n" \
  "s_andn2_b32 s84, s75, s73\n" \
  "s_cbranch_scc0 .LCQ_TA_%=\n" \
  "s_mov_b32 m0, s68\n" \
  "s_xor_b32 s68, s68, 0x8000\n" \
  "global_load_lds_dwordx4 v2, s[60:61] offset:0 sc1\n" \
  "global_load_lds_dwordx4 v2, s[60:61] offset:1024 sc1\n" \
  "global_load_lds_dwordx4 v2, s[60:61] offset:2048 sc1\n" \
  "global_load_lds_dwordx4 v2, s[60:61] offset:3072 sc1\n" \
  "s_add_u32 s60, s60, 0x8000\n" \
  "s_addc_u32 s61, s61, 0\n" \
  "s_add_u32 s83, s71, 0x2000\n" \
  "v_bfe_u32 v5, v2, 2, 5\n" \
  "buffer_load_dword v5, v5, s[52:55], s83 offen sc1\n" \
  "s_mov_b32 s73, 1\n" \
  ".LCQ_TA_%=:\n" \
  "s_cmp_lg_u32 s77, 0\n" \
  "s_cbranch_scc1 .LCQ_TB_%=\n" \
  "s_mov_b32 s82, 0x40000\n" \
  "v_lshrrev_b32 v10, 1, v2\n" \
  "v_lshrrev_b32 v11, 7, v2\n" \
  "v_and_b32 v10, 0xf8, v10\n" \
  "v_and_or_b32 v10, v11, 4, v10\n" \
  "s_branch .LCQ_SW_%=\n" \
  ".LCQ_SV_%=:\n" \
  "s_sleep 8\n" \
  ".LCQ_SW_%=:\n" \
  "s_mov_b32 m0, s69\n" \
  "s_nop 0\n" \
  "global_load_lds_dword v10, s[62:63] sc1\n" \
  "v_lshrrev_b32 v9, 2, v2\n" \
  "v_add_u32 v9, s69, v9\n" \
  "s_sub_u32 s82, s82, 1\n" \
  "s_cbranch_scc1 .LCQ_END_%=\n" \
  "s_waitcnt vmcnt(0)\n" \
  "ds_read_b32 v9, v9\n" \
  "s_waitcnt lgkmcnt(0)\n" \
  "v_cmp_eq_u32 vcc, 0, v9\n" \
  "s_cmp_eq_u64 vcc, 0\n" \
  "s_cbranch_scc0 .LCQ_SV_%=\n" \
  "ds_read2_b32 v[6:7], v3 offset0:0 offset1:32\n" \
  "ds_read_b128 v[10:13], v4 offset:256\n" \
  "ds_read_b128 v[14:17], v4 offset:320\n" \
  "ds_read_b32 v8, v3 offset:384\n" \
  "s_waitcnt lgkmcnt(2)\n" \
  "v_fma_f32 v34, -v6, v10, v34\n" \
  "v_fma_f32 v35, -v6, v11, v35\n" \
  "v_fma_f32 v36, -v6, v12, v36\n" \
  "v_fma_f32 v37, -v6, v13, v37\n" \
  "ds_read_b128 v[10:13], v4 offset:0\n" \
  "s_waitcnt lgkmcnt(2)\n" \
  "v_fma_f32 v38, -v6, v14, v38\n" \
  "v_fma_f32 v39, -v6, v15, v39\n" \
  "v_fma_f32 v40, -v6, v16, v40\n" \
  "v_fma_f32 v41, -v6, v17, v41\n" \
  "ds_read_b128 v[14:17], v4 offset:128\n" \
  "v_mul_f32 v34, v34, v7\n" \
  "v_mul_f32 v35, v35, v7\n" \
  "v_mul_f32 v36, v36, v7\n" \
  "v_mul_f32 v37, v37, v7\n" \
  "v_cvt_pk_bf16_f32 v46, v34, v35\n" \
  "v_cvt_pk_bf16_f32 v47, v36, v37\n" \
  "v_mul_f32 v38, v38, v7\n" \
  "v_mul_f32 v39, v39, v7\n" \
  "v_mul_f32 v40, v40, v7\n" \
  "v_mul_f32 v41, v41, v7\n" \
  "v_cvt_pk_bf16_f32 v52, v38, v39\n" \
  "v_cvt_pk_bf16_f32 v53, v40, v41\n" \
  "s_waitcnt lgkmcnt(1)\n" \
  "v_fma_f32 v42, -v10, v8, v42\n" \
  "v_fma_f32 v43, -v11, v8, v43\n" \
  "v_fma_f32 v44, -v12, v8, v44\n" \
  "v_fma_f32 v45, -v13, v8, v45\n" \
  "s_waitcnt lgkmcnt(0)\n" \
  "v_mul_f32 v42, v42, v14\n" \
  "v_mul_f32 v43, v43, v15\n" \
  "v_mul_f32 v44, v44, v16\n" \
  "v_mul_f32 v45, v45, v17\n" \
  "v_cvt_pk_bf16_f32 v58, v42, v43\n" \
  "v_cvt_pk_bf16_f32 v59, v44, v45\n" \
  ".LCQ_TB_%=:\n" \
  "ds_read2_b32 v[6:7], v3 offset0:16 offset1:48\n" \
  "ds_read_b128 v[10:13], v4 offset:256\n" \
  "ds_read_b128 v[14:17], v4 offset:320\n" \
  "ds_read_b32 v8, v3 offset:384\n" \
  "s_waitcnt lgkmcnt(2)\n" \
  "v_fma_f32 v48, -v6, v10, v48\n" \
  "v_fma_f32 v49, -v6, v11, v49\n" \
  "v_fma_f32 v50, -v6, v12, v50\n" \
  "v_fma_f32 v51, -v6, v13, v51\n" \
  "ds_read_b128 v[10:13], v4 offset:64\n" \
  "s_waitcnt lgkmcnt(2)\n" \
  "v_fma_f32 v54, -v6, v14, v54\n" \
  "v_fma_f32 v55, -v6, v15, v55\n" \
  "v_fma_f32 v56, -v6, v16, v56\n" \
  "v_fma_f32 v57, -v6, v17, v57\n" \
  "ds_read_b128 v[14:17], v4 offset:192\n" \
  "v_mul_f32 v48, v48, v7\n" \
  "v_mul_f32 v49, v49, v7\n" \
  "v_mul_f32 v50, v50, v7\n" \
  "v_mul_f32 v51, v51, v7\n" \
  "v_cvt_pk_bf16_f32 v48, v48, v49\n" \
  "v_cvt_pk_bf16_f32 v49, v50, v51\n" \
  "v_mul_f32 v54, v54, v7\n" \
  "v_mul_f32 v55, v55, v7\n" \
  "v_mul_f32 v56, v56, v7\n" \
  "v_mul_f32 v57, v57, v7\n" \
  "v_cvt_pk_bf16_f32 v54, v54, v55\n" \
  "v_cvt_pk_bf16_f32 v55, v56, v57\n" \
  "s_waitcnt lgkmcnt(1)\n" \
  "v_fma_f32 v60, -v10, v8, v60\n" \
  "v_fma_f32 v61, -v11, v8, v61\n" \
  "v_fma_f32 v62, -v12, v8, v62\n" \
  "v_fma_f32 v63, -v13, v8, v63\n" \
  "v_permlane16_swap_b32 v46, v48\n" \
  "v_permlane16_swap_b32 v47, v49\n" \
  "v_permlane16_swap_b32 v52, v54\n" \
  "v_permlane16_swap_b32 v53, v55\n" \
  "s_waitcnt lgkmcnt(0)\n" \
  "v_mul_f32 v60, v60, v14\n" \
  "v_mul_f32 v61, v61, v15\n" \
  "v_mul_f32 v62, v62, v16\n" \
  "v_mul_f32 v63, v63, v17\n" \
  "v_cvt_pk_bf16_f32 v60, v60, v61\n" \
  "v_cvt_pk_bf16_f32 v61, v62, v63\n" \
  "v_mov_b64 v[10:11], 0\n" \
  "v_lshrrev_b32 v9, 1, v2\n" \
  "s_add_u32 s83, s65, 0x2000\n" \
  "buffer_store_dwordx4 v[52:55], v1, s[40:43], s83 offen sc1\n" \
  "buffer_store_dwordx4 v[58:61], v2, s[44:47], s66 offen sc1\n" \
  "buffer_store_dwordx4 v[46:49], v1, s[40:43], s65 offen sc1\n" \
  "buffer_store_dwordx2 v[10:11], v9, s[48:51], s67 offen sc1\n" \
  "s_add_u32 s65, s65, 0x80000\n" \
  "s_add_u32 s66, s66, 0x40000\n" \
  "s_add_u32 s67, s67, 0x10000\n" \
  "s_mov_b32 s80, s72\n" \
  "s_add_u32 s72, s72, 0x1000\n" \
  "s_mov_b32 s81, s70\n" \
  "s_xor_b32 s70, s70, 4\n" \
  "s_mov_b32 s74, 1\n" \
  "s_cmp_eq_u32 s73, 0\n" \
  "s_cbranch_scc1 .LCQ_POLL_%=\n" \
  "s_waitcnt vmcnt(4)\n" \
  "v_cmp_eq_u32 vcc, 0, v5\n" \
  "s_cmp_eq_u64 vcc, 0\n" \
  "s_cselect_b32 s75, 1, 0\n" \
  "s_branch .LCQ_TOP_%=\n" \
  ".LCQ_POLL_%=:\n" \
  "s_mov_b32 s82, 0x40000\n" \
  "s_branch .LCQ_PW_%=\n" \
  ".LCQ_PV_%=:\n" \
  "s_sleep 12\n" \
  ".LCQ_PW_%=:\n" \
  "v_bfe_u32 v5, v2, 2, 5\n" \
  "buffer_load_dword v5, v5, s[52:55], s71 offen sc1\n" \
  "s_sub_u32 s82, s82, 1\n" \
  "s_cbranch_scc1 .LCQ_END_%=\n" \
  "s_waitcnt vmcnt(0)\n" \
  "s_cmp_eq_u32 s74, 0\n" \
  "s_cbranch_scc1 .LCQ_NPD_%=\n" \
  "v_mov_b32 v10, s81\n" \
  "v_mov_b32 v11, 1\n" \
  "s_mov_b64 exec, 1\n" \
  "ds_add_rtn_u32 v10, v10, v11\n" \
  "s_mov_b64 exec, -1\n" \
  "s_waitcnt lgkmcnt(0)\n" \
  "v_readfirstlane_b32 s83, v10\n" \
  "s_mov_b32 s74, 0\n" \
  "s_and_b32 s83, s83, 7\n" \
  "s_cmp_eq_u32 s83, 7\n" \
  "s_cbranch_scc0 .LCQ_F10_%=\n" \
  "s_mov_b64 exec, 1\n" \
  "buffer_store_dword v11, off, s[56:59], s80 sc1\n" \
  "s_mov_b64 exec, -1\n" \
  ".LCQ_F10_%=:\n" \
  "s_cmp_eq_u32 s64, 0\n" \
  "s_cbranch_scc1 .LCQ_END_%=\n" \
  ".LCQ_NPD_%=:\n" \
  "v_cmp_eq_u32 vcc, 0, v5\n" \
  "s_cmp_eq_u64 vcc, 0\n" \
  "s_cbranch_scc0 .LCQ_PV_%=\n" \
  "s_mov_b32 m0, s68\n" \
  "s_xor_b32 s68, s68, 0x8000\n" \
  "global_load_lds_dwordx4 v2, s[60:61] offset:0 sc1\n" \
  "global_load_lds_dwordx4 v2, s[60:61] offset:1024 sc1\n" \
  "global_load_lds_dwordx4 v2, s[60:61] offset:2048 sc1\n" \
  "global_load_lds_dwordx4 v2, s[60:61] offset:3072 sc1\n" \
  "s_add_u32 s60, s60, 0x8000\n" \
  "s_addc_u32 s61, s61, 0\n" \
  "s_add_u32 s83, s71, 0x2000\n" \
  "v_bfe_u32 v5, v2, 2, 5\n" \
  "buffer_load_dword v5, v5, s[52:55], s83 offen sc1\n" \
  "s_waitcnt vmcnt(0)\n" \
  "v_cmp_eq_u32 vcc, 0, v5\n" \
  "s_cmp_eq_u64 vcc, 0\n" \
  "s_cselect_b32 s75, 1, 0\n" \
  "s_branch .LCQ_TOP_%=\n" \
  ".LCQ_END_%=:\n" \
  "s_endpgm\n"
__device__ __forceinline__ void role_qkv(const MegaArgs& A, char* smem, const int L, const int h) {
  const int w = __builtin_amdgcn_readfirstlane(threadIdx.x >> 6);
  char* ws = A.ws;
  unsigned* FL = (unsigned*)(ws + OFF_FL);
  const int l = lane_id();
  const int c = l & 15, g = l >> 4;
  const unsigned sb = lds_base(smem);
  v8u wq[8], wk[8], wv[8];
  {
    const int q = (l >> 2) & 3, p = l & 3, f = q | ((g & 1) << 2);
    const unsigned ra = sb + (8 * g + q) * 256 + (w ^ f) * 32 + p * 8;
#define W_STAGE(W, hf, VM, NEXT)                                               \
  {                                                                           \
    WAIT_VM(VM);                                                              \
    RAW_BAR();                                                                \
    _Pragma("unroll") for (int t = 0; t < 4; ++t) {                           \
      const unsigned a_ = ra + (hf) * 8192 + t * 32768;                       \
      const v2u r0 = trd(a_), r1 = trd(a_ + 1024), r2 = trd(a_ + 16384), r3 = trd(a_ + 16384 + 1024); \
      W[4 * (hf) + t] = v8u{r0.x, r0.y, r1.x, r1.y, r2.x, r2.y, r3.x, r3.y};  \
    }                                                                         \
    _Pragma("unroll") for (int t = 0; t < 4; ++t) asm volatile("" : "+v"(W[4 * (hf) + t])); \
    RAW_BAR();                                                                \
    NEXT;                                                                     \
  }
    wqkv_issue<false>(A.Wq, 0, h, w, l, sb);
    wqkv_issue<false>(A.Wq, 1, h, w, l, sb);
    W_STAGE(wq, 0, 8, wqkv_issue<false>(A.Wk, 0, h, w, l, sb));
    SST(4);
    W_STAGE(wq, 1, 8, wqkv_issue<false>(A.Wk, 1, h, w, l, sb));
    SST(5);
    W_STAGE(wk, 0, 8, wqkv_issue<false>(A.Wv, 0, h, w, l, sb));
    W_STAGE(wk, 1, 8, wqkv_issue<false>(A.Wv, 1, h, w, l, sb));
    SST(6);
    W_STAGE(wv, 0, 8, (void)0);
    W_STAGE(wv, 1, 0, (void)0);
#undef W_STAGE
  }
  WSTAMP2();
  constexpr int Q_ST = 65536, Q_CN = Q_ST + 8192;
  {
    const v4u one = {0x3f803f80u, 0x3f803f80u, 0x3f803f80u, 0x3f803f80u};
    f32x4 csq = {}, csk = {}, t = {};
#define HALF(x, i) BF8(((i) ? __builtin_shufflevector(x, x, 4, 5, 6, 7) : __builtin_shufflevector(x, x, 0, 1, 2, 3)))
#pragma unroll
    for (int ks = 0; ks < 16; ++ks) {
      csq = MFMA16(HALF(wq[ks >> 1], ks & 1), BF8(one), csq);
      csk = MFMA16(HALF(wk[ks >> 1], ks & 1), BF8(one), csk);
      t = MFMA16(BF8(one), HALF(wv[ks >> 1], ks & 1), t);
    }
#undef HALF
#pragma unroll
    for (int i = 0; i < 2; ++i) {
      float* cp = reinterpret_cast<float*>(smem + Q_ST + w * 1024 + i * 512 + 256);
      *reinterpret_cast<f32x4*>(cp + 4 * g) = csq;
      *reinterpret_cast<f32x4*>(cp + 16 + 4 * g) = csk;
      cp[32 + c] = t[0];
    }
  }
  if (threadIdx.x < 2) reinterpret_cast<unsigned*>(smem + Q_CN)[threadIdx.x] = 0u;
  const int rc = blockIdx.x & (NR_C - 1);
  const v4u rsQ = mk_srd(ws + OFF_QK + L * SZ_QK, SZ_QK);
  const v4u rsV = mk_srd(ws + OFF_VF + L * SZ_O, SZ_O);
  const v4u rsM = mk_srd(ws + (w >= 4 ? OFF_MEMV : OFF_MEMK) + L * SZ_MEM, SZ_MEM);
  const v4u rsF = mk_srd(FL + FL_FC + (L * 64 * 8 + rc) * 8 * 32 + w * 32, (64 * 64 - rc * 8 - w) * 128);
  const v4u rsFQ = mk_srd(FL + FL_FQ + (L * 64 * 32 + h) * 32, 64 * 4096 - h * 128);
  const char* Xb = ws + (L ? OFF_X3 : OFF_XA) + w * 4096;
  const char* Sb = (const char*)(FL + FL_FN + (L * 64 * 8 + (blockIdx.x & (NR_N - 1))) * 64) - 2048;
  v8u pv;
  pv[0] = sb + 32768 + g * 512 + c * 16;
  pv[1] = (c + 16 * (2 * (w & 1) + (g >> 1))) * 16 + 4096 * (g & 1);
  pv[2] = l * 16;
  pv[3] = sb + Q_ST + w * 1024 + 512 + c * 4;
  pv[4] = sb + Q_ST + w * 1024 + 512 + g * 16;
  pv[5] = pv[6] = pv[7] = 0u;
  const char* dbg = nullptr;
  asm volatile("s_mov_b64 exec, -1\ns_waitcnt vmcnt(0) lgkmcnt(0)\ns_nop 4\n" Q_ASM
               ::"{v[64:71]}"(wq[0]), "{v[72:79]}"(wq[1]), "{v[80:87]}"(wq[2]), "{v[88:95]}"(wq[3]), "{v[96:103]}"(wq[4]),
               "{v[104:111]}"(wq[5]), "{v[112:119]}"(wq[6]), "{v[120:127]}"(wq[7]), "{v[128:135]}"(wk[0]), "{v[136:143]}"(wk[1]),
               "{v[144:151]}"(wk[2]), "{v[152:159]}"(wk[3]), "{v[160:167]}"(wk[4]), "{v[168:175]}"(wk[5]), "{v[176:183]}"(wk[6]),
               "{v[184:191]}"(wk[7]), "{v[192:199]}"(wv[0]), "{v[200:207]}"(wv[1]), "{v[208:215]}"(wv[2]), "{v[216:223]}"(wv[3]),
               "{v[224:231]}"(wv[4]), "{v[232:239]}"(wv[5]), "{v[240:247]}"(wv[6]), "{v[248:255]}"(wv[7]), "{v[0:7]}"(pv),
               "{s[40:43]}"(rsQ), "{s[44:47]}"(rsV), "{s[48:51]}"(rsM), "{s[52:55]}"(rsF), "{s[56:59]}"(rsFQ), "{s[60:61]}"(Xb),
               "{s[62:63]}"(Sb), "{s64}"(NSEG), "{s65}"((h * 16 + (w >> 1)) * 1024), "{s66}"((h * 512 + w * 64) * 16),
               "{s67}"(h * 2048 + (w & 3) * 512), "{s68}"(sb + w * 4096), "{s69}"(sb + Q_ST + w * 1024 + 512), "{s70}"(sb + Q_CN),
               "{s71}"(0), "{s72}"(0), "{s[36:37]}"(dbg)
               : "memory", "vcc", "scc", "s84", "v63");
}

constexpr int MEGA_WGS = 256;
constexpr size_t MEGA_LDS = (size_t)(32 * 4 * 64 * 8 + 8 * SMS + 15 * QS + 128 + 16 * 64 * 8 + 8 * SOS + 512) * 2;
static_assert(MEGA_LDS <= 163840, "lds");
static_assert(MEGA_LDS >= (size_t)AW_CNT + 64, "lds");
static_assert(MEGA_LDS >= (size_t)(8 * 2 * CKN + 8 * 2 * CUN + 4 * 32 * 8) * 2, "lds");
static_assert(MEGA_LDS >= (size_t)2 * 32 * SMS * 2 && MEGA_LDS >= 131072, "lds");
static_assert(MEGA_LDS >= (size_t)(40 * NST + 32 * 400 + 4 * 392) * 4, "lds");
__global__ __launch_bounds__(512, 2) void mega_kernel(const u16* mem0, const u16* Dskip, char* ws, u16* out, const u16* x0,
                                                      const u16* logAre, const u16* Aim, const u16* Cre, const u16* Cim,
                                                      const u16* logdt, const u16* Wq, const u16* Wk, const u16* Wv,
                                                      const u16* Wo) {
  extern __shared__ __attribute__((aligned(16))) char smem[];
  const MegaArgs A{mem0, Dskip, ws, out, x0, logAre, Aim, Cre, Cim, logdt, Wq, Wk, Wv, Wo, vbid()};
  const int b = A.vb;
  {
    const int hi = (b >= 32 && b < 96) ? b - 32 : (b >= 200 && b < 248) ? b - 136 : (b >= 96 && b < 104) ? b + 16 : (b >= 248 ? b - 128 : -1);
    if (hi >= 0) start_kgen(A, smem, hi, b - 248);
  }
  if ((EXP_OFF & 1) && b >= 64 && b < 80) return;
  if ((EXP_OFF & 8) && b >= 200 && b < 232) return;
  if ((EXP_OFF & 16) && b >= 32 && b < 64) return;
  if ((EXP_OFF & 32) && b >= 80 && b < 96) return;
  if (b < 64) role_chain(A, smem, b >> 5, b & 31, 0);
  else if (b < 96) role_aw(A, smem, (b >> 4) & 1, b & 15);
  else if (b < 104) role_red(A, smem, 0, b - 96);
  else if (b < 168) role_conv(A, smem, b - 104);
  else if (b < 232) role_qkv(A, smem, (b - 168) >> 5, (b - 168) & 31);
  else if (b < 240) role_red(A, smem, 0, b - 224);
  else role_red(A, smem, 1, b - 240);
}

extern "C" void kernel_launch(void* const* d_in, const int* in_sizes, int n_in, void* d_out, int out_size, void* d_ws,
                              size_t ws_size, hipStream_t stream) {
  MegaArgs A;
  A.x0 = (const u16*)d_in[0];
  A.logAre = (const u16*)d_in[1];
  A.Aim = (const u16*)d_in[2];
  A.Cre = (const u16*)d_in[3];
  A.Cim = (const u16*)d_in[4];
  A.logdt = (const u16*)d_in[5];
  A.Dskip = (const u16*)d_in[6];
  A.mem0 = (const u16*)d_in[8];
  A.Wq = (const u16*)d_in[9];
  A.Wk = (const u16*)d_in[10];
  A.Wv = (const u16*)d_in[11];
  A.Wo = (const u16*)d_in[12];
  A.ws = (char*)d_ws;
  A.out = (u16*)d_out;

  (void)hipFuncSetAttribute((const void*)mega_kernel, hipFuncAttributeMaxDynamicSharedMemorySize, (int)MEGA_LDS);
  zero_kernel<<<ZBLK, 256, 0, stream>>>((uint4*)(A.ws + OFF_Z));
  mega_kernel<<<MEGA_WGS, 512, MEGA_LDS, stream>>>(A.mem0, A.Dskip, A.ws, A.out, A.x0, A.logAre, A.Aim, A.Cre, A.Cim, A.logdt,
                                                   A.Wq, A.Wk, A.Wv, A.Wo);
}
```

```cpp
#include <hip/hip_runtime.h>
#include <hip/hip_bf16.h>
#include <stdint.h>

constexpr int L_SEQ = 2048;
constexpr int DM    = 512;
constexpr int NST   = 128;
constexpr int NH    = 32;
constexpr int DH    = 128;
constexpr int HD    = NH * DH;
constexpr int MM    = 8;
constexpr int SEG   = 32;
constexpr int NSEG  = L_SEQ / SEG;
constexpr int LK    = MM + SEG;

typedef unsigned short u16;
using bf16x8 = __attribute__((ext_vector_type(8))) short;
using f32x4  = __attribute__((ext_vector_type(4))) float;
using f32x16 = __attribute__((ext_vector_type(16))) float;

static __device__ __forceinline__ u16 f2bf(float f) {
  unsigned u = __float_as_uint(f);
  u += 0x7fffu + ((u >> 16) & 1u);
  return (u16)(u >> 16);
}
static __device__ __forceinline__ float bf2f(u16 h) { return __uint_as_float(((unsigned)h) << 16); }
static __device__ __forceinline__ float bfr(float f) { return bf2f(f2bf(f)); }

constexpr size_t SZ_X      = (size_t)L_SEQ * DM * 2;
constexpr size_t OFF_KF    = 0;
constexpr size_t OFF_UT    = OFF_KF + (size_t)DM * L_SEQ * 4;
constexpr size_t OFF_YT    = OFF_UT + SZ_X;
constexpr size_t OFF_XA    = OFF_YT + SZ_X;
constexpr size_t OFF_H     = OFF_XA + SZ_X;
constexpr size_t OFF_X2    = OFF_H + SZ_X;
constexpr size_t OFF_U2    = OFF_X2 + SZ_X;
constexpr size_t OFF_X3    = OFF_U2 + SZ_X;
constexpr size_t SZ_QK     = (size_t)L_SEQ * 2 * HD * 2;
constexpr size_t OFF_QK    = OFF_X3 + SZ_X;
constexpr size_t SZ_O      = (size_t)L_SEQ * HD * 2;
constexpr size_t OFF_VF    = OFF_QK + 2 * SZ_QK;
constexpr size_t SZ_PW     = (size_t)2 * NSEG * 16 * SEG * DM * 2;
constexpr size_t OFF_PW    = OFF_VF + 2 * SZ_O;
constexpr size_t SZ_MEM    = (size_t)NSEG * MM * HD * 2;
constexpr size_t OFF_MEMK  = OFF_PW + SZ_PW;
constexpr size_t OFF_MEMV  = OFF_MEMK + 2 * SZ_MEM;
constexpr size_t OFF_GT    = OFF_MEMV + 2 * SZ_MEM;
constexpr size_t SZ_ACC    = (size_t)2 * 32768;
constexpr size_t OFF_Z     = OFF_GT + (size_t)64 * DM * 4;
constexpr size_t OFF_ACC   = OFF_Z;
constexpr size_t OFF_FL    = OFF_ACC + 2 * SZ_ACC;
constexpr int FL_FK  = 0;
constexpr int FL_FW  = FL_FK + 64 * 8 * 32;
constexpr int FL_FR  = FL_FW + 2 * 64 * 32 * 32;
constexpr int FL_FC  = FL_FR + 64 * 32 * 32;
constexpr int FL_FQ  = FL_FC + 2 * 64 * 64 * 32;
constexpr int FL_FS  = FL_FQ + 2 * 64 * 32 * 32;
constexpr int FL_F0  = FL_FS + 8 * 32 * 32;
constexpr int FL_FZ  = FL_F0 + 8 * 8 * 32;
constexpr int FL_FN  = FL_FZ + 16 * 32;
constexpr int FL_KG  = FL_FN + 2 * 64 * 16 * 32;
constexpr int FL_G   = FL_KG + 64 * 32;
constexpr int FL_TMO = FL_G + 2 * 8 * 32;
constexpr int FL_END = FL_TMO + 64;
constexpr size_t Z_BYTES   = OFF_FL + (size_t)FL_END * 4 - OFF_Z;
constexpr size_t WS_TOTAL  = OFF_Z + Z_BYTES;
static_assert(Z_BYTES % 16 == 0 && OFF_Z % 256 == 0, "zero region");
static_assert(WS_TOTAL + (4 << 20) <= (size_t)256 * 1024 * 1024, "workspace too large");

static_assert(WS_TOTAL - OFF_XA <= (size_t)384 * 8 * 65536, "translation warm-up");
constexpr int CKN = 2208;
static_assert((size_t)DM * 2 * CKN * 2 <= (size_t)DM * L_SEQ * 4 + SZ_X, "K buffer");
__device__ __forceinline__ float pos_of_index(int p) {
  if (p < 256) return (float)p;
  if (p < 384) return (float)(256 + 2 * (p - 256));
  if (p < 512) return (float)(512 + 4 * (p - 384));
  if (p < 640) return (float)(1024 + 8 * (p - 512));
  return 2048.0f;
}
__device__ __forceinline__ int index_of_l(int l) {
  float pf = bfr((float)l);
  int p = (int)pf;
  if (p < 256) return p;
  if (p < 512) return 256 + ((p - 256) >> 1);
  if (p < 1024) return 384 + ((p - 512) >> 2);
  if (p < 2048) return 512 + ((p - 1024) >> 3);
  return 640;
}

template <int CTRL> __device__ __forceinline__ float dppr(float x) {
  return __builtin_bit_cast(float, __builtin_amdgcn_update_dpp(0, __builtin_bit_cast(int, x), CTRL, 0xf, 0xf, false));
}
__device__ __forceinline__ float wave_sum(float v) {
  v += dppr<0x121>(v);
  v += dppr<0x122>(v);
  v += dppr<0x124>(v);
  v += dppr<0x128>(v);
  {
    auto r = __builtin_amdgcn_permlane16_swap(__float_as_uint(v), __float_as_uint(v), false, false);
    v = __uint_as_float(r[0]) + __uint_as_float(r[1]);
  }
  {
    auto r = __builtin_amdgcn_permlane32_swap(__float_as_uint(v), __float_as_uint(v), false, false);
    v = __uint_as_float(r[0]) + __uint_as_float(r[1]);
  }
  return v;
}
__device__ __forceinline__ void ln_row8(float (&v)[8]) {
  float s = 0.f;
#pragma unroll
  for (int j = 0; j < 8; ++j) s += v[j];
  s = wave_sum(s);
  const float mean = s * (1.0f / DM);
  float q = 0.f;
#pragma unroll
  for (int j = 0; j < 8; ++j) { float t = v[j] - mean; q += t * t; }
  q = wave_sum(q);
  const float var = q * (1.0f / DM);
  const float mb = bfr(mean);
  const float sd = bfr(sqrtf(bfr(bfr(var) + 1e-5f)));
#pragma unroll
  for (int j = 0; j < 8; ++j) v[j] = bfr(bfr(v[j] - mb) / sd);
}

constexpr int ZBLK = 512;
__global__ __launch_bounds__(256) void zero_kernel(uint4* __restrict__ zero) {
  for (size_t i = (size_t)blockIdx.x * 256 + threadIdx.x; i < Z_BYTES / 16; i += (size_t)ZBLK * 256) zero[i] = uint4{0u, 0u, 0u, 0u};
}

#define RLX_AGENT __ATOMIC_RELAXED, __HIP_MEMORY_SCOPE_AGENT
typedef __attribute__((address_space(1))) unsigned gu32;
typedef unsigned v4u __attribute__((ext_vector_type(4)));
constexpr int AUX_SC1 = 16;
constexpr unsigned SPIN_CAP = 1u << 18;

__device__ __forceinline__ bool wait_flags(const unsigned* flags, int lane, int nfl, unsigned want, unsigned* tmo) {
  for (unsigned spins = 0;; ++spins) {
    unsigned v = want;
    if (lane < nfl) v = __hip_atomic_load((gu32*)flags + lane * 32, RLX_AGENT);
    if (__all(v >= want)) break;
    if (spins > SPIN_CAP) {
      if (lane == 0) __hip_atomic_store((gu32*)tmo, 1u, RLX_AGENT);
      return false;
    }
  }
  __builtin_amdgcn_fence(__ATOMIC_ACQUIRE, "wavefront");
  return true;
}

#define STAMP(i)
#define STAMP_DUMP()
#define WSTAMP(i)
template <int CTRL> __device__ __forceinline__ float dppf(float old, float x) {
  return __builtin_bit_cast(float, __builtin_amdgcn_update_dpp(__builtin_bit_cast(int, old), __builtin_bit_cast(int, x), CTRL, 0xf, 0xf, false));
}
__device__ __forceinline__ float wave_max_dpp(float x) {
  const float ninf = -3e38f;
  x = fmaxf(x, dppf<0x111>(ninf, x));
  x = fmaxf(x, dppf<0x112>(ninf, x));
  x = fmaxf(x, dppf<0x114>(ninf, x));
  x = fmaxf(x, dppf<0x118>(ninf, x));
  x = fmaxf(x, dppf<0x142>(ninf, x));
  x = fmaxf(x, dppf<0x143>(ninf, x));
  return __builtin_bit_cast(float, __builtin_amdgcn_readlane(__builtin_bit_cast(int, x), 63));
}
__device__ __forceinline__ float wave_sum_dpp(float x) {
  x += dppf<0x111>(0.f, x);
  x += dppf<0x112>(0.f, x);
  x += dppf<0x114>(0.f, x);
  x += dppf<0x118>(0.f, x);
  x += dppf<0x142>(0.f, x);
  x += dppf<0x143>(0.f, x);
  return __builtin_bit_cast(float, __builtin_amdgcn_readlane(__builtin_bit_cast(int, x), 63));
}
constexpr int SMS = 528;
constexpr int QS = 144;
constexpr int VS = 80;
constexpr int SOS = 144;
#define VAR_NREP 4
constexpr int NREP = VAR_NREP;
__device__ __forceinline__ float xsum16(float x) {
  auto r = __builtin_amdgcn_permlane16_swap(__float_as_uint(x), __float_as_uint(x), false, false);
  return __uint_as_float(r[0]) + __uint_as_float(r[1]);
}
__device__ __forceinline__ float xsum32(float x) {
  auto r = __builtin_amdgcn_permlane32_swap(__float_as_uint(x), __float_as_uint(x), false, false);
  return __uint_as_float(r[0]) + __uint_as_float(r[1]);
}
__device__ __forceinline__ float xmax16(float x) {
  auto r = __builtin_amdgcn_permlane16_swap(__float_as_uint(x), __float_as_uint(x), false, false);
  return fmaxf(__uint_as_float(r[0]), __uint_as_float(r[1]));
}
__device__ __forceinline__ float xmax32(float x) {
  auto r = __builtin_amdgcn_permlane32_swap(__float_as_uint(x), __float_as_uint(x), false, false);
  return fmaxf(__uint_as_float(r[0]), __uint_as_float(r[1]));
}
__device__ __forceinline__ float red_mod4(float x) {
  x += dppf<0x124>(0.f, x);
  x += dppf<0x128>(0.f, x);
  return xsum32(xsum16(x));
}
typedef __bf16 bf16x2_t __attribute__((ext_vector_type(2)));
typedef float f32x2_t __attribute__((ext_vector_type(2)));
__device__ __forceinline__ unsigned pk2(float a, float b) {
  f32x2_t v = {a, b};
  bf16x2_t r = __builtin_convertvector(v, bf16x2_t);
  return __builtin_bit_cast(unsigned, r);
}
__device__ __forceinline__ float one(float x) {
  asm("" : "+v"(x));
  return x;
}
__device__ __forceinline__ unsigned pk2s(float a, float b) { return pk2(one(a), one(b)); }
__device__ __forceinline__ float dot2sq(const unsigned x, const float acc) {
  return __builtin_amdgcn_fdot2_f32_bf16(__builtin_bit_cast(bf16x2_t, x), __builtin_bit_cast(bf16x2_t, x), acc, false);
}
__device__ __forceinline__ float lo16(unsigned u) { return __uint_as_float(u << 16); }
__device__ __forceinline__ float hi16(unsigned u) { return __uint_as_float(u & 0xffff0000u); }
__device__ __forceinline__ void scale_pair(float a, float b, float scale, float& oa, float& ob) {
  unsigned u = pk2(a, b);
  unsigned v = pk2s(lo16(u) * scale, hi16(u) * scale);
  oa = lo16(v);
  ob = hi16(v);
}

#define XMAP 2
__device__ __forceinline__ int vbid() {
  const int b = blockIdx.x, x = b & 7, t = b >> 3;
  if (XMAP == 0) {
    if (b < 64) return (b & 32) + 4 * x + (t & 3);
    if (b < 96) return (b & ~15) + 2 * x + (t & 1);
    if (b >= 104 && b < 168) return 104 + 8 * x + t - 13;
    if (b >= 168 && b < 232) return 168 + ((b - 168) & 32) + 4 * x + ((t - 21) & 3);
    return b;
  }
  if (XMAP == 2) {
    if (x < 4) return t < 16 ? 4 * (t & 8) + 8 * x + (t & 7) : 104 + 16 * x + t - 16;
    const int y = x - 4;
    if (t >= 24) return (y == 0 ? 96 : 224 + 8 * y) + t - 24;
    if (t < 16) return 168 + 4 * (t & 8) + 8 * y + (t & 7);
    return 64 + 4 * (t & 4) + 4 * y + (t & 3);
  }
  if (x < 2) return 2 * (t & 16) + 16 * x + (t & 15);
  const int i = 16 * (x - 2) + (t & 15);
  if (t < 16) {
    const int k = (i * 171) >> 9, r = i - 3 * k;
    return r == 0 ? 168 + k : r == 1 ? 200 + k : 64 + 16 * (k & 1) + (k >> 1);
  }
  const int g = i >> 3, e = i & 7;
  if (g == 3) return 96 + e;
  if (g == 7) return 232 + e;
  if (g == 9) return 240 + e;
  if (g == 11) return 248 + e;
  return 104 + 8 * (g - (g > 3) - (g > 7) - (g > 9)) + e;
}
#define MSTAMP()
#define WSTAMP2()
#define SST(k)
#define TS(k, ss, t)
#define LS(k, ss)
#define LS_DUMP()
struct MegaArgs {
  const u16* mem0;
  const u16* Dskip;
  char* ws;
  u16* out;
  const u16* x0;
  const u16 *logAre, *Aim, *Cre, *Cim, *logdt;
  const u16 *Wq, *Wk, *Wv, *Wo;
  int vb;
};

__device__ __forceinline__ int lane_id() {
  int l;
  asm volatile("v_mbcnt_lo_u32_b32 %0, -1, 0\n\tv_mbcnt_hi_u32_b32 %0, -1, %0" : "=v"(l));
  return l;
}
__device__ __forceinline__ bool spin_timeout(unsigned spins, unsigned* tmo) {
  if (spins > SPIN_CAP) {
    if (lane_id() == 0) __hip_atomic_store((gu32*)tmo, 1u, RLX_AGENT);
    return true;
  }
  return false;
}
#define ROT_H (4 * (h & 1))
#define AWDIR 1
#define AWAT 0
#define AWRP 0
#define PEPS 4
#define FGATE 0
#define DL_C 0
#define DL_A 0
#define DL_Q 0
#define CPRIO 1
#define P1PRIO 3
#define QLEAD 2
#define POLL_SLEEP 12
#define SLP_C POLL_SLEEP
#define SLP_A POLL_SLEEP
#define SLP_W POLL_SLEEP
#define SLP_R POLL_SLEEP
#define SLP_V POLL_SLEEP
#define SLP_Q POLL_SLEEP
#define SLP_M 4
#define AWPRIO 2
#define SLP_S 8
#define SPOS 4
#define CVPRE 2
#define QPD 3
#define QPRI 2
#define TLBW 1
#define QHV 0
#define QCHK 0
#define LEADN 99
#define GATE_V 170
#define GATE_R 380
#define GATE_N 380
#define AWAHEAD 1
#define GATE_M 150
#define PRI_A 3
#define PRI_WA 2
#define PRI_WB 1
#define AWSKIP 3
#define PRI_0 3
#define AWDEF 0
#define SLP_F 2
#define EXP_OFF 0
#define AWSPEC 1
#define CVTHR 48
#define NR_C 8
#define NR_R 8
#define NR_W 8
#define NR_N 2
#define QAHEAD 0
#define NS0 4
#define SLP_N POLL_SLEEP
template <int SLP, int WPL>
__device__ __forceinline__ bool wait_n(const unsigned* f, int n, unsigned* tmo) {
  const int l = lane_id();
  for (unsigned spins = 0;; ++spins) {
    unsigned v = 1u;
    if (l < n) v = __hip_atomic_load((gu32*)f + (l / WPL) * 32 + l % WPL, RLX_AGENT);
    if (__all(v != 0u)) break;
    if (spin_timeout(spins, tmo)) return false;
    __builtin_amdgcn_s_sleep(SLP);
  }
  asm volatile("" ::: "memory");
  return true;
}
__device__ __forceinline__ unsigned now10() { return (unsigned)__builtin_amdgcn_s_memrealtime(); }
__device__ __forceinline__ void gate(const unsigned t) {
  for (int i = 0; i < 64; ++i) {
    const int d = (int)(t - now10());
    if (d <= 0) break;
    if (d > 100) __builtin_amdgcn_s_sleep(32);
    else if (d > 25) __builtin_amdgcn_s_sleep(8);
    else __builtin_amdgcn_s_sleep(2);
  }
}
template <int SLP, int WPL, int GT>
__device__ __forceinline__ void grp_wait(const bool leader, volatile unsigned* sw, const unsigned val, const unsigned* f, int n,
                                         unsigned* tmo, unsigned& tl) {
  if (leader) {
    const int l = lane_id();
    for (unsigned spins = 0;; ++spins) {
      unsigned v = 1u;
      if (l < n) v = __hip_atomic_load((gu32*)f + (l / WPL) * 32 + l % WPL, RLX_AGENT);
      if (__all(v != 0u) || spin_timeout(spins, tmo)) break;
      if (GT && spins == 0) gate(tl);
      else __builtin_amdgcn_s_sleep(SLP);
    }
    if (GT) tl = now10() + GT;
    if ((threadIdx.x & 63) == 0) *sw = val;
  } else {
    for (unsigned spins = 0; *sw < val; ++spins) {
      if (spins > 64u * SPIN_CAP) break;
      if (FGATE && GT && spins == 0) gate(tl);
      __builtin_amdgcn_s_sleep(SLP_F);
    }
    if (FGATE && GT) tl = now10() + GT;
  }
  asm volatile("" ::: "memory");
}
template <int SLP, int WPL>
__device__ __forceinline__ bool wg_wait(const unsigned* f, int n, unsigned* tmo) {
  if (threadIdx.x < 64) (void)wait_n<SLP, WPL>(f, n, tmo);
  __syncthreads();
  asm volatile("" ::: "memory");
  return true;
}
__device__ __forceinline__ void set_flag(unsigned* f) { __hip_atomic_store((gu32*)f, 1u, RLX_AGENT); }
#define DRAIN() asm volatile("s_waitcnt vmcnt(0)" ::: "memory")
#define MKRS(ptr, bytes) __builtin_amdgcn_make_buffer_rsrc((void*)(ptr), 0, (int)(bytes), 0x00020000)
typedef unsigned v2u __attribute__((ext_vector_type(2)));
#define LD128(rs, voff, soff) __builtin_amdgcn_raw_buffer_load_b128(rs, voff, soff, AUX_SC1)
#define LD64(rs, voff, soff) __builtin_amdgcn_raw_buffer_load_b64(rs, voff, soff, AUX_SC1)
#define ST128(v, rs, voff, soff)                                              \
  do {                                                                        \
    const v4u st_d_ = (v);                                                    \
    __builtin_amdgcn_raw_buffer_store_b128(st_d_, rs, voff, soff, AUX_SC1);   \
    asm volatile("s_nop 1" ::"v"(st_d_) : "memory");                          \
  } while (0)
#define ST64(v, rs, voff, soff) __builtin_amdgcn_raw_buffer_store_b64(v, rs, voff, soff, AUX_SC1)
#define BF8(x) __builtin_bit_cast(bf16x8, x)
#define MFMA16(a, b, c) __builtin_amdgcn_mfma_f32_16x16x32_bf16(a, b, c, 0, 0, 0)
typedef short bf16x4 __attribute__((ext_vector_type(4)));
#define MFMA16K(a, b, c) \
  __builtin_amdgcn_mfma_f32_16x16x16bf16_1k(__builtin_bit_cast(bf16x4, a), __builtin_bit_cast(bf16x4, b), c, 0, 0, 0)
__device__ __forceinline__ float fmax_nn(float a, float b) { return __builtin_elementwise_maximum(a, b); }
typedef __bf16 bfv8 __attribute__((ext_vector_type(8)));
typedef __bf16 bfv16 __attribute__((ext_vector_type(16)));
typedef unsigned v8u __attribute__((ext_vector_type(8)));
#define SMF(a, b, c, idx) \
  __builtin_amdgcn_smfmac_f32_16x16x64_bf16(__builtin_bit_cast(bfv8, a), __builtin_bit_cast(bfv16, b), c, idx, 0, 0)
__device__ __forceinline__ float swap_add32(float a, float b) {
  auto r = __builtin_amdgcn_permlane32_swap(__float_as_uint(a), __float_as_uint(b), false, false);
  return __uint_as_float(r[0]) + __uint_as_float(r[1]);
}
__device__ __forceinline__ float swap_add16(float a, float b) {
  auto r = __builtin_amdgcn_permlane16_swap(__float_as_uint(a), __float_as_uint(b), false, false);
  return __uint_as_float(r[0]) + __uint_as_float(r[1]);
}
__device__ __forceinline__ v4u smf_fix(const v4u d) {
  auto x = __builtin_amdgcn_permlane16_swap(d.x, d.z, false, false);
  auto y = __builtin_amdgcn_permlane16_swap(d.y, d.w, false, false);
  return v4u{x[0], x[1], y[0], y[1]};
}
__device__ __forceinline__ v8u smf_bfrag(const u16* p) {
  const v2u a = *reinterpret_cast<const v2u*>(p), b = *reinterpret_cast<const v2u*>(p + 8);
  const v2u c = *reinterpret_cast<const v2u*>(p + 32), d = *reinterpret_cast<const v2u*>(p + 40);
  return v8u{a.x, b.x, a.y, b.y, c.x, d.x, c.y, d.y};
}

#define VAR_PPA 0
#define VAR_PPB 0
#define VAR_THR 0
#define VAR_THR2 0
#define CV_LEAD 100
#define VAR_EARLY 1
#define VAR_DRAIN 1
#define VAR_D1 0
#define VAR_D2 0
#define VAR_DR2 1
#define VAR_S1 0
#define VAR_S2 0
typedef __attribute__((address_space(1))) unsigned long long gu64;
typedef __attribute__((address_space(1))) char gch;
__device__ __forceinline__ int cvt_rpi(float x) {
  int r;
  asm("v_cvt_rpi_i32_f32 %0, %1" : "=v"(r) : "v"(x));
  return r;
}
typedef short v4s __attribute__((ext_vector_type(4)));
typedef __attribute__((address_space(3))) v4s* lp4s;
__device__ __forceinline__ void glds16(const void* sbase, const unsigned voff, const unsigned lds) {
  unsigned keep;
  asm volatile("s_nop 4\n\ts_mov_b32 %0, m0\n\ts_mov_b32 m0, %3\n\ts_nop 0\n\tglobal_load_lds_dwordx4 %1, %2\n\ts_mov_b32 m0, %0"
               : "=&s"(keep)
               : "v"(voff), "s"(sbase), "s"(lds)
               : "memory");
}
__device__ __forceinline__ void glds16c(const void* sbase, const unsigned voff, const unsigned lds) {
  unsigned keep;
  asm volatile("s_nop 4\n\ts_mov_b32 %0, m0\n\ts_mov_b32 m0, %3\n\ts_nop 0\n\tglobal_load_lds_dwordx4 %1, %2 sc1\n\ts_mov_b32 m0, %0"
               : "=&s"(keep)
               : "v"(voff), "s"(sbase), "s"(lds)
               : "memory");
}
__device__ __forceinline__ v2u trd(const unsigned a) {
  return __builtin_bit_cast(v2u, __builtin_amdgcn_ds_read_tr16_b64_v4i16((lp4s)(unsigned long)a));
}
__device__ __forceinline__ unsigned lds_base(char* smem) { return (unsigned)(unsigned long)(__attribute__((address_space(3))) char*)smem; }
#define RAW_BAR()                                           \
  do {                                                      \
    asm volatile("s_waitcnt lgkmcnt(0)" ::: "memory");      \
    __builtin_amdgcn_s_barrier();                           \
    asm volatile("" ::: "memory");                          \
  } while (0)
#define WAIT_VM(n) asm volatile("s_waitcnt vmcnt(" #n ")" ::: "memory")
template <bool SP>
__device__ __forceinline__ void wqkv_issue(const u16* Wm, const int hf, const int h, const int w, const int l, const unsigned sb) {
  const char* base = (const char*)Wm + (size_t)(256 * hf + 32 * w) * (HD * 2) + h * (DH * 2);
#pragma unroll
  for (int i = 0; i < 8; ++i) {
    const int r = l >> 4;
    const int f = SP ? ((r & 1) | ((i & 1) << 1) | (((i >> 1) & 1) << 2)) : (r | (((i >> 1) & 1) << 2));
    glds16(base, (unsigned)((4 * i + r) * (HD * 2) + (((l & 15) ^ (f << 1)) << 4)), sb + w * 16384 + hf * 8192 + i * 1024);
  }
}

__device__ __forceinline__ unsigned row_stats(const v4u raw) {
  float v[8];
#pragma unroll
  for (int e = 0; e < 4; ++e) { v[2 * e] = lo16(raw[e]); v[2 * e + 1] = hi16(raw[e]); }
  float s = 0.f;
#pragma unroll
  for (int e = 0; e < 8; ++e) s += v[e];
  const float mean = wave_sum(s) * (1.0f / DM);
  float q = 0.f;
#pragma unroll
  for (int e = 0; e < 8; ++e) { const float t = v[e] - mean; q += t * t; }
  const float var = wave_sum(q) * (1.0f / DM);
  const float sd = bfr(sqrtf(bfr(bfr(var) + 1e-5f)));
  return (unsigned)f2bf(mean) | ((unsigned)f2bf(sd) << 16);
}
__device__ __forceinline__ void start_kgen(const MegaArgs& A, char* smem, const int hi, const int xi) {
  const int tid = threadIdx.x, j = tid >> 7, u = tid & 127, d = 4 * hi + j;
  const int w = __builtin_amdgcn_readfirstlane(tid >> 6), l = tid & 63;
  float* prm = reinterpret_cast<float*>(smem) + j * 10 * NST;
  float* part = reinterpret_cast<float*>(smem) + 40 * NST + j * 8 * 400;
  float* kd = reinterpret_cast<float*>(smem) + 40 * NST + 32 * 400 + j * 392;
  v4u raw[8], ra, rb;
  const int r0 = 16 * xi + 2 * w;
  if (xi >= 0) {
    ra = *reinterpret_cast<const v4u*>(A.x0 + (size_t)r0 * DM + l * 8);
    rb = *reinterpret_cast<const v4u*>(A.x0 + (size_t)(r0 + 1) * DM + l * 8);
  }
  const u16 p0 = A.logdt[d], p1 = A.logAre[d * NST + u], p2 = A.Aim[d * NST + u], p3 = A.Cre[d * NST + u], p4 = A.Cim[d * NST + u];
  if (w >= 6) {
#pragma unroll
    for (int i = 0; i < 8; ++i) raw[i] = *reinterpret_cast<const v4u*>(A.x0 + (size_t)(16 * hi + 8 * (w - 6) + i) * DM + l * 8);
  }
  if (xi >= 0) {
    const unsigned sa = row_stats(ra), sb = row_stats(rb);
    if (l < 2) __hip_atomic_store((gu32*)(A.ws + OFF_GT) + r0 + l, l ? sb : sa, RLX_AGENT);
  }
  {
    const float dt = bfr(expf(bf2f(p0)));
    const float ar = -bfr(expf(bf2f(p1))) * dt, ai = bf2f(p2) * dt;
    prm[u] = ar;
    prm[NST + u] = ai;
    prm[2 * NST + u] = bf2f(p3) * dt;
    prm[3 * NST + u] = bf2f(p4) * dt;
#pragma unroll
    for (int cls = 1; cls < 4; ++cls) {
      const float st = (float)(1 << cls);
      const float e = expf(ar * st);
      float sn, cs;
      sincosf(ai * st, &sn, &cs);
      prm[(2 + 2 * cls) * NST + u] = e * cs;
      prm[(3 + 2 * cls) * NST + u] = e * sn;
    }
  }
  if (xi >= 0) {
    DRAIN();
    if (l < 8) set_flag((unsigned*)(A.ws + OFF_FL) + FL_F0 + (l * 8 + xi) * 32 + w);
    SST(11);
  }
  __syncthreads();
  SST(1);
  unsigned tw = 0u;
  if (TLBW && w < 6 && (blockIdx.x >> 6) == 1) {
    const unsigned o = (unsigned)OFF_XA + (unsigned)(tid * 8 + ((blockIdx.x >> 3) & 7)) * 65536u;
    if (o < WS_TOTAL) asm volatile("global_load_dword %0, %1, %2" : "=v"(tw) : "v"(o), "s"(A.ws) : "memory");
  }
  if (w >= 6) {
    unsigned stw = 0u;
#pragma unroll
    for (int i = 0; i < 8; ++i) {
      const unsigned t = row_stats(raw[i]);
      if (l == i) stw = t;
    }
    const int sx = 2 * hi + w - 6;
    if (l < 8) __hip_atomic_store((gu32*)(A.ws + OFF_GT) + 8 * sx + l, stw, RLX_AGENT);
    DRAIN();
    if (l < 8) set_flag((unsigned*)(A.ws + OFF_FL) + FL_FS + (l * 32 + (sx >> 3)) * 32 + (sx & 7));
    if (w == 6 && hi < 64) {
      unsigned* FL = (unsigned*)(A.ws + OFF_FL);
      const int s = hi >> 5, r = hi & 31;
      (void)wait_n<4, 8>(FL + FL_FC + (s * 8 + (blockIdx.x & (NR_C - 1))) * 8 * 32, 64, FL + FL_TMO);
      const unsigned st = row_stats(LD128(MKRS(A.ws + OFF_XA, SZ_X), l * 512, s * 32768 + r * 16));
      const unsigned long long v = ((unsigned long long)__float_as_uint(1.0f / hi16(st)) << 32) | ((st << 16) | 1u);
      if (l < NR_N) __hip_atomic_store((gu64*)(FL + FL_FN + (s * 8 + l) * 64) + r, v, RLX_AGENT);
    }
  } else {
    const int jm = tid / 96, um = tid % 96;
    const int r = um % 12, ng = um / 12;
    const float* prm = reinterpret_cast<float*>(smem) + jm * 10 * NST;
    float* part = reinterpret_cast<float*>(smem) + 40 * NST + jm * 8 * 400;
    const float* pw = prm + (4 + 2 * (r >> 2)) * NST;
    const float p0 = pos_of_index(256 + 32 * r);
    f32x2_t acc[33];
#pragma unroll
    for (int i = 0; i < 33; ++i) acc[i] = f32x2_t{0.f, 0.f};
#pragma unroll 1
    for (int bt = 0; bt < 4; ++bt) {
      const int n = 16 * ng + 4 * bt;
      const f32x4 ar = *reinterpret_cast<const f32x4*>(prm + n), ai = *reinterpret_cast<const f32x4*>(prm + NST + n);
      const f32x4 cr = *reinterpret_cast<const f32x4*>(prm + 2 * NST + n), ci = *reinterpret_cast<const f32x4*>(prm + 3 * NST + n);
      const f32x4 wr = *reinterpret_cast<const f32x4*>(pw + n), wi = *reinterpret_cast<const f32x4*>(pw + NST + n);
      float yr0[4], yi0[4];
#pragma unroll
      for (int k = 0; k < 4; ++k) {
        const float e = __expf(ar[k] * p0);
        float sn, cs;
        __sincosf(ai[k] * p0, &sn, &cs);
        const float zr = e * cs, zi = e * sn;
        yr0[k] = cr[k] * zr - ci[k] * zi;
        yi0[k] = cr[k] * zi + ci[k] * zr;
      }
      f32x2_t yra = {yr0[0], yr0[1]}, yia = {yi0[0], yi0[1]}, wra = {wr[0], wr[1]}, wia = {wi[0], wi[1]};
      f32x2_t yrb = {yr0[2], yr0[3]}, yib = {yi0[2], yi0[3]}, wrb = {wr[2], wr[3]}, wib = {wi[2], wi[3]};
#pragma unroll
      for (int i = 0; i < 33; ++i) {
        acc[i] += yra + yrb;
        const f32x2_t ta = yra * wra - yia * wia, tb = yrb * wrb - yib * wib;
        yia = yra * wia + yia * wra;
        yib = yrb * wib + yib * wrb;
        yra = ta;
        yrb = tb;
      }
    }
#pragma unroll
    for (int i = 0; i < 33; ++i) part[ng * 400 + 33 * r + i] = acc[i].x + acc[i].y;
  }
  __syncthreads();
  SST(2);
  for (int i = u; i < 385; i += 128) {
    const int o = i < 384 ? 33 * (i >> 5) + (i & 31) : 33 * 11 + 32;
    float v = 0.f;
#pragma unroll
    for (int g = 0; g < 8; ++g) v += part[g * 400 + o];
    kd[i] = 2.0f * v;
  }
  __syncthreads();
  const __amdgpu_buffer_rsrc_t rs = MKRS(A.ws + OFF_KF, DM * 2 * CKN * 2);
  for (int un = u; un < 229; un += 128) {
    v4u oh, ol;
#pragma unroll
    for (int e = 0; e < 4; ++e) {
      float kv[2];
#pragma unroll
      for (int k = 0; k < 2; ++k) {
        const int lag = 2080 - (8 * un + 2 * e + k);
        kv[k] = (lag >= 256 && lag < L_SEQ) ? kd[index_of_l(lag) - 256] : 0.f;
      }
      oh[e] = pk2(kv[0], kv[1]);
      ol[e] = pk2(kv[0] - lo16(oh[e]), kv[1] - hi16(oh[e]));
    }
    ST128(oh, rs, (d * 2 * CKN + 8 * un) * 2, 0);
    ST128(ol, rs, (d * 2 * CKN + CKN + 8 * un) * 2, 0);
  }
  DRAIN();
  asm volatile("" ::"v"(tw));
  __syncthreads();
  if (tid == 0) set_flag((unsigned*)(A.ws + OFF_FL) + FL_KG + (hi >> 1) * 32 + (hi & 1));
  SST(3);
}

constexpr int C_SM = 131072, C_SO = C_SM + 8192, C_KT = C_SO + 2048, C_X = C_KT + 16384, C_SQ = C_X + 1024;
constexpr int C_SK = C_SQ + (7 * QS + 128) * 2;
static_assert(C_SK + 8 * QS * 2 <= 163840, "lds");
#define TDSLOT 0
#define CHAIN_ASM_ATT \
  "s_mov_b32 s92, 1152\n" \
  "s_mov_b32 s93, 576\n" \
  "s_mov_b32 s94, 2272\n" \
  "s_mov_b32 s95, 272\n" \
  "s_mov_b32 s96, 288\n" \
  "s_mov_b32 s91, 0x40000001\n" \
  "s_mov_b32 s84, 0x4b400000\n" \
  "s_mov_b32 s85, 0x4b400000\n" \
  "s_mov_b32 s78, 0x3e0293ee\n" \
  "s_mov_b32 s76, 0xff00ff00\n" \
  "s_mov_b32 s77, 0xff00ff00\n" \
  "s_add_u32 s58, s56, s60\n" \
  "s_addc_u32 s59, s57, 0\n" \
  "s_lshl_b32 s98, s73, 10\n" \
  "s_add_u32 s98, s98, s74\n" \
  "s_add_u32 s98, s98, 131072\n" \
  "s_lshl_b32 s97, s73, 5\n" \
  "s_add_u32 s97, s97, s74\n" \
  "s_add_u32 s97, s97, 158720\n" \
  "s_lshl_b32 s99, s73, 10\n" \
  "v_mbcnt_lo_u32_b32 v48, -1, 0\n" \
  "v_mbcnt_hi_u32_b32 v48, -1, v48\n" \
  "v_and_b32 v49, 15, v48\n" \
  "v_lshl_add_u32 v8, v49, 1, s97\n" \
  "v_bfe_u32 v49, v48, 4, 1\n" \
  "v_mad_u32_u24 v8, v49, s92, v8\n" \
  "v_lshrrev_b32 v49, 5, v48\n" \
  "v_mad_u32_u24 v9, v49, s93, v8\n" \
  "v_mad_u32_u24 v8, v49, s94, v8\n" \
  "v_add_u32 v10, s99, v1\n" \
  "s_lshr_b32 s88, s99, 2\n" \
  "s_add_u32 s88, s88, s74\n" \
  "s_add_u32 s88, s88, 139264\n" \
  "v_lshl_add_u32 v7, v49, 10, s88\n" \
  "v_and_b32 v49, 31, v48\n" \
  "v_lshl_add_u32 v7, v49, 3, v7\n" \
  "v_mov_b64 v[12:13], 0\n" \
  "v_mov_b64 v[14:15], 0\n" \
  "v_mov_b64 v[16:17], 0\n" \
  "v_mov_b64 v[18:19], 0\n" \
  "v_mov_b64 v[20:21], 0\n" \
  "v_mov_b64 v[22:23], 0\n" \
  "s_add_u32 s88, s74, 157760\n" \
  "v_mov_b32 v59, s88\n" \
  "v_mov_b32 v24, 0x44870000\n" \
  "v_mul_f32 v24, s72, v24\n" \
  "s_nop 0\n" \
  "v_readfirstlane_b32 s86, v24\n" \
  "s_lshr_b32 s86, s86, 23\n" \
  "s_addk_i32 s86, 0xff83\n" \
  "s_ashr_i32 s86, s86, 1\n" \
  "s_max_i32 s86, s86, -20\n" \
  "s_min_i32 s86, s86, 100\n" \
  "s_mov_b32 s82, s86\n" \
  "s_lshr_b32 s83, s72, 23\n" \
  "s_sub_i32 s83, s83, 242\n" \
  "s_lshl_b32 s88, s73, 10\n" \
  "v_add_u32 v24, s88, v1\n" \
  "ds_read_b128 v[32:35], v24 offset:0\n" \
  "s_waitcnt lgkmcnt(0)\n" \
  "v_mov_b32 v11, 0\n" \
  "v_dot2c_f32_bf16 v11, v32, v32\n" \
  "v_dot2c_f32_bf16 v11, v33, v33\n" \
  "v_dot2c_f32_bf16 v11, v34, v34\n" \
  "v_dot2c_f32_bf16 v11, v35, v35\n" \
  "s_nop 1\n" \
  "s_nop 0\n" \
  "v_cmpx_lt_f32 vcc, 2.0, v11\n" \
  "ds_max_u32 v59, v11\n" \
  "s_mov_b64 exec, -1\n" \
  ".LCA_TOP_%=:\n" \
  "s_waitcnt lgkmcnt(0)\n" \
  "s_barrier\n" \
  "ds_read_b128 v[40:43], v1 offset:0\n" \
  "ds_read_b128 v[44:47], v1 offset:1024\n" \
  "ds_read_b128 v[48:51], v1 offset:2048\n" \
  "ds_read_b128 v[52:55], v1 offset:3072\n" \
  "ds_read_b128 v[56:59], v1 offset:4096\n" \
  "ds_read_b128 v[60:63], v1 offset:5120\n" \
  "s_cmp_lg_u32 s73, 0\n" \
  "s_cbranch_scc1 .LCA_NOZ_%=\n" \
  "s_xor_b32 s88, s60, 0x8000\n" \
  "s_add_u32 s88, s88, s61\n" \
  "buffer_store_dwordx2 v[12:13], v5, s[40:43], s88 offen sc1\n" \
  ".LCA_NOZ_%=:\n" \
  "s_waitcnt lgkmcnt(5)\n" \
  "v_smfmac_f32_16x16x64_bf16 v[20:23], v[40:43], v[192:199], v0\n" \
  "v_smfmac_f32_16x16x64_bf16 v[12:15], v[40:43], v[64:71], v0\n" \
  "v_smfmac_f32_16x16x64_bf16 v[16:19], v[40:43], v[128:135], v0\n" \
  "ds_read_b128 v[40:43], v1 offset:6144\n" \
  "s_waitcnt lgkmcnt(5)\n" \
  "v_smfmac_f32_16x16x64_bf16 v[20:23], v[44:47], v[200:207], v0\n" \
  "v_smfmac_f32_16x16x64_bf16 v[12:15], v[44:47], v[72:79], v0\n" \
  "v_smfmac_f32_16x16x64_bf16 v[16:19], v[44:47], v[136:143], v0\n" \
  "ds_read_b128 v[44:47], v1 offset:7168\n" \
  "s_waitcnt lgkmcnt(5)\n" \
  "v_smfmac_f32_16x16x64_bf16 v[20:23], v[48:51], v[208:215], v0\n" \
  "v_smfmac_f32_16x16x64_bf16 v[12:15], v[48:51], v[80:87], v0\n" \
  "v_smfmac_f32_16x16x64_bf16 v[16:19], v[48:51], v[144:151], v0\n" \
  "ds_read_b128 v[24:27], v1 offset:10240\n" \
  "ds_read_b128 v[28:31], v1 offset:14336\n" \
  "s_waitcnt lgkmcnt(6)\n" \
  "v_smfmac_f32_16x16x64_bf16 v[20:23], v[52:55], v[216:223], v0\n" \
  "v_smfmac_f32_16x16x64_bf16 v[12:15], v[52:55], v[88:95], v0\n" \
  "v_smfmac_f32_16x16x64_bf16 v[16:19], v[52:55], v[152:159], v0\n" \
  "ds_read_b128 v[32:35], v1 offset:11264\n" \
  "ds_read_b128 v[36:39], v1 offset:15360\n" \
  "s_waitcnt lgkmcnt(7)\n" \
  "v_smfmac_f32_16x16x64_bf16 v[20:23], v[56:59], v[224:231], v0\n" \
  "v_smfmac_f32_16x16x64_bf16 v[12:15], v[56:59], v[96:103], v0\n" \
  "v_smfmac_f32_16x16x64_bf16 v[16:19], v[56:59], v[160:167], v0\n" \
  "s_waitcnt lgkmcnt(6)\n" \
  "v_smfmac_f32_16x16x64_bf16 v[20:23], v[60:63], v[232:239], v0\n" \
  "v_smfmac_f32_16x16x64_bf16 v[12:15], v[60:63], v[104:111], v0\n" \
  "v_smfmac_f32_16x16x64_bf16 v[16:19], v[60:63], v[168:175], v0\n" \
  "s_waitcnt lgkmcnt(5)\n" \
  "v_smfmac_f32_16x16x64_bf16 v[20:23], v[40:43], v[240:247], v0\n" \
  "v_smfmac_f32_16x16x64_bf16 v[12:15], v[40:43], v[112:119], v0\n" \
  "v_smfmac_f32_16x16x64_bf16 v[16:19], v[40:43], v[176:183], v0\n" \
  "s_waitcnt lgkmcnt(4)\n" \
  "v_smfmac_f32_16x16x64_bf16 v[20:23], v[44:47], v[248:255], v0\n" \
  "v_smfmac_f32_16x16x64_bf16 v[12:15], v[44:47], v[120:127], v0\n" \
  "v_smfmac_f32_16x16x64_bf16 v[16:19], v[44:47], v[184:191], v0\n" \
  "s_cmp_eq_u32 s71, 0\n" \
  "s_nop 4\n" \
  "v_permlane32_swap_b32 v20, v22\n" \
  "v_permlane32_swap_b32 v21, v23\n" \
  "v_pk_add_f32 v[20:21], v[20:21], v[22:23]\n" \
  "v_permlane32_swap_b32 v12, v16\n" \
  "v_permlane32_swap_b32 v13, v17\n" \
  "v_cvt_pk_bf16_f32 v20, v20, v21\n" \
  "v_pk_add_f32 v[12:13], v[12:13], v[16:17]\n" \
  "v_permlane32_swap_b32 v14, v18\n" \
  "v_permlane32_swap_b32 v15, v19\n" \
  "ds_write_b16 v9, v20 offset:128\n" \
  "v_cvt_pk_bf16_f32 v12, v12, v13\n" \
  "v_pk_add_f32 v[14:15], v[14:15], v[18:19]\n" \
  "ds_write_b16_d16_hi v9, v20 offset:416\n" \
  "ds_write_b16 v8, v12\n" \
  "v_cvt_pk_bf16_f32 v14, v14, v15\n" \
  "ds_write_b16_d16_hi v8, v12 offset:288\n" \
  "ds_write_b16 v8, v14 offset:576\n" \
  "ds_write_b16_d16_hi v8, v14 offset:864\n" \
  "s_setprio 3\n" \
  "s_waitcnt lgkmcnt(0)\n" \
  "s_barrier\n" \
  "s_cbranch_scc1 .LCA_LAST_%=\n" \
  "ds_read_b32 v11, v1 offset:26688\n" \
  "ds_read_b128 v[40:43], v2 offset:0\n" \
  "ds_read_b128 v[44:47], v3 offset:0\n" \
  "ds_read_b128 v[48:51], v2 offset:64\n" \
  "ds_read_b128 v[52:55], v3 offset:64\n" \
  "ds_read_b128 v[56:59], v2 offset:128\n" \
  "ds_read_b128 v[60:63], v3 offset:128\n" \
  "s_waitcnt lgkmcnt(5)\n" \
  "v_mfma_f32_16x16x32_bf16 v[12:15], v[24:27], v[40:43], 0\n" \
  "v_mfma_f32_16x16x32_bf16 v[16:19], v[28:31], v[40:43], 0\n" \
  "v_readfirstlane_b32 s86, v11\n" \
  "ds_read_b128 v[24:27], v1 offset:12288\n" \
  "ds_read_b128 v[28:31], v1 offset:16384\n" \
  "s_waitcnt lgkmcnt(6)\n" \
  "v_mfma_f32_16x16x32_bf16 v[20:23], v[44:47], v[40:43], 0\n" \
  "s_lshr_b32 s86, s86, 23\n" \
  "s_add_i32 s86, s86, s83\n" \
  "ds_read_b128 v[40:43], v2 offset:192\n" \
  "ds_read_b128 v[44:47], v3 offset:192\n" \
  "s_waitcnt lgkmcnt(7)\n" \
  "v_mfma_f32_16x16x32_bf16 v[12:15], v[32:35], v[48:51], v[12:15]\n" \
  "v_mfma_f32_16x16x32_bf16 v[16:19], v[36:39], v[48:51], v[16:19]\n" \
  "s_ashr_i32 s86, s86, 1\n" \
  "s_max_i32 s86, s86, s82\n" \
  "ds_read_b128 v[32:35], v1 offset:13312\n" \
  "ds_read_b128 v[36:39], v1 offset:17408\n" \
  "s_waitcnt lgkmcnt(8)\n" \
  "v_mfma_f32_16x16x32_bf16 v[20:23], v[52:55], v[48:51], v[20:23]\n" \
  "s_min_i32 s86, s86, 100\n" \
  "s_lshl_b32 s86, s86, 23\n" \
  "s_sub_i32 s84, 0x4a800000, s86\n" \
  "ds_read_b128 v[48:51], v10 offset:18432\n" \
  "ds_read_b128 v[52:55], v10 offset:22528\n" \
  "s_waitcnt lgkmcnt(8)\n" \
  "v_mfma_f32_16x16x32_bf16 v[20:23], v[60:63], v[56:59], v[20:23]\n" \
  "s_waitcnt lgkmcnt(6)\n" \
  "v_mfma_f32_16x16x32_bf16 v[12:15], v[24:27], v[56:59], v[12:15]\n" \
  "v_mfma_f32_16x16x32_bf16 v[16:19], v[28:31], v[56:59], v[16:19]\n" \
  "ds_read_b128 v[56:59], v6 offset:0\n" \
  "ds_read_b128 v[60:63], v6 offset:1024\n" \
  "s_waitcnt lgkmcnt(6)\n" \
  "v_mfma_f32_16x16x32_bf16 v[20:23], v[44:47], v[40:43], v[20:23]\n" \
  "ds_read_b128 v[24:27], v6 offset:2048\n" \
  "ds_read_b128 v[28:31], v6 offset:3072\n" \
  "s_waitcnt lgkmcnt(6)\n" \
  "v_mfma_f32_16x16x32_bf16 v[12:15], v[32:35], v[40:43], v[12:15]\n" \
  "v_mfma_f32_16x16x32_bf16 v[16:19], v[36:39], v[40:43], v[16:19]\n" \
  "ds_read_b128 v[32:35], v6 offset:8192\n" \
  "ds_read_b128 v[36:39], v6 offset:9216\n" \
  "s_waitcnt lgkmcnt(6)\n" \
  "v_cndmask_b32 v20, v20, v21, s[76:77]\n" \
  "v_mov_b32 v41, 0\n" \
  "v_mov_b32 v42, 0\n" \
  "s_nop 1\n" \
  "v_cndmask_b32 v12, v12, v16, s[76:77]\n" \
  "v_cndmask_b32 v13, v13, v17, s[76:77]\n" \
  "v_cndmask_b32 v14, v14, v18, s[76:77]\n" \
  "v_cndmask_b32 v15, v15, v19, s[76:77]\n" \
  "v_maximum3_f32 v16, v20, v12, v13\n" \
  "v_maximum3_f32 v16, v16, v14, v15\n" \
  "s_nop 1\n" \
  "v_max_f32_dpp v16, v16, v16 row_ror:8 row_mask:0xf bank_mask:0xf\n" \
  "v_mov_b32 v17, v16\n" \
  "s_nop 1\n" \
  "v_permlane16_swap_b32 v16, v17\n" \
  "v_max_f32 v16, v16, v17\n" \
  "v_mov_b32 v17, v16\n" \
  "s_nop 1\n" \
  "v_permlane32_swap_b32 v16, v17\n" \
  "v_max_f32 v16, v16, v17\n" \
  "v_mul_f32 v16, s78, v16\n" \
  "v_pk_fma_f32 v[12:13], v[12:13], s[78:79], v[16:17] op_sel_hi:[1,0,0] neg_lo:[0,0,1] neg_hi:[0,0,1]\n" \
  "v_pk_fma_f32 v[14:15], v[14:15], s[78:79], v[16:17] op_sel_hi:[1,0,0] neg_lo:[0,0,1] neg_hi:[0,0,1]\n" \
  "v_fma_f32 v20, v20, s78, -v16\n" \
  "v_exp_f32 v12, v12\n" \
  "v_exp_f32 v13, v13\n" \
  "v_exp_f32 v14, v14\n" \
  "v_exp_f32 v15, v15\n" \
  "v_exp_f32 v20, v20\n" \
  "s_barrier\n" \
  "ds_read_b64 v[22:23], v7 offset:0\n" \
  "v_cvt_pk_bf16_f32 v44, v12, v13\n" \
  "v_cvt_pk_bf16_f32 v45, v14, v15\n" \
  "v_pk_add_f32 v[8:9], v[12:13], v[14:15]\n" \
  "v_mov_b32_dpp v46, v44 row_ror:8 row_mask:0xf bank_mask:0xf\n" \
  "v_mov_b32_dpp v17, v20 row_ror:8 row_mask:0xf bank_mask:0xf\n" \
  "v_mov_b32_dpp v47, v45 row_ror:8 row_mask:0xf bank_mask:0xf\n" \
  "v_add_f32 v10, v8, v20\n" \
  "v_cvt_pk_bf16_f32 v40, v20, v17\n" \
  "v_add_f32 v10, v10, v9\n" \
  "v_mfma_f32_16x16x32_bf16 v[12:15], v[48:51], v[44:47], 0\n" \
  "v_cvt_pk_bf16_f32 v43, v20, v17\n" \
  "v_add_f32_dpp v10, v10, v10 row_ror:8 row_mask:0xf bank_mask:0xf\n" \
  "v_mfma_f32_16x16x32_bf16 v[16:19], v[52:55], v[44:47], 0\n" \
  "v_mov_b32 v11, v10\n" \
  "s_waitcnt lgkmcnt(0)\n" \
  "v_mfma_f32_16x16x16_bf16 v[12:15], v[22:23], v[40:41], v[12:15]\n" \
  "v_mfma_f32_16x16x16_bf16 v[16:19], v[22:23], v[42:43], v[16:19]\n" \
  "v_permlane16_swap_b32 v10, v11\n" \
  "v_add_f32 v10, v10, v11\n" \
  "ds_read_b128 v[48:51], v6 offset:10240\n" \
  "v_mov_b32 v11, v10\n" \
  "ds_read_b128 v[52:55], v6 offset:11264\n" \
  "v_mov_b64 v[20:21], 0\n" \
  "v_mov_b64 v[22:23], 0\n" \
  "v_permlane32_swap_b32 v10, v11\n" \
  "v_add_f32 v10, v10, v11\n" \
  "v_rcp_f32 v10, v10\n" \
  "s_nop 0\n" \
  "v_pk_mul_f32 v[12:13], v[12:13], v[10:11] op_sel_hi:[1,0]\n" \
  "v_pk_mul_f32 v[14:15], v[14:15], v[10:11] op_sel_hi:[1,0]\n" \
  "v_pk_mul_f32 v[16:17], v[16:17], v[10:11] op_sel_hi:[1,0]\n" \
  "v_pk_mul_f32 v[18:19], v[18:19], v[10:11] op_sel_hi:[1,0]\n" \
  "v_cvt_pk_bf16_f32 v40, v12, v13\n" \
  "v_cvt_pk_bf16_f32 v41, v14, v15\n" \
  "v_cvt_pk_bf16_f32 v42, v16, v17\n" \
  "ds_write_b64 v4, v[40:41]\n" \
  "v_cvt_pk_bf16_f32 v43, v18, v19\n" \
  "ds_write_b64 v4, v[42:43] offset:1024\n" \
  "v_mov_b64 v[12:13], 0\n" \
  "v_mov_b64 v[14:15], 0\n" \
  "v_mov_b64 v[16:17], 0\n" \
  "v_mov_b64 v[18:19], 0\n" \
  "v_mov_b64 v[8:9], 0\n" \
  "v_mov_b64 v[10:11], 0\n" \
  "s_setprio 0\n" \
  "s_waitcnt lgkmcnt(0)\n" \
  "s_barrier\n" \
  "ds_read_b128 v[40:43], v1 offset:8192\n" \
  "ds_read_b128 v[44:47], v1 offset:9216\n" \
  "s_waitcnt lgkmcnt(1)\n" \
  "v_smfmac_f32_16x16x64_bf16 v[12:15], v[40:43], v[56:63], v0\n" \
  "ds_read_b128 v[56:59], v6 offset:4096\n" \
  "ds_read_b128 v[60:63], v6 offset:5120\n" \
  "v_smfmac_f32_16x16x64_bf16 v[16:19], v[40:43], v[24:31], v0\n" \
  "ds_read_b128 v[24:27], v6 offset:6144\n" \
  "ds_read_b128 v[28:31], v6 offset:7168\n" \
  "s_waitcnt lgkmcnt(4)\n" \
  "v_smfmac_f32_16x16x64_bf16 v[12:15], v[44:47], v[32:39], v0\n" \
  "ds_read_b128 v[32:35], v6 offset:12288\n" \
  "ds_read_b128 v[36:39], v6 offset:13312\n" \
  "v_smfmac_f32_16x16x64_bf16 v[16:19], v[44:47], v[48:55], v0\n" \
  "ds_read_b128 v[48:51], v6 offset:14336\n" \
  "ds_read_b128 v[52:55], v6 offset:15360\n" \
  "s_nop 0\n" \
  "s_waitcnt lgkmcnt(6)\n" \
  "v_smfmac_f32_16x16x64_bf16 v[20:23], v[40:43], v[56:63], v0\n" \
  "s_nop 0\n" \
  "s_waitcnt lgkmcnt(4)\n" \
  "v_smfmac_f32_16x16x64_bf16 v[8:11], v[40:43], v[24:31], v0\n" \
  "v_permlane32_swap_b32 v12, v17\n" \
  "v_permlane32_swap_b32 v13, v16\n" \
  "v_permlane32_swap_b32 v14, v19\n" \
  "v_permlane32_swap_b32 v15, v18\n" \
  "s_waitcnt lgkmcnt(2)\n" \
  "v_smfmac_f32_16x16x64_bf16 v[20:23], v[44:47], v[32:39], v0\n" \
  "v_pk_add_f32 v[12:13], v[12:13], v[16:17] op_sel:[0,1] op_sel_hi:[1,0]\n" \
  "v_pk_add_f32 v[14:15], v[14:15], v[18:19] op_sel:[0,1] op_sel_hi:[1,0]\n" \
  "v_pk_fma_f32 v[12:13], v[12:13], s[84:85], s[84:85] op_sel:[0,0,1] op_sel_hi:[1,0,1]\n" \
  "v_pk_fma_f32 v[14:15], v[14:15], s[84:85], s[84:85] op_sel:[0,0,1] op_sel_hi:[1,0,1]\n" \
  "s_waitcnt lgkmcnt(0)\n" \
  "v_smfmac_f32_16x16x64_bf16 v[8:11], v[44:47], v[48:55], v0\n" \
  "v_lshl_add_u32 v12, v12, 6, s91\n" \
  "v_lshl_add_u32 v13, v13, 2, s75\n" \
  "global_atomic_add_x2 v5, v[12:13], s[58:59] offset:0\n" \
  "v_lshl_add_u32 v14, v14, 6, s91\n" \
  "v_lshl_add_u32 v15, v15, 2, s75\n" \
  "global_atomic_add_x2 v5, v[14:15], s[58:59] offset:2048\n" \
  "s_nop 1\n" \
  "v_permlane32_swap_b32 v20, v9\n" \
  "v_permlane32_swap_b32 v21, v8\n" \
  "v_permlane32_swap_b32 v22, v11\n" \
  "v_permlane32_swap_b32 v23, v10\n" \
  "v_pk_add_f32 v[20:21], v[20:21], v[8:9] op_sel:[0,1] op_sel_hi:[1,0]\n" \
  "v_pk_add_f32 v[22:23], v[22:23], v[10:11] op_sel:[0,1] op_sel_hi:[1,0]\n" \
  "v_pk_fma_f32 v[20:21], v[20:21], s[84:85], s[84:85] op_sel:[0,0,1] op_sel_hi:[1,0,1]\n" \
  "v_pk_fma_f32 v[22:23], v[22:23], s[84:85], s[84:85] op_sel:[0,0,1] op_sel_hi:[1,0,1]\n" \
  "v_lshl_add_u32 v20, v20, 6, s91\n" \
  "v_lshl_add_u32 v21, v21, 2, s75\n" \
  "global_atomic_add_x2 v5, v[20:21], s[58:59] offset:3072\n" \
  "v_lshl_add_u32 v22, v22, 6, s91\n" \
  "v_lshl_add_u32 v23, v23, 2, s75\n" \
  "global_atomic_add_x2 v5, v[22:23], s[58:59] offset:1024\n" \
  "buffer_load_dword v52, v5, s[0:3], s66 offen sc1\n" \
  "v_mbcnt_lo_u32_b32 v48, -1, 0\n" \
  "v_mbcnt_hi_u32_b32 v48, -1, v48\n" \
  "v_and_b32 v49, 15, v48\n" \
  "v_lshl_add_u32 v8, v49, 1, s97\n" \
  "v_bfe_u32 v49, v48, 4, 1\n" \
  "v_mad_u32_u24 v8, v49, s92, v8\n" \
  "v_lshrrev_b32 v49, 5, v48\n" \
  "v_mad_u32_u24 v9, v49, s93, v8\n" \
  "v_mad_u32_u24 v8, v49, s94, v8\n" \
  "v_add_u32 v10, s99, v1\n" \
  "s_lshr_b32 s88, s99, 2\n" \
  "s_add_u32 s88, s88, s74\n" \
  "s_add_u32 s88, s88, 139264\n" \
  "v_lshl_add_u32 v7, v49, 10, s88\n" \
  "v_and_b32 v49, 31, v48\n" \
  "v_lshl_add_u32 v7, v49, 3, v7\n" \
  "v_mov_b64 v[12:13], 0\n" \
  "v_mov_b64 v[14:15], 0\n" \
  "v_mov_b64 v[16:17], 0\n" \
  "v_mov_b64 v[18:19], 0\n" \
  "v_mov_b64 v[20:21], 0\n" \
  "v_mov_b64 v[22:23], 0\n" \
  "s_add_u32 s88, s74, 157760\n" \
  "v_mov_b32 v59, s88\n" \
  "v_lshrrev_b32 v49, 3, v48\n" \
  "v_and_b32 v50, 7, v48\n" \
  "v_lshlrev_b32 v50, 4, v50\n" \
  "v_lshl_or_b32 v44, v49, 8, v50\n" \
  "v_and_b32 v50, 3, v48\n" \
  "v_lshl_add_u32 v40, v50, 2, s98\n" \
  "v_lshrrev_b32 v50, 4, v48\n" \
  "v_mad_u32_u24 v40, v50, s95, v40\n" \
  "v_and_b32 v49, 1, v49\n" \
  "v_lshl_add_u32 v40, v49, 6, v40\n" \
  "v_bfe_u32 v49, v48, 2, 1\n" \
  "v_lshl_add_u32 v40, v49, 7, v40\n" \
  "v_xor_b32 v41, 16, v40\n" \
  "v_xor_b32 v42, 32, v40\n" \
  "v_xor_b32 v43, 48, v40\n" \
  "s_add_i32 s80, s86, 0x32800000\n" \
  "s_add_i32 s81, s86, 0x33800000\n" \
  "s_add_i32 s88, s86, 0xb4000000\n" \
  "v_mov_b32 v46, s88\n" \
  "v_mov_b32 v47, 0\n" \
  "v_mov_b32 v11, 0\n" \
  "s_mov_b32 s89, s60\n" \
  "s_xor_b32 s60, s60, 0x80\n" \
  "s_bitcmp0_b32 s60, 7\n" \
  "s_cselect_b32 s88, 0x8000, 0\n" \
  "s_add_u32 s60, s60, s88\n" \
  "s_and_b32 s60, s60, 0xffff\n" \
  "s_add_u32 s58, s56, s60\n" \
  "s_addc_u32 s59, s57, 0\n" \
  "s_sub_u32 s71, s71, 1\n" \
  "s_mov_b32 s87, 0x40000\n" \
  "s_waitcnt vmcnt(0)\n" \
  "s_memrealtime s[38:39]\n" \
  ".LCA_CHK_%=:\n" \
  "v_cmp_eq_u32 vcc, 0, v52\n" \
  "v_readlane_b32 s88, v52, s79\n" \
  "s_cmp_eq_u64 vcc, 0\n" \
  "s_cbranch_scc1 .LCA_OKM_%=\n" \
  "s_sleep 4\n" \
  "buffer_load_dword v52, v5, s[0:3], s66 offen sc1\n" \
  "s_sub_u32 s87, s87, 1\n" \
  "s_cbranch_scc1 .LCA_END_%=\n" \
  "s_waitcnt vmcnt(0)\n" \
  "s_branch .LCA_CHK_%=\n" \
  ".LCA_OKM_%=:\n" \
  "v_subrev_u32 v52, s88, v52\n" \
  "s_nop 1\n" \
  "v_max_i32_dpp v52, v52, v52 row_ror:1 row_mask:0xf bank_mask:0xf\n" \
  "s_nop 1\n" \
  "v_max_i32_dpp v52, v52, v52 row_ror:2 row_mask:0xf bank_mask:0xf\n" \
  "s_nop 1\n" \
  "v_max_i32_dpp v52, v52, v52 row_ror:4 row_mask:0xf bank_mask:0xf\n" \
  "s_add_u32 s66, s66, 1024\n" \
  "v_readlane_b32 vcc_lo, v52, 0\n" \
  "v_readlane_b32 s88, v52, 16\n" \
  "s_max_i32 vcc_lo, vcc_lo, s88\n" \
  "v_readlane_b32 s88, v52, 32\n" \
  "s_max_i32 vcc_lo, vcc_lo, s88\n" \
  "v_readlane_b32 s88, v52, 48\n" \
  "s_max_i32 vcc_lo, vcc_lo, s88\n" \
  "s_waitcnt lgkmcnt(0)\n" \
  "s_add_u32 vcc_lo, vcc_lo, s38\n" \
  "s_add_u32 vcc_lo, vcc_lo, -6\n" \
  ".LCA_TW_%=:\n" \
  "s_memrealtime s[38:39]\n" \
  "s_waitcnt lgkmcnt(0)\n" \
  "s_sub_u32 s38, s38, vcc_lo\n" \
  "s_cmp_lt_i32 s38, 0\n" \
  "s_cbranch_scc1 .LCA_TW_%=\n" \
  ".LCA_POLL_%=:\n" \
  "buffer_load_dwordx4 v[24:27], v44, s[40:43], s89 offen sc1\n" \
  "buffer_load_dwordx4 v[28:31], v44, s[40:43], s89 offen offset:2048 sc1\n" \
  "s_sub_u32 s87, s87, 1\n" \
  "s_cbranch_scc1 .LCA_END_%=\n" \
  "s_waitcnt vmcnt(1)\n" \
  "v_bitop3_b32 v45, v24, v26, 32 bitop3:0x80\n" \
  "v_alignbit_b32 v32, v25, v24, 2\n" \
  "v_alignbit_b32 v34, v27, v26, 2\n" \
  "s_waitcnt vmcnt(0)\n" \
  "v_bitop3_b32 v45, v45, v28, v30 bitop3:0x80\n" \
  "v_cmp_eq_u32 vcc, 0, v45\n" \
  "v_cvt_f32_i32 v32, v32\n" \
  "v_cvt_f32_i32 v33, v25\n" \
  "s_cmp_eq_u64 vcc, 0\n" \
  "s_cbranch_scc0 .LCA_POLL_%=\n" \
  "v_cvt_f32_i32 v34, v34\n" \
  "v_cvt_f32_i32 v35, v27\n" \
  "v_pk_fma_f32 v[32:33], v[32:33], s[80:81], v[46:47]\n" \
  "v_alignbit_b32 v36, v29, v28, 2\n" \
  "v_pk_fma_f32 v[34:35], v[34:35], s[80:81], v[46:47]\n" \
  "v_alignbit_b32 v38, v31, v30, 2\n" \
  "v_cvt_pk_bf16_f32 v32, v32, v34\n" \
  "v_cvt_pk_bf16_f32 v33, v33, v35\n" \
  "v_cvt_f32_i32 v36, v36\n" \
  "ds_write_b32 v40, v32\n" \
  "v_cvt_f32_i32 v37, v29\n" \
  "ds_write_b32 v41, v33\n" \
  "v_cvt_f32_i32 v38, v38\n" \
  "v_cvt_f32_i32 v39, v31\n" \
  "v_pk_fma_f32 v[36:37], v[36:37], s[80:81], v[46:47]\n" \
  "v_pk_fma_f32 v[38:39], v[38:39], s[80:81], v[46:47]\n" \
  "v_dot2c_f32_bf16 v11, v32, v32\n" \
  "v_cvt_pk_bf16_f32 v36, v36, v38\n" \
  "v_cvt_pk_bf16_f32 v37, v37, v39\n" \
  "ds_write_b32 v42, v36\n" \
  "ds_write_b32 v43, v37\n" \
  "v_dot2c_f32_bf16 v11, v33, v33\n" \
  "v_dot2c_f32_bf16 v11, v36, v36\n" \
  "v_dot2c_f32_bf16 v11, v37, v37\n" \
  "s_nop 1\n" \
  "s_nop 0\n" \
  "v_cmpx_lt_f32 vcc, 2.0, v11\n" \
  "ds_max_u32 v59, v11\n" \
  "s_mov_b64 exec, -1\n" \
  "s_branch .LCA_TOP_%=\n" \
  ".LCA_LAST_%=:\n" \
  ".LCA_END_%=:\n" \
  "s_endpgm\n"
#define CHAIN_ASM_VO \
  "s_mov_b32 s92, 1152\n" \
  "s_mov_b32 s93, 576\n" \
  "s_mov_b32 s94, 2272\n" \
  "s_mov_b32 s95, 272\n" \
  "s_mov_b32 s96, 288\n" \
  "s_mov_b32 s91, 0x40000001\n" \
  "s_mov_b32 s84, 0x4b400000\n" \
  "s_mov_b32 s85, 0x4b400000\n" \
  "s_mov_b32 s78, 0x3e0293ee\n" \
  "s_mov_b32 s76, 0xff00ff00\n" \
  "s_mov_b32 s77, 0xff00ff00\n" \
  "s_add_u32 s58, s56, s60\n" \
  "s_addc_u32 s59, s57, 0\n" \
  "s_lshl_b32 s98, s73, 10\n" \
  "s_add_u32 s98, s98, s74\n" \
  "s_add_u32 s98, s98, 131072\n" \
  "s_lshl_b32 s97, s73, 5\n" \
  "s_add_u32 s97, s97, s74\n" \
  "s_add_u32 s97, s97, 160992\n" \
  "s_lshl_b32 s99, s73, 11\n" \
  "s_add_u32 s99, s99, s74\n" \
  "s_add_u32 s99, s99, 133120\n" \
  "v_mbcnt_lo_u32_b32 v48, -1, 0\n" \
  "v_mbcnt_hi_u32_b32 v48, -1, v48\n" \
  "s_sub_u32 s88, s73, 4\n" \
  "v_and_b32 v51, 15, v48\n" \
  "v_lshl_add_u32 v4, v51, 1, s97\n" \
  "v_bfe_u32 v49, v48, 4, 1\n" \
  "v_mad_u32_u24 v4, v49, s92, v4\n" \
  "v_lshrrev_b32 v50, 5, v48\n" \
  "v_mad_u32_u24 v4, v50, s93, v4\n" \
  "v_lshlrev_b32 v8, 4, v49\n" \
  "v_lshl_add_u32 v8, v51, 5, v8\n" \
  "v_lshl_add_u32 v8, v50, 3, v8\n" \
  "s_lshl_b32 s89, s88, 8\n" \
  "s_add_u32 s89, s89, s74\n" \
  "s_add_u32 s89, s89, 139264\n" \
  "v_and_b32 v49, 31, v48\n" \
  "v_lshl_add_u32 v5, v49, 3, s89\n" \
  "v_lshl_add_u32 v5, v50, 10, v5\n" \
  "v_lshrrev_b32 v49, 2, v51\n" \
  "v_and_b32 v50, 5, v51\n" \
  "v_and_or_b32 v49, v49, 2, v50\n" \
  "s_lshl_b32 s89, s88, 6\n" \
  "s_add_u32 s89, s89, s74\n" \
  "s_add_u32 s89, s89, 160992\n" \
  "v_and_b32 v50, 48, v48\n" \
  "v_add_u32 v6, s89, v50\n" \
  "v_mad_u32_u24 v6, v49, s96, v6\n" \
  "v_lshlrev_b32 v7, 3, v50\n" \
  "v_lshl_add_u32 v7, v49, 4, v7\n" \
  "v_and_b32 v49, 2, v51\n" \
  "v_lshl_add_u32 v7, v49, 29, v7\n" \
  "v_mov_b32 v9, 0\n" \
  "v_lshlrev_b32 v10, 4, v48\n" \
  "v_mov_b64 v[12:13], 0\n" \
  "v_mov_b64 v[14:15], 0\n" \
  "v_mov_b64 v[16:17], 0\n" \
  "v_mov_b64 v[18:19], 0\n" \
  "v_mov_b64 v[20:21], 0\n" \
  "v_mov_b64 v[22:23], 0\n" \
  "s_add_u32 s88, s74, 157760\n" \
  "v_mov_b32 v59, s88\n" \
  "v_mov_b32 v24, 0x44870000\n" \
  "v_mul_f32 v24, s72, v24\n" \
  "s_nop 0\n" \
  "v_readfirstlane_b32 s86, v24\n" \
  "s_lshr_b32 s86, s86, 23\n" \
  "s_addk_i32 s86, 0xff83\n" \
  "s_ashr_i32 s86, s86, 1\n" \
  "s_max_i32 s86, s86, -20\n" \
  "s_min_i32 s86, s86, 100\n" \
  "s_mov_b32 s82, s86\n" \
  "s_lshr_b32 s83, s72, 23\n" \
  "s_sub_i32 s83, s83, 242\n" \
  "s_lshl_b32 s88, s73, 10\n" \
  "v_add_u32 v24, s88, v1\n" \
  "ds_read_b128 v[32:35], v24 offset:0\n" \
  "s_waitcnt lgkmcnt(0)\n" \
  "v_mov_b32 v11, 0\n" \
  "v_dot2c_f32_bf16 v11, v32, v32\n" \
  "v_dot2c_f32_bf16 v11, v33, v33\n" \
  "v_dot2c_f32_bf16 v11, v34, v34\n" \
  "v_dot2c_f32_bf16 v11, v35, v35\n" \
  "s_nop 1\n" \
  "s_nop 0\n" \
  "v_cmpx_lt_f32 vcc, 2.0, v11\n" \
  "ds_max_u32 v59, v11\n" \
  "s_mov_b64 exec, -1\n" \
  ".LCV_TOP_%=:\n" \
  "s_waitcnt lgkmcnt(0)\n" \
  "s_barrier\n" \
  "s_setprio 3\n" \
  "ds_read_b128 v[24:27], v1 offset:0\n" \
  "ds_read_b128 v[28:31], v1 offset:1024\n" \
  "ds_read_b128 v[32:35], v1 offset:2048\n" \
  "ds_read_b128 v[36:39], v1 offset:3072\n" \
  "ds_read_b128 v[40:43], v1 offset:4096\n" \
  "ds_read_b128 v[44:47], v1 offset:5120\n" \
  "ds_read_b128 v[48:51], v1 offset:6144\n" \
  "ds_read_b128 v[52:55], v1 offset:7168\n" \
  "buffer_load_dword v58, v9, s[44:47], s68 offen sc1\n" \
  "s_waitcnt lgkmcnt(7)\n" \
  "v_smfmac_f32_16x16x64_bf16 v[12:15], v[24:27], v[64:71], v0\n" \
  "s_waitcnt lgkmcnt(6)\n" \
  "v_smfmac_f32_16x16x64_bf16 v[12:15], v[28:31], v[72:79], v0\n" \
  "s_waitcnt lgkmcnt(5)\n" \
  "v_smfmac_f32_16x16x64_bf16 v[12:15], v[32:35], v[80:87], v0\n" \
  "s_waitcnt lgkmcnt(4)\n" \
  "v_smfmac_f32_16x16x64_bf16 v[12:15], v[36:39], v[88:95], v0\n" \
  "s_waitcnt lgkmcnt(3)\n" \
  "v_smfmac_f32_16x16x64_bf16 v[12:15], v[40:43], v[96:103], v0\n" \
  "s_waitcnt lgkmcnt(2)\n" \
  "v_smfmac_f32_16x16x64_bf16 v[12:15], v[44:47], v[104:111], v0\n" \
  "s_waitcnt lgkmcnt(1)\n" \
  "v_smfmac_f32_16x16x64_bf16 v[12:15], v[48:51], v[112:119], v0\n" \
  "s_waitcnt lgkmcnt(0)\n" \
  "v_smfmac_f32_16x16x64_bf16 v[12:15], v[52:55], v[120:127], v0\n" \
  "s_memrealtime s[38:39]\n" \
  "ds_read_b32 v11, v1 offset:26688\n" \
  "s_nop 6\n" \
  "v_permlane32_swap_b32 v12, v14\n" \
  "v_permlane32_swap_b32 v13, v15\n" \
  "v_pk_add_f32 v[12:13], v[12:13], v[14:15]\n" \
  "s_nop 0\n" \
  "v_cvt_pk_bf16_f32 v12, v12, v13\n" \
  "ds_write_b16 v4, v12\n" \
  "ds_write_b16_d16_hi v4, v12 offset:288\n" \
  "s_setprio 0\n" \
  "v_mov_b64 v[12:13], 0\n" \
  "v_mov_b64 v[14:15], 0\n" \
  "s_waitcnt lgkmcnt(2)\n" \
  "v_readfirstlane_b32 s86, v11\n" \
  "s_lshr_b32 s86, s86, 23\n" \
  "s_add_i32 s86, s86, s83\n" \
  "s_ashr_i32 s86, s86, 1\n" \
  "s_max_i32 s86, s86, s82\n" \
  "s_min_i32 s86, s86, 100\n" \
  "s_lshl_b32 s86, s86, 23\n" \
  "s_sub_i32 s84, 0x4a800000, s86\n" \
  "s_cmp_eq_u32 s71, 0\n" \
  "s_waitcnt lgkmcnt(0)\n" \
  "s_barrier\n" \
  "ds_read_b128 v[60:63], v6 offset:0\n" \
  "v_smfmac_f32_16x16x64_bf16 v[16:19], v[24:27], v[128:135], v0\n" \
  "v_or_b32 v57, 1, s38\n" \
  "v_smfmac_f32_16x16x64_bf16 v[20:23], v[24:27], v[192:199], v0\n" \
  "v_add_u32 v56, s67, v10\n" \
  "v_smfmac_f32_16x16x64_bf16 v[16:19], v[28:31], v[136:143], v0\n" \
  "s_mov_b64 exec, 0x11\n" \
  "buffer_store_dword v57, v56, s[0:3], 0 offen sc1\n" \
  "s_mov_b64 exec, -1\n" \
  "v_smfmac_f32_16x16x64_bf16 v[20:23], v[28:31], v[200:207], v0\n" \
  "v_smfmac_f32_16x16x64_bf16 v[16:19], v[32:35], v[144:151], v0\n" \
  "s_waitcnt lgkmcnt(0)\n" \
  "v_max_u32 v60, 1, v60\n" \
  "v_smfmac_f32_16x16x64_bf16 v[20:23], v[32:35], v[208:215], v0\n" \
  "v_max_u32 v61, 1, v61\n" \
  "v_smfmac_f32_16x16x64_bf16 v[16:19], v[36:39], v[152:159], v0\n" \
  "v_max_u32 v62, 1, v62\n" \
  "v_smfmac_f32_16x16x64_bf16 v[20:23], v[36:39], v[216:223], v0\n" \
  "v_max_u32 v63, 1, v63\n" \
  "v_smfmac_f32_16x16x64_bf16 v[16:19], v[40:43], v[160:167], v0\n" \
  "buffer_store_dwordx4 v[60:63], v7, s[48:51], s69 offen sc1\n" \
  "v_smfmac_f32_16x16x64_bf16 v[20:23], v[40:43], v[224:231], v0\n" \
  "v_smfmac_f32_16x16x64_bf16 v[16:19], v[44:47], v[168:175], v0\n" \
  "v_smfmac_f32_16x16x64_bf16 v[20:23], v[44:47], v[232:239], v0\n" \
  "v_smfmac_f32_16x16x64_bf16 v[16:19], v[48:51], v[176:183], v0\n" \
  "v_smfmac_f32_16x16x64_bf16 v[20:23], v[48:51], v[240:247], v0\n" \
  "v_smfmac_f32_16x16x64_bf16 v[16:19], v[52:55], v[184:191], v0\n" \
  "v_smfmac_f32_16x16x64_bf16 v[20:23], v[52:55], v[248:255], v0\n" \
  "ds_read_b128 v[24:27], v3 offset:0\n" \
  "ds_read_b128 v[28:31], v3 offset:1024\n" \
  "ds_read_b128 v[32:35], v3 offset:2048\n" \
  "ds_read_b128 v[36:39], v3 offset:3072\n" \
  "ds_read_b128 v[40:43], v3 offset:8192\n" \
  "ds_read_b128 v[44:47], v3 offset:9216\n" \
  "s_nop 0\n" \
  "v_permlane32_swap_b32 v16, v18\n" \
  "v_permlane32_swap_b32 v17, v19\n" \
  "v_permlane32_swap_b32 v20, v22\n" \
  "v_permlane32_swap_b32 v21, v23\n" \
  "v_pk_add_f32 v[16:17], v[16:17], v[18:19]\n" \
  "v_pk_add_f32 v[20:21], v[20:21], v[22:23]\n" \
  "v_cvt_pk_bf16_f32 v16, v16, v17\n" \
  "v_cvt_pk_bf16_f32 v17, v20, v21\n" \
  "ds_write_b64 v5, v[16:17]\n" \
  "v_max_u32 v20, 1, v16\n" \
  "v_max_u32 v21, 1, v17\n" \
  "s_nop 1\n" \
  "v_permlane32_swap_b32 v20, v21\n" \
  "buffer_store_dwordx2 v[20:21], v8, s[52:55], s70 offen sc1\n" \
  "s_cbranch_scc1 .LCV_LAST_%=\n" \
  "s_waitcnt vmcnt(3)\n" \
  "v_cmp_eq_u32 vcc, 0, v58\n" \
  "s_cmp_eq_u64 vcc, 0\n" \
  "s_cselect_b32 s90, 1, 0\n" \
  "s_waitcnt lgkmcnt(0)\n" \
  "s_barrier\n" \
  "s_cbranch_scc0 .LCV_NOTOK_%=\n" \
  "s_mov_b32 m0, s99\n" \
  "s_add_u32 s88, s99, 8192\n" \
  "global_load_lds_dwordx4 v10, s[62:63] sc1\n" \
  "global_load_lds_dwordx4 v10, s[62:63] offset:1024 sc1\n" \
  "s_mov_b32 m0, s88\n" \
  "s_nop 0\n" \
  "global_load_lds_dwordx4 v10, s[64:65] sc1\n" \
  "global_load_lds_dwordx4 v10, s[64:65] offset:1024 sc1\n" \
  ".LCV_NOTOK_%=:\n" \
  "s_add_u32 s67, s67, 1024\n" \
  "ds_write_b32 v59, v12\n" \
  "ds_read_b128 v[48:51], v3 offset:10240\n" \
  "ds_read_b128 v[52:55], v3 offset:11264\n" \
  "v_mov_b64 v[18:19], 0\n" \
  "v_mov_b64 v[20:21], 0\n" \
  "v_mov_b64 v[22:23], 0\n" \
  "v_mov_b64 v[16:17], 0\n" \
  "ds_read_b128 v[56:59], v3 offset:4096\n" \
  "ds_read_b128 v[60:63], v3 offset:5120\n" \
  "s_waitcnt lgkmcnt(0)\n" \
  "s_barrier\n" \
  "ds_read_b128 v[4:7], v1 offset:8192\n" \
  "ds_read_b128 v[8:11], v1 offset:9216\n" \
  "s_waitcnt lgkmcnt(1)\n" \
  "v_smfmac_f32_16x16x64_bf16 v[12:15], v[4:7], v[24:31], v0\n" \
  "ds_read_b128 v[24:27], v3 offset:6144\n" \
  "ds_read_b128 v[28:31], v3 offset:7168\n" \
  "v_smfmac_f32_16x16x64_bf16 v[16:19], v[4:7], v[32:39], v0\n" \
  "ds_read_b128 v[32:35], v3 offset:12288\n" \
  "ds_read_b128 v[36:39], v3 offset:13312\n" \
  "s_waitcnt lgkmcnt(4)\n" \
  "v_smfmac_f32_16x16x64_bf16 v[12:15], v[8:11], v[40:47], v0\n" \
  "v_smfmac_f32_16x16x64_bf16 v[16:19], v[8:11], v[48:55], v0\n" \
  "v_smfmac_f32_16x16x64_bf16 v[20:23], v[4:7], v[56:63], v0\n" \
  "ds_read_b128 v[56:59], v3 offset:14336\n" \
  "ds_read_b128 v[60:63], v3 offset:15360\n" \
  "v_mov_b64 v[40:41], 0\n" \
  "v_mov_b64 v[42:43], 0\n" \
  "s_waitcnt lgkmcnt(2)\n" \
  "v_smfmac_f32_16x16x64_bf16 v[20:23], v[8:11], v[32:39], v0\n" \
  "s_nop 0\n" \
  "v_permlane32_swap_b32 v12, v17\n" \
  "v_permlane32_swap_b32 v13, v16\n" \
  "v_permlane32_swap_b32 v14, v19\n" \
  "v_permlane32_swap_b32 v15, v18\n" \
  "v_smfmac_f32_16x16x64_bf16 v[40:43], v[4:7], v[24:31], v0\n" \
  "v_pk_add_f32 v[12:13], v[12:13], v[16:17] op_sel:[0,1] op_sel_hi:[1,0]\n" \
  "v_pk_add_f32 v[14:15], v[14:15], v[18:19] op_sel:[0,1] op_sel_hi:[1,0]\n" \
  "v_pk_fma_f32 v[12:13], v[12:13], s[84:85], s[84:85] op_sel:[0,0,1] op_sel_hi:[1,0,1]\n" \
  "v_pk_fma_f32 v[14:15], v[14:15], s[84:85], s[84:85] op_sel:[0,0,1] op_sel_hi:[1,0,1]\n" \
  "s_waitcnt lgkmcnt(0)\n" \
  "v_smfmac_f32_16x16x64_bf16 v[40:43], v[8:11], v[56:63], v0\n" \
  "v_lshl_add_u32 v12, v12, 6, s91\n" \
  "v_lshl_add_u32 v13, v13, 2, s75\n" \
  "global_atomic_add_x2 v2, v[12:13], s[58:59] offset:0\n" \
  "v_lshl_add_u32 v14, v14, 6, s91\n" \
  "v_lshl_add_u32 v15, v15, 2, s75\n" \
  "global_atomic_add_x2 v2, v[14:15], s[58:59] offset:2048\n" \
  "s_nop 1\n" \
  "v_permlane32_swap_b32 v20, v41\n" \
  "v_permlane32_swap_b32 v21, v40\n" \
  "v_permlane32_swap_b32 v22, v43\n" \
  "v_permlane32_swap_b32 v23, v42\n" \
  "v_pk_add_f32 v[20:21], v[20:21], v[40:41] op_sel:[0,1] op_sel_hi:[1,0]\n" \
  "v_pk_add_f32 v[22:23], v[22:23], v[42:43] op_sel:[0,1] op_sel_hi:[1,0]\n" \
  "v_pk_fma_f32 v[20:21], v[20:21], s[84:85], s[84:85] op_sel:[0,0,1] op_sel_hi:[1,0,1]\n" \
  "v_pk_fma_f32 v[22:23], v[22:23], s[84:85], s[84:85] op_sel:[0,0,1] op_sel_hi:[1,0,1]\n" \
  "v_lshl_add_u32 v20, v20, 6, s91\n" \
  "v_lshl_add_u32 v21, v21, 2, s75\n" \
  "global_atomic_add_x2 v2, v[20:21], s[58:59] offset:3072\n" \
  "v_lshl_add_u32 v22, v22, 6, s91\n" \
  "v_lshl_add_u32 v23, v23, 2, s75\n" \
  "global_atomic_add_x2 v2, v[22:23], s[58:59] offset:1024\n" \
  "buffer_load_dword v52, v2, s[0:3], s66 offen sc1\n" \
  "v_mbcnt_lo_u32_b32 v48, -1, 0\n" \
  "v_mbcnt_hi_u32_b32 v48, -1, v48\n" \
  "s_sub_u32 s88, s73, 4\n" \
  "v_and_b32 v51, 15, v48\n" \
  "v_lshl_add_u32 v4, v51, 1, s97\n" \
  "v_bfe_u32 v49, v48, 4, 1\n" \
  "v_mad_u32_u24 v4, v49, s92, v4\n" \
  "v_lshrrev_b32 v50, 5, v48\n" \
  "v_mad_u32_u24 v4, v50, s93, v4\n" \
  "v_lshlrev_b32 v8, 4, v49\n" \
  "v_lshl_add_u32 v8, v51, 5, v8\n" \
  "v_lshl_add_u32 v8, v50, 3, v8\n" \
  "s_lshl_b32 s89, s88, 8\n" \
  "s_add_u32 s89, s89, s74\n" \
  "s_add_u32 s89, s89, 139264\n" \
  "v_and_b32 v49, 31, v48\n" \
  "v_lshl_add_u32 v5, v49, 3, s89\n" \
  "v_lshl_add_u32 v5, v50, 10, v5\n" \
  "v_lshrrev_b32 v49, 2, v51\n" \
  "v_and_b32 v50, 5, v51\n" \
  "v_and_or_b32 v49, v49, 2, v50\n" \
  "s_lshl_b32 s89, s88, 6\n" \
  "s_add_u32 s89, s89, s74\n" \
  "s_add_u32 s89, s89, 160992\n" \
  "v_and_b32 v50, 48, v48\n" \
  "v_add_u32 v6, s89, v50\n" \
  "v_mad_u32_u24 v6, v49, s96, v6\n" \
  "v_lshlrev_b32 v7, 3, v50\n" \
  "v_lshl_add_u32 v7, v49, 4, v7\n" \
  "v_and_b32 v49, 2, v51\n" \
  "v_lshl_add_u32 v7, v49, 29, v7\n" \
  "v_mov_b32 v9, 0\n" \
  "v_lshlrev_b32 v10, 4, v48\n" \
  "v_mov_b64 v[12:13], 0\n" \
  "v_mov_b64 v[14:15], 0\n" \
  "v_mov_b64 v[16:17], 0\n" \
  "v_mov_b64 v[18:19], 0\n" \
  "v_mov_b64 v[20:21], 0\n" \
  "v_mov_b64 v[22:23], 0\n" \
  "s_add_u32 s88, s74, 157760\n" \
  "v_mov_b32 v59, s88\n" \
  "s_cmp_eq_u32 s90, 1\n" \
  "s_cbranch_scc1 .LCV_NQF_%=\n" \
  "buffer_load_dword v58, v9, s[44:47], s68 offen sc1\n" \
  ".LCV_NQF_%=:\n" \
  "v_lshrrev_b32 v49, 3, v48\n" \
  "v_and_b32 v50, 7, v48\n" \
  "v_lshlrev_b32 v50, 4, v50\n" \
  "v_lshl_or_b32 v44, v49, 8, v50\n" \
  "v_and_b32 v50, 3, v48\n" \
  "v_lshl_add_u32 v40, v50, 2, s98\n" \
  "v_lshrrev_b32 v50, 4, v48\n" \
  "v_mad_u32_u24 v40, v50, s95, v40\n" \
  "v_and_b32 v49, 1, v49\n" \
  "v_lshl_add_u32 v40, v49, 6, v40\n" \
  "v_bfe_u32 v49, v48, 2, 1\n" \
  "v_lshl_add_u32 v40, v49, 7, v40\n" \
  "v_xor_b32 v41, 16, v40\n" \
  "v_xor_b32 v42, 32, v40\n" \
  "v_xor_b32 v43, 48, v40\n" \
  "s_add_i32 s80, s86, 0x32800000\n" \
  "s_add_i32 s81, s86, 0x33800000\n" \
  "s_add_i32 s88, s86, 0xb4000000\n" \
  "v_mov_b32 v46, s88\n" \
  "v_mov_b32 v47, 0\n" \
  "v_mov_b32 v11, 0\n" \
  "s_mov_b32 s89, s60\n" \
  "s_xor_b32 s60, s60, 0x80\n" \
  "s_bitcmp0_b32 s60, 7\n" \
  "s_cselect_b32 s88, 0x8000, 0\n" \
  "s_add_u32 s60, s60, s88\n" \
  "s_and_b32 s60, s60, 0xffff\n" \
  "s_add_u32 s58, s56, s60\n" \
  "s_addc_u32 s59, s57, 0\n" \
  "s_sub_u32 s71, s71, 1\n" \
  "s_mov_b32 s87, 0x40000\n" \
  "s_waitcnt vmcnt(0)\n" \
  "s_memrealtime s[38:39]\n" \
  "s_cmp_eq_u32 s90, 1\n" \
  "s_cbranch_scc1 .LCV_HAVE_%=\n" \
  ".LCV_LATE_%=:\n" \
  "v_cmp_eq_u32 vcc, 0, v58\n" \
  "s_cmp_eq_u64 vcc, 0\n" \
  "s_cbranch_scc1 .LCV_GOT_%=\n" \
  "s_sleep 12\n" \
  "buffer_load_dword v58, v9, s[44:47], s68 offen sc1\n" \
  "s_sub_u32 s87, s87, 1\n" \
  "s_cbranch_scc1 .LCV_END_%=\n" \
  "s_waitcnt vmcnt(0)\n" \
  "s_branch .LCV_LATE_%=\n" \
  ".LCV_GOT_%=:\n" \
  "s_mov_b32 m0, s99\n" \
  "s_add_u32 s88, s99, 8192\n" \
  "global_load_lds_dwordx4 v10, s[62:63] sc1\n" \
  "global_load_lds_dwordx4 v10, s[62:63] offset:1024 sc1\n" \
  "s_mov_b32 m0, s88\n" \
  "s_nop 0\n" \
  "global_load_lds_dwordx4 v10, s[64:65] sc1\n" \
  "global_load_lds_dwordx4 v10, s[64:65] offset:1024 sc1\n" \
  ".LCV_HAVE_%=:\n" \
  "s_add_u32 s62, s62, 0x80000\n" \
  "s_addc_u32 s63, s63, 0\n" \
  "s_add_u32 s64, s64, 0x40000\n" \
  "s_addc_u32 s65, s65, 0\n" \
  "s_add_u32 s68, s68, 0x1000\n" \
  "s_add_u32 s69, s69, 0x10000\n" \
  "s_add_u32 s70, s70, 0x10000\n" \
  ".LCV_CHK_%=:\n" \
  "v_cmp_eq_u32 vcc, 0, v52\n" \
  "v_readlane_b32 s88, v52, s79\n" \
  "s_cmp_eq_u64 vcc, 0\n" \
  "s_cbranch_scc1 .LCV_OKM_%=\n" \
  "s_sleep 4\n" \
  "buffer_load_dword v52, v2, s[0:3], s66 offen sc1\n" \
  "s_sub_u32 s87, s87, 1\n" \
  "s_cbranch_scc1 .LCV_END_%=\n" \
  "s_waitcnt vmcnt(0)\n" \
  "s_branch .LCV_CHK_%=\n" \
  ".LCV_OKM_%=:\n" \
  "v_subrev_u32 v52, s88, v52\n" \
  "s_nop 1\n" \
  "v_max_i32_dpp v52, v52, v52 row_ror:1 row_mask:0xf bank_mask:0xf\n" \
  "s_nop 1\n" \
  "v_max_i32_dpp v52, v52, v52 row_ror:2 row_mask:0xf bank_mask:0xf\n" \
  "s_nop 1\n" \
  "v_max_i32_dpp v52, v52, v52 row_ror:4 row_mask:0xf bank_mask:0xf\n" \
  "s_add_u32 s66, s66, 1024\n" \
  "v_readlane_b32 vcc_lo, v52, 0\n" \
  "v_readlane_b32 s88, v52, 16\n" \
  "s_max_i32 vcc_lo, vcc_lo, s88\n" \
  "v_readlane_b32 s88, v52, 32\n" \
  "s_max_i32 vcc_lo, vcc_lo, s88\n" \
  "v_readlane_b32 s88, v52, 48\n" \
  "s_max_i32 vcc_lo, vcc_lo, s88\n" \
  "s_waitcnt lgkmcnt(0)\n" \
  "s_add_u32 vcc_lo, vcc_lo, s38\n" \
  "s_add_u32 vcc_lo, vcc_lo, -14\n" \
  ".LCV_TW_%=:\n" \
  "s_memrealtime s[38:39]\n" \
  "s_waitcnt lgkmcnt(0)\n" \
  "s_sub_u32 s38, s38, vcc_lo\n" \
  "s_cmp_lt_i32 s38, 0\n" \
  "s_cbranch_scc1 .LCV_TW_%=\n" \
  ".LCV_POLL_%=:\n" \
  "buffer_load_dwordx4 v[24:27], v44, s[40:43], s89 offen sc1\n" \
  "buffer_load_dwordx4 v[28:31], v44, s[40:43], s89 offen offset:2048 sc1\n" \
  "s_sub_u32 s87, s87, 1\n" \
  "s_cbranch_scc1 .LCV_END_%=\n" \
  "s_waitcnt vmcnt(1)\n" \
  "v_bitop3_b32 v45, v24, v26, 32 bitop3:0x80\n" \
  "v_alignbit_b32 v32, v25, v24, 2\n" \
  "v_alignbit_b32 v34, v27, v26, 2\n" \
  "s_waitcnt vmcnt(0)\n" \
  "v_bitop3_b32 v45, v45, v28, v30 bitop3:0x80\n" \
  "v_cmp_eq_u32 vcc, 0, v45\n" \
  "v_cvt_f32_i32 v32, v32\n" \
  "v_cvt_f32_i32 v33, v25\n" \
  "s_cmp_eq_u64 vcc, 0\n" \
  "s_cbranch_scc0 .LCV_POLL_%=\n" \
  "v_cvt_f32_i32 v34, v34\n" \
  "v_cvt_f32_i32 v35, v27\n" \
  "v_pk_fma_f32 v[32:33], v[32:33], s[80:81], v[46:47]\n" \
  "v_alignbit_b32 v36, v29, v28, 2\n" \
  "v_pk_fma_f32 v[34:35], v[34:35], s[80:81], v[46:47]\n" \
  "v_alignbit_b32 v38, v31, v30, 2\n" \
  "v_cvt_pk_bf16_f32 v32, v32, v34\n" \
  "v_cvt_pk_bf16_f32 v33, v33, v35\n" \
  "v_cvt_f32_i32 v36, v36\n" \
  "ds_write_b32 v40, v32\n" \
  "v_cvt_f32_i32 v37, v29\n" \
  "ds_write_b32 v41, v33\n" \
  "v_cvt_f32_i32 v38, v38\n" \
  "v_cvt_f32_i32 v39, v31\n" \
  "v_pk_fma_f32 v[36:37], v[36:37], s[80:81], v[46:47]\n" \
  "v_pk_fma_f32 v[38:39], v[38:39], s[80:81], v[46:47]\n" \
  "v_dot2c_f32_bf16 v11, v32, v32\n" \
  "v_cvt_pk_bf16_f32 v36, v36, v38\n" \
  "v_cvt_pk_bf16_f32 v37, v37, v39\n" \
  "ds_write_b32 v42, v36\n" \
  "ds_write_b32 v43, v37\n" \
  "v_dot2c_f32_bf16 v11, v33, v33\n" \
  "v_dot2c_f32_bf16 v11, v36, v36\n" \
  "v_dot2c_f32_bf16 v11, v37, v37\n" \
  "s_nop 1\n" \
  "s_nop 0\n" \
  "v_cmpx_lt_f32 vcc, 2.0, v11\n" \
  "ds_max_u32 v59, v11\n" \
  "s_mov_b64 exec, -1\n" \
  "s_branch .LCV_TOP_%=\n" \
  ".LCV_LAST_%=:\n" \
  "s_waitcnt vmcnt(0)\n" \
  ".LCV_END_%=:\n" \
  "s_endpgm\n"
__device__ __forceinline__ v4u mk_srd(const void* p, const unsigned bytes) {
  const unsigned long long a = (unsigned long long)p;
  return v4u{(unsigned)a, (unsigned)(a >> 32) & 0xffffu, bytes, 0x00020000u};
}
template <bool ATT>
__device__ __forceinline__ void role_chain_t(const MegaArgs& A, char* smem, const int L, const int h, const int rep) {
  u16* swo = reinterpret_cast<u16*>(smem);
  const int w = __builtin_amdgcn_readfirstlane(threadIdx.x >> 6);
  char* ws = A.ws;
  unsigned* FL = (unsigned*)(ws + OFF_FL);
  unsigned* TMO = FL + FL_TMO;
#define LANE_IDS()                         \
  const int l = lane_id();                 \
  const int c = l & 15, g = l >> 4;        \
  (void)c; (void)g;
  float* sgm = reinterpret_cast<float*>(smem + C_X);
  float* sgf = sgm + 8;
  unsigned* FQ = FL + FL_FQ + L * 64 * 32 * 32;
  const int wb = (w + ROT_H) & 7;
  v8u w0[8], w1[8], w2[8];
  {
    LANE_IDS();
    const unsigned sb = lds_base(smem);
    const int q = (l >> 2) & 3, p = l & 3;
    const int f = (q & 1) | ((g & 1) << 1) | ((q >> 1) << 2);
    const unsigned ra = sb + (16 * (g >> 1) + 4 * (g & 1) + (q & 1) + 8 * (q >> 1)) * 256 + (w ^ f) * 32 + p * 8;
#define W_READ(W, hf, X)                                                       \
  {                                                                           \
    _Pragma("unroll") for (int t = 0; t < 4; ++t) {                           \
      const unsigned a_ = (ra ^ (X)) + (hf) * 8192 + t * 32768;               \
      const v2u r0 = trd(a_), r1 = trd(a_ + 512), r2 = trd(a_ + 16384), r3 = trd(a_ + 16384 + 512); \
      W[4 * (hf) + t] = v8u{r0.x, r0.y, r1.x, r1.y, r2.x, r2.y, r3.x, r3.y};  \
    }                                                                         \
    _Pragma("unroll") for (int t = 0; t < 4; ++t) asm volatile("" : "+v"(W[4 * (hf) + t])); \
  }
#define W_STAGE(M, hf, VM, NEXT)                                               \
  {                                                                           \
    WAIT_VM(VM);                                                              \
    RAW_BAR();                                                                \
    if ((M) == 0 && ATT) { W_READ(w0, hf, 0); W_READ(w2, hf, 128); }          \
    if ((M) == 1 && ATT) { W_READ(w1, hf, 0); }                               \
    if ((M) == 1 && !ATT) { W_READ(w0, hf, 0); }                              \
    if ((M) == 2 && !ATT) { W_READ(w1, hf, 128); W_READ(w2, hf, 0); }         \
    RAW_BAR();                                                                \
    NEXT;                                                                     \
  }
#define WO_ISSUE(hf)                                                          \
  {                                                                           \
    const char* base_ = (const char*)A.Wo + (size_t)(h * DH + 64 * (hf) + 8 * w) * (DM * 2); \
    _Pragma("unroll") for (int i = 0; i < 8; ++i) {                           \
      const int fd = (i & 1) | (((i >> 2) & 1) << 1) | ((w & 1) << 2);        \
      glds16(base_, (unsigned)(i * 1024 + ((l ^ (fd << 1)) << 4)), sb + w * 16384 + (hf) * 8192 + i * 1024); \
    }                                                                         \
  }
    wqkv_issue<true>(A.Wv, 0, h, w, l, sb);
    wqkv_issue<true>(A.Wv, 1, h, w, l, sb);
    v4u wr[16];
#define WR_LD(i, R) \
  asm volatile("global_load_dwordx4 %0, %1, %2" : "={" R "}"(wr[i]) : "v"(l * 16), "s"((const char*)A.Wo + (size_t)(h * DH + 16 * w + i) * (DM * 2)) : "memory")
    WR_LD(0, "v[64:67]");    WR_LD(1, "v[68:71]");    WR_LD(2, "v[72:75]");    WR_LD(3, "v[76:79]");
    WR_LD(4, "v[80:83]");    WR_LD(5, "v[84:87]");    WR_LD(6, "v[88:91]");    WR_LD(7, "v[92:95]");
    WR_LD(8, "v[96:99]");    WR_LD(9, "v[100:103]");    WR_LD(10, "v[104:107]");    WR_LD(11, "v[108:111]");
    WR_LD(12, "v[112:115]");    WR_LD(13, "v[116:119]");    WR_LD(14, "v[120:123]");    WR_LD(15, "v[124:127]");
#undef WR_LD
    W_STAGE(2, 0, 24, wqkv_issue<true>(A.Wk, 0, h, w, l, sb));
    SST(4);
    W_STAGE(2, 1, 24, wqkv_issue<true>(A.Wk, 1, h, w, l, sb));
    SST(5);
#define DOT2(x, acc) dot2sq(x, acc)
    float* cs = reinterpret_cast<float*>(smem + C_SM);
    {
      float f2 = 0.f;
      if (!ATT) {
#pragma unroll
        for (int t = 0; t < 8; ++t)
#pragma unroll
          for (int e = 0; e < 8; ++e) f2 = DOT2(w2[t][e], DOT2(w1[t][e], f2));
      }
      f2 = wave_sum_dpp(f2);
      if (l == 0) { sgf[w] = f2; sgm[16] = 0.f; }
      WAIT_VM(16);
      f32x4 ca = {}, cb = {};
#pragma unroll
      for (int i = 0; i < 16; ++i) {
        asm volatile("" : "+v"(wr[i]), "+v"(ca), "+v"(cb));
        const f32x4 x = {lo16(wr[i].x), hi16(wr[i].x), lo16(wr[i].y), hi16(wr[i].y)};
        const f32x4 y = {lo16(wr[i].z), hi16(wr[i].z), lo16(wr[i].w), hi16(wr[i].w)};
        ca += x * x;
        cb += y * y;
      }
      *reinterpret_cast<f32x4*>(cs + w * 512 + l * 8) = ca;
      *reinterpret_cast<f32x4*>(cs + w * 512 + l * 8 + 4) = cb;
    }
    W_STAGE(1, 0, 8, wqkv_issue<true>(A.Wq, 0, h, w, l, sb));
    {
      float m2 = 0.f;
#pragma unroll
      for (int i = 0; i < 8; ++i) m2 += cs[i * 512 + w * 64 + l];
      m2 = wave_max_dpp(m2);
      if (l == 0) sgm[w] = m2;
    }
    W_STAGE(1, 1, 8, wqkv_issue<true>(A.Wq, 1, h, w, l, sb));
    if (w == 0) {
      float m2 = 0.f, f2 = 0.f;
#pragma unroll
      for (int i = 0; i < 8; ++i) { m2 = fmaxf(m2, sgm[i]); f2 += sgf[i]; }
      __builtin_amdgcn_raw_buffer_store_b32(__float_as_uint(sqrtf(m2 * f2) * 1.001f) | 1u, MKRS(FL + FL_G + L * 8 * 32, 8 * 128), l * 128 + h * 4, 0, AUX_SC1);
    }
    SST(6);
    W_STAGE(0, 0, 8, WO_ISSUE(0));
    W_STAGE(0, 1, 8, WO_ISSUE(1));
    SST(7);
#undef W_STAGE
#undef W_READ
#undef WO_ISSUE
#undef DOT2
    const unsigned rb = sb + (2 * (g >> 1) + (q >> 1)) * 16384 + (4 * (g & 1) + (q & 1)) * 1024 + p * 8;
#pragma unroll
    for (int hf = 0; hf < 2; ++hf) {
      if (hf == 0) WAIT_VM(8); else WAIT_VM(0);
      RAW_BAR();
      v4u fr[8];
#pragma unroll
      for (int i = 0; i < 8; ++i) {
        const unsigned a_ = rb + hf * 8192 + (i & 1) * 65536 + ((4 * wb + (i >> 1)) ^ f) * 32;
        const v2u r0 = trd(a_), r1 = trd(a_ + 2048);
        fr[i] = v4u{r0.x, r0.y, r1.x, r1.y};
      }
#pragma unroll
      for (int i = 0; i < 8; ++i) asm volatile("" : "+v"(fr[i]));
      RAW_BAR();
#pragma unroll
      for (int i = 0; i < 8; ++i) *reinterpret_cast<v4u*>(swo + w * 8192 + hf * 4096 + i * 512 + l * 8) = fr[i];
    }
  }
  WSTAMP2();
  v4u m0r;
  {
    LANE_IDS();
    m0r = *reinterpret_cast<const v4u*>(A.mem0 + (c & 7) * DM + 64 * w + 16 * g + 8 * (c >> 3));
  }
  const unsigned sb = lds_base(smem);
  const char* gK = ws + OFF_QK + L * SZ_QK + ((size_t)h * 16 + 2 * w) * 1024;
  const char* gV = ws + OFF_VF + L * SZ_O + ((size_t)h * 8 + 2 * w - 8) * 1024;
  float gw2;
  {
    const unsigned* gp = FL + FL_G + (L * 8 + (blockIdx.x & 7)) * 32;
    unsigned gv;
    for (unsigned spins = 0;; ++spins) {
      gv = __hip_atomic_load((gu32*)gp + (lane_id() & 31), RLX_AGENT);
      if (__all(gv != 0u)) break;
      if (spin_timeout(spins, TMO)) return;
      __builtin_amdgcn_s_sleep(4);
    }
    const float gm = wave_max_dpp(__uint_as_float(gv));
    asm volatile("s_nop 0\n\tv_readfirstlane_b32 %0, %1" : "=s"(gw2) : "v"(gm * gm * 1.06f));
  }
  v8u pv;
  {
    LANE_IDS();
    *reinterpret_cast<v4u*>(smem + C_SM + w * 1024 + l * 16) = m0r;
    const unsigned voff = g * 256 + c * 8, wp = sb + w * 16384 + l * 16;
    pv[0] = (c & 8) ? 0xEEEE : 0x4444;
    pv[1] = sb + C_SM + l * 16;
    pv[2] = ATT ? sb + C_SQ + ((c & 7) * QS + 8 * g) * 2 : voff;
    pv[3] = ATT ? sb + C_SK + ((4 * ((c >> 2) & 1) + 2 * (c >> 3) + (c & 1)) * QS + 8 * g) * 2 : wp;
    pv[4] = sb + (c < 8 ? C_SO : C_SQ) + w * 256 + ((c & 7) + 8 * (g >> 1)) * 16 + 8 * (g & 1);
    pv[5] = voff;
    pv[6] = wp;
    pv[7] = sb + C_SO + (l >> 5) * 1024 + w * 256 + (l & 31) * 8;
  }
  if (!ATT) {
    if (!wait_n<4, 1>(FQ + h * 32, 1, TMO)) return;
    const unsigned lo = lane_id() * 16, d = sb + C_KT + (2 * w - 8) * 1024;
    glds16c(gK, lo, d);
    glds16c(gK + 1024, lo, d + 1024);
    glds16c(gV, lo, d + 8192);
    glds16c(gV + 1024, lo, d + 9216);
  }
  DRAIN();
  SST(8);
  const v4u rsA = mk_srd(ws + OFF_ACC + L * SZ_ACC, SZ_ACC);
  const v4u rsF = mk_srd(FQ, 64 * 32 * 32 * 4);
  const v4u rsMK = mk_srd(ws + OFF_MEMK + L * SZ_MEM, SZ_MEM);
  const v4u rsMV = mk_srd(ws + OFF_MEMV + L * SZ_MEM, SZ_MEM);
  const char* pAB = ws + OFF_ACC + L * SZ_ACC + (wb - w) * 4096;
  const char* pGK = gK + NH * 16 * 1024;
  const char* pGV = gV + NH * 8 * 1024;
  const v4u rsT = mk_srd(FL + FL_FK + L * 32, 64 * 1024 - 128);
  unsigned sc[8];
  sc[0] = (32 + h) * 128;
  sc[1] = (h * 4 + w - 4) * 512;
  sc[2] = (h * 4 + w - 4) * 512;
  sc[3] = NSEG - 1;
  sc[4] = __float_as_uint(gw2);
  sc[5] = w;
  sc[6] = sb;
  sc[7] = h == 0 ? 0xd4000000u - 2u - (1u << 29) : 0xd4000000u;
  const char* dbg = ws;
#define CHAIN_OPS                                                                                                          \
  ::"{v[64:71]}"(w0[0]), "{v[72:79]}"(w0[1]), "{v[80:87]}"(w0[2]), "{v[88:95]}"(w0[3]), "{v[96:103]}"(w0[4]),              \
      "{v[104:111]}"(w0[5]), "{v[112:119]}"(w0[6]), "{v[120:127]}"(w0[7]), "{v[128:135]}"(w1[0]), "{v[136:143]}"(w1[1]),    \
      "{v[144:151]}"(w1[2]), "{v[152:159]}"(w1[3]), "{v[160:167]}"(w1[4]), "{v[168:175]}"(w1[5]), "{v[176:183]}"(w1[6]),    \
      "{v[184:191]}"(w1[7]), "{v[192:199]}"(w2[0]), "{v[200:207]}"(w2[1]), "{v[208:215]}"(w2[2]), "{v[216:223]}"(w2[3]),    \
      "{v[224:231]}"(w2[4]), "{v[232:239]}"(w2[5]), "{v[240:247]}"(w2[6]), "{v[248:255]}"(w2[7]), "{v[0:7]}"(pv),          \
      "{s[40:43]}"(rsA), "{s[44:47]}"(rsF), "{s[48:51]}"(rsMK), "{s[52:55]}"(rsMV), "{s[56:57]}"(pAB), "{s60}"(w * 4096), "{s61}"(h * 1024), \
      "{s[62:63]}"(pGK), "{s[64:65]}"(pGV), "{s66}"(0), "{s67}"(w == 4 ? (h >> 3) * 256 + (h & 7) * 8 : 0x40000000), "{s68}"(sc[0]), "{s69}"(sc[1]), "{s70}"(sc[2]),    \
      "{s71}"(sc[3]), "{s72}"(sc[4]), "{s73}"(sc[5]), "{s74}"(sc[6]), "{s75}"(sc[7]), "{s[36:37]}"(dbg),                   \
      "{s[0:3]}"(rsT), "{s79}"((h >> 3) * 16 + (h & 7))                                                                        \
      : "memory", "vcc", "scc", "s99", "v63"
  if (ATT) asm volatile("s_mov_b64 exec, -1\ns_nop 4\n" CHAIN_ASM_ATT CHAIN_OPS);
  else asm volatile("s_mov_b64 exec, -1\ns_nop 4\n" CHAIN_ASM_VO CHAIN_OPS);
#undef CHAIN_OPS
#undef LANE_IDS
}

__device__ __forceinline__ void role_chain(const MegaArgs& A, char* smem, const int L, const int h, const int rep) {
  if (__builtin_amdgcn_readfirstlane(threadIdx.x >> 6) < 4) role_chain_t<true>(A, smem, L, h, rep);
  else role_chain_t<false>(A, smem, L, h, rep);
}

constexpr int AW_SO = 0, AW_TK = 32768, AW_CNT = AW_TK + 98304;
__device__ __forceinline__ unsigned umin3(unsigned a, unsigned b, unsigned c) {
  return __builtin_elementwise_min(__builtin_elementwise_min(a, b), c);
}
#define AW_ASM_X \
  "s_mov_b32 s61, 0\n" \
  "s_mov_b32 s67, 1\n" \
  "s_mov_b32 s68, 0\n" \
  "s_mov_b32 s70, 0\n" \
  "s_mov_b32 s73, 0\n" \
  "s_mov_b32 s71, 0x3e0293ee\n" \
  "s_mov_b32 s72, 0xbe0293ee\n" \
  "s_mov_b32 s65, 49152\n" \
  "s_mov_b32 s66, 16384\n" \
  "v_mov_b32 v38, s58\n" \
  "s_memrealtime s[38:39]\n" \
  "s_waitcnt lgkmcnt(0)\n" \
  "s_mov_b32 s69, s38\n" \
  "s_setprio 3\n" \
  ".LCX_TOP_%=:\n" \
  "s_mov_b32 s62, 0x200000\n" \
  "s_cmp_ge_u32 s73, s67\n" \
  "s_cbranch_scc1 .LCX_TOK_%=\n" \
  "s_cmp_eq_u32 s61, 0\n" \
  "s_cbranch_scc1 .LCX_SPIN_%=\n" \
  "s_waitcnt vmcnt(0)\n" \
  "v_mov_b32 v46, s59\n" \
  "v_mov_b32 v47, 1\n" \
  "s_mov_b64 exec, 1\n" \
  "ds_add_rtn_u32 v46, v46, v47\n" \
  "s_mov_b64 exec, -1\n" \
  "s_xor_b32 s59, s59, 4\n" \
  "s_mov_b32 s61, 0\n" \
  "s_waitcnt lgkmcnt(0)\n" \
  "v_readfirstlane_b32 s63, v46\n" \
  "v_lshlrev_b32 v46, 5, v5\n" \
  "s_and_b32 s63, s63, 7\n" \
  "s_cmp_eq_u32 s63, 7\n" \
  "s_cbranch_scc0 .LCX_W1_%=\n" \
  "s_mov_b64 exec, 0xff\n" \
  "global_store_dword v46, v47, s[52:53] sc1\n" \
  "s_mov_b64 exec, -1\n" \
  ".LCX_W1_%=:\n" \
  "s_add_u32 s52, s52, 4096\n" \
  "s_addc_u32 s53, s53, 0\n" \
  ".LCX_SPIN_%=:\n" \
  "s_sleep 2\n" \
  "s_memrealtime s[38:39]\n" \
  "ds_read_b32 v37, v38\n" \
  "s_sub_u32 s62, s62, 1\n" \
  "s_cbranch_scc1 .LCX_END_%=\n" \
  "s_waitcnt lgkmcnt(0)\n" \
  "v_readfirstlane_b32 s73, v37\n" \
  "s_add_u32 s38, s38, 80\n" \
  "s_bfe_u32 s64, s73, 0x80008\n" \
  "s_and_b32 s73, s73, 0xff\n" \
  "s_min_u32 s73, s73, s64\n" \
  "s_cmp_ge_u32 s73, s67\n" \
  "s_cbranch_scc0 .LCX_SPIN_%=\n" \
  "s_cmp_eq_u32 s70, 0\n" \
  "s_cbranch_scc1 .LCX_TOK_%=\n" \
  "s_sub_u32 s63, s38, s69\n" \
  "s_max_i32 s63, s63, 0\n" \
  "s_add_u32 s69, s69, s63\n" \
  ".LCX_TOK_%=:\n" \
  "ds_read_b128 v[48:51], v6 offset:0\n" \
  "ds_read_b128 v[64:67], v0 offset:8192\n" \
  "ds_read_b128 v[80:83], v0 offset:12288\n" \
  "ds_read_b128 v[52:55], v6 offset:1024\n" \
  "ds_read_b128 v[68:71], v0 offset:9216\n" \
  "ds_read_b128 v[84:87], v0 offset:13312\n" \
  "ds_read_b128 v[56:59], v6 offset:2048\n" \
  "ds_read_b128 v[72:75], v0 offset:10240\n" \
  "ds_read_b128 v[88:91], v0 offset:14336\n" \
  "ds_read_b128 v[60:63], v6 offset:3072\n" \
  "ds_read_b128 v[76:79], v0 offset:11264\n" \
  "ds_read_b128 v[92:95], v0 offset:15360\n" \
  "s_waitcnt lgkmcnt(9)\n" \
  "v_mfma_f32_16x16x32_bf16 v[20:23], v[64:67], v[48:51], v[8:11]\n" \
  "v_mfma_f32_16x16x32_bf16 v[24:27], v[80:83], v[48:51], v[12:15]\n" \
  "ds_read_b128 v[96:99], v0 offset:16384\n" \
  "ds_read_b128 v[100:103], v0 offset:17408\n" \
  "s_waitcnt lgkmcnt(8)\n" \
  "v_mfma_f32_16x16x32_bf16 v[20:23], v[68:71], v[52:55], v[20:23]\n" \
  "v_mfma_f32_16x16x32_bf16 v[24:27], v[84:87], v[52:55], v[24:27]\n" \
  "ds_read_b128 v[104:107], v0 offset:18432\n" \
  "ds_read_b128 v[108:111], v0 offset:19456\n" \
  "s_waitcnt lgkmcnt(7)\n" \
  "v_mfma_f32_16x16x32_bf16 v[20:23], v[72:75], v[56:59], v[20:23]\n" \
  "v_mfma_f32_16x16x32_bf16 v[24:27], v[88:91], v[56:59], v[24:27]\n" \
  "ds_read_b128 v[112:115], v0 offset:20480\n" \
  "ds_read_b128 v[116:119], v0 offset:21504\n" \
  "s_waitcnt lgkmcnt(6)\n" \
  "v_mfma_f32_16x16x32_bf16 v[20:23], v[76:79], v[60:63], v[20:23]\n" \
  "v_mfma_f32_16x16x32_bf16 v[24:27], v[92:95], v[60:63], v[24:27]\n" \
  "ds_read_b128 v[120:123], v0 offset:22528\n" \
  "ds_read_b128 v[124:127], v0 offset:23552\n" \
  "s_add_u32 s67, s67, 1\n" \
  "v_add_u32 v0, s65, v0\n" \
  "v_add_u32 v6, s65, v6\n" \
  "s_sub_u32 s65, 0, s65\n" \
  "s_nop 1\n" \
  "v_max3_f32 v32, v20, v21, v22\n" \
  "v_max3_f32 v33, v23, v24, v25\n" \
  "v_max3_f32 v32, v32, v26, v27\n" \
  "v_max_f32 v32, v32, v33\n" \
  "v_mov_b32 v33, v32\n" \
  "s_nop 1\n" \
  "v_permlane16_swap_b32 v32, v33\n" \
  "v_max_f32 v32, v32, v33\n" \
  "v_mov_b32 v33, v32\n" \
  "s_nop 1\n" \
  "v_permlane32_swap_b32 v32, v33\n" \
  "v_max_f32 v32, v32, v33\n" \
  "v_mul_f32 v33, s72, v32\n" \
  "v_fma_f32 v20, v20, s71, v33\n" \
  "v_fma_f32 v21, v21, s71, v33\n" \
  "v_fma_f32 v22, v22, s71, v33\n" \
  "v_fma_f32 v23, v23, s71, v33\n" \
  "v_fma_f32 v24, v24, s71, v33\n" \
  "v_fma_f32 v25, v25, s71, v33\n" \
  "v_fma_f32 v26, v26, s71, v33\n" \
  "v_fma_f32 v27, v27, s71, v33\n" \
  "v_exp_f32 v20, v20\n" \
  "v_exp_f32 v21, v21\n" \
  "v_exp_f32 v22, v22\n" \
  "v_exp_f32 v23, v23\n" \
  "v_exp_f32 v24, v24\n" \
  "v_exp_f32 v25, v25\n" \
  "v_exp_f32 v26, v26\n" \
  "v_exp_f32 v27, v27\n" \
  "v_add_f32 v34, v20, v21\n" \
  "v_add_f32 v33, v22, v23\n" \
  "v_add_f32 v36, v24, v25\n" \
  "v_add_f32 v34, v34, v33\n" \
  "v_add_f32 v33, v26, v27\n" \
  "v_add_f32 v34, v34, v36\n" \
  "v_add_f32 v34, v34, v33\n" \
  "s_waitcnt vmcnt(0) lgkmcnt(0)\n" \
  "s_cmp_eq_u32 s61, 0\n" \
  "s_cbranch_scc1 .LCX_NP_%=\n" \
  "v_mov_b32 v46, s59\n" \
  "v_mov_b32 v47, 1\n" \
  "s_mov_b64 exec, 1\n" \
  "ds_add_rtn_u32 v46, v46, v47\n" \
  "s_mov_b64 exec, -1\n" \
  "s_xor_b32 s59, s59, 4\n" \
  "s_mov_b32 s61, 0\n" \
  "s_waitcnt lgkmcnt(0)\n" \
  "v_readfirstlane_b32 s63, v46\n" \
  "v_lshlrev_b32 v46, 5, v5\n" \
  "s_and_b32 s63, s63, 7\n" \
  "s_cmp_eq_u32 s63, 7\n" \
  "s_cbranch_scc0 .LCX_W2_%=\n" \
  "s_mov_b64 exec, 0xff\n" \
  "global_store_dword v46, v47, s[52:53] sc1\n" \
  "s_mov_b64 exec, -1\n" \
  ".LCX_W2_%=:\n" \
  "s_add_u32 s52, s52, 4096\n" \
  "s_addc_u32 s53, s53, 0\n" \
  ".LCX_NP_%=:\n" \
  "s_cmp_lg_u32 s68, 0\n" \
  "s_cbranch_scc1 .LCX_TEST_%=\n" \
  ".LCX_WF_%=:\n" \
  "s_memrealtime s[38:39]\n" \
  "s_waitcnt lgkmcnt(0)\n" \
  "s_sub_u32 s63, s69, s38\n" \
  "s_cmp_le_i32 s63, 0\n" \
  "s_cbranch_scc1 .LCX_GO_%=\n" \
  "s_cmp_lt_i32 s63, 12\n" \
  "s_cbranch_scc1 .LCX_WF_%=\n" \
  "s_sleep 3\n" \
  "s_branch .LCX_WF_%=\n" \
  ".LCX_GO_%=:\n" \
  "v_mov_b32 v35, 0\n" \
  "global_load_dwordx4 v[128:131], v2, s[48:49] offset:0 sc1\n" \
  "global_load_dwordx4 v[132:135], v2, s[48:49] offset:512 sc1\n" \
  "global_load_dwordx4 v[136:139], v2, s[48:49] offset:1024 sc1\n" \
  "global_load_dwordx4 v[140:143], v2, s[48:49] offset:1536 sc1\n" \
  "global_load_dwordx4 v[144:147], v3, s[48:49] offset:0 sc1\n" \
  "global_load_dwordx4 v[148:151], v3, s[48:49] offset:512 sc1\n" \
  "global_load_dwordx4 v[152:155], v3, s[48:49] offset:1024 sc1\n" \
  "global_load_dwordx4 v[156:159], v3, s[48:49] offset:1536 sc1\n" \
  "global_load_dword v35, v35, s[50:51] sc1\n" \
  "s_sub_u32 s62, s62, 8\n" \
  "s_cbranch_scc1 .LCX_END_%=\n" \
  "s_waitcnt vmcnt(0)\n" \
  ".LCX_TEST_%=:\n" \
  "v_mfma_f32_16x16x32_bf16 v[28:31], v[128:131], v[48:51], v[16:19]\n" \
  "v_min3_u32 v33, v128, v129, v130\n" \
  "v_min3_u32 v36, v131, v132, v133\n" \
  "v_min3_u32 v33, v33, v134, v135\n" \
  "v_min3_u32 v36, v36, v136, v137\n" \
  "v_mfma_f32_16x16x32_bf16 v[28:31], v[132:135], v[52:55], v[28:31]\n" \
  "v_min3_u32 v33, v33, v138, v139\n" \
  "v_min3_u32 v36, v36, v140, v141\n" \
  "v_min3_u32 v33, v33, v142, v143\n" \
  "v_min3_u32 v36, v36, v144, v145\n" \
  "v_mfma_f32_16x16x32_bf16 v[28:31], v[136:139], v[56:59], v[28:31]\n" \
  "v_min3_u32 v33, v33, v146, v147\n" \
  "v_min3_u32 v36, v36, v148, v149\n" \
  "v_min3_u32 v33, v33, v150, v151\n" \
  "v_min3_u32 v36, v36, v152, v153\n" \
  "v_mfma_f32_16x16x32_bf16 v[28:31], v[140:143], v[60:63], v[28:31]\n" \
  "v_min3_u32 v33, v33, v154, v155\n" \
  "v_min3_u32 v36, v36, v156, v157\n" \
  "v_min3_u32 v33, v33, v158, v159\n" \
  "v_min_u32 v33, v33, v36\n" \
  "v_cmp_eq_u32 vcc, 0, v33\n" \
  "v_readfirstlane_b32 s64, v35\n" \
  "s_mov_b32 s68, 0\n" \
  "s_cmp_eq_u64 vcc, 0\n" \
  "s_cbranch_scc0 .LCX_FAIL_%=\n" \
  "v_max3_f32 v36, v28, v29, v30\n" \
  "v_max3_f32 v36, v36, v31, v32\n" \
  "v_mov_b32 v33, v36\n" \
  "s_cmp_eq_u32 s64, 0\n" \
  "s_cselect_b32 s64, s38, s64\n" \
  "v_permlane16_swap_b32 v36, v33\n" \
  "v_max_f32 v36, v36, v33\n" \
  "v_mov_b32 v33, v36\n" \
  "s_sub_u32 s63, s64, s70\n" \
  "s_mov_b32 s70, s64\n" \
  "v_permlane32_swap_b32 v36, v33\n" \
  "v_max_f32 v36, v36, v33\n" \
  "v_mul_f32 v36, s72, v36\n" \
  "v_fma_f32 v32, v32, s71, v36\n" \
  "v_fma_f32 v28, v28, s71, v36\n" \
  "v_fma_f32 v29, v29, s71, v36\n" \
  "v_fma_f32 v30, v30, s71, v36\n" \
  "v_fma_f32 v31, v31, s71, v36\n" \
  "v_exp_f32 v32, v32\n" \
  "v_exp_f32 v28, v28\n" \
  "v_exp_f32 v29, v29\n" \
  "v_exp_f32 v30, v30\n" \
  "v_exp_f32 v31, v31\n" \
  "v_fma_f32 v34, v34, v32, v28\n" \
  "v_add_f32 v33, v29, v30\n" \
  "v_add_f32 v34, v34, v31\n" \
  "v_add_f32 v34, v34, v33\n" \
  "v_mov_b32 v33, v34\n" \
  "s_min_u32 s63, s63, 250\n" \
  "s_add_u32 s69, s64, s63\n" \
  "v_permlane16_swap_b32 v34, v33\n" \
  "v_add_f32 v34, v34, v33\n" \
  "v_mov_b32 v33, v34\n" \
  "s_add_u32 s69, s69, 68\n" \
  "s_add_u32 s48, s48, 0x10000\n" \
  "s_addc_u32 s49, s49, 0\n" \
  "v_permlane32_swap_b32 v34, v33\n" \
  "v_add_f32 v34, v34, v33\n" \
  "v_rcp_f32 v34, v34\n" \
  "s_add_u32 s50, s50, 1024\n" \
  "s_addc_u32 s51, s51, 0\n" \
  "v_mul_f32 v32, v32, v34\n" \
  "v_mul_f32 v20, v20, v32\n" \
  "v_mul_f32 v21, v21, v32\n" \
  "v_mul_f32 v22, v22, v32\n" \
  "v_mul_f32 v23, v23, v32\n" \
  "v_mul_f32 v24, v24, v32\n" \
  "v_mul_f32 v25, v25, v32\n" \
  "v_mul_f32 v26, v26, v32\n" \
  "v_mul_f32 v27, v27, v32\n" \
  "v_cvt_pk_bf16_f32 v40, v20, v21\n" \
  "v_cvt_pk_bf16_f32 v41, v22, v23\n" \
  "v_cvt_pk_bf16_f32 v42, v24, v25\n" \
  "v_cvt_pk_bf16_f32 v43, v26, v27\n" \
  "v_mul_f32 v28, v28, v34\n" \
  "v_mul_f32 v29, v29, v34\n" \
  "v_mfma_f32_16x16x32_bf16 v[64:67], v[96:99], v[40:43], 0\n" \
  "v_mul_f32 v30, v30, v34\n" \
  "v_mfma_f32_16x16x32_bf16 v[68:71], v[100:103], v[40:43], 0\n" \
  "v_mul_f32 v31, v31, v34\n" \
  "v_mfma_f32_16x16x32_bf16 v[72:75], v[104:107], v[40:43], 0\n" \
  "v_cvt_pk_bf16_f32 v44, v28, v29\n" \
  "v_mfma_f32_16x16x32_bf16 v[76:79], v[108:111], v[40:43], 0\n" \
  "v_cvt_pk_bf16_f32 v45, v30, v31\n" \
  "v_mfma_f32_16x16x32_bf16 v[80:83], v[112:115], v[40:43], 0\n" \
  "v_mfma_f32_16x16x32_bf16 v[84:87], v[116:119], v[40:43], 0\n" \
  "v_mfma_f32_16x16x32_bf16 v[88:91], v[120:123], v[40:43], 0\n" \
  "v_mfma_f32_16x16x32_bf16 v[92:95], v[124:127], v[40:43], 0\n" \
  "s_memrealtime s[38:39]\n" \
  "ds_read_b32 v37, v38\n" \
  "v_mfma_f32_16x16x16_bf16 v[64:67], v[144:145], v[44:45], v[64:67]\n" \
  "v_mfma_f32_16x16x16_bf16 v[68:71], v[148:149], v[44:45], v[68:71]\n" \
  "v_mfma_f32_16x16x16_bf16 v[72:75], v[152:153], v[44:45], v[72:75]\n" \
  "v_mfma_f32_16x16x16_bf16 v[76:79], v[156:157], v[44:45], v[76:79]\n" \
  "v_mfma_f32_16x16x16_bf16 v[80:83], v[146:147], v[44:45], v[80:83]\n" \
  "v_mfma_f32_16x16x16_bf16 v[84:87], v[150:151], v[44:45], v[84:87]\n" \
  "v_mfma_f32_16x16x16_bf16 v[88:91], v[154:155], v[44:45], v[88:91]\n" \
  "v_mfma_f32_16x16x16_bf16 v[92:95], v[158:159], v[44:45], v[92:95]\n" \
  "s_nop 0\n" \
  "v_cvt_pk_bf16_f32 v160, v64, v65\n" \
  "v_cvt_pk_bf16_f32 v161, v66, v67\n" \
  "v_cvt_pk_bf16_f32 v162, v68, v69\n" \
  "v_cvt_pk_bf16_f32 v163, v70, v71\n" \
  "ds_write_b128 v7, v[160:163] offset:0\n" \
  "v_cvt_pk_bf16_f32 v164, v72, v73\n" \
  "v_cvt_pk_bf16_f32 v165, v74, v75\n" \
  "v_cvt_pk_bf16_f32 v166, v76, v77\n" \
  "v_cvt_pk_bf16_f32 v167, v78, v79\n" \
  "ds_write_b128 v7, v[164:167] offset:1024\n" \
  "v_cvt_pk_bf16_f32 v168, v80, v81\n" \
  "v_cvt_pk_bf16_f32 v169, v82, v83\n" \
  "v_cvt_pk_bf16_f32 v170, v84, v85\n" \
  "v_cvt_pk_bf16_f32 v171, v86, v87\n" \
  "ds_write_b128 v7, v[168:171] offset:2048\n" \
  "v_cvt_pk_bf16_f32 v172, v88, v89\n" \
  "v_cvt_pk_bf16_f32 v173, v90, v91\n" \
  "v_cvt_pk_bf16_f32 v174, v92, v93\n" \
  "v_cvt_pk_bf16_f32 v175, v94, v95\n" \
  "ds_write_b128 v7, v[172:175] offset:3072\n" \
  "v_add_u32 v7, s66, v7\n" \
  "s_waitcnt lgkmcnt(0)\n" \
  "s_barrier\n" \
  "ds_read_b128 v[48:51], v1 offset:0\n" \
  "ds_read_b128 v[52:55], v1 offset:4096\n" \
  "ds_read_b128 v[56:59], v1 offset:1024\n" \
  "ds_read_b128 v[60:63], v1 offset:5120\n" \
  "ds_read_b128 v[64:67], v1 offset:2048\n" \
  "ds_read_b128 v[68:71], v1 offset:6144\n" \
  "ds_read_b128 v[72:75], v1 offset:3072\n" \
  "ds_read_b128 v[76:79], v1 offset:7168\n" \
  "ds_read_b128 v[80:83], v1 offset:8192\n" \
  "ds_read_b128 v[84:87], v1 offset:12288\n" \
  "ds_read_b128 v[88:91], v1 offset:9216\n" \
  "ds_read_b128 v[92:95], v1 offset:13312\n" \
  "ds_read_b128 v[96:99], v1 offset:10240\n" \
  "ds_read_b128 v[100:103], v1 offset:14336\n" \
  "v_readfirstlane_b32 s73, v37\n" \
  "s_sub_u32 s63, s38, s69\n" \
  "s_bfe_u32 s64, s73, 0x80010\n" \
  "s_sub_u32 s64, s64, s67\n" \
  "s_or_b32 s63, s63, s64\n" \
  "s_bfe_u32 s64, s73, 0x80008\n" \
  "s_and_b32 s73, s73, 0xff\n" \
  "s_min_u32 s73, s73, s64\n" \
  "s_or_b32 s63, s63, s68\n" \
  "s_cmp_lt_i32 s63, 0\n" \
  "s_cbranch_scc1 .LCX_E3_%=\n" \
  "v_mov_b32 v35, 0\n" \
  "global_load_dwordx4 v[128:131], v2, s[48:49] offset:0 sc1\n" \
  "global_load_dwordx4 v[132:135], v2, s[48:49] offset:512 sc1\n" \
  "global_load_dwordx4 v[136:139], v2, s[48:49] offset:1024 sc1\n" \
  "global_load_dwordx4 v[140:143], v2, s[48:49] offset:1536 sc1\n" \
  "global_load_dwordx4 v[144:147], v3, s[48:49] offset:0 sc1\n" \
  "global_load_dwordx4 v[148:151], v3, s[48:49] offset:512 sc1\n" \
  "global_load_dwordx4 v[152:155], v3, s[48:49] offset:1024 sc1\n" \
  "global_load_dwordx4 v[156:159], v3, s[48:49] offset:1536 sc1\n" \
  "global_load_dword v35, v35, s[50:51] sc1\n" \
  "s_mov_b32 s68, -1\n" \
  ".LCX_E3_%=:\n" \
  "s_waitcnt lgkmcnt(12)\n" \
  "v_mfma_f32_16x16x32_bf16 v[112:115], v[192:195], v[48:51], 0\n" \
  "v_mfma_f32_16x16x32_bf16 v[116:119], v[224:227], v[48:51], 0\n" \
  "v_mfma_f32_16x16x32_bf16 v[120:123], v[192:195], v[52:55], 0\n" \
  "v_mfma_f32_16x16x32_bf16 v[124:127], v[224:227], v[52:55], 0\n" \
  "ds_read_b128 v[104:107], v1 offset:11264\n" \
  "ds_read_b128 v[108:111], v1 offset:15360\n" \
  "s_waitcnt lgkmcnt(12)\n" \
  "v_mfma_f32_16x16x32_bf16 v[112:115], v[196:199], v[56:59], v[112:115]\n" \
  "v_mfma_f32_16x16x32_bf16 v[116:119], v[228:231], v[56:59], v[116:119]\n" \
  "v_mfma_f32_16x16x32_bf16 v[120:123], v[196:199], v[60:63], v[120:123]\n" \
  "v_mfma_f32_16x16x32_bf16 v[124:127], v[228:231], v[60:63], v[124:127]\n" \
  "s_waitcnt lgkmcnt(10)\n" \
  "v_mfma_f32_16x16x32_bf16 v[112:115], v[200:203], v[64:67], v[112:115]\n" \
  "v_mfma_f32_16x16x32_bf16 v[116:119], v[232:235], v[64:67], v[116:119]\n" \
  "v_mfma_f32_16x16x32_bf16 v[120:123], v[200:203], v[68:71], v[120:123]\n" \
  "v_mfma_f32_16x16x32_bf16 v[124:127], v[232:235], v[68:71], v[124:127]\n" \
  "s_waitcnt lgkmcnt(8)\n" \
  "v_mfma_f32_16x16x32_bf16 v[112:115], v[204:207], v[72:75], v[112:115]\n" \
  "v_mfma_f32_16x16x32_bf16 v[116:119], v[236:239], v[72:75], v[116:119]\n" \
  "v_mfma_f32_16x16x32_bf16 v[120:123], v[204:207], v[76:79], v[120:123]\n" \
  "v_mfma_f32_16x16x32_bf16 v[124:127], v[236:239], v[76:79], v[124:127]\n" \
  "s_waitcnt lgkmcnt(6)\n" \
  "v_mfma_f32_16x16x32_bf16 v[112:115], v[208:211], v[80:83], v[112:115]\n" \
  "v_mfma_f32_16x16x32_bf16 v[116:119], v[240:243], v[80:83], v[116:119]\n" \
  "v_mfma_f32_16x16x32_bf16 v[120:123], v[208:211], v[84:87], v[120:123]\n" \
  "v_mfma_f32_16x16x32_bf16 v[124:127], v[240:243], v[84:87], v[124:127]\n" \
  "s_waitcnt lgkmcnt(4)\n" \
  "v_mfma_f32_16x16x32_bf16 v[112:115], v[212:215], v[88:91], v[112:115]\n" \
  "v_mfma_f32_16x16x32_bf16 v[116:119], v[244:247], v[88:91], v[116:119]\n" \
  "v_mfma_f32_16x16x32_bf16 v[120:123], v[212:215], v[92:95], v[120:123]\n" \
  "v_mfma_f32_16x16x32_bf16 v[124:127], v[244:247], v[92:95], v[124:127]\n" \
  "s_memrealtime s[38:39]\n" \
  "ds_read_b32 v37, v38\n" \
  "s_waitcnt lgkmcnt(3)\n" \
  "v_mfma_f32_16x16x32_bf16 v[112:115], v[216:219], v[96:99], v[112:115]\n" \
  "v_mfma_f32_16x16x32_bf16 v[116:119], v[248:251], v[96:99], v[116:119]\n" \
  "v_mfma_f32_16x16x32_bf16 v[120:123], v[216:219], v[100:103], v[120:123]\n" \
  "v_mfma_f32_16x16x32_bf16 v[124:127], v[248:251], v[100:103], v[124:127]\n" \
  "s_waitcnt lgkmcnt(0)\n" \
  "v_mfma_f32_16x16x32_bf16 v[112:115], v[220:223], v[104:107], v[112:115]\n" \
  "v_mfma_f32_16x16x32_bf16 v[116:119], v[252:255], v[104:107], v[116:119]\n" \
  "v_mfma_f32_16x16x32_bf16 v[120:123], v[220:223], v[108:111], v[120:123]\n" \
  "v_mfma_f32_16x16x32_bf16 v[124:127], v[252:255], v[108:111], v[124:127]\n" \
  "v_add_u32 v1, s66, v1\n" \
  "s_sub_u32 s66, 0, s66\n" \
  "s_add_u32 s64, s54, 16384\n" \
  "s_mov_b32 s61, 1\n" \
  "s_nop 1\n" \
  "v_cvt_pk_bf16_f32 v20, v112, v113\n" \
  "v_cvt_pk_bf16_f32 v21, v114, v115\n" \
  "v_cvt_pk_bf16_f32 v22, v116, v117\n" \
  "v_cvt_pk_bf16_f32 v23, v118, v119\n" \
  "buffer_store_dwordx4 v[20:23], v4, s[40:43], s54 offen sc1\n" \
  "v_cvt_pk_bf16_f32 v24, v120, v121\n" \
  "v_cvt_pk_bf16_f32 v25, v122, v123\n" \
  "v_cvt_pk_bf16_f32 v26, v124, v125\n" \
  "v_cvt_pk_bf16_f32 v27, v126, v127\n" \
  "buffer_store_dwordx4 v[24:27], v4, s[40:43], s64 offen sc1\n" \
  "v_readfirstlane_b32 s73, v37\n" \
  "s_sub_u32 s63, s38, s69\n" \
  "s_bfe_u32 s64, s73, 0x80010\n" \
  "s_sub_u32 s64, s64, s67\n" \
  "s_or_b32 s63, s63, s64\n" \
  "s_bfe_u32 s64, s73, 0x80008\n" \
  "s_and_b32 s73, s73, 0xff\n" \
  "s_min_u32 s73, s73, s64\n" \
  "s_or_b32 s63, s63, s68\n" \
  "s_cmp_lt_i32 s63, 0\n" \
  "s_cbranch_scc1 .LCX_E4_%=\n" \
  "v_mov_b32 v35, 0\n" \
  "global_load_dwordx4 v[128:131], v2, s[48:49] offset:0 sc1\n" \
  "global_load_dwordx4 v[132:135], v2, s[48:49] offset:512 sc1\n" \
  "global_load_dwordx4 v[136:139], v2, s[48:49] offset:1024 sc1\n" \
  "global_load_dwordx4 v[140:143], v2, s[48:49] offset:1536 sc1\n" \
  "global_load_dwordx4 v[144:147], v3, s[48:49] offset:0 sc1\n" \
  "global_load_dwordx4 v[148:151], v3, s[48:49] offset:512 sc1\n" \
  "global_load_dwordx4 v[152:155], v3, s[48:49] offset:1024 sc1\n" \
  "global_load_dwordx4 v[156:159], v3, s[48:49] offset:1536 sc1\n" \
  "global_load_dword v35, v35, s[50:51] sc1\n" \
  "s_mov_b32 s68, -1\n" \
  ".LCX_E4_%=:\n" \
  "s_add_u32 s54, s54, 0x80000\n" \
  "s_sub_u32 s60, s60, 1\n" \
  "s_cmp_lg_u32 s60, 0\n" \
  "s_cbranch_scc1 .LCX_TOP_%=\n" \
  "s_waitcnt vmcnt(0)\n" \
  "v_mov_b32 v46, s59\n" \
  "v_mov_b32 v47, 1\n" \
  "s_mov_b64 exec, 1\n" \
  "ds_add_rtn_u32 v46, v46, v47\n" \
  "s_mov_b64 exec, -1\n" \
  "s_xor_b32 s59, s59, 4\n" \
  "s_mov_b32 s61, 0\n" \
  "s_waitcnt lgkmcnt(0)\n" \
  "v_readfirstlane_b32 s63, v46\n" \
  "v_lshlrev_b32 v46, 5, v5\n" \
  "s_and_b32 s63, s63, 7\n" \
  "s_cmp_eq_u32 s63, 7\n" \
  "s_cbranch_scc0 .LCX_W5_%=\n" \
  "s_mov_b64 exec, 0xff\n" \
  "global_store_dword v46, v47, s[52:53] sc1\n" \
  "s_mov_b64 exec, -1\n" \
  ".LCX_W5_%=:\n" \
  "s_add_u32 s52, s52, 4096\n" \
  "s_addc_u32 s53, s53, 0\n" \
  ".LCX_END_%=:\n" \
  "s_endpgm\n" \
  ".LCX_FAIL_%=:\n" \
  "s_add_u32 s69, s64, 68\n" \
  "s_cmp_lg_u32 s64, 0\n" \
  "s_cbranch_scc1 .LCX_WF_%=\n" \
  "s_sleep 4\n" \
  "global_load_dword v35, v35, s[50:51] sc1\n" \
  "s_sub_u32 s62, s62, 8\n" \
  "s_cbranch_scc1 .LCX_END_%=\n" \
  "s_waitcnt vmcnt(0)\n" \
  "v_readfirstlane_b32 s64, v35\n" \
  "s_branch .LCX_FAIL_%=\n"
#define AW_ASM_Y \
  "s_mov_b32 s67, 0\n" \
  "s_mov_b32 s68, 2\n" \
  "s_mov_b32 s69, 0\n" \
  "s_mov_b32 s65, 49152\n" \
  "s_mov_b32 s66, 16384\n" \
  "v_mov_b32 v5, s58\n" \
  "v_add_u32 v3, 4096, v2\n" \
  "s_mov_b32 s62, 0x40000\n" \
  "s_branch .LCY_ASK_%=\n" \
  ".LCY_BAR_%=:\n" \
  "s_barrier\n" \
  "ds_read_b128 v[8:11], v0 offset:0\n" \
  "ds_read_b128 v[12:15], v0 offset:1024\n" \
  "ds_read_b128 v[16:19], v0 offset:2048\n" \
  "s_add_u32 s68, s68, 1\n" \
  "s_add_u32 s64, s54, 16384\n" \
  "s_cmp_eq_u32 s69, 0\n" \
  "s_cbranch_scc1 .LCY_NI_%=\n" \
  "s_waitcnt vmcnt(1)\n" \
  "s_cmp_lg_u32 s61, 0\n" \
  "s_cbranch_scc1 .LCY_NJ_%=\n" \
  "s_waitcnt vmcnt(0)\n" \
  ".LCY_NJ_%=:\n" \
  "v_mov_b32 v6, s55\n" \
  "s_mov_b64 exec, 1\n" \
  "ds_add_u32 v5, v6\n" \
  "s_mov_b64 exec, -1\n" \
  "s_mov_b32 s69, 0\n" \
  ".LCY_NI_%=:\n" \
  "v_cmp_eq_u32 vcc, 0, v4\n" \
  "s_cmp_eq_u64 vcc, 0\n" \
  "s_cselect_b32 s70, 1, 0\n" \
  "s_cbranch_scc0 .LCY_NS_%=\n" \
  "v_lshlrev_b32 v6, 16, s55\n" \
  "s_mov_b64 exec, 1\n" \
  "ds_add_u32 v5, v6\n" \
  "s_mov_b64 exec, -1\n" \
  "s_mov_b32 m0, s56\n" \
  "s_add_u32 s71, s56, 4096\n" \
  "global_load_lds_dwordx4 v2, s[48:49] offset:0 sc1\n" \
  "global_load_lds_dwordx4 v2, s[48:49] offset:1024 sc1\n" \
  "global_load_lds_dwordx4 v2, s[48:49] offset:2048 sc1\n" \
  "global_load_lds_dwordx4 v2, s[48:49] offset:3072 sc1\n" \
  "s_mov_b32 m0, s71\n" \
  "s_add_u32 s56, s56, s65\n" \
  "global_load_lds_dwordx4 v3, s[48:49] offset:0 sc1\n" \
  "global_load_lds_dwordx4 v3, s[48:49] offset:1024 sc1\n" \
  ".LCY_NS_%=:\n" \
  "ds_read_b128 v[20:23], v0 offset:3072\n" \
  "s_waitcnt lgkmcnt(3)\n" \
  "v_mfma_f32_16x16x32_bf16 v[32:35], v[64:67], v[8:11], 0\n" \
  "v_mfma_f32_16x16x32_bf16 v[36:39], v[96:99], v[8:11], 0\n" \
  "v_mfma_f32_16x16x32_bf16 v[40:43], v[128:131], v[8:11], 0\n" \
  "v_mfma_f32_16x16x32_bf16 v[44:47], v[160:163], v[8:11], 0\n" \
  "v_mfma_f32_16x16x32_bf16 v[48:51], v[192:195], v[8:11], 0\n" \
  "v_mfma_f32_16x16x32_bf16 v[52:55], v[224:227], v[8:11], 0\n" \
  "ds_read_b128 v[8:11], v0 offset:8192\n" \
  "s_waitcnt lgkmcnt(3)\n" \
  "v_mfma_f32_16x16x32_bf16 v[32:35], v[68:71], v[12:15], v[32:35]\n" \
  "v_mfma_f32_16x16x32_bf16 v[36:39], v[100:103], v[12:15], v[36:39]\n" \
  "v_mfma_f32_16x16x32_bf16 v[40:43], v[132:135], v[12:15], v[40:43]\n" \
  "v_mfma_f32_16x16x32_bf16 v[44:47], v[164:167], v[12:15], v[44:47]\n" \
  "v_mfma_f32_16x16x32_bf16 v[48:51], v[196:199], v[12:15], v[48:51]\n" \
  "v_mfma_f32_16x16x32_bf16 v[52:55], v[228:231], v[12:15], v[52:55]\n" \
  "ds_read_b128 v[12:15], v0 offset:9216\n" \
  "s_waitcnt lgkmcnt(3)\n" \
  "v_mfma_f32_16x16x32_bf16 v[32:35], v[72:75], v[16:19], v[32:35]\n" \
  "v_mfma_f32_16x16x32_bf16 v[36:39], v[104:107], v[16:19], v[36:39]\n" \
  "v_mfma_f32_16x16x32_bf16 v[40:43], v[136:139], v[16:19], v[40:43]\n" \
  "v_mfma_f32_16x16x32_bf16 v[44:47], v[168:171], v[16:19], v[44:47]\n" \
  "v_mfma_f32_16x16x32_bf16 v[48:51], v[200:203], v[16:19], v[48:51]\n" \
  "v_mfma_f32_16x16x32_bf16 v[52:55], v[232:235], v[16:19], v[52:55]\n" \
  "ds_read_b128 v[16:19], v0 offset:10240\n" \
  "s_waitcnt lgkmcnt(3)\n" \
  "v_mfma_f32_16x16x32_bf16 v[32:35], v[76:79], v[20:23], v[32:35]\n" \
  "v_mfma_f32_16x16x32_bf16 v[36:39], v[108:111], v[20:23], v[36:39]\n" \
  "v_mfma_f32_16x16x32_bf16 v[40:43], v[140:143], v[20:23], v[40:43]\n" \
  "v_mfma_f32_16x16x32_bf16 v[44:47], v[172:175], v[20:23], v[44:47]\n" \
  "v_mfma_f32_16x16x32_bf16 v[48:51], v[204:207], v[20:23], v[48:51]\n" \
  "v_mfma_f32_16x16x32_bf16 v[52:55], v[236:239], v[20:23], v[52:55]\n" \
  "ds_read_b128 v[20:23], v0 offset:11264\n" \
  "s_waitcnt lgkmcnt(3)\n" \
  "v_mfma_f32_16x16x32_bf16 v[32:35], v[80:83], v[8:11], v[32:35]\n" \
  "v_mfma_f32_16x16x32_bf16 v[36:39], v[112:115], v[8:11], v[36:39]\n" \
  "v_mfma_f32_16x16x32_bf16 v[40:43], v[144:147], v[8:11], v[40:43]\n" \
  "v_mfma_f32_16x16x32_bf16 v[44:47], v[176:179], v[8:11], v[44:47]\n" \
  "v_mfma_f32_16x16x32_bf16 v[48:51], v[208:211], v[8:11], v[48:51]\n" \
  "v_mfma_f32_16x16x32_bf16 v[52:55], v[240:243], v[8:11], v[52:55]\n" \
  "ds_read_b128 v[8:11], v0 offset:4096\n" \
  "s_waitcnt lgkmcnt(3)\n" \
  "v_mfma_f32_16x16x32_bf16 v[32:35], v[84:87], v[12:15], v[32:35]\n" \
  "v_mfma_f32_16x16x32_bf16 v[36:39], v[116:119], v[12:15], v[36:39]\n" \
  "v_mfma_f32_16x16x32_bf16 v[40:43], v[148:151], v[12:15], v[40:43]\n" \
  "v_mfma_f32_16x16x32_bf16 v[44:47], v[180:183], v[12:15], v[44:47]\n" \
  "v_mfma_f32_16x16x32_bf16 v[48:51], v[212:215], v[12:15], v[48:51]\n" \
  "v_mfma_f32_16x16x32_bf16 v[52:55], v[244:247], v[12:15], v[52:55]\n" \
  "ds_read_b128 v[12:15], v0 offset:5120\n" \
  "s_waitcnt lgkmcnt(3)\n" \
  "v_mfma_f32_16x16x32_bf16 v[32:35], v[88:91], v[16:19], v[32:35]\n" \
  "v_mfma_f32_16x16x32_bf16 v[36:39], v[120:123], v[16:19], v[36:39]\n" \
  "v_mfma_f32_16x16x32_bf16 v[40:43], v[152:155], v[16:19], v[40:43]\n" \
  "v_mfma_f32_16x16x32_bf16 v[44:47], v[184:187], v[16:19], v[44:47]\n" \
  "v_mfma_f32_16x16x32_bf16 v[48:51], v[216:219], v[16:19], v[48:51]\n" \
  "v_mfma_f32_16x16x32_bf16 v[52:55], v[248:251], v[16:19], v[52:55]\n" \
  "ds_read_b128 v[16:19], v0 offset:6144\n" \
  "s_waitcnt lgkmcnt(3)\n" \
  "v_mfma_f32_16x16x32_bf16 v[32:35], v[92:95], v[20:23], v[32:35]\n" \
  "v_mfma_f32_16x16x32_bf16 v[36:39], v[124:127], v[20:23], v[36:39]\n" \
  "v_mfma_f32_16x16x32_bf16 v[40:43], v[156:159], v[20:23], v[40:43]\n" \
  "v_mfma_f32_16x16x32_bf16 v[44:47], v[188:191], v[20:23], v[44:47]\n" \
  "v_mfma_f32_16x16x32_bf16 v[48:51], v[220:223], v[20:23], v[48:51]\n" \
  "v_mfma_f32_16x16x32_bf16 v[52:55], v[252:255], v[20:23], v[52:55]\n" \
  "ds_read_b128 v[20:23], v0 offset:7168\n" \
  "s_waitcnt lgkmcnt(3)\n" \
  "s_nop 1\n" \
  "v_cvt_pk_bf16_f32 v24, v32, v33\n" \
  "v_cvt_pk_bf16_f32 v25, v34, v35\n" \
  "v_cvt_pk_bf16_f32 v26, v36, v37\n" \
  "v_cvt_pk_bf16_f32 v27, v38, v39\n" \
  "s_nop 0\n" \
  "v_mfma_f32_16x16x32_bf16 v[32:35], v[64:67], v[8:11], 0\n" \
  "v_mfma_f32_16x16x32_bf16 v[36:39], v[96:99], v[8:11], 0\n" \
  "buffer_store_dwordx4 v[24:27], v1, s[40:43], s54 offen offset:0 sc1\n" \
  "v_cvt_pk_bf16_f32 v28, v40, v41\n" \
  "v_cvt_pk_bf16_f32 v29, v42, v43\n" \
  "v_cvt_pk_bf16_f32 v30, v44, v45\n" \
  "v_cvt_pk_bf16_f32 v31, v46, v47\n" \
  "s_nop 0\n" \
  "v_mfma_f32_16x16x32_bf16 v[40:43], v[128:131], v[8:11], 0\n" \
  "v_mfma_f32_16x16x32_bf16 v[44:47], v[160:163], v[8:11], 0\n" \
  "buffer_store_dwordx4 v[28:31], v1, s[40:43], s54 offen offset:128 sc1\n" \
  "v_cvt_pk_bf16_f32 v56, v48, v49\n" \
  "v_cvt_pk_bf16_f32 v57, v50, v51\n" \
  "v_cvt_pk_bf16_f32 v58, v52, v53\n" \
  "v_cvt_pk_bf16_f32 v59, v54, v55\n" \
  "s_nop 0\n" \
  "v_mfma_f32_16x16x32_bf16 v[48:51], v[192:195], v[8:11], 0\n" \
  "v_mfma_f32_16x16x32_bf16 v[52:55], v[224:227], v[8:11], 0\n" \
  "buffer_store_dwordx4 v[56:59], v1, s[40:43], s54 offen offset:256 sc1\n" \
  "ds_read_b128 v[8:11], v0 offset:12288\n" \
  "s_waitcnt lgkmcnt(3)\n" \
  "v_mfma_f32_16x16x32_bf16 v[32:35], v[68:71], v[12:15], v[32:35]\n" \
  "v_mfma_f32_16x16x32_bf16 v[36:39], v[100:103], v[12:15], v[36:39]\n" \
  "v_mfma_f32_16x16x32_bf16 v[40:43], v[132:135], v[12:15], v[40:43]\n" \
  "v_mfma_f32_16x16x32_bf16 v[44:47], v[164:167], v[12:15], v[44:47]\n" \
  "v_mfma_f32_16x16x32_bf16 v[48:51], v[196:199], v[12:15], v[48:51]\n" \
  "v_mfma_f32_16x16x32_bf16 v[52:55], v[228:231], v[12:15], v[52:55]\n" \
  "ds_read_b128 v[12:15], v0 offset:13312\n" \
  "s_waitcnt lgkmcnt(3)\n" \
  "v_mfma_f32_16x16x32_bf16 v[32:35], v[72:75], v[16:19], v[32:35]\n" \
  "v_mfma_f32_16x16x32_bf16 v[36:39], v[104:107], v[16:19], v[36:39]\n" \
  "v_mfma_f32_16x16x32_bf16 v[40:43], v[136:139], v[16:19], v[40:43]\n" \
  "v_mfma_f32_16x16x32_bf16 v[44:47], v[168:171], v[16:19], v[44:47]\n" \
  "v_mfma_f32_16x16x32_bf16 v[48:51], v[200:203], v[16:19], v[48:51]\n" \
  "v_mfma_f32_16x16x32_bf16 v[52:55], v[232:235], v[16:19], v[52:55]\n" \
  "ds_read_b128 v[16:19], v0 offset:14336\n" \
  "s_waitcnt lgkmcnt(3)\n" \
  "v_mfma_f32_16x16x32_bf16 v[32:35], v[76:79], v[20:23], v[32:35]\n" \
  "v_mfma_f32_16x16x32_bf16 v[36:39], v[108:111], v[20:23], v[36:39]\n" \
  "v_mfma_f32_16x16x32_bf16 v[40:43], v[140:143], v[20:23], v[40:43]\n" \
  "v_mfma_f32_16x16x32_bf16 v[44:47], v[172:175], v[20:23], v[44:47]\n" \
  "v_mfma_f32_16x16x32_bf16 v[48:51], v[204:207], v[20:23], v[48:51]\n" \
  "v_mfma_f32_16x16x32_bf16 v[52:55], v[236:239], v[20:23], v[52:55]\n" \
  "ds_read_b128 v[20:23], v0 offset:15360\n" \
  "s_waitcnt lgkmcnt(3)\n" \
  "v_mfma_f32_16x16x32_bf16 v[32:35], v[80:83], v[8:11], v[32:35]\n" \
  "v_mfma_f32_16x16x32_bf16 v[36:39], v[112:115], v[8:11], v[36:39]\n" \
  "v_mfma_f32_16x16x32_bf16 v[40:43], v[144:147], v[8:11], v[40:43]\n" \
  "v_mfma_f32_16x16x32_bf16 v[44:47], v[176:179], v[8:11], v[44:47]\n" \
  "v_mfma_f32_16x16x32_bf16 v[48:51], v[208:211], v[8:11], v[48:51]\n" \
  "v_mfma_f32_16x16x32_bf16 v[52:55], v[240:243], v[8:11], v[52:55]\n" \
  "s_waitcnt lgkmcnt(2)\n" \
  "v_mfma_f32_16x16x32_bf16 v[32:35], v[84:87], v[12:15], v[32:35]\n" \
  "v_mfma_f32_16x16x32_bf16 v[36:39], v[116:119], v[12:15], v[36:39]\n" \
  "v_mfma_f32_16x16x32_bf16 v[40:43], v[148:151], v[12:15], v[40:43]\n" \
  "v_mfma_f32_16x16x32_bf16 v[44:47], v[180:183], v[12:15], v[44:47]\n" \
  "v_mfma_f32_16x16x32_bf16 v[48:51], v[212:215], v[12:15], v[48:51]\n" \
  "v_mfma_f32_16x16x32_bf16 v[52:55], v[244:247], v[12:15], v[52:55]\n" \
  "s_waitcnt lgkmcnt(1)\n" \
  "v_mfma_f32_16x16x32_bf16 v[32:35], v[88:91], v[16:19], v[32:35]\n" \
  "v_mfma_f32_16x16x32_bf16 v[36:39], v[120:123], v[16:19], v[36:39]\n" \
  "v_mfma_f32_16x16x32_bf16 v[40:43], v[152:155], v[16:19], v[40:43]\n" \
  "v_mfma_f32_16x16x32_bf16 v[44:47], v[184:187], v[16:19], v[44:47]\n" \
  "v_mfma_f32_16x16x32_bf16 v[48:51], v[216:219], v[16:19], v[48:51]\n" \
  "v_mfma_f32_16x16x32_bf16 v[52:55], v[248:251], v[16:19], v[52:55]\n" \
  "s_waitcnt lgkmcnt(0)\n" \
  "v_mfma_f32_16x16x32_bf16 v[32:35], v[92:95], v[20:23], v[32:35]\n" \
  "v_mfma_f32_16x16x32_bf16 v[36:39], v[124:127], v[20:23], v[36:39]\n" \
  "v_mfma_f32_16x16x32_bf16 v[40:43], v[156:159], v[20:23], v[40:43]\n" \
  "v_mfma_f32_16x16x32_bf16 v[44:47], v[188:191], v[20:23], v[44:47]\n" \
  "v_mfma_f32_16x16x32_bf16 v[48:51], v[220:223], v[20:23], v[48:51]\n" \
  "v_mfma_f32_16x16x32_bf16 v[52:55], v[252:255], v[20:23], v[52:55]\n" \
  "v_add_u32 v0, s66, v0\n" \
  "s_sub_u32 s66, 0, s66\n" \
  "s_add_u32 s54, s54, 0x80000\n" \
  "s_mov_b32 s62, 0x40000\n" \
  "v_cvt_pk_bf16_f32 v24, v32, v33\n" \
  "v_cvt_pk_bf16_f32 v25, v34, v35\n" \
  "v_cvt_pk_bf16_f32 v26, v36, v37\n" \
  "v_cvt_pk_bf16_f32 v27, v38, v39\n" \
  "buffer_store_dwordx4 v[24:27], v1, s[40:43], s64 offen offset:0 sc1\n" \
  "v_cvt_pk_bf16_f32 v28, v40, v41\n" \
  "v_cvt_pk_bf16_f32 v29, v42, v43\n" \
  "v_cvt_pk_bf16_f32 v30, v44, v45\n" \
  "v_cvt_pk_bf16_f32 v31, v46, v47\n" \
  "buffer_store_dwordx4 v[28:31], v1, s[40:43], s64 offen offset:128 sc1\n" \
  "v_cvt_pk_bf16_f32 v56, v48, v49\n" \
  "v_cvt_pk_bf16_f32 v57, v50, v51\n" \
  "v_cvt_pk_bf16_f32 v58, v52, v53\n" \
  "v_cvt_pk_bf16_f32 v59, v54, v55\n" \
  "buffer_store_dwordx4 v[56:59], v1, s[40:43], s64 offen offset:256 sc1\n" \
  "s_cmp_eq_u32 s70, 0\n" \
  "s_cbranch_scc1 .LCY_ND_%=\n" \
  "global_load_lds_dwordx4 v3, s[48:49] offset:2048 sc1\n" \
  "global_load_lds_dwordx4 v3, s[48:49] offset:3072 sc1\n" \
  "s_add_u32 s48, s48, 0x80000\n" \
  "s_addc_u32 s49, s49, 0\n" \
  "s_mov_b32 m0, s57\n" \
  "s_add_u32 s57, s57, s65\n" \
  "global_load_lds_dwordx4 v2, s[50:51] offset:0 sc1\n" \
  "global_load_lds_dwordx4 v2, s[50:51] offset:1024 sc1\n" \
  "s_sub_u32 s65, 0, s65\n" \
  "s_add_u32 s67, s67, 4096\n" \
  "global_load_lds_dwordx4 v2, s[50:51] offset:2048 sc1\n" \
  "s_sub_u32 s68, s68, 1\n" \
  "s_mov_b32 s69, 1\n" \
  "global_load_lds_dwordx4 v2, s[50:51] offset:3072 sc1\n" \
  "s_add_u32 s50, s50, 0x40000\n" \
  "s_addc_u32 s51, s51, 0\n" \
  "v_mov_b32 v4, 0\n" \
  "buffer_load_dword v4, v4, s[44:47], s67 offen sc1\n" \
  "s_waitcnt vmcnt(7)\n" \
  "s_branch .LCY_WC_%=\n" \
  ".LCY_ND_%=:\n" \
  "v_mov_b32 v4, 0\n" \
  "buffer_load_dword v4, v4, s[44:47], s67 offen sc1\n" \
  "s_waitcnt vmcnt(0)\n" \
  ".LCY_WC_%=:\n" \
  "v_mov_b32 v6, s59\n" \
  "v_mov_b32 v7, 1\n" \
  "s_mov_b64 exec, 1\n" \
  "ds_add_rtn_u32 v6, v6, v7\n" \
  "s_mov_b64 exec, -1\n" \
  "s_xor_b32 s59, s59, 4\n" \
  "s_mov_b32 s61, 0\n" \
  "s_waitcnt lgkmcnt(0)\n" \
  "v_readfirstlane_b32 s63, v6\n" \
  "v_lshlrev_b32 v6, 5, v2\n" \
  "s_and_b32 s63, s63, 7\n" \
  "s_cmp_eq_u32 s63, 7\n" \
  "s_cbranch_scc0 .LCY_W1_%=\n" \
  "s_mov_b64 exec, 0xff\n" \
  "global_store_dword v6, v7, s[52:53] sc1\n" \
  "s_mov_b64 exec, -1\n" \
  "s_mov_b32 s61, 1\n" \
  ".LCY_W1_%=:\n" \
  "s_add_u32 s52, s52, 4096\n" \
  "s_addc_u32 s53, s53, 0\n" \
  "s_sub_u32 s60, s60, 1\n" \
  "s_cmp_eq_u32 s60, 0\n" \
  "s_cbranch_scc1 .LCY_END_%=\n" \
  ".LCY_NEED_%=:\n" \
  "s_cmp_eq_u32 s68, 0\n" \
  "s_cbranch_scc1 .LCY_BAR_%=\n" \
  "v_cmp_eq_u32 vcc, 0, v4\n" \
  "s_cmp_eq_u64 vcc, 0\n" \
  "s_cbranch_scc1 .LCY_GET_%=\n" \
  "s_cmp_eq_u32 s68, 1\n" \
  "s_cbranch_scc1 .LCY_BAR_%=\n" \
  "s_cmp_ge_u32 s67, 0x40000\n" \
  "s_cbranch_scc1 .LCY_BAR_%=\n" \
  "s_sleep 12\n" \
  ".LCY_ASK_%=:\n" \
  "v_mov_b32 v4, 0\n" \
  "buffer_load_dword v4, v4, s[44:47], s67 offen sc1\n" \
  "s_sub_u32 s62, s62, 1\n" \
  "s_cbranch_scc1 .LCY_END_%=\n" \
  "s_waitcnt vmcnt(0)\n" \
  "s_branch .LCY_NEED_%=\n" \
  ".LCY_GET_%=:\n" \
  "v_lshlrev_b32 v6, 16, s55\n" \
  "s_mov_b64 exec, 1\n" \
  "ds_add_u32 v5, v6\n" \
  "s_mov_b64 exec, -1\n" \
  "s_mov_b32 m0, s56\n" \
  "s_add_u32 s71, s56, 4096\n" \
  "global_load_lds_dwordx4 v2, s[48:49] offset:0 sc1\n" \
  "global_load_lds_dwordx4 v2, s[48:49] offset:1024 sc1\n" \
  "global_load_lds_dwordx4 v2, s[48:49] offset:2048 sc1\n" \
  "global_load_lds_dwordx4 v2, s[48:49] offset:3072 sc1\n" \
  "s_mov_b32 m0, s71\n" \
  "s_add_u32 s56, s56, s65\n" \
  "global_load_lds_dwordx4 v3, s[48:49] offset:0 sc1\n" \
  "global_load_lds_dwordx4 v3, s[48:49] offset:1024 sc1\n" \
  "global_load_lds_dwordx4 v3, s[48:49] offset:2048 sc1\n" \
  "global_load_lds_dwordx4 v3, s[48:49] offset:3072 sc1\n" \
  "s_add_u32 s48, s48, 0x80000\n" \
  "s_addc_u32 s49, s49, 0\n" \
  "s_mov_b32 m0, s57\n" \
  "s_add_u32 s57, s57, s65\n" \
  "global_load_lds_dwordx4 v2, s[50:51] offset:0 sc1\n" \
  "global_load_lds_dwordx4 v2, s[50:51] offset:1024 sc1\n" \
  "s_sub_u32 s65, 0, s65\n" \
  "s_add_u32 s67, s67, 4096\n" \
  "global_load_lds_dwordx4 v2, s[50:51] offset:2048 sc1\n" \
  "s_sub_u32 s68, s68, 1\n" \
  "s_mov_b32 s69, 1\n" \
  "global_load_lds_dwordx4 v2, s[50:51] offset:3072 sc1\n" \
  "s_add_u32 s50, s50, 0x40000\n" \
  "s_addc_u32 s51, s51, 0\n" \
  "v_mov_b32 v4, 0\n" \
  "buffer_load_dword v4, v4, s[44:47], s67 offen sc1\n" \
  "s_waitcnt vmcnt(0)\n" \
  "v_mov_b32 v6, s55\n" \
  "s_mov_b64 exec, 1\n" \
  "ds_add_u32 v5, v6\n" \
  "s_mov_b64 exec, -1\n" \
  "s_mov_b32 s69, 0\n" \
  "s_branch .LCY_NEED_%=\n" \
  ".LCY_END_%=:\n" \
  "s_endpgm\n"
template <bool AT>
__device__ __forceinline__ void role_aw_t(const MegaArgs& A, char* smem, const int L, const int a) {
  const int w = __builtin_amdgcn_readfirstlane(threadIdx.x >> 6);
  const int l = threadIdx.x & 63, c = l & 15, g = l >> 4;
  const int qt = w & 1, hh = (w >> 1) & 1, h = 2 * a + hh;
  char* ws = A.ws;
  unsigned* FL = (unsigned*)(ws + OFF_FL);
  const unsigned sb = lds_base(smem);
  if (threadIdx.x < 4) reinterpret_cast<unsigned*>(smem + AW_CNT)[threadIdx.x] = 0u;
  constexpr int NP = AT ? 1 : 3;
  const int cp0 = AT ? w : 3 * w - 8;
  v8u wo[2 * NP][4];
  {
    const int q = (l >> 2) & 3, p = l & 3, f = q | ((g & 1) << 2);
    const unsigned ra = sb + (4 * g + q) * 256 + (p & 1) * 16;
#pragma unroll
    for (int r = 0; r < 2; ++r) {
#pragma unroll
      for (int k = 0; k < 2; ++k) {
        const char* base_ = (const char*)A.Wo + ((size_t)((2 * a + r) * DH + 64 * (w & 1) + 32 * k) * DM + 128 * (w >> 1)) * 2;
#pragma unroll
        for (int i = 0; i < 8; ++i) {
          const int fd = g | ((i & 1) << 2);
          glds16(base_, (unsigned)((4 * i + g) * (DM * 2) + ((c ^ (fd << 1)) << 4)), sb + w * 16384 + k * 8192 + i * 1024);
        }
      }
      SST(4 + 3 * r);
      WAIT_VM(0);
      RAW_BAR();
      SST(5 + 3 * r);
#pragma unroll
      for (int k2 = 0; k2 < 2; ++k2) {
#pragma unroll
        for (int ct = 0; ct < 2 * NP; ++ct) {
          const int cp = cp0 + (ct >> 1);
          const unsigned a_ = ra + (cp >> 2) * 32768 + k2 * 16384 + ((2 * (cp & 3) + (p >> 1)) ^ f) * 32 + (ct & 1) * 8;
          const v2u r0 = trd(a_), r1 = trd(a_ + 4096), r2 = trd(a_ + 8192), r3 = trd(a_ + 12288);
          wo[ct][2 * r + k2] = v8u{r0.x, r0.y, r1.x, r1.y, r2.x, r2.y, r3.x, r3.y};
          asm volatile("" : "+v"(wo[ct][2 * r + k2]));
        }
      }
      RAW_BAR();
      SST(6 + 3 * r);
    }
  }
  WSTAMP2();
  const v4u rsW = mk_srd(ws + OFF_PW, SZ_PW);
  const v4u rsF = mk_srd(FL + FL_FQ + (L * 64 * 32 + h) * 32, (64 * 32 - h) * 128);
  const char* p0 = AT ? ws + OFF_MEMK + L * SZ_MEM + h * 2048 : ws + OFF_QK + L * SZ_QK + ((size_t)h * 16 + 8 * qt) * 1024;
  const char* p1 = AT ? (const char*)(FL + FL_FK + L * 32 + (h >> 3) * 64 + (h & 7) * 2) : ws + OFF_VF + L * SZ_O + ((size_t)h * 8 + 4 * qt) * 1024;
  const unsigned* pFW = FL + FL_FW + (L * 64 * 32 + (a >> 2)) * 32 + (a & 3);
  const unsigned tk = sb + AW_TK + hh * 24 * 1024, voff = (c >> 1) * 2048 + (c & 1) * 64 + g * 16;
  const char* dbg = ws + WS_TOTAL + A.vb * 64 * 8;
#define AW_SOPS                                                                                                                 \
  "{s[40:43]}"(rsW), "{s[44:47]}"(rsF), "{s[48:49]}"(p0), "{s[50:51]}"(p1), "{s[52:53]}"(pFW),                                   \
      "{s54}"((L * 64 * 16 + a) * 32768 + cp0 * 128), "{s55}"(1 << (8 * qt)), "{s56}"(tk + qt * 8192), "{s57}"(tk + 16384 + qt * 4096),                  \
      "{s58}"(sb + AW_CNT + 8 + 4 * hh), "{s59}"(sb + AW_CNT), "{s60}"(NSEG), "{s[36:37]}"(dbg)                                  \
      : "memory", "vcc", "scc", "s80"
  if constexpr (AT) {
    const v8u pv = {tk + l * 16, sb + AW_SO + l * 16, (unsigned)(g * 8 + (c & 7)) * 16, (unsigned)(OFF_MEMV - OFF_MEMK) + c * 32 + (g & 1) * 16,
                    voff, (unsigned)l * 16, tk + qt * 4096 + l * 16, sb + AW_SO + w * 4096 + l * 16};
    const unsigned neg = 0xff61b1e6u;
    v8u bt;
#pragma unroll
    for (int i = 0; i < 4; ++i) {
      const unsigned d = 4 * g + i > c ? neg : 0u;
      bt[i] = qt ? 0u : d;
      bt[4 + i] = qt ? d : neg;
    }
    const unsigned e = g >= 2 ? neg : 0u;
    const v4u bm = {e, e, e, e};
    asm volatile("s_mov_b64 exec, -1\ns_waitcnt vmcnt(0) lgkmcnt(0)\ns_nop 4\n" AW_ASM_X
                 ::"{v[192:199]}"(wo[0][0]), "{v[200:207]}"(wo[0][1]), "{v[208:215]}"(wo[0][2]), "{v[216:223]}"(wo[0][3]),
                 "{v[224:231]}"(wo[1][0]), "{v[232:239]}"(wo[1][1]), "{v[240:247]}"(wo[1][2]), "{v[248:255]}"(wo[1][3]),
                 "{v[0:7]}"(pv), "{v[8:15]}"(bt), "{v[16:19]}"(bm), AW_SOPS);
  } else {
    const v4u pv = {sb + AW_SO + l * 16, voff, (unsigned)l * 16, 0u};
    asm volatile("s_mov_b64 exec, -1\ns_waitcnt vmcnt(0) lgkmcnt(0)\ns_nop 4\n" AW_ASM_Y
                 ::"{v[64:71]}"(wo[0][0]), "{v[72:79]}"(wo[0][1]), "{v[80:87]}"(wo[0][2]), "{v[88:95]}"(wo[0][3]),
                 "{v[96:103]}"(wo[1][0]), "{v[104:111]}"(wo[1][1]), "{v[112:119]}"(wo[1][2]), "{v[120:127]}"(wo[1][3]),
                 "{v[128:135]}"(wo[2][0]), "{v[136:143]}"(wo[2][1]), "{v[144:151]}"(wo[2][2]), "{v[152:159]}"(wo[2][3]),
                 "{v[160:167]}"(wo[3][0]), "{v[168:175]}"(wo[3][1]), "{v[176:183]}"(wo[3][2]), "{v[184:191]}"(wo[3][3]),
                 "{v[192:199]}"(wo[4][0]), "{v[200:207]}"(wo[4][1]), "{v[208:215]}"(wo[4][2]), "{v[216:223]}"(wo[4][3]),
                 "{v[224:231]}"(wo[5][0]), "{v[232:239]}"(wo[5][1]), "{v[240:247]}"(wo[5][2]), "{v[248:255]}"(wo[5][3]),
                 "{v[0:3]}"(pv), AW_SOPS);
  }
#undef AW_SOPS
}
__device__ __forceinline__ void role_aw(const MegaArgs& A, char* smem, const int L, const int a) {
  if (__builtin_amdgcn_readfirstlane(threadIdx.x >> 6) < 4) role_aw_t<true>(A, smem, L, a);
  else role_aw_t<false>(A, smem, L, a);
}

constexpr int RG = 2, RW = 2;
__device__ __forceinline__ void role_red(const MegaArgs& A, char* smem, const int L, const int idx) {
  const int w = __builtin_amdgcn_readfirstlane(threadIdx.x >> 6);
  const int l = threadIdx.x & 63;
  const int gr = (w >> 1) & 1, r = idx * 2 + (w & 1);
  char* ws = A.ws;
  unsigned* FL = (unsigned*)(ws + OFF_FL);
  unsigned* TMO = FL + FL_TMO;
  const __amdgpu_buffer_rsrc_t rsW = MKRS(ws + OFF_PW, SZ_PW);
  const __amdgpu_buffer_rsrc_t rsX = MKRS(ws + (L ? OFF_X3 : OFF_XA), SZ_X);
  const __amdgpu_buffer_rsrc_t rsY = L ? MKRS(A.out, SZ_X) : MKRS(ws + OFF_X2, SZ_X);
  volatile unsigned* sw = reinterpret_cast<volatile unsigned*>(smem) + (w >> 1) * 32;
  if (threadIdx.x < 128) reinterpret_cast<volatile unsigned*>(smem)[threadIdx.x] = 0u;
  __syncthreads();
  if ((EXP_OFF & 2) && L == 0 && w < 4) return;
  if ((EXP_OFF & 64) && L == 1 && w < 4) return;
  if ((EXP_OFF & 8) && L == 1 && w >= 4) return;
  unsigned tl = now10();
  if (w >= 4) {
    for (int s = gr + (L ? 0 : 2); s < NSEG; s += RG) {
      grp_wait<SLP_N, 8, GATE_N>((w & 1) == 0, sw, s + 1, FL + FL_FC + ((L * 64 + s) * 8 + (blockIdx.x & (NR_C - 1))) * 8 * 32, 64, TMO, tl);
      if (L == 0) tl = now10();
      const unsigned st = row_stats(LD128(rsX, l * 512, s * 32768 + r * 16));
      const unsigned long long v = ((unsigned long long)__float_as_uint(1.0f / hi16(st)) << 32) | ((st << 16) | 1u);
      if (l < NR_N) __hip_atomic_store((gu64*)(FL + FL_FN + ((L * 64 + s) * 8 + l) * 64) + r, v, RLX_AGENT);
      TS(7, s, 256 + gr * 128);
    }
    return;
  }
  if (L == 0) {
    for (int s = gr; s < NSEG; s += RG) ST128((v4u{0u, 0u, 0u, 0u}), rsY, l * 16, (s * SEG + r) * DM * 2);
    DRAIN();
    if (l < 8) set_flag(FL + FL_FZ + (2 * l + (idx >> 3)) * 32 + (idx & 7) * 4 + w);
  }
  for (int s = gr; s < NSEG; s += RG) {
    grp_wait<SLP_R, 4, GATE_R>((w & 1) == 0, sw, s + 1, FL + FL_FW + ((L * 64 + s) * 8 + (blockIdx.x & (NR_W - 1))) * 4 * 32, 16, TMO, tl);
    if (s >= NSEG - 6) tl = now10();
    TS(0, s, gr * RW * 64);
    TS(4, s, gr * RW * 64 + (RW - 1) * 64);
    v4u p[16];
    const v4u xr = LD128(rsX, l * 512, s * 32768 + r * 16);
#pragma unroll
    for (int k = 0; k < 16; ++k)
      p[k] = LD128(rsW, (l >> 2) * 128 + (l & 3) * 16, ((L * 64 + s) * 16 + k) * 32768 + (r >> 1) * 2048 + (r & 1) * 64);
    v4u xo;
#pragma unroll
    for (int e = 0; e < 4; ++e) {
      f32x2_t t[4];
#pragma unroll
      for (int k = 0; k < 4; ++k)
        t[k] = (f32x2_t{lo16(p[4 * k][e]), hi16(p[4 * k][e])} + f32x2_t{lo16(p[4 * k + 1][e]), hi16(p[4 * k + 1][e])}) +
               (f32x2_t{lo16(p[4 * k + 2][e]), hi16(p[4 * k + 2][e])} + f32x2_t{lo16(p[4 * k + 3][e]), hi16(p[4 * k + 3][e])});
      const f32x2_t u = (t[0] + t[1]) + (t[2] + t[3]);
      const unsigned a0 = pk2(u.x, u.y);
      xo[e] = pk2(lo16(xr[e]) + lo16(a0), hi16(xr[e]) + hi16(a0));
      if (L == 0) xo[e] = xo[e] ? xo[e] : 0x8000u;
    }
    ST128(xo, rsY, l * 16, (s * SEG + r) * DM * 2);
    TS(1, s, gr * RW * 64);
    TS(5, s, gr * RW * 64 + (RW - 1) * 64);
    if (L == 0) {
      const unsigned st = row_stats(xo);
      if (l < NR_R) __hip_atomic_store((gu32*)(FL + FL_FR + ((s * 8 + l) * 4 + (r >> 3)) * 32 + (r & 7)), st, RLX_AGENT);
    }
    TS(6, s, gr * RW * 64 + (RW - 1) * 64);
    TS(3, s, gr * RW * 64);
  }
}

constexpr int CUN = 2144;
__device__ __forceinline__ bf16x8 conv_bfrag(const unsigned* up, const unsigned sh) {
  const unsigned d0 = up[0], d1 = up[1], d2 = up[2], d3 = up[3], d4 = up[4];
  const v4u r = {__builtin_amdgcn_alignbit(d1, d0, sh), __builtin_amdgcn_alignbit(d2, d1, sh),
                 __builtin_amdgcn_alignbit(d3, d2, sh), __builtin_amdgcn_alignbit(d4, d3, sh)};
  return BF8(r);
}
__device__ __forceinline__ void role_conv(const MegaArgs& A, char* smem, const int cv) {
  u16* Kr = reinterpret_cast<u16*>(smem);
  u16* Uc = Kr + 8 * 2 * CKN;
  u16* Xr = Uc + 8 * CUN;
  u16* To = Uc + 8 * 2 * CUN;
  u16* Su = To + 32 * 8;
  u16* Sx = Su + 32 * 8;
  const int tid = threadIdx.x;
  const int w = __builtin_amdgcn_readfirstlane(tid >> 6);
  const int l = tid & 63, c = l & 15, g = l >> 4;
  const int d = 8 * cv + w;
  char* ws = A.ws;
  unsigned* FL = (unsigned*)(ws + OFF_FL);
  unsigned* TMO = FL + FL_TMO;
  u16* kr = Kr + w * 2 * CKN;
  float* prm = reinterpret_cast<float*>(kr);
  v4u tx[4];
  {
    const unsigned pa = *reinterpret_cast<const unsigned*>(A.logAre + d * NST + 2 * l);
    const unsigned pb = *reinterpret_cast<const unsigned*>(A.Aim + d * NST + 2 * l);
    const unsigned pc = *reinterpret_cast<const unsigned*>(A.Cre + d * NST + 2 * l);
    const unsigned pd = *reinterpret_cast<const unsigned*>(A.Cim + d * NST + 2 * l);
    const float dt = bfr(expf(bf2f(A.logdt[d])));
    if (w < 2) tx[0] = *reinterpret_cast<const v4u*>(A.x0 + (size_t)tid * DM + 8 * cv);
    if (l < 16) reinterpret_cast<unsigned*>(Uc + w * CUN)[l] = 0u;
    if (l < 32) reinterpret_cast<unsigned*>(Uc + w * CUN + 32 + L_SEQ)[l] = 0u;
    if (l < 32) reinterpret_cast<unsigned*>(Uc + w * CUN + 32 + 128)[l] = 0u;
    if (l < 48) {
      reinterpret_cast<v4u*>(kr + 1824)[l] = v4u{0u, 0u, 0u, 0u};
      reinterpret_cast<v4u*>(kr + CKN + 1824)[l] = v4u{0u, 0u, 0u, 0u};
    }
#pragma unroll
    for (int e = 0; e < 2; ++e) {
      const float ar = -bfr(expf(e ? hi16(pa) : lo16(pa))) * dt, ai = (e ? hi16(pb) : lo16(pb)) * dt;
      const float ex = expf(ar);
      float sn, cs;
      sincosf(ai, &sn, &cs);
      prm[2 * l + e] = ar;
      prm[NST + 2 * l + e] = ai;
      prm[2 * NST + 2 * l + e] = (e ? hi16(pc) : lo16(pc)) * dt;
      prm[3 * NST + 2 * l + e] = (e ? hi16(pd) : lo16(pd)) * dt;
      prm[4 * NST + 2 * l + e] = ex * cs;
      prm[5 * NST + 2 * l + e] = ex * sn;
    }
  }
  const float dsk = bf2f(A.Dskip[d]);
  const u16* ka = kr + (c >> 3) * CKN + 2080 - 16 * (c & 7) + 8 * g;
  const unsigned ush = (c & 1) * 16;
#define KFRAG(D0) (*reinterpret_cast<const bf16x8*>(ka - (D0)))
  f32x4 acc[16], far[9];
  v4u xf[3];
  const unsigned* ub1 = reinterpret_cast<const unsigned*>(Uc + w * CUN + (c & ~1) + 8 * g + 32);
  const __amdgpu_buffer_rsrc_t rz1 = MKRS(ws + OFF_XA, SZ_X);
  const int t0 = 16 * (4 * (g & 1) + (g >> 1)) + c;
  {
    const u16* uc = Uc + w * CUN;
    const unsigned* ub = reinterpret_cast<const unsigned*>(uc + (c & ~1) + 8 * g + 32);
    const __amdgpu_buffer_rsrc_t rz = MKRS(ws + OFF_XA, SZ_X);
    const __amdgpu_buffer_rsrc_t rst = MKRS(ws + OFF_GT, L_SEQ * 4);
#define TILE_OUT(T)                                                                            \
  {                                                                                            \
    const float u0 = bf2f(uc[32 + 128 * (T) + t0]), u1 = bf2f(uc[64 + 128 * (T) + t0]);        \
    const float a0 = bfr(bfr(y0) + bfr(u0 * dsk)), a1 = bfr(bfr(y1) + bfr(u1 * dsk));          \
    u16* xp = Xr + (128 * (T) + t0) * 8 + w;                                                   \
    xp[0] = f2bf(bf2f(xp[0]) + a0);                                                            \
    xp[256] = f2bf(bf2f(xp[256]) + a1);                                                        \
  }
#define CHUNK_ST(ck)                                                                           \
  {                                                                                            \
    const v4u o = *reinterpret_cast<const v4u*>(Xr + (64 * (ck) + l) * 8);                     \
    ST128(o, rz, (l >> 5) * 32768 + (l & 31) * 16, (2 * (ck) * 64 + cv) * 512);                \
  }
#define U_ROW(row)                                                                             \
  {                                                                                            \
    const unsigned st_ = __builtin_amdgcn_raw_buffer_load_b32(rst, (row) * 4, 0, AUX_SC1);     \
    const v4u xr_ = *reinterpret_cast<const v4u*>(Xr + (row) * 8);                             \
    const float mb_ = lo16(st_), ri_ = 1.0f / hi16(st_);                                       \
    _Pragma("unroll") for (int e = 0; e < 4; ++e) {                                            \
      const unsigned t_ = pk2(lo16(xr_[e]) - mb_, hi16(xr_[e]) - mb_);                         \
      const unsigned q_ = pk2(lo16(t_) * ri_, hi16(t_) * ri_);                                 \
      Uc[(2 * e) * CUN + 32 + (row)] = (u16)q_;                                                \
      Uc[(2 * e + 1) * CUN + 32 + (row)] = (u16)(q_ >> 16);                                    \
    }                                                                                          \
  }
    const unsigned* FS = FL + FL_FS + (blockIdx.x & 7) * 32 * 32;
    const int gi = 4 * (g * 8 + (l & 7));
    const int tl = 32 * ((l >> 3) & 1) + 8 * g + (l & 7);
    unsigned khl = 0u;
#pragma unroll 1
    for (int ps = 0; ps < 4; ++ps) {
      float v[32];
      {
        const f32x4 ar = *reinterpret_cast<const f32x4*>(prm + gi), ai = *reinterpret_cast<const f32x4*>(prm + NST + gi);
        const f32x4 cr = *reinterpret_cast<const f32x4*>(prm + 2 * NST + gi), ci = *reinterpret_cast<const f32x4*>(prm + 3 * NST + gi);
        const f32x4 wr = *reinterpret_cast<const f32x4*>(prm + 4 * NST + gi), wi = *reinterpret_cast<const f32x4*>(prm + 5 * NST + gi);
        const float p0 = (float)(64 * ps + 32 * ((l >> 3) & 1));
        float yr0[4], yi0[4];
#pragma unroll
        for (int k = 0; k < 4; ++k) {
          const float e = __expf(ar[k] * p0);
          float sn, cs;
          __sincosf(ai[k] * p0, &sn, &cs);
          const float zr = e * cs, zi = e * sn;
          yr0[k] = cr[k] * zr - ci[k] * zi;
          yi0[k] = cr[k] * zi + ci[k] * zr;
        }
        f32x2_t yra = {yr0[0], yr0[1]}, yia = {yi0[0], yi0[1]}, wra = {wr[0], wr[1]}, wia = {wi[0], wi[1]};
        f32x2_t yrb = {yr0[2], yr0[3]}, yib = {yi0[2], yi0[3]}, wrb = {wr[2], wr[3]}, wib = {wi[2], wi[3]};
#pragma unroll
        for (int i = 0; i < 32; ++i) {
          const f32x2_t sm2 = yra + yrb;
          v[i] = sm2.x + sm2.y;
          const f32x2_t ta = yra * wra - yia * wia, tb = yrb * wrb - yib * wib;
          yia = yra * wia + yia * wra;
          yib = yrb * wib + yib * wrb;
          yra = ta;
          yrb = tb;
        }
      }
#pragma unroll
      for (int i = 0; i < 16; ++i) v[i] = swap_add32(v[i], v[i + 16]);
#pragma unroll
      for (int i = 0; i < 8; ++i) {
        v[i] = swap_add16(v[i], v[i + 8]);
        v[i] += dppf<0xB1>(0.f, v[i]);
        v[i] += dppf<0x4E>(0.f, v[i]);
        v[i] += dppf<0x141>(0.f, v[i]);
      }
      float s0 = v[0], s1 = v[1], s2 = v[2], s3 = v[3], s4 = v[4], s5 = v[5], s6 = v[6], s7 = v[7];
      asm volatile("" : "+v"(s0), "+v"(s1), "+v"(s2), "+v"(s3), "+v"(s4), "+v"(s5), "+v"(s6), "+v"(s7));
      s0 = (l & 4) ? s4 : s0; s1 = (l & 4) ? s5 : s1; s2 = (l & 4) ? s6 : s2; s3 = (l & 4) ? s7 : s3;
      s0 = (l & 2) ? s2 : s0; s1 = (l & 2) ? s3 : s1;
      const float kv = 2.0f * ((l & 1) ? s1 : s0);
      khl = pk2(kv, 0.f);
      khl = pk2(kv, kv - lo16(khl));
      kr[2080 - 64 * ps - tl] = (u16)khl;
      kr[CKN + 2080 - 64 * ps - tl] = (u16)(khl >> 16);
      SST(ps);
      if (ps < 2) {
        if (ps == 0) {
          if (w < 2) {
            reinterpret_cast<v4u*>(Xr)[tid] = tx[0];
            (void)wait_n<2, 8>(FL + FL_F0 + ((blockIdx.x & 7) * 8 + 4 * w) * 32, 32, TMO);
            U_ROW(tid);
          }
          __syncthreads();
        }
        f32x4 a0t = {};
#pragma unroll
        for (int b = -1; b < 4; ++b) a0t = MFMA16(KFRAG(-32 * b), conv_bfrag(ub + 16 * b, ush), a0t);
        const float y0 = swap_add32(a0t[0], a0t[1]), y1 = swap_add32(a0t[2], a0t[3]);
        if ((g & 1) == ps) TILE_OUT(0);
        __syncthreads();
        if (w == ps) {
          CHUNK_ST(w);
          DRAIN();
          if (l < 16) set_flag(FL + FL_FC + (((2 * w + (l >> 3)) * 8 + (l & 7)) * 8 + (cv >> 3)) * 32 + (cv & 7));
        }
        SST(4 + ps);
      }
      if (ps == 2) {
        if (w >= 2) tx[0] = *reinterpret_cast<const v4u*>(A.x0 + (size_t)tid * DM + 8 * cv);
#pragma unroll
        for (int i = 1; i < 4; ++i) tx[i] = *reinterpret_cast<const v4u*>(A.x0 + (size_t)(tid + 512 * i) * DM + 8 * cv);
      }
    }
    if (w >= 2) reinterpret_cast<v4u*>(Xr)[tid] = tx[0];
#pragma unroll
    for (int i = 1; i < 4; ++i) reinterpret_cast<v4u*>(Xr)[tid + 512 * i] = tx[i];
    {
      unsigned spins = 0;
      while (!__all(__hip_atomic_load((gu32*)FS + (w + 8 * ((l >> 3) & 3)) * 32 + (l & 7), RLX_AGENT) != 0u)) {
        if (spin_timeout(++spins, TMO)) break;
        __builtin_amdgcn_s_sleep(4);
      }
      asm volatile("" ::: "memory");
    }
    if (w >= 2) U_ROW(tid);
#pragma unroll
    for (int i = 1; i < 4; ++i) U_ROW(tid + 512 * i);
    SST(6);
    (void)wait_n<8, 1>(FL + FL_KG + cv * 32 + (w >> 2), 1, TMO);
    SST(7);
    {
      const __amdgpu_buffer_rsrc_t rk = MKRS(ws + OFF_KF + (size_t)d * 2 * CKN * 2, 2 * CKN * 2);
      v4u t[8];
#pragma unroll
      for (int i = 0; i < 4; ++i) {
        t[i] = LD128(rk, (64 * i + l) * 16, 0);
        t[4 + i] = LD128(rk, (64 * i + l) * 16, CKN * 2);
      }
#pragma unroll
      for (int i = 0; i < 4; ++i)
        if (64 * i + l < 229) {
          reinterpret_cast<v4u*>(kr)[64 * i + l] = t[i];
          reinterpret_cast<v4u*>(kr + CKN)[64 * i + l] = t[4 + i];
        }
      kr[2080 - 192 - tl] = (u16)khl;
      kr[CKN + 2080 - 192 - tl] = (u16)(khl >> 16);
    }
    __syncthreads();
#define BULK(TA, TB, HOOK)                                                                      \
  {                                                                                            \
    const bf16x8 bm = conv_bfrag(ub - 16, ush);                                                \
    _Pragma("unroll") for (int T = (TA); T < (TB); ++T) acc[T] = MFMA16(KFRAG(128 * T + 32), bm, (f32x4{})); \
    _Pragma("unroll 1") for (int r = 0; r < 4; ++r) {                                          \
      bf16x8 bf[TB];                                                                           \
      _Pragma("unroll") for (int j = 0; j < (TB); ++j) bf[j] = conv_bfrag(ub + 16 * r + 64 * j, ush); \
      _Pragma("unroll") for (int e = 0; e < (TB); ++e) {                                       \
        const bf16x8 a = KFRAG(128 * e - 32 * r);                                              \
        _Pragma("unroll") for (int T = (e > (TA) ? e : (TA)); T < (TB); ++T) acc[T] = MFMA16(a, bf[T - e], acc[T]); \
      }                                                                                        \
      if (r == 0) { HOOK }                                                                     \
    }                                                                                          \
  }
    BULK(1, 4, );
#pragma unroll
    for (int T = 1; T < 4; ++T) {
      const float y0 = swap_add32(acc[T][0], acc[T][1]), y1 = swap_add32(acc[T][2], acc[T][3]);
      TILE_OUT(T);
    }
    __syncthreads();
    if (w < 6) CHUNK_ST(2 + w);
    SST(8);
    BULK(4, 7, if (w < 6) {
      DRAIN();
      if (l < 16) set_flag(FL + FL_FC + (((4 + 2 * w + (l >> 3)) * 8 + (l & 7)) * 8 + (cv >> 3)) * 32 + (cv & 7));
      if (w == 1) (void)(wait_n<SLP_V, 32>(FL + FL_FZ + (blockIdx.x & 7) * 64, 64, TMO));
    });
#pragma unroll
    for (int T = 4; T < 7; ++T) {
      const float y0 = swap_add32(acc[T][0], acc[T][1]), y1 = swap_add32(acc[T][2], acc[T][3]);
      TILE_OUT(T);
    }
    __syncthreads();
    if (w >= 2) {
      CHUNK_ST(6 + w);
#pragma unroll
      for (int i = 0; i < 3; ++i) xf[i] = *reinterpret_cast<const v4u*>(Xr + (64 * (8 + 3 * w + i) + l) * 8);
    }
    SST(10);
    {
      const bf16x8 bm = conv_bfrag(ub - 16, ush);
#pragma unroll
      for (int T = 7; T < 16; ++T) far[T - 7] = MFMA16(KFRAG(128 * T + 32), bm, (f32x4{}));
    }
#define FAR_HALF(r, E0, E1)                                                                     \
  {                                                                                            \
    bf16x8 bf[16];                                                                             \
    _Pragma("unroll") for (int j = (8 - (E1) > 0 ? 8 - (E1) : 0); j < 16 - (E0); ++j) {        \
      bf[j] = conv_bfrag(ub1 + 16 * (r) + 64 * j, ush);                                        \
      if (j == 7 && (E1) - (E0) > 8) __builtin_amdgcn_sched_barrier(0);                        \
    }                                                                                          \
    __builtin_amdgcn_sched_barrier(0);                                                         \
    _Pragma("unroll") for (int e = (E0); e < (E1); ++e) {                                      \
      const bf16x8 a = KFRAG(128 * e - 32 * (r));                                              \
      _Pragma("unroll") for (int T = (e > 7 ? e : 7); T < 16; ++T) far[T - 7] = MFMA16(a, bf[T - e], far[T - 7]); \
    }                                                                                          \
  }
#define FAR_PIECE(p)                                                                            \
  {                                                                                            \
    if ((p) < 8) {                                                                             \
      const int r_ = 2 + ((p) >> 2);                                                           \
      if (((p) & 3) == 0) FAR_HALF(r_, 0, 3) else if (((p) & 3) == 1) FAR_HALF(r_, 3, 6)       \
      else if (((p) & 3) == 2) FAR_HALF(r_, 6, 9) else FAR_HALF(r_, 9, 16)                     \
    } else if ((p) == 8) {                                                                     \
                     \
      u16* ucw = Uc + w * CUN + 32 + t0;                                                       \
      _Pragma("unroll") for (int T = 7; T < 16; ++T) {                                         \
        const float y0 = swap_add32(far[T - 7][0], far[T - 7][1]), y1 = swap_add32(far[T - 7][2], far[T - 7][3]); \
        const float u0 = bf2f(ucw[128 * T]), u1 = bf2f(ucw[128 * T + 32]);                     \
        const unsigned yy = pk2(y0, y1), uu = pk2(u0 * dsk, u1 * dsk);                         \
        const unsigned aa = pk2(lo16(yy) + lo16(uu), hi16(yy) + hi16(uu));                     \
        ucw[128 * T] = (u16)aa;                                                                \
        ucw[128 * T + 32] = (u16)(aa >> 16);                                                   \
      }                                                                                        \
    } else if (w >= 2) {                                                                       \
      int l = tid & 63;                                                                        \
      asm volatile("" : "+v"(l), "+v"(xf[0]), "+v"(xf[1]), "+v"(xf[2]));                       \
      if ((p) == 9) {                                                                          \
        _Pragma("unroll") for (int i = 0; i < 3; ++i) {                                        \
          const int ck = 8 + 3 * w + i;                                                        \
          const u16* ur = Uc + 32 + 64 * ck + l;                                               \
          v4u o;                                                                               \
          _Pragma("unroll") for (int e = 0; e < 4; ++e)                                        \
            o[e] = pk2(lo16(xf[i][e]) + bf2f(ur[(2 * e) * CUN]), hi16(xf[i][e]) + bf2f(ur[(2 * e + 1) * CUN])); \
          ST128(o, rz1, (l >> 5) * 32768 + (l & 31) * 16, (2 * ck * 64 + cv) * 512);           \
        }                                                                                      \
      } else {                                                                                 \
        DRAIN();                                                                               \
        if (l < 48) set_flag(FL + FL_FC + (((16 + 6 * w + (l >> 3)) * 8 + (l & 7)) * 8 + (cv >> 3)) * 32 + (cv & 7)); \
      }                                                                                        \
    }                                                                                          \
  }
#pragma unroll 1
    for (int p = 0; p < 2; ++p) {
      FAR_HALF(p, 0, 16);
      if (p == 0 && w >= 2) {
        DRAIN();
        if (l < 16) set_flag(FL + FL_FC + (((12 + 2 * w + (l >> 3)) * 8 + (l & 7)) * 8 + (cv >> 3)) * 32 + (cv & 7));
      }
    }
#undef BULK
    SST(9);
#undef TILE_OUT
#undef CHUNK_ST
#undef U_ROW
  }
  if (EXP_OFF & 4) {
    for (int p = 0; p < 11; ++p) {
      __syncthreads();
      FAR_PIECE(p);
    }
    return;
  }
  u16* uc = Uc + (8 + w) * CUN;
  const unsigned* ub = reinterpret_cast<const unsigned*>(uc + (c & ~1) + 8 * g + 32);
#pragma unroll
  for (int j = 0; j < 16; ++j) acc[j] = f32x4{};
  const __amdgpu_buffer_rsrc_t rx = MKRS(ws + OFF_X2, SZ_X), rz = MKRS(ws + OFF_X3, SZ_X);
  const __amdgpu_buffer_rsrc_t rf = MKRS(FL + FL_FR, 64 * 32 * 128);
  unsigned st = 0u;
  v2u xx = {0u, 0u};
#define X2_LOAD(ss, far)                                                                                                      \
  {                                                                                                                      \
    xx = LD64(rx, (l >> 1) * (DM * 2) + (l & 1) * 8, (far) ? (int)SZ_X : ((ss) * SEG * DM + 8 * cv) * 2);                 \
    st = __builtin_amdgcn_raw_buffer_load_b32(rf, (l >> 4) * 128 + ((l >> 1) & 7) * 4, ((ss) * 8 + (blockIdx.x & (NR_R - 1))) * 512, AUX_SC1); \
  }
  unsigned tl = now10();
  for (int s = 0; s < NSEG; ++s) {
    if (w == 1) {
      for (unsigned spins = 0; !__all(umin3(xx.x, xx.y, st) != 0u); ++spins) {
        if (spin_timeout(spins, TMO)) break;
        if (GATE_V && spins == 0 && !__any(st != 0u)) gate(tl);
        if (spins && !__all(st != 0u)) __builtin_amdgcn_s_sleep(SLP_V);
        X2_LOAD(s, __builtin_popcountll(__ballot(st != 0u)) < CVTHR);
      }
      tl = now10() + GATE_V;
      TS(0, s, 64);
      const float mb = lo16(st), ri = 1.0f / hi16(st);
      const unsigned t0 = pk2(lo16(xx.x) - mb, hi16(xx.x) - mb), t1 = pk2(lo16(xx.y) - mb, hi16(xx.y) - mb);
      *reinterpret_cast<v2u*>(Su + l * 4) = v2u{pk2(lo16(t0) * ri, hi16(t0) * ri), pk2(lo16(t1) * ri, hi16(t1) * ri)};
      *reinterpret_cast<v2u*>(Sx + l * 4) = xx;
    }
    const int q = s & 3;
    const u16* kq = ka + 32 * q;
    const bf16x8 a0 = *reinterpret_cast<const bf16x8*>(kq - 32), a1 = *reinterpret_cast<const bf16x8*>(kq);
    __syncthreads();
    TS(1, s, 0);
    if (s == 0)
      for (int x = l; x < CUN / 2; x += 64) reinterpret_cast<unsigned*>(uc)[x] = 0u;
    if (l < 32) uc[32 + 32 * s + l] = Su[l * 8 + w];
    asm volatile("s_waitcnt lgkmcnt(0)" ::: "memory");
    const bf16x8 bp = conv_bfrag(ub + 16 * (s - 1), ush);
    acc[0] = MFMA16(a0, bp, (q == 0 ? acc[1] : acc[0]));
    const f32x4 tmp = MFMA16(a1, conv_bfrag(ub + 16 * s, ush), acc[0]);
    float y0 = (q & 1) ? tmp[2] : tmp[0], y1 = (q & 1) ? tmp[3] : tmp[1];
    y0 = xsum32(y0);
    y1 = xsum32(y1);
    if (g == (q >> 1)) {
      const float u0 = bf2f(Su[c * 8 + w]), u1 = bf2f(Su[(16 + c) * 8 + w]);
      const float a0 = bfr(bfr(y0) + bfr(u0 * dsk)), a1 = bfr(bfr(y1) + bfr(u1 * dsk));
      To[c * 8 + w] = f2bf(bf2f(Sx[c * 8 + w]) + a0);
      To[(16 + c) * 8 + w] = f2bf(bf2f(Sx[(16 + c) * 8 + w]) + a1);
    }
    __syncthreads();
    if (w == 0 && l < 32) {
      const v4u o = *reinterpret_cast<const v4u*>(To + l * 8);
      ST128(o, rz, l * 16, (s * 64 + cv) * 512);
    }
    if (w == 1) X2_LOAD(s + 1, CVTHR > 0);
    const int left = 15 - (s >> 2);
#define PUSH(J0, SH)                                                                                              \
  if (left >= (J0)) {                                                                                             \
    _Pragma("unroll") for (int j = (J0); j < (J0) + 4 && j < 16; ++j)                                             \
      acc[j] = MFMA16(*reinterpret_cast<const bf16x8*>(kq - 32 - 128 * j), bp, (j + (SH) < 16 ? acc[(j + (SH)) & 15] : f32x4{})); \
  }
    TS(2, s, 0);
    if (w == 0) {
      DRAIN();
      if (l < NR_C) set_flag(FL + FL_FC + (((64 + s) * 8 + l) * 8 + (cv >> 3)) * 32 + (cv & 7));
    }
    TS(3, s, 0);
    if (q == 0) { PUSH(1, 1) PUSH(5, 1) PUSH(9, 1) PUSH(13, 1) }
    else { PUSH(1, 0) PUSH(5, 0) PUSH(9, 0) PUSH(13, 0) }
#undef PUSH
    MSTAMP();
    if (s >= CVPRE && s < 11 + CVPRE) FAR_PIECE(s - CVPRE);
  }
#undef KFRAG
#undef X2_LOAD
#undef FAR_PIECE
#undef FAR_HALF
}

__device__ __forceinline__ v4u ln_row_packed(const v4u xr) {
  float v[8];
#pragma unroll
  for (int e = 0; e < 4; ++e) { v[2 * e] = lo16(xr[e]); v[2 * e + 1] = hi16(xr[e]); }
  float s = ((v[0] + v[1]) + (v[2] + v[3])) + ((v[4] + v[5]) + (v[6] + v[7]));
  s = wave_sum(s);
  const float mean = s * (1.0f / DM);
  float q = 0.f;
#pragma unroll
  for (int j = 0; j < 8; ++j) { float t = v[j] - mean; q += t * t; }
  q = wave_sum(q);
  const float var = q * (1.0f / DM);
  const float mb = bfr(mean);
  const float sd = bfr(sqrtf(bfr(bfr(var) + 1e-5f)));
  const float ri = 1.0f / sd;
  v4u o;
#pragma unroll
  for (int e = 0; e < 4; ++e) {
    const unsigned t = pk2(v[2 * e] - mb, v[2 * e + 1] - mb);
    const float x0 = lo16(t), x1 = hi16(t);
    float q0 = x0 * ri, q1 = x1 * ri;
    q0 = __builtin_fmaf(__builtin_fmaf(-q0, sd, x0), ri, q0);
    q1 = __builtin_fmaf(__builtin_fmaf(-q1, sd, x1), ri, q1);
    o[e] = pk2(q0, q1);
  }
  return o;
}

__device__ __forceinline__ float bfr1(float x) { return lo16(pk2(x, x)); }
__device__ __forceinline__ void ln4_packed(v4u (&x)[4]) {
  f32x2_t v[16];
#pragma unroll
  for (int e = 0; e < 16; ++e) v[e] = f32x2_t{lo16(x[e >> 2][e & 3]), hi16(x[e >> 2][e & 3])};
  const f32x2_t t = (((v[0] + v[1]) + (v[2] + v[3])) + ((v[4] + v[5]) + (v[6] + v[7]))) +
                    (((v[8] + v[9]) + (v[10] + v[11])) + ((v[12] + v[13]) + (v[14] + v[15])));
  const float mean = red_mod4(t.x + t.y) * (1.0f / DM);
  const float mb = bfr1(mean);
  f32x2_t q0 = {0.f, 0.f}, q1 = {0.f, 0.f};
#pragma unroll
  for (int e = 0; e < 16; e += 2) {
    v[e] -= mb;
    v[e + 1] -= mb;
    q0 += v[e] * v[e];
    q1 += v[e + 1] * v[e + 1];
  }
  q0 += q1;
  const float dm = mean - mb;
  const float var = fmaxf(red_mod4(q0.x + q0.y) * (1.0f / DM) - dm * dm, 0.f);
  const float sd = bfr1(sqrtf(bfr1(bfr1(var) + 1e-5f)));
  const float ri = 1.0f / sd;
#pragma unroll
  for (int e = 0; e < 16; ++e) {
    const unsigned t2 = pk2(v[e].x, v[e].y);
    const f32x2_t q = f32x2_t{lo16(t2), hi16(t2)} * ri;
    x[e >> 2][e & 3] = pk2(q.x, q.y);
  }
}

#define Q_ASM \
  "s_mov_b32 s74, 0\n" \
  "s_mov_b32 s76, 0\n" \
  "s_mov_b32 s78, 0x8000\n" \
  "s_mov_b32 s79, 512\n" \
  "s_branch .LCQ_POLL_%=\n" \
  ".LCQ_TOP_%=:\n" \
  "s_barrier\n" \
  "s_sub_i32 s78, 0, s78\n" \
  "v_add_u32 v0, s78, v0\n" \
  "ds_read_b128 v[18:21], v0 offset:0\n" \
  "ds_read_b128 v[22:25], v0 offset:2048\n" \
  "ds_read_b128 v[26:29], v0 offset:4096\n" \
  "ds_read2_b32 v[6:7], v3 offset0:16 offset1:48\n" \
  "ds_read_b128 v[10:13], v4 offset:256\n" \
  "ds_read_b128 v[14:17], v4 offset:320\n" \
  "ds_read_b32 v8, v3 offset:384\n" \
  "s_sub_i32 s79, 0, s79\n" \
  "s_add_u32 s69, s69, s79\n" \
  "s_add_u32 s62, s62, 0x800\n" \
  "s_addc_u32 s63, s63, 0\n" \
  "s_add_u32 s71, s71, 0x2000\n" \
  "s_mov_b32 s73, s75\n" \
  "v_lshrrev_b32 v9, 1, v2\n" \
  "v_lshrrev_b32 v5, 7, v2\n" \
  "v_and_b32 v9, 0xf8, v9\n" \
  "v_and_or_b32 v9, v5, 4, v9\n" \
  "s_setprio 3\n" \
  "ds_read_b128 v[30:33], v0 offset:6144\n" \
  "s_waitcnt lgkmcnt(7)\n" \
  "v_mfma_f32_16x16x32_bf16 v[34:37], v[64:67], v[18:21], 0\n" \
  "v_mfma_f32_16x16x32_bf16 v[38:41], v[128:131], v[18:21], 0\n" \
  "v_mfma_f32_16x16x32_bf16 v[42:45], v[18:21], v[192:195], 0\n" \
  "s_cmp_eq_u32 s73, 0\n" \
  "s_cbranch_scc1 .LCQ_X1_%=\n" \
  "s_mov_b32 m0, s69\n" \
  "s_nop 0\n" \
  "global_load_lds_dword v9, s[62:63] sc1\n" \
  ".LCQ_X1_%=:\n" \
  "ds_read_b128 v[18:21], v0 offset:8192\n" \
  "s_waitcnt lgkmcnt(7)\n" \
  "v_mfma_f32_16x16x32_bf16 v[34:37], v[68:71], v[22:25], v[34:37]\n" \
  "s_waitcnt lgkmcnt(4)\n" \
  "v_fma_f32 v48, -v6, v10, v48\n" \
  "v_fma_f32 v49, -v6, v11, v49\n" \
  "v_mfma_f32_16x16x32_bf16 v[38:41], v[132:135], v[22:25], v[38:41]\n" \
  "v_fma_f32 v50, -v6, v12, v50\n" \
  "v_fma_f32 v51, -v6, v13, v51\n" \
  "v_mfma_f32_16x16x32_bf16 v[42:45], v[22:25], v[196:199], v[42:45]\n" \
  "ds_read_b128 v[10:13], v4 offset:64\n" \
  "s_waitcnt lgkmcnt(4)\n" \
  "v_fma_f32 v54, -v6, v14, v54\n" \
  "v_fma_f32 v55, -v6, v15, v55\n" \
  "s_cmp_eq_u32 s73, 0\n" \
  "s_cbranch_scc1 .LCQ_X2_%=\n" \
  "s_mov_b32 m0, s68\n" \
  "s_xor_b32 s68, s68, 0x8000\n" \
  "global_load_lds_dwordx4 v2, s[60:61] offset:0 sc1\n" \
  "global_load_lds_dwordx4 v2, s[60:61] offset:1024 sc1\n" \
  ".LCQ_X2_%=:\n" \
  "ds_read_b128 v[22:25], v0 offset:10240\n" \
  "v_mfma_f32_16x16x32_bf16 v[34:37], v[72:75], v[26:29], v[34:37]\n" \
  "v_fma_f32 v56, -v6, v16, v56\n" \
  "v_fma_f32 v57, -v6, v17, v57\n" \
  "v_mfma_f32_16x16x32_bf16 v[38:41], v[136:139], v[26:29], v[38:41]\n" \
  "ds_read_b128 v[14:17], v4 offset:192\n" \
  "v_mul_f32 v48, v48, v7\n" \
  "v_mul_f32 v49, v49, v7\n" \
  "v_mfma_f32_16x16x32_bf16 v[42:45], v[26:29], v[200:203], v[42:45]\n" \
  "v_mul_f32 v50, v50, v7\n" \
  "v_mul_f32 v51, v51, v7\n" \
  "s_cmp_eq_u32 s73, 0\n" \
  "s_cbranch_scc1 .LCQ_X3_%=\n" \
  "global_load_lds_dwordx4 v2, s[60:61] offset:2048 sc1\n" \
  "global_load_lds_dwordx4 v2, s[60:61] offset:3072 sc1\n" \
  "s_add_u32 s60, s60, 0x8000\n" \
  "s_addc_u32 s61, s61, 0\n" \
  ".LCQ_X3_%=:\n" \
  "ds_read_b128 v[26:29], v0 offset:12288\n" \
  "s_waitcnt lgkmcnt(5)\n" \
  "v_mfma_f32_16x16x32_bf16 v[34:37], v[76:79], v[30:33], v[34:37]\n" \
  "v_cvt_pk_bf16_f32 v48, v48, v49\n" \
  "v_cvt_pk_bf16_f32 v49, v50, v51\n" \
  "v_mfma_f32_16x16x32_bf16 v[38:41], v[140:143], v[30:33], v[38:41]\n" \
  "v_mul_f32 v54, v54, v7\n" \
  "v_mul_f32 v55, v55, v7\n" \
  "v_mfma_f32_16x16x32_bf16 v[42:45], v[30:33], v[204:207], v[42:45]\n" \
  "v_mul_f32 v56, v56, v7\n" \
  "v_mul_f32 v57, v57, v7\n" \
  "s_cmp_eq_u32 s73, 0\n" \
  "s_cbranch_scc1 .LCQ_X4_%=\n" \
  "s_add_u32 s83, s71, 0x2000\n" \
  "v_bfe_u32 v5, v2, 2, 5\n" \
  "buffer_load_dword v5, v5, s[52:55], s83 offen sc1\n" \
  ".LCQ_X4_%=:\n" \
  "s_cmp_eq_u32 s74, 0\n" \
  "s_cbranch_scc1 .LCQ_NPA_%=\n" \
  "s_waitcnt vmcnt(6)\n" \
  "s_cmp_lg_u32 s73, 0\n" \
  "s_cbranch_scc1 .LCQ_PA_%=\n" \
  "s_waitcnt vmcnt(0)\n" \
  ".LCQ_PA_%=:\n" \
  "v_mov_b32 v46, s81\n" \
  "v_mov_b32 v47, 1\n" \
  "s_mov_b64 exec, 1\n" \
  "ds_add_rtn_u32 v46, v46, v47\n" \
  "s_mov_b64 exec, -1\n" \
  "s_waitcnt lgkmcnt(0)\n" \
  "v_readfirstlane_b32 s83, v46\n" \
  "s_mov_b32 s74, 0\n" \
  "s_and_b32 s83, s83, 7\n" \
  "s_cmp_eq_u32 s83, 7\n" \
  "s_cbranch_scc0 .LCQ_F5_%=\n" \
  "s_mov_b64 exec, 1\n" \
  "buffer_store_dword v47, off, s[56:59], s80 sc1\n" \
  "s_mov_b64 exec, -1\n" \
  ".LCQ_F5_%=:\n" \
  ".LCQ_NPA_%=:\n" \
  "ds_read_b128 v[30:33], v0 offset:14336\n" \
  "s_waitcnt lgkmcnt(5)\n" \
  "v_mfma_f32_16x16x32_bf16 v[34:37], v[80:83], v[18:21], v[34:37]\n" \
  "v_cvt_pk_bf16_f32 v54, v54, v55\n" \
  "v_cvt_pk_bf16_f32 v55, v56, v57\n" \
  "v_mfma_f32_16x16x32_bf16 v[38:41], v[144:147], v[18:21], v[38:41]\n" \
  "s_waitcnt lgkmcnt(4)\n" \
  "v_fma_f32 v60, -v10, v8, v60\n" \
  "v_fma_f32 v61, -v11, v8, v61\n" \
  "v_mfma_f32_16x16x32_bf16 v[42:45], v[18:21], v[208:211], v[42:45]\n" \
  "v_fma_f32 v62, -v12, v8, v62\n" \
  "v_fma_f32 v63, -v13, v8, v63\n" \
  "ds_read_b128 v[18:21], v0 offset:16384\n" \
  "s_waitcnt lgkmcnt(4)\n" \
  "v_mfma_f32_16x16x32_bf16 v[34:37], v[84:87], v[22:25], v[34:37]\n" \
  "v_permlane16_swap_b32 v46, v48\n" \
  "v_mfma_f32_16x16x32_bf16 v[38:41], v[148:151], v[22:25], v[38:41]\n" \
  "v_permlane16_swap_b32 v47, v49\n" \
  "v_mfma_f32_16x16x32_bf16 v[42:45], v[22:25], v[212:215], v[42:45]\n" \
  "v_permlane16_swap_b32 v52, v54\n" \
  "ds_read_b128 v[22:25], v0 offset:18432\n" \
  "s_waitcnt lgkmcnt(3)\n" \
  "v_mfma_f32_16x16x32_bf16 v[34:37], v[88:91], v[26:29], v[34:37]\n" \
  "v_permlane16_swap_b32 v53, v55\n" \
  "v_mfma_f32_16x16x32_bf16 v[38:41], v[152:155], v[26:29], v[38:41]\n" \
  "v_mul_f32 v60, v60, v14\n" \
  "v_mul_f32 v61, v61, v15\n" \
  "v_mfma_f32_16x16x32_bf16 v[42:45], v[26:29], v[216:219], v[42:45]\n" \
  "v_mul_f32 v62, v62, v16\n" \
  "v_mul_f32 v63, v63, v17\n" \
  "ds_read_b128 v[26:29], v0 offset:20480\n" \
  "s_waitcnt lgkmcnt(3)\n" \
  "v_mfma_f32_16x16x32_bf16 v[34:37], v[92:95], v[30:33], v[34:37]\n" \
  "v_cvt_pk_bf16_f32 v60, v60, v61\n" \
  "v_cvt_pk_bf16_f32 v61, v62, v63\n" \
  "v_mfma_f32_16x16x32_bf16 v[38:41], v[156:159], v[30:33], v[38:41]\n" \
  "v_mov_b64 v[10:11], 0\n" \
  "v_lshrrev_b32 v9, 1, v2\n" \
  "v_mfma_f32_16x16x32_bf16 v[42:45], v[30:33], v[220:223], v[42:45]\n" \
  "s_cmp_eq_u32 s76, 0\n" \
  "s_cbranch_scc1 .LCQ_NST_%=\n" \
  "s_add_u32 s83, s65, 0x2000\n" \
  "buffer_store_dwordx4 v[52:55], v1, s[40:43], s83 offen sc1\n" \
  "buffer_store_dwordx4 v[58:61], v2, s[44:47], s66 offen sc1\n" \
  "buffer_store_dwordx4 v[46:49], v1, s[40:43], s65 offen sc1\n" \
  "buffer_store_dwordx2 v[10:11], v9, s[48:51], s67 offen sc1\n" \
  "s_add_u32 s65, s65, 0x80000\n" \
  "s_add_u32 s66, s66, 0x40000\n" \
  "s_add_u32 s67, s67, 0x10000\n" \
  "s_mov_b32 s80, s72\n" \
  "s_add_u32 s72, s72, 0x1000\n" \
  "s_mov_b32 s81, s70\n" \
  "s_xor_b32 s70, s70, 4\n" \
  "s_mov_b32 s74, 1\n" \
  ".LCQ_NST_%=:\n" \
  "s_setprio 2\n" \
  "ds_read_b128 v[30:33], v0 offset:22528\n" \
  "s_waitcnt lgkmcnt(3)\n" \
  "v_mfma_f32_16x16x32_bf16 v[34:37], v[96:99], v[18:21], v[34:37]\n" \
  "v_mfma_f32_16x16x32_bf16 v[38:41], v[160:163], v[18:21], v[38:41]\n" \
  "v_mfma_f32_16x16x32_bf16 v[42:45], v[18:21], v[224:227], v[42:45]\n" \
  "ds_read_b128 v[18:21], v0 offset:24576\n" \
  "s_waitcnt lgkmcnt(3)\n" \
  "v_mfma_f32_16x16x32_bf16 v[34:37], v[100:103], v[22:25], v[34:37]\n" \
  "v_lshrrev_b32 v10, 1, v2\n" \
  "v_lshrrev_b32 v11, 7, v2\n" \
  "v_mfma_f32_16x16x32_bf16 v[38:41], v[164:167], v[22:25], v[38:41]\n" \
  "v_and_b32 v10, 0xf8, v10\n" \
  "v_and_or_b32 v10, v11, 4, v10\n" \
  "v_mfma_f32_16x16x32_bf16 v[42:45], v[22:25], v[228:231], v[42:45]\n" \
  "v_add_u32 v3, s79, v3\n" \
  "v_add_u32 v4, s79, v4\n" \
  "s_cmp_lg_u32 s73, 0\n" \
  "s_cbranch_scc1 .LCQ_NSD_%=\n" \
  "s_mov_b32 m0, s69\n" \
  "s_nop 0\n" \
  "global_load_lds_dword v10, s[62:63] sc1\n" \
  ".LCQ_NSD_%=:\n" \
  "ds_read_b128 v[22:25], v0 offset:26624\n" \
  "s_waitcnt lgkmcnt(3)\n" \
  "v_mfma_f32_16x16x32_bf16 v[34:37], v[104:107], v[26:29], v[34:37]\n" \
  "v_mfma_f32_16x16x32_bf16 v[38:41], v[168:171], v[26:29], v[38:41]\n" \
  "v_mfma_f32_16x16x32_bf16 v[42:45], v[26:29], v[232:235], v[42:45]\n" \
  "ds_read_b128 v[26:29], v0 offset:28672\n" \
  "s_waitcnt lgkmcnt(3)\n" \
  "v_mfma_f32_16x16x32_bf16 v[34:37], v[108:111], v[30:33], v[34:37]\n" \
  "v_mfma_f32_16x16x32_bf16 v[38:41], v[172:175], v[30:33], v[38:41]\n" \
  "v_mfma_f32_16x16x32_bf16 v[42:45], v[30:33], v[236:239], v[42:45]\n" \
  "ds_read_b128 v[30:33], v0 offset:30720\n" \
  "s_waitcnt lgkmcnt(3)\n" \
  "v_mfma_f32_16x16x32_bf16 v[34:37], v[112:115], v[18:21], v[34:37]\n" \
  "v_mfma_f32_16x16x32_bf16 v[38:41], v[176:179], v[18:21], v[38:41]\n" \
  "v_mfma_f32_16x16x32_bf16 v[42:45], v[18:21], v[240:243], v[42:45]\n" \
  "ds_read_b128 v[18:21], v0 offset:256\n" \
  "s_waitcnt lgkmcnt(3)\n" \
  "v_mfma_f32_16x16x32_bf16 v[34:37], v[116:119], v[22:25], v[34:37]\n" \
  "v_mfma_f32_16x16x32_bf16 v[38:41], v[180:183], v[22:25], v[38:41]\n" \
  "v_mfma_f32_16x16x32_bf16 v[42:45], v[22:25], v[244:247], v[42:45]\n" \
  "ds_read_b128 v[22:25], v0 offset:2304\n" \
  "s_waitcnt lgkmcnt(3)\n" \
  "v_mfma_f32_16x16x32_bf16 v[34:37], v[120:123], v[26:29], v[34:37]\n" \
  "v_mfma_f32_16x16x32_bf16 v[38:41], v[184:187], v[26:29], v[38:41]\n" \
  "v_mfma_f32_16x16x32_bf16 v[42:45], v[26:29], v[248:251], v[42:45]\n" \
  "ds_read_b128 v[26:29], v0 offset:4352\n" \
  "s_waitcnt lgkmcnt(3)\n" \
  "v_mfma_f32_16x16x32_bf16 v[34:37], v[124:127], v[30:33], v[34:37]\n" \
  "v_mfma_f32_16x16x32_bf16 v[38:41], v[188:191], v[30:33], v[38:41]\n" \
  "v_mfma_f32_16x16x32_bf16 v[42:45], v[30:33], v[252:255], v[42:45]\n" \
  "s_setprio 1\n" \
  "ds_read_b128 v[30:33], v0 offset:6400\n" \
  "s_waitcnt lgkmcnt(3)\n" \
  "v_mfma_f32_16x16x32_bf16 v[48:51], v[64:67], v[18:21], 0\n" \
  "v_mfma_f32_16x16x32_bf16 v[54:57], v[128:131], v[18:21], 0\n" \
  "v_mfma_f32_16x16x32_bf16 v[60:63], v[18:21], v[192:195], 0\n" \
  "ds_read_b128 v[18:21], v0 offset:8448\n" \
  "s_waitcnt lgkmcnt(3)\n" \
  "v_mfma_f32_16x16x32_bf16 v[48:51], v[68:71], v[22:25], v[48:51]\n" \
  "v_mfma_f32_16x16x32_bf16 v[54:57], v[132:135], v[22:25], v[54:57]\n" \
  "v_mfma_f32_16x16x32_bf16 v[60:63], v[22:25], v[196:199], v[60:63]\n" \
  "ds_read_b128 v[22:25], v0 offset:10496\n" \
  "s_waitcnt lgkmcnt(3)\n" \
  "v_mfma_f32_16x16x32_bf16 v[48:51], v[72:75], v[26:29], v[48:51]\n" \
  "v_mfma_f32_16x16x32_bf16 v[54:57], v[136:139], v[26:29], v[54:57]\n" \
  "v_mfma_f32_16x16x32_bf16 v[60:63], v[26:29], v[200:203], v[60:63]\n" \
  "v_lshrrev_b32 v9, 2, v2\n" \
  "v_add_u32 v9, s69, v9\n" \
  "s_waitcnt vmcnt(4)\n" \
  "s_and_b32 s76, s76, s73\n" \
  "s_cbranch_scc1 .LCQ_NPB_%=\n" \
  "s_waitcnt vmcnt(0)\n" \
  "s_cmp_eq_u32 s74, 0\n" \
  "s_cbranch_scc1 .LCQ_PB_%=\n" \
  "v_mov_b32 v10, s81\n" \
  "v_mov_b32 v11, 1\n" \
  "s_mov_b64 exec, 1\n" \
  "ds_add_rtn_u32 v10, v10, v11\n" \
  "s_mov_b64 exec, -1\n" \
  ".LCQ_PB_%=:\n" \
  "s_cmp_lg_u32 s73, 0\n" \
  "s_cbranch_scc1 .LCQ_NPB_%=\n" \
  "v_bfe_u32 v5, v2, 2, 5\n" \
  "buffer_load_dword v5, v5, s[52:55], s71 offen sc1\n" \
  ".LCQ_NPB_%=:\n" \
  "ds_read_b32 v9, v9\n" \
  "ds_read_b128 v[26:29], v0 offset:12544\n" \
  "s_waitcnt lgkmcnt(4)\n" \
  "v_mfma_f32_16x16x32_bf16 v[48:51], v[76:79], v[30:33], v[48:51]\n" \
  "v_mfma_f32_16x16x32_bf16 v[54:57], v[140:143], v[30:33], v[54:57]\n" \
  "v_mfma_f32_16x16x32_bf16 v[60:63], v[30:33], v[204:207], v[60:63]\n" \
  "ds_read_b128 v[30:33], v0 offset:14592\n" \
  "s_waitcnt lgkmcnt(4)\n" \
  "v_mfma_f32_16x16x32_bf16 v[48:51], v[80:83], v[18:21], v[48:51]\n" \
  "v_mfma_f32_16x16x32_bf16 v[54:57], v[144:147], v[18:21], v[54:57]\n" \
  "v_mfma_f32_16x16x32_bf16 v[60:63], v[18:21], v[208:211], v[60:63]\n" \
  "s_waitcnt lgkmcnt(2)\n" \
  "s_andn2_b32 s83, s74, s76\n" \
  "s_cbranch_scc0 .LCQ_NPC_%=\n" \
  "v_readfirstlane_b32 s83, v10\n" \
  "s_mov_b32 s74, 0\n" \
  "s_and_b32 s83, s83, 7\n" \
  "s_cmp_eq_u32 s83, 7\n" \
  "s_cbranch_scc0 .LCQ_F6_%=\n" \
  "s_mov_b64 exec, 1\n" \
  "buffer_store_dword v11, off, s[56:59], s80 sc1\n" \
  "s_mov_b64 exec, -1\n" \
  ".LCQ_F6_%=:\n" \
  ".LCQ_NPC_%=:\n" \
  "v_cmp_eq_u32 vcc, 0, v9\n" \
  "s_cmp_eq_u64 vcc, 0\n" \
  "s_cselect_b32 s77, 1, 0\n" \
  "s_cbranch_scc0 .LCQ_NOE_%=\n" \
  "ds_read2_b32 v[6:7], v3 offset0:0 offset1:32\n" \
  "ds_read_b128 v[10:13], v4 offset:256\n" \
  "ds_read_b128 v[14:17], v4 offset:320\n" \
  "ds_read_b32 v8, v3 offset:384\n" \
  "ds_read_b128 v[18:21], v0 offset:16640\n" \
  "v_mfma_f32_16x16x32_bf16 v[48:51], v[84:87], v[22:25], v[48:51]\n" \
  "v_mfma_f32_16x16x32_bf16 v[54:57], v[148:151], v[22:25], v[54:57]\n" \
  "v_mfma_f32_16x16x32_bf16 v[60:63], v[22:25], v[212:215], v[60:63]\n" \
  "ds_read_b128 v[22:25], v0 offset:18688\n" \
  "s_waitcnt lgkmcnt(7)\n" \
  "v_mfma_f32_16x16x32_bf16 v[48:51], v[88:91], v[26:29], v[48:51]\n" \
  "s_waitcnt lgkmcnt(4)\n" \
  "v_fma_f32 v34, -v6, v10, v34\n" \
  "v_fma_f32 v35, -v6, v11, v35\n" \
  "v_mfma_f32_16x16x32_bf16 v[54:57], v[152:155], v[26:29], v[54:57]\n" \
  "v_fma_f32 v36, -v6, v12, v36\n" \
  "v_fma_f32 v37, -v6, v13, v37\n" \
  "v_mfma_f32_16x16x32_bf16 v[60:63], v[26:29], v[216:219], v[60:63]\n" \
  "ds_read_b128 v[10:13], v4 offset:0\n" \
  "s_waitcnt lgkmcnt(4)\n" \
  "v_fma_f32 v38, -v6, v14, v38\n" \
  "v_fma_f32 v39, -v6, v15, v39\n" \
  "ds_read_b128 v[26:29], v0 offset:20736\n" \
  "v_mfma_f32_16x16x32_bf16 v[48:51], v[92:95], v[30:33], v[48:51]\n" \
  "v_fma_f32 v40, -v6, v16, v40\n" \
  "v_fma_f32 v41, -v6, v17, v41\n" \
  "v_mfma_f32_16x16x32_bf16 v[54:57], v[156:159], v[30:33], v[54:57]\n" \
  "ds_read_b128 v[14:17], v4 offset:128\n" \
  "v_mul_f32 v34, v34, v7\n" \
  "v_mul_f32 v35, v35, v7\n" \
  "v_mfma_f32_16x16x32_bf16 v[60:63], v[30:33], v[220:223], v[60:63]\n" \
  "v_mul_f32 v36, v36, v7\n" \
  "v_mul_f32 v37, v37, v7\n" \
  "s_setprio 0\n" \
  "ds_read_b128 v[30:33], v0 offset:22784\n" \
  "s_waitcnt lgkmcnt(5)\n" \
  "v_mfma_f32_16x16x32_bf16 v[48:51], v[96:99], v[18:21], v[48:51]\n" \
  "v_cvt_pk_bf16_f32 v46, v34, v35\n" \
  "v_cvt_pk_bf16_f32 v47, v36, v37\n" \
  "v_mfma_f32_16x16x32_bf16 v[54:57], v[160:163], v[18:21], v[54:57]\n" \
  "v_mul_f32 v38, v38, v7\n" \
  "v_mul_f32 v39, v39, v7\n" \
  "v_mfma_f32_16x16x32_bf16 v[60:63], v[18:21], v[224:227], v[60:63]\n" \
  "v_mul_f32 v40, v40, v7\n" \
  "v_mul_f32 v41, v41, v7\n" \
  "ds_read_b128 v[18:21], v0 offset:24832\n" \
  "s_waitcnt lgkmcnt(5)\n" \
  "v_mfma_f32_16x16x32_bf16 v[48:51], v[100:103], v[22:25], v[48:51]\n" \
  "v_cvt_pk_bf16_f32 v52, v38, v39\n" \
  "v_cvt_pk_bf16_f32 v53, v40, v41\n" \
  "v_mfma_f32_16x16x32_bf16 v[54:57], v[164:167], v[22:25], v[54:57]\n" \
  "s_waitcnt lgkmcnt(4)\n" \
  "v_fma_f32 v42, -v10, v8, v42\n" \
  "v_fma_f32 v43, -v11, v8, v43\n" \
  "v_mfma_f32_16x16x32_bf16 v[60:63], v[22:25], v[228:231], v[60:63]\n" \
  "v_fma_f32 v44, -v12, v8, v44\n" \
  "v_fma_f32 v45, -v13, v8, v45\n" \
  "ds_read_b128 v[22:25], v0 offset:26880\n" \
  "s_waitcnt lgkmcnt(4)\n" \
  "v_mfma_f32_16x16x32_bf16 v[48:51], v[104:107], v[26:29], v[48:51]\n" \
  "s_waitcnt lgkmcnt(3)\n" \
  "v_mul_f32 v42, v42, v14\n" \
  "v_mul_f32 v43, v43, v15\n" \
  "v_mfma_f32_16x16x32_bf16 v[54:57], v[168:171], v[26:29], v[54:57]\n" \
  "v_mul_f32 v44, v44, v16\n" \
  "v_mul_f32 v45, v45, v17\n" \
  "v_mfma_f32_16x16x32_bf16 v[60:63], v[26:29], v[232:235], v[60:63]\n" \
  "v_cvt_pk_bf16_f32 v58, v42, v43\n" \
  "v_cvt_pk_bf16_f32 v59, v44, v45\n" \
  "ds_read_b128 v[26:29], v0 offset:28928\n" \
  "s_waitcnt lgkmcnt(3)\n" \
  "v_mfma_f32_16x16x32_bf16 v[48:51], v[108:111], v[30:33], v[48:51]\n" \
  "v_mfma_f32_16x16x32_bf16 v[54:57], v[172:175], v[30:33], v[54:57]\n" \
  "v_mfma_f32_16x16x32_bf16 v[60:63], v[30:33], v[236:239], v[60:63]\n" \
  "ds_read_b128 v[30:33], v0 offset:30976\n" \
  "s_waitcnt lgkmcnt(3)\n" \
  "v_mfma_f32_16x16x32_bf16 v[48:51], v[112:115], v[18:21], v[48:51]\n" \
  "v_mfma_f32_16x16x32_bf16 v[54:57], v[176:179], v[18:21], v[54:57]\n" \
  "v_mfma_f32_16x16x32_bf16 v[60:63], v[18:21], v[240:243], v[60:63]\n" \
  "s_waitcnt vmcnt(0)\n" \
  "s_cmp_eq_u32 s74, 0\n" \
  "s_cbranch_scc1 .LCQ_G7_%=\n" \
  "v_mov_b32 v6, s81\n" \
  "v_mov_b32 v7, 1\n" \
  "s_mov_b64 exec, 1\n" \
  "ds_add_rtn_u32 v6, v6, v7\n" \
  "s_mov_b64 exec, -1\n" \
  ".LCQ_G7_%=:\n" \
  "s_waitcnt lgkmcnt(2)\n" \
  "v_mfma_f32_16x16x32_bf16 v[48:51], v[116:119], v[22:25], v[48:51]\n" \
  "v_mfma_f32_16x16x32_bf16 v[54:57], v[180:183], v[22:25], v[54:57]\n" \
  "v_mfma_f32_16x16x32_bf16 v[60:63], v[22:25], v[244:247], v[60:63]\n" \
  "s_waitcnt lgkmcnt(1)\n" \
  "v_mfma_f32_16x16x32_bf16 v[48:51], v[120:123], v[26:29], v[48:51]\n" \
  "v_mfma_f32_16x16x32_bf16 v[54:57], v[184:187], v[26:29], v[54:57]\n" \
  "v_mfma_f32_16x16x32_bf16 v[60:63], v[26:29], v[248:251], v[60:63]\n" \
  "s_waitcnt lgkmcnt(0)\n" \
  "v_mfma_f32_16x16x32_bf16 v[48:51], v[124:127], v[30:33], v[48:51]\n" \
  "v_mfma_f32_16x16x32_bf16 v[54:57], v[188:191], v[30:33], v[54:57]\n" \
  "v_mfma_f32_16x16x32_bf16 v[60:63], v[30:33], v[252:255], v[60:63]\n" \
  "s_branch .LCQ_END2_%=\n" \
  ".LCQ_NOE_%=:\n" \
  "ds_read_b128 v[18:21], v0 offset:16640\n" \
  "v_mfma_f32_16x16x32_bf16 v[48:51], v[84:87], v[22:25], v[48:51]\n" \
  "v_mfma_f32_16x16x32_bf16 v[54:57], v[148:151], v[22:25], v[54:57]\n" \
  "v_mfma_f32_16x16x32_bf16 v[60:63], v[22:25], v[212:215], v[60:63]\n" \
  "ds_read_b128 v[22:25], v0 offset:18688\n" \
  "s_waitcnt lgkmcnt(3)\n" \
  "v_mfma_f32_16x16x32_bf16 v[48:51], v[88:91], v[26:29], v[48:51]\n" \
  "v_mfma_f32_16x16x32_bf16 v[54:57], v[152:155], v[26:29], v[54:57]\n" \
  "v_mfma_f32_16x16x32_bf16 v[60:63], v[26:29], v[216:219], v[60:63]\n" \
  "ds_read_b128 v[26:29], v0 offset:20736\n" \
  "s_waitcnt lgkmcnt(3)\n" \
  "v_mfma_f32_16x16x32_bf16 v[48:51], v[92:95], v[30:33], v[48:51]\n" \
  "v_mfma_f32_16x16x32_bf16 v[54:57], v[156:159], v[30:33], v[54:57]\n" \
  "v_mfma_f32_16x16x32_bf16 v[60:63], v[30:33], v[220:223], v[60:63]\n" \
  "s_setprio 0\n" \
  "ds_read_b128 v[30:33], v0 offset:22784\n" \
  "s_waitcnt lgkmcnt(3)\n" \
  "v_mfma_f32_16x16x32_bf16 v[48:51], v[96:99], v[18:21], v[48:51]\n" \
  "v_mfma_f32_16x16x32_bf16 v[54:57], v[160:163], v[18:21], v[54:57]\n" \
  "v_mfma_f32_16x16x32_bf16 v[60:63], v[18:21], v[224:227], v[60:63]\n" \
  "ds_read_b128 v[18:21], v0 offset:24832\n" \
  "s_waitcnt lgkmcnt(3)\n" \
  "v_mfma_f32_16x16x32_bf16 v[48:51], v[100:103], v[22:25], v[48:51]\n" \
  "v_mfma_f32_16x16x32_bf16 v[54:57], v[164:167], v[22:25], v[54:57]\n" \
  "v_mfma_f32_16x16x32_bf16 v[60:63], v[22:25], v[228:231], v[60:63]\n" \
  "ds_read_b128 v[22:25], v0 offset:26880\n" \
  "s_waitcnt lgkmcnt(3)\n" \
  "v_mfma_f32_16x16x32_bf16 v[48:51], v[104:107], v[26:29], v[48:51]\n" \
  "v_mfma_f32_16x16x32_bf16 v[54:57], v[168:171], v[26:29], v[54:57]\n" \
  "v_mfma_f32_16x16x32_bf16 v[60:63], v[26:29], v[232:235], v[60:63]\n" \
  "ds_read_b128 v[26:29], v0 offset:28928\n" \
  "s_waitcnt lgkmcnt(3)\n" \
  "v_mfma_f32_16x16x32_bf16 v[48:51], v[108:111], v[30:33], v[48:51]\n" \
  "v_mfma_f32_16x16x32_bf16 v[54:57], v[172:175], v[30:33], v[54:57]\n" \
  "v_mfma_f32_16x16x32_bf16 v[60:63], v[30:33], v[236:239], v[60:63]\n" \
  "ds_read_b128 v[30:33], v0 offset:30976\n" \
  "s_waitcnt lgkmcnt(3)\n" \
  "v_mfma_f32_16x16x32_bf16 v[48:51], v[112:115], v[18:21], v[48:51]\n" \
  "v_mfma_f32_16x16x32_bf16 v[54:57], v[176:179], v[18:21], v[54:57]\n" \
  "v_mfma_f32_16x16x32_bf16 v[60:63], v[18:21], v[240:243], v[60:63]\n" \
  "s_waitcnt vmcnt(0)\n" \
  "s_cmp_eq_u32 s74, 0\n" \
  "s_cbranch_scc1 .LCQ_G8_%=\n" \
  "v_mov_b32 v6, s81\n" \
  "v_mov_b32 v7, 1\n" \
  "s_mov_b64 exec, 1\n" \
  "ds_add_rtn_u32 v6, v6, v7\n" \
  "s_mov_b64 exec, -1\n" \
  ".LCQ_G8_%=:\n" \
  "s_waitcnt lgkmcnt(2)\n" \
  "v_mfma_f32_16x16x32_bf16 v[48:51], v[116:119], v[22:25], v[48:51]\n" \
  "v_mfma_f32_16x16x32_bf16 v[54:57], v[180:183], v[22:25], v[54:57]\n" \
  "v_mfma_f32_16x16x32_bf16 v[60:63], v[22:25], v[244:247], v[60:63]\n" \
  "s_waitcnt lgkmcnt(1)\n" \
  "v_mfma_f32_16x16x32_bf16 v[48:51], v[120:123], v[26:29], v[48:51]\n" \
  "v_mfma_f32_16x16x32_bf16 v[54:57], v[184:187], v[26:29], v[54:57]\n" \
  "v_mfma_f32_16x16x32_bf16 v[60:63], v[26:29], v[248:251], v[60:63]\n" \
  "s_waitcnt lgkmcnt(0)\n" \
  "v_mfma_f32_16x16x32_bf16 v[48:51], v[124:127], v[30:33], v[48:51]\n" \
  "v_mfma_f32_16x16x32_bf16 v[54:57], v[188:191], v[30:33], v[54:57]\n" \
  "v_mfma_f32_16x16x32_bf16 v[60:63], v[30:33], v[252:255], v[60:63]\n" \
  ".LCQ_END2_%=:\n" \
  "s_cmp_eq_u32 s74, 0\n" \
  "s_cbranch_scc1 .LCQ_NPE_%=\n" \
  "s_waitcnt lgkmcnt(0)\n" \
  "v_readfirstlane_b32 s83, v6\n" \
  "s_mov_b32 s74, 0\n" \
  "s_and_b32 s83, s83, 7\n" \
  "s_cmp_eq_u32 s83, 7\n" \
  "s_cbranch_scc0 .LCQ_F9_%=\n" \
  "s_mov_b64 exec, 1\n" \
  "buffer_store_dword v7, off, s[56:59], s80 sc1\n" \
  "s_mov_b64 exec, -1\n" \
  ".LCQ_F9_%=:\n" \
  ".LCQ_NPE_%=:\n" \
  "v_cmp_eq_u32 vcc, 0, v5\n" \
  "s_cmp_eq_u64 vcc, 0\n" \
  "s_cselect_b32 s75, 1, 0\n" \
  "s_sub_u32 s64, s64, 1\n" \
  "s_and_b32 s76, s73, s77\n" \
  "s_cbranch_scc1 .LCQ_TOP_%=\n" \
  "s_andn2_b32 s84, s75, s73\n" \
  "s_cbranch_scc0 .LCQ_TA_%=\n" \
  "s_mov_b32 m0, s68\n" \
  "s_xor_b32 s68, s68, 0x8000\n" \
  "global_load_lds_dwordx4 v2, s[60:61] offset:0 sc1\n" \
  "global_load_lds_dwordx4 v2, s[60:61] offset:1024 sc1\n" \
  "global_load_lds_dwordx4 v2, s[60:61] offset:2048 sc1\n" \
  "global_load_lds_dwordx4 v2, s[60:61] offset:3072 sc1\n" \
  "s_add_u32 s60, s60, 0x8000\n" \
  "s_addc_u32 s61, s61, 0\n" \
  "s_add_u32 s83, s71, 0x2000\n" \
  "v_bfe_u32 v5, v2, 2, 5\n" \
  "buffer_load_dword v5, v5, s[52:55], s83 offen sc1\n" \
  "s_mov_b32 s73, 1\n" \
  ".LCQ_TA_%=:\n" \
  "s_cmp_lg_u32 s77, 0\n" \
  "s_cbranch_scc1 .LCQ_TB_%=\n" \
  "s_mov_b32 s82, 0x40000\n" \
  "v_lshrrev_b32 v10, 1, v2\n" \
  "v_lshrrev_b32 v11, 7, v2\n" \
  "v_and_b32 v10, 0xf8, v10\n" \
  "v_and_or_b32 v10, v11, 4, v10\n" \
  "s_branch .LCQ_SW_%=\n" \
  ".LCQ_SV_%=:\n" \
  "s_sleep 8\n" \
  ".LCQ_SW_%=:\n" \
  "s_mov_b32 m0, s69\n" \
  "s_nop 0\n" \
  "global_load_lds_dword v10, s[62:63] sc1\n" \
  "v_lshrrev_b32 v9, 2, v2\n" \
  "v_add_u32 v9, s69, v9\n" \
  "s_sub_u32 s82, s82, 1\n" \
  "s_cbranch_scc1 .LCQ_END_%=\n" \
  "s_waitcnt vmcnt(0)\n" \
  "ds_read_b32 v9, v9\n" \
  "s_waitcnt lgkmcnt(0)\n" \
  "v_cmp_eq_u32 vcc, 0, v9\n" \
  "s_cmp_eq_u64 vcc, 0\n" \
  "s_cbranch_scc0 .LCQ_SV_%=\n" \
  "ds_read2_b32 v[6:7], v3 offset0:0 offset1:32\n" \
  "ds_read_b128 v[10:13], v4 offset:256\n" \
  "ds_read_b128 v[14:17], v4 offset:320\n" \
  "ds_read_b32 v8, v3 offset:384\n" \
  "s_waitcnt lgkmcnt(2)\n" \
  "v_fma_f32 v34, -v6, v10, v34\n" \
  "v_fma_f32 v35, -v6, v11, v35\n" \
  "v_fma_f32 v36, -v6, v12, v36\n" \
  "v_fma_f32 v37, -v6, v13, v37\n" \
  "ds_read_b128 v[10:13], v4 offset:0\n" \
  "s_waitcnt lgkmcnt(2)\n" \
  "v_fma_f32 v38, -v6, v14, v38\n" \
  "v_fma_f32 v39, -v6, v15, v39\n" \
  "v_fma_f32 v40, -v6, v16, v40\n" \
  "v_fma_f32 v41, -v6, v17, v41\n" \
  "ds_read_b128 v[14:17], v4 offset:128\n" \
  "v_mul_f32 v34, v34, v7\n" \
  "v_mul_f32 v35, v35, v7\n" \
  "v_mul_f32 v36, v36, v7\n" \
  "v_mul_f32 v37, v37, v7\n" \
  "v_cvt_pk_bf16_f32 v46, v34, v35\n" \
  "v_cvt_pk_bf16_f32 v47, v36, v37\n" \
  "v_mul_f32 v38, v38, v7\n" \
  "v_mul_f32 v39, v39, v7\n" \
  "v_mul_f32 v40, v40, v7\n" \
  "v_mul_f32 v41, v41, v7\n" \
  "v_cvt_pk_bf16_f32 v52, v38, v39\n" \
  "v_cvt_pk_bf16_f32 v53, v40, v41\n" \
  "s_waitcnt lgkmcnt(1)\n" \
  "v_fma_f32 v42, -v10, v8, v42\n" \
  "v_fma_f32 v43, -v11, v8, v43\n" \
  "v_fma_f32 v44, -v12, v8, v44\n" \
  "v_fma_f32 v45, -v13, v8, v45\n" \
  "s_waitcnt lgkmcnt(0)\n" \
  "v_mul_f32 v42, v42, v14\n" \
  "v_mul_f32 v43, v43, v15\n" \
  "v_mul_f32 v44, v44, v16\n" \
  "v_mul_f32 v45, v45, v17\n" \
  "v_cvt_pk_bf16_f32 v58, v42, v43\n" \
  "v_cvt_pk_bf16_f32 v59, v44, v45\n" \
  ".LCQ_TB_%=:\n" \
  "ds_read2_b32 v[6:7], v3 offset0:16 offset1:48\n" \
  "ds_read_b128 v[10:13], v4 offset:256\n" \
  "ds_read_b128 v[14:17], v4 offset:320\n" \
  "ds_read_b32 v8, v3 offset:384\n" \
  "s_waitcnt lgkmcnt(2)\n" \
  "v_fma_f32 v48, -v6, v10, v48\n" \
  "v_fma_f32 v49, -v6, v11, v49\n" \
  "v_fma_f32 v50, -v6, v12, v50\n" \
  "v_fma_f32 v51, -v6, v13, v51\n" \
  "ds_read_b128 v[10:13], v4 offset:64\n" \
  "s_waitcnt lgkmcnt(2)\n" \
  "v_fma_f32 v54, -v6, v14, v54\n" \
  "v_fma_f32 v55, -v6, v15, v55\n" \
  "v_fma_f32 v56, -v6, v16, v56\n" \
  "v_fma_f32 v57, -v6, v17, v57\n" \
  "ds_read_b128 v[14:17], v4 offset:192\n" \
  "v_mul_f32 v48, v48, v7\n" \
  "v_mul_f32 v49, v49, v7\n" \
  "v_mul_f32 v50, v50, v7\n" \
  "v_mul_f32 v51, v51, v7\n" \
  "v_cvt_pk_bf16_f32 v48, v48, v49\n" \
  "v_cvt_pk_bf16_f32 v49, v50, v51\n" \
  "v_mul_f32 v54, v54, v7\n" \
  "v_mul_f32 v55, v55, v7\n" \
  "v_mul_f32 v56, v56, v7\n" \
  "v_mul_f32 v57, v57, v7\n" \
  "v_cvt_pk_bf16_f32 v54, v54, v55\n" \
  "v_cvt_pk_bf16_f32 v55, v56, v57\n" \
  "s_waitcnt lgkmcnt(1)\n" \
  "v_fma_f32 v60, -v10, v8, v60\n" \
  "v_fma_f32 v61, -v11, v8, v61\n" \
  "v_fma_f32 v62, -v12, v8, v62\n" \
  "v_fma_f32 v63, -v13, v8, v63\n" \
  "v_permlane16_swap_b32 v46, v48\n" \
  "v_permlane16_swap_b32 v47, v49\n" \
  "v_permlane16_swap_b32 v52, v54\n" \
  "v_permlane16_swap_b32 v53, v55\n" \
  "s_waitcnt lgkmcnt(0)\n" \
  "v_mul_f32 v60, v60, v14\n" \
  "v_mul_f32 v61, v61, v15\n" \
  "v_mul_f32 v62, v62, v16\n" \
  "v_mul_f32 v63, v63, v17\n" \
  "v_cvt_pk_bf16_f32 v60, v60, v61\n" \
  "v_cvt_pk_bf16_f32 v61, v62, v63\n" \
  "v_mov_b64 v[10:11], 0\n" \
  "v_lshrrev_b32 v9, 1, v2\n" \
  "s_add_u32 s83, s65, 0x2000\n" \
  "buffer_store_dwordx4 v[52:55], v1, s[40:43], s83 offen sc1\n" \
  "buffer_store_dwordx4 v[58:61], v2, s[44:47], s66 offen sc1\n" \
  "buffer_store_dwordx4 v[46:49], v1, s[40:43], s65 offen sc1\n" \
  "buffer_store_dwordx2 v[10:11], v9, s[48:51], s67 offen sc1\n" \
  "s_add_u32 s65, s65, 0x80000\n" \
  "s_add_u32 s66, s66, 0x40000\n" \
  "s_add_u32 s67, s67, 0x10000\n" \
  "s_mov_b32 s80, s72\n" \
  "s_add_u32 s72, s72, 0x1000\n" \
  "s_mov_b32 s81, s70\n" \
  "s_xor_b32 s70, s70, 4\n" \
  "s_mov_b32 s74, 1\n" \
  "s_cmp_eq_u32 s73, 0\n" \
  "s_cbranch_scc1 .LCQ_POLL_%=\n" \
  "s_waitcnt vmcnt(4)\n" \
  "v_cmp_eq_u32 vcc, 0, v5\n" \
  "s_cmp_eq_u64 vcc, 0\n" \
  "s_cselect_b32 s75, 1, 0\n" \
  "s_branch .LCQ_TOP_%=\n" \
  ".LCQ_POLL_%=:\n" \
  "s_mov_b32 s82, 0x40000\n" \
  "s_branch .LCQ_PW_%=\n" \
  ".LCQ_PV_%=:\n" \
  "s_sleep 12\n" \
  ".LCQ_PW_%=:\n" \
  "v_bfe_u32 v5, v2, 2, 5\n" \
  "buffer_load_dword v5, v5, s[52:55], s71 offen sc1\n" \
  "s_sub_u32 s82, s82, 1\n" \
  "s_cbranch_scc1 .LCQ_END_%=\n" \
  "s_waitcnt vmcnt(0)\n" \
  "s_cmp_eq_u32 s74, 0\n" \
  "s_cbranch_scc1 .LCQ_NPD_%=\n" \
  "v_mov_b32 v10, s81\n" \
  "v_mov_b32 v11, 1\n" \
  "s_mov_b64 exec, 1\n" \
  "ds_add_rtn_u32 v10, v10, v11\n" \
  "s_mov_b64 exec, -1\n" \
  "s_waitcnt lgkmcnt(0)\n" \
  "v_readfirstlane_b32 s83, v10\n" \
  "s_mov_b32 s74, 0\n" \
  "s_and_b32 s83, s83, 7\n" \
  "s_cmp_eq_u32 s83, 7\n" \
  "s_cbranch_scc0 .LCQ_F10_%=\n" \
  "s_mov_b64 exec, 1\n" \
  "buffer_store_dword v11, off, s[56:59], s80 sc1\n" \
  "s_mov_b64 exec, -1\n" \
  ".LCQ_F10_%=:\n" \
  "s_cmp_eq_u32 s64, 0\n" \
  "s_cbranch_scc1 .LCQ_END_%=\n" \
  ".LCQ_NPD_%=:\n" \
  "v_cmp_eq_u32 vcc, 0, v5\n" \
  "s_cmp_eq_u64 vcc, 0\n" \
  "s_cbranch_scc0 .LCQ_PV_%=\n" \
  "s_mov_b32 m0, s68\n" \
  "s_xor_b32 s68, s68, 0x8000\n" \
  "global_load_lds_dwordx4 v2, s[60:61] offset:0 sc1\n" \
  "global_load_lds_dwordx4 v2, s[60:61] offset:1024 sc1\n" \
  "global_load_lds_dwordx4 v2, s[60:61] offset:2048 sc1\n" \
  "global_load_lds_dwordx4 v2, s[60:61] offset:3072 sc1\n" \
  "s_add_u32 s60, s60, 0x8000\n" \
  "s_addc_u32 s61, s61, 0\n" \
  "s_add_u32 s83, s71, 0x2000\n" \
  "v_bfe_u32 v5, v2, 2, 5\n" \
  "buffer_load_dword v5, v5, s[52:55], s83 offen sc1\n" \
  "s_waitcnt vmcnt(0)\n" \
  "v_cmp_eq_u32 vcc, 0, v5\n" \
  "s_cmp_eq_u64 vcc, 0\n" \
  "s_cselect_b32 s75, 1, 0\n" \
  "s_branch .LCQ_TOP_%=\n" \
  ".LCQ_END_%=:\n" \
  "s_endpgm\n"
__device__ __forceinline__ void role_qkv(const MegaArgs& A, char* smem, const int L, const int h) {
  const int w = __builtin_amdgcn_readfirstlane(threadIdx.x >> 6);
  char* ws = A.ws;
  unsigned* FL = (unsigned*)(ws + OFF_FL);
  const int l = lane_id();
  const int c = l & 15, g = l >> 4;
  const unsigned sb = lds_base(smem);
  v8u wq[8], wk[8], wv[8];
  {
    const int q = (l >> 2) & 3, p = l & 3, f = q | ((g & 1) << 2);
    const unsigned ra = sb + (8 * g + q) * 256 + (w ^ f) * 32 + p * 8;
#define W_STAGE(W, hf, VM, NEXT)                                               \
  {                                                                           \
    WAIT_VM(VM);                                                              \
    RAW_BAR();                                                                \
    _Pragma("unroll") for (int t = 0; t < 4; ++t) {                           \
      const unsigned a_ = ra + (hf) * 8192 + t * 32768;                       \
      const v2u r0 = trd(a_), r1 = trd(a_ + 1024), r2 = trd(a_ + 16384), r3 = trd(a_ + 16384 + 1024); \
      W[4 * (hf) + t] = v8u{r0.x, r0.y, r1.x, r1.y, r2.x, r2.y, r3.x, r3.y};  \
    }                                                                         \
    _Pragma("unroll") for (int t = 0; t < 4; ++t) asm volatile("" : "+v"(W[4 * (hf) + t])); \
    RAW_BAR();                                                                \
    NEXT;                                                                     \
  }
    wqkv_issue<false>(A.Wq, 0, h, w, l, sb);
    wqkv_issue<false>(A.Wq, 1, h, w, l, sb);
    W_STAGE(wq, 0, 8, wqkv_issue<false>(A.Wk, 0, h, w, l, sb));
    SST(4);
    W_STAGE(wq, 1, 8, wqkv_issue<false>(A.Wk, 1, h, w, l, sb));
    SST(5);
    W_STAGE(wk, 0, 8, wqkv_issue<false>(A.Wv, 0, h, w, l, sb));
    W_STAGE(wk, 1, 8, wqkv_issue<false>(A.Wv, 1, h, w, l, sb));
    SST(6);
    W_STAGE(wv, 0, 8, (void)0);
    W_STAGE(wv, 1, 0, (void)0);
#undef W_STAGE
  }
  WSTAMP2();
  constexpr int Q_ST = 65536, Q_CN = Q_ST + 8192;
  {
    const v4u one = {0x3f803f80u, 0x3f803f80u, 0x3f803f80u, 0x3f803f80u};
    f32x4 csq = {}, csk = {}, t = {};
#define HALF(x, i) BF8(((i) ? __builtin_shufflevector(x, x, 4, 5, 6, 7) : __builtin_shufflevector(x, x, 0, 1, 2, 3)))
#pragma unroll
    for (int ks = 0; ks < 16; ++ks) {
      csq = MFMA16(HALF(wq[ks >> 1], ks & 1), BF8(one), csq);
      csk = MFMA16(HALF(wk[ks >> 1], ks & 1), BF8(one), csk);
      t = MFMA16(BF8(one), HALF(wv[ks >> 1], ks & 1), t);
    }
#undef HALF
#pragma unroll
    for (int i = 0; i < 2; ++i) {
      float* cp = reinterpret_cast<float*>(smem + Q_ST + w * 1024 + i * 512 + 256);
      *reinterpret_cast<f32x4*>(cp + 4 * g) = csq;
      *reinterpret_cast<f32x4*>(cp + 16 + 4 * g) = csk;
      cp[32 + c] = t[0];
    }
  }
  if (threadIdx.x < 2) reinterpret_cast<unsigned*>(smem + Q_CN)[threadIdx.x] = 0u;
  const int rc = blockIdx.x & (NR_C - 1);
  const v4u rsQ = mk_srd(ws + OFF_QK + L * SZ_QK, SZ_QK);
  const v4u rsV = mk_srd(ws + OFF_VF + L * SZ_O, SZ_O);
  const v4u rsM = mk_srd(ws + (w >= 4 ? OFF_MEMV : OFF_MEMK) + L * SZ_MEM, SZ_MEM);
  const v4u rsF = mk_srd(FL + FL_FC + (L * 64 * 8 + rc) * 8 * 32 + w * 32, (64 * 64 - rc * 8 - w) * 128);
  const v4u rsFQ = mk_srd(FL + FL_FQ + (L * 64 * 32 + h) * 32, 64 * 4096 - h * 128);
  const char* Xb = ws + (L ? OFF_X3 : OFF_XA) + w * 4096;
  const char* Sb = (const char*)(FL + FL_FN + (L * 64 * 8 + (blockIdx.x & (NR_N - 1))) * 64) - 2048;
  v8u pv;
  pv[0] = sb + 32768 + g * 512 + c * 16;
  pv[1] = (c + 16 * (2 * (w & 1) + (g >> 1))) * 16 + 4096 * (g & 1);
  pv[2] = l * 16;
  pv[3] = sb + Q_ST + w * 1024 + 512 + c * 4;
  pv[4] = sb + Q_ST + w * 1024 + 512 + g * 16;
  pv[5] = pv[6] = pv[7] = 0u;
  const char* dbg = nullptr;
  asm volatile("s_mov_b64 exec, -1\ns_waitcnt vmcnt(0) lgkmcnt(0)\ns_nop 4\n" Q_ASM
               ::"{v[64:71]}"(wq[0]), "{v[72:79]}"(wq[1]), "{v[80:87]}"(wq[2]), "{v[88:95]}"(wq[3]), "{v[96:103]}"(wq[4]),
               "{v[104:111]}"(wq[5]), "{v[112:119]}"(wq[6]), "{v[120:127]}"(wq[7]), "{v[128:135]}"(wk[0]), "{v[136:143]}"(wk[1]),
               "{v[144:151]}"(wk[2]), "{v[152:159]}"(wk[3]), "{v[160:167]}"(wk[4]), "{v[168:175]}"(wk[5]), "{v[176:183]}"(wk[6]),
               "{v[184:191]}"(wk[7]), "{v[192:199]}"(wv[0]), "{v[200:207]}"(wv[1]), "{v[208:215]}"(wv[2]), "{v[216:223]}"(wv[3]),
               "{v[224:231]}"(wv[4]), "{v[232:239]}"(wv[5]), "{v[240:247]}"(wv[6]), "{v[248:255]}"(wv[7]), "{v[0:7]}"(pv),
               "{s[40:43]}"(rsQ), "{s[44:47]}"(rsV), "{s[48:51]}"(rsM), "{s[52:55]}"(rsF), "{s[56:59]}"(rsFQ), "{s[60:61]}"(Xb),
               "{s[62:63]}"(Sb), "{s64}"(NSEG), "{s65}"((h * 16 + (w >> 1)) * 1024), "{s66}"((h * 512 + w * 64) * 16),
               "{s67}"(h * 2048 + (w & 3) * 512), "{s68}"(sb + w * 4096), "{s69}"(sb + Q_ST + w * 1024 + 512), "{s70}"(sb + Q_CN),
               "{s71}"(0), "{s72}"(0), "{s[36:37]}"(dbg)
               : "memory", "vcc", "scc", "s84", "v63");
}

constexpr int MEGA_WGS = 256;
constexpr size_t MEGA_LDS = (size_t)(32 * 4 * 64 * 8 + 8 * SMS + 15 * QS + 128 + 16 * 64 * 8 + 8 * SOS + 512) * 2;
static_assert(MEGA_LDS <= 163840, "lds");
static_assert(MEGA_LDS >= (size_t)AW_CNT + 64, "lds");
static_assert(MEGA_LDS >= (size_t)(8 * 2 * CKN + 8 * 2 * CUN + 4 * 32 * 8) * 2, "lds");
static_assert(MEGA_LDS >= (size_t)2 * 32 * SMS * 2 && MEGA_LDS >= 131072, "lds");
static_assert(MEGA_LDS >= (size_t)(40 * NST + 32 * 400 + 4 * 392) * 4, "lds");
__global__ __launch_bounds__(512, 2) void mega_kernel(const u16* mem0, const u16* Dskip, char* ws, u16* out, const u16* x0,
                                                      const u16* logAre, const u16* Aim, const u16* Cre, const u16* Cim,
                                                      const u16* logdt, const u16* Wq, const u16* Wk, const u16* Wv,
                                                      const u16* Wo) {
  extern __shared__ __attribute__((aligned(16))) char smem[];
  const MegaArgs A{mem0, Dskip, ws, out, x0, logAre, Aim, Cre, Cim, logdt, Wq, Wk, Wv, Wo, vbid()};
  const int b = A.vb;
  {
    const int hi = (b >= 32 && b < 96) ? b - 32 : (b >= 200 && b < 248) ? b - 136 : (b >= 96 && b < 104) ? b + 16 : (b >= 248 ? b - 128 : -1);
    const unsigned t0 = now10();
    if (hi >= 0) start_kgen(A, smem, hi, b - 248);
    const unsigned dl = (b >= 32 && b < 64) ? DL_C : (b >= 80 && b < 96) ? DL_A : (b >= 200 && b < 232) ? DL_Q : 0u;
    while (now10() - t0 < dl) __builtin_amdgcn_s_sleep(16);
  }
  if ((EXP_OFF & 1) && b >= 64 && b < 80) return;
  if ((EXP_OFF & 8) && b >= 200 && b < 232) return;
  if ((EXP_OFF & 16) && b >= 32 && b < 64) return;
  if ((EXP_OFF & 32) && b >= 80 && b < 96) return;
  if (b < 64) role_chain(A, smem, b >> 5, b & 31, 0);
  else if (b < 96) role_aw(A, smem, (b >> 4) & 1, b & 15);
  else if (b < 104) role_red(A, smem, 0, b - 96);
  else if (b < 168) role_conv(A, smem, b - 104);
  else if (b < 232) role_qkv(A, smem, (b - 168) >> 5, (b - 168) & 31);
  else if (b < 240) role_red(A, smem, 0, b - 224);
  else role_red(A, smem, 1, b - 240);
}

extern "C" void kernel_launch(void* const* d_in, const int* in_sizes, int n_in, void* d_out, int out_size, void* d_ws,
                              size_t ws_size, hipStream_t stream) {
  MegaArgs A;
  A.x0 = (const u16*)d_in[0];
  A.logAre = (const u16*)d_in[1];
  A.Aim = (const u16*)d_in[2];
  A.Cre = (const u16*)d_in[3];
  A.Cim = (const u16*)d_in[4];
  A.logdt = (const u16*)d_in[5];
  A.Dskip = (const u16*)d_in[6];
  A.mem0 = (const u16*)d_in[8];
  A.Wq = (const u16*)d_in[9];
  A.Wk = (const u16*)d_in[10];
  A.Wv = (const u16*)d_in[11];
  A.Wo = (const u16*)d_in[12];
  A.ws = (char*)d_ws;
  A.out = (u16*)d_out;

  (void)hipFuncSetAttribute((const void*)mega_kernel, hipFuncAttributeMaxDynamicSharedMemorySize, (int)MEGA_LDS);
  zero_kernel<<<ZBLK, 256, 0, stream>>>((uint4*)(A.ws + OFF_Z));
  mega_kernel<<<MEGA_WGS, 512, MEGA_LDS, stream>>>(A.mem0, A.Dskip, A.ws, A.out, A.x0, A.logAre, A.Aim, A.Cre, A.Cim, A.logdt,
                                                   A.Wq, A.Wk, A.Wv, A.Wo);
}
```

```cpp
#include <hip/hip_runtime.h>
#include <hip/hip_bf16.h>
#include <stdint.h>

constexpr int L_SEQ = 2048;
constexpr int DM    = 512;
constexpr int NST   = 128;
constexpr int NH    = 32;
constexpr int DH    = 128;
constexpr int HD    = NH * DH;
constexpr int MM    = 8;
constexpr int SEG   = 32;
constexpr int NSEG  = L_SEQ / SEG;
constexpr int LK    = MM + SEG;

typedef unsigned short u16;
using bf16x8 = __attribute__((ext_vector_type(8))) short;
using f32x4  = __attribute__((ext_vector_type(4))) float;
using f32x16 = __attribute__((ext_vector_type(16))) float;

static __device__ __forceinline__ u16 f2bf(float f) {
  unsigned u = __float_as_uint(f);
  u += 0x7fffu + ((u >> 16) & 1u);
  return (u16)(u >> 16);
}
static __device__ __forceinline__ float bf2f(u16 h) { return __uint_as_float(((unsigned)h) << 16); }
static __device__ __forceinline__ float bfr(float f) { return bf2f(f2bf(f)); }

constexpr size_t SZ_X      = (size_t)L_SEQ * DM * 2;
constexpr size_t OFF_KF    = 0;
constexpr size_t OFF_UT    = OFF_KF + (size_t)DM * L_SEQ * 4;
constexpr size_t OFF_YT    = OFF_UT + SZ_X;
constexpr size_t OFF_XA    = OFF_YT + SZ_X;
constexpr size_t OFF_H     = OFF_XA + SZ_X;
constexpr size_t OFF_X2    = OFF_H + SZ_X;
constexpr size_t OFF_U2    = OFF_X2 + SZ_X;
constexpr size_t OFF_X3    = OFF_U2 + SZ_X;
constexpr size_t SZ_QK     = (size_t)L_SEQ * 2 * HD * 2;
constexpr size_t OFF_QK    = OFF_X3 + SZ_X;
constexpr size_t SZ_O      = (size_t)L_SEQ * HD * 2;
constexpr size_t OFF_VF    = OFF_QK + 2 * SZ_QK;
constexpr size_t SZ_PW     = (size_t)2 * NSEG * 16 * SEG * DM * 2;
constexpr size_t OFF_PW    = OFF_VF + 2 * SZ_O;
constexpr size_t SZ_MEM    = (size_t)NSEG * MM * HD * 2;
constexpr size_t OFF_MEMK  = OFF_PW + SZ_PW;
constexpr size_t OFF_MEMV  = OFF_MEMK + 2 * SZ_MEM;
constexpr size_t OFF_GT    = OFF_MEMV + 2 * SZ_MEM;
constexpr size_t SZ_ACC    = (size_t)2 * 32768;
constexpr size_t OFF_Z     = OFF_GT + (size_t)64 * DM * 4;
constexpr size_t OFF_ACC   = OFF_Z;
constexpr size_t OFF_FL    = OFF_ACC + 2 * SZ_ACC;
constexpr int FL_FK  = 0;
constexpr int FL_FW  = FL_FK + 64 * 8 * 32;
constexpr int FL_FR  = FL_FW + 2 * 64 * 32 * 32;
constexpr int FL_FC  = FL_FR + 64 * 32 * 32;
constexpr int FL_FQ  = FL_FC + 2 * 64 * 64 * 32;
constexpr int FL_FS  = FL_FQ + 2 * 64 * 32 * 32;
constexpr int FL_F0  = FL_FS + 8 * 32 * 32;
constexpr int FL_FZ  = FL_F0 + 8 * 8 * 32;
constexpr int FL_KG  = FL_FZ + 16 * 32;
constexpr int FL_G   = FL_KG + 192 * 32;
constexpr int FL_TMO = FL_G + 2 * 8 * 32;
constexpr int FL_END = FL_TMO + 64;
constexpr size_t Z_BYTES   = OFF_FL + (size_t)FL_END * 4 - OFF_Z;
constexpr size_t WS_TOTAL  = OFF_Z + Z_BYTES;
static_assert(Z_BYTES % 16 == 0 && OFF_Z % 256 == 0, "zero region");
static_assert(WS_TOTAL + (4 << 20) <= (size_t)256 * 1024 * 1024, "workspace too large");

static_assert(WS_TOTAL - OFF_XA <= (size_t)384 * 8 * 65536, "translation warm-up");
constexpr int CKN = 2208;
static_assert((size_t)DM * 2 * CKN * 2 <= (size_t)DM * L_SEQ * 4 + SZ_X, "K buffer");
__device__ __forceinline__ float pos_of_index(int p) {
  if (p < 256) return (float)p;
  if (p < 384) return (float)(256 + 2 * (p - 256));
  if (p < 512) return (float)(512 + 4 * (p - 384));
  if (p < 640) return (float)(1024 + 8 * (p - 512));
  return 2048.0f;
}
__device__ __forceinline__ int index_of_l(int l) {
  float pf = bfr((float)l);
  int p = (int)pf;
  if (p < 256) return p;
  if (p < 512) return 256 + ((p - 256) >> 1);
  if (p < 1024) return 384 + ((p - 512) >> 2);
  if (p < 2048) return 512 + ((p - 1024) >> 3);
  return 640;
}

template <int CTRL> __device__ __forceinline__ float dppr(float x) {
  return __builtin_bit_cast(float, __builtin_amdgcn_update_dpp(0, __builtin_bit_cast(int, x), CTRL, 0xf, 0xf, false));
}
__device__ __forceinline__ float wave_sum(float v) {
  v += dppr<0x121>(v);
  v += dppr<0x122>(v);
  v += dppr<0x124>(v);
  v += dppr<0x128>(v);
  {
    auto r = __builtin_amdgcn_permlane16_swap(__float_as_uint(v), __float_as_uint(v), false, false);
    v = __uint_as_float(r[0]) + __uint_as_float(r[1]);
  }
  {
    auto r = __builtin_amdgcn_permlane32_swap(__float_as_uint(v), __float_as_uint(v), false, false);
    v = __uint_as_float(r[0]) + __uint_as_float(r[1]);
  }
  return v;
}
__device__ __forceinline__ void ln_row8(float (&v)[8]) {
  float s = 0.f;
#pragma unroll
  for (int j = 0; j < 8; ++j) s += v[j];
  s = wave_sum(s);
  const float mean = s * (1.0f / DM);
  float q = 0.f;
#pragma unroll
  for (int j = 0; j < 8; ++j) { float t = v[j] - mean; q += t * t; }
  q = wave_sum(q);
  const float var = q * (1.0f / DM);
  const float mb = bfr(mean);
  const float sd = bfr(sqrtf(bfr(bfr(var) + 1e-5f)));
#pragma unroll
  for (int j = 0; j < 8; ++j) v[j] = bfr(bfr(v[j] - mb) / sd);
}

constexpr int ZBLK = 512;
__global__ __launch_bounds__(256) void zero_kernel(uint4* __restrict__ zero) {
  for (size_t i = (size_t)blockIdx.x * 256 + threadIdx.x; i < Z_BYTES / 16; i += (size_t)ZBLK * 256) zero[i] = uint4{0u, 0u, 0u, 0u};
}

#define RLX_AGENT __ATOMIC_RELAXED, __HIP_MEMORY_SCOPE_AGENT
typedef __attribute__((address_space(1))) unsigned gu32;
typedef unsigned v4u __attribute__((ext_vector_type(4)));
constexpr int AUX_SC1 = 16;
constexpr unsigned SPIN_CAP = 1u << 18;

__device__ __forceinline__ bool wait_flags(const unsigned* flags, int lane, int nfl, unsigned want, unsigned* tmo) {
  for (unsigned spins = 0;; ++spins) {
    unsigned v = want;
    if (lane < nfl) v = __hip_atomic_load((gu32*)flags + lane * 32, RLX_AGENT);
    if (__all(v >= want)) break;
    if (spins > SPIN_CAP) {
      if (lane == 0) __hip_atomic_store((gu32*)tmo, 1u, RLX_AGENT);
      return false;
    }
  }
  __builtin_amdgcn_fence(__ATOMIC_ACQUIRE, "wavefront");
  return true;
}

#define STAMP(i)
#define STAMP_DUMP()
#define WSTAMP(i)
template <int CTRL> __device__ __forceinline__ float dppf(float old, float x) {
  return __builtin_bit_cast(float, __builtin_amdgcn_update_dpp(__builtin_bit_cast(int, old), __builtin_bit_cast(int, x), CTRL, 0xf, 0xf, false));
}
__device__ __forceinline__ float wave_max_dpp(float x) {
  const float ninf = -3e38f;
  x = fmaxf(x, dppf<0x111>(ninf, x));
  x = fmaxf(x, dppf<0x112>(ninf, x));
  x = fmaxf(x, dppf<0x114>(ninf, x));
  x = fmaxf(x, dppf<0x118>(ninf, x));
  x = fmaxf(x, dppf<0x142>(ninf, x));
  x = fmaxf(x, dppf<0x143>(ninf, x));
  return __builtin_bit_cast(float, __builtin_amdgcn_readlane(__builtin_bit_cast(int, x), 63));
}
__device__ __forceinline__ float wave_sum_dpp(float x) {
  x += dppf<0x111>(0.f, x);
  x += dppf<0x112>(0.f, x);
  x += dppf<0x114>(0.f, x);
  x += dppf<0x118>(0.f, x);
  x += dppf<0x142>(0.f, x);
  x += dppf<0x143>(0.f, x);
  return __builtin_bit_cast(float, __builtin_amdgcn_readlane(__builtin_bit_cast(int, x), 63));
}
constexpr int SMS = 528;
constexpr int QS = 144;
constexpr int VS = 80;
constexpr int SOS = 144;
#define VAR_NREP 4
constexpr int NREP = VAR_NREP;
__device__ __forceinline__ float xsum16(float x) {
  auto r = __builtin_amdgcn_permlane16_swap(__float_as_uint(x), __float_as_uint(x), false, false);
  return __uint_as_float(r[0]) + __uint_as_float(r[1]);
}
__device__ __forceinline__ float xsum32(float x) {
  auto r = __builtin_amdgcn_permlane32_swap(__float_as_uint(x), __float_as_uint(x), false, false);
  return __uint_as_float(r[0]) + __uint_as_float(r[1]);
}
__device__ __forceinline__ float xmax16(float x) {
  auto r = __builtin_amdgcn_permlane16_swap(__float_as_uint(x), __float_as_uint(x), false, false);
  return fmaxf(__uint_as_float(r[0]), __uint_as_float(r[1]));
}
__device__ __forceinline__ float xmax32(float x) {
  auto r = __builtin_amdgcn_permlane32_swap(__float_as_uint(x), __float_as_uint(x), false, false);
  return fmaxf(__uint_as_float(r[0]), __uint_as_float(r[1]));
}
__device__ __forceinline__ float red_mod4(float x) {
  x += dppf<0x124>(0.f, x);
  x += dppf<0x128>(0.f, x);
  return xsum32(xsum16(x));
}
typedef __bf16 bf16x2_t __attribute__((ext_vector_type(2)));
typedef float f32x2_t __attribute__((ext_vector_type(2)));
__device__ __forceinline__ unsigned pk2(float a, float b) {
  f32x2_t v = {a, b};
  bf16x2_t r = __builtin_convertvector(v, bf16x2_t);
  return __builtin_bit_cast(unsigned, r);
}
__device__ __forceinline__ float one(float x) {
  asm("" : "+v"(x));
  return x;
}
__device__ __forceinline__ unsigned pk2s(float a, float b) { return pk2(one(a), one(b)); }
__device__ __forceinline__ float dot2sq(const unsigned x, const float acc) {
  return __builtin_amdgcn_fdot2_f32_bf16(__builtin_bit_cast(bf16x2_t, x), __builtin_bit_cast(bf16x2_t, x), acc, false);
}
__device__ __forceinline__ float lo16(unsigned u) { return __uint_as_float(u << 16); }
__device__ __forceinline__ float hi16(unsigned u) { return __uint_as_float(u & 0xffff0000u); }
__device__ __forceinline__ void scale_pair(float a, float b, float scale, float& oa, float& ob) {
  unsigned u = pk2(a, b);
  unsigned v = pk2s(lo16(u) * scale, hi16(u) * scale);
  oa = lo16(v);
  ob = hi16(v);
}

#define XMAP 2
__device__ __forceinline__ int vbid() {
  const int b = blockIdx.x, x = b & 7, t = b >> 3;
  if (XMAP == 0) {
    if (b < 64) return (b & 32) + 4 * x + (t & 3);
    if (b < 96) return (b & ~15) + 2 * x + (t & 1);
    if (b >= 104 && b < 168) return 104 + 8 * x + t - 13;
    if (b >= 168 && b < 232) return 168 + ((b - 168) & 32) + 4 * x + ((t - 21) & 3);
    return b;
  }
  if (XMAP == 2) {
    if (x < 4) return t < 16 ? 4 * (t & 8) + 8 * x + (t & 7) : 104 + 16 * x + t - 16;
    const int y = x - 4;
    if (t >= 24) return (y == 0 ? 96 : 224 + 8 * y) + t - 24;
    if (t < 16) return 168 + 4 * (t & 8) + 8 * y + (t & 7);
    return 64 + 4 * (t & 4) + 4 * y + (t & 3);
  }
  if (x < 2) return 2 * (t & 16) + 16 * x + (t & 15);
  const int i = 16 * (x - 2) + (t & 15);
  if (t < 16) {
    const int k = (i * 171) >> 9, r = i - 3 * k;
    return r == 0 ? 168 + k : r == 1 ? 200 + k : 64 + 16 * (k & 1) + (k >> 1);
  }
  const int g = i >> 3, e = i & 7;
  if (g == 3) return 96 + e;
  if (g == 7) return 232 + e;
  if (g == 9) return 240 + e;
  if (g == 11) return 248 + e;
  return 104 + 8 * (g - (g > 3) - (g > 7) - (g > 9)) + e;
}
#define MSTAMP()
#define WSTAMP2()
#define SST(k)
#define TS(k, ss, t)
#define LS(k, ss)
#define LS_DUMP()
struct MegaArgs {
  const u16* mem0;
  const u16* Dskip;
  char* ws;
  u16* out;
  const u16* x0;
  const u16 *logAre, *Aim, *Cre, *Cim, *logdt;
  const u16 *Wq, *Wk, *Wv, *Wo;
  int vb;
};

__device__ __forceinline__ int lane_id() {
  int l;
  asm volatile("v_mbcnt_lo_u32_b32 %0, -1, 0\n\tv_mbcnt_hi_u32_b32 %0, -1, %0" : "=v"(l));
  return l;
}
__device__ __forceinline__ bool spin_timeout(unsigned spins, unsigned* tmo) {
  if (spins > SPIN_CAP) {
    if (lane_id() == 0) __hip_atomic_store((gu32*)tmo, 1u, RLX_AGENT);
    return true;
  }
  return false;
}
#define ROT_H (4 * (h & 1))
#define AWDIR 1
#define AWAT 0
#define AWRP 0
#define PEPS 4
#define FGATE 0
#define DL_C 0
#define DL_A 0
#define DL_Q 0
#define KC0 0
#define CPRIO 1
#define P1PRIO 3
#define QLEAD 2
#define POLL_SLEEP 12
#define SLP_C POLL_SLEEP
#define SLP_A POLL_SLEEP
#define SLP_W POLL_SLEEP
#define SLP_R POLL_SLEEP
#define SLP_V POLL_SLEEP
#define SLP_Q POLL_SLEEP
#define SLP_M 4
#define AWPRIO 2
#define SLP_S 8
#define SPOS 4
#define CVPRE 1
#define QPD 3
#define QPRI 2
#define TLBW 1
#define QHV 0
#define QCHK 0
#define LEADN 99
#define GATE_V 170
#define GATE_R 380
#define GATE_N 380
#define AWAHEAD 1
#define GATE_M 150
#define PRI_A 3
#define PRI_WA 2
#define PRI_WB 1
#define AWSKIP 3
#define PRI_0 3
#define AWDEF 0
#define SLP_F 2
#define EXP_OFF 0
#define AWSPEC 1
#define CVTHR 48
#define NR_C 8
#define NR_R 8
#define NR_W 8
#define NR_N 2
#define QAHEAD 0
#define NS0 4
#define SLP_N POLL_SLEEP
template <int SLP, int WPL>
__device__ __forceinline__ bool wait_n(const unsigned* f, int n, unsigned* tmo) {
  const int l = lane_id();
  for (unsigned spins = 0;; ++spins) {
    unsigned v = 1u;
    if (l < n) v = __hip_atomic_load((gu32*)f + (l / WPL) * 32 + l % WPL, RLX_AGENT);
    if (__all(v != 0u)) break;
    if (spin_timeout(spins, tmo)) return false;
    __builtin_amdgcn_s_sleep(SLP);
  }
  asm volatile("" ::: "memory");
  return true;
}
__device__ __forceinline__ unsigned now10() { return (unsigned)__builtin_amdgcn_s_memrealtime(); }
__device__ __forceinline__ void gate(const unsigned t) {
  for (int i = 0; i < 64; ++i) {
    const int d = (int)(t - now10());
    if (d <= 0) break;
    if (d > 100) __builtin_amdgcn_s_sleep(32);
    else if (d > 25) __builtin_amdgcn_s_sleep(8);
    else __builtin_amdgcn_s_sleep(2);
  }
}
template <int SLP, int WPL, int GT>
__device__ __forceinline__ void grp_wait(const bool leader, volatile unsigned* sw, const unsigned val, const unsigned* f, int n,
                                         unsigned* tmo, unsigned& tl) {
  if (leader) {
    const int l = lane_id();
    for (unsigned spins = 0;; ++spins) {
      unsigned v = 1u;
      if (l < n) v = __hip_atomic_load((gu32*)f + (l / WPL) * 32 + l % WPL, RLX_AGENT);
      if (__all(v != 0u) || spin_timeout(spins, tmo)) break;
      if (GT && spins == 0) gate(tl);
      else __builtin_amdgcn_s_sleep(SLP);
    }
    if (GT) tl = now10() + GT;
    if ((threadIdx.x & 63) == 0) *sw = val;
  } else {
    for (unsigned spins = 0; *sw < val; ++spins) {
      if (spins > 64u * SPIN_CAP) break;
      if (FGATE && GT && spins == 0) gate(tl);
      __builtin_amdgcn_s_sleep(SLP_F);
    }
    if (FGATE && GT) tl = now10() + GT;
  }
  asm volatile("" ::: "memory");
}
template <int SLP, int WPL>
__device__ __forceinline__ bool wg_wait(const unsigned* f, int n, unsigned* tmo) {
  if (threadIdx.x < 64) (void)wait_n<SLP, WPL>(f, n, tmo);
  __syncthreads();
  asm volatile("" ::: "memory");
  return true;
}
__device__ __forceinline__ void set_flag(unsigned* f) { __hip_atomic_store((gu32*)f, 1u, RLX_AGENT); }
#define DRAIN() asm volatile("s_waitcnt vmcnt(0)" ::: "memory")
#define MKRS(ptr, bytes) __builtin_amdgcn_make_buffer_rsrc((void*)(ptr), 0, (int)(bytes), 0x00020000)
typedef unsigned v2u __attribute__((ext_vector_type(2)));
#define LD128(rs, voff, soff) __builtin_amdgcn_raw_buffer_load_b128(rs, voff, soff, AUX_SC1)
#define LD64(rs, voff, soff) __builtin_amdgcn_raw_buffer_load_b64(rs, voff, soff, AUX_SC1)
#define ST128(v, rs, voff, soff)                                              \
  do {                                                                        \
    const v4u st_d_ = (v);                                                    \
    __builtin_amdgcn_raw_buffer_store_b128(st_d_, rs, voff, soff, AUX_SC1);   \
    asm volatile("s_nop 1" ::"v"(st_d_) : "memory");                          \
  } while (0)
#define ST64(v, rs, voff, soff) __builtin_amdgcn_raw_buffer_store_b64(v, rs, voff, soff, AUX_SC1)
#define BF8(x) __builtin_bit_cast(bf16x8, x)
#define MFMA16(a, b, c) __builtin_amdgcn_mfma_f32_16x16x32_bf16(a, b, c, 0, 0, 0)
typedef short bf16x4 __attribute__((ext_vector_type(4)));
#define MFMA16K(a, b, c) \
  __builtin_amdgcn_mfma_f32_16x16x16bf16_1k(__builtin_bit_cast(bf16x4, a), __builtin_bit_cast(bf16x4, b), c, 0, 0, 0)
__device__ __forceinline__ float fmax_nn(float a, float b) { return __builtin_elementwise_maximum(a, b); }
typedef __bf16 bfv8 __attribute__((ext_vector_type(8)));
typedef __bf16 bfv16 __attribute__((ext_vector_type(16)));
typedef unsigned v8u __attribute__((ext_vector_type(8)));
#define SMF(a, b, c, idx) \
  __builtin_amdgcn_smfmac_f32_16x16x64_bf16(__builtin_bit_cast(bfv8, a), __builtin_bit_cast(bfv16, b), c, idx, 0, 0)
__device__ __forceinline__ float swap_add32(float a, float b) {
  auto r = __builtin_amdgcn_permlane32_swap(__float_as_uint(a), __float_as_uint(b), false, false);
  return __uint_as_float(r[0]) + __uint_as_float(r[1]);
}
__device__ __forceinline__ float swap_add16(float a, float b) {
  auto r = __builtin_amdgcn_permlane16_swap(__float_as_uint(a), __float_as_uint(b), false, false);
  return __uint_as_float(r[0]) + __uint_as_float(r[1]);
}
__device__ __forceinline__ v4u smf_fix(const v4u d) {
  auto x = __builtin_amdgcn_permlane16_swap(d.x, d.z, false, false);
  auto y = __builtin_amdgcn_permlane16_swap(d.y, d.w, false, false);
  return v4u{x[0], x[1], y[0], y[1]};
}
__device__ __forceinline__ v8u smf_bfrag(const u16* p) {
  const v2u a = *reinterpret_cast<const v2u*>(p), b = *reinterpret_cast<const v2u*>(p + 8);
  const v2u c = *reinterpret_cast<const v2u*>(p + 32), d = *reinterpret_cast<const v2u*>(p + 40);
  return v8u{a.x, b.x, a.y, b.y, c.x, d.x, c.y, d.y};
}

#define VAR_PPA 0
#define VAR_PPB 0
#define VAR_THR 0
#define VAR_THR2 0
#define CV_LEAD 100
#define VAR_EARLY 1
#define VAR_DRAIN 1
#define VAR_D1 0
#define VAR_D2 0
#define VAR_DR2 1
#define VAR_S1 0
#define VAR_S2 0
typedef __attribute__((address_space(1))) unsigned long long gu64;
typedef __attribute__((address_space(1))) char gch;
__device__ __forceinline__ int cvt_rpi(float x) {
  int r;
  asm("v_cvt_rpi_i32_f32 %0, %1" : "=v"(r) : "v"(x));
  return r;
}
typedef short v4s __attribute__((ext_vector_type(4)));
typedef __attribute__((address_space(3))) v4s* lp4s;
__device__ __forceinline__ void glds16(const void* sbase, const unsigned voff, const unsigned lds) {
  unsigned keep;
  asm volatile("s_nop 4\n\ts_mov_b32 %0, m0\n\ts_mov_b32 m0, %3\n\ts_nop 0\n\tglobal_load_lds_dwordx4 %1, %2\n\ts_mov_b32 m0, %0"
               : "=&s"(keep)
               : "v"(voff), "s"(sbase), "s"(lds)
               : "memory");
}
__device__ __forceinline__ void glds16c(const void* sbase, const unsigned voff, const unsigned lds) {
  unsigned keep;
  asm volatile("s_nop 4\n\ts_mov_b32 %0, m0\n\ts_mov_b32 m0, %3\n\ts_nop 0\n\tglobal_load_lds_dwordx4 %1, %2 sc1\n\ts_mov_b32 m0, %0"
               : "=&s"(keep)
               : "v"(voff), "s"(sbase), "s"(lds)
               : "memory");
}
__device__ __forceinline__ v2u trd(const unsigned a) {
  return __builtin_bit_cast(v2u, __builtin_amdgcn_ds_read_tr16_b64_v4i16((lp4s)(unsigned long)a));
}
__device__ __forceinline__ unsigned lds_base(char* smem) { return (unsigned)(unsigned long)(__attribute__((address_space(3))) char*)smem; }
#define RAW_BAR()                                           \
  do {                                                      \
    asm volatile("s_waitcnt lgkmcnt(0)" ::: "memory");      \
    __builtin_amdgcn_s_barrier();                           \
    asm volatile("" ::: "memory");                          \
  } while (0)
#define WAIT_VM(n) asm volatile("s_waitcnt vmcnt(" #n ")" ::: "memory")
template <bool SP>
__device__ __forceinline__ void wqkv_issue(const u16* Wm, const int hf, const int h, const int w, const int l, const unsigned sb) {
  const char* base = (const char*)Wm + (size_t)(256 * hf + 32 * w) * (HD * 2) + h * (DH * 2);
#pragma unroll
  for (int i = 0; i < 8; ++i) {
    const int r = l >> 4;
    const int f = SP ? ((r & 1) | ((i & 1) << 1) | (((i >> 1) & 1) << 2)) : (r | (((i >> 1) & 1) << 2));
    glds16(base, (unsigned)((4 * i + r) * (HD * 2) + (((l & 15) ^ (f << 1)) << 4)), sb + w * 16384 + hf * 8192 + i * 1024);
  }
}

__device__ __forceinline__ unsigned row_stats(const v4u raw) {
  float v[8];
#pragma unroll
  for (int e = 0; e < 4; ++e) { v[2 * e] = lo16(raw[e]); v[2 * e + 1] = hi16(raw[e]); }
  float s = 0.f;
#pragma unroll
  for (int e = 0; e < 8; ++e) s += v[e];
  const float mean = wave_sum(s) * (1.0f / DM);
  float q = 0.f;
#pragma unroll
  for (int e = 0; e < 8; ++e) { const float t = v[e] - mean; q += t * t; }
  const float var = wave_sum(q) * (1.0f / DM);
  const float sd = bfr(sqrtf(bfr(bfr(var) + 1e-5f)));
  return (unsigned)f2bf(mean) | ((unsigned)f2bf(sd) << 16);
}
__device__ __forceinline__ void start_stats(const MegaArgs& A, const int idx) {
  const int w = __builtin_amdgcn_readfirstlane(threadIdx.x >> 6), l = threadIdx.x & 63;
  const int r0 = 16 * idx + 2 * w, sx = 8 * idx + w;
  v4u raw[8], ra, rb;
  if (idx < 8) {
    ra = *reinterpret_cast<const v4u*>(A.x0 + (size_t)r0 * DM + l * 8);
    rb = *reinterpret_cast<const v4u*>(A.x0 + (size_t)(r0 + 1) * DM + l * 8);
  }
#pragma unroll
  for (int i = 0; i < 8; ++i) raw[i] = *reinterpret_cast<const v4u*>(A.x0 + (size_t)(8 * sx + i) * DM + l * 8);
  if (idx < 8) {
    const unsigned sa = row_stats(ra), sb = row_stats(rb);
    if (l < 2) __hip_atomic_store((gu32*)(A.ws + OFF_GT) + r0 + l, l ? sb : sa, RLX_AGENT);
    DRAIN();
    if (l < 8) set_flag((unsigned*)(A.ws + OFF_FL) + FL_F0 + (l * 8 + idx) * 32 + w);
    SST(11);
  }
  unsigned stw = 0u;
#pragma unroll
  for (int i = 0; i < 8; ++i) {
    const unsigned t = row_stats(raw[i]);
    if (l == i) stw = t;
  }
  if (l < 8) __hip_atomic_store((gu32*)(A.ws + OFF_GT) + 8 * sx + l, stw, RLX_AGENT);
  DRAIN();
  if (l < 8) set_flag((unsigned*)(A.ws + OFF_FL) + FL_FS + (l * 32 + idx) * 32 + w);
  SST(0);
}
__device__ __forceinline__ void start_kgen(const MegaArgs& A, char* smem, const int kc, const int kg) {
  const int tid = threadIdx.x;
  float* prm = reinterpret_cast<float*>(smem);
  float* part = prm + 16 * 6 * NST;
  float* kd = part + 16 * 8 * 132;
  {
    const int j = tid >> 5, u = 4 * (tid & 31), d = 16 * kg + j;
    const u16 p0 = A.logdt[d];
    const v2u p1 = *reinterpret_cast<const v2u*>(A.logAre + d * NST + u), p2 = *reinterpret_cast<const v2u*>(A.Aim + d * NST + u);
    const v2u p3 = *reinterpret_cast<const v2u*>(A.Cre + d * NST + u), p4 = *reinterpret_cast<const v2u*>(A.Cim + d * NST + u);
    const float dt = bfr(expf(bf2f(p0))), st = (float)(2 << kc);
    f32x4 o[6];
#pragma unroll
    for (int e = 0; e < 4; ++e) {
      const unsigned q1 = p1[e >> 1], q2 = p2[e >> 1], q3 = p3[e >> 1], q4 = p4[e >> 1];
      const float ar = -bfr(expf((e & 1) ? hi16(q1) : lo16(q1))) * dt, ai = ((e & 1) ? hi16(q2) : lo16(q2)) * dt;
      const float ex = expf(ar * st);
      float sn, cs;
      sincosf(ai * st, &sn, &cs);
      o[0][e] = ar;
      o[1][e] = ai;
      o[2][e] = ((e & 1) ? hi16(q3) : lo16(q3)) * dt;
      o[3][e] = ((e & 1) ? hi16(q4) : lo16(q4)) * dt;
      o[4][e] = ex * cs;
      o[5][e] = ex * sn;
    }
#pragma unroll
    for (int i = 0; i < 6; ++i) *reinterpret_cast<f32x4*>(prm + (j * 6 + i) * NST + u) = o[i];
  }
  asm volatile("s_waitcnt lgkmcnt(0)" ::: "memory");
  SST(1);
  unsigned tw = 0u;
  if (TLBW && tid < 384 && (blockIdx.x >> 6) == 1) {
    const unsigned o = (unsigned)OFF_XA + (unsigned)(tid * 8 + ((blockIdx.x >> 3) & 7)) * 65536u;
    if (o < WS_TOTAL) asm volatile("global_load_dword %0, %1, %2" : "=v"(tw) : "v"(o), "s"(A.ws) : "memory");
  }
  const int j = tid >> 5, m = tid & 31;
  {
    const int r = m & 3, ng = m >> 2;
    const float* pp = prm + j * 6 * NST;
    const float p0 = (float)((256 + 64 * r) << kc);
    f32x2_t acc[33];
#pragma unroll
    for (int i = 0; i < 33; ++i) acc[i] = f32x2_t{0.f, 0.f};
#pragma unroll 1
    for (int bt = 0; bt < 4; ++bt) {
      const int n = 4 * ng + 32 * bt;
      const f32x4 ar = *reinterpret_cast<const f32x4*>(pp + n), ai = *reinterpret_cast<const f32x4*>(pp + NST + n);
      const f32x4 cr = *reinterpret_cast<const f32x4*>(pp + 2 * NST + n), ci = *reinterpret_cast<const f32x4*>(pp + 3 * NST + n);
      const f32x4 wr = *reinterpret_cast<const f32x4*>(pp + 4 * NST + n), wi = *reinterpret_cast<const f32x4*>(pp + 5 * NST + n);
      float yr0[4], yi0[4];
#pragma unroll
      for (int k = 0; k < 4; ++k) {
        const float e = __expf(ar[k] * p0);
        float sn, cs;
        __sincosf(ai[k] * p0, &sn, &cs);
        const float zr = e * cs, zi = e * sn;
        yr0[k] = cr[k] * zr - ci[k] * zi;
        yi0[k] = cr[k] * zi + ci[k] * zr;
      }
      f32x2_t yra = {yr0[0], yr0[1]}, yia = {yi0[0], yi0[1]}, wra = {wr[0], wr[1]}, wia = {wi[0], wi[1]};
      f32x2_t yrb = {yr0[2], yr0[3]}, yib = {yi0[2], yi0[3]}, wrb = {wr[2], wr[3]}, wib = {wi[2], wi[3]};
#pragma unroll
      for (int i = 0; i < 33; ++i) {
        acc[i] += yra + yrb;
        const f32x2_t ta = yra * wra - yia * wia, tb = yrb * wrb - yib * wib;
        yia = yra * wia + yia * wra;
        yib = yrb * wib + yib * wrb;
        yra = ta;
        yrb = tb;
      }
    }
#pragma unroll
    for (int i = 0; i < 33; ++i) part[(j * 8 + ng) * 132 + 33 * r + i] = acc[i].x + acc[i].y;
  }
  asm volatile("s_waitcnt lgkmcnt(0)" ::: "memory");
  SST(2);
#pragma unroll
  for (int r = 0; r < 5; ++r) {
    const int o = r < 4 ? 33 * r + m : 131;
    float v = 0.f;
#pragma unroll
    for (int g = 0; g < 8; ++g) v += part[(j * 8 + g) * 132 + o];
    v *= 2.0f;
    if (r < 4 || m == 0) reinterpret_cast<unsigned*>(kd)[j * 132 + 32 * r + m] = pk2(v, v - lo16(pk2(v, v)));
  }
  asm volatile("s_waitcnt lgkmcnt(0)" ::: "memory");
  const __amdgpu_buffer_rsrc_t rs = MKRS(A.ws + OFF_KF, DM * 2 * CKN * 2);
  const unsigned* kp = reinterpret_cast<const unsigned*>(kd) + j * 132 - 128;
  const int ue = kc == 0 ? 229 : 260 - (32 << kc);
  for (int un = (kc == 2 ? 0 : 260 - (64 << kc)) + m; un < ue; un += 32) {
    v4u oh, ol;
#pragma unroll
    for (int e = 0; e < 4; ++e) {
      unsigned kv[2];
#pragma unroll
      for (int k = 0; k < 2; ++k) {
        const int lag = 2080 - (8 * un + 2 * e + k);
        const int q = (lag + (1 << kc) - 1 + ((lag >> (kc + 1)) & 1)) >> (kc + 1);
        const bool in = lag >= 256 && lag < L_SEQ;
        kv[k] = kp[in ? q : 128];
        if (!in) kv[k] = 0u;
      }
      oh[e] = __builtin_amdgcn_perm(kv[1], kv[0], 0x05040100);
      ol[e] = __builtin_amdgcn_perm(kv[1], kv[0], 0x07060302);
    }
    ST128(oh, rs, ((16 * kg + j) * 2 * CKN + 8 * un) * 2, 0);
    ST128(ol, rs, ((16 * kg + j) * 2 * CKN + CKN + 8 * un) * 2, 0);
  }
  DRAIN();
  asm volatile("" ::"v"(tw));
  if ((tid & 63) == 0) set_flag((unsigned*)(A.ws + OFF_FL) + FL_KG + ((2 * kg + (tid >> 8)) * 3 + kc) * 32 + ((tid >> 6) & 3));
  __syncthreads();
  SST(3);
}

constexpr int C_SM = 131072, C_SO = C_SM + 8192, C_KT = C_SO + 2048, C_X = C_KT + 16384, C_SQ = C_X + 1024;
constexpr int C_SK = C_SQ + (7 * QS + 128) * 2;
static_assert(C_SK + 8 * QS * 2 <= 163840, "lds");
#define TDSLOT 0
#define CHAIN_ASM_ATT \
  "s_mov_b32 s92, 1152\n" \
  "s_mov_b32 s93, 576\n" \
  "s_mov_b32 s94, 2272\n" \
  "s_mov_b32 s95, 272\n" \
  "s_mov_b32 s96, 288\n" \
  "s_mov_b32 s91, 0x40000001\n" \
  "s_mov_b32 s84, 0x4b400000\n" \
  "s_mov_b32 s85, 0x4b400000\n" \
  "s_mov_b32 s78, 0x3e0293ee\n" \
  "s_mov_b32 s76, 0xff00ff00\n" \
  "s_mov_b32 s77, 0xff00ff00\n" \
  "s_add_u32 s58, s56, s60\n" \
  "s_addc_u32 s59, s57, 0\n" \
  "s_lshl_b32 s98, s73, 10\n" \
  "s_add_u32 s98, s98, s74\n" \
  "s_add_u32 s98, s98, 131072\n" \
  "s_lshl_b32 s97, s73, 5\n" \
  "s_add_u32 s97, s97, s74\n" \
  "s_add_u32 s97, s97, 158720\n" \
  "s_lshl_b32 s99, s73, 10\n" \
  "v_mbcnt_lo_u32_b32 v48, -1, 0\n" \
  "v_mbcnt_hi_u32_b32 v48, -1, v48\n" \
  "v_and_b32 v49, 15, v48\n" \
  "v_lshl_add_u32 v8, v49, 1, s97\n" \
  "v_bfe_u32 v49, v48, 4, 1\n" \
  "v_mad_u32_u24 v8, v49, s92, v8\n" \
  "v_lshrrev_b32 v49, 5, v48\n" \
  "v_mad_u32_u24 v9, v49, s93, v8\n" \
  "v_mad_u32_u24 v8, v49, s94, v8\n" \
  "v_add_u32 v10, s99, v1\n" \
  "s_lshr_b32 s88, s99, 2\n" \
  "s_add_u32 s88, s88, s74\n" \
  "s_add_u32 s88, s88, 139264\n" \
  "v_lshl_add_u32 v7, v49, 10, s88\n" \
  "v_and_b32 v49, 31, v48\n" \
  "v_lshl_add_u32 v7, v49, 3, v7\n" \
  "v_mov_b64 v[12:13], 0\n" \
  "v_mov_b64 v[14:15], 0\n" \
  "v_mov_b64 v[16:17], 0\n" \
  "v_mov_b64 v[18:19], 0\n" \
  "v_mov_b64 v[20:21], 0\n" \
  "v_mov_b64 v[22:23], 0\n" \
  "s_add_u32 s88, s74, 157760\n" \
  "v_mov_b32 v59, s88\n" \
  "v_mov_b32 v24, 0x44870000\n" \
  "v_mul_f32 v24, s72, v24\n" \
  "s_nop 0\n" \
  "v_readfirstlane_b32 s86, v24\n" \
  "s_lshr_b32 s86, s86, 23\n" \
  "s_addk_i32 s86, 0xff83\n" \
  "s_ashr_i32 s86, s86, 1\n" \
  "s_max_i32 s86, s86, -20\n" \
  "s_min_i32 s86, s86, 100\n" \
  "s_mov_b32 s82, s86\n" \
  "s_lshr_b32 s83, s72, 23\n" \
  "s_sub_i32 s83, s83, 242\n" \
  "s_lshl_b32 s88, s73, 10\n" \
  "v_add_u32 v24, s88, v1\n" \
  "ds_read_b128 v[32:35], v24 offset:0\n" \
  "s_waitcnt lgkmcnt(0)\n" \
  "v_mov_b32 v11, 0\n" \
  "v_dot2c_f32_bf16 v11, v32, v32\n" \
  "v_dot2c_f32_bf16 v11, v33, v33\n" \
  "v_dot2c_f32_bf16 v11, v34, v34\n" \
  "v_dot2c_f32_bf16 v11, v35, v35\n" \
  "s_nop 1\n" \
  "s_nop 0\n" \
  "v_cmpx_lt_f32 vcc, 2.0, v11\n" \
  "ds_max_u32 v59, v11\n" \
  "s_mov_b64 exec, -1\n" \
  ".LCA_TOP_%=:\n" \
  "s_waitcnt lgkmcnt(0)\n" \
  "s_barrier\n" \
  "ds_read_b128 v[40:43], v1 offset:0\n" \
  "ds_read_b128 v[44:47], v1 offset:1024\n" \
  "ds_read_b128 v[48:51], v1 offset:2048\n" \
  "ds_read_b128 v[52:55], v1 offset:3072\n" \
  "ds_read_b128 v[56:59], v1 offset:4096\n" \
  "ds_read_b128 v[60:63], v1 offset:5120\n" \
  "s_cmp_lg_u32 s73, 0\n" \
  "s_cbranch_scc1 .LCA_NOZ_%=\n" \
  "s_xor_b32 s88, s60, 0x8000\n" \
  "s_add_u32 s88, s88, s61\n" \
  "buffer_store_dwordx2 v[12:13], v5, s[40:43], s88 offen sc1\n" \
  ".LCA_NOZ_%=:\n" \
  "s_waitcnt lgkmcnt(5)\n" \
  "v_smfmac_f32_16x16x64_bf16 v[20:23], v[40:43], v[192:199], v0\n" \
  "v_smfmac_f32_16x16x64_bf16 v[12:15], v[40:43], v[64:71], v0\n" \
  "v_smfmac_f32_16x16x64_bf16 v[16:19], v[40:43], v[128:135], v0\n" \
  "ds_read_b128 v[40:43], v1 offset:6144\n" \
  "s_waitcnt lgkmcnt(5)\n" \
  "v_smfmac_f32_16x16x64_bf16 v[20:23], v[44:47], v[200:207], v0\n" \
  "v_smfmac_f32_16x16x64_bf16 v[12:15], v[44:47], v[72:79], v0\n" \
  "v_smfmac_f32_16x16x64_bf16 v[16:19], v[44:47], v[136:143], v0\n" \
  "ds_read_b128 v[44:47], v1 offset:7168\n" \
  "s_waitcnt lgkmcnt(5)\n" \
  "v_smfmac_f32_16x16x64_bf16 v[20:23], v[48:51], v[208:215], v0\n" \
  "v_smfmac_f32_16x16x64_bf16 v[12:15], v[48:51], v[80:87], v0\n" \
  "v_smfmac_f32_16x16x64_bf16 v[16:19], v[48:51], v[144:151], v0\n" \
  "ds_read_b128 v[24:27], v1 offset:10240\n" \
  "ds_read_b128 v[28:31], v1 offset:14336\n" \
  "s_waitcnt lgkmcnt(6)\n" \
  "v_smfmac_f32_16x16x64_bf16 v[20:23], v[52:55], v[216:223], v0\n" \
  "v_smfmac_f32_16x16x64_bf16 v[12:15], v[52:55], v[88:95], v0\n" \
  "v_smfmac_f32_16x16x64_bf16 v[16:19], v[52:55], v[152:159], v0\n" \
  "ds_read_b128 v[32:35], v1 offset:11264\n" \
  "ds_read_b128 v[36:39], v1 offset:15360\n" \
  "s_waitcnt lgkmcnt(7)\n" \
  "v_smfmac_f32_16x16x64_bf16 v[20:23], v[56:59], v[224:231], v0\n" \
  "v_smfmac_f32_16x16x64_bf16 v[12:15], v[56:59], v[96:103], v0\n" \
  "v_smfmac_f32_16x16x64_bf16 v[16:19], v[56:59], v[160:167], v0\n" \
  "s_waitcnt lgkmcnt(6)\n" \
  "v_smfmac_f32_16x16x64_bf16 v[20:23], v[60:63], v[232:239], v0\n" \
  "v_smfmac_f32_16x16x64_bf16 v[12:15], v[60:63], v[104:111], v0\n" \
  "v_smfmac_f32_16x16x64_bf16 v[16:19], v[60:63], v[168:175], v0\n" \
  "s_waitcnt lgkmcnt(5)\n" \
  "v_smfmac_f32_16x16x64_bf16 v[20:23], v[40:43], v[240:247], v0\n" \
  "v_smfmac_f32_16x16x64_bf16 v[12:15], v[40:43], v[112:119], v0\n" \
  "v_smfmac_f32_16x16x64_bf16 v[16:19], v[40:43], v[176:183], v0\n" \
  "s_waitcnt lgkmcnt(4)\n" \
  "v_smfmac_f32_16x16x64_bf16 v[20:23], v[44:47], v[248:255], v0\n" \
  "v_smfmac_f32_16x16x64_bf16 v[12:15], v[44:47], v[120:127], v0\n" \
  "v_smfmac_f32_16x16x64_bf16 v[16:19], v[44:47], v[184:191], v0\n" \
  "s_cmp_eq_u32 s71, 0\n" \
  "s_nop 4\n" \
  "v_permlane32_swap_b32 v20, v22\n" \
  "v_permlane32_swap_b32 v21, v23\n" \
  "v_pk_add_f32 v[20:21], v[20:21], v[22:23]\n" \
  "v_permlane32_swap_b32 v12, v16\n" \
  "v_permlane32_swap_b32 v13, v17\n" \
  "v_cvt_pk_bf16_f32 v20, v20, v21\n" \
  "v_pk_add_f32 v[12:13], v[12:13], v[16:17]\n" \
  "v_permlane32_swap_b32 v14, v18\n" \
  "v_permlane32_swap_b32 v15, v19\n" \
  "ds_write_b16 v9, v20 offset:128\n" \
  "v_cvt_pk_bf16_f32 v12, v12, v13\n" \
  "v_pk_add_f32 v[14:15], v[14:15], v[18:19]\n" \
  "ds_write_b16_d16_hi v9, v20 offset:416\n" \
  "ds_write_b16 v8, v12\n" \
  "v_cvt_pk_bf16_f32 v14, v14, v15\n" \
  "ds_write_b16_d16_hi v8, v12 offset:288\n" \
  "ds_write_b16 v8, v14 offset:576\n" \
  "ds_write_b16_d16_hi v8, v14 offset:864\n" \
  "s_setprio 3\n" \
  "s_waitcnt lgkmcnt(0)\n" \
  "s_barrier\n" \
  "s_cbranch_scc1 .LCA_LAST_%=\n" \
  "ds_read_b32 v11, v1 offset:26688\n" \
  "ds_read_b128 v[40:43], v2 offset:0\n" \
  "ds_read_b128 v[44:47], v3 offset:0\n" \
  "ds_read_b128 v[48:51], v2 offset:64\n" \
  "ds_read_b128 v[52:55], v3 offset:64\n" \
  "ds_read_b128 v[56:59], v2 offset:128\n" \
  "ds_read_b128 v[60:63], v3 offset:128\n" \
  "s_waitcnt lgkmcnt(5)\n" \
  "v_mfma_f32_16x16x32_bf16 v[12:15], v[24:27], v[40:43], 0\n" \
  "v_mfma_f32_16x16x32_bf16 v[16:19], v[28:31], v[40:43], 0\n" \
  "v_readfirstlane_b32 s86, v11\n" \
  "ds_read_b128 v[24:27], v1 offset:12288\n" \
  "ds_read_b128 v[28:31], v1 offset:16384\n" \
  "s_waitcnt lgkmcnt(6)\n" \
  "v_mfma_f32_16x16x32_bf16 v[20:23], v[44:47], v[40:43], 0\n" \
  "s_lshr_b32 s86, s86, 23\n" \
  "s_add_i32 s86, s86, s83\n" \
  "ds_read_b128 v[40:43], v2 offset:192\n" \
  "ds_read_b128 v[44:47], v3 offset:192\n" \
  "s_waitcnt lgkmcnt(7)\n" \
  "v_mfma_f32_16x16x32_bf16 v[12:15], v[32:35], v[48:51], v[12:15]\n" \
  "v_mfma_f32_16x16x32_bf16 v[16:19], v[36:39], v[48:51], v[16:19]\n" \
  "s_ashr_i32 s86, s86, 1\n" \
  "s_max_i32 s86, s86, s82\n" \
  "ds_read_b128 v[32:35], v1 offset:13312\n" \
  "ds_read_b128 v[36:39], v1 offset:17408\n" \
  "s_waitcnt lgkmcnt(8)\n" \
  "v_mfma_f32_16x16x32_bf16 v[20:23], v[52:55], v[48:51], v[20:23]\n" \
  "s_min_i32 s86, s86, 100\n" \
  "s_lshl_b32 s86, s86, 23\n" \
  "s_sub_i32 s84, 0x4a800000, s86\n" \
  "ds_read_b128 v[48:51], v10 offset:18432\n" \
  "ds_read_b128 v[52:55], v10 offset:22528\n" \
  "s_waitcnt lgkmcnt(8)\n" \
  "v_mfma_f32_16x16x32_bf16 v[20:23], v[60:63], v[56:59], v[20:23]\n" \
  "s_waitcnt lgkmcnt(6)\n" \
  "v_mfma_f32_16x16x32_bf16 v[12:15], v[24:27], v[56:59], v[12:15]\n" \
  "v_mfma_f32_16x16x32_bf16 v[16:19], v[28:31], v[56:59], v[16:19]\n" \
  "ds_read_b128 v[56:59], v6 offset:0\n" \
  "ds_read_b128 v[60:63], v6 offset:1024\n" \
  "s_waitcnt lgkmcnt(6)\n" \
  "v_mfma_f32_16x16x32_bf16 v[20:23], v[44:47], v[40:43], v[20:23]\n" \
  "ds_read_b128 v[24:27], v6 offset:2048\n" \
  "ds_read_b128 v[28:31], v6 offset:3072\n" \
  "s_waitcnt lgkmcnt(6)\n" \
  "v_mfma_f32_16x16x32_bf16 v[12:15], v[32:35], v[40:43], v[12:15]\n" \
  "v_mfma_f32_16x16x32_bf16 v[16:19], v[36:39], v[40:43], v[16:19]\n" \
  "ds_read_b128 v[32:35], v6 offset:8192\n" \
  "ds_read_b128 v[36:39], v6 offset:9216\n" \
  "s_waitcnt lgkmcnt(6)\n" \
  "v_cndmask_b32 v20, v20, v21, s[76:77]\n" \
  "v_mov_b32 v41, 0\n" \
  "v_mov_b32 v42, 0\n" \
  "s_nop 1\n" \
  "v_cndmask_b32 v12, v12, v16, s[76:77]\n" \
  "v_cndmask_b32 v13, v13, v17, s[76:77]\n" \
  "v_cndmask_b32 v14, v14, v18, s[76:77]\n" \
  "v_cndmask_b32 v15, v15, v19, s[76:77]\n" \
  "v_maximum3_f32 v16, v20, v12, v13\n" \
  "v_maximum3_f32 v16, v16, v14, v15\n" \
  "s_nop 1\n" \
  "v_max_f32_dpp v16, v16, v16 row_ror:8 row_mask:0xf bank_mask:0xf\n" \
  "v_mov_b32 v17, v16\n" \
  "s_nop 1\n" \
  "v_permlane16_swap_b32 v16, v17\n" \
  "v_max_f32 v16, v16, v17\n" \
  "v_mov_b32 v17, v16\n" \
  "s_nop 1\n" \
  "v_permlane32_swap_b32 v16, v17\n" \
  "v_max_f32 v16, v16, v17\n" \
  "v_mul_f32 v16, s78, v16\n" \
  "v_pk_fma_f32 v[12:13], v[12:13], s[78:79], v[16:17] op_sel_hi:[1,0,0] neg_lo:[0,0,1] neg_hi:[0,0,1]\n" \
  "v_pk_fma_f32 v[14:15], v[14:15], s[78:79], v[16:17] op_sel_hi:[1,0,0] neg_lo:[0,0,1] neg_hi:[0,0,1]\n" \
  "v_fma_f32 v20, v20, s78, -v16\n" \
  "v_exp_f32 v12, v12\n" \
  "v_exp_f32 v13, v13\n" \
  "v_exp_f32 v14, v14\n" \
  "v_exp_f32 v15, v15\n" \
  "v_exp_f32 v20, v20\n" \
  "s_barrier\n" \
  "ds_read_b64 v[22:23], v7 offset:0\n" \
  "v_cvt_pk_bf16_f32 v44, v12, v13\n" \
  "v_cvt_pk_bf16_f32 v45, v14, v15\n" \
  "v_pk_add_f32 v[8:9], v[12:13], v[14:15]\n" \
  "v_mov_b32_dpp v46, v44 row_ror:8 row_mask:0xf bank_mask:0xf\n" \
  "v_mov_b32_dpp v17, v20 row_ror:8 row_mask:0xf bank_mask:0xf\n" \
  "v_mov_b32_dpp v47, v45 row_ror:8 row_mask:0xf bank_mask:0xf\n" \
  "v_add_f32 v10, v8, v20\n" \
  "v_cvt_pk_bf16_f32 v40, v20, v17\n" \
  "v_add_f32 v10, v10, v9\n" \
  "v_mfma_f32_16x16x32_bf16 v[12:15], v[48:51], v[44:47], 0\n" \
  "v_cvt_pk_bf16_f32 v43, v20, v17\n" \
  "v_add_f32_dpp v10, v10, v10 row_ror:8 row_mask:0xf bank_mask:0xf\n" \
  "v_mfma_f32_16x16x32_bf16 v[16:19], v[52:55], v[44:47], 0\n" \
  "v_mov_b32 v11, v10\n" \
  "s_waitcnt lgkmcnt(0)\n" \
  "v_mfma_f32_16x16x16_bf16 v[12:15], v[22:23], v[40:41], v[12:15]\n" \
  "v_mfma_f32_16x16x16_bf16 v[16:19], v[22:23], v[42:43], v[16:19]\n" \
  "v_permlane16_swap_b32 v10, v11\n" \
  "v_add_f32 v10, v10, v11\n" \
  "ds_read_b128 v[48:51], v6 offset:10240\n" \
  "v_mov_b32 v11, v10\n" \
  "ds_read_b128 v[52:55], v6 offset:11264\n" \
  "v_mov_b64 v[20:21], 0\n" \
  "v_mov_b64 v[22:23], 0\n" \
  "v_permlane32_swap_b32 v10, v11\n" \
  "v_add_f32 v10, v10, v11\n" \
  "v_rcp_f32 v10, v10\n" \
  "s_nop 0\n" \
  "v_pk_mul_f32 v[12:13], v[12:13], v[10:11] op_sel_hi:[1,0]\n" \
  "v_pk_mul_f32 v[14:15], v[14:15], v[10:11] op_sel_hi:[1,0]\n" \
  "v_pk_mul_f32 v[16:17], v[16:17], v[10:11] op_sel_hi:[1,0]\n" \
  "v_pk_mul_f32 v[18:19], v[18:19], v[10:11] op_sel_hi:[1,0]\n" \
  "v_cvt_pk_bf16_f32 v40, v12, v13\n" \
  "v_cvt_pk_bf16_f32 v41, v14, v15\n" \
  "v_cvt_pk_bf16_f32 v42, v16, v17\n" \
  "ds_write_b64 v4, v[40:41]\n" \
  "v_cvt_pk_bf16_f32 v43, v18, v19\n" \
  "ds_write_b64 v4, v[42:43] offset:1024\n" \
  "v_mov_b64 v[12:13], 0\n" \
  "v_mov_b64 v[14:15], 0\n" \
  "v_mov_b64 v[16:17], 0\n" \
  "v_mov_b64 v[18:19], 0\n" \
  "v_mov_b64 v[8:9], 0\n" \
  "v_mov_b64 v[10:11], 0\n" \
  "s_setprio 0\n" \
  "s_waitcnt lgkmcnt(0)\n" \
  "s_barrier\n" \
  "ds_read_b128 v[40:43], v1 offset:8192\n" \
  "ds_read_b128 v[44:47], v1 offset:9216\n" \
  "s_waitcnt lgkmcnt(1)\n" \
  "v_smfmac_f32_16x16x64_bf16 v[12:15], v[40:43], v[56:63], v0\n" \
  "ds_read_b128 v[56:59], v6 offset:4096\n" \
  "ds_read_b128 v[60:63], v6 offset:5120\n" \
  "v_smfmac_f32_16x16x64_bf16 v[16:19], v[40:43], v[24:31], v0\n" \
  "ds_read_b128 v[24:27], v6 offset:6144\n" \
  "ds_read_b128 v[28:31], v6 offset:7168\n" \
  "s_waitcnt lgkmcnt(4)\n" \
  "v_smfmac_f32_16x16x64_bf16 v[12:15], v[44:47], v[32:39], v0\n" \
  "ds_read_b128 v[32:35], v6 offset:12288\n" \
  "ds_read_b128 v[36:39], v6 offset:13312\n" \
  "v_smfmac_f32_16x16x64_bf16 v[16:19], v[44:47], v[48:55], v0\n" \
  "ds_read_b128 v[48:51], v6 offset:14336\n" \
  "ds_read_b128 v[52:55], v6 offset:15360\n" \
  "s_nop 0\n" \
  "s_waitcnt lgkmcnt(6)\n" \
  "v_smfmac_f32_16x16x64_bf16 v[20:23], v[40:43], v[56:63], v0\n" \
  "s_nop 0\n" \
  "s_waitcnt lgkmcnt(4)\n" \
  "v_smfmac_f32_16x16x64_bf16 v[8:11], v[40:43], v[24:31], v0\n" \
  "v_permlane32_swap_b32 v12, v17\n" \
  "v_permlane32_swap_b32 v13, v16\n" \
  "v_permlane32_swap_b32 v14, v19\n" \
  "v_permlane32_swap_b32 v15, v18\n" \
  "s_waitcnt lgkmcnt(2)\n" \
  "v_smfmac_f32_16x16x64_bf16 v[20:23], v[44:47], v[32:39], v0\n" \
  "v_pk_add_f32 v[12:13], v[12:13], v[16:17] op_sel:[0,1] op_sel_hi:[1,0]\n" \
  "v_pk_add_f32 v[14:15], v[14:15], v[18:19] op_sel:[0,1] op_sel_hi:[1,0]\n" \
  "v_pk_fma_f32 v[12:13], v[12:13], s[84:85], s[84:85] op_sel:[0,0,1] op_sel_hi:[1,0,1]\n" \
  "v_pk_fma_f32 v[14:15], v[14:15], s[84:85], s[84:85] op_sel:[0,0,1] op_sel_hi:[1,0,1]\n" \
  "s_waitcnt lgkmcnt(0)\n" \
  "v_smfmac_f32_16x16x64_bf16 v[8:11], v[44:47], v[48:55], v0\n" \
  "v_lshl_add_u32 v12, v12, 6, s91\n" \
  "v_lshl_add_u32 v13, v13, 2, s75\n" \
  "global_atomic_add_x2 v5, v[12:13], s[58:59] offset:0\n" \
  "v_lshl_add_u32 v14, v14, 6, s91\n" \
  "v_lshl_add_u32 v15, v15, 2, s75\n" \
  "global_atomic_add_x2 v5, v[14:15], s[58:59] offset:2048\n" \
  "s_nop 1\n" \
  "v_permlane32_swap_b32 v20, v9\n" \
  "v_permlane32_swap_b32 v21, v8\n" \
  "v_permlane32_swap_b32 v22, v11\n" \
  "v_permlane32_swap_b32 v23, v10\n" \
  "v_pk_add_f32 v[20:21], v[20:21], v[8:9] op_sel:[0,1] op_sel_hi:[1,0]\n" \
  "v_pk_add_f32 v[22:23], v[22:23], v[10:11] op_sel:[0,1] op_sel_hi:[1,0]\n" \
  "v_pk_fma_f32 v[20:21], v[20:21], s[84:85], s[84:85] op_sel:[0,0,1] op_sel_hi:[1,0,1]\n" \
  "v_pk_fma_f32 v[22:23], v[22:23], s[84:85], s[84:85] op_sel:[0,0,1] op_sel_hi:[1,0,1]\n" \
  "v_lshl_add_u32 v20, v20, 6, s91\n" \
  "v_lshl_add_u32 v21, v21, 2, s75\n" \
  "global_atomic_add_x2 v5, v[20:21], s[58:59] offset:3072\n" \
  "v_lshl_add_u32 v22, v22, 6, s91\n" \
  "v_lshl_add_u32 v23, v23, 2, s75\n" \
  "global_atomic_add_x2 v5, v[22:23], s[58:59] offset:1024\n" \
  "buffer_load_dword v52, v5, s[0:3], s66 offen sc1\n" \
  "v_mbcnt_lo_u32_b32 v48, -1, 0\n" \
  "v_mbcnt_hi_u32_b32 v48, -1, v48\n" \
  "v_and_b32 v49, 15, v48\n" \
  "v_lshl_add_u32 v8, v49, 1, s97\n" \
  "v_bfe_u32 v49, v48, 4, 1\n" \
  "v_mad_u32_u24 v8, v49, s92, v8\n" \
  "v_lshrrev_b32 v49, 5, v48\n" \
  "v_mad_u32_u24 v9, v49, s93, v8\n" \
  "v_mad_u32_u24 v8, v49, s94, v8\n" \
  "v_add_u32 v10, s99, v1\n" \
  "s_lshr_b32 s88, s99, 2\n" \
  "s_add_u32 s88, s88, s74\n" \
  "s_add_u32 s88, s88, 139264\n" \
  "v_lshl_add_u32 v7, v49, 10, s88\n" \
  "v_and_b32 v49, 31, v48\n" \
  "v_lshl_add_u32 v7, v49, 3, v7\n" \
  "v_mov_b64 v[12:13], 0\n" \
  "v_mov_b64 v[14:15], 0\n" \
  "v_mov_b64 v[16:17], 0\n" \
  "v_mov_b64 v[18:19], 0\n" \
  "v_mov_b64 v[20:21], 0\n" \
  "v_mov_b64 v[22:23], 0\n" \
  "s_add_u32 s88, s74, 157760\n" \
  "v_mov_b32 v59, s88\n" \
  "v_lshrrev_b32 v49, 3, v48\n" \
  "v_and_b32 v50, 7, v48\n" \
  "v_lshlrev_b32 v50, 4, v50\n" \
  "v_lshl_or_b32 v44, v49, 8, v50\n" \
  "v_and_b32 v50, 3, v48\n" \
  "v_lshl_add_u32 v40, v50, 2, s98\n" \
  "v_lshrrev_b32 v50, 4, v48\n" \
  "v_mad_u32_u24 v40, v50, s95, v40\n" \
  "v_and_b32 v49, 1, v49\n" \
  "v_lshl_add_u32 v40, v49, 6, v40\n" \
  "v_bfe_u32 v49, v48, 2, 1\n" \
  "v_lshl_add_u32 v40, v49, 7, v40\n" \
  "v_xor_b32 v41, 16, v40\n" \
  "v_xor_b32 v42, 32, v40\n" \
  "v_xor_b32 v43, 48, v40\n" \
  "s_add_i32 s80, s86, 0x32800000\n" \
  "s_add_i32 s81, s86, 0x33800000\n" \
  "s_add_i32 s88, s86, 0xb4000000\n" \
  "v_mov_b32 v46, s88\n" \
  "v_mov_b32 v47, 0\n" \
  "v_mov_b32 v11, 0\n" \
  "s_mov_b32 s89, s60\n" \
  "s_xor_b32 s60, s60, 0x80\n" \
  "s_bitcmp0_b32 s60, 7\n" \
  "s_cselect_b32 s88, 0x8000, 0\n" \
  "s_add_u32 s60, s60, s88\n" \
  "s_and_b32 s60, s60, 0xffff\n" \
  "s_add_u32 s58, s56, s60\n" \
  "s_addc_u32 s59, s57, 0\n" \
  "s_sub_u32 s71, s71, 1\n" \
  "s_mov_b32 s87, 0x40000\n" \
  "s_waitcnt vmcnt(0)\n" \
  "s_memrealtime s[38:39]\n" \
  ".LCA_CHK_%=:\n" \
  "v_cmp_eq_u32 vcc, 0, v52\n" \
  "v_readlane_b32 s88, v52, s79\n" \
  "s_cmp_eq_u64 vcc, 0\n" \
  "s_cbranch_scc1 .LCA_OKM_%=\n" \
  "s_sleep 4\n" \
  "buffer_load_dword v52, v5, s[0:3], s66 offen sc1\n" \
  "s_sub_u32 s87, s87, 1\n" \
  "s_cbranch_scc1 .LCA_END_%=\n" \
  "s_waitcnt vmcnt(0)\n" \
  "s_branch .LCA_CHK_%=\n" \
  ".LCA_OKM_%=:\n" \
  "v_subrev_u32 v52, s88, v52\n" \
  "s_nop 1\n" \
  "v_max_i32_dpp v52, v52, v52 row_ror:1 row_mask:0xf bank_mask:0xf\n" \
  "s_nop 1\n" \
  "v_max_i32_dpp v52, v52, v52 row_ror:2 row_mask:0xf bank_mask:0xf\n" \
  "s_nop 1\n" \
  "v_max_i32_dpp v52, v52, v52 row_ror:4 row_mask:0xf bank_mask:0xf\n" \
  "s_add_u32 s66, s66, 1024\n" \
  "v_readlane_b32 vcc_lo, v52, 0\n" \
  "v_readlane_b32 s88, v52, 16\n" \
  "s_max_i32 vcc_lo, vcc_lo, s88\n" \
  "v_readlane_b32 s88, v52, 32\n" \
  "s_max_i32 vcc_lo, vcc_lo, s88\n" \
  "v_readlane_b32 s88, v52, 48\n" \
  "s_max_i32 vcc_lo, vcc_lo, s88\n" \
  "s_waitcnt lgkmcnt(0)\n" \
  "s_add_u32 vcc_lo, vcc_lo, s38\n" \
  "s_add_u32 vcc_lo, vcc_lo, -6\n" \
  ".LCA_TW_%=:\n" \
  "s_memrealtime s[38:39]\n" \
  "s_waitcnt lgkmcnt(0)\n" \
  "s_sub_u32 s38, s38, vcc_lo\n" \
  "s_cmp_lt_i32 s38, 0\n" \
  "s_cbranch_scc1 .LCA_TW_%=\n" \
  ".LCA_POLL_%=:\n" \
  "buffer_load_dwordx4 v[24:27], v44, s[40:43], s89 offen sc1\n" \
  "buffer_load_dwordx4 v[28:31], v44, s[40:43], s89 offen offset:2048 sc1\n" \
  "s_sub_u32 s87, s87, 1\n" \
  "s_cbranch_scc1 .LCA_END_%=\n" \
  "s_waitcnt vmcnt(1)\n" \
  "v_bitop3_b32 v45, v24, v26, 32 bitop3:0x80\n" \
  "v_alignbit_b32 v32, v25, v24, 2\n" \
  "v_alignbit_b32 v34, v27, v26, 2\n" \
  "s_waitcnt vmcnt(0)\n" \
  "v_bitop3_b32 v45, v45, v28, v30 bitop3:0x80\n" \
  "v_cmp_eq_u32 vcc, 0, v45\n" \
  "v_cvt_f32_i32 v32, v32\n" \
  "v_cvt_f32_i32 v33, v25\n" \
  "s_cmp_eq_u64 vcc, 0\n" \
  "s_cbranch_scc0 .LCA_POLL_%=\n" \
  "v_cvt_f32_i32 v34, v34\n" \
  "v_cvt_f32_i32 v35, v27\n" \
  "v_pk_fma_f32 v[32:33], v[32:33], s[80:81], v[46:47]\n" \
  "v_alignbit_b32 v36, v29, v28, 2\n" \
  "v_pk_fma_f32 v[34:35], v[34:35], s[80:81], v[46:47]\n" \
  "v_alignbit_b32 v38, v31, v30, 2\n" \
  "v_cvt_pk_bf16_f32 v32, v32, v34\n" \
  "v_cvt_pk_bf16_f32 v33, v33, v35\n" \
  "v_cvt_f32_i32 v36, v36\n" \
  "ds_write_b32 v40, v32\n" \
  "v_cvt_f32_i32 v37, v29\n" \
  "ds_write_b32 v41, v33\n" \
  "v_cvt_f32_i32 v38, v38\n" \
  "v_cvt_f32_i32 v39, v31\n" \
  "v_pk_fma_f32 v[36:37], v[36:37], s[80:81], v[46:47]\n" \
  "v_pk_fma_f32 v[38:39], v[38:39], s[80:81], v[46:47]\n" \
  "v_dot2c_f32_bf16 v11, v32, v32\n" \
  "v_cvt_pk_bf16_f32 v36, v36, v38\n" \
  "v_cvt_pk_bf16_f32 v37, v37, v39\n" \
  "ds_write_b32 v42, v36\n" \
  "ds_write_b32 v43, v37\n" \
  "v_dot2c_f32_bf16 v11, v33, v33\n" \
  "v_dot2c_f32_bf16 v11, v36, v36\n" \
  "v_dot2c_f32_bf16 v11, v37, v37\n" \
  "s_nop 1\n" \
  "s_nop 0\n" \
  "v_cmpx_lt_f32 vcc, 2.0, v11\n" \
  "ds_max_u32 v59, v11\n" \
  "s_mov_b64 exec, -1\n" \
  "s_branch .LCA_TOP_%=\n" \
  ".LCA_LAST_%=:\n" \
  ".LCA_END_%=:\n" \
  "s_endpgm\n"
#define CHAIN_ASM_VO \
  "s_mov_b32 s92, 1152\n" \
  "s_mov_b32 s93, 576\n" \
  "s_mov_b32 s94, 2272\n" \
  "s_mov_b32 s95, 272\n" \
  "s_mov_b32 s96, 288\n" \
  "s_mov_b32 s91, 0x40000001\n" \
  "s_mov_b32 s84, 0x4b400000\n" \
  "s_mov_b32 s85, 0x4b400000\n" \
  "s_mov_b32 s78, 0x3e0293ee\n" \
  "s_mov_b32 s76, 0xff00ff00\n" \
  "s_mov_b32 s77, 0xff00ff00\n" \
  "s_add_u32 s58, s56, s60\n" \
  "s_addc_u32 s59, s57, 0\n" \
  "s_lshl_b32 s98, s73, 10\n" \
  "s_add_u32 s98, s98, s74\n" \
  "s_add_u32 s98, s98, 131072\n" \
  "s_lshl_b32 s97, s73, 5\n" \
  "s_add_u32 s97, s97, s74\n" \
  "s_add_u32 s97, s97, 160992\n" \
  "s_lshl_b32 s99, s73, 11\n" \
  "s_add_u32 s99, s99, s74\n" \
  "s_add_u32 s99, s99, 133120\n" \
  "v_mbcnt_lo_u32_b32 v48, -1, 0\n" \
  "v_mbcnt_hi_u32_b32 v48, -1, v48\n" \
  "s_sub_u32 s88, s73, 4\n" \
  "v_and_b32 v51, 15, v48\n" \
  "v_lshl_add_u32 v4, v51, 1, s97\n" \
  "v_bfe_u32 v49, v48, 4, 1\n" \
  "v_mad_u32_u24 v4, v49, s92, v4\n" \
  "v_lshrrev_b32 v50, 5, v48\n" \
  "v_mad_u32_u24 v4, v50, s93, v4\n" \
  "v_lshlrev_b32 v8, 4, v49\n" \
  "v_lshl_add_u32 v8, v51, 5, v8\n" \
  "v_lshl_add_u32 v8, v50, 3, v8\n" \
  "s_lshl_b32 s89, s88, 8\n" \
  "s_add_u32 s89, s89, s74\n" \
  "s_add_u32 s89, s89, 139264\n" \
  "v_and_b32 v49, 31, v48\n" \
  "v_lshl_add_u32 v5, v49, 3, s89\n" \
  "v_lshl_add_u32 v5, v50, 10, v5\n" \
  "v_lshrrev_b32 v49, 2, v51\n" \
  "v_and_b32 v50, 5, v51\n" \
  "v_and_or_b32 v49, v49, 2, v50\n" \
  "s_lshl_b32 s89, s88, 6\n" \
  "s_add_u32 s89, s89, s74\n" \
  "s_add_u32 s89, s89, 160992\n" \
  "v_and_b32 v50, 48, v48\n" \
  "v_add_u32 v6, s89, v50\n" \
  "v_mad_u32_u24 v6, v49, s96, v6\n" \
  "v_lshlrev_b32 v7, 3, v50\n" \
  "v_lshl_add_u32 v7, v49, 4, v7\n" \
  "v_and_b32 v49, 2, v51\n" \
  "v_lshl_add_u32 v7, v49, 29, v7\n" \
  "v_mov_b32 v9, 0\n" \
  "v_lshlrev_b32 v10, 4, v48\n" \
  "v_mov_b64 v[12:13], 0\n" \
  "v_mov_b64 v[14:15], 0\n" \
  "v_mov_b64 v[16:17], 0\n" \
  "v_mov_b64 v[18:19], 0\n" \
  "v_mov_b64 v[20:21], 0\n" \
  "v_mov_b64 v[22:23], 0\n" \
  "s_add_u32 s88, s74, 157760\n" \
  "v_mov_b32 v59, s88\n" \
  "v_mov_b32 v24, 0x44870000\n" \
  "v_mul_f32 v24, s72, v24\n" \
  "s_nop 0\n" \
  "v_readfirstlane_b32 s86, v24\n" \
  "s_lshr_b32 s86, s86, 23\n" \
  "s_addk_i32 s86, 0xff83\n" \
  "s_ashr_i32 s86, s86, 1\n" \
  "s_max_i32 s86, s86, -20\n" \
  "s_min_i32 s86, s86, 100\n" \
  "s_mov_b32 s82, s86\n" \
  "s_lshr_b32 s83, s72, 23\n" \
  "s_sub_i32 s83, s83, 242\n" \
  "s_lshl_b32 s88, s73, 10\n" \
  "v_add_u32 v24, s88, v1\n" \
  "ds_read_b128 v[32:35], v24 offset:0\n" \
  "s_waitcnt lgkmcnt(0)\n" \
  "v_mov_b32 v11, 0\n" \
  "v_dot2c_f32_bf16 v11, v32, v32\n" \
  "v_dot2c_f32_bf16 v11, v33, v33\n" \
  "v_dot2c_f32_bf16 v11, v34, v34\n" \
  "v_dot2c_f32_bf16 v11, v35, v35\n" \
  "s_nop 1\n" \
  "s_nop 0\n" \
  "v_cmpx_lt_f32 vcc, 2.0, v11\n" \
  "ds_max_u32 v59, v11\n" \
  "s_mov_b64 exec, -1\n" \
  ".LCV_TOP_%=:\n" \
  "s_waitcnt lgkmcnt(0)\n" \
  "s_barrier\n" \
  "s_setprio 3\n" \
  "ds_read_b128 v[24:27], v1 offset:0\n" \
  "ds_read_b128 v[28:31], v1 offset:1024\n" \
  "ds_read_b128 v[32:35], v1 offset:2048\n" \
  "ds_read_b128 v[36:39], v1 offset:3072\n" \
  "ds_read_b128 v[40:43], v1 offset:4096\n" \
  "ds_read_b128 v[44:47], v1 offset:5120\n" \
  "ds_read_b128 v[48:51], v1 offset:6144\n" \
  "ds_read_b128 v[52:55], v1 offset:7168\n" \
  "buffer_load_dword v58, v9, s[44:47], s68 offen sc1\n" \
  "s_waitcnt lgkmcnt(7)\n" \
  "v_smfmac_f32_16x16x64_bf16 v[12:15], v[24:27], v[64:71], v0\n" \
  "s_waitcnt lgkmcnt(6)\n" \
  "v_smfmac_f32_16x16x64_bf16 v[12:15], v[28:31], v[72:79], v0\n" \
  "s_waitcnt lgkmcnt(5)\n" \
  "v_smfmac_f32_16x16x64_bf16 v[12:15], v[32:35], v[80:87], v0\n" \
  "s_waitcnt lgkmcnt(4)\n" \
  "v_smfmac_f32_16x16x64_bf16 v[12:15], v[36:39], v[88:95], v0\n" \
  "s_waitcnt lgkmcnt(3)\n" \
  "v_smfmac_f32_16x16x64_bf16 v[12:15], v[40:43], v[96:103], v0\n" \
  "s_waitcnt lgkmcnt(2)\n" \
  "v_smfmac_f32_16x16x64_bf16 v[12:15], v[44:47], v[104:111], v0\n" \
  "s_waitcnt lgkmcnt(1)\n" \
  "v_smfmac_f32_16x16x64_bf16 v[12:15], v[48:51], v[112:119], v0\n" \
  "s_waitcnt lgkmcnt(0)\n" \
  "v_smfmac_f32_16x16x64_bf16 v[12:15], v[52:55], v[120:127], v0\n" \
  "s_memrealtime s[38:39]\n" \
  "ds_read_b32 v11, v1 offset:26688\n" \
  "s_nop 6\n" \
  "v_permlane32_swap_b32 v12, v14\n" \
  "v_permlane32_swap_b32 v13, v15\n" \
  "v_pk_add_f32 v[12:13], v[12:13], v[14:15]\n" \
  "s_nop 0\n" \
  "v_cvt_pk_bf16_f32 v12, v12, v13\n" \
  "ds_write_b16 v4, v12\n" \
  "ds_write_b16_d16_hi v4, v12 offset:288\n" \
  "s_setprio 0\n" \
  "v_mov_b64 v[12:13], 0\n" \
  "v_mov_b64 v[14:15], 0\n" \
  "s_waitcnt lgkmcnt(2)\n" \
  "v_readfirstlane_b32 s86, v11\n" \
  "s_lshr_b32 s86, s86, 23\n" \
  "s_add_i32 s86, s86, s83\n" \
  "s_ashr_i32 s86, s86, 1\n" \
  "s_max_i32 s86, s86, s82\n" \
  "s_min_i32 s86, s86, 100\n" \
  "s_lshl_b32 s86, s86, 23\n" \
  "s_sub_i32 s84, 0x4a800000, s86\n" \
  "s_cmp_eq_u32 s71, 0\n" \
  "s_waitcnt lgkmcnt(0)\n" \
  "s_barrier\n" \
  "ds_read_b128 v[60:63], v6 offset:0\n" \
  "v_smfmac_f32_16x16x64_bf16 v[16:19], v[24:27], v[128:135], v0\n" \
  "v_or_b32 v57, 1, s38\n" \
  "v_smfmac_f32_16x16x64_bf16 v[20:23], v[24:27], v[192:199], v0\n" \
  "v_add_u32 v56, s67, v10\n" \
  "v_smfmac_f32_16x16x64_bf16 v[16:19], v[28:31], v[136:143], v0\n" \
  "s_mov_b64 exec, 0x11\n" \
  "buffer_store_dword v57, v56, s[0:3], 0 offen sc1\n" \
  "s_mov_b64 exec, -1\n" \
  "v_smfmac_f32_16x16x64_bf16 v[20:23], v[28:31], v[200:207], v0\n" \
  "v_smfmac_f32_16x16x64_bf16 v[16:19], v[32:35], v[144:151], v0\n" \
  "s_waitcnt lgkmcnt(0)\n" \
  "v_max_u32 v60, 1, v60\n" \
  "v_smfmac_f32_16x16x64_bf16 v[20:23], v[32:35], v[208:215], v0\n" \
  "v_max_u32 v61, 1, v61\n" \
  "v_smfmac_f32_16x16x64_bf16 v[16:19], v[36:39], v[152:159], v0\n" \
  "v_max_u32 v62, 1, v62\n" \
  "v_smfmac_f32_16x16x64_bf16 v[20:23], v[36:39], v[216:223], v0\n" \
  "v_max_u32 v63, 1, v63\n" \
  "v_smfmac_f32_16x16x64_bf16 v[16:19], v[40:43], v[160:167], v0\n" \
  "buffer_store_dwordx4 v[60:63], v7, s[48:51], s69 offen sc1\n" \
  "v_smfmac_f32_16x16x64_bf16 v[20:23], v[40:43], v[224:231], v0\n" \
  "v_smfmac_f32_16x16x64_bf16 v[16:19], v[44:47], v[168:175], v0\n" \
  "v_smfmac_f32_16x16x64_bf16 v[20:23], v[44:47], v[232:239], v0\n" \
  "v_smfmac_f32_16x16x64_bf16 v[16:19], v[48:51], v[176:183], v0\n" \
  "v_smfmac_f32_16x16x64_bf16 v[20:23], v[48:51], v[240:247], v0\n" \
  "v_smfmac_f32_16x16x64_bf16 v[16:19], v[52:55], v[184:191], v0\n" \
  "v_smfmac_f32_16x16x64_bf16 v[20:23], v[52:55], v[248:255], v0\n" \
  "ds_read_b128 v[24:27], v3 offset:0\n" \
  "ds_read_b128 v[28:31], v3 offset:1024\n" \
  "ds_read_b128 v[32:35], v3 offset:2048\n" \
  "ds_read_b128 v[36:39], v3 offset:3072\n" \
  "ds_read_b128 v[40:43], v3 offset:8192\n" \
  "ds_read_b128 v[44:47], v3 offset:9216\n" \
  "s_nop 0\n" \
  "v_permlane32_swap_b32 v16, v18\n" \
  "v_permlane32_swap_b32 v17, v19\n" \
  "v_permlane32_swap_b32 v20, v22\n" \
  "v_permlane32_swap_b32 v21, v23\n" \
  "v_pk_add_f32 v[16:17], v[16:17], v[18:19]\n" \
  "v_pk_add_f32 v[20:21], v[20:21], v[22:23]\n" \
  "v_cvt_pk_bf16_f32 v16, v16, v17\n" \
  "v_cvt_pk_bf16_f32 v17, v20, v21\n" \
  "ds_write_b64 v5, v[16:17]\n" \
  "v_max_u32 v20, 1, v16\n" \
  "v_max_u32 v21, 1, v17\n" \
  "s_nop 1\n" \
  "v_permlane32_swap_b32 v20, v21\n" \
  "buffer_store_dwordx2 v[20:21], v8, s[52:55], s70 offen sc1\n" \
  "s_cbranch_scc1 .LCV_LAST_%=\n" \
  "s_waitcnt vmcnt(3)\n" \
  "v_cmp_eq_u32 vcc, 0, v58\n" \
  "s_cmp_eq_u64 vcc, 0\n" \
  "s_cselect_b32 s90, 1, 0\n" \
  "s_waitcnt lgkmcnt(0)\n" \
  "s_barrier\n" \
  "s_cbranch_scc0 .LCV_NOTOK_%=\n" \
  "s_mov_b32 m0, s99\n" \
  "s_add_u32 s88, s99, 8192\n" \
  "global_load_lds_dwordx4 v10, s[62:63] sc1\n" \
  "global_load_lds_dwordx4 v10, s[62:63] offset:1024 sc1\n" \
  "s_mov_b32 m0, s88\n" \
  "s_nop 0\n" \
  "global_load_lds_dwordx4 v10, s[64:65] sc1\n" \
  "global_load_lds_dwordx4 v10, s[64:65] offset:1024 sc1\n" \
  ".LCV_NOTOK_%=:\n" \
  "s_add_u32 s67, s67, 1024\n" \
  "ds_write_b32 v59, v12\n" \
  "ds_read_b128 v[48:51], v3 offset:10240\n" \
  "ds_read_b128 v[52:55], v3 offset:11264\n" \
  "v_mov_b64 v[18:19], 0\n" \
  "v_mov_b64 v[20:21], 0\n" \
  "v_mov_b64 v[22:23], 0\n" \
  "v_mov_b64 v[16:17], 0\n" \
  "ds_read_b128 v[56:59], v3 offset:4096\n" \
  "ds_read_b128 v[60:63], v3 offset:5120\n" \
  "s_waitcnt lgkmcnt(0)\n" \
  "s_barrier\n" \
  "ds_read_b128 v[4:7], v1 offset:8192\n" \
  "ds_read_b128 v[8:11], v1 offset:9216\n" \
  "s_waitcnt lgkmcnt(1)\n" \
  "v_smfmac_f32_16x16x64_bf16 v[12:15], v[4:7], v[24:31], v0\n" \
  "ds_read_b128 v[24:27], v3 offset:6144\n" \
  "ds_read_b128 v[28:31], v3 offset:7168\n" \
  "v_smfmac_f32_16x16x64_bf16 v[16:19], v[4:7], v[32:39], v0\n" \
  "ds_read_b128 v[32:35], v3 offset:12288\n" \
  "ds_read_b128 v[36:39], v3 offset:13312\n" \
  "s_waitcnt lgkmcnt(4)\n" \
  "v_smfmac_f32_16x16x64_bf16 v[12:15], v[8:11], v[40:47], v0\n" \
  "v_smfmac_f32_16x16x64_bf16 v[16:19], v[8:11], v[48:55], v0\n" \
  "v_smfmac_f32_16x16x64_bf16 v[20:23], v[4:7], v[56:63], v0\n" \
  "ds_read_b128 v[56:59], v3 offset:14336\n" \
  "ds_read_b128 v[60:63], v3 offset:15360\n" \
  "v_mov_b64 v[40:41], 0\n" \
  "v_mov_b64 v[42:43], 0\n" \
  "s_waitcnt lgkmcnt(2)\n" \
  "v_smfmac_f32_16x16x64_bf16 v[20:23], v[8:11], v[32:39], v0\n" \
  "s_nop 0\n" \
  "v_permlane32_swap_b32 v12, v17\n" \
  "v_permlane32_swap_b32 v13, v16\n" \
  "v_permlane32_swap_b32 v14, v19\n" \
  "v_permlane32_swap_b32 v15, v18\n" \
  "v_smfmac_f32_16x16x64_bf16 v[40:43], v[4:7], v[24:31], v0\n" \
  "v_pk_add_f32 v[12:13], v[12:13], v[16:17] op_sel:[0,1] op_sel_hi:[1,0]\n" \
  "v_pk_add_f32 v[14:15], v[14:15], v[18:19] op_sel:[0,1] op_sel_hi:[1,0]\n" \
  "v_pk_fma_f32 v[12:13], v[12:13], s[84:85], s[84:85] op_sel:[0,0,1] op_sel_hi:[1,0,1]\n" \
  "v_pk_fma_f32 v[14:15], v[14:15], s[84:85], s[84:85] op_sel:[0,0,1] op_sel_hi:[1,0,1]\n" \
  "s_waitcnt lgkmcnt(0)\n" \
  "v_smfmac_f32_16x16x64_bf16 v[40:43], v[8:11], v[56:63], v0\n" \
  "v_lshl_add_u32 v12, v12, 6, s91\n" \
  "v_lshl_add_u32 v13, v13, 2, s75\n" \
  "global_atomic_add_x2 v2, v[12:13], s[58:59] offset:0\n" \
  "v_lshl_add_u32 v14, v14, 6, s91\n" \
  "v_lshl_add_u32 v15, v15, 2, s75\n" \
  "global_atomic_add_x2 v2, v[14:15], s[58:59] offset:2048\n" \
  "s_nop 1\n" \
  "v_permlane32_swap_b32 v20, v41\n" \
  "v_permlane32_swap_b32 v21, v40\n" \
  "v_permlane32_swap_b32 v22, v43\n" \
  "v_permlane32_swap_b32 v23, v42\n" \
  "v_pk_add_f32 v[20:21], v[20:21], v[40:41] op_sel:[0,1] op_sel_hi:[1,0]\n" \
  "v_pk_add_f32 v[22:23], v[22:23], v[42:43] op_sel:[0,1] op_sel_hi:[1,0]\n" \
  "v_pk_fma_f32 v[20:21], v[20:21], s[84:85], s[84:85] op_sel:[0,0,1] op_sel_hi:[1,0,1]\n" \
  "v_pk_fma_f32 v[22:23], v[22:23], s[84:85], s[84:85] op_sel:[0,0,1] op_sel_hi:[1,0,1]\n" \
  "v_lshl_add_u32 v20, v20, 6, s91\n" \
  "v_lshl_add_u32 v21, v21, 2, s75\n" \
  "global_atomic_add_x2 v2, v[20:21], s[58:59] offset:3072\n" \
  "v_lshl_add_u32 v22, v22, 6, s91\n" \
  "v_lshl_add_u32 v23, v23, 2, s75\n" \
  "global_atomic_add_x2 v2, v[22:23], s[58:59] offset:1024\n" \
  "buffer_load_dword v52, v2, s[0:3], s66 offen sc1\n" \
  "v_mbcnt_lo_u32_b32 v48, -1, 0\n" \
  "v_mbcnt_hi_u32_b32 v48, -1, v48\n" \
  "s_sub_u32 s88, s73, 4\n" \
  "v_and_b32 v51, 15, v48\n" \
  "v_lshl_add_u32 v4, v51, 1, s97\n" \
  "v_bfe_u32 v49, v48, 4, 1\n" \
  "v_mad_u32_u24 v4, v49, s92, v4\n" \
  "v_lshrrev_b32 v50, 5, v48\n" \
  "v_mad_u32_u24 v4, v50, s93, v4\n" \
  "v_lshlrev_b32 v8, 4, v49\n" \
  "v_lshl_add_u32 v8, v51, 5, v8\n" \
  "v_lshl_add_u32 v8, v50, 3, v8\n" \
  "s_lshl_b32 s89, s88, 8\n" \
  "s_add_u32 s89, s89, s74\n" \
  "s_add_u32 s89, s89, 139264\n" \
  "v_and_b32 v49, 31, v48\n" \
  "v_lshl_add_u32 v5, v49, 3, s89\n" \
  "v_lshl_add_u32 v5, v50, 10, v5\n" \
  "v_lshrrev_b32 v49, 2, v51\n" \
  "v_and_b32 v50, 5, v51\n" \
  "v_and_or_b32 v49, v49, 2, v50\n" \
  "s_lshl_b32 s89, s88, 6\n" \
  "s_add_u32 s89, s89, s74\n" \
  "s_add_u32 s89, s89, 160992\n" \
  "v_and_b32 v50, 48, v48\n" \
  "v_add_u32 v6, s89, v50\n" \
  "v_mad_u32_u24 v6, v49, s96, v6\n" \
  "v_lshlrev_b32 v7, 3, v50\n" \
  "v_lshl_add_u32 v7, v49, 4, v7\n" \
  "v_and_b32 v49, 2, v51\n" \
  "v_lshl_add_u32 v7, v49, 29, v7\n" \
  "v_mov_b32 v9, 0\n" \
  "v_lshlrev_b32 v10, 4, v48\n" \
  "v_mov_b64 v[12:13], 0\n" \
  "v_mov_b64 v[14:15], 0\n" \
  "v_mov_b64 v[16:17], 0\n" \
  "v_mov_b64 v[18:19], 0\n" \
  "v_mov_b64 v[20:21], 0\n" \
  "v_mov_b64 v[22:23], 0\n" \
  "s_add_u32 s88, s74, 157760\n" \
  "v_mov_b32 v59, s88\n" \
  "s_cmp_eq_u32 s90, 1\n" \
  "s_cbranch_scc1 .LCV_NQF_%=\n" \
  "buffer_load_dword v58, v9, s[44:47], s68 offen sc1\n" \
  ".LCV_NQF_%=:\n" \
  "v_lshrrev_b32 v49, 3, v48\n" \
  "v_and_b32 v50, 7, v48\n" \
  "v_lshlrev_b32 v50, 4, v50\n" \
  "v_lshl_or_b32 v44, v49, 8, v50\n" \
  "v_and_b32 v50, 3, v48\n" \
  "v_lshl_add_u32 v40, v50, 2, s98\n" \
  "v_lshrrev_b32 v50, 4, v48\n" \
  "v_mad_u32_u24 v40, v50, s95, v40\n" \
  "v_and_b32 v49, 1, v49\n" \
  "v_lshl_add_u32 v40, v49, 6, v40\n" \
  "v_bfe_u32 v49, v48, 2, 1\n" \
  "v_lshl_add_u32 v40, v49, 7, v40\n" \
  "v_xor_b32 v41, 16, v40\n" \
  "v_xor_b32 v42, 32, v40\n" \
  "v_xor_b32 v43, 48, v40\n" \
  "s_add_i32 s80, s86, 0x32800000\n" \
  "s_add_i32 s81, s86, 0x33800000\n" \
  "s_add_i32 s88, s86, 0xb4000000\n" \
  "v_mov_b32 v46, s88\n" \
  "v_mov_b32 v47, 0\n" \
  "v_mov_b32 v11, 0\n" \
  "s_mov_b32 s89, s60\n" \
  "s_xor_b32 s60, s60, 0x80\n" \
  "s_bitcmp0_b32 s60, 7\n" \
  "s_cselect_b32 s88, 0x8000, 0\n" \
  "s_add_u32 s60, s60, s88\n" \
  "s_and_b32 s60, s60, 0xffff\n" \
  "s_add_u32 s58, s56, s60\n" \
  "s_addc_u32 s59, s57, 0\n" \
  "s_sub_u32 s71, s71, 1\n" \
  "s_mov_b32 s87, 0x40000\n" \
  "s_waitcnt vmcnt(0)\n" \
  "s_memrealtime s[38:39]\n" \
  "s_cmp_eq_u32 s90, 1\n" \
  "s_cbranch_scc1 .LCV_HAVE_%=\n" \
  ".LCV_LATE_%=:\n" \
  "v_cmp_eq_u32 vcc, 0, v58\n" \
  "s_cmp_eq_u64 vcc, 0\n" \
  "s_cbranch_scc1 .LCV_GOT_%=\n" \
  "s_sleep 12\n" \
  "buffer_load_dword v58, v9, s[44:47], s68 offen sc1\n" \
  "s_sub_u32 s87, s87, 1\n" \
  "s_cbranch_scc1 .LCV_END_%=\n" \
  "s_waitcnt vmcnt(0)\n" \
  "s_branch .LCV_LATE_%=\n" \
  ".LCV_GOT_%=:\n" \
  "s_mov_b32 m0, s99\n" \
  "s_add_u32 s88, s99, 8192\n" \
  "global_load_lds_dwordx4 v10, s[62:63] sc1\n" \
  "global_load_lds_dwordx4 v10, s[62:63] offset:1024 sc1\n" \
  "s_mov_b32 m0, s88\n" \
  "s_nop 0\n" \
  "global_load_lds_dwordx4 v10, s[64:65] sc1\n" \
  "global_load_lds_dwordx4 v10, s[64:65] offset:1024 sc1\n" \
  ".LCV_HAVE_%=:\n" \
  "s_add_u32 s62, s62, 0x80000\n" \
  "s_addc_u32 s63, s63, 0\n" \
  "s_add_u32 s64, s64, 0x40000\n" \
  "s_addc_u32 s65, s65, 0\n" \
  "s_add_u32 s68, s68, 0x1000\n" \
  "s_add_u32 s69, s69, 0x10000\n" \
  "s_add_u32 s70, s70, 0x10000\n" \
  ".LCV_CHK_%=:\n" \
  "v_cmp_eq_u32 vcc, 0, v52\n" \
  "v_readlane_b32 s88, v52, s79\n" \
  "s_cmp_eq_u64 vcc, 0\n" \
  "s_cbranch_scc1 .LCV_OKM_%=\n" \
  "s_sleep 4\n" \
  "buffer_load_dword v52, v2, s[0:3], s66 offen sc1\n" \
  "s_sub_u32 s87, s87, 1\n" \
  "s_cbranch_scc1 .LCV_END_%=\n" \
  "s_waitcnt vmcnt(0)\n" \
  "s_branch .LCV_CHK_%=\n" \
  ".LCV_OKM_%=:\n" \
  "v_subrev_u32 v52, s88, v52\n" \
  "s_nop 1\n" \
  "v_max_i32_dpp v52, v52, v52 row_ror:1 row_mask:0xf bank_mask:0xf\n" \
  "s_nop 1\n" \
  "v_max_i32_dpp v52, v52, v52 row_ror:2 row_mask:0xf bank_mask:0xf\n" \
  "s_nop 1\n" \
  "v_max_i32_dpp v52, v52, v52 row_ror:4 row_mask:0xf bank_mask:0xf\n" \
  "s_add_u32 s66, s66, 1024\n" \
  "v_readlane_b32 vcc_lo, v52, 0\n" \
  "v_readlane_b32 s88, v52, 16\n" \
  "s_max_i32 vcc_lo, vcc_lo, s88\n" \
  "v_readlane_b32 s88, v52, 32\n" \
  "s_max_i32 vcc_lo, vcc_lo, s88\n" \
  "v_readlane_b32 s88, v52, 48\n" \
  "s_max_i32 vcc_lo, vcc_lo, s88\n" \
  "s_waitcnt lgkmcnt(0)\n" \
  "s_add_u32 vcc_lo, vcc_lo, s38\n" \
  "s_add_u32 vcc_lo, vcc_lo, -14\n" \
  ".LCV_TW_%=:\n" \
  "s_memrealtime s[38:39]\n" \
  "s_waitcnt lgkmcnt(0)\n" \
  "s_sub_u32 s38, s38, vcc_lo\n" \
  "s_cmp_lt_i32 s38, 0\n" \
  "s_cbranch_scc1 .LCV_TW_%=\n" \
  ".LCV_POLL_%=:\n" \
  "buffer_load_dwordx4 v[24:27], v44, s[40:43], s89 offen sc1\n" \
  "buffer_load_dwordx4 v[28:31], v44, s[40:43], s89 offen offset:2048 sc1\n" \
  "s_sub_u32 s87, s87, 1\n" \
  "s_cbranch_scc1 .LCV_END_%=\n" \
  "s_waitcnt vmcnt(1)\n" \
  "v_bitop3_b32 v45, v24, v26, 32 bitop3:0x80\n" \
  "v_alignbit_b32 v32, v25, v24, 2\n" \
  "v_alignbit_b32 v34, v27, v26, 2\n" \
  "s_waitcnt vmcnt(0)\n" \
  "v_bitop3_b32 v45, v45, v28, v30 bitop3:0x80\n" \
  "v_cmp_eq_u32 vcc, 0, v45\n" \
  "v_cvt_f32_i32 v32, v32\n" \
  "v_cvt_f32_i32 v33, v25\n" \
  "s_cmp_eq_u64 vcc, 0\n" \
  "s_cbranch_scc0 .LCV_POLL_%=\n" \
  "v_cvt_f32_i32 v34, v34\n" \
  "v_cvt_f32_i32 v35, v27\n" \
  "v_pk_fma_f32 v[32:33], v[32:33], s[80:81], v[46:47]\n" \
  "v_alignbit_b32 v36, v29, v28, 2\n" \
  "v_pk_fma_f32 v[34:35], v[34:35], s[80:81], v[46:47]\n" \
  "v_alignbit_b32 v38, v31, v30, 2\n" \
  "v_cvt_pk_bf16_f32 v32, v32, v34\n" \
  "v_cvt_pk_bf16_f32 v33, v33, v35\n" \
  "v_cvt_f32_i32 v36, v36\n" \
  "ds_write_b32 v40, v32\n" \
  "v_cvt_f32_i32 v37, v29\n" \
  "ds_write_b32 v41, v33\n" \
  "v_cvt_f32_i32 v38, v38\n" \
  "v_cvt_f32_i32 v39, v31\n" \
  "v_pk_fma_f32 v[36:37], v[36:37], s[80:81], v[46:47]\n" \
  "v_pk_fma_f32 v[38:39], v[38:39], s[80:81], v[46:47]\n" \
  "v_dot2c_f32_bf16 v11, v32, v32\n" \
  "v_cvt_pk_bf16_f32 v36, v36, v38\n" \
  "v_cvt_pk_bf16_f32 v37, v37, v39\n" \
  "ds_write_b32 v42, v36\n" \
  "ds_write_b32 v43, v37\n" \
  "v_dot2c_f32_bf16 v11, v33, v33\n" \
  "v_dot2c_f32_bf16 v11, v36, v36\n" \
  "v_dot2c_f32_bf16 v11, v37, v37\n" \
  "s_nop 1\n" \
  "s_nop 0\n" \
  "v_cmpx_lt_f32 vcc, 2.0, v11\n" \
  "ds_max_u32 v59, v11\n" \
  "s_mov_b64 exec, -1\n" \
  "s_branch .LCV_TOP_%=\n" \
  ".LCV_LAST_%=:\n" \
  "s_waitcnt vmcnt(0)\n" \
  ".LCV_END_%=:\n" \
  "s_endpgm\n"
__device__ __forceinline__ v4u mk_srd(const void* p, const unsigned bytes) {
  const unsigned long long a = (unsigned long long)p;
  return v4u{(unsigned)a, (unsigned)(a >> 32) & 0xffffu, bytes, 0x00020000u};
}
template <bool ATT>
__device__ __forceinline__ void role_chain_t(const MegaArgs& A, char* smem, const int L, const int h, const int rep) {
  u16* swo = reinterpret_cast<u16*>(smem);
  const int w = __builtin_amdgcn_readfirstlane(threadIdx.x >> 6);
  char* ws = A.ws;
  unsigned* FL = (unsigned*)(ws + OFF_FL);
  unsigned* TMO = FL + FL_TMO;
#define LANE_IDS()                         \
  const int l = lane_id();                 \
  const int c = l & 15, g = l >> 4;        \
  (void)c; (void)g;
  float* sgm = reinterpret_cast<float*>(smem + C_X);
  float* sgf = sgm + 8;
  unsigned* FQ = FL + FL_FQ + L * 64 * 32 * 32;
  const int wb = (w + ROT_H) & 7;
  v8u w0[8], w1[8], w2[8];
  {
    LANE_IDS();
    const unsigned sb = lds_base(smem);
    const int q = (l >> 2) & 3, p = l & 3;
    const int f = (q & 1) | ((g & 1) << 1) | ((q >> 1) << 2);
    const unsigned ra = sb + (16 * (g >> 1) + 4 * (g & 1) + (q & 1) + 8 * (q >> 1)) * 256 + (w ^ f) * 32 + p * 8;
#define W_READ(W, hf, X)                                                       \
  {                                                                           \
    _Pragma("unroll") for (int t = 0; t < 4; ++t) {                           \
      const unsigned a_ = (ra ^ (X)) + (hf) * 8192 + t * 32768;               \
      const v2u r0 = trd(a_), r1 = trd(a_ + 512), r2 = trd(a_ + 16384), r3 = trd(a_ + 16384 + 512); \
      W[4 * (hf) + t] = v8u{r0.x, r0.y, r1.x, r1.y, r2.x, r2.y, r3.x, r3.y};  \
    }                                                                         \
    _Pragma("unroll") for (int t = 0; t < 4; ++t) asm volatile("" : "+v"(W[4 * (hf) + t])); \
  }
#define W_STAGE(M, hf, VM, NEXT)                                               \
  {                                                                           \
    WAIT_VM(VM);                                                              \
    RAW_BAR();                                                                \
    if ((M) == 0 && ATT) { W_READ(w0, hf, 0); W_READ(w2, hf, 128); }          \
    if ((M) == 1 && ATT) { W_READ(w1, hf, 0); }                               \
    if ((M) == 1 && !ATT) { W_READ(w0, hf, 0); }                              \
    if ((M) == 2 && !ATT) { W_READ(w1, hf, 128); W_READ(w2, hf, 0); }         \
    RAW_BAR();                                                                \
    NEXT;                                                                     \
  }
#define WO_ISSUE(hf)                                                          \
  {                                                                           \
    const char* base_ = (const char*)A.Wo + (size_t)(h * DH + 64 * (hf) + 8 * w) * (DM * 2); \
    _Pragma("unroll") for (int i = 0; i < 8; ++i) {                           \
      const int fd = (i & 1) | (((i >> 2) & 1) << 1) | ((w & 1) << 2);        \
      glds16(base_, (unsigned)(i * 1024 + ((l ^ (fd << 1)) << 4)), sb + w * 16384 + (hf) * 8192 + i * 1024); \
    }                                                                         \
  }
    wqkv_issue<true>(A.Wv, 0, h, w, l, sb);
    wqkv_issue<true>(A.Wv, 1, h, w, l, sb);
    v4u wr[16];
#define WR_LD(i, R) \
  asm volatile("global_load_dwordx4 %0, %1, %2" : "={" R "}"(wr[i]) : "v"(l * 16), "s"((const char*)A.Wo + (size_t)(h * DH + 16 * w + i) * (DM * 2)) : "memory")
    WR_LD(0, "v[64:67]");    WR_LD(1, "v[68:71]");    WR_LD(2, "v[72:75]");    WR_LD(3, "v[76:79]");
    WR_LD(4, "v[80:83]");    WR_LD(5, "v[84:87]");    WR_LD(6, "v[88:91]");    WR_LD(7, "v[92:95]");
    WR_LD(8, "v[96:99]");    WR_LD(9, "v[100:103]");    WR_LD(10, "v[104:107]");    WR_LD(11, "v[108:111]");
    WR_LD(12, "v[112:115]");    WR_LD(13, "v[116:119]");    WR_LD(14, "v[120:123]");    WR_LD(15, "v[124:127]");
#undef WR_LD
    W_STAGE(2, 0, 24, wqkv_issue<true>(A.Wk, 0, h, w, l, sb));
    SST(4);
    W_STAGE(2, 1, 24, wqkv_issue<true>(A.Wk, 1, h, w, l, sb));
    SST(5);
#define DOT2(x, acc) dot2sq(x, acc)
    float* cs = reinterpret_cast<float*>(smem + C_SM);
    {
      float f2 = 0.f;
      if (!ATT) {
#pragma unroll
        for (int t = 0; t < 8; ++t)
#pragma unroll
          for (int e = 0; e < 8; ++e) f2 = DOT2(w2[t][e], DOT2(w1[t][e], f2));
      }
      f2 = wave_sum_dpp(f2);
      if (l == 0) { sgf[w] = f2; sgm[16] = 0.f; }
      WAIT_VM(16);
      f32x4 ca = {}, cb = {};
#pragma unroll
      for (int i = 0; i < 16; ++i) {
        asm volatile("" : "+v"(wr[i]), "+v"(ca), "+v"(cb));
        const f32x4 x = {lo16(wr[i].x), hi16(wr[i].x), lo16(wr[i].y), hi16(wr[i].y)};
        const f32x4 y = {lo16(wr[i].z), hi16(wr[i].z), lo16(wr[i].w), hi16(wr[i].w)};
        ca += x * x;
        cb += y * y;
      }
      *reinterpret_cast<f32x4*>(cs + w * 512 + l * 8) = ca;
      *reinterpret_cast<f32x4*>(cs + w * 512 + l * 8 + 4) = cb;
    }
    W_STAGE(1, 0, 8, wqkv_issue<true>(A.Wq, 0, h, w, l, sb));
    {
      float m2 = 0.f;
#pragma unroll
      for (int i = 0; i < 8; ++i) m2 += cs[i * 512 + w * 64 + l];
      m2 = wave_max_dpp(m2);
      if (l == 0) sgm[w] = m2;
    }
    W_STAGE(1, 1, 8, wqkv_issue<true>(A.Wq, 1, h, w, l, sb));
    if (w == 0) {
      float m2 = 0.f, f2 = 0.f;
#pragma unroll
      for (int i = 0; i < 8; ++i) { m2 = fmaxf(m2, sgm[i]); f2 += sgf[i]; }
      __builtin_amdgcn_raw_buffer_store_b32(__float_as_uint(sqrtf(m2 * f2) * 1.001f) | 1u, MKRS(FL + FL_G + L * 8 * 32, 8 * 128), l * 128 + h * 4, 0, AUX_SC1);
    }
    SST(6);
    W_STAGE(0, 0, 8, WO_ISSUE(0));
    W_STAGE(0, 1, 8, WO_ISSUE(1));
    SST(7);
#undef W_STAGE
#undef W_READ
#undef WO_ISSUE
#undef DOT2
    const unsigned rb = sb + (2 * (g >> 1) + (q >> 1)) * 16384 + (4 * (g & 1) + (q & 1)) * 1024 + p * 8;
#pragma unroll
    for (int hf = 0; hf < 2; ++hf) {
      if (hf == 0) WAIT_VM(8); else WAIT_VM(0);
      RAW_BAR();
      v4u fr[8];
#pragma unroll
      for (int i = 0; i < 8; ++i) {
        const unsigned a_ = rb + hf * 8192 + (i & 1) * 65536 + ((4 * wb + (i >> 1)) ^ f) * 32;
        const v2u r0 = trd(a_), r1 = trd(a_ + 2048);
        fr[i] = v4u{r0.x, r0.y, r1.x, r1.y};
      }
#pragma unroll
      for (int i = 0; i < 8; ++i) asm volatile("" : "+v"(fr[i]));
      RAW_BAR();
#pragma unroll
      for (int i = 0; i < 8; ++i) *reinterpret_cast<v4u*>(swo + w * 8192 + hf * 4096 + i * 512 + l * 8) = fr[i];
    }
  }
  WSTAMP2();
  v4u m0r;
  {
    LANE_IDS();
    m0r = *reinterpret_cast<const v4u*>(A.mem0 + (c & 7) * DM + 64 * w + 16 * g + 8 * (c >> 3));
  }
  const unsigned sb = lds_base(smem);
  const char* gK = ws + OFF_QK + L * SZ_QK + ((size_t)h * 16 + 2 * w) * 1024;
  const char* gV = ws + OFF_VF + L * SZ_O + ((size_t)h * 8 + 2 * w - 8) * 1024;
  float gw2;
  {
    const unsigned* gp = FL + FL_G + (L * 8 + (blockIdx.x & 7)) * 32;
    unsigned gv;
    for (unsigned spins = 0;; ++spins) {
      gv = __hip_atomic_load((gu32*)gp + (lane_id() & 31), RLX_AGENT);
      if (__all(gv != 0u)) break;
      if (spin_timeout(spins, TMO)) return;
      __builtin_amdgcn_s_sleep(4);
    }
    const float gm = wave_max_dpp(__uint_as_float(gv));
    asm volatile("s_nop 0\n\tv_readfirstlane_b32 %0, %1" : "=s"(gw2) : "v"(gm * gm * 1.06f));
  }
  v8u pv;
  {
    LANE_IDS();
    *reinterpret_cast<v4u*>(smem + C_SM + w * 1024 + l * 16) = m0r;
    const unsigned voff = g * 256 + c * 8, wp = sb + w * 16384 + l * 16;
    pv[0] = (c & 8) ? 0xEEEE : 0x4444;
    pv[1] = sb + C_SM + l * 16;
    pv[2] = ATT ? sb + C_SQ + ((c & 7) * QS + 8 * g) * 2 : voff;
    pv[3] = ATT ? sb + C_SK + ((4 * ((c >> 2) & 1) + 2 * (c >> 3) + (c & 1)) * QS + 8 * g) * 2 : wp;
    pv[4] = sb + (c < 8 ? C_SO : C_SQ) + w * 256 + ((c & 7) + 8 * (g >> 1)) * 16 + 8 * (g & 1);
    pv[5] = voff;
    pv[6] = wp;
    pv[7] = sb + C_SO + (l >> 5) * 1024 + w * 256 + (l & 31) * 8;
  }
  if (!ATT) {
    if (!wait_n<4, 1>(FQ + h * 32, 1, TMO)) return;
    const unsigned lo = lane_id() * 16, d = sb + C_KT + (2 * w - 8) * 1024;
    glds16c(gK, lo, d);
    glds16c(gK + 1024, lo, d + 1024);
    glds16c(gV, lo, d + 8192);
    glds16c(gV + 1024, lo, d + 9216);
  }
  DRAIN();
  SST(8);
  const v4u rsA = mk_srd(ws + OFF_ACC + L * SZ_ACC, SZ_ACC);
  const v4u rsF = mk_srd(FQ, 64 * 32 * 32 * 4);
  const v4u rsMK = mk_srd(ws + OFF_MEMK + L * SZ_MEM, SZ_MEM);
  const v4u rsMV = mk_srd(ws + OFF_MEMV + L * SZ_MEM, SZ_MEM);
  const char* pAB = ws + OFF_ACC + L * SZ_ACC + (wb - w) * 4096;
  const char* pGK = gK + NH * 16 * 1024;
  const char* pGV = gV + NH * 8 * 1024;
  const v4u rsT = mk_srd(FL + FL_FK + L * 32, 64 * 1024 - 128);
  unsigned sc[8];
  sc[0] = (32 + h) * 128;
  sc[1] = (h * 4 + w - 4) * 512;
  sc[2] = (h * 4 + w - 4) * 512;
  sc[3] = NSEG - 1;
  sc[4] = __float_as_uint(gw2);
  sc[5] = w;
  sc[6] = sb;
  sc[7] = h == 0 ? 0xd4000000u - 2u - (1u << 29) : 0xd4000000u;
  const char* dbg = ws;
#define CHAIN_OPS                                                                                                          \
  ::"{v[64:71]}"(w0[0]), "{v[72:79]}"(w0[1]), "{v[80:87]}"(w0[2]), "{v[88:95]}"(w0[3]), "{v[96:103]}"(w0[4]),              \
      "{v[104:111]}"(w0[5]), "{v[112:119]}"(w0[6]), "{v[120:127]}"(w0[7]), "{v[128:135]}"(w1[0]), "{v[136:143]}"(w1[1]),    \
      "{v[144:151]}"(w1[2]), "{v[152:159]}"(w1[3]), "{v[160:167]}"(w1[4]), "{v[168:175]}"(w1[5]), "{v[176:183]}"(w1[6]),    \
      "{v[184:191]}"(w1[7]), "{v[192:199]}"(w2[0]), "{v[200:207]}"(w2[1]), "{v[208:215]}"(w2[2]), "{v[216:223]}"(w2[3]),    \
      "{v[224:231]}"(w2[4]), "{v[232:239]}"(w2[5]), "{v[240:247]}"(w2[6]), "{v[248:255]}"(w2[7]), "{v[0:7]}"(pv),          \
      "{s[40:43]}"(rsA), "{s[44:47]}"(rsF), "{s[48:51]}"(rsMK), "{s[52:55]}"(rsMV), "{s[56:57]}"(pAB), "{s60}"(w * 4096), "{s61}"(h * 1024), \
      "{s[62:63]}"(pGK), "{s[64:65]}"(pGV), "{s66}"(0), "{s67}"(w == 4 ? (h >> 3) * 256 + (h & 7) * 8 : 0x40000000), "{s68}"(sc[0]), "{s69}"(sc[1]), "{s70}"(sc[2]),    \
      "{s71}"(sc[3]), "{s72}"(sc[4]), "{s73}"(sc[5]), "{s74}"(sc[6]), "{s75}"(sc[7]), "{s[36:37]}"(dbg),                   \
      "{s[0:3]}"(rsT), "{s79}"((h >> 3) * 16 + (h & 7))                                                                        \
      : "memory", "vcc", "scc", "s99", "v63"
  if (ATT) asm volatile("s_mov_b64 exec, -1\ns_nop 4\n" CHAIN_ASM_ATT CHAIN_OPS);
  else asm volatile("s_mov_b64 exec, -1\ns_nop 4\n" CHAIN_ASM_VO CHAIN_OPS);
#undef CHAIN_OPS
#undef LANE_IDS
}

__device__ __forceinline__ void role_chain(const MegaArgs& A, char* smem, const int L, const int h, const int rep) {
  if (__builtin_amdgcn_readfirstlane(threadIdx.x >> 6) < 4) role_chain_t<true>(A, smem, L, h, rep);
  else role_chain_t<false>(A, smem, L, h, rep);
}

constexpr int AW_SO = 0, AW_TK = 32768, AW_CNT = AW_TK + 98304;
__device__ __forceinline__ unsigned umin3(unsigned a, unsigned b, unsigned c) {
  return __builtin_elementwise_min(__builtin_elementwise_min(a, b), c);
}
#define AW_ASM_X \
  "s_mov_b32 s61, 0\n" \
  "s_mov_b32 s67, 1\n" \
  "s_mov_b32 s68, 0\n" \
  "s_mov_b32 s70, 0\n" \
  "s_mov_b32 s73, 0\n" \
  "s_mov_b32 s71, 0x3e0293ee\n" \
  "s_mov_b32 s72, 0xbe0293ee\n" \
  "s_mov_b32 s65, 49152\n" \
  "s_mov_b32 s66, 16384\n" \
  "v_mov_b32 v38, s58\n" \
  "s_memrealtime s[38:39]\n" \
  "s_waitcnt lgkmcnt(0)\n" \
  "s_mov_b32 s69, s38\n" \
  "s_setprio 3\n" \
  ".LCX_TOP_%=:\n" \
  "s_mov_b32 s62, 0x200000\n" \
  "s_cmp_ge_u32 s73, s67\n" \
  "s_cbranch_scc1 .LCX_TOK_%=\n" \
  "s_cmp_eq_u32 s61, 0\n" \
  "s_cbranch_scc1 .LCX_SPIN_%=\n" \
  "s_waitcnt vmcnt(0)\n" \
  "v_mov_b32 v46, s59\n" \
  "v_mov_b32 v47, 1\n" \
  "s_mov_b64 exec, 1\n" \
  "ds_add_rtn_u32 v46, v46, v47\n" \
  "s_mov_b64 exec, -1\n" \
  "s_xor_b32 s59, s59, 4\n" \
  "s_mov_b32 s61, 0\n" \
  "s_waitcnt lgkmcnt(0)\n" \
  "v_readfirstlane_b32 s63, v46\n" \
  "v_lshlrev_b32 v46, 5, v5\n" \
  "s_and_b32 s63, s63, 7\n" \
  "s_cmp_eq_u32 s63, 7\n" \
  "s_cbranch_scc0 .LCX_W1_%=\n" \
  "s_mov_b64 exec, 0xff\n" \
  "global_store_dword v46, v47, s[52:53] sc1\n" \
  "s_mov_b64 exec, -1\n" \
  ".LCX_W1_%=:\n" \
  "s_add_u32 s52, s52, 4096\n" \
  "s_addc_u32 s53, s53, 0\n" \
  ".LCX_SPIN_%=:\n" \
  "s_sleep 2\n" \
  "s_memrealtime s[38:39]\n" \
  "ds_read_b32 v37, v38\n" \
  "s_sub_u32 s62, s62, 1\n" \
  "s_cbranch_scc1 .LCX_END_%=\n" \
  "s_waitcnt lgkmcnt(0)\n" \
  "v_readfirstlane_b32 s73, v37\n" \
  "s_add_u32 s38, s38, 80\n" \
  "s_bfe_u32 s64, s73, 0x80008\n" \
  "s_and_b32 s73, s73, 0xff\n" \
  "s_min_u32 s73, s73, s64\n" \
  "s_cmp_ge_u32 s73, s67\n" \
  "s_cbranch_scc0 .LCX_SPIN_%=\n" \
  "s_cmp_eq_u32 s70, 0\n" \
  "s_cbranch_scc1 .LCX_TOK_%=\n" \
  "s_sub_u32 s63, s38, s69\n" \
  "s_max_i32 s63, s63, 0\n" \
  "s_add_u32 s69, s69, s63\n" \
  ".LCX_TOK_%=:\n" \
  "ds_read_b128 v[48:51], v6 offset:0\n" \
  "ds_read_b128 v[64:67], v0 offset:8192\n" \
  "ds_read_b128 v[80:83], v0 offset:12288\n" \
  "ds_read_b128 v[52:55], v6 offset:1024\n" \
  "ds_read_b128 v[68:71], v0 offset:9216\n" \
  "ds_read_b128 v[84:87], v0 offset:13312\n" \
  "ds_read_b128 v[56:59], v6 offset:2048\n" \
  "ds_read_b128 v[72:75], v0 offset:10240\n" \
  "ds_read_b128 v[88:91], v0 offset:14336\n" \
  "ds_read_b128 v[60:63], v6 offset:3072\n" \
  "ds_read_b128 v[76:79], v0 offset:11264\n" \
  "ds_read_b128 v[92:95], v0 offset:15360\n" \
  "s_waitcnt lgkmcnt(9)\n" \
  "v_mfma_f32_16x16x32_bf16 v[20:23], v[64:67], v[48:51], v[8:11]\n" \
  "v_mfma_f32_16x16x32_bf16 v[24:27], v[80:83], v[48:51], v[12:15]\n" \
  "ds_read_b128 v[96:99], v0 offset:16384\n" \
  "ds_read_b128 v[100:103], v0 offset:17408\n" \
  "s_waitcnt lgkmcnt(8)\n" \
  "v_mfma_f32_16x16x32_bf16 v[20:23], v[68:71], v[52:55], v[20:23]\n" \
  "v_mfma_f32_16x16x32_bf16 v[24:27], v[84:87], v[52:55], v[24:27]\n" \
  "ds_read_b128 v[104:107], v0 offset:18432\n" \
  "ds_read_b128 v[108:111], v0 offset:19456\n" \
  "s_waitcnt lgkmcnt(7)\n" \
  "v_mfma_f32_16x16x32_bf16 v[20:23], v[72:75], v[56:59], v[20:23]\n" \
  "v_mfma_f32_16x16x32_bf16 v[24:27], v[88:91], v[56:59], v[24:27]\n" \
  "ds_read_b128 v[112:115], v0 offset:20480\n" \
  "ds_read_b128 v[116:119], v0 offset:21504\n" \
  "s_waitcnt lgkmcnt(6)\n" \
  "v_mfma_f32_16x16x32_bf16 v[20:23], v[76:79], v[60:63], v[20:23]\n" \
  "v_mfma_f32_16x16x32_bf16 v[24:27], v[92:95], v[60:63], v[24:27]\n" \
  "ds_read_b128 v[120:123], v0 offset:22528\n" \
  "ds_read_b128 v[124:127], v0 offset:23552\n" \
  "s_add_u32 s67, s67, 1\n" \
  "v_add_u32 v0, s65, v0\n" \
  "v_add_u32 v6, s65, v6\n" \
  "s_sub_u32 s65, 0, s65\n" \
  "s_nop 1\n" \
  "v_max3_f32 v32, v20, v21, v22\n" \
  "v_max3_f32 v33, v23, v24, v25\n" \
  "v_max3_f32 v32, v32, v26, v27\n" \
  "v_max_f32 v32, v32, v33\n" \
  "v_mov_b32 v33, v32\n" \
  "s_nop 1\n" \
  "v_permlane16_swap_b32 v32, v33\n" \
  "v_max_f32 v32, v32, v33\n" \
  "v_mov_b32 v33, v32\n" \
  "s_nop 1\n" \
  "v_permlane32_swap_b32 v32, v33\n" \
  "v_max_f32 v32, v32, v33\n" \
  "v_mul_f32 v33, s72, v32\n" \
  "v_fma_f32 v20, v20, s71, v33\n" \
  "v_fma_f32 v21, v21, s71, v33\n" \
  "v_fma_f32 v22, v22, s71, v33\n" \
  "v_fma_f32 v23, v23, s71, v33\n" \
  "v_fma_f32 v24, v24, s71, v33\n" \
  "v_fma_f32 v25, v25, s71, v33\n" \
  "v_fma_f32 v26, v26, s71, v33\n" \
  "v_fma_f32 v27, v27, s71, v33\n" \
  "v_exp_f32 v20, v20\n" \
  "v_exp_f32 v21, v21\n" \
  "v_exp_f32 v22, v22\n" \
  "v_exp_f32 v23, v23\n" \
  "v_exp_f32 v24, v24\n" \
  "v_exp_f32 v25, v25\n" \
  "v_exp_f32 v26, v26\n" \
  "v_exp_f32 v27, v27\n" \
  "v_add_f32 v34, v20, v21\n" \
  "v_add_f32 v33, v22, v23\n" \
  "v_add_f32 v36, v24, v25\n" \
  "v_add_f32 v34, v34, v33\n" \
  "v_add_f32 v33, v26, v27\n" \
  "v_add_f32 v34, v34, v36\n" \
  "v_add_f32 v34, v34, v33\n" \
  "s_waitcnt vmcnt(0) lgkmcnt(0)\n" \
  "s_cmp_eq_u32 s61, 0\n" \
  "s_cbranch_scc1 .LCX_NP_%=\n" \
  "v_mov_b32 v46, s59\n" \
  "v_mov_b32 v47, 1\n" \
  "s_mov_b64 exec, 1\n" \
  "ds_add_rtn_u32 v46, v46, v47\n" \
  "s_mov_b64 exec, -1\n" \
  "s_xor_b32 s59, s59, 4\n" \
  "s_mov_b32 s61, 0\n" \
  "s_waitcnt lgkmcnt(0)\n" \
  "v_readfirstlane_b32 s63, v46\n" \
  "v_lshlrev_b32 v46, 5, v5\n" \
  "s_and_b32 s63, s63, 7\n" \
  "s_cmp_eq_u32 s63, 7\n" \
  "s_cbranch_scc0 .LCX_W2_%=\n" \
  "s_mov_b64 exec, 0xff\n" \
  "global_store_dword v46, v47, s[52:53] sc1\n" \
  "s_mov_b64 exec, -1\n" \
  ".LCX_W2_%=:\n" \
  "s_add_u32 s52, s52, 4096\n" \
  "s_addc_u32 s53, s53, 0\n" \
  ".LCX_NP_%=:\n" \
  "s_cmp_lg_u32 s68, 0\n" \
  "s_cbranch_scc1 .LCX_TEST_%=\n" \
  ".LCX_WF_%=:\n" \
  "s_memrealtime s[38:39]\n" \
  "s_waitcnt lgkmcnt(0)\n" \
  "s_sub_u32 s63, s69, s38\n" \
  "s_cmp_le_i32 s63, 0\n" \
  "s_cbranch_scc1 .LCX_GO_%=\n" \
  "s_cmp_lt_i32 s63, 12\n" \
  "s_cbranch_scc1 .LCX_WF_%=\n" \
  "s_sleep 3\n" \
  "s_branch .LCX_WF_%=\n" \
  ".LCX_GO_%=:\n" \
  "v_mov_b32 v35, 0\n" \
  "global_load_dwordx4 v[128:131], v2, s[48:49] offset:0 sc1\n" \
  "global_load_dwordx4 v[132:135], v2, s[48:49] offset:512 sc1\n" \
  "global_load_dwordx4 v[136:139], v2, s[48:49] offset:1024 sc1\n" \
  "global_load_dwordx4 v[140:143], v2, s[48:49] offset:1536 sc1\n" \
  "global_load_dwordx4 v[144:147], v3, s[48:49] offset:0 sc1\n" \
  "global_load_dwordx4 v[148:151], v3, s[48:49] offset:512 sc1\n" \
  "global_load_dwordx4 v[152:155], v3, s[48:49] offset:1024 sc1\n" \
  "global_load_dwordx4 v[156:159], v3, s[48:49] offset:1536 sc1\n" \
  "global_load_dword v35, v35, s[50:51] sc1\n" \
  "s_sub_u32 s62, s62, 8\n" \
  "s_cbranch_scc1 .LCX_END_%=\n" \
  "s_waitcnt vmcnt(0)\n" \
  ".LCX_TEST_%=:\n" \
  "v_mfma_f32_16x16x32_bf16 v[28:31], v[128:131], v[48:51], v[16:19]\n" \
  "v_min3_u32 v33, v128, v129, v130\n" \
  "v_min3_u32 v36, v131, v132, v133\n" \
  "v_min3_u32 v33, v33, v134, v135\n" \
  "v_min3_u32 v36, v36, v136, v137\n" \
  "v_mfma_f32_16x16x32_bf16 v[28:31], v[132:135], v[52:55], v[28:31]\n" \
  "v_min3_u32 v33, v33, v138, v139\n" \
  "v_min3_u32 v36, v36, v140, v141\n" \
  "v_min3_u32 v33, v33, v142, v143\n" \
  "v_min3_u32 v36, v36, v144, v145\n" \
  "v_mfma_f32_16x16x32_bf16 v[28:31], v[136:139], v[56:59], v[28:31]\n" \
  "v_min3_u32 v33, v33, v146, v147\n" \
  "v_min3_u32 v36, v36, v148, v149\n" \
  "v_min3_u32 v33, v33, v150, v151\n" \
  "v_min3_u32 v36, v36, v152, v153\n" \
  "v_mfma_f32_16x16x32_bf16 v[28:31], v[140:143], v[60:63], v[28:31]\n" \
  "v_min3_u32 v33, v33, v154, v155\n" \
  "v_min3_u32 v36, v36, v156, v157\n" \
  "v_min3_u32 v33, v33, v158, v159\n" \
  "v_min_u32 v33, v33, v36\n" \
  "v_cmp_eq_u32 vcc, 0, v33\n" \
  "v_readfirstlane_b32 s64, v35\n" \
  "s_mov_b32 s68, 0\n" \
  "s_cmp_eq_u64 vcc, 0\n" \
  "s_cbranch_scc0 .LCX_FAIL_%=\n" \
  "v_max3_f32 v36, v28, v29, v30\n" \
  "v_max3_f32 v36, v36, v31, v32\n" \
  "v_mov_b32 v33, v36\n" \
  "s_cmp_eq_u32 s64, 0\n" \
  "s_cselect_b32 s64, s38, s64\n" \
  "v_permlane16_swap_b32 v36, v33\n" \
  "v_max_f32 v36, v36, v33\n" \
  "v_mov_b32 v33, v36\n" \
  "s_sub_u32 s63, s64, s70\n" \
  "s_mov_b32 s70, s64\n" \
  "v_permlane32_swap_b32 v36, v33\n" \
  "v_max_f32 v36, v36, v33\n" \
  "v_mul_f32 v36, s72, v36\n" \
  "v_fma_f32 v32, v32, s71, v36\n" \
  "v_fma_f32 v28, v28, s71, v36\n" \
  "v_fma_f32 v29, v29, s71, v36\n" \
  "v_fma_f32 v30, v30, s71, v36\n" \
  "v_fma_f32 v31, v31, s71, v36\n" \
  "v_exp_f32 v32, v32\n" \
  "v_exp_f32 v28, v28\n" \
  "v_exp_f32 v29, v29\n" \
  "v_exp_f32 v30, v30\n" \
  "v_exp_f32 v31, v31\n" \
  "v_fma_f32 v34, v34, v32, v28\n" \
  "v_add_f32 v33, v29, v30\n" \
  "v_add_f32 v34, v34, v31\n" \
  "v_add_f32 v34, v34, v33\n" \
  "v_mov_b32 v33, v34\n" \
  "s_min_u32 s63, s63, 250\n" \
  "s_add_u32 s69, s64, s63\n" \
  "v_permlane16_swap_b32 v34, v33\n" \
  "v_add_f32 v34, v34, v33\n" \
  "v_mov_b32 v33, v34\n" \
  "s_add_u32 s69, s69, 68\n" \
  "s_add_u32 s48, s48, 0x10000\n" \
  "s_addc_u32 s49, s49, 0\n" \
  "v_permlane32_swap_b32 v34, v33\n" \
  "v_add_f32 v34, v34, v33\n" \
  "v_rcp_f32 v34, v34\n" \
  "s_add_u32 s50, s50, 1024\n" \
  "s_addc_u32 s51, s51, 0\n" \
  "v_mul_f32 v32, v32, v34\n" \
  "v_mul_f32 v20, v20, v32\n" \
  "v_mul_f32 v21, v21, v32\n" \
  "v_mul_f32 v22, v22, v32\n" \
  "v_mul_f32 v23, v23, v32\n" \
  "v_mul_f32 v24, v24, v32\n" \
  "v_mul_f32 v25, v25, v32\n" \
  "v_mul_f32 v26, v26, v32\n" \
  "v_mul_f32 v27, v27, v32\n" \
  "v_cvt_pk_bf16_f32 v40, v20, v21\n" \
  "v_cvt_pk_bf16_f32 v41, v22, v23\n" \
  "v_cvt_pk_bf16_f32 v42, v24, v25\n" \
  "v_cvt_pk_bf16_f32 v43, v26, v27\n" \
  "v_mul_f32 v28, v28, v34\n" \
  "v_mul_f32 v29, v29, v34\n" \
  "v_mfma_f32_16x16x32_bf16 v[64:67], v[96:99], v[40:43], 0\n" \
  "v_mul_f32 v30, v30, v34\n" \
  "v_mfma_f32_16x16x32_bf16 v[68:71], v[100:103], v[40:43], 0\n" \
  "v_mul_f32 v31, v31, v34\n" \
  "v_mfma_f32_16x16x32_bf16 v[72:75], v[104:107], v[40:43], 0\n" \
  "v_cvt_pk_bf16_f32 v44, v28, v29\n" \
  "v_mfma_f32_16x16x32_bf16 v[76:79], v[108:111], v[40:43], 0\n" \
  "v_cvt_pk_bf16_f32 v45, v30, v31\n" \
  "v_mfma_f32_16x16x32_bf16 v[80:83], v[112:115], v[40:43], 0\n" \
  "v_mfma_f32_16x16x32_bf16 v[84:87], v[116:119], v[40:43], 0\n" \
  "v_mfma_f32_16x16x32_bf16 v[88:91], v[120:123], v[40:43], 0\n" \
  "v_mfma_f32_16x16x32_bf16 v[92:95], v[124:127], v[40:43], 0\n" \
  "s_memrealtime s[38:39]\n" \
  "ds_read_b32 v37, v38\n" \
  "v_mfma_f32_16x16x16_bf16 v[64:67], v[144:145], v[44:45], v[64:67]\n" \
  "v_mfma_f32_16x16x16_bf16 v[68:71], v[148:149], v[44:45], v[68:71]\n" \
  "v_mfma_f32_16x16x16_bf16 v[72:75], v[152:153], v[44:45], v[72:75]\n" \
  "v_mfma_f32_16x16x16_bf16 v[76:79], v[156:157], v[44:45], v[76:79]\n" \
  "v_mfma_f32_16x16x16_bf16 v[80:83], v[146:147], v[44:45], v[80:83]\n" \
  "v_mfma_f32_16x16x16_bf16 v[84:87], v[150:151], v[44:45], v[84:87]\n" \
  "v_mfma_f32_16x16x16_bf16 v[88:91], v[154:155], v[44:45], v[88:91]\n" \
  "v_mfma_f32_16x16x16_bf16 v[92:95], v[158:159], v[44:45], v[92:95]\n" \
  "s_nop 0\n" \
  "v_cvt_pk_bf16_f32 v160, v64, v65\n" \
  "v_cvt_pk_bf16_f32 v161, v66, v67\n" \
  "v_cvt_pk_bf16_f32 v162, v68, v69\n" \
  "v_cvt_pk_bf16_f32 v163, v70, v71\n" \
  "ds_write_b128 v7, v[160:163] offset:0\n" \
  "v_cvt_pk_bf16_f32 v164, v72, v73\n" \
  "v_cvt_pk_bf16_f32 v165, v74, v75\n" \
  "v_cvt_pk_bf16_f32 v166, v76, v77\n" \
  "v_cvt_pk_bf16_f32 v167, v78, v79\n" \
  "ds_write_b128 v7, v[164:167] offset:1024\n" \
  "v_cvt_pk_bf16_f32 v168, v80, v81\n" \
  "v_cvt_pk_bf16_f32 v169, v82, v83\n" \
  "v_cvt_pk_bf16_f32 v170, v84, v85\n" \
  "v_cvt_pk_bf16_f32 v171, v86, v87\n" \
  "ds_write_b128 v7, v[168:171] offset:2048\n" \
  "v_cvt_pk_bf16_f32 v172, v88, v89\n" \
  "v_cvt_pk_bf16_f32 v173, v90, v91\n" \
  "v_cvt_pk_bf16_f32 v174, v92, v93\n" \
  "v_cvt_pk_bf16_f32 v175, v94, v95\n" \
  "ds_write_b128 v7, v[172:175] offset:3072\n" \
  "v_add_u32 v7, s66, v7\n" \
  "s_waitcnt lgkmcnt(0)\n" \
  "s_barrier\n" \
  "ds_read_b128 v[48:51], v1 offset:0\n" \
  "ds_read_b128 v[52:55], v1 offset:4096\n" \
  "ds_read_b128 v[56:59], v1 offset:1024\n" \
  "ds_read_b128 v[60:63], v1 offset:5120\n" \
  "ds_read_b128 v[64:67], v1 offset:2048\n" \
  "ds_read_b128 v[68:71], v1 offset:6144\n" \
  "ds_read_b128 v[72:75], v1 offset:3072\n" \
  "ds_read_b128 v[76:79], v1 offset:7168\n" \
  "ds_read_b128 v[80:83], v1 offset:8192\n" \
  "ds_read_b128 v[84:87], v1 offset:12288\n" \
  "ds_read_b128 v[88:91], v1 offset:9216\n" \
  "ds_read_b128 v[92:95], v1 offset:13312\n" \
  "ds_read_b128 v[96:99], v1 offset:10240\n" \
  "ds_read_b128 v[100:103], v1 offset:14336\n" \
  "v_readfirstlane_b32 s73, v37\n" \
  "s_sub_u32 s63, s38, s69\n" \
  "s_bfe_u32 s64, s73, 0x80010\n" \
  "s_sub_u32 s64, s64, s67\n" \
  "s_or_b32 s63, s63, s64\n" \
  "s_bfe_u32 s64, s73, 0x80008\n" \
  "s_and_b32 s73, s73, 0xff\n" \
  "s_min_u32 s73, s73, s64\n" \
  "s_or_b32 s63, s63, s68\n" \
  "s_cmp_lt_i32 s63, 0\n" \
  "s_cbranch_scc1 .LCX_E3_%=\n" \
  "v_mov_b32 v35, 0\n" \
  "global_load_dwordx4 v[128:131], v2, s[48:49] offset:0 sc1\n" \
  "global_load_dwordx4 v[132:135], v2, s[48:49] offset:512 sc1\n" \
  "global_load_dwordx4 v[136:139], v2, s[48:49] offset:1024 sc1\n" \
  "global_load_dwordx4 v[140:143], v2, s[48:49] offset:1536 sc1\n" \
  "global_load_dwordx4 v[144:147], v3, s[48:49] offset:0 sc1\n" \
  "global_load_dwordx4 v[148:151], v3, s[48:49] offset:512 sc1\n" \
  "global_load_dwordx4 v[152:155], v3, s[48:49] offset:1024 sc1\n" \
  "global_load_dwordx4 v[156:159], v3, s[48:49] offset:1536 sc1\n" \
  "global_load_dword v35, v35, s[50:51] sc1\n" \
  "s_mov_b32 s68, -1\n" \
  ".LCX_E3_%=:\n" \
  "s_waitcnt lgkmcnt(12)\n" \
  "v_mfma_f32_16x16x32_bf16 v[112:115], v[192:195], v[48:51], 0\n" \
  "v_mfma_f32_16x16x32_bf16 v[116:119], v[224:227], v[48:51], 0\n" \
  "v_mfma_f32_16x16x32_bf16 v[120:123], v[192:195], v[52:55], 0\n" \
  "v_mfma_f32_16x16x32_bf16 v[124:127], v[224:227], v[52:55], 0\n" \
  "ds_read_b128 v[104:107], v1 offset:11264\n" \
  "ds_read_b128 v[108:111], v1 offset:15360\n" \
  "s_waitcnt lgkmcnt(12)\n" \
  "v_mfma_f32_16x16x32_bf16 v[112:115], v[196:199], v[56:59], v[112:115]\n" \
  "v_mfma_f32_16x16x32_bf16 v[116:119], v[228:231], v[56:59], v[116:119]\n" \
  "v_mfma_f32_16x16x32_bf16 v[120:123], v[196:199], v[60:63], v[120:123]\n" \
  "v_mfma_f32_16x16x32_bf16 v[124:127], v[228:231], v[60:63], v[124:127]\n" \
  "s_waitcnt lgkmcnt(10)\n" \
  "v_mfma_f32_16x16x32_bf16 v[112:115], v[200:203], v[64:67], v[112:115]\n" \
  "v_mfma_f32_16x16x32_bf16 v[116:119], v[232:235], v[64:67], v[116:119]\n" \
  "v_mfma_f32_16x16x32_bf16 v[120:123], v[200:203], v[68:71], v[120:123]\n" \
  "v_mfma_f32_16x16x32_bf16 v[124:127], v[232:235], v[68:71], v[124:127]\n" \
  "s_waitcnt lgkmcnt(8)\n" \
  "v_mfma_f32_16x16x32_bf16 v[112:115], v[204:207], v[72:75], v[112:115]\n" \
  "v_mfma_f32_16x16x32_bf16 v[116:119], v[236:239], v[72:75], v[116:119]\n" \
  "v_mfma_f32_16x16x32_bf16 v[120:123], v[204:207], v[76:79], v[120:123]\n" \
  "v_mfma_f32_16x16x32_bf16 v[124:127], v[236:239], v[76:79], v[124:127]\n" \
  "s_waitcnt lgkmcnt(6)\n" \
  "v_mfma_f32_16x16x32_bf16 v[112:115], v[208:211], v[80:83], v[112:115]\n" \
  "v_mfma_f32_16x16x32_bf16 v[116:119], v[240:243], v[80:83], v[116:119]\n" \
  "v_mfma_f32_16x16x32_bf16 v[120:123], v[208:211], v[84:87], v[120:123]\n" \
  "v_mfma_f32_16x16x32_bf16 v[124:127], v[240:243], v[84:87], v[124:127]\n" \
  "s_waitcnt lgkmcnt(4)\n" \
  "v_mfma_f32_16x16x32_bf16 v[112:115], v[212:215], v[88:91], v[112:115]\n" \
  "v_mfma_f32_16x16x32_bf16 v[116:119], v[244:247], v[88:91], v[116:119]\n" \
  "v_mfma_f32_16x16x32_bf16 v[120:123], v[212:215], v[92:95], v[120:123]\n" \
  "v_mfma_f32_16x16x32_bf16 v[124:127], v[244:247], v[92:95], v[124:127]\n" \
  "s_memrealtime s[38:39]\n" \
  "ds_read_b32 v37, v38\n" \
  "s_waitcnt lgkmcnt(3)\n" \
  "v_mfma_f32_16x16x32_bf16 v[112:115], v[216:219], v[96:99], v[112:115]\n" \
  "v_mfma_f32_16x16x32_bf16 v[116:119], v[248:251], v[96:99], v[116:119]\n" \
  "v_mfma_f32_16x16x32_bf16 v[120:123], v[216:219], v[100:103], v[120:123]\n" \
  "v_mfma_f32_16x16x32_bf16 v[124:127], v[248:251], v[100:103], v[124:127]\n" \
  "s_waitcnt lgkmcnt(0)\n" \
  "v_mfma_f32_16x16x32_bf16 v[112:115], v[220:223], v[104:107], v[112:115]\n" \
  "v_mfma_f32_16x16x32_bf16 v[116:119], v[252:255], v[104:107], v[116:119]\n" \
  "v_mfma_f32_16x16x32_bf16 v[120:123], v[220:223], v[108:111], v[120:123]\n" \
  "v_mfma_f32_16x16x32_bf16 v[124:127], v[252:255], v[108:111], v[124:127]\n" \
  "v_add_u32 v1, s66, v1\n" \
  "s_sub_u32 s66, 0, s66\n" \
  "s_add_u32 s64, s54, 16384\n" \
  "s_mov_b32 s61, 1\n" \
  "s_nop 1\n" \
  "v_cvt_pk_bf16_f32 v20, v112, v113\n" \
  "v_cvt_pk_bf16_f32 v21, v114, v115\n" \
  "v_cvt_pk_bf16_f32 v22, v116, v117\n" \
  "v_cvt_pk_bf16_f32 v23, v118, v119\n" \
  "buffer_store_dwordx4 v[20:23], v4, s[40:43], s54 offen sc1\n" \
  "v_cvt_pk_bf16_f32 v24, v120, v121\n" \
  "v_cvt_pk_bf16_f32 v25, v122, v123\n" \
  "v_cvt_pk_bf16_f32 v26, v124, v125\n" \
  "v_cvt_pk_bf16_f32 v27, v126, v127\n" \
  "buffer_store_dwordx4 v[24:27], v4, s[40:43], s64 offen sc1\n" \
  "v_readfirstlane_b32 s73, v37\n" \
  "s_sub_u32 s63, s38, s69\n" \
  "s_bfe_u32 s64, s73, 0x80010\n" \
  "s_sub_u32 s64, s64, s67\n" \
  "s_or_b32 s63, s63, s64\n" \
  "s_bfe_u32 s64, s73, 0x80008\n" \
  "s_and_b32 s73, s73, 0xff\n" \
  "s_min_u32 s73, s73, s64\n" \
  "s_or_b32 s63, s63, s68\n" \
  "s_cmp_lt_i32 s63, 0\n" \
  "s_cbranch_scc1 .LCX_E4_%=\n" \
  "v_mov_b32 v35, 0\n" \
  "global_load_dwordx4 v[128:131], v2, s[48:49] offset:0 sc1\n" \
  "global_load_dwordx4 v[132:135], v2, s[48:49] offset:512 sc1\n" \
  "global_load_dwordx4 v[136:139], v2, s[48:49] offset:1024 sc1\n" \
  "global_load_dwordx4 v[140:143], v2, s[48:49] offset:1536 sc1\n" \
  "global_load_dwordx4 v[144:147], v3, s[48:49] offset:0 sc1\n" \
  "global_load_dwordx4 v[148:151], v3, s[48:49] offset:512 sc1\n" \
  "global_load_dwordx4 v[152:155], v3, s[48:49] offset:1024 sc1\n" \
  "global_load_dwordx4 v[156:159], v3, s[48:49] offset:1536 sc1\n" \
  "global_load_dword v35, v35, s[50:51] sc1\n" \
  "s_mov_b32 s68, -1\n" \
  ".LCX_E4_%=:\n" \
  "s_add_u32 s54, s54, 0x80000\n" \
  "s_sub_u32 s60, s60, 1\n" \
  "s_cmp_lg_u32 s60, 0\n" \
  "s_cbranch_scc1 .LCX_TOP_%=\n" \
  "s_waitcnt vmcnt(0)\n" \
  "v_mov_b32 v46, s59\n" \
  "v_mov_b32 v47, 1\n" \
  "s_mov_b64 exec, 1\n" \
  "ds_add_rtn_u32 v46, v46, v47\n" \
  "s_mov_b64 exec, -1\n" \
  "s_xor_b32 s59, s59, 4\n" \
  "s_mov_b32 s61, 0\n" \
  "s_waitcnt lgkmcnt(0)\n" \
  "v_readfirstlane_b32 s63, v46\n" \
  "v_lshlrev_b32 v46, 5, v5\n" \
  "s_and_b32 s63, s63, 7\n" \
  "s_cmp_eq_u32 s63, 7\n" \
  "s_cbranch_scc0 .LCX_W5_%=\n" \
  "s_mov_b64 exec, 0xff\n" \
  "global_store_dword v46, v47, s[52:53] sc1\n" \
  "s_mov_b64 exec, -1\n" \
  ".LCX_W5_%=:\n" \
  "s_add_u32 s52, s52, 4096\n" \
  "s_addc_u32 s53, s53, 0\n" \
  ".LCX_END_%=:\n" \
  "s_endpgm\n" \
  ".LCX_FAIL_%=:\n" \
  "s_add_u32 s69, s64, 68\n" \
  "s_cmp_lg_u32 s64, 0\n" \
  "s_cbranch_scc1 .LCX_WF_%=\n" \
  "s_sleep 4\n" \
  "global_load_dword v35, v35, s[50:51] sc1\n" \
  "s_sub_u32 s62, s62, 8\n" \
  "s_cbranch_scc1 .LCX_END_%=\n" \
  "s_waitcnt vmcnt(0)\n" \
  "v_readfirstlane_b32 s64, v35\n" \
  "s_branch .LCX_FAIL_%=\n"
#define AW_ASM_Y \
  "s_mov_b32 s67, 0\n" \
  "s_mov_b32 s68, 2\n" \
  "s_mov_b32 s69, 0\n" \
  "s_mov_b32 s65, 49152\n" \
  "s_mov_b32 s66, 16384\n" \
  "v_mov_b32 v5, s58\n" \
  "v_add_u32 v3, 4096, v2\n" \
  "s_mov_b32 s62, 0x40000\n" \
  "s_branch .LCY_ASK_%=\n" \
  ".LCY_BAR_%=:\n" \
  "s_barrier\n" \
  "ds_read_b128 v[8:11], v0 offset:0\n" \
  "ds_read_b128 v[12:15], v0 offset:1024\n" \
  "ds_read_b128 v[16:19], v0 offset:2048\n" \
  "s_add_u32 s68, s68, 1\n" \
  "s_add_u32 s64, s54, 16384\n" \
  "s_cmp_eq_u32 s69, 0\n" \
  "s_cbranch_scc1 .LCY_NI_%=\n" \
  "s_waitcnt vmcnt(1)\n" \
  "s_cmp_lg_u32 s61, 0\n" \
  "s_cbranch_scc1 .LCY_NJ_%=\n" \
  "s_waitcnt vmcnt(0)\n" \
  ".LCY_NJ_%=:\n" \
  "v_mov_b32 v6, s55\n" \
  "s_mov_b64 exec, 1\n" \
  "ds_add_u32 v5, v6\n" \
  "s_mov_b64 exec, -1\n" \
  "s_mov_b32 s69, 0\n" \
  ".LCY_NI_%=:\n" \
  "v_cmp_eq_u32 vcc, 0, v4\n" \
  "s_cmp_eq_u64 vcc, 0\n" \
  "s_cselect_b32 s70, 1, 0\n" \
  "s_cbranch_scc0 .LCY_NS_%=\n" \
  "v_lshlrev_b32 v6, 16, s55\n" \
  "s_mov_b64 exec, 1\n" \
  "ds_add_u32 v5, v6\n" \
  "s_mov_b64 exec, -1\n" \
  "s_mov_b32 m0, s56\n" \
  "s_add_u32 s71, s56, 4096\n" \
  "global_load_lds_dwordx4 v2, s[48:49] offset:0 sc1\n" \
  "global_load_lds_dwordx4 v2, s[48:49] offset:1024 sc1\n" \
  "global_load_lds_dwordx4 v2, s[48:49] offset:2048 sc1\n" \
  "global_load_lds_dwordx4 v2, s[48:49] offset:3072 sc1\n" \
  "s_mov_b32 m0, s71\n" \
  "s_add_u32 s56, s56, s65\n" \
  "global_load_lds_dwordx4 v3, s[48:49] offset:0 sc1\n" \
  "global_load_lds_dwordx4 v3, s[48:49] offset:1024 sc1\n" \
  ".LCY_NS_%=:\n" \
  "ds_read_b128 v[20:23], v0 offset:3072\n" \
  "s_waitcnt lgkmcnt(3)\n" \
  "v_mfma_f32_16x16x32_bf16 v[32:35], v[64:67], v[8:11], 0\n" \
  "v_mfma_f32_16x16x32_bf16 v[36:39], v[96:99], v[8:11], 0\n" \
  "v_mfma_f32_16x16x32_bf16 v[40:43], v[128:131], v[8:11], 0\n" \
  "v_mfma_f32_16x16x32_bf16 v[44:47], v[160:163], v[8:11], 0\n" \
  "v_mfma_f32_16x16x32_bf16 v[48:51], v[192:195], v[8:11], 0\n" \
  "v_mfma_f32_16x16x32_bf16 v[52:55], v[224:227], v[8:11], 0\n" \
  "ds_read_b128 v[8:11], v0 offset:8192\n" \
  "s_waitcnt lgkmcnt(3)\n" \
  "v_mfma_f32_16x16x32_bf16 v[32:35], v[68:71], v[12:15], v[32:35]\n" \
  "v_mfma_f32_16x16x32_bf16 v[36:39], v[100:103], v[12:15], v[36:39]\n" \
  "v_mfma_f32_16x16x32_bf16 v[40:43], v[132:135], v[12:15], v[40:43]\n" \
  "v_mfma_f32_16x16x32_bf16 v[44:47], v[164:167], v[12:15], v[44:47]\n" \
  "v_mfma_f32_16x16x32_bf16 v[48:51], v[196:199], v[12:15], v[48:51]\n" \
  "v_mfma_f32_16x16x32_bf16 v[52:55], v[228:231], v[12:15], v[52:55]\n" \
  "ds_read_b128 v[12:15], v0 offset:9216\n" \
  "s_waitcnt lgkmcnt(3)\n" \
  "v_mfma_f32_16x16x32_bf16 v[32:35], v[72:75], v[16:19], v[32:35]\n" \
  "v_mfma_f32_16x16x32_bf16 v[36:39], v[104:107], v[16:19], v[36:39]\n" \
  "v_mfma_f32_16x16x32_bf16 v[40:43], v[136:139], v[16:19], v[40:43]\n" \
  "v_mfma_f32_16x16x32_bf16 v[44:47], v[168:171], v[16:19], v[44:47]\n" \
  "v_mfma_f32_16x16x32_bf16 v[48:51], v[200:203], v[16:19], v[48:51]\n" \
  "v_mfma_f32_16x16x32_bf16 v[52:55], v[232:235], v[16:19], v[52:55]\n" \
  "ds_read_b128 v[16:19], v0 offset:10240\n" \
  "s_waitcnt lgkmcnt(3)\n" \
  "v_mfma_f32_16x16x32_bf16 v[32:35], v[76:79], v[20:23], v[32:35]\n" \
  "v_mfma_f32_16x16x32_bf16 v[36:39], v[108:111], v[20:23], v[36:39]\n" \
  "v_mfma_f32_16x16x32_bf16 v[40:43], v[140:143], v[20:23], v[40:43]\n" \
  "v_mfma_f32_16x16x32_bf16 v[44:47], v[172:175], v[20:23], v[44:47]\n" \
  "v_mfma_f32_16x16x32_bf16 v[48:51], v[204:207], v[20:23], v[48:51]\n" \
  "v_mfma_f32_16x16x32_bf16 v[52:55], v[236:239], v[20:23], v[52:55]\n" \
  "ds_read_b128 v[20:23], v0 offset:11264\n" \
  "s_waitcnt lgkmcnt(3)\n" \
  "v_mfma_f32_16x16x32_bf16 v[32:35], v[80:83], v[8:11], v[32:35]\n" \
  "v_mfma_f32_16x16x32_bf16 v[36:39], v[112:115], v[8:11], v[36:39]\n" \
  "v_mfma_f32_16x16x32_bf16 v[40:43], v[144:147], v[8:11], v[40:43]\n" \
  "v_mfma_f32_16x16x32_bf16 v[44:47], v[176:179], v[8:11], v[44:47]\n" \
  "v_mfma_f32_16x16x32_bf16 v[48:51], v[208:211], v[8:11], v[48:51]\n" \
  "v_mfma_f32_16x16x32_bf16 v[52:55], v[240:243], v[8:11], v[52:55]\n" \
  "ds_read_b128 v[8:11], v0 offset:4096\n" \
  "s_waitcnt lgkmcnt(3)\n" \
  "v_mfma_f32_16x16x32_bf16 v[32:35], v[84:87], v[12:15], v[32:35]\n" \
  "v_mfma_f32_16x16x32_bf16 v[36:39], v[116:119], v[12:15], v[36:39]\n" \
  "v_mfma_f32_16x16x32_bf16 v[40:43], v[148:151], v[12:15], v[40:43]\n" \
  "v_mfma_f32_16x16x32_bf16 v[44:47], v[180:183], v[12:15], v[44:47]\n" \
  "v_mfma_f32_16x16x32_bf16 v[48:51], v[212:215], v[12:15], v[48:51]\n" \
  "v_mfma_f32_16x16x32_bf16 v[52:55], v[244:247], v[12:15], v[52:55]\n" \
  "ds_read_b128 v[12:15], v0 offset:5120\n" \
  "s_waitcnt lgkmcnt(3)\n" \
  "v_mfma_f32_16x16x32_bf16 v[32:35], v[88:91], v[16:19], v[32:35]\n" \
  "v_mfma_f32_16x16x32_bf16 v[36:39], v[120:123], v[16:19], v[36:39]\n" \
  "v_mfma_f32_16x16x32_bf16 v[40:43], v[152:155], v[16:19], v[40:43]\n" \
  "v_mfma_f32_16x16x32_bf16 v[44:47], v[184:187], v[16:19], v[44:47]\n" \
  "v_mfma_f32_16x16x32_bf16 v[48:51], v[216:219], v[16:19], v[48:51]\n" \
  "v_mfma_f32_16x16x32_bf16 v[52:55], v[248:251], v[16:19], v[52:55]\n" \
  "ds_read_b128 v[16:19], v0 offset:6144\n" \
  "s_waitcnt lgkmcnt(3)\n" \
  "v_mfma_f32_16x16x32_bf16 v[32:35], v[92:95], v[20:23], v[32:35]\n" \
  "v_mfma_f32_16x16x32_bf16 v[36:39], v[124:127], v[20:23], v[36:39]\n" \
  "v_mfma_f32_16x16x32_bf16 v[40:43], v[156:159], v[20:23], v[40:43]\n" \
  "v_mfma_f32_16x16x32_bf16 v[44:47], v[188:191], v[20:23], v[44:47]\n" \
  "v_mfma_f32_16x16x32_bf16 v[48:51], v[220:223], v[20:23], v[48:51]\n" \
  "v_mfma_f32_16x16x32_bf16 v[52:55], v[252:255], v[20:23], v[52:55]\n" \
  "ds_read_b128 v[20:23], v0 offset:7168\n" \
  "s_waitcnt lgkmcnt(3)\n" \
  "s_nop 1\n" \
  "v_cvt_pk_bf16_f32 v24, v32, v33\n" \
  "v_cvt_pk_bf16_f32 v25, v34, v35\n" \
  "v_cvt_pk_bf16_f32 v26, v36, v37\n" \
  "v_cvt_pk_bf16_f32 v27, v38, v39\n" \
  "s_nop 0\n" \
  "v_mfma_f32_16x16x32_bf16 v[32:35], v[64:67], v[8:11], 0\n" \
  "v_mfma_f32_16x16x32_bf16 v[36:39], v[96:99], v[8:11], 0\n" \
  "buffer_store_dwordx4 v[24:27], v1, s[40:43], s54 offen offset:0 sc1\n" \
  "v_cvt_pk_bf16_f32 v28, v40, v41\n" \
  "v_cvt_pk_bf16_f32 v29, v42, v43\n" \
  "v_cvt_pk_bf16_f32 v30, v44, v45\n" \
  "v_cvt_pk_bf16_f32 v31, v46, v47\n" \
  "s_nop 0\n" \
  "v_mfma_f32_16x16x32_bf16 v[40:43], v[128:131], v[8:11], 0\n" \
  "v_mfma_f32_16x16x32_bf16 v[44:47], v[160:163], v[8:11], 0\n" \
  "buffer_store_dwordx4 v[28:31], v1, s[40:43], s54 offen offset:128 sc1\n" \
  "v_cvt_pk_bf16_f32 v56, v48, v49\n" \
  "v_cvt_pk_bf16_f32 v57, v50, v51\n" \
  "v_cvt_pk_bf16_f32 v58, v52, v53\n" \
  "v_cvt_pk_bf16_f32 v59, v54, v55\n" \
  "s_nop 0\n" \
  "v_mfma_f32_16x16x32_bf16 v[48:51], v[192:195], v[8:11], 0\n" \
  "v_mfma_f32_16x16x32_bf16 v[52:55], v[224:227], v[8:11], 0\n" \
  "buffer_store_dwordx4 v[56:59], v1, s[40:43], s54 offen offset:256 sc1\n" \
  "ds_read_b128 v[8:11], v0 offset:12288\n" \
  "s_waitcnt lgkmcnt(3)\n" \
  "v_mfma_f32_16x16x32_bf16 v[32:35], v[68:71], v[12:15], v[32:35]\n" \
  "v_mfma_f32_16x16x32_bf16 v[36:39], v[100:103], v[12:15], v[36:39]\n" \
  "v_mfma_f32_16x16x32_bf16 v[40:43], v[132:135], v[12:15], v[40:43]\n" \
  "v_mfma_f32_16x16x32_bf16 v[44:47], v[164:167], v[12:15], v[44:47]\n" \
  "v_mfma_f32_16x16x32_bf16 v[48:51], v[196:199], v[12:15], v[48:51]\n" \
  "v_mfma_f32_16x16x32_bf16 v[52:55], v[228:231], v[12:15], v[52:55]\n" \
  "ds_read_b128 v[12:15], v0 offset:13312\n" \
  "s_waitcnt lgkmcnt(3)\n" \
  "v_mfma_f32_16x16x32_bf16 v[32:35], v[72:75], v[16:19], v[32:35]\n" \
  "v_mfma_f32_16x16x32_bf16 v[36:39], v[104:107], v[16:19], v[36:39]\n" \
  "v_mfma_f32_16x16x32_bf16 v[40:43], v[136:139], v[16:19], v[40:43]\n" \
  "v_mfma_f32_16x16x32_bf16 v[44:47], v[168:171], v[16:19], v[44:47]\n" \
  "v_mfma_f32_16x16x32_bf16 v[48:51], v[200:203], v[16:19], v[48:51]\n" \
  "v_mfma_f32_16x16x32_bf16 v[52:55], v[232:235], v[16:19], v[52:55]\n" \
  "ds_read_b128 v[16:19], v0 offset:14336\n" \
  "s_waitcnt lgkmcnt(3)\n" \
  "v_mfma_f32_16x16x32_bf16 v[32:35], v[76:79], v[20:23], v[32:35]\n" \
  "v_mfma_f32_16x16x32_bf16 v[36:39], v[108:111], v[20:23], v[36:39]\n" \
  "v_mfma_f32_16x16x32_bf16 v[40:43], v[140:143], v[20:23], v[40:43]\n" \
  "v_mfma_f32_16x16x32_bf16 v[44:47], v[172:175], v[20:23], v[44:47]\n" \
  "v_mfma_f32_16x16x32_bf16 v[48:51], v[204:207], v[20:23], v[48:51]\n" \
  "v_mfma_f32_16x16x32_bf16 v[52:55], v[236:239], v[20:23], v[52:55]\n" \
  "ds_read_b128 v[20:23], v0 offset:15360\n" \
  "s_waitcnt lgkmcnt(3)\n" \
  "v_mfma_f32_16x16x32_bf16 v[32:35], v[80:83], v[8:11], v[32:35]\n" \
  "v_mfma_f32_16x16x32_bf16 v[36:39], v[112:115], v[8:11], v[36:39]\n" \
  "v_mfma_f32_16x16x32_bf16 v[40:43], v[144:147], v[8:11], v[40:43]\n" \
  "v_mfma_f32_16x16x32_bf16 v[44:47], v[176:179], v[8:11], v[44:47]\n" \
  "v_mfma_f32_16x16x32_bf16 v[48:51], v[208:211], v[8:11], v[48:51]\n" \
  "v_mfma_f32_16x16x32_bf16 v[52:55], v[240:243], v[8:11], v[52:55]\n" \
  "s_waitcnt lgkmcnt(2)\n" \
  "v_mfma_f32_16x16x32_bf16 v[32:35], v[84:87], v[12:15], v[32:35]\n" \
  "v_mfma_f32_16x16x32_bf16 v[36:39], v[116:119], v[12:15], v[36:39]\n" \
  "v_mfma_f32_16x16x32_bf16 v[40:43], v[148:151], v[12:15], v[40:43]\n" \
  "v_mfma_f32_16x16x32_bf16 v[44:47], v[180:183], v[12:15], v[44:47]\n" \
  "v_mfma_f32_16x16x32_bf16 v[48:51], v[212:215], v[12:15], v[48:51]\n" \
  "v_mfma_f32_16x16x32_bf16 v[52:55], v[244:247], v[12:15], v[52:55]\n" \
  "s_waitcnt lgkmcnt(1)\n" \
  "v_mfma_f32_16x16x32_bf16 v[32:35], v[88:91], v[16:19], v[32:35]\n" \
  "v_mfma_f32_16x16x32_bf16 v[36:39], v[120:123], v[16:19], v[36:39]\n" \
  "v_mfma_f32_16x16x32_bf16 v[40:43], v[152:155], v[16:19], v[40:43]\n" \
  "v_mfma_f32_16x16x32_bf16 v[44:47], v[184:187], v[16:19], v[44:47]\n" \
  "v_mfma_f32_16x16x32_bf16 v[48:51], v[216:219], v[16:19], v[48:51]\n" \
  "v_mfma_f32_16x16x32_bf16 v[52:55], v[248:251], v[16:19], v[52:55]\n" \
  "s_waitcnt lgkmcnt(0)\n" \
  "v_mfma_f32_16x16x32_bf16 v[32:35], v[92:95], v[20:23], v[32:35]\n" \
  "v_mfma_f32_16x16x32_bf16 v[36:39], v[124:127], v[20:23], v[36:39]\n" \
  "v_mfma_f32_16x16x32_bf16 v[40:43], v[156:159], v[20:23], v[40:43]\n" \
  "v_mfma_f32_16x16x32_bf16 v[44:47], v[188:191], v[20:23], v[44:47]\n" \
  "v_mfma_f32_16x16x32_bf16 v[48:51], v[220:223], v[20:23], v[48:51]\n" \
  "v_mfma_f32_16x16x32_bf16 v[52:55], v[252:255], v[20:23], v[52:55]\n" \
  "v_add_u32 v0, s66, v0\n" \
  "s_sub_u32 s66, 0, s66\n" \
  "s_add_u32 s54, s54, 0x80000\n" \
  "s_mov_b32 s62, 0x40000\n" \
  "v_cvt_pk_bf16_f32 v24, v32, v33\n" \
  "v_cvt_pk_bf16_f32 v25, v34, v35\n" \
  "v_cvt_pk_bf16_f32 v26, v36, v37\n" \
  "v_cvt_pk_bf16_f32 v27, v38, v39\n" \
  "buffer_store_dwordx4 v[24:27], v1, s[40:43], s64 offen offset:0 sc1\n" \
  "v_cvt_pk_bf16_f32 v28, v40, v41\n" \
  "v_cvt_pk_bf16_f32 v29, v42, v43\n" \
  "v_cvt_pk_bf16_f32 v30, v44, v45\n" \
  "v_cvt_pk_bf16_f32 v31, v46, v47\n" \
  "buffer_store_dwordx4 v[28:31], v1, s[40:43], s64 offen offset:128 sc1\n" \
  "v_cvt_pk_bf16_f32 v56, v48, v49\n" \
  "v_cvt_pk_bf16_f32 v57, v50, v51\n" \
  "v_cvt_pk_bf16_f32 v58, v52, v53\n" \
  "v_cvt_pk_bf16_f32 v59, v54, v55\n" \
  "buffer_store_dwordx4 v[56:59], v1, s[40:43], s64 offen offset:256 sc1\n" \
  "s_cmp_eq_u32 s70, 0\n" \
  "s_cbranch_scc1 .LCY_ND_%=\n" \
  "global_load_lds_dwordx4 v3, s[48:49] offset:2048 sc1\n" \
  "global_load_lds_dwordx4 v3, s[48:49] offset:3072 sc1\n" \
  "s_add_u32 s48, s48, 0x80000\n" \
  "s_addc_u32 s49, s49, 0\n" \
  "s_mov_b32 m0, s57\n" \
  "s_add_u32 s57, s57, s65\n" \
  "global_load_lds_dwordx4 v2, s[50:51] offset:0 sc1\n" \
  "global_load_lds_dwordx4 v2, s[50:51] offset:1024 sc1\n" \
  "s_sub_u32 s65, 0, s65\n" \
  "s_add_u32 s67, s67, 4096\n" \
  "global_load_lds_dwordx4 v2, s[50:51] offset:2048 sc1\n" \
  "s_sub_u32 s68, s68, 1\n" \
  "s_mov_b32 s69, 1\n" \
  "global_load_lds_dwordx4 v2, s[50:51] offset:3072 sc1\n" \
  "s_add_u32 s50, s50, 0x40000\n" \
  "s_addc_u32 s51, s51, 0\n" \
  "v_mov_b32 v4, 0\n" \
  "buffer_load_dword v4, v4, s[44:47], s67 offen sc1\n" \
  "s_waitcnt vmcnt(7)\n" \
  "s_branch .LCY_WC_%=\n" \
  ".LCY_ND_%=:\n" \
  "v_mov_b32 v4, 0\n" \
  "buffer_load_dword v4, v4, s[44:47], s67 offen sc1\n" \
  "s_waitcnt vmcnt(0)\n" \
  ".LCY_WC_%=:\n" \
  "v_mov_b32 v6, s59\n" \
  "v_mov_b32 v7, 1\n" \
  "s_mov_b64 exec, 1\n" \
  "ds_add_rtn_u32 v6, v6, v7\n" \
  "s_mov_b64 exec, -1\n" \
  "s_xor_b32 s59, s59, 4\n" \
  "s_mov_b32 s61, 0\n" \
  "s_waitcnt lgkmcnt(0)\n" \
  "v_readfirstlane_b32 s63, v6\n" \
  "v_lshlrev_b32 v6, 5, v2\n" \
  "s_and_b32 s63, s63, 7\n" \
  "s_cmp_eq_u32 s63, 7\n" \
  "s_cbranch_scc0 .LCY_W1_%=\n" \
  "s_mov_b64 exec, 0xff\n" \
  "global_store_dword v6, v7, s[52:53] sc1\n" \
  "s_mov_b64 exec, -1\n" \
  "s_mov_b32 s61, 1\n" \
  ".LCY_W1_%=:\n" \
  "s_add_u32 s52, s52, 4096\n" \
  "s_addc_u32 s53, s53, 0\n" \
  "s_sub_u32 s60, s60, 1\n" \
  "s_cmp_eq_u32 s60, 0\n" \
  "s_cbranch_scc1 .LCY_END_%=\n" \
  ".LCY_NEED_%=:\n" \
  "s_cmp_eq_u32 s68, 0\n" \
  "s_cbranch_scc1 .LCY_BAR_%=\n" \
  "v_cmp_eq_u32 vcc, 0, v4\n" \
  "s_cmp_eq_u64 vcc, 0\n" \
  "s_cbranch_scc1 .LCY_GET_%=\n" \
  "s_cmp_eq_u32 s68, 1\n" \
  "s_cbranch_scc1 .LCY_BAR_%=\n" \
  "s_cmp_ge_u32 s67, 0x40000\n" \
  "s_cbranch_scc1 .LCY_BAR_%=\n" \
  "s_sleep 12\n" \
  ".LCY_ASK_%=:\n" \
  "v_mov_b32 v4, 0\n" \
  "buffer_load_dword v4, v4, s[44:47], s67 offen sc1\n" \
  "s_sub_u32 s62, s62, 1\n" \
  "s_cbranch_scc1 .LCY_END_%=\n" \
  "s_waitcnt vmcnt(0)\n" \
  "s_branch .LCY_NEED_%=\n" \
  ".LCY_GET_%=:\n" \
  "v_lshlrev_b32 v6, 16, s55\n" \
  "s_mov_b64 exec, 1\n" \
  "ds_add_u32 v5, v6\n" \
  "s_mov_b64 exec, -1\n" \
  "s_mov_b32 m0, s56\n" \
  "s_add_u32 s71, s56, 4096\n" \
  "global_load_lds_dwordx4 v2, s[48:49] offset:0 sc1\n" \
  "global_load_lds_dwordx4 v2, s[48:49] offset:1024 sc1\n" \
  "global_load_lds_dwordx4 v2, s[48:49] offset:2048 sc1\n" \
  "global_load_lds_dwordx4 v2, s[48:49] offset:3072 sc1\n" \
  "s_mov_b32 m0, s71\n" \
  "s_add_u32 s56, s56, s65\n" \
  "global_load_lds_dwordx4 v3, s[48:49] offset:0 sc1\n" \
  "global_load_lds_dwordx4 v3, s[48:49] offset:1024 sc1\n" \
  "global_load_lds_dwordx4 v3, s[48:49] offset:2048 sc1\n" \
  "global_load_lds_dwordx4 v3, s[48:49] offset:3072 sc1\n" \
  "s_add_u32 s48, s48, 0x80000\n" \
  "s_addc_u32 s49, s49, 0\n" \
  "s_mov_b32 m0, s57\n" \
  "s_add_u32 s57, s57, s65\n" \
  "global_load_lds_dwordx4 v2, s[50:51] offset:0 sc1\n" \
  "global_load_lds_dwordx4 v2, s[50:51] offset:1024 sc1\n" \
  "s_sub_u32 s65, 0, s65\n" \
  "s_add_u32 s67, s67, 4096\n" \
  "global_load_lds_dwordx4 v2, s[50:51] offset:2048 sc1\n" \
  "s_sub_u32 s68, s68, 1\n" \
  "s_mov_b32 s69, 1\n" \
  "global_load_lds_dwordx4 v2, s[50:51] offset:3072 sc1\n" \
  "s_add_u32 s50, s50, 0x40000\n" \
  "s_addc_u32 s51, s51, 0\n" \
  "v_mov_b32 v4, 0\n" \
  "buffer_load_dword v4, v4, s[44:47], s67 offen sc1\n" \
  "s_waitcnt vmcnt(0)\n" \
  "v_mov_b32 v6, s55\n" \
  "s_mov_b64 exec, 1\n" \
  "ds_add_u32 v5, v6\n" \
  "s_mov_b64 exec, -1\n" \
  "s_mov_b32 s69, 0\n" \
  "s_branch .LCY_NEED_%=\n" \
  ".LCY_END_%=:\n" \
  "s_endpgm\n"
template <bool AT>
__device__ __forceinline__ void role_aw_t(const MegaArgs& A, char* smem, const int L, const int a) {
  const int w = __builtin_amdgcn_readfirstlane(threadIdx.x >> 6);
  const int l = threadIdx.x & 63, c = l & 15, g = l >> 4;
  const int qt = w & 1, hh = (w >> 1) & 1, h = 2 * a + hh;
  char* ws = A.ws;
  unsigned* FL = (unsigned*)(ws + OFF_FL);
  const unsigned sb = lds_base(smem);
  if (threadIdx.x < 4) reinterpret_cast<unsigned*>(smem + AW_CNT)[threadIdx.x] = 0u;
  constexpr int NP = AT ? 1 : 3;
  const int cp0 = AT ? w : 3 * w - 8;
  v8u wo[2 * NP][4];
  {
    const int q = (l >> 2) & 3, p = l & 3, f = q | ((g & 1) << 2);
    const unsigned ra = sb + (4 * g + q) * 256 + (p & 1) * 16;
#pragma unroll
    for (int r = 0; r < 2; ++r) {
#pragma unroll
      for (int k = 0; k < 2; ++k) {
        const char* base_ = (const char*)A.Wo + ((size_t)((2 * a + r) * DH + 64 * (w & 1) + 32 * k) * DM + 128 * (w >> 1)) * 2;
#pragma unroll
        for (int i = 0; i < 8; ++i) {
          const int fd = g | ((i & 1) << 2);
          glds16(base_, (unsigned)((4 * i + g) * (DM * 2) + ((c ^ (fd << 1)) << 4)), sb + w * 16384 + k * 8192 + i * 1024);
        }
      }
      SST(4 + 3 * r);
      WAIT_VM(0);
      RAW_BAR();
      SST(5 + 3 * r);
#pragma unroll
      for (int k2 = 0; k2 < 2; ++k2) {
#pragma unroll
        for (int ct = 0; ct < 2 * NP; ++ct) {
          const int cp = cp0 + (ct >> 1);
          const unsigned a_ = ra + (cp >> 2) * 32768 + k2 * 16384 + ((2 * (cp & 3) + (p >> 1)) ^ f) * 32 + (ct & 1) * 8;
          const v2u r0 = trd(a_), r1 = trd(a_ + 4096), r2 = trd(a_ + 8192), r3 = trd(a_ + 12288);
          wo[ct][2 * r + k2] = v8u{r0.x, r0.y, r1.x, r1.y, r2.x, r2.y, r3.x, r3.y};
          asm volatile("" : "+v"(wo[ct][2 * r + k2]));
        }
      }
      RAW_BAR();
      SST(6 + 3 * r);
    }
  }
  WSTAMP2();
  const v4u rsW = mk_srd(ws + OFF_PW, SZ_PW);
  const v4u rsF = mk_srd(FL + FL_FQ + (L * 64 * 32 + h) * 32, (64 * 32 - h) * 128);
  const char* p0 = AT ? ws + OFF_MEMK + L * SZ_MEM + h * 2048 : ws + OFF_QK + L * SZ_QK + ((size_t)h * 16 + 8 * qt) * 1024;
  const char* p1 = AT ? (const char*)(FL + FL_FK + L * 32 + (h >> 3) * 64 + (h & 7) * 2) : ws + OFF_VF + L * SZ_O + ((size_t)h * 8 + 4 * qt) * 1024;
  const unsigned* pFW = FL + FL_FW + (L * 64 * 32 + (a >> 2)) * 32 + (a & 3);
  const unsigned tk = sb + AW_TK + hh * 24 * 1024, voff = (c >> 1) * 2048 + (c & 1) * 64 + g * 16;
  const char* dbg = ws + WS_TOTAL + A.vb * 64 * 8;
#define AW_SOPS                                                                                                                 \
  "{s[40:43]}"(rsW), "{s[44:47]}"(rsF), "{s[48:49]}"(p0), "{s[50:51]}"(p1), "{s[52:53]}"(pFW),                                   \
      "{s54}"((L * 64 * 16 + a) * 32768 + cp0 * 128), "{s55}"(1 << (8 * qt)), "{s56}"(tk + qt * 8192), "{s57}"(tk + 16384 + qt * 4096),                  \
      "{s58}"(sb + AW_CNT + 8 + 4 * hh), "{s59}"(sb + AW_CNT), "{s60}"(NSEG), "{s[36:37]}"(dbg)                                  \
      : "memory", "vcc", "scc", "s80"
  if constexpr (AT) {
    const v8u pv = {tk + l * 16, sb + AW_SO + l * 16, (unsigned)(g * 8 + (c & 7)) * 16, (unsigned)(OFF_MEMV - OFF_MEMK) + c * 32 + (g & 1) * 16,
                    voff, (unsigned)l * 16, tk + qt * 4096 + l * 16, sb + AW_SO + w * 4096 + l * 16};
    const unsigned neg = 0xff61b1e6u;
    v8u bt;
#pragma unroll
    for (int i = 0; i < 4; ++i) {
      const unsigned d = 4 * g + i > c ? neg : 0u;
      bt[i] = qt ? 0u : d;
      bt[4 + i] = qt ? d : neg;
    }
    const unsigned e = g >= 2 ? neg : 0u;
    const v4u bm = {e, e, e, e};
    asm volatile("s_mov_b64 exec, -1\ns_waitcnt vmcnt(0) lgkmcnt(0)\ns_nop 4\n" AW_ASM_X
                 ::"{v[192:199]}"(wo[0][0]), "{v[200:207]}"(wo[0][1]), "{v[208:215]}"(wo[0][2]), "{v[216:223]}"(wo[0][3]),
                 "{v[224:231]}"(wo[1][0]), "{v[232:239]}"(wo[1][1]), "{v[240:247]}"(wo[1][2]), "{v[248:255]}"(wo[1][3]),
                 "{v[0:7]}"(pv), "{v[8:15]}"(bt), "{v[16:19]}"(bm), AW_SOPS);
  } else {
    const v4u pv = {sb + AW_SO + l * 16, voff, (unsigned)l * 16, 0u};
    asm volatile("s_mov_b64 exec, -1\ns_waitcnt vmcnt(0) lgkmcnt(0)\ns_nop 4\n" AW_ASM_Y
                 ::"{v[64:71]}"(wo[0][0]), "{v[72:79]}"(wo[0][1]), "{v[80:87]}"(wo[0][2]), "{v[88:95]}"(wo[0][3]),
                 "{v[96:103]}"(wo[1][0]), "{v[104:111]}"(wo[1][1]), "{v[112:119]}"(wo[1][2]), "{v[120:127]}"(wo[1][3]),
                 "{v[128:135]}"(wo[2][0]), "{v[136:143]}"(wo[2][1]), "{v[144:151]}"(wo[2][2]), "{v[152:159]}"(wo[2][3]),
                 "{v[160:167]}"(wo[3][0]), "{v[168:175]}"(wo[3][1]), "{v[176:183]}"(wo[3][2]), "{v[184:191]}"(wo[3][3]),
                 "{v[192:199]}"(wo[4][0]), "{v[200:207]}"(wo[4][1]), "{v[208:215]}"(wo[4][2]), "{v[216:223]}"(wo[4][3]),
                 "{v[224:231]}"(wo[5][0]), "{v[232:239]}"(wo[5][1]), "{v[240:247]}"(wo[5][2]), "{v[248:255]}"(wo[5][3]),
                 "{v[0:3]}"(pv), AW_SOPS);
  }
#undef AW_SOPS
}
__device__ __forceinline__ void role_aw(const MegaArgs& A, char* smem, const int L, const int a) {
  if (__builtin_amdgcn_readfirstlane(threadIdx.x >> 6) < 4) role_aw_t<true>(A, smem, L, a);
  else role_aw_t<false>(A, smem, L, a);
}

constexpr int RG = 2, RW = 2;
__device__ __forceinline__ void role_red(const MegaArgs& A, char* smem, const int L, const int idx) {
  const int w = __builtin_amdgcn_readfirstlane(threadIdx.x >> 6);
  const int l = threadIdx.x & 63;
  const int gr = (w >> 1) & 1, r = idx * 2 + (w & 1);
  char* ws = A.ws;
  unsigned* FL = (unsigned*)(ws + OFF_FL);
  unsigned* TMO = FL + FL_TMO;
  const __amdgpu_buffer_rsrc_t rsW = MKRS(ws + OFF_PW, SZ_PW);
  const __amdgpu_buffer_rsrc_t rsX = MKRS(ws + (L ? OFF_X3 : OFF_XA), SZ_X);
  const __amdgpu_buffer_rsrc_t rsY = L ? MKRS(A.out, SZ_X) : MKRS(ws + OFF_X2, SZ_X);
  volatile unsigned* sw = reinterpret_cast<volatile unsigned*>(smem) + (w >> 1) * 32;
  if (threadIdx.x < 128) reinterpret_cast<volatile unsigned*>(smem)[threadIdx.x] = 0u;
  __syncthreads();
  if ((EXP_OFF & 2) && L == 0 && w < 4) return;
  if ((EXP_OFF & 64) && L == 1 && w < 4) return;
  if ((EXP_OFF & 8) && L == 1 && w >= 4) return;
  unsigned tl = now10();
  if (w >= 4) return;
  if (L == 0) {
    for (int s = gr; s < NSEG; s += RG) ST128((v4u{0u, 0u, 0u, 0u}), rsY, l * 16, (s * SEG + r) * DM * 2);
    DRAIN();
    if (l < 8) set_flag(FL + FL_FZ + (2 * l + (idx >> 3)) * 32 + (idx & 7) * 4 + w);
  }
  for (int s = gr; s < NSEG; s += RG) {
    grp_wait<SLP_R, 4, GATE_R>((w & 1) == 0, sw, s + 1, FL + FL_FW + ((L * 64 + s) * 8 + (blockIdx.x & (NR_W - 1))) * 4 * 32, 16, TMO, tl);
    if (s >= NSEG - 6) tl = now10();
    TS(0, s, gr * RW * 64);
    TS(4, s, gr * RW * 64 + (RW - 1) * 64);
    v4u p[16];
    const v4u xr = LD128(rsX, l * 512, s * 32768 + r * 16);
#pragma unroll
    for (int k = 0; k < 16; ++k)
      p[k] = LD128(rsW, (l >> 2) * 128 + (l & 3) * 16, ((L * 64 + s) * 16 + k) * 32768 + (r >> 1) * 2048 + (r & 1) * 64);
    v4u xo;
#pragma unroll
    for (int e = 0; e < 4; ++e) {
      f32x2_t t[4];
#pragma unroll
      for (int k = 0; k < 4; ++k)
        t[k] = (f32x2_t{lo16(p[4 * k][e]), hi16(p[4 * k][e])} + f32x2_t{lo16(p[4 * k + 1][e]), hi16(p[4 * k + 1][e])}) +
               (f32x2_t{lo16(p[4 * k + 2][e]), hi16(p[4 * k + 2][e])} + f32x2_t{lo16(p[4 * k + 3][e]), hi16(p[4 * k + 3][e])});
      const f32x2_t u = (t[0] + t[1]) + (t[2] + t[3]);
      const unsigned a0 = pk2(u.x, u.y);
      xo[e] = pk2(lo16(xr[e]) + lo16(a0), hi16(xr[e]) + hi16(a0));
      if (L == 0) xo[e] = xo[e] ? xo[e] : 0x8000u;
    }
    ST128(xo, rsY, l * 16, (s * SEG + r) * DM * 2);
    TS(1, s, gr * RW * 64);
    TS(5, s, gr * RW * 64 + (RW - 1) * 64);
    if (L == 0) {
      const unsigned st = row_stats(xo);
      if (l < NR_R) __hip_atomic_store((gu32*)(FL + FL_FR + ((s * 8 + l) * 4 + (r >> 3)) * 32 + (r & 7)), st, RLX_AGENT);
    }
    TS(6, s, gr * RW * 64 + (RW - 1) * 64);
    TS(3, s, gr * RW * 64);
  }
}

constexpr int CUN = 2144;
__device__ __forceinline__ bf16x8 conv_bfrag(const unsigned* up, const unsigned sh) {
  const unsigned d0 = up[0], d1 = up[1], d2 = up[2], d3 = up[3], d4 = up[4];
  const v4u r = {__builtin_amdgcn_alignbit(d1, d0, sh), __builtin_amdgcn_alignbit(d2, d1, sh),
                 __builtin_amdgcn_alignbit(d3, d2, sh), __builtin_amdgcn_alignbit(d4, d3, sh)};
  return BF8(r);
}
__device__ __forceinline__ v2u part_stats(const v4u o) {
  float v[8];
#pragma unroll
  for (int e = 0; e < 4; ++e) { v[2 * e] = lo16(o[e]); v[2 * e + 1] = hi16(o[e]); }
  const float s = ((v[0] + v[1]) + (v[2] + v[3])) + ((v[4] + v[5]) + (v[6] + v[7])), m = s * 0.125f;
  float q0 = 0.f, q1 = 0.f;
#pragma unroll
  for (int e = 0; e < 4; ++e) {
    const float a = v[e] - m, b = v[4 + e] - m;
    q0 += a * a;
    q1 += b * b;
  }
  return v2u{__float_as_uint(s), __float_as_uint(q0 + q1)};
}
__device__ __forceinline__ void role_conv(const MegaArgs& A, char* smem, const int cv) {
  u16* Kr = reinterpret_cast<u16*>(smem);
  u16* Uc = Kr + 8 * 2 * CKN;
  u16* Xr = Uc + 8 * CUN;
  u16* To = Uc + 8 * 2 * CUN;
  u16* Su = To + 32 * 8;
  u16* Sx = Su + 32 * 8;
  const int tid = threadIdx.x;
  const int w = __builtin_amdgcn_readfirstlane(tid >> 6);
  const int l = tid & 63, c = l & 15, g = l >> 4;
  const int d = 8 * cv + w;
  char* ws = A.ws;
  unsigned* FL = (unsigned*)(ws + OFF_FL);
  unsigned* TMO = FL + FL_TMO;
  u16* kr = Kr + w * 2 * CKN;
  float* prm = reinterpret_cast<float*>(kr);
  v4u tx[4];
  {
    const unsigned pa = *reinterpret_cast<const unsigned*>(A.logAre + d * NST + 2 * l);
    const unsigned pb = *reinterpret_cast<const unsigned*>(A.Aim + d * NST + 2 * l);
    const unsigned pc = *reinterpret_cast<const unsigned*>(A.Cre + d * NST + 2 * l);
    const unsigned pd = *reinterpret_cast<const unsigned*>(A.Cim + d * NST + 2 * l);
    const float dt = bfr(expf(bf2f(A.logdt[d])));
    if (w < 2) tx[0] = *reinterpret_cast<const v4u*>(A.x0 + (size_t)tid * DM + 8 * cv);
    if (l < 16) reinterpret_cast<unsigned*>(Uc + w * CUN)[l] = 0u;
    if (l < 32) reinterpret_cast<unsigned*>(Uc + w * CUN + 32 + L_SEQ)[l] = 0u;
    if (l < 32) reinterpret_cast<unsigned*>(Uc + w * CUN + 32 + 128)[l] = 0u;
    if (l < 50) {
      reinterpret_cast<v4u*>(kr)[l < 48 ? 228 + l : 146 + l] = v4u{0u, 0u, 0u, 0u};
      reinterpret_cast<v4u*>(kr + CKN)[l < 48 ? 228 + l : 146 + l] = v4u{0u, 0u, 0u, 0u};
    }
#pragma unroll
    for (int e = 0; e < 2; ++e) {
      const float ar = -bfr(expf(e ? hi16(pa) : lo16(pa))) * dt, ai = (e ? hi16(pb) : lo16(pb)) * dt;
      const float ex = expf(ar);
      float sn, cs;
      sincosf(ai, &sn, &cs);
      prm[2 * l + e] = ar;
      prm[NST + 2 * l + e] = ai;
      prm[2 * NST + 2 * l + e] = (e ? hi16(pc) : lo16(pc)) * dt;
      prm[3 * NST + 2 * l + e] = (e ? hi16(pd) : lo16(pd)) * dt;
      prm[4 * NST + 2 * l + e] = ex * cs;
      prm[5 * NST + 2 * l + e] = ex * sn;
    }
  }
  const float dsk = bf2f(A.Dskip[d]);
  const u16* ka = kr + (c >> 3) * CKN + 2080 - 16 * (c & 7) + 8 * g;
  const unsigned ush = (c & 1) * 16;
#define KFRAG(D0) (*reinterpret_cast<const bf16x8*>(ka - (D0)))
  f32x4 acc[16], far[9];
  v4u xf[3];
  const unsigned* ub1 = reinterpret_cast<const unsigned*>(Uc + w * CUN + (c & ~1) + 8 * g + 32);
  const __amdgpu_buffer_rsrc_t rz1 = MKRS(ws + OFF_XA, SZ_X), rps = MKRS(ws + OFF_H, SZ_X);
  const int t0 = 16 * (4 * (g & 1) + (g >> 1)) + c;
  {
    const u16* uc = Uc + w * CUN;
    const unsigned* ub = reinterpret_cast<const unsigned*>(uc + (c & ~1) + 8 * g + 32);
    const __amdgpu_buffer_rsrc_t rz = MKRS(ws + OFF_XA, SZ_X);
    const __amdgpu_buffer_rsrc_t rst = MKRS(ws + OFF_GT, L_SEQ * 4);
#define TILE_OUT(T)                                                                            \
  {                                                                                            \
    const float u0 = bf2f(uc[32 + 128 * (T) + t0]), u1 = bf2f(uc[64 + 128 * (T) + t0]);        \
    const float a0 = bfr(bfr(y0) + bfr(u0 * dsk)), a1 = bfr(bfr(y1) + bfr(u1 * dsk));          \
    u16* xp = Xr + (128 * (T) + t0) * 8 + w;                                                   \
    xp[0] = f2bf(bf2f(xp[0]) + a0);                                                            \
    xp[256] = f2bf(bf2f(xp[256]) + a1);                                                        \
  }
#define CHUNK_ST(ck)                                                                           \
  {                                                                                            \
    const v4u o = *reinterpret_cast<const v4u*>(Xr + (64 * (ck) + l) * 8);                     \
    ST128(o, rz, (l >> 5) * 32768 + (l & 31) * 16, (2 * (ck) * 64 + cv) * 512);                \
    ST64(part_stats(o), rps, (l >> 5) * 16384 + (l & 31) * 8, (2 * (ck) * 64 + cv) * 256);     \
  }
#define U_ROW(row)                                                                             \
  {                                                                                            \
    const unsigned st_ = __builtin_amdgcn_raw_buffer_load_b32(rst, (row) * 4, 0, AUX_SC1);     \
    const v4u xr_ = *reinterpret_cast<const v4u*>(Xr + (row) * 8);                             \
    const float mb_ = lo16(st_), ri_ = 1.0f / hi16(st_);                                       \
    _Pragma("unroll") for (int e = 0; e < 4; ++e) {                                            \
      const unsigned t_ = pk2(lo16(xr_[e]) - mb_, hi16(xr_[e]) - mb_);                         \
      const unsigned q_ = pk2(lo16(t_) * ri_, hi16(t_) * ri_);                                 \
      Uc[(2 * e) * CUN + 32 + (row)] = (u16)q_;                                                \
      Uc[(2 * e + 1) * CUN + 32 + (row)] = (u16)(q_ >> 16);                                    \
    }                                                                                          \
  }
    const unsigned* FS = FL + FL_FS + (blockIdx.x & 7) * 32 * 32;
    const int gi = 4 * (g * 8 + (l & 7));
    const int tl = 32 * ((l >> 3) & 1) + 8 * g + (l & 7);
    unsigned khl = 0u;
#pragma unroll 1
    for (int ps = 0; ps < 4; ++ps) {
      float v[32];
      {
        const f32x4 ar = *reinterpret_cast<const f32x4*>(prm + gi), ai = *reinterpret_cast<const f32x4*>(prm + NST + gi);
        const f32x4 cr = *reinterpret_cast<const f32x4*>(prm + 2 * NST + gi), ci = *reinterpret_cast<const f32x4*>(prm + 3 * NST + gi);
        const f32x4 wr = *reinterpret_cast<const f32x4*>(prm + 4 * NST + gi), wi = *reinterpret_cast<const f32x4*>(prm + 5 * NST + gi);
        const float p0 = (float)(64 * ps + 32 * ((l >> 3) & 1));
        float yr0[4], yi0[4];
#pragma unroll
        for (int k = 0; k < 4; ++k) {
          const float e = __expf(ar[k] * p0);
          float sn, cs;
          __sincosf(ai[k] * p0, &sn, &cs);
          const float zr = e * cs, zi = e * sn;
          yr0[k] = cr[k] * zr - ci[k] * zi;
          yi0[k] = cr[k] * zi + ci[k] * zr;
        }
        f32x2_t yra = {yr0[0], yr0[1]}, yia = {yi0[0], yi0[1]}, wra = {wr[0], wr[1]}, wia = {wi[0], wi[1]};
        f32x2_t yrb = {yr0[2], yr0[3]}, yib = {yi0[2], yi0[3]}, wrb = {wr[2], wr[3]}, wib = {wi[2], wi[3]};
#pragma unroll
        for (int i = 0; i < 32; ++i) {
          const f32x2_t sm2 = yra + yrb;
          v[i] = sm2.x + sm2.y;
          const f32x2_t ta = yra * wra - yia * wia, tb = yrb * wrb - yib * wib;
          yia = yra * wia + yia * wra;
          yib = yrb * wib + yib * wrb;
          yra = ta;
          yrb = tb;
        }
      }
#pragma unroll
      for (int i = 0; i < 16; ++i) v[i] = swap_add32(v[i], v[i + 16]);
#pragma unroll
      for (int i = 0; i < 8; ++i) {
        v[i] = swap_add16(v[i], v[i + 8]);
        v[i] += dppf<0xB1>(0.f, v[i]);
        v[i] += dppf<0x4E>(0.f, v[i]);
        v[i] += dppf<0x141>(0.f, v[i]);
      }
      float s0 = v[0], s1 = v[1], s2 = v[2], s3 = v[3], s4 = v[4], s5 = v[5], s6 = v[6], s7 = v[7];
      asm volatile("" : "+v"(s0), "+v"(s1), "+v"(s2), "+v"(s3), "+v"(s4), "+v"(s5), "+v"(s6), "+v"(s7));
      s0 = (l & 4) ? s4 : s0; s1 = (l & 4) ? s5 : s1; s2 = (l & 4) ? s6 : s2; s3 = (l & 4) ? s7 : s3;
      s0 = (l & 2) ? s2 : s0; s1 = (l & 2) ? s3 : s1;
      const float kv = 2.0f * ((l & 1) ? s1 : s0);
      khl = pk2(kv, 0.f);
      khl = pk2(kv, kv - lo16(khl));
      kr[2080 - 64 * ps - tl] = (u16)khl;
      kr[CKN + 2080 - 64 * ps - tl] = (u16)(khl >> 16);
      SST(ps);
      if (ps < 2) {
        if (ps == 0) {
          if (w < 2) {
            reinterpret_cast<v4u*>(Xr)[tid] = tx[0];
            (void)wait_n<2, 8>(FL + FL_F0 + ((blockIdx.x & 7) * 8 + 4 * w) * 32, 32, TMO);
            U_ROW(tid);
          }
          __syncthreads();
        }
        f32x4 a0t = {};
#pragma unroll
        for (int b = -1; b < 4; ++b) a0t = MFMA16(KFRAG(-32 * b), conv_bfrag(ub + 16 * b, ush), a0t);
        const float y0 = swap_add32(a0t[0], a0t[1]), y1 = swap_add32(a0t[2], a0t[3]);
        if ((g & 1) == ps) TILE_OUT(0);
        __syncthreads();
        if (w == ps) {
          CHUNK_ST(w);
          DRAIN();
          if (l < 16) set_flag(FL + FL_FC + (((2 * w + (l >> 3)) * 8 + (l & 7)) * 8 + (cv >> 3)) * 32 + (cv & 7));
        }
        SST(4 + ps);
      }
      if (ps == 2) {
        if (w >= 2) tx[0] = *reinterpret_cast<const v4u*>(A.x0 + (size_t)tid * DM + 8 * cv);
#pragma unroll
        for (int i = 1; i < 4; ++i) tx[i] = *reinterpret_cast<const v4u*>(A.x0 + (size_t)(tid + 512 * i) * DM + 8 * cv);
      }
    }
    if (w >= 2) reinterpret_cast<v4u*>(Xr)[tid] = tx[0];
#pragma unroll
    for (int i = 1; i < 4; ++i) reinterpret_cast<v4u*>(Xr)[tid + 512 * i] = tx[i];
    {
      unsigned spins = 0;
      while (!__all(__hip_atomic_load((gu32*)FS + (w + 8 * ((l >> 3) & 3)) * 32 + (l & 7), RLX_AGENT) != 0u)) {
        if (spin_timeout(++spins, TMO)) break;
        __builtin_amdgcn_s_sleep(4);
      }
      asm volatile("" ::: "memory");
    }
    if (w >= 2) U_ROW(tid);
#pragma unroll
    for (int i = 1; i < 4; ++i) U_ROW(tid + 512 * i);
    SST(6);
    const __amdgpu_buffer_rsrc_t rk = MKRS(ws + OFF_KF + (size_t)d * 2 * CKN * 2, 2 * CKN * 2);
#define TAPS(kc, U0, N, HOOK)                                                                  \
  {                                                                                            \
    (void)wait_n<8, 1>(FL + FL_KG + (cv * 3 + (kc)) * 32 + (w >> 1), 1 + (kc), TMO);            \
    HOOK                                                                                       \
    v4u t[2 * (((N) + 63) / 64)];                                                              \
    _Pragma("unroll") for (int i = 0; i < ((N) + 63) / 64; ++i) {                              \
      t[2 * i] = LD128(rk, ((U0) + 64 * i + l) * 16, 0);                                       \
      t[2 * i + 1] = LD128(rk, ((U0) + 64 * i + l) * 16, CKN * 2);                             \
    }                                                                                          \
    _Pragma("unroll") for (int i = 0; i < ((N) + 63) / 64; ++i)                                \
      if (64 * i + l < (N)) {                                                                  \
        reinterpret_cast<v4u*>(kr)[(U0) + 64 * i + l] = t[2 * i];                              \
        reinterpret_cast<v4u*>(kr + CKN)[(U0) + 64 * i + l] = t[2 * i + 1];                    \
      }                                                                                        \
  }
    TAPS(0, 196, 33, );
    SST(7);
    kr[2080 - 192 - tl] = (u16)khl;
    kr[CKN + 2080 - 192 - tl] = (u16)(khl >> 16);
    __syncthreads();
#define BULK(TA, TB, HOOK)                                                                      \
  {                                                                                            \
    const bf16x8 bm = conv_bfrag(ub - 16, ush);                                                \
    _Pragma("unroll") for (int T = (TA); T < (TB); ++T) acc[T] = MFMA16(KFRAG(128 * T + 32), bm, (f32x4{})); \
    _Pragma("unroll 1") for (int r = 0; r < 4; ++r) {                                          \
      bf16x8 bf[TB];                                                                           \
      _Pragma("unroll") for (int j = 0; j < (TB); ++j) bf[j] = conv_bfrag(ub + 16 * r + 64 * j, ush); \
      _Pragma("unroll") for (int e = 0; e < (TB); ++e) {                                       \
        const bf16x8 a = KFRAG(128 * e - 32 * r);                                              \
        _Pragma("unroll") for (int T = (e > (TA) ? e : (TA)); T < (TB); ++T) acc[T] = MFMA16(a, bf[T - e], acc[T]); \
      }                                                                                        \
      if (r == 0) { HOOK }                                                                     \
    }                                                                                          \
  }
    BULK(1, 4, );
#pragma unroll
    for (int T = 1; T < 4; ++T) {
      const float y0 = swap_add32(acc[T][0], acc[T][1]), y1 = swap_add32(acc[T][2], acc[T][3]);
      TILE_OUT(T);
    }
    __syncthreads();
    if (w < 6) CHUNK_ST(2 + w);
    SST(8);
    if (w < 6) {
      DRAIN();
      if (l < 16) set_flag(FL + FL_FC + (((4 + 2 * w + (l >> 3)) * 8 + (l & 7)) * 8 + (cv >> 3)) * 32 + (cv & 7));
    }
    TAPS(1, 0, 196, );
#undef TAPS
    BULK(4, 7, if (w == 1) (void)(wait_n<SLP_V, 32>(FL + FL_FZ + (blockIdx.x & 7) * 64, 64, TMO)););
#pragma unroll
    for (int T = 4; T < 7; ++T) {
      const float y0 = swap_add32(acc[T][0], acc[T][1]), y1 = swap_add32(acc[T][2], acc[T][3]);
      TILE_OUT(T);
    }
    __syncthreads();
    if (w >= 2) {
      CHUNK_ST(6 + w);
#pragma unroll
      for (int i = 0; i < 3; ++i) xf[i] = *reinterpret_cast<const v4u*>(Xr + (64 * (8 + 3 * w + i) + l) * 8);
    }
    SST(10);
    {
      const bf16x8 bm = conv_bfrag(ub - 16, ush);
#pragma unroll
      for (int T = 7; T < 16; ++T) far[T - 7] = MFMA16(KFRAG(128 * T + 32), bm, (f32x4{}));
    }
#define FAR_HALF(r, J0, J1)                                                                     \
  {                                                                                            \
    bf16x8 bf[16], af[16];                                                                     \
    _Pragma("unroll") for (int j = (J0); j < (J1); ++j) bf[j] = conv_bfrag(ub1 + 16 * (r) + 64 * j, ush); \
    _Pragma("unroll") for (int e = (8 - (J1) > 0 ? 8 - (J1) : 0); e < 16 - (J0); ++e) af[e] = KFRAG(128 * e - 32 * (r)); \
    __builtin_amdgcn_sched_barrier(0);                                                         \
    _Pragma("unroll") for (int j = (J0); j < (J1); ++j)                                        \
      _Pragma("unroll") for (int T = (j > 7 ? j : 7); T < 16; ++T) far[T - 7] = MFMA16(af[T - j], bf[j], far[T - 7]); \
  }
#define FAR_PIECE(p)                                                                            \
  {                                                                                            \
    if ((p) < 8) {                                                                             \
      const int r_ = 2 + ((p) >> 2);                                                           \
      if (((p) & 3) == 0) FAR_HALF(r_, 0, 3) else if (((p) & 3) == 1) FAR_HALF(r_, 3, 6)       \
      else if (((p) & 3) == 2) FAR_HALF(r_, 6, 9) else FAR_HALF(r_, 9, 16)                     \
    } else if ((p) == 8) {                                                                     \
                     \
      u16* ucw = Uc + w * CUN + 32 + t0;                                                       \
      _Pragma("unroll") for (int T = 7; T < 16; ++T) {                                         \
        const float y0 = swap_add32(far[T - 7][0], far[T - 7][1]), y1 = swap_add32(far[T - 7][2], far[T - 7][3]); \
        const float u0 = bf2f(ucw[128 * T]), u1 = bf2f(ucw[128 * T + 32]);                     \
        const unsigned yy = pk2(y0, y1), uu = pk2(u0 * dsk, u1 * dsk);                         \
        const unsigned aa = pk2(lo16(yy) + lo16(uu), hi16(yy) + hi16(uu));                     \
        ucw[128 * T] = (u16)aa;                                                                \
        ucw[128 * T + 32] = (u16)(aa >> 16);                                                   \
      }                                                                                        \
    } else if (w >= 2) {                                                                       \
      int l = tid & 63;                                                                        \
      asm volatile("" : "+v"(l), "+v"(xf[0]), "+v"(xf[1]), "+v"(xf[2]));                       \
      if ((p) == 9) {                                                                          \
        _Pragma("unroll") for (int i = 0; i < 3; ++i) {                                        \
          const int ck = 8 + 3 * w + i;                                                        \
          const u16* ur = Uc + 32 + 64 * ck + l;                                               \
          v4u o;                                                                               \
          _Pragma("unroll") for (int e = 0; e < 4; ++e)                                        \
            o[e] = pk2(lo16(xf[i][e]) + bf2f(ur[(2 * e) * CUN]), hi16(xf[i][e]) + bf2f(ur[(2 * e + 1) * CUN])); \
          ST128(o, rz1, (l >> 5) * 32768 + (l & 31) * 16, (2 * ck * 64 + cv) * 512);           \
          ST64(part_stats(o), rps, (l >> 5) * 16384 + (l & 31) * 8, (2 * ck * 64 + cv) * 256); \
        }                                                                                      \
      } else {                                                                                 \
        DRAIN();                                                                               \
        if (l < 48) set_flag(FL + FL_FC + (((16 + 6 * w + (l >> 3)) * 8 + (l & 7)) * 8 + (cv >> 3)) * 32 + (cv & 7)); \
      }                                                                                        \
    }                                                                                          \
  }
    if (w >= 2) {
      DRAIN();
      if (l < 16) set_flag(FL + FL_FC + (((12 + 2 * w + (l >> 3)) * 8 + (l & 7)) * 8 + (cv >> 3)) * 32 + (cv & 7));
    }
#undef BULK
    SST(9);
#undef TILE_OUT
#undef CHUNK_ST
#undef U_ROW
  }
  if (EXP_OFF & 4) {
    for (int p = -8; p < 11; ++p) {
      __syncthreads();
      FAR_PIECE(p);
    }
    return;
  }
  u16* uc = Uc + (8 + w) * CUN;
  const unsigned* ub = reinterpret_cast<const unsigned*>(uc + (c & ~1) + 8 * g + 32);
#pragma unroll
  for (int j = 0; j < 16; ++j) acc[j] = f32x4{};
  const __amdgpu_buffer_rsrc_t rx = MKRS(ws + OFF_X2, SZ_X), rz = MKRS(ws + OFF_X3, SZ_X);
  const __amdgpu_buffer_rsrc_t rf = MKRS(FL + FL_FR, 64 * 32 * 128);
  unsigned st = 0u;
  v2u xx = {0u, 0u};
#define X2_LOAD(ss, far)                                                                                                      \
  {                                                                                                                      \
    xx = LD64(rx, (l >> 1) * (DM * 2) + (l & 1) * 8, (far) ? (int)SZ_X : ((ss) * SEG * DM + 8 * cv) * 2);                 \
    st = __builtin_amdgcn_raw_buffer_load_b32(rf, (l >> 4) * 128 + ((l >> 1) & 7) * 4, ((ss) * 8 + (blockIdx.x & (NR_R - 1))) * 512, AUX_SC1); \
  }
  unsigned tl = now10();
  for (int s = -CVPRE; s < NSEG; ++s) {
   if (s >= 0) {
    if (w == 1) {
      for (unsigned spins = 0; !__all(umin3(xx.x, xx.y, st) != 0u); ++spins) {
        if (spin_timeout(spins, TMO)) break;
        if (GATE_V && spins == 0 && !__any(st != 0u)) gate(tl);
        if (spins && !__all(st != 0u)) __builtin_amdgcn_s_sleep(SLP_V);
        X2_LOAD(s, __builtin_popcountll(__ballot(st != 0u)) < CVTHR);
      }
      tl = now10() + GATE_V;
      TS(0, s, 64);
      const float mb = lo16(st), ri = 1.0f / hi16(st);
      const unsigned t0 = pk2(lo16(xx.x) - mb, hi16(xx.x) - mb), t1 = pk2(lo16(xx.y) - mb, hi16(xx.y) - mb);
      *reinterpret_cast<v2u*>(Su + l * 4) = v2u{pk2(lo16(t0) * ri, hi16(t0) * ri), pk2(lo16(t1) * ri, hi16(t1) * ri)};
      *reinterpret_cast<v2u*>(Sx + l * 4) = xx;
    }
    const int q = s & 3;
    const u16* kq = ka + 32 * q;
    const bf16x8 a0 = *reinterpret_cast<const bf16x8*>(kq - 32), a1 = *reinterpret_cast<const bf16x8*>(kq);
    __syncthreads();
    TS(1, s, 0);
    if (s == 0)
      for (int x = l; x < CUN / 2; x += 64) reinterpret_cast<unsigned*>(uc)[x] = 0u;
    if (l < 32) uc[32 + 32 * s + l] = Su[l * 8 + w];
    asm volatile("s_waitcnt lgkmcnt(0)" ::: "memory");
    const bf16x8 bp = conv_bfrag(ub + 16 * (s - 1), ush);
    acc[0] = MFMA16(a0, bp, (q == 0 ? acc[1] : acc[0]));
    const f32x4 tmp = MFMA16(a1, conv_bfrag(ub + 16 * s, ush), acc[0]);
    float y0 = (q & 1) ? tmp[2] : tmp[0], y1 = (q & 1) ? tmp[3] : tmp[1];
    y0 = xsum32(y0);
    y1 = xsum32(y1);
    if (g == (q >> 1)) {
      const float u0 = bf2f(Su[c * 8 + w]), u1 = bf2f(Su[(16 + c) * 8 + w]);
      const float a0 = bfr(bfr(y0) + bfr(u0 * dsk)), a1 = bfr(bfr(y1) + bfr(u1 * dsk));
      To[c * 8 + w] = f2bf(bf2f(Sx[c * 8 + w]) + a0);
      To[(16 + c) * 8 + w] = f2bf(bf2f(Sx[(16 + c) * 8 + w]) + a1);
    }
    __syncthreads();
    if (w == 0 && l < 32) {
      const v4u o = *reinterpret_cast<const v4u*>(To + l * 8);
      ST128(o, rz, l * 16, (s * 64 + cv) * 512);
      ST64(part_stats(o), rps, l * 8, ((64 + s) * 64 + cv) * 256);
    }
    if (w == 1) X2_LOAD(s + 1, CVTHR > 0);
    const int left = 15 - (s >> 2);
#define PUSH(J0, SH)                                                                                              \
  if (left >= (J0)) {                                                                                             \
    _Pragma("unroll") for (int j = (J0); j < (J0) + 4 && j < 16; ++j)                                             \
      acc[j] = MFMA16(*reinterpret_cast<const bf16x8*>(kq - 32 - 128 * j), bp, (j + (SH) < 16 ? acc[(j + (SH)) & 15] : f32x4{})); \
  }
    TS(2, s, 0);
    if (w == 0) {
      DRAIN();
      if (l < NR_C) set_flag(FL + FL_FC + (((64 + s) * 8 + l) * 8 + (cv >> 3)) * 32 + (cv & 7));
    }
    TS(3, s, 0);
    if (q == 0) { PUSH(1, 1) PUSH(5, 1) PUSH(9, 1) PUSH(13, 1) }
    else { PUSH(1, 0) PUSH(5, 0) PUSH(9, 0) PUSH(13, 0) }
#undef PUSH
    MSTAMP();
   }
    if (s < 19 - CVPRE) FAR_PIECE(s + CVPRE - 8);
  }
#undef KFRAG
#undef X2_LOAD
#undef FAR_PIECE
#undef FAR_HALF
}

__device__ __forceinline__ v4u ln_row_packed(const v4u xr) {
  float v[8];
#pragma unroll
  for (int e = 0; e < 4; ++e) { v[2 * e] = lo16(xr[e]); v[2 * e + 1] = hi16(xr[e]); }
  float s = ((v[0] + v[1]) + (v[2] + v[3])) + ((v[4] + v[5]) + (v[6] + v[7]));
  s = wave_sum(s);
  const float mean = s * (1.0f / DM);
  float q = 0.f;
#pragma unroll
  for (int j = 0; j < 8; ++j) { float t = v[j] - mean; q += t * t; }
  q = wave_sum(q);
  const float var = q * (1.0f / DM);
  const float mb = bfr(mean);
  const float sd = bfr(sqrtf(bfr(bfr(var) + 1e-5f)));
  const float ri = 1.0f / sd;
  v4u o;
#pragma unroll
  for (int e = 0; e < 4; ++e) {
    const unsigned t = pk2(v[2 * e] - mb, v[2 * e + 1] - mb);
    const float x0 = lo16(t), x1 = hi16(t);
    float q0 = x0 * ri, q1 = x1 * ri;
    q0 = __builtin_fmaf(__builtin_fmaf(-q0, sd, x0), ri, q0);
    q1 = __builtin_fmaf(__builtin_fmaf(-q1, sd, x1), ri, q1);
    o[e] = pk2(q0, q1);
  }
  return o;
}

__device__ __forceinline__ float bfr1(float x) { return lo16(pk2(x, x)); }
__device__ __forceinline__ void ln4_packed(v4u (&x)[4]) {
  f32x2_t v[16];
#pragma unroll
  for (int e = 0; e < 16; ++e) v[e] = f32x2_t{lo16(x[e >> 2][e & 3]), hi16(x[e >> 2][e & 3])};
  const f32x2_t t = (((v[0] + v[1]) + (v[2] + v[3])) + ((v[4] + v[5]) + (v[6] + v[7]))) +
                    (((v[8] + v[9]) + (v[10] + v[11])) + ((v[12] + v[13]) + (v[14] + v[15])));
  const float mean = red_mod4(t.x + t.y) * (1.0f / DM);
  const float mb = bfr1(mean);
  f32x2_t q0 = {0.f, 0.f}, q1 = {0.f, 0.f};
#pragma unroll
  for (int e = 0; e < 16; e += 2) {
    v[e] -= mb;
    v[e + 1] -= mb;
    q0 += v[e] * v[e];
    q1 += v[e + 1] * v[e + 1];
  }
  q0 += q1;
  const float dm = mean - mb;
  const float var = fmaxf(red_mod4(q0.x + q0.y) * (1.0f / DM) - dm * dm, 0.f);
  const float sd = bfr1(sqrtf(bfr1(bfr1(var) + 1e-5f)));
  const float ri = 1.0f / sd;
#pragma unroll
  for (int e = 0; e < 16; ++e) {
    const unsigned t2 = pk2(v[e].x, v[e].y);
    const f32x2_t q = f32x2_t{lo16(t2), hi16(t2)} * ri;
    x[e >> 2][e & 3] = pk2(q.x, q.y);
  }
}

#define Q_ASM \
  "s_mov_b32 s79, 0\n" \
  "s_mov_b32 s81, 0\n" \
  "s_mov_b32 s83, 0x8000\n" \
  "s_mov_b32 s76, 0\n" \
  "s_mov_b32 s77, -1\n" \
  "s_branch .LCQ_POLL_%=\n" \
  ".LCQ_TOP_%=:\n" \
  "s_waitcnt lgkmcnt(0)\n" \
  "s_barrier\n" \
  "s_sub_i32 s83, 0, s83\n" \
  "v_add_u32 v0, s83, v0\n" \
  "ds_read_b128 v[18:21], v0 offset:0\n" \
  "ds_read_b128 v[22:25], v0 offset:2048\n" \
  "ds_read_b128 v[26:29], v0 offset:4096\n" \
  "ds_read2_b32 v[6:7], v3 offset0:16 offset1:48\n" \
  "ds_read_b128 v[10:13], v4 offset:256\n" \
  "ds_read_b128 v[14:17], v4 offset:320\n" \
  "ds_read_b32 v8, v3 offset:384\n" \
  "s_add_u32 s71, s71, 0x2000\n" \
  "s_mov_b32 s78, s80\n" \
  "s_xor_b32 s74, s74, 2048\n" \
  "s_xor_b32 s75, s75, 2048\n" \
  "s_setprio 3\n" \
  "ds_read_b128 v[30:33], v0 offset:6144\n" \
  "s_waitcnt lgkmcnt(7)\n" \
  "v_mfma_f32_16x16x32_bf16 v[34:37], v[64:67], v[18:21], 0\n" \
  "v_mfma_f32_16x16x32_bf16 v[38:41], v[128:131], v[18:21], 0\n" \
  "v_mfma_f32_16x16x32_bf16 v[42:45], v[18:21], v[192:195], 0\n" \
  "s_cmp_eq_u32 s78, 1\n" \
  "s_cbranch_scc0 .LCQ_X1_%=\n" \
  "s_mov_b32 m0, s73\n" \
  "s_nop 0\n" \
  "global_load_lds_dwordx4 v2, s[62:63] offset:0 sc1\n" \
  "global_load_lds_dwordx4 v2, s[62:63] offset:1024 sc1\n" \
  "s_add_u32 s62, s62, 0x4000\n" \
  "s_addc_u32 s63, s63, 0\n" \
  ".LCQ_X1_%=:\n" \
  "ds_read_b128 v[18:21], v0 offset:8192\n" \
  "s_waitcnt lgkmcnt(7)\n" \
  "v_mfma_f32_16x16x32_bf16 v[34:37], v[68:71], v[22:25], v[34:37]\n" \
  "s_waitcnt lgkmcnt(4)\n" \
  "v_fma_f32 v48, -v6, v10, v48\n" \
  "v_fma_f32 v49, -v6, v11, v49\n" \
  "v_mfma_f32_16x16x32_bf16 v[38:41], v[132:135], v[22:25], v[38:41]\n" \
  "v_fma_f32 v50, -v6, v12, v50\n" \
  "v_fma_f32 v51, -v6, v13, v51\n" \
  "v_mfma_f32_16x16x32_bf16 v[42:45], v[22:25], v[196:199], v[42:45]\n" \
  "ds_read_b128 v[10:13], v4 offset:64\n" \
  "s_waitcnt lgkmcnt(4)\n" \
  "v_fma_f32 v54, -v6, v14, v54\n" \
  "v_fma_f32 v55, -v6, v15, v55\n" \
  "s_cmp_eq_u32 s78, 1\n" \
  "s_cbranch_scc0 .LCQ_X2_%=\n" \
  "s_mov_b32 m0, s68\n" \
  "s_xor_b32 s68, s68, 0x8000\n" \
  "global_load_lds_dwordx4 v2, s[60:61] offset:0 sc1\n" \
  "global_load_lds_dwordx4 v2, s[60:61] offset:1024 sc1\n" \
  ".LCQ_X2_%=:\n" \
  "ds_read_b128 v[22:25], v0 offset:10240\n" \
  "v_mfma_f32_16x16x32_bf16 v[34:37], v[72:75], v[26:29], v[34:37]\n" \
  "v_fma_f32 v56, -v6, v16, v56\n" \
  "v_fma_f32 v57, -v6, v17, v57\n" \
  "v_mfma_f32_16x16x32_bf16 v[38:41], v[136:139], v[26:29], v[38:41]\n" \
  "ds_read_b128 v[14:17], v4 offset:192\n" \
  "v_mul_f32 v48, v48, v7\n" \
  "v_mul_f32 v49, v49, v7\n" \
  "v_mfma_f32_16x16x32_bf16 v[42:45], v[26:29], v[200:203], v[42:45]\n" \
  "v_mul_f32 v50, v50, v7\n" \
  "v_mul_f32 v51, v51, v7\n" \
  "s_cmp_eq_u32 s78, 1\n" \
  "s_cbranch_scc0 .LCQ_X3_%=\n" \
  "global_load_lds_dwordx4 v2, s[60:61] offset:2048 sc1\n" \
  "global_load_lds_dwordx4 v2, s[60:61] offset:3072 sc1\n" \
  "s_add_u32 s60, s60, 0x8000\n" \
  "s_addc_u32 s61, s61, 0\n" \
  ".LCQ_X3_%=:\n" \
  "ds_read_b128 v[26:29], v0 offset:12288\n" \
  "s_waitcnt lgkmcnt(5)\n" \
  "v_mfma_f32_16x16x32_bf16 v[34:37], v[76:79], v[30:33], v[34:37]\n" \
  "v_cvt_pk_bf16_f32 v48, v48, v49\n" \
  "v_cvt_pk_bf16_f32 v49, v50, v51\n" \
  "v_mfma_f32_16x16x32_bf16 v[38:41], v[140:143], v[30:33], v[38:41]\n" \
  "v_mul_f32 v54, v54, v7\n" \
  "v_mul_f32 v55, v55, v7\n" \
  "v_mfma_f32_16x16x32_bf16 v[42:45], v[30:33], v[204:207], v[42:45]\n" \
  "v_mul_f32 v56, v56, v7\n" \
  "v_mul_f32 v57, v57, v7\n" \
  "s_cmp_eq_u32 s78, 1\n" \
  "s_cbranch_scc0 .LCQ_X4_%=\n" \
  "s_add_u32 s87, s71, 0x2000\n" \
  "v_bfe_u32 v5, v2, 2, 5\n" \
  "buffer_load_dword v5, v5, s[52:55], s87 offen sc1\n" \
  ".LCQ_X4_%=:\n" \
  "ds_read_b128 v[30:33], v0 offset:14336\n" \
  "s_waitcnt lgkmcnt(5)\n" \
  "v_mfma_f32_16x16x32_bf16 v[34:37], v[80:83], v[18:21], v[34:37]\n" \
  "v_cvt_pk_bf16_f32 v54, v54, v55\n" \
  "v_cvt_pk_bf16_f32 v55, v56, v57\n" \
  "v_mfma_f32_16x16x32_bf16 v[38:41], v[144:147], v[18:21], v[38:41]\n" \
  "s_waitcnt lgkmcnt(4)\n" \
  "v_fma_f32 v60, -v10, v8, v60\n" \
  "v_fma_f32 v61, -v11, v8, v61\n" \
  "v_mfma_f32_16x16x32_bf16 v[42:45], v[18:21], v[208:211], v[42:45]\n" \
  "v_fma_f32 v62, -v12, v8, v62\n" \
  "v_fma_f32 v63, -v13, v8, v63\n" \
  "ds_read_b128 v[18:21], v0 offset:16384\n" \
  "s_waitcnt lgkmcnt(4)\n" \
  "v_mfma_f32_16x16x32_bf16 v[34:37], v[84:87], v[22:25], v[34:37]\n" \
  "v_permlane16_swap_b32 v46, v48\n" \
  "v_mfma_f32_16x16x32_bf16 v[38:41], v[148:151], v[22:25], v[38:41]\n" \
  "v_permlane16_swap_b32 v47, v49\n" \
  "v_mfma_f32_16x16x32_bf16 v[42:45], v[22:25], v[212:215], v[42:45]\n" \
  "v_permlane16_swap_b32 v52, v54\n" \
  "ds_read_b128 v[22:25], v0 offset:18432\n" \
  "s_waitcnt lgkmcnt(3)\n" \
  "v_mfma_f32_16x16x32_bf16 v[34:37], v[88:91], v[26:29], v[34:37]\n" \
  "v_permlane16_swap_b32 v53, v55\n" \
  "v_mfma_f32_16x16x32_bf16 v[38:41], v[152:155], v[26:29], v[38:41]\n" \
  "v_mul_f32 v60, v60, v14\n" \
  "v_mul_f32 v61, v61, v15\n" \
  "v_mfma_f32_16x16x32_bf16 v[42:45], v[26:29], v[216:219], v[42:45]\n" \
  "v_mul_f32 v62, v62, v16\n" \
  "v_mul_f32 v63, v63, v17\n" \
  "ds_read_b128 v[26:29], v0 offset:20480\n" \
  "s_waitcnt lgkmcnt(3)\n" \
  "v_mfma_f32_16x16x32_bf16 v[34:37], v[92:95], v[30:33], v[34:37]\n" \
  "v_cvt_pk_bf16_f32 v60, v60, v61\n" \
  "v_cvt_pk_bf16_f32 v61, v62, v63\n" \
  "v_mfma_f32_16x16x32_bf16 v[38:41], v[156:159], v[30:33], v[38:41]\n" \
  "v_mov_b64 v[10:11], 0\n" \
  "v_lshrrev_b32 v12, 1, v2\n" \
  "v_mfma_f32_16x16x32_bf16 v[42:45], v[30:33], v[220:223], v[42:45]\n" \
  "s_cmp_eq_u32 s81, 0\n" \
  "s_cbranch_scc1 .LCQ_S5_%=\n" \
  "s_add_u32 s87, s65, 0x2000\n" \
  "buffer_store_dwordx4 v[52:55], v1, s[40:43], s87 offen sc1\n" \
  "buffer_store_dwordx4 v[58:61], v2, s[44:47], s66 offen sc1\n" \
  "buffer_store_dwordx4 v[46:49], v1, s[40:43], s65 offen sc1\n" \
  "buffer_store_dwordx2 v[10:11], v12, s[48:51], s67 offen sc1\n" \
  "s_add_u32 s65, s65, 0x80000\n" \
  "s_add_u32 s66, s66, 0x40000\n" \
  "s_add_u32 s67, s67, 0x10000\n" \
  "s_mov_b32 s84, s72\n" \
  "s_add_u32 s72, s72, 0x1000\n" \
  "s_mov_b32 s85, s70\n" \
  "s_xor_b32 s70, s70, 4\n" \
  "s_mov_b32 s79, 1\n" \
  ".LCQ_S5_%=:\n" \
  "s_setprio 2\n" \
  "ds_read_b128 v[30:33], v0 offset:22528\n" \
  "s_waitcnt lgkmcnt(3)\n" \
  "v_mfma_f32_16x16x32_bf16 v[34:37], v[96:99], v[18:21], v[34:37]\n" \
  "v_bfe_u32 v7, v2, 4, 5\n" \
  "v_and_b32 v8, 0x200, v2\n" \
  "v_mfma_f32_16x16x32_bf16 v[38:41], v[160:163], v[18:21], v[38:41]\n" \
  "v_lshl_add_u32 v6, v7, 3, s74\n" \
  "v_lshl_add_u32 v6, v8, 1, v6\n" \
  "v_mfma_f32_16x16x32_bf16 v[42:45], v[18:21], v[224:227], v[42:45]\n" \
  "ds_read2_b64 v[10:13], v6 offset0:0 offset1:32\n" \
  "ds_read2_b64 v[14:17], v6 offset0:64 offset1:96\n" \
  "v_lshrrev_b32 v7, 2, v2\n" \
  "s_andn2_b32 s82, s79, s81\n" \
  "s_cbranch_scc0 .LCQ_NPA_%=\n" \
  "s_waitcnt vmcnt(7)\n" \
  "s_cmp_lg_u32 s78, 0\n" \
  "s_cbranch_scc1 .LCQ_PA_%=\n" \
  "s_waitcnt vmcnt(0)\n" \
  ".LCQ_PA_%=:\n" \
  "v_mov_b32 v9, s85\n" \
  "s_mov_b64 exec, 2\n" \
  "ds_add_rtn_u32 v9, v9, v2\n" \
  "s_mov_b64 exec, -1\n" \
  ".LCQ_NPA_%=:\n" \
  "s_cmp_eq_u32 s78, 0\n" \
  "s_cbranch_scc0 .LCQ_X6_%=\n" \
  "v_bfe_u32 v5, v2, 2, 5\n" \
  "buffer_load_dword v5, v5, s[52:55], s71 offen sc1\n" \
  ".LCQ_X6_%=:\n" \
  "ds_read_b128 v[18:21], v0 offset:24576\n" \
  "s_waitcnt lgkmcnt(5)\n" \
  "v_mfma_f32_16x16x32_bf16 v[34:37], v[100:103], v[22:25], v[34:37]\n" \
  "s_waitcnt lgkmcnt(1)\n" \
  "v_add_f32 v6, v10, v12\n" \
  "v_add_f32 v8, v14, v16\n" \
  "v_mfma_f32_16x16x32_bf16 v[38:41], v[164:167], v[22:25], v[38:41]\n" \
  "v_add_f32 v6, v6, v8\n" \
  "v_mov_b32 v8, v6\n" \
  "v_mfma_f32_16x16x32_bf16 v[42:45], v[22:25], v[228:231], v[42:45]\n" \
  "v_add_f32 v11, v11, v13\n" \
  "v_add_f32 v15, v15, v17\n" \
  "ds_read_b128 v[22:25], v0 offset:26624\n" \
  "v_mfma_f32_16x16x32_bf16 v[34:37], v[104:107], v[26:29], v[34:37]\n" \
  "v_permlane32_swap_b32 v6, v8\n" \
  "v_mfma_f32_16x16x32_bf16 v[38:41], v[168:171], v[26:29], v[38:41]\n" \
  "v_add_f32 v6, v6, v8\n" \
  "v_fmamk_f32 v10, v6, 0xbe000000, v10\n" \
  "v_mfma_f32_16x16x32_bf16 v[42:45], v[26:29], v[232:235], v[42:45]\n" \
  "v_fmamk_f32 v12, v6, 0xbe000000, v12\n" \
  "v_fmamk_f32 v14, v6, 0xbe000000, v14\n" \
  "s_cmp_eq_u32 s82, 1\n" \
  "s_cbranch_scc0 .LCQ_X8_%=\n" \
  "s_waitcnt lgkmcnt(2)\n" \
  "v_readlane_b32 s87, v9, 1\n" \
  "s_mov_b32 s79, 0\n" \
  "s_and_b32 s87, s87, 0x70\n" \
  "s_cmp_eq_u32 s87, 0x70\n" \
  "s_cbranch_scc0 .LCQ_F7_%=\n" \
  "s_mov_b64 exec, 2\n" \
  "buffer_store_dword v9, off, s[56:59], s84 sc1\n" \
  "s_mov_b64 exec, -1\n" \
  ".LCQ_F7_%=:\n" \
  ".LCQ_X8_%=:\n" \
  "ds_read_b128 v[26:29], v0 offset:28672\n" \
  "v_mfma_f32_16x16x32_bf16 v[34:37], v[108:111], v[30:33], v[34:37]\n" \
  "v_fmamk_f32 v16, v6, 0xbe000000, v16\n" \
  "v_add_f32 v11, v11, v15\n" \
  "v_mfma_f32_16x16x32_bf16 v[38:41], v[172:175], v[30:33], v[38:41]\n" \
  "v_mul_f32 v10, v10, v10\n" \
  "v_fmac_f32 v10, v12, v12\n" \
  "v_mfma_f32_16x16x32_bf16 v[42:45], v[30:33], v[236:239], v[42:45]\n" \
  "v_fmac_f32 v10, v14, v14\n" \
  "v_fmac_f32 v10, v16, v16\n" \
  "ds_read_b128 v[30:33], v0 offset:30720\n" \
  "s_waitcnt lgkmcnt(3)\n" \
  "v_mfma_f32_16x16x32_bf16 v[34:37], v[112:115], v[18:21], v[34:37]\n" \
  "v_fmamk_f32 v11, v10, 0x3c800000, v11\n" \
  "v_mov_b32 v8, v11\n" \
  "v_mfma_f32_16x16x32_bf16 v[38:41], v[176:179], v[18:21], v[38:41]\n" \
  "v_mul_f32 v6, 0x3b000000, v6\n" \
  "v_add_u32 v7, s69, v7\n" \
  "v_mfma_f32_16x16x32_bf16 v[42:45], v[18:21], v[240:243], v[42:45]\n" \
  "v_permlane32_swap_b32 v11, v8\n" \
  "ds_read_b128 v[18:21], v0 offset:256\n" \
  "s_waitcnt lgkmcnt(3)\n" \
  "v_mfma_f32_16x16x32_bf16 v[34:37], v[116:119], v[22:25], v[34:37]\n" \
  "v_add_f32 v11, v11, v8\n" \
  "v_mul_f32 v10, 0x3b000000, v11\n" \
  "v_mfma_f32_16x16x32_bf16 v[38:41], v[180:183], v[22:25], v[38:41]\n" \
  "v_cvt_pk_bf16_f32 v6, v6, v10\n" \
  "v_and_b32 v10, 0xffff0000, v6\n" \
  "v_mfma_f32_16x16x32_bf16 v[42:45], v[22:25], v[244:247], v[42:45]\n" \
  "v_lshlrev_b32 v6, 16, v6\n" \
  "v_add_f32 v10, 0x3727c5ac, v10\n" \
  "ds_read_b128 v[22:25], v0 offset:2304\n" \
  "s_waitcnt lgkmcnt(3)\n" \
  "v_mfma_f32_16x16x32_bf16 v[34:37], v[120:123], v[26:29], v[34:37]\n" \
  "v_cvt_pk_bf16_f32 v10, v10, v10\n" \
  "v_lshlrev_b32 v10, 16, v10\n" \
  "v_mfma_f32_16x16x32_bf16 v[38:41], v[184:187], v[26:29], v[38:41]\n" \
  "v_sqrt_f32 v10, v10\n" \
  "s_nop 0\n" \
  "v_cvt_pk_bf16_f32 v10, v10, v10\n" \
  "v_mfma_f32_16x16x32_bf16 v[42:45], v[26:29], v[248:251], v[42:45]\n" \
  "v_lshlrev_b32 v10, 16, v10\n" \
  "v_rcp_f32 v10, v10\n" \
  "ds_read_b128 v[26:29], v0 offset:4352\n" \
  "s_waitcnt lgkmcnt(3)\n" \
  "v_mfma_f32_16x16x32_bf16 v[34:37], v[124:127], v[30:33], v[34:37]\n" \
  "s_nop 0\n" \
  "v_cndmask_b32 v10, v6, v10, s[76:77]\n" \
  "ds_write_b32 v7, v10\n" \
  "v_mfma_f32_16x16x32_bf16 v[38:41], v[188:191], v[30:33], v[38:41]\n" \
  "v_mfma_f32_16x16x32_bf16 v[42:45], v[30:33], v[252:255], v[42:45]\n" \
  "s_setprio 1\n" \
  "ds_read_b128 v[30:33], v0 offset:6400\n" \
  "s_waitcnt lgkmcnt(4)\n" \
  "v_mfma_f32_16x16x32_bf16 v[48:51], v[64:67], v[18:21], 0\n" \
  "ds_read2_b32 v[6:7], v3 offset0:0 offset1:32\n" \
  "ds_read_b128 v[10:13], v4 offset:256\n" \
  "ds_read_b128 v[14:17], v4 offset:320\n" \
  "ds_read_b32 v8, v3 offset:384\n" \
  "v_mfma_f32_16x16x32_bf16 v[54:57], v[128:131], v[18:21], 0\n" \
  "v_mfma_f32_16x16x32_bf16 v[60:63], v[18:21], v[192:195], 0\n" \
  "ds_read_b128 v[18:21], v0 offset:8448\n" \
  "s_waitcnt lgkmcnt(8)\n" \
  "v_mfma_f32_16x16x32_bf16 v[48:51], v[68:71], v[22:25], v[48:51]\n" \
  "s_waitcnt lgkmcnt(3)\n" \
  "v_fma_f32 v34, -v6, v10, v34\n" \
  "v_fma_f32 v35, -v6, v11, v35\n" \
  "v_mfma_f32_16x16x32_bf16 v[54:57], v[132:135], v[22:25], v[54:57]\n" \
  "v_fma_f32 v36, -v6, v12, v36\n" \
  "v_fma_f32 v37, -v6, v13, v37\n" \
  "v_mfma_f32_16x16x32_bf16 v[60:63], v[22:25], v[196:199], v[60:63]\n" \
  "ds_read_b128 v[10:13], v4 offset:0\n" \
  "s_waitcnt lgkmcnt(3)\n" \
  "v_fma_f32 v38, -v6, v14, v38\n" \
  "v_fma_f32 v39, -v6, v15, v39\n" \
  "s_mov_b32 s82, 0\n" \
  "s_cmp_eq_u32 s78, 0\n" \
  "s_cbranch_scc0 .LCQ_X9_%=\n" \
  "s_waitcnt vmcnt(0)\n" \
  "v_cmp_eq_u32 vcc, 0, v5\n" \
  "s_cmp_eq_u64 vcc, 0\n" \
  "s_cselect_b32 s78, 2, 0\n" \
  "s_mov_b32 s82, s79\n" \
  ".LCQ_X9_%=:\n" \
  "s_cmp_eq_u32 s82, 1\n" \
  "s_cbranch_scc0 .LCQ_X10_%=\n" \
  "v_mov_b32 v9, s85\n" \
  "s_mov_b64 exec, 2\n" \
  "ds_add_rtn_u32 v9, v9, v2\n" \
  "s_mov_b64 exec, -1\n" \
  ".LCQ_X10_%=:\n" \
  "s_cmp_eq_u32 s78, 2\n" \
  "s_cbranch_scc0 .LCQ_X11_%=\n" \
  "s_mov_b32 m0, s73\n" \
  "s_nop 0\n" \
  "global_load_lds_dwordx4 v2, s[62:63] offset:0 sc1\n" \
  "global_load_lds_dwordx4 v2, s[62:63] offset:1024 sc1\n" \
  "s_add_u32 s62, s62, 0x4000\n" \
  "s_addc_u32 s63, s63, 0\n" \
  ".LCQ_X11_%=:\n" \
  "ds_read_b128 v[22:25], v0 offset:10496\n" \
  "v_mfma_f32_16x16x32_bf16 v[48:51], v[72:75], v[26:29], v[48:51]\n" \
  "v_fma_f32 v40, -v6, v16, v40\n" \
  "v_fma_f32 v41, -v6, v17, v41\n" \
  "v_mfma_f32_16x16x32_bf16 v[54:57], v[136:139], v[26:29], v[54:57]\n" \
  "ds_read_b128 v[14:17], v4 offset:128\n" \
  "v_mul_f32 v34, v34, v7\n" \
  "v_mul_f32 v35, v35, v7\n" \
  "v_mfma_f32_16x16x32_bf16 v[60:63], v[26:29], v[200:203], v[60:63]\n" \
  "v_mul_f32 v36, v36, v7\n" \
  "v_mul_f32 v37, v37, v7\n" \
  "s_cmp_eq_u32 s78, 2\n" \
  "s_cbranch_scc0 .LCQ_X12_%=\n" \
  "s_mov_b32 m0, s68\n" \
  "s_xor_b32 s68, s68, 0x8000\n" \
  "global_load_lds_dwordx4 v2, s[60:61] offset:0 sc1\n" \
  "global_load_lds_dwordx4 v2, s[60:61] offset:1024 sc1\n" \
  ".LCQ_X12_%=:\n" \
  "ds_read_b128 v[26:29], v0 offset:12544\n" \
  "v_mfma_f32_16x16x32_bf16 v[48:51], v[76:79], v[30:33], v[48:51]\n" \
  "v_cvt_pk_bf16_f32 v46, v34, v35\n" \
  "v_cvt_pk_bf16_f32 v47, v36, v37\n" \
  "v_mfma_f32_16x16x32_bf16 v[54:57], v[140:143], v[30:33], v[54:57]\n" \
  "v_mul_f32 v38, v38, v7\n" \
  "v_mul_f32 v39, v39, v7\n" \
  "v_mfma_f32_16x16x32_bf16 v[60:63], v[30:33], v[204:207], v[60:63]\n" \
  "v_mul_f32 v40, v40, v7\n" \
  "v_mul_f32 v41, v41, v7\n" \
  "s_cmp_eq_u32 s82, 1\n" \
  "s_cbranch_scc0 .LCQ_X14_%=\n" \
  "s_waitcnt lgkmcnt(2)\n" \
  "v_readlane_b32 s87, v9, 1\n" \
  "s_mov_b32 s79, 0\n" \
  "s_and_b32 s87, s87, 0x70\n" \
  "s_cmp_eq_u32 s87, 0x70\n" \
  "s_cbranch_scc0 .LCQ_F13_%=\n" \
  "s_mov_b64 exec, 2\n" \
  "buffer_store_dword v9, off, s[56:59], s84 sc1\n" \
  "s_mov_b64 exec, -1\n" \
  ".LCQ_F13_%=:\n" \
  ".LCQ_X14_%=:\n" \
  "s_cmp_eq_u32 s78, 2\n" \
  "s_cbranch_scc0 .LCQ_X15_%=\n" \
  "global_load_lds_dwordx4 v2, s[60:61] offset:2048 sc1\n" \
  "global_load_lds_dwordx4 v2, s[60:61] offset:3072 sc1\n" \
  "s_add_u32 s60, s60, 0x8000\n" \
  "s_addc_u32 s61, s61, 0\n" \
  ".LCQ_X15_%=:\n" \
  "ds_read_b128 v[30:33], v0 offset:14592\n" \
  "s_waitcnt lgkmcnt(5)\n" \
  "v_mfma_f32_16x16x32_bf16 v[48:51], v[80:83], v[18:21], v[48:51]\n" \
  "v_cvt_pk_bf16_f32 v52, v38, v39\n" \
  "v_cvt_pk_bf16_f32 v53, v40, v41\n" \
  "v_mfma_f32_16x16x32_bf16 v[54:57], v[144:147], v[18:21], v[54:57]\n" \
  "s_waitcnt lgkmcnt(4)\n" \
  "v_fma_f32 v42, -v10, v8, v42\n" \
  "v_fma_f32 v43, -v11, v8, v43\n" \
  "v_mfma_f32_16x16x32_bf16 v[60:63], v[18:21], v[208:211], v[60:63]\n" \
  "v_fma_f32 v44, -v12, v8, v44\n" \
  "v_fma_f32 v45, -v13, v8, v45\n" \
  "s_cmp_eq_u32 s78, 2\n" \
  "s_cbranch_scc0 .LCQ_X16_%=\n" \
  "s_add_u32 s87, s71, 0x2000\n" \
  "v_bfe_u32 v5, v2, 2, 5\n" \
  "buffer_load_dword v5, v5, s[52:55], s87 offen sc1\n" \
  ".LCQ_X16_%=:\n" \
  "s_and_b32 s82, s79, s78\n" \
  "s_cmp_eq_u32 s78, 1\n" \
  "s_cbranch_scc0 .LCQ_X17_%=\n" \
  "s_waitcnt vmcnt(0)\n" \
  ".LCQ_X17_%=:\n" \
  "s_cmp_eq_u32 s82, 1\n" \
  "s_cbranch_scc0 .LCQ_X18_%=\n" \
  "v_mov_b32 v9, s85\n" \
  "s_mov_b64 exec, 2\n" \
  "ds_add_rtn_u32 v9, v9, v2\n" \
  "s_mov_b64 exec, -1\n" \
  ".LCQ_X18_%=:\n" \
  "ds_read_b128 v[18:21], v0 offset:16640\n" \
  "s_waitcnt lgkmcnt(4)\n" \
  "v_mfma_f32_16x16x32_bf16 v[48:51], v[84:87], v[22:25], v[48:51]\n" \
  "s_waitcnt lgkmcnt(3)\n" \
  "v_mul_f32 v42, v42, v14\n" \
  "v_mul_f32 v43, v43, v15\n" \
  "v_mfma_f32_16x16x32_bf16 v[54:57], v[148:151], v[22:25], v[54:57]\n" \
  "v_mul_f32 v44, v44, v16\n" \
  "v_mul_f32 v45, v45, v17\n" \
  "v_mfma_f32_16x16x32_bf16 v[60:63], v[22:25], v[212:215], v[60:63]\n" \
  "v_cvt_pk_bf16_f32 v58, v42, v43\n" \
  "v_cvt_pk_bf16_f32 v59, v44, v45\n" \
  "ds_read_b128 v[22:25], v0 offset:18688\n" \
  "s_waitcnt lgkmcnt(3)\n" \
  "v_mfma_f32_16x16x32_bf16 v[48:51], v[88:91], v[26:29], v[48:51]\n" \
  "v_mfma_f32_16x16x32_bf16 v[54:57], v[152:155], v[26:29], v[54:57]\n" \
  "v_mfma_f32_16x16x32_bf16 v[60:63], v[26:29], v[216:219], v[60:63]\n" \
  "s_cmp_eq_u32 s82, 1\n" \
  "s_cbranch_scc0 .LCQ_X20_%=\n" \
  "s_waitcnt lgkmcnt(2)\n" \
  "v_readlane_b32 s87, v9, 1\n" \
  "s_mov_b32 s79, 0\n" \
  "s_and_b32 s87, s87, 0x70\n" \
  "s_cmp_eq_u32 s87, 0x70\n" \
  "s_cbranch_scc0 .LCQ_F19_%=\n" \
  "s_mov_b64 exec, 2\n" \
  "buffer_store_dword v9, off, s[56:59], s84 sc1\n" \
  "s_mov_b64 exec, -1\n" \
  ".LCQ_F19_%=:\n" \
  ".LCQ_X20_%=:\n" \
  "s_cmp_eq_u32 s78, 0\n" \
  "s_cbranch_scc0 .LCQ_X21_%=\n" \
  "v_bfe_u32 v5, v2, 2, 5\n" \
  "buffer_load_dword v5, v5, s[52:55], s71 offen sc1\n" \
  ".LCQ_X21_%=:\n" \
  "ds_read_b128 v[26:29], v0 offset:20736\n" \
  "s_waitcnt lgkmcnt(3)\n" \
  "v_mfma_f32_16x16x32_bf16 v[48:51], v[92:95], v[30:33], v[48:51]\n" \
  "v_mfma_f32_16x16x32_bf16 v[54:57], v[156:159], v[30:33], v[54:57]\n" \
  "v_mfma_f32_16x16x32_bf16 v[60:63], v[30:33], v[220:223], v[60:63]\n" \
  "s_setprio 0\n" \
  "ds_read_b128 v[30:33], v0 offset:22784\n" \
  "s_waitcnt lgkmcnt(3)\n" \
  "v_mfma_f32_16x16x32_bf16 v[48:51], v[96:99], v[18:21], v[48:51]\n" \
  "v_mfma_f32_16x16x32_bf16 v[54:57], v[160:163], v[18:21], v[54:57]\n" \
  "v_mfma_f32_16x16x32_bf16 v[60:63], v[18:21], v[224:227], v[60:63]\n" \
  "ds_read_b128 v[18:21], v0 offset:24832\n" \
  "s_waitcnt lgkmcnt(3)\n" \
  "v_mfma_f32_16x16x32_bf16 v[48:51], v[100:103], v[22:25], v[48:51]\n" \
  "v_mfma_f32_16x16x32_bf16 v[54:57], v[164:167], v[22:25], v[54:57]\n" \
  "v_mfma_f32_16x16x32_bf16 v[60:63], v[22:25], v[228:231], v[60:63]\n" \
  "ds_read_b128 v[22:25], v0 offset:26880\n" \
  "s_waitcnt lgkmcnt(3)\n" \
  "v_mfma_f32_16x16x32_bf16 v[48:51], v[104:107], v[26:29], v[48:51]\n" \
  "v_bfe_u32 v7, v2, 4, 5\n" \
  "v_and_b32 v8, 0x200, v2\n" \
  "v_mfma_f32_16x16x32_bf16 v[54:57], v[168:171], v[26:29], v[54:57]\n" \
  "v_lshl_add_u32 v6, v7, 3, s73\n" \
  "v_lshl_add_u32 v6, v8, 1, v6\n" \
  "v_mfma_f32_16x16x32_bf16 v[60:63], v[26:29], v[232:235], v[60:63]\n" \
  "s_cmp_eq_u32 s78, 2\n" \
  "s_cbranch_scc0 .LCQ_X22_%=\n" \
  "s_waitcnt vmcnt(5)\n" \
  ".LCQ_X22_%=:\n" \
  "ds_read_b128 v[26:29], v0 offset:28928\n" \
  "s_waitcnt lgkmcnt(3)\n" \
  "v_mfma_f32_16x16x32_bf16 v[48:51], v[108:111], v[30:33], v[48:51]\n" \
  "ds_read2_b64 v[10:13], v6 offset0:0 offset1:32\n" \
  "ds_read2_b64 v[14:17], v6 offset0:64 offset1:96\n" \
  "v_lshl_add_u32 v7, v7, 3, s75\n" \
  "v_mfma_f32_16x16x32_bf16 v[54:57], v[172:175], v[30:33], v[54:57]\n" \
  "v_mfma_f32_16x16x32_bf16 v[60:63], v[30:33], v[236:239], v[60:63]\n" \
  "ds_read_b128 v[30:33], v0 offset:30976\n" \
  "s_waitcnt lgkmcnt(5)\n" \
  "v_mfma_f32_16x16x32_bf16 v[48:51], v[112:115], v[18:21], v[48:51]\n" \
  "s_waitcnt lgkmcnt(1)\n" \
  "v_add_f32 v6, v10, v12\n" \
  "v_add_f32 v8, v14, v16\n" \
  "v_mfma_f32_16x16x32_bf16 v[54:57], v[176:179], v[18:21], v[54:57]\n" \
  "v_add_f32 v6, v6, v8\n" \
  "v_mov_b32 v8, v6\n" \
  "v_mfma_f32_16x16x32_bf16 v[60:63], v[18:21], v[240:243], v[60:63]\n" \
  "v_add_f32 v11, v11, v13\n" \
  "v_add_f32 v15, v15, v17\n" \
  "v_mfma_f32_16x16x32_bf16 v[48:51], v[116:119], v[22:25], v[48:51]\n" \
  "v_permlane32_swap_b32 v6, v8\n" \
  "v_mfma_f32_16x16x32_bf16 v[54:57], v[180:183], v[22:25], v[54:57]\n" \
  "v_add_f32 v6, v6, v8\n" \
  "v_fmamk_f32 v10, v6, 0xbe000000, v10\n" \
  "v_mfma_f32_16x16x32_bf16 v[60:63], v[22:25], v[244:247], v[60:63]\n" \
  "v_fmamk_f32 v12, v6, 0xbe000000, v12\n" \
  "v_fmamk_f32 v14, v6, 0xbe000000, v14\n" \
  "v_mfma_f32_16x16x32_bf16 v[48:51], v[120:123], v[26:29], v[48:51]\n" \
  "v_fmamk_f32 v16, v6, 0xbe000000, v16\n" \
  "v_add_f32 v11, v11, v15\n" \
  "v_mfma_f32_16x16x32_bf16 v[54:57], v[184:187], v[26:29], v[54:57]\n" \
  "v_mul_f32 v10, v10, v10\n" \
  "v_fmac_f32 v10, v12, v12\n" \
  "v_mfma_f32_16x16x32_bf16 v[60:63], v[26:29], v[248:251], v[60:63]\n" \
  "v_fmac_f32 v10, v14, v14\n" \
  "v_fmac_f32 v10, v16, v16\n" \
  "s_waitcnt lgkmcnt(0)\n" \
  "v_mfma_f32_16x16x32_bf16 v[48:51], v[124:127], v[30:33], v[48:51]\n" \
  "v_fmamk_f32 v11, v10, 0x3e000000, v11\n" \
  "v_mov_b32 v8, v11\n" \
  "v_mfma_f32_16x16x32_bf16 v[54:57], v[188:191], v[30:33], v[54:57]\n" \
  "v_mov_b32 v10, v6\n" \
  "s_nop 0\n" \
  "v_permlane32_swap_b32 v11, v8\n" \
  "v_mfma_f32_16x16x32_bf16 v[60:63], v[30:33], v[252:255], v[60:63]\n" \
  "v_add_f32 v11, v11, v8\n" \
  "ds_write_b64 v7, v[10:11]\n" \
  "s_cmp_eq_u32 s78, 1\n" \
  "s_cbranch_scc1 .LCQ_NV_%=\n" \
  "s_waitcnt vmcnt(0)\n" \
  ".LCQ_NV_%=:\n" \
  "v_cmp_eq_u32 vcc, 0, v5\n" \
  "s_cmp_eq_u64 vcc, 0\n" \
  "s_cselect_b32 s80, 1, 0\n" \
  "s_sub_u32 s64, s64, 1\n" \
  "s_min_u32 s81, s78, 1\n" \
  "s_cmp_lg_u32 s78, 0\n" \
  "s_cbranch_scc1 .LCQ_TOP_%=\n" \
  "s_cmp_eq_u32 s80, 1\n" \
  "s_cbranch_scc0 .LCQ_X23_%=\n" \
  "s_mov_b32 m0, s73\n" \
  "s_nop 0\n" \
  "global_load_lds_dwordx4 v2, s[62:63] offset:0 sc1\n" \
  "global_load_lds_dwordx4 v2, s[62:63] offset:1024 sc1\n" \
  "s_add_u32 s62, s62, 0x4000\n" \
  "s_addc_u32 s63, s63, 0\n" \
  "s_mov_b32 m0, s68\n" \
  "s_xor_b32 s68, s68, 0x8000\n" \
  "global_load_lds_dwordx4 v2, s[60:61] offset:0 sc1\n" \
  "global_load_lds_dwordx4 v2, s[60:61] offset:1024 sc1\n" \
  "global_load_lds_dwordx4 v2, s[60:61] offset:2048 sc1\n" \
  "global_load_lds_dwordx4 v2, s[60:61] offset:3072 sc1\n" \
  "s_add_u32 s60, s60, 0x8000\n" \
  "s_addc_u32 s61, s61, 0\n" \
  "s_add_u32 s87, s71, 0x2000\n" \
  "v_bfe_u32 v5, v2, 2, 5\n" \
  "buffer_load_dword v5, v5, s[52:55], s87 offen sc1\n" \
  ".LCQ_X23_%=:\n" \
  "ds_read2_b32 v[6:7], v3 offset0:16 offset1:48\n" \
  "ds_read_b128 v[10:13], v4 offset:256\n" \
  "ds_read_b128 v[14:17], v4 offset:320\n" \
  "ds_read_b32 v8, v3 offset:384\n" \
  "s_waitcnt lgkmcnt(2)\n" \
  "v_fma_f32 v48, -v6, v10, v48\n" \
  "v_fma_f32 v49, -v6, v11, v49\n" \
  "v_fma_f32 v50, -v6, v12, v50\n" \
  "v_fma_f32 v51, -v6, v13, v51\n" \
  "ds_read_b128 v[10:13], v4 offset:64\n" \
  "s_waitcnt lgkmcnt(2)\n" \
  "v_fma_f32 v54, -v6, v14, v54\n" \
  "v_fma_f32 v55, -v6, v15, v55\n" \
  "v_fma_f32 v56, -v6, v16, v56\n" \
  "v_fma_f32 v57, -v6, v17, v57\n" \
  "ds_read_b128 v[14:17], v4 offset:192\n" \
  "v_mul_f32 v48, v48, v7\n" \
  "v_mul_f32 v49, v49, v7\n" \
  "v_mul_f32 v50, v50, v7\n" \
  "v_mul_f32 v51, v51, v7\n" \
  "v_cvt_pk_bf16_f32 v48, v48, v49\n" \
  "v_cvt_pk_bf16_f32 v49, v50, v51\n" \
  "v_mul_f32 v54, v54, v7\n" \
  "v_mul_f32 v55, v55, v7\n" \
  "v_mul_f32 v56, v56, v7\n" \
  "v_mul_f32 v57, v57, v7\n" \
  "v_cvt_pk_bf16_f32 v54, v54, v55\n" \
  "v_cvt_pk_bf16_f32 v55, v56, v57\n" \
  "s_waitcnt lgkmcnt(1)\n" \
  "v_fma_f32 v60, -v10, v8, v60\n" \
  "v_fma_f32 v61, -v11, v8, v61\n" \
  "v_fma_f32 v62, -v12, v8, v62\n" \
  "v_fma_f32 v63, -v13, v8, v63\n" \
  "v_permlane16_swap_b32 v46, v48\n" \
  "v_permlane16_swap_b32 v47, v49\n" \
  "v_permlane16_swap_b32 v52, v54\n" \
  "v_permlane16_swap_b32 v53, v55\n" \
  "s_waitcnt lgkmcnt(0)\n" \
  "v_mul_f32 v60, v60, v14\n" \
  "v_mul_f32 v61, v61, v15\n" \
  "v_mul_f32 v62, v62, v16\n" \
  "v_mul_f32 v63, v63, v17\n" \
  "v_cvt_pk_bf16_f32 v60, v60, v61\n" \
  "v_cvt_pk_bf16_f32 v61, v62, v63\n" \
  "v_mov_b64 v[10:11], 0\n" \
  "v_lshrrev_b32 v12, 1, v2\n" \
  "s_add_u32 s87, s65, 0x2000\n" \
  "buffer_store_dwordx4 v[52:55], v1, s[40:43], s87 offen sc1\n" \
  "buffer_store_dwordx4 v[58:61], v2, s[44:47], s66 offen sc1\n" \
  "buffer_store_dwordx4 v[46:49], v1, s[40:43], s65 offen sc1\n" \
  "buffer_store_dwordx2 v[10:11], v12, s[48:51], s67 offen sc1\n" \
  "s_add_u32 s65, s65, 0x80000\n" \
  "s_add_u32 s66, s66, 0x40000\n" \
  "s_add_u32 s67, s67, 0x10000\n" \
  "s_mov_b32 s84, s72\n" \
  "s_add_u32 s72, s72, 0x1000\n" \
  "s_mov_b32 s85, s70\n" \
  "s_xor_b32 s70, s70, 4\n" \
  "s_mov_b32 s79, 1\n" \
  "s_cmp_eq_u32 s80, 0\n" \
  "s_cbranch_scc1 .LCQ_POLL_%=\n" \
  "s_waitcnt vmcnt(4)\n" \
  "s_branch .LCQ_GOT_%=\n" \
  ".LCQ_POLL_%=:\n" \
  "s_mov_b32 s86, 0x40000\n" \
  "s_branch .LCQ_PW_%=\n" \
  ".LCQ_PV_%=:\n" \
  "s_sleep 12\n" \
  ".LCQ_PW_%=:\n" \
  "v_bfe_u32 v5, v2, 2, 5\n" \
  "buffer_load_dword v5, v5, s[52:55], s71 offen sc1\n" \
  "s_sub_u32 s86, s86, 1\n" \
  "s_cbranch_scc1 .LCQ_END_%=\n" \
  "s_waitcnt vmcnt(0)\n" \
  "s_cmp_eq_u32 s79, 0\n" \
  "s_cbranch_scc1 .LCQ_NPD_%=\n" \
  "v_mov_b32 v9, s85\n" \
  "s_mov_b64 exec, 2\n" \
  "ds_add_rtn_u32 v9, v9, v2\n" \
  "s_mov_b64 exec, -1\n" \
  "s_waitcnt lgkmcnt(0)\n" \
  "v_readlane_b32 s87, v9, 1\n" \
  "s_mov_b32 s79, 0\n" \
  "s_and_b32 s87, s87, 0x70\n" \
  "s_cmp_eq_u32 s87, 0x70\n" \
  "s_cbranch_scc0 .LCQ_F24_%=\n" \
  "s_mov_b64 exec, 2\n" \
  "buffer_store_dword v9, off, s[56:59], s84 sc1\n" \
  "s_mov_b64 exec, -1\n" \
  ".LCQ_F24_%=:\n" \
  "s_cmp_eq_u32 s64, 0\n" \
  "s_cbranch_scc1 .LCQ_END_%=\n" \
  ".LCQ_NPD_%=:\n" \
  "v_cmp_eq_u32 vcc, 0, v5\n" \
  "s_cmp_eq_u64 vcc, 0\n" \
  "s_cbranch_scc0 .LCQ_PV_%=\n" \
  "s_mov_b32 m0, s73\n" \
  "s_nop 0\n" \
  "global_load_lds_dwordx4 v2, s[62:63] offset:0 sc1\n" \
  "global_load_lds_dwordx4 v2, s[62:63] offset:1024 sc1\n" \
  "s_add_u32 s62, s62, 0x4000\n" \
  "s_addc_u32 s63, s63, 0\n" \
  "s_mov_b32 m0, s68\n" \
  "s_xor_b32 s68, s68, 0x8000\n" \
  "global_load_lds_dwordx4 v2, s[60:61] offset:0 sc1\n" \
  "global_load_lds_dwordx4 v2, s[60:61] offset:1024 sc1\n" \
  "global_load_lds_dwordx4 v2, s[60:61] offset:2048 sc1\n" \
  "global_load_lds_dwordx4 v2, s[60:61] offset:3072 sc1\n" \
  "s_add_u32 s60, s60, 0x8000\n" \
  "s_addc_u32 s61, s61, 0\n" \
  "s_add_u32 s87, s71, 0x2000\n" \
  "v_bfe_u32 v5, v2, 2, 5\n" \
  "buffer_load_dword v5, v5, s[52:55], s87 offen sc1\n" \
  "s_waitcnt vmcnt(0)\n" \
  ".LCQ_GOT_%=:\n" \
  "v_bfe_u32 v7, v2, 4, 5\n" \
  "v_and_b32 v8, 0x200, v2\n" \
  "v_lshl_add_u32 v6, v7, 3, s73\n" \
  "v_lshl_add_u32 v6, v8, 1, v6\n" \
  "ds_read2_b64 v[10:13], v6 offset0:0 offset1:32\n" \
  "ds_read2_b64 v[14:17], v6 offset0:64 offset1:96\n" \
  "v_lshl_add_u32 v7, v7, 3, s75\n" \
  "s_waitcnt lgkmcnt(0)\n" \
  "v_add_f32 v6, v10, v12\n" \
  "v_add_f32 v8, v14, v16\n" \
  "v_add_f32 v6, v6, v8\n" \
  "v_mov_b32 v8, v6\n" \
  "v_add_f32 v11, v11, v13\n" \
  "v_add_f32 v15, v15, v17\n" \
  "v_permlane32_swap_b32 v6, v8\n" \
  "v_add_f32 v6, v6, v8\n" \
  "v_fmamk_f32 v10, v6, 0xbe000000, v10\n" \
  "v_fmamk_f32 v12, v6, 0xbe000000, v12\n" \
  "v_fmamk_f32 v14, v6, 0xbe000000, v14\n" \
  "v_fmamk_f32 v16, v6, 0xbe000000, v16\n" \
  "v_add_f32 v11, v11, v15\n" \
  "v_mul_f32 v10, v10, v10\n" \
  "v_fmac_f32 v10, v12, v12\n" \
  "v_fmac_f32 v10, v14, v14\n" \
  "v_fmac_f32 v10, v16, v16\n" \
  "v_fmamk_f32 v11, v10, 0x3e000000, v11\n" \
  "v_mov_b32 v8, v11\n" \
  "v_mov_b32 v10, v6\n" \
  "s_nop 0\n" \
  "v_permlane32_swap_b32 v11, v8\n" \
  "v_add_f32 v11, v11, v8\n" \
  "ds_write_b64 v7, v[10:11]\n" \
  "v_cmp_eq_u32 vcc, 0, v5\n" \
  "s_cmp_eq_u64 vcc, 0\n" \
  "s_cselect_b32 s80, 1, 0\n" \
  "s_branch .LCQ_TOP_%=\n" \
  ".LCQ_END_%=:\n" \
  "s_endpgm\n"
__device__ __forceinline__ void role_qkv(const MegaArgs& A, char* smem, const int L, const int h) {
  const int w = __builtin_amdgcn_readfirstlane(threadIdx.x >> 6);
  char* ws = A.ws;
  unsigned* FL = (unsigned*)(ws + OFF_FL);
  const int l = lane_id();
  const int c = l & 15, g = l >> 4;
  const unsigned sb = lds_base(smem);
  v8u wq[8], wk[8], wv[8];
  {
    const int q = (l >> 2) & 3, p = l & 3, f = q | ((g & 1) << 2);
    const unsigned ra = sb + (8 * g + q) * 256 + (w ^ f) * 32 + p * 8;
#define W_STAGE(W, hf, VM, NEXT)                                               \
  {                                                                           \
    WAIT_VM(VM);                                                              \
    RAW_BAR();                                                                \
    _Pragma("unroll") for (int t = 0; t < 4; ++t) {                           \
      const unsigned a_ = ra + (hf) * 8192 + t * 32768;                       \
      const v2u r0 = trd(a_), r1 = trd(a_ + 1024), r2 = trd(a_ + 16384), r3 = trd(a_ + 16384 + 1024); \
      W[4 * (hf) + t] = v8u{r0.x, r0.y, r1.x, r1.y, r2.x, r2.y, r3.x, r3.y};  \
    }                                                                         \
    _Pragma("unroll") for (int t = 0; t < 4; ++t) asm volatile("" : "+v"(W[4 * (hf) + t])); \
    RAW_BAR();                                                                \
    NEXT;                                                                     \
  }
    wqkv_issue<false>(A.Wq, 0, h, w, l, sb);
    wqkv_issue<false>(A.Wq, 1, h, w, l, sb);
    W_STAGE(wq, 0, 8, wqkv_issue<false>(A.Wk, 0, h, w, l, sb));
    SST(4);
    W_STAGE(wq, 1, 8, wqkv_issue<false>(A.Wk, 1, h, w, l, sb));
    SST(5);
    W_STAGE(wk, 0, 8, wqkv_issue<false>(A.Wv, 0, h, w, l, sb));
    W_STAGE(wk, 1, 8, wqkv_issue<false>(A.Wv, 1, h, w, l, sb));
    SST(6);
    W_STAGE(wv, 0, 8, (void)0);
    W_STAGE(wv, 1, 0, (void)0);
#undef W_STAGE
  }
  WSTAMP2();
  constexpr int Q_ST = 65536, Q_PR = Q_ST + 8192, Q_WP = Q_PR + 16384, Q_CN = Q_WP + 4096;
  {
    const v4u one = {0x3f803f80u, 0x3f803f80u, 0x3f803f80u, 0x3f803f80u};
    f32x4 csq = {}, csk = {}, t = {};
#define HALF(x, i) BF8(((i) ? __builtin_shufflevector(x, x, 4, 5, 6, 7) : __builtin_shufflevector(x, x, 0, 1, 2, 3)))
#pragma unroll
    for (int ks = 0; ks < 16; ++ks) {
      csq = MFMA16(HALF(wq[ks >> 1], ks & 1), BF8(one), csq);
      csk = MFMA16(HALF(wk[ks >> 1], ks & 1), BF8(one), csk);
      t = MFMA16(BF8(one), HALF(wv[ks >> 1], ks & 1), t);
    }
#undef HALF
    float* cp = reinterpret_cast<float*>(smem + Q_ST + w * 1024 + 256);
    *reinterpret_cast<f32x4*>(cp + 4 * g) = csq;
    *reinterpret_cast<f32x4*>(cp + 16 + 4 * g) = csk;
    cp[32 + c] = t[0];
  }
  if (threadIdx.x < 2) reinterpret_cast<unsigned*>(smem + Q_CN)[threadIdx.x] = 0u;
  const int rc = blockIdx.x & (NR_C - 1);
  const v4u rsQ = mk_srd(ws + OFF_QK + L * SZ_QK, SZ_QK);
  const v4u rsV = mk_srd(ws + OFF_VF + L * SZ_O, SZ_O);
  const v4u rsM = mk_srd(ws + (w >= 4 ? OFF_MEMV : OFF_MEMK) + L * SZ_MEM, SZ_MEM);
  const v4u rsF = mk_srd(FL + FL_FC + (L * 64 * 8 + rc) * 8 * 32 + w * 32, (64 * 64 - rc * 8 - w) * 128);
  const v4u rsFQ = mk_srd(FL + FL_FQ + (L * 64 * 32 + h) * 32, 64 * 4096 - h * 128);
  const char* Xb = ws + (L ? OFF_X3 : OFF_XA) + w * 4096;
  const char* Sb = ws + OFF_H + L * (SZ_X / 2) + w * 2048;
  v8u pv;
  pv[0] = sb + 32768 + g * 512 + c * 16;
  pv[1] = (c + 16 * (2 * (w & 1) + (g >> 1))) * 16 + 4096 * (g & 1);
  pv[2] = l * 16;
  pv[3] = sb + Q_ST + w * 1024 + c * 4;
  pv[4] = sb + Q_ST + w * 1024 + g * 16;
  pv[5] = pv[6] = pv[7] = 0u;
  const char* dbg = nullptr;
  asm volatile("s_mov_b64 exec, -1\ns_waitcnt vmcnt(0) lgkmcnt(0)\ns_nop 4\n" Q_ASM
               ::"{v[64:71]}"(wq[0]), "{v[72:79]}"(wq[1]), "{v[80:87]}"(wq[2]), "{v[88:95]}"(wq[3]), "{v[96:103]}"(wq[4]),
               "{v[104:111]}"(wq[5]), "{v[112:119]}"(wq[6]), "{v[120:127]}"(wq[7]), "{v[128:135]}"(wk[0]), "{v[136:143]}"(wk[1]),
               "{v[144:151]}"(wk[2]), "{v[152:159]}"(wk[3]), "{v[160:167]}"(wk[4]), "{v[168:175]}"(wk[5]), "{v[176:183]}"(wk[6]),
               "{v[184:191]}"(wk[7]), "{v[192:199]}"(wv[0]), "{v[200:207]}"(wv[1]), "{v[208:215]}"(wv[2]), "{v[216:223]}"(wv[3]),
               "{v[224:231]}"(wv[4]), "{v[232:239]}"(wv[5]), "{v[240:247]}"(wv[6]), "{v[248:255]}"(wv[7]), "{v[0:7]}"(pv),
               "{s[40:43]}"(rsQ), "{s[44:47]}"(rsV), "{s[48:51]}"(rsM), "{s[52:55]}"(rsF), "{s[56:59]}"(rsFQ), "{s[60:61]}"(Xb),
               "{s[62:63]}"(Sb), "{s64}"(NSEG), "{s65}"((h * 16 + (w >> 1)) * 1024), "{s66}"((h * 512 + w * 64) * 16),
               "{s67}"(h * 2048 + (w & 3) * 512), "{s68}"(sb + w * 4096), "{s69}"(sb + Q_ST + w * 1024), "{s70}"(sb + Q_CN),
               "{s71}"(0), "{s72}"(0), "{s73}"(sb + Q_PR + w * 2048), "{s74}"(sb + Q_WP + 2048), "{s75}"(sb + Q_WP + w * 256), "{s[36:37]}"(dbg)
               : "memory", "vcc", "scc", "s88", "v63");
}

constexpr int MEGA_WGS = 256;
constexpr size_t MEGA_LDS = (size_t)(32 * 4 * 64 * 8 + 8 * SMS + 15 * QS + 128 + 16 * 64 * 8 + 8 * SOS + 512) * 2;
static_assert(MEGA_LDS <= 163840, "lds");
static_assert(MEGA_LDS >= (size_t)AW_CNT + 64, "lds");
static_assert(MEGA_LDS >= (size_t)(8 * 2 * CKN + 8 * 2 * CUN + 4 * 32 * 8) * 2, "lds");
static_assert(MEGA_LDS >= (size_t)2 * 32 * SMS * 2 && MEGA_LDS >= 131072, "lds");
static_assert(MEGA_LDS >= (size_t)16 * (6 * NST + 9 * 132) * 4, "lds");
__global__ __launch_bounds__(512, 2) void mega_kernel(const u16* mem0, const u16* Dskip, char* ws, u16* out, const u16* x0,
                                                      const u16* logAre, const u16* Aim, const u16* Cre, const u16* Cim,
                                                      const u16* logdt, const u16* Wq, const u16* Wk, const u16* Wv,
                                                      const u16* Wo) {
  extern __shared__ __attribute__((aligned(16))) char smem[];
  const MegaArgs A{mem0, Dskip, ws, out, x0, logAre, Aim, Cre, Cim, logdt, Wq, Wk, Wv, Wo, vbid()};
  const int b = A.vb;
  {
    const unsigned t0 = now10();
    const int kc = (b >= 32 && b < 64) ? 2 - KC0 : (b >= 200 && b < 232) ? 1 : ((b >= 80 && b < 96) || b >= 240) ? KC0 : -1;
    if (kc >= 0) start_kgen(A, smem, kc, b >= 240 ? b - 224 : b >= 200 ? b - 200 : b >= 80 ? b - 80 : b - 32);
    else if ((b >= 64 && b < 104) || b >= 232) start_stats(A, b >= 232 ? b - 224 : b >= 96 ? b - 96 : b - 48);
    const unsigned dl = (b >= 32 && b < 64) ? DL_C : (b >= 80 && b < 96) ? DL_A : (b >= 200 && b < 232) ? DL_Q : 0u;
    while (now10() - t0 < dl) __builtin_amdgcn_s_sleep(16);
  }
  if ((EXP_OFF & 1) && b >= 64 && b < 80) return;
  if ((EXP_OFF & 8) && b >= 200 && b < 232) return;
  if ((EXP_OFF & 16) && b >= 32 && b < 64) return;
  if ((EXP_OFF & 32) && b >= 80 && b < 96) return;
  if (b < 64) role_chain(A, smem, b >> 5, b & 31, 0);
  else if (b < 96) role_aw(A, smem, (b >> 4) & 1, b & 15);
  else if (b < 104) role_red(A, smem, 0, b - 96);
  else if (b < 168) role_conv(A, smem, b - 104);
  else if (b < 232) role_qkv(A, smem, (b - 168) >> 5, (b - 168) & 31);
  else if (b < 240) role_red(A, smem, 0, b - 224);
  else role_red(A, smem, 1, b - 240);
}

extern "C" void kernel_launch(void* const* d_in, const int* in_sizes, int n_in, void* d_out, int out_size, void* d_ws,
                              size_t ws_size, hipStream_t stream) {
  MegaArgs A;
  A.x0 = (const u16*)d_in[0];
  A.logAre = (const u16*)d_in[1];
  A.Aim = (const u16*)d_in[2];
  A.Cre = (const u16*)d_in[3];
  A.Cim = (const u16*)d_in[4];
  A.logdt = (const u16*)d_in[5];
  A.Dskip = (const u16*)d_in[6];
  A.mem0 = (const u16*)d_in[8];
  A.Wq = (const u16*)d_in[9];
  A.Wk = (const u16*)d_in[10];
  A.Wv = (const u16*)d_in[11];
  A.Wo = (const u16*)d_in[12];
  A.ws = (char*)d_ws;
  A.out = (u16*)d_out;

  (void)hipFuncSetAttribute((const void*)mega_kernel, hipFuncAttributeMaxDynamicSharedMemorySize, (int)MEGA_LDS);
  zero_kernel<<<ZBLK, 256, 0, stream>>>((uint4*)(A.ws + OFF_Z));
  mega_kernel<<<MEGA_WGS, 512, MEGA_LDS, stream>>>(A.mem0, A.Dskip, A.ws, A.out, A.x0, A.logAre, A.Aim, A.Cre, A.Cim, A.logdt,
                                                   A.Wq, A.Wk, A.Wv, A.Wo);
}
```

```cpp
#include <hip/hip_runtime.h>
#include <hip/hip_bf16.h>
#include <stdint.h>

constexpr int L_SEQ = 2048;
constexpr int DM    = 512;
constexpr int NST   = 128;
constexpr int NH    = 32;
constexpr int DH    = 128;
constexpr int HD    = NH * DH;
constexpr int MM    = 8;
constexpr int SEG   = 32;
constexpr int NSEG  = L_SEQ / SEG;
constexpr int LK    = MM + SEG;

typedef unsigned short u16;
using bf16x8 = __attribute__((ext_vector_type(8))) short;
using f32x4  = __attribute__((ext_vector_type(4))) float;
using f32x16 = __attribute__((ext_vector_type(16))) float;

static __device__ __forceinline__ u16 f2bf(float f) {
  unsigned u = __float_as_uint(f);
  u += 0x7fffu + ((u >> 16) & 1u);
  return (u16)(u >> 16);
}
static __device__ __forceinline__ float bf2f(u16 h) { return __uint_as_float(((unsigned)h) << 16); }
static __device__ __forceinline__ float bfr(float f) { return bf2f(f2bf(f)); }

constexpr size_t SZ_X      = (size_t)L_SEQ * DM * 2;
constexpr size_t OFF_KF    = 0;
constexpr size_t OFF_UT    = OFF_KF + (size_t)DM * L_SEQ * 4;
constexpr size_t OFF_YT    = OFF_UT + SZ_X;
constexpr size_t OFF_XA    = OFF_YT + SZ_X;
constexpr size_t OFF_H     = OFF_XA + SZ_X;
constexpr size_t OFF_X2    = OFF_H + SZ_X;
constexpr size_t OFF_U2    = OFF_X2 + SZ_X;
constexpr size_t OFF_X3    = OFF_U2 + SZ_X;
constexpr size_t SZ_QK     = (size_t)L_SEQ * 2 * HD * 2;
constexpr size_t OFF_QK    = OFF_X3 + SZ_X;
constexpr size_t SZ_O      = (size_t)L_SEQ * HD * 2;
constexpr size_t OFF_VF    = OFF_QK + 2 * SZ_QK;
constexpr size_t SZ_PW     = (size_t)2 * NSEG * 16 * SEG * DM * 2;
constexpr size_t OFF_PW    = OFF_VF + 2 * SZ_O;
constexpr size_t SZ_MEM    = (size_t)NSEG * MM * HD * 2;
constexpr size_t OFF_MEMK  = OFF_PW + SZ_PW;
constexpr size_t OFF_MEMV  = OFF_MEMK + 2 * SZ_MEM;
constexpr size_t OFF_GT    = OFF_MEMV + 2 * SZ_MEM;
constexpr size_t OFF_EP    = OFF_GT + 65536;
constexpr size_t OFF_RD    = OFF_EP + 4096;
constexpr size_t OFF_F0    = OFF_RD + 4096;
constexpr size_t OFF_G     = OFF_F0 + 8192;
constexpr size_t SZ_ACC    = (size_t)2 * 32768;
constexpr size_t OFF_Z     = OFF_GT + (size_t)64 * DM * 4;
constexpr size_t OFF_ACC   = OFF_Z;
constexpr size_t OFF_FL    = OFF_ACC + 2 * SZ_ACC;
constexpr int FL_FK  = 0;
constexpr int FL_FW  = FL_FK + 64 * 8 * 32;
constexpr int FL_FR  = FL_FW + 2 * 64 * 32 * 32;
constexpr int FL_FC  = FL_FR + 64 * 32 * 32;
constexpr int FL_FQ  = FL_FC + 2 * 64 * 64 * 32;
constexpr int FL_FS  = FL_FQ + 2 * 64 * 32 * 32;
constexpr int FL_FZ  = FL_FS + 8 * 32 * 32;
constexpr int FL_KG  = FL_FZ + 16 * 32;
constexpr int FL_TMO = FL_KG + 192 * 32;
constexpr int FL_END = FL_TMO + 64;
constexpr size_t Z_BYTES   = OFF_FL + (size_t)FL_END * 4 - OFF_Z;
constexpr size_t WS_TOTAL  = OFF_Z + Z_BYTES;
static_assert(Z_BYTES % 16 == 0 && OFF_Z % 256 == 0 && Z_BYTES <= 5 * 64 * 8192 && OFF_G + 8192 <= OFF_Z, "zero region");
static_assert(WS_TOTAL + (4 << 20) <= (size_t)256 * 1024 * 1024, "workspace too large");

static_assert(WS_TOTAL - OFF_XA <= (size_t)384 * 8 * 65536, "translation warm-up");
constexpr int CKN = 2208;
static_assert((size_t)DM * 2 * CKN * 2 <= (size_t)DM * L_SEQ * 4 + SZ_X, "K buffer");
__device__ __forceinline__ float pos_of_index(int p) {
  if (p < 256) return (float)p;
  if (p < 384) return (float)(256 + 2 * (p - 256));
  if (p < 512) return (float)(512 + 4 * (p - 384));
  if (p < 640) return (float)(1024 + 8 * (p - 512));
  return 2048.0f;
}
__device__ __forceinline__ int index_of_l(int l) {
  float pf = bfr((float)l);
  int p = (int)pf;
  if (p < 256) return p;
  if (p < 512) return 256 + ((p - 256) >> 1);
  if (p < 1024) return 384 + ((p - 512) >> 2);
  if (p < 2048) return 512 + ((p - 1024) >> 3);
  return 640;
}

template <int CTRL> __device__ __forceinline__ float dppr(float x) {
  return __builtin_bit_cast(float, __builtin_amdgcn_update_dpp(0, __builtin_bit_cast(int, x), CTRL, 0xf, 0xf, false));
}
__device__ __forceinline__ float wave_sum(float v) {
  v += dppr<0x121>(v);
  v += dppr<0x122>(v);
  v += dppr<0x124>(v);
  v += dppr<0x128>(v);
  {
    auto r = __builtin_amdgcn_permlane16_swap(__float_as_uint(v), __float_as_uint(v), false, false);
    v = __uint_as_float(r[0]) + __uint_as_float(r[1]);
  }
  {
    auto r = __builtin_amdgcn_permlane32_swap(__float_as_uint(v), __float_as_uint(v), false, false);
    v = __uint_as_float(r[0]) + __uint_as_float(r[1]);
  }
  return v;
}
__device__ __forceinline__ void ln_row8(float (&v)[8]) {
  float s = 0.f;
#pragma unroll
  for (int j = 0; j < 8; ++j) s += v[j];
  s = wave_sum(s);
  const float mean = s * (1.0f / DM);
  float q = 0.f;
#pragma unroll
  for (int j = 0; j < 8; ++j) { float t = v[j] - mean; q += t * t; }
  q = wave_sum(q);
  const float var = q * (1.0f / DM);
  const float mb = bfr(mean);
  const float sd = bfr(sqrtf(bfr(bfr(var) + 1e-5f)));
#pragma unroll
  for (int j = 0; j < 8; ++j) v[j] = bfr(bfr(v[j] - mb) / sd);
}

#define RLX_AGENT __ATOMIC_RELAXED, __HIP_MEMORY_SCOPE_AGENT
typedef __attribute__((address_space(1))) unsigned gu32;
typedef unsigned v4u __attribute__((ext_vector_type(4)));
constexpr int AUX_SC1 = 16;
constexpr unsigned SPIN_CAP = 1u << 18;

__device__ __forceinline__ bool wait_flags(const unsigned* flags, int lane, int nfl, unsigned want, unsigned* tmo) {
  for (unsigned spins = 0;; ++spins) {
    unsigned v = want;
    if (lane < nfl) v = __hip_atomic_load((gu32*)flags + lane * 32, RLX_AGENT);
    if (__all(v >= want)) break;
    if (spins > SPIN_CAP) {
      if (lane == 0) __hip_atomic_store((gu32*)tmo, 1u, RLX_AGENT);
      return false;
    }
  }
  __builtin_amdgcn_fence(__ATOMIC_ACQUIRE, "wavefront");
  return true;
}

#define STAMP(i)
#define STAMP_DUMP()
#define WSTAMP(i)
template <int CTRL> __device__ __forceinline__ float dppf(float old, float x) {
  return __builtin_bit_cast(float, __builtin_amdgcn_update_dpp(__builtin_bit_cast(int, old), __builtin_bit_cast(int, x), CTRL, 0xf, 0xf, false));
}
__device__ __forceinline__ float wave_max_dpp(float x) {
  const float ninf = -3e38f;
  x = fmaxf(x, dppf<0x111>(ninf, x));
  x = fmaxf(x, dppf<0x112>(ninf, x));
  x = fmaxf(x, dppf<0x114>(ninf, x));
  x = fmaxf(x, dppf<0x118>(ninf, x));
  x = fmaxf(x, dppf<0x142>(ninf, x));
  x = fmaxf(x, dppf<0x143>(ninf, x));
  return __builtin_bit_cast(float, __builtin_amdgcn_readlane(__builtin_bit_cast(int, x), 63));
}
__device__ __forceinline__ float wave_sum_dpp(float x) {
  x += dppf<0x111>(0.f, x);
  x += dppf<0x112>(0.f, x);
  x += dppf<0x114>(0.f, x);
  x += dppf<0x118>(0.f, x);
  x += dppf<0x142>(0.f, x);
  x += dppf<0x143>(0.f, x);
  return __builtin_bit_cast(float, __builtin_amdgcn_readlane(__builtin_bit_cast(int, x), 63));
}
constexpr int SMS = 528;
constexpr int QS = 144;
constexpr int VS = 80;
constexpr int SOS = 144;
#define VAR_NREP 4
constexpr int NREP = VAR_NREP;
__device__ __forceinline__ float xsum16(float x) {
  auto r = __builtin_amdgcn_permlane16_swap(__float_as_uint(x), __float_as_uint(x), false, false);
  return __uint_as_float(r[0]) + __uint_as_float(r[1]);
}
__device__ __forceinline__ float xsum32(float x) {
  auto r = __builtin_amdgcn_permlane32_swap(__float_as_uint(x), __float_as_uint(x), false, false);
  return __uint_as_float(r[0]) + __uint_as_float(r[1]);
}
__device__ __forceinline__ float xmax16(float x) {
  auto r = __builtin_amdgcn_permlane16_swap(__float_as_uint(x), __float_as_uint(x), false, false);
  return fmaxf(__uint_as_float(r[0]), __uint_as_float(r[1]));
}
__device__ __forceinline__ float xmax32(float x) {
  auto r = __builtin_amdgcn_permlane32_swap(__float_as_uint(x), __float_as_uint(x), false, false);
  return fmaxf(__uint_as_float(r[0]), __uint_as_float(r[1]));
}
__device__ __forceinline__ float red_mod4(float x) {
  x += dppf<0x124>(0.f, x);
  x += dppf<0x128>(0.f, x);
  return xsum32(xsum16(x));
}
typedef __bf16 bf16x2_t __attribute__((ext_vector_type(2)));
typedef float f32x2_t __attribute__((ext_vector_type(2)));
__device__ __forceinline__ unsigned pk2(float a, float b) {
  f32x2_t v = {a, b};
  bf16x2_t r = __builtin_convertvector(v, bf16x2_t);
  return __builtin_bit_cast(unsigned, r);
}
__device__ __forceinline__ float one(float x) {
  asm("" : "+v"(x));
  return x;
}
__device__ __forceinline__ unsigned pk2s(float a, float b) { return pk2(one(a), one(b)); }
__device__ __forceinline__ float dot2sq(const unsigned x, const float acc) {
  return __builtin_amdgcn_fdot2_f32_bf16(__builtin_bit_cast(bf16x2_t, x), __builtin_bit_cast(bf16x2_t, x), acc, false);
}
__device__ __forceinline__ float lo16(unsigned u) { return __uint_as_float(u << 16); }
__device__ __forceinline__ float hi16(unsigned u) { return __uint_as_float(u & 0xffff0000u); }
__device__ __forceinline__ void scale_pair(float a, float b, float scale, float& oa, float& ob) {
  unsigned u = pk2(a, b);
  unsigned v = pk2s(lo16(u) * scale, hi16(u) * scale);
  oa = lo16(v);
  ob = hi16(v);
}

#define XMAP 2
__device__ __forceinline__ int vbid() {
  const int b = blockIdx.x, x = b & 7, t = b >> 3;
  if (XMAP == 0) {
    if (b < 64) return (b & 32) + 4 * x + (t & 3);
    if (b < 96) return (b & ~15) + 2 * x + (t & 1);
    if (b >= 104 && b < 168) return 104 + 8 * x + t - 13;
    if (b >= 168 && b < 232) return 168 + ((b - 168) & 32) + 4 * x + ((t - 21) & 3);
    return b;
  }
  if (XMAP == 2) {
    if (x < 4) return t < 16 ? 4 * (t & 8) + 8 * x + (t & 7) : 104 + 16 * x + t - 16;
    const int y = x - 4;
    if (t >= 24) return (y == 0 ? 96 : 224 + 8 * y) + t - 24;
    if (t < 16) return 168 + 4 * (t & 8) + 8 * y + (t & 7);
    return 64 + 4 * (t & 4) + 4 * y + (t & 3);
  }
  if (x < 2) return 2 * (t & 16) + 16 * x + (t & 15);
  const int i = 16 * (x - 2) + (t & 15);
  if (t < 16) {
    const int k = (i * 171) >> 9, r = i - 3 * k;
    return r == 0 ? 168 + k : r == 1 ? 200 + k : 64 + 16 * (k & 1) + (k >> 1);
  }
  const int g = i >> 3, e = i & 7;
  if (g == 3) return 96 + e;
  if (g == 7) return 232 + e;
  if (g == 9) return 240 + e;
  if (g == 11) return 248 + e;
  return 104 + 8 * (g - (g > 3) - (g > 7) - (g > 9)) + e;
}
#define MSTAMP()
#define WSTAMP2()
#define SST(k)
#define TS(k, ss, t)
#define LS(k, ss)
#define LS_DUMP()
struct MegaArgs {
  const u16* mem0;
  const u16* Dskip;
  char* ws;
  u16* out;
  const u16* x0;
  const u16 *logAre, *Aim, *Cre, *Cim, *logdt;
  const u16 *Wq, *Wk, *Wv, *Wo;
  int vb;
};

__device__ __forceinline__ int lane_id() {
  int l;
  asm volatile("v_mbcnt_lo_u32_b32 %0, -1, 0\n\tv_mbcnt_hi_u32_b32 %0, -1, %0" : "=v"(l));
  return l;
}
__device__ __forceinline__ bool spin_timeout(unsigned spins, unsigned* tmo) {
  if (spins > SPIN_CAP) {
    if (lane_id() == 0) __hip_atomic_store((gu32*)tmo, 1u, RLX_AGENT);
    return true;
  }
  return false;
}
#define ROT_H (4 * (h & 1))
#define PNL 8
#define AWDIR 1
#define AWAT 0
#define AWRP 0
#define PEPS 4
#define FGATE 0
#define DL_C 0
#define DL_A 0
#define DL_Q 0
#define KC0 0
#define CPRIO 1
#define P1PRIO 3
#define QLEAD 2
#define POLL_SLEEP 12
#define SLP_C POLL_SLEEP
#define SLP_A POLL_SLEEP
#define SLP_W POLL_SLEEP
#define SLP_R POLL_SLEEP
#define SLP_V POLL_SLEEP
#define SLP_Q POLL_SLEEP
#define SLP_M 4
#define AWPRIO 2
#define SLP_S 8
#define SPOS 4
#define CVPRE 1
#define QPD 3
#define QPRI 2
#define TLBW 1
#define QHV 0
#define QCHK 0
#define LEADN 99
#define GATE_V 170
#define GATE_R 380
#define GATE_N 380
#define AWAHEAD 1
#define GATE_M 150
#define PRI_A 3
#define PRI_WA 2
#define PRI_WB 1
#define AWSKIP 3
#define PRI_0 3
#define AWDEF 0
#define SLP_F 2
#define EXP_OFF 0
#define AWSPEC 1
#define CVTHR 48
#define NR_C 8
#define NR_R 8
#define NR_W 8
#define NR_N 2
#define QAHEAD 0
#define NS0 4
#define SLP_N POLL_SLEEP
template <int SLP, int WPL>
__device__ __forceinline__ bool wait_n(const unsigned* f, int n, unsigned* tmo) {
  const int l = lane_id();
  for (unsigned spins = 0;; ++spins) {
    unsigned v = 1u;
    if (l < n) v = __hip_atomic_load((gu32*)f + (l / WPL) * 32 + l % WPL, RLX_AGENT);
    if (__all(v != 0u)) break;
    if (spin_timeout(spins, tmo)) return false;
    __builtin_amdgcn_s_sleep(SLP);
  }
  asm volatile("" ::: "memory");
  return true;
}
__device__ __forceinline__ unsigned now10() { return (unsigned)__builtin_amdgcn_s_memrealtime(); }
__device__ __forceinline__ void gate(const unsigned t) {
  for (int i = 0; i < 64; ++i) {
    const int d = (int)(t - now10());
    if (d <= 0) break;
    if (d > 100) __builtin_amdgcn_s_sleep(32);
    else if (d > 25) __builtin_amdgcn_s_sleep(8);
    else __builtin_amdgcn_s_sleep(2);
  }
}
template <int SLP, int WPL, int GT>
__device__ __forceinline__ void grp_wait(const bool leader, volatile unsigned* sw, const unsigned val, const unsigned* f, int n,
                                         unsigned* tmo, unsigned& tl) {
  if (leader) {
    const int l = lane_id();
    for (unsigned spins = 0;; ++spins) {
      unsigned v = 1u;
      if (l < n) v = __hip_atomic_load((gu32*)f + (l / WPL) * 32 + l % WPL, RLX_AGENT);
      if (__all(v != 0u) || spin_timeout(spins, tmo)) break;
      if (GT && spins == 0) gate(tl);
      else __builtin_amdgcn_s_sleep(SLP);
    }
    if (GT) tl = now10() + GT;
    if ((threadIdx.x & 63) == 0) *sw = val;
  } else {
    for (unsigned spins = 0; *sw < val; ++spins) {
      if (spins > 64u * SPIN_CAP) break;
      if (FGATE && GT && spins == 0) gate(tl);
      __builtin_amdgcn_s_sleep(SLP_F);
    }
    if (FGATE && GT) tl = now10() + GT;
  }
  asm volatile("" ::: "memory");
}
template <int SLP, int WPL>
__device__ __forceinline__ bool wg_wait(const unsigned* f, int n, unsigned* tmo) {
  if (threadIdx.x < 64) (void)wait_n<SLP, WPL>(f, n, tmo);
  __syncthreads();
  asm volatile("" ::: "memory");
  return true;
}
__device__ __forceinline__ void set_flag(unsigned* f) { __hip_atomic_store((gu32*)f, 1u, RLX_AGENT); }
#define DRAIN() asm volatile("s_waitcnt vmcnt(0)" ::: "memory")
#define MKRS(ptr, bytes) __builtin_amdgcn_make_buffer_rsrc((void*)(ptr), 0, (int)(bytes), 0x00020000)
typedef unsigned v2u __attribute__((ext_vector_type(2)));
#define LD128(rs, voff, soff) __builtin_amdgcn_raw_buffer_load_b128(rs, voff, soff, AUX_SC1)
#define LD64(rs, voff, soff) __builtin_amdgcn_raw_buffer_load_b64(rs, voff, soff, AUX_SC1)
#define ST128(v, rs, voff, soff)                                              \
  do {                                                                        \
    const v4u st_d_ = (v);                                                    \
    __builtin_amdgcn_raw_buffer_store_b128(st_d_, rs, voff, soff, AUX_SC1);   \
    asm volatile("s_nop 1" ::"v"(st_d_) : "memory");                          \
  } while (0)
#define ST64(v, rs, voff, soff) __builtin_amdgcn_raw_buffer_store_b64(v, rs, voff, soff, AUX_SC1)
#define BF8(x) __builtin_bit_cast(bf16x8, x)
#define MFMA16(a, b, c) __builtin_amdgcn_mfma_f32_16x16x32_bf16(a, b, c, 0, 0, 0)
typedef short bf16x4 __attribute__((ext_vector_type(4)));
#define MFMA16K(a, b, c) \
  __builtin_amdgcn_mfma_f32_16x16x16bf16_1k(__builtin_bit_cast(bf16x4, a), __builtin_bit_cast(bf16x4, b), c, 0, 0, 0)
__device__ __forceinline__ float fmax_nn(float a, float b) { return __builtin_elementwise_maximum(a, b); }
typedef __bf16 bfv8 __attribute__((ext_vector_type(8)));
typedef __bf16 bfv16 __attribute__((ext_vector_type(16)));
typedef unsigned v8u __attribute__((ext_vector_type(8)));
#define SMF(a, b, c, idx) \
  __builtin_amdgcn_smfmac_f32_16x16x64_bf16(__builtin_bit_cast(bfv8, a), __builtin_bit_cast(bfv16, b), c, idx, 0, 0)
__device__ __forceinline__ float swap_add32(float a, float b) {
  auto r = __builtin_amdgcn_permlane32_swap(__float_as_uint(a), __float_as_uint(b), false, false);
  return __uint_as_float(r[0]) + __uint_as_float(r[1]);
}
__device__ __forceinline__ float swap_add16(float a, float b) {
  auto r = __builtin_amdgcn_permlane16_swap(__float_as_uint(a), __float_as_uint(b), false, false);
  return __uint_as_float(r[0]) + __uint_as_float(r[1]);
}
__device__ __forceinline__ v4u smf_fix(const v4u d) {
  auto x = __builtin_amdgcn_permlane16_swap(d.x, d.z, false, false);
  auto y = __builtin_amdgcn_permlane16_swap(d.y, d.w, false, false);
  return v4u{x[0], x[1], y[0], y[1]};
}
__device__ __forceinline__ v8u smf_bfrag(const u16* p) {
  const v2u a = *reinterpret_cast<const v2u*>(p), b = *reinterpret_cast<const v2u*>(p + 8);
  const v2u c = *reinterpret_cast<const v2u*>(p + 32), d = *reinterpret_cast<const v2u*>(p + 40);
  return v8u{a.x, b.x, a.y, b.y, c.x, d.x, c.y, d.y};
}

#define VAR_PPA 0
#define VAR_PPB 0
#define VAR_THR 0
#define VAR_THR2 0
#define CV_LEAD 100
#define VAR_EARLY 1
#define VAR_DRAIN 1
#define VAR_D1 0
#define VAR_D2 0
#define VAR_DR2 1
#define VAR_S1 0
#define VAR_S2 0
typedef __attribute__((address_space(1))) unsigned long long gu64;
typedef __attribute__((address_space(1))) char gch;
__device__ __forceinline__ int cvt_rpi(float x) {
  int r;
  asm("v_cvt_rpi_i32_f32 %0, %1" : "=v"(r) : "v"(x));
  return r;
}
typedef unsigned long long u64;
__device__ __forceinline__ u64 call_tag(const MegaArgs& A) {
  return *(const __attribute__((address_space(4))) u64*)(A.ws + OFF_EP) * 6364136223846793005ull + 1442695040888963407ull;
}
__device__ __forceinline__ void tag_early(const MegaArgs& A) { asm volatile("" ::"s"(call_tag(A)) : "memory"); }
__device__ __forceinline__ u64 ready_look(const MegaArgs& A) {
  return __hip_atomic_load((gu64*)(A.ws + OFF_RD) + (blockIdx.x & 7) * 64 + lane_id(), RLX_AGENT);
}
__device__ __forceinline__ void ready_wait(const MegaArgs& A, u64 v) {
  const u64 tag = call_tag(A);
  for (unsigned spins = 0; !__all(v == tag) && spins < SPIN_CAP; ++spins) {
    __builtin_amdgcn_s_sleep(8);
    v = ready_look(A);
  }
  asm volatile("" ::: "memory");
}
typedef short v4s __attribute__((ext_vector_type(4)));
typedef __attribute__((address_space(3))) v4s* lp4s;
__device__ __forceinline__ void glds16(const void* sbase, const unsigned voff, const unsigned lds) {
  unsigned keep;
  asm volatile("s_nop 4\n\ts_mov_b32 %0, m0\n\ts_mov_b32 m0, %3\n\ts_nop 0\n\tglobal_load_lds_dwordx4 %1, %2\n\ts_mov_b32 m0, %0"
               : "=&s"(keep)
               : "v"(voff), "s"(sbase), "s"(lds)
               : "memory");
}
__device__ __forceinline__ void glds16c(const void* sbase, const unsigned voff, const unsigned lds) {
  unsigned keep;
  asm volatile("s_nop 4\n\ts_mov_b32 %0, m0\n\ts_mov_b32 m0, %3\n\ts_nop 0\n\tglobal_load_lds_dwordx4 %1, %2 sc1\n\ts_mov_b32 m0, %0"
               : "=&s"(keep)
               : "v"(voff), "s"(sbase), "s"(lds)
               : "memory");
}
__device__ __forceinline__ v2u trd(const unsigned a) {
  return __builtin_bit_cast(v2u, __builtin_amdgcn_ds_read_tr16_b64_v4i16((lp4s)(unsigned long)a));
}
__device__ __forceinline__ unsigned lds_base(char* smem) { return (unsigned)(unsigned long)(__attribute__((address_space(3))) char*)smem; }
#define RAW_BAR()                                           \
  do {                                                      \
    asm volatile("s_waitcnt lgkmcnt(0)" ::: "memory");      \
    __builtin_amdgcn_s_barrier();                           \
    asm volatile("" ::: "memory");                          \
  } while (0)
#define WAIT_VM(n) asm volatile("s_waitcnt vmcnt(" #n ")" ::: "memory")
template <bool SP>
__device__ __forceinline__ void wqkv_issue(const u16* Wm, const int hf, const int h, const int w, const int l, const unsigned sb) {
  const char* base = (const char*)Wm + (size_t)(256 * hf + 32 * w) * (HD * 2) + h * (DH * 2);
#pragma unroll
  for (int i = 0; i < 8; ++i) {
    const int r = l >> 4;
    const int f = SP ? ((r & 1) | ((i & 1) << 1) | (((i >> 1) & 1) << 2)) : (r | (((i >> 1) & 1) << 2));
    glds16(base, (unsigned)((4 * i + r) * (HD * 2) + (((l & 15) ^ (f << 1)) << 4)), sb + w * 16384 + hf * 8192 + i * 1024);
  }
}

__device__ __forceinline__ unsigned row_stats(const v4u raw) {
  float v[8];
#pragma unroll
  for (int e = 0; e < 4; ++e) { v[2 * e] = lo16(raw[e]); v[2 * e + 1] = hi16(raw[e]); }
  float s = 0.f;
#pragma unroll
  for (int e = 0; e < 8; ++e) s += v[e];
  const float mean = wave_sum(s) * (1.0f / DM);
  float q = 0.f;
#pragma unroll
  for (int e = 0; e < 8; ++e) { const float t = v[e] - mean; q += t * t; }
  const float var = wave_sum(q) * (1.0f / DM);
  const float sd = bfr(sqrtf(bfr(bfr(var) + 1e-5f)));
  return (unsigned)f2bf(mean) | ((unsigned)f2bf(sd) << 16);
}
__device__ __forceinline__ void start_stats(const MegaArgs& A, const int idx) {
  const int w = __builtin_amdgcn_readfirstlane(threadIdx.x >> 6), l = threadIdx.x & 63;
  const int r0 = 16 * idx + 2 * w, sx = 8 * idx + w;
  v4u raw[8], ra, rb;
  if (idx < 8) {
    ra = *reinterpret_cast<const v4u*>(A.x0 + (size_t)r0 * DM + l * 8);
    rb = *reinterpret_cast<const v4u*>(A.x0 + (size_t)(r0 + 1) * DM + l * 8);
  }
#pragma unroll
  for (int i = 0; i < 8; ++i) raw[i] = *reinterpret_cast<const v4u*>(A.x0 + (size_t)(8 * sx + i) * DM + l * 8);
  tag_early(A);
  if (idx < 8) {
    const unsigned sa = row_stats(ra), sb = row_stats(rb);
    if (l < 2) __hip_atomic_store((gu32*)(A.ws + OFF_GT) + r0 + l, l ? sb : sa, RLX_AGENT);
    DRAIN();
    if (l < 8) __hip_atomic_store((gu64*)(A.ws + OFF_F0) + (l * 8 + idx) * 16 + w, call_tag(A), RLX_AGENT);
    SST(11);
  }
  unsigned stw = 0u;
#pragma unroll
  for (int i = 0; i < 8; ++i) {
    const unsigned t = row_stats(raw[i]);
    if (l == i) stw = t;
  }
  if (l < 8) __hip_atomic_store((gu32*)(A.ws + OFF_GT) + 8 * sx + l, stw, RLX_AGENT);
  SST(0);
}
__device__ __forceinline__ void stats_flag(const MegaArgs& A, const int idx) {
  const int l = threadIdx.x & 63;
  if (l < 8) set_flag((unsigned*)(A.ws + OFF_FL) + FL_FS + (l * 32 + idx) * 32 + (threadIdx.x >> 6));
}
__device__ __forceinline__ void start_kgen(const MegaArgs& A, char* smem, const int kc, const int kg, const int zi) {
  const int tid = threadIdx.x;
  float* prm = reinterpret_cast<float*>(smem);
  float* part = prm + 16 * 6 * NST;
  float* kd = part + 16 * 8 * 132;
  {
    const int j = tid >> 5, u = 4 * (tid & 31), d = 16 * kg + j;
    const u16 p0 = A.logdt[d];
    const v2u p1 = *reinterpret_cast<const v2u*>(A.logAre + d * NST + u), p2 = *reinterpret_cast<const v2u*>(A.Aim + d * NST + u);
    const v2u p3 = *reinterpret_cast<const v2u*>(A.Cre + d * NST + u), p4 = *reinterpret_cast<const v2u*>(A.Cim + d * NST + u);
    const __amdgpu_buffer_rsrc_t rz = MKRS(A.ws + OFF_Z, zi >= 0 ? Z_BYTES : 0);
#pragma unroll
    for (int i = 0; i < 5; ++i) ST128((v4u{0u, 0u, 0u, 0u}), rz, tid * 16, (i * 64 + (zi & 63)) * 8192);
    tag_early(A);
    const float dt = bfr(expf(bf2f(p0))), st = (float)(2 << kc);
    f32x4 o[6];
#pragma unroll
    for (int e = 0; e < 4; ++e) {
      const unsigned q1 = p1[e >> 1], q2 = p2[e >> 1], q3 = p3[e >> 1], q4 = p4[e >> 1];
      const float ar = -bfr(expf((e & 1) ? hi16(q1) : lo16(q1))) * dt, ai = ((e & 1) ? hi16(q2) : lo16(q2)) * dt;
      const float ex = expf(ar * st);
      float sn, cs;
      sincosf(ai * st, &sn, &cs);
      o[0][e] = ar;
      o[1][e] = ai;
      o[2][e] = ((e & 1) ? hi16(q3) : lo16(q3)) * dt;
      o[3][e] = ((e & 1) ? hi16(q4) : lo16(q4)) * dt;
      o[4][e] = ex * cs;
      o[5][e] = ex * sn;
    }
#pragma unroll
    for (int i = 0; i < 6; ++i) *reinterpret_cast<f32x4*>(prm + (j * 6 + i) * NST + u) = o[i];
  }
  asm volatile("s_waitcnt lgkmcnt(0)" ::: "memory");
  if (zi >= 0) {
    DRAIN();
    __syncthreads();
    if (tid < 8) __hip_atomic_store((gu64*)(A.ws + OFF_RD) + tid * 64 + zi, call_tag(A), RLX_AGENT);
  }
  SST(1);
  unsigned tw = 0u;
  if (TLBW && tid < 384 && (blockIdx.x >> 6) == 1) {
    const unsigned o = (unsigned)OFF_XA + (unsigned)(tid * 8 + ((blockIdx.x >> 3) & 7)) * 65536u;
    if (o < WS_TOTAL) asm volatile("global_load_dword %0, %1, %2" : "=v"(tw) : "v"(o), "s"(A.ws) : "memory");
  }
  const int j = tid >> 5, m = tid & 31;
  {
    const int r = m & 3, ng = m >> 2;
    const float* pp = prm + j * 6 * NST;
    const float p0 = (float)((256 + 64 * r) << kc);
    f32x2_t acc[33];
#pragma unroll
    for (int i = 0; i < 33; ++i) acc[i] = f32x2_t{0.f, 0.f};
#pragma unroll 1
    for (int bt = 0; bt < 4; ++bt) {
      const int n = 4 * ng + 32 * bt;
      const f32x4 ar = *reinterpret_cast<const f32x4*>(pp + n), ai = *reinterpret_cast<const f32x4*>(pp + NST + n);
      const f32x4 cr = *reinterpret_cast<const f32x4*>(pp + 2 * NST + n), ci = *reinterpret_cast<const f32x4*>(pp + 3 * NST + n);
      const f32x4 wr = *reinterpret_cast<const f32x4*>(pp + 4 * NST + n), wi = *reinterpret_cast<const f32x4*>(pp + 5 * NST + n);
      float yr0[4], yi0[4];
#pragma unroll
      for (int k = 0; k < 4; ++k) {
        const float e = __expf(ar[k] * p0);
        float sn, cs;
        __sincosf(ai[k] * p0, &sn, &cs);
        const float zr = e * cs, zi = e * sn;
        yr0[k] = cr[k] * zr - ci[k] * zi;
        yi0[k] = cr[k] * zi + ci[k] * zr;
      }
      f32x2_t yra = {yr0[0], yr0[1]}, yia = {yi0[0], yi0[1]}, wra = {wr[0], wr[1]}, wia = {wi[0], wi[1]};
      f32x2_t yrb = {yr0[2], yr0[3]}, yib = {yi0[2], yi0[3]}, wrb = {wr[2], wr[3]}, wib = {wi[2], wi[3]};
#pragma unroll
      for (int i = 0; i < 33; ++i) {
        acc[i] += yra + yrb;
        const f32x2_t ta = yra * wra - yia * wia, tb = yrb * wrb - yib * wib;
        yia = yra * wia + yia * wra;
        yib = yrb * wib + yib * wrb;
        yra = ta;
        yrb = tb;
      }
    }
#pragma unroll
    for (int i = 0; i < 33; ++i) part[(j * 8 + ng) * 132 + 33 * r + i] = acc[i].x + acc[i].y;
  }
  asm volatile("s_waitcnt lgkmcnt(0)" ::: "memory");
  SST(2);
#pragma unroll
  for (int r = 0; r < 5; ++r) {
    const int o = r < 4 ? 33 * r + m : 131;
    float v = 0.f;
#pragma unroll
    for (int g = 0; g < 8; ++g) v += part[(j * 8 + g) * 132 + o];
    v *= 2.0f;
    if (r < 4 || m == 0) reinterpret_cast<unsigned*>(kd)[j * 132 + 32 * r + m] = pk2(v, v - lo16(pk2(v, v)));
  }
  asm volatile("s_waitcnt lgkmcnt(0)" ::: "memory");
  const __amdgpu_buffer_rsrc_t rs = MKRS(A.ws + OFF_KF, DM * 2 * CKN * 2);
  const unsigned* kp = reinterpret_cast<const unsigned*>(kd) + j * 132 - 128;
  const int ue = kc == 0 ? 229 : 260 - (32 << kc);
  const u64 rv = ready_look(A);
  for (int un = (kc == 2 ? 0 : 260 - (64 << kc)) + m; un < ue; un += 32) {
    v4u oh, ol;
#pragma unroll
    for (int e = 0; e < 4; ++e) {
      unsigned kv[2];
#pragma unroll
      for (int k = 0; k < 2; ++k) {
        const int lag = 2080 - (8 * un + 2 * e + k);
        const int q = (lag + (1 << kc) - 1 + ((lag >> (kc + 1)) & 1)) >> (kc + 1);
        const bool in = lag >= 256 && lag < L_SEQ;
        kv[k] = kp[in ? q : 128];
        if (!in) kv[k] = 0u;
      }
      oh[e] = __builtin_amdgcn_perm(kv[1], kv[0], 0x05040100);
      ol[e] = __builtin_amdgcn_perm(kv[1], kv[0], 0x07060302);
    }
    ST128(oh, rs, ((16 * kg + j) * 2 * CKN + 8 * un) * 2, 0);
    ST128(ol, rs, ((16 * kg + j) * 2 * CKN + CKN + 8 * un) * 2, 0);
  }
  DRAIN();
  asm volatile("" ::"v"(tw));
  ready_wait(A, rv);
  if ((tid & 63) == 0) set_flag((unsigned*)(A.ws + OFF_FL) + FL_KG + ((2 * kg + (tid >> 8)) * 3 + kc) * 32 + ((tid >> 6) & 3));
  __syncthreads();
  SST(3);
}

constexpr int C_SM = 131072, C_SO = C_SM + 8192, C_KT = C_SO + 2048, C_X = C_KT + 16384, C_SQ = C_X + 1024;
constexpr int C_SK = C_SQ + (7 * QS + 128) * 2;
static_assert(C_SK + 8 * QS * 2 <= 163840, "lds");
#define TDSLOT 0
#define CHAIN_ASM_ATT \
  "s_mov_b32 s92, 1152\n" \
  "s_mov_b32 s93, 576\n" \
  "s_mov_b32 s94, 2272\n" \
  "s_mov_b32 s95, 272\n" \
  "s_mov_b32 s96, 288\n" \
  "s_mov_b32 s91, 0x40000001\n" \
  "s_mov_b32 s84, 0x4b400000\n" \
  "s_mov_b32 s85, 0x4b400000\n" \
  "s_mov_b32 s78, 0x3e0293ee\n" \
  "s_mov_b32 s76, 0xff00ff00\n" \
  "s_mov_b32 s77, 0xff00ff00\n" \
  "s_add_u32 s58, s56, s60\n" \
  "s_addc_u32 s59, s57, 0\n" \
  "s_lshl_b32 s98, s73, 10\n" \
  "s_add_u32 s98, s98, s74\n" \
  "s_add_u32 s98, s98, 131072\n" \
  "s_lshl_b32 s97, s73, 5\n" \
  "s_add_u32 s97, s97, s74\n" \
  "s_add_u32 s97, s97, 158720\n" \
  "s_lshl_b32 s99, s73, 10\n" \
  "v_mbcnt_lo_u32_b32 v48, -1, 0\n" \
  "v_mbcnt_hi_u32_b32 v48, -1, v48\n" \
  "v_and_b32 v49, 15, v48\n" \
  "v_lshl_add_u32 v8, v49, 1, s97\n" \
  "v_bfe_u32 v49, v48, 4, 1\n" \
  "v_mad_u32_u24 v8, v49, s92, v8\n" \
  "v_lshrrev_b32 v49, 5, v48\n" \
  "v_mad_u32_u24 v9, v49, s93, v8\n" \
  "v_mad_u32_u24 v8, v49, s94, v8\n" \
  "v_add_u32 v10, s99, v1\n" \
  "s_lshr_b32 s88, s99, 2\n" \
  "s_add_u32 s88, s88, s74\n" \
  "s_add_u32 s88, s88, 139264\n" \
  "v_lshl_add_u32 v7, v49, 10, s88\n" \
  "v_and_b32 v49, 31, v48\n" \
  "v_lshl_add_u32 v7, v49, 3, v7\n" \
  "v_mov_b64 v[12:13], 0\n" \
  "v_mov_b64 v[14:15], 0\n" \
  "v_mov_b64 v[16:17], 0\n" \
  "v_mov_b64 v[18:19], 0\n" \
  "v_mov_b64 v[20:21], 0\n" \
  "v_mov_b64 v[22:23], 0\n" \
  "s_add_u32 s88, s74, 157760\n" \
  "v_mov_b32 v59, s88\n" \
  "v_mov_b32 v24, 0x44870000\n" \
  "v_mul_f32 v24, s72, v24\n" \
  "s_nop 0\n" \
  "v_readfirstlane_b32 s86, v24\n" \
  "s_lshr_b32 s86, s86, 23\n" \
  "s_addk_i32 s86, 0xff83\n" \
  "s_ashr_i32 s86, s86, 1\n" \
  "s_max_i32 s86, s86, -20\n" \
  "s_min_i32 s86, s86, 100\n" \
  "s_mov_b32 s82, s86\n" \
  "s_lshr_b32 s83, s72, 23\n" \
  "s_sub_i32 s83, s83, 242\n" \
  "s_lshl_b32 s88, s73, 10\n" \
  "v_add_u32 v24, s88, v1\n" \
  "ds_read_b128 v[32:35], v24 offset:0\n" \
  "s_waitcnt lgkmcnt(0)\n" \
  "v_mov_b32 v11, 0\n" \
  "v_dot2c_f32_bf16 v11, v32, v32\n" \
  "v_dot2c_f32_bf16 v11, v33, v33\n" \
  "v_dot2c_f32_bf16 v11, v34, v34\n" \
  "v_dot2c_f32_bf16 v11, v35, v35\n" \
  "s_nop 1\n" \
  "s_nop 0\n" \
  "v_cmpx_lt_f32 vcc, 2.0, v11\n" \
  "ds_max_u32 v59, v11\n" \
  "s_mov_b64 exec, -1\n" \
  ".LCA_TOP_%=:\n" \
  "s_waitcnt lgkmcnt(0)\n" \
  "s_barrier\n" \
  "ds_read_b128 v[40:43], v1 offset:0\n" \
  "ds_read_b128 v[44:47], v1 offset:1024\n" \
  "ds_read_b128 v[48:51], v1 offset:2048\n" \
  "ds_read_b128 v[52:55], v1 offset:3072\n" \
  "ds_read_b128 v[56:59], v1 offset:4096\n" \
  "ds_read_b128 v[60:63], v1 offset:5120\n" \
  "s_cmp_lg_u32 s73, 0\n" \
  "s_cbranch_scc1 .LCA_NOZ_%=\n" \
  "s_xor_b32 s88, s60, 0x8000\n" \
  "s_add_u32 s88, s88, s61\n" \
  "buffer_store_dwordx2 v[12:13], v5, s[40:43], s88 offen sc1\n" \
  ".LCA_NOZ_%=:\n" \
  "s_waitcnt lgkmcnt(5)\n" \
  "v_smfmac_f32_16x16x64_bf16 v[20:23], v[40:43], v[192:199], v0\n" \
  "v_smfmac_f32_16x16x64_bf16 v[12:15], v[40:43], v[64:71], v0\n" \
  "v_smfmac_f32_16x16x64_bf16 v[16:19], v[40:43], v[128:135], v0\n" \
  "ds_read_b128 v[40:43], v1 offset:6144\n" \
  "s_waitcnt lgkmcnt(5)\n" \
  "v_smfmac_f32_16x16x64_bf16 v[20:23], v[44:47], v[200:207], v0\n" \
  "v_smfmac_f32_16x16x64_bf16 v[12:15], v[44:47], v[72:79], v0\n" \
  "v_smfmac_f32_16x16x64_bf16 v[16:19], v[44:47], v[136:143], v0\n" \
  "ds_read_b128 v[44:47], v1 offset:7168\n" \
  "s_waitcnt lgkmcnt(5)\n" \
  "v_smfmac_f32_16x16x64_bf16 v[20:23], v[48:51], v[208:215], v0\n" \
  "v_smfmac_f32_16x16x64_bf16 v[12:15], v[48:51], v[80:87], v0\n" \
  "v_smfmac_f32_16x16x64_bf16 v[16:19], v[48:51], v[144:151], v0\n" \
  "ds_read_b128 v[24:27], v1 offset:10240\n" \
  "ds_read_b128 v[28:31], v1 offset:14336\n" \
  "s_waitcnt lgkmcnt(6)\n" \
  "v_smfmac_f32_16x16x64_bf16 v[20:23], v[52:55], v[216:223], v0\n" \
  "v_smfmac_f32_16x16x64_bf16 v[12:15], v[52:55], v[88:95], v0\n" \
  "v_smfmac_f32_16x16x64_bf16 v[16:19], v[52:55], v[152:159], v0\n" \
  "ds_read_b128 v[32:35], v1 offset:11264\n" \
  "ds_read_b128 v[36:39], v1 offset:15360\n" \
  "s_waitcnt lgkmcnt(7)\n" \
  "v_smfmac_f32_16x16x64_bf16 v[20:23], v[56:59], v[224:231], v0\n" \
  "v_smfmac_f32_16x16x64_bf16 v[12:15], v[56:59], v[96:103], v0\n" \
  "v_smfmac_f32_16x16x64_bf16 v[16:19], v[56:59], v[160:167], v0\n" \
  "s_waitcnt lgkmcnt(6)\n" \
  "v_smfmac_f32_16x16x64_bf16 v[20:23], v[60:63], v[232:239], v0\n" \
  "v_smfmac_f32_16x16x64_bf16 v[12:15], v[60:63], v[104:111], v0\n" \
  "v_smfmac_f32_16x16x64_bf16 v[16:19], v[60:63], v[168:175], v0\n" \
  "s_waitcnt lgkmcnt(5)\n" \
  "v_smfmac_f32_16x16x64_bf16 v[20:23], v[40:43], v[240:247], v0\n" \
  "v_smfmac_f32_16x16x64_bf16 v[12:15], v[40:43], v[112:119], v0\n" \
  "v_smfmac_f32_16x16x64_bf16 v[16:19], v[40:43], v[176:183], v0\n" \
  "s_waitcnt lgkmcnt(4)\n" \
  "v_smfmac_f32_16x16x64_bf16 v[20:23], v[44:47], v[248:255], v0\n" \
  "v_smfmac_f32_16x16x64_bf16 v[12:15], v[44:47], v[120:127], v0\n" \
  "v_smfmac_f32_16x16x64_bf16 v[16:19], v[44:47], v[184:191], v0\n" \
  "s_cmp_eq_u32 s71, 0\n" \
  "s_nop 4\n" \
  "v_permlane32_swap_b32 v20, v22\n" \
  "v_permlane32_swap_b32 v21, v23\n" \
  "v_pk_add_f32 v[20:21], v[20:21], v[22:23]\n" \
  "v_permlane32_swap_b32 v12, v16\n" \
  "v_permlane32_swap_b32 v13, v17\n" \
  "v_cvt_pk_bf16_f32 v20, v20, v21\n" \
  "v_pk_add_f32 v[12:13], v[12:13], v[16:17]\n" \
  "v_permlane32_swap_b32 v14, v18\n" \
  "v_permlane32_swap_b32 v15, v19\n" \
  "ds_write_b16 v9, v20 offset:128\n" \
  "v_cvt_pk_bf16_f32 v12, v12, v13\n" \
  "v_pk_add_f32 v[14:15], v[14:15], v[18:19]\n" \
  "ds_write_b16_d16_hi v9, v20 offset:416\n" \
  "ds_write_b16 v8, v12\n" \
  "v_cvt_pk_bf16_f32 v14, v14, v15\n" \
  "ds_write_b16_d16_hi v8, v12 offset:288\n" \
  "ds_write_b16 v8, v14 offset:576\n" \
  "ds_write_b16_d16_hi v8, v14 offset:864\n" \
  "s_setprio 3\n" \
  "s_waitcnt lgkmcnt(0)\n" \
  "s_barrier\n" \
  "s_cbranch_scc1 .LCA_LAST_%=\n" \
  "ds_read_b32 v11, v1 offset:26688\n" \
  "ds_read_b128 v[40:43], v2 offset:0\n" \
  "ds_read_b128 v[44:47], v3 offset:0\n" \
  "ds_read_b128 v[48:51], v2 offset:64\n" \
  "ds_read_b128 v[52:55], v3 offset:64\n" \
  "ds_read_b128 v[56:59], v2 offset:128\n" \
  "ds_read_b128 v[60:63], v3 offset:128\n" \
  "s_waitcnt lgkmcnt(5)\n" \
  "v_mfma_f32_16x16x32_bf16 v[12:15], v[24:27], v[40:43], 0\n" \
  "v_mfma_f32_16x16x32_bf16 v[16:19], v[28:31], v[40:43], 0\n" \
  "v_readfirstlane_b32 s86, v11\n" \
  "ds_read_b128 v[24:27], v1 offset:12288\n" \
  "ds_read_b128 v[28:31], v1 offset:16384\n" \
  "s_waitcnt lgkmcnt(6)\n" \
  "v_mfma_f32_16x16x32_bf16 v[20:23], v[44:47], v[40:43], 0\n" \
  "s_lshr_b32 s86, s86, 23\n" \
  "s_add_i32 s86, s86, s83\n" \
  "ds_read_b128 v[40:43], v2 offset:192\n" \
  "ds_read_b128 v[44:47], v3 offset:192\n" \
  "s_waitcnt lgkmcnt(7)\n" \
  "v_mfma_f32_16x16x32_bf16 v[12:15], v[32:35], v[48:51], v[12:15]\n" \
  "v_mfma_f32_16x16x32_bf16 v[16:19], v[36:39], v[48:51], v[16:19]\n" \
  "s_ashr_i32 s86, s86, 1\n" \
  "s_max_i32 s86, s86, s82\n" \
  "ds_read_b128 v[32:35], v1 offset:13312\n" \
  "ds_read_b128 v[36:39], v1 offset:17408\n" \
  "s_waitcnt lgkmcnt(8)\n" \
  "v_mfma_f32_16x16x32_bf16 v[20:23], v[52:55], v[48:51], v[20:23]\n" \
  "s_min_i32 s86, s86, 100\n" \
  "s_lshl_b32 s86, s86, 23\n" \
  "s_sub_i32 s84, 0x4a800000, s86\n" \
  "ds_read_b128 v[48:51], v10 offset:18432\n" \
  "ds_read_b128 v[52:55], v10 offset:22528\n" \
  "s_waitcnt lgkmcnt(8)\n" \
  "v_mfma_f32_16x16x32_bf16 v[20:23], v[60:63], v[56:59], v[20:23]\n" \
  "s_waitcnt lgkmcnt(6)\n" \
  "v_mfma_f32_16x16x32_bf16 v[12:15], v[24:27], v[56:59], v[12:15]\n" \
  "v_mfma_f32_16x16x32_bf16 v[16:19], v[28:31], v[56:59], v[16:19]\n" \
  "ds_read_b128 v[56:59], v6 offset:0\n" \
  "ds_read_b128 v[60:63], v6 offset:1024\n" \
  "s_waitcnt lgkmcnt(6)\n" \
  "v_mfma_f32_16x16x32_bf16 v[20:23], v[44:47], v[40:43], v[20:23]\n" \
  "ds_read_b128 v[24:27], v6 offset:2048\n" \
  "ds_read_b128 v[28:31], v6 offset:3072\n" \
  "s_waitcnt lgkmcnt(6)\n" \
  "v_mfma_f32_16x16x32_bf16 v[12:15], v[32:35], v[40:43], v[12:15]\n" \
  "v_mfma_f32_16x16x32_bf16 v[16:19], v[36:39], v[40:43], v[16:19]\n" \
  "ds_read_b128 v[32:35], v6 offset:8192\n" \
  "ds_read_b128 v[36:39], v6 offset:9216\n" \
  "s_waitcnt lgkmcnt(6)\n" \
  "v_cndmask_b32 v20, v20, v21, s[76:77]\n" \
  "v_mov_b32 v41, 0\n" \
  "v_mov_b32 v42, 0\n" \
  "s_nop 1\n" \
  "v_cndmask_b32 v12, v12, v16, s[76:77]\n" \
  "v_cndmask_b32 v13, v13, v17, s[76:77]\n" \
  "v_cndmask_b32 v14, v14, v18, s[76:77]\n" \
  "v_cndmask_b32 v15, v15, v19, s[76:77]\n" \
  "v_maximum3_f32 v16, v20, v12, v13\n" \
  "v_maximum3_f32 v16, v16, v14, v15\n" \
  "s_nop 1\n" \
  "v_max_f32_dpp v16, v16, v16 row_ror:8 row_mask:0xf bank_mask:0xf\n" \
  "v_mov_b32 v17, v16\n" \
  "s_nop 1\n" \
  "v_permlane16_swap_b32 v16, v17\n" \
  "v_max_f32 v16, v16, v17\n" \
  "v_mov_b32 v17, v16\n" \
  "s_nop 1\n" \
  "v_permlane32_swap_b32 v16, v17\n" \
  "v_max_f32 v16, v16, v17\n" \
  "v_mul_f32 v16, s78, v16\n" \
  "v_pk_fma_f32 v[12:13], v[12:13], s[78:79], v[16:17] op_sel_hi:[1,0,0] neg_lo:[0,0,1] neg_hi:[0,0,1]\n" \
  "v_pk_fma_f32 v[14:15], v[14:15], s[78:79], v[16:17] op_sel_hi:[1,0,0] neg_lo:[0,0,1] neg_hi:[0,0,1]\n" \
  "v_fma_f32 v20, v20, s78, -v16\n" \
  "v_exp_f32 v12, v12\n" \
  "v_exp_f32 v13, v13\n" \
  "v_exp_f32 v14, v14\n" \
  "v_exp_f32 v15, v15\n" \
  "v_exp_f32 v20, v20\n" \
  "s_barrier\n" \
  "ds_read_b64 v[22:23], v7 offset:0\n" \
  "v_cvt_pk_bf16_f32 v44, v12, v13\n" \
  "v_cvt_pk_bf16_f32 v45, v14, v15\n" \
  "v_pk_add_f32 v[8:9], v[12:13], v[14:15]\n" \
  "v_mov_b32_dpp v46, v44 row_ror:8 row_mask:0xf bank_mask:0xf\n" \
  "v_mov_b32_dpp v17, v20 row_ror:8 row_mask:0xf bank_mask:0xf\n" \
  "v_mov_b32_dpp v47, v45 row_ror:8 row_mask:0xf bank_mask:0xf\n" \
  "v_add_f32 v10, v8, v20\n" \
  "v_cvt_pk_bf16_f32 v40, v20, v17\n" \
  "v_add_f32 v10, v10, v9\n" \
  "v_mfma_f32_16x16x32_bf16 v[12:15], v[48:51], v[44:47], 0\n" \
  "v_cvt_pk_bf16_f32 v43, v20, v17\n" \
  "v_add_f32_dpp v10, v10, v10 row_ror:8 row_mask:0xf bank_mask:0xf\n" \
  "v_mfma_f32_16x16x32_bf16 v[16:19], v[52:55], v[44:47], 0\n" \
  "v_mov_b32 v11, v10\n" \
  "s_waitcnt lgkmcnt(0)\n" \
  "v_mfma_f32_16x16x16_bf16 v[12:15], v[22:23], v[40:41], v[12:15]\n" \
  "v_mfma_f32_16x16x16_bf16 v[16:19], v[22:23], v[42:43], v[16:19]\n" \
  "v_permlane16_swap_b32 v10, v11\n" \
  "v_add_f32 v10, v10, v11\n" \
  "ds_read_b128 v[48:51], v6 offset:10240\n" \
  "v_mov_b32 v11, v10\n" \
  "ds_read_b128 v[52:55], v6 offset:11264\n" \
  "v_mov_b64 v[20:21], 0\n" \
  "v_mov_b64 v[22:23], 0\n" \
  "v_permlane32_swap_b32 v10, v11\n" \
  "v_add_f32 v10, v10, v11\n" \
  "v_rcp_f32 v10, v10\n" \
  "s_nop 0\n" \
  "v_pk_mul_f32 v[12:13], v[12:13], v[10:11] op_sel_hi:[1,0]\n" \
  "v_pk_mul_f32 v[14:15], v[14:15], v[10:11] op_sel_hi:[1,0]\n" \
  "v_pk_mul_f32 v[16:17], v[16:17], v[10:11] op_sel_hi:[1,0]\n" \
  "v_pk_mul_f32 v[18:19], v[18:19], v[10:11] op_sel_hi:[1,0]\n" \
  "v_cvt_pk_bf16_f32 v40, v12, v13\n" \
  "v_cvt_pk_bf16_f32 v41, v14, v15\n" \
  "v_cvt_pk_bf16_f32 v42, v16, v17\n" \
  "ds_write_b64 v4, v[40:41]\n" \
  "v_cvt_pk_bf16_f32 v43, v18, v19\n" \
  "ds_write_b64 v4, v[42:43] offset:1024\n" \
  "v_mov_b64 v[12:13], 0\n" \
  "v_mov_b64 v[14:15], 0\n" \
  "v_mov_b64 v[16:17], 0\n" \
  "v_mov_b64 v[18:19], 0\n" \
  "v_mov_b64 v[8:9], 0\n" \
  "v_mov_b64 v[10:11], 0\n" \
  "s_setprio 0\n" \
  "s_waitcnt lgkmcnt(0)\n" \
  "s_barrier\n" \
  "ds_read_b128 v[40:43], v1 offset:8192\n" \
  "ds_read_b128 v[44:47], v1 offset:9216\n" \
  "s_waitcnt lgkmcnt(1)\n" \
  "v_smfmac_f32_16x16x64_bf16 v[12:15], v[40:43], v[56:63], v0\n" \
  "ds_read_b128 v[56:59], v6 offset:4096\n" \
  "ds_read_b128 v[60:63], v6 offset:5120\n" \
  "v_smfmac_f32_16x16x64_bf16 v[16:19], v[40:43], v[24:31], v0\n" \
  "ds_read_b128 v[24:27], v6 offset:6144\n" \
  "ds_read_b128 v[28:31], v6 offset:7168\n" \
  "s_waitcnt lgkmcnt(4)\n" \
  "v_smfmac_f32_16x16x64_bf16 v[12:15], v[44:47], v[32:39], v0\n" \
  "ds_read_b128 v[32:35], v6 offset:12288\n" \
  "ds_read_b128 v[36:39], v6 offset:13312\n" \
  "v_smfmac_f32_16x16x64_bf16 v[16:19], v[44:47], v[48:55], v0\n" \
  "ds_read_b128 v[48:51], v6 offset:14336\n" \
  "ds_read_b128 v[52:55], v6 offset:15360\n" \
  "s_nop 0\n" \
  "s_waitcnt lgkmcnt(6)\n" \
  "v_smfmac_f32_16x16x64_bf16 v[20:23], v[40:43], v[56:63], v0\n" \
  "s_nop 0\n" \
  "s_waitcnt lgkmcnt(4)\n" \
  "v_smfmac_f32_16x16x64_bf16 v[8:11], v[40:43], v[24:31], v0\n" \
  "v_permlane32_swap_b32 v12, v17\n" \
  "v_permlane32_swap_b32 v13, v16\n" \
  "v_permlane32_swap_b32 v14, v19\n" \
  "v_permlane32_swap_b32 v15, v18\n" \
  "s_waitcnt lgkmcnt(2)\n" \
  "v_smfmac_f32_16x16x64_bf16 v[20:23], v[44:47], v[32:39], v0\n" \
  "v_pk_add_f32 v[12:13], v[12:13], v[16:17] op_sel:[0,1] op_sel_hi:[1,0]\n" \
  "v_pk_add_f32 v[14:15], v[14:15], v[18:19] op_sel:[0,1] op_sel_hi:[1,0]\n" \
  "v_pk_fma_f32 v[12:13], v[12:13], s[84:85], s[84:85] op_sel:[0,0,1] op_sel_hi:[1,0,1]\n" \
  "v_pk_fma_f32 v[14:15], v[14:15], s[84:85], s[84:85] op_sel:[0,0,1] op_sel_hi:[1,0,1]\n" \
  "s_waitcnt lgkmcnt(0)\n" \
  "v_smfmac_f32_16x16x64_bf16 v[8:11], v[44:47], v[48:55], v0\n" \
  "v_lshl_add_u32 v12, v12, 6, s91\n" \
  "v_lshl_add_u32 v13, v13, 2, s75\n" \
  "global_atomic_add_x2 v5, v[12:13], s[58:59] offset:0\n" \
  "v_lshl_add_u32 v14, v14, 6, s91\n" \
  "v_lshl_add_u32 v15, v15, 2, s75\n" \
  "global_atomic_add_x2 v5, v[14:15], s[58:59] offset:2048\n" \
  "s_nop 1\n" \
  "v_permlane32_swap_b32 v20, v9\n" \
  "v_permlane32_swap_b32 v21, v8\n" \
  "v_permlane32_swap_b32 v22, v11\n" \
  "v_permlane32_swap_b32 v23, v10\n" \
  "v_pk_add_f32 v[20:21], v[20:21], v[8:9] op_sel:[0,1] op_sel_hi:[1,0]\n" \
  "v_pk_add_f32 v[22:23], v[22:23], v[10:11] op_sel:[0,1] op_sel_hi:[1,0]\n" \
  "v_pk_fma_f32 v[20:21], v[20:21], s[84:85], s[84:85] op_sel:[0,0,1] op_sel_hi:[1,0,1]\n" \
  "v_pk_fma_f32 v[22:23], v[22:23], s[84:85], s[84:85] op_sel:[0,0,1] op_sel_hi:[1,0,1]\n" \
  "v_lshl_add_u32 v20, v20, 6, s91\n" \
  "v_lshl_add_u32 v21, v21, 2, s75\n" \
  "global_atomic_add_x2 v5, v[20:21], s[58:59] offset:3072\n" \
  "v_lshl_add_u32 v22, v22, 6, s91\n" \
  "v_lshl_add_u32 v23, v23, 2, s75\n" \
  "global_atomic_add_x2 v5, v[22:23], s[58:59] offset:1024\n" \
  "buffer_load_dword v52, v5, s[0:3], s66 offen sc1\n" \
  "v_mbcnt_lo_u32_b32 v48, -1, 0\n" \
  "v_mbcnt_hi_u32_b32 v48, -1, v48\n" \
  "v_and_b32 v49, 15, v48\n" \
  "v_lshl_add_u32 v8, v49, 1, s97\n" \
  "v_bfe_u32 v49, v48, 4, 1\n" \
  "v_mad_u32_u24 v8, v49, s92, v8\n" \
  "v_lshrrev_b32 v49, 5, v48\n" \
  "v_mad_u32_u24 v9, v49, s93, v8\n" \
  "v_mad_u32_u24 v8, v49, s94, v8\n" \
  "v_add_u32 v10, s99, v1\n" \
  "s_lshr_b32 s88, s99, 2\n" \
  "s_add_u32 s88, s88, s74\n" \
  "s_add_u32 s88, s88, 139264\n" \
  "v_lshl_add_u32 v7, v49, 10, s88\n" \
  "v_and_b32 v49, 31, v48\n" \
  "v_lshl_add_u32 v7, v49, 3, v7\n" \
  "v_mov_b64 v[12:13], 0\n" \
  "v_mov_b64 v[14:15], 0\n" \
  "v_mov_b64 v[16:17], 0\n" \
  "v_mov_b64 v[18:19], 0\n" \
  "v_mov_b64 v[20:21], 0\n" \
  "v_mov_b64 v[22:23], 0\n" \
  "s_add_u32 s88, s74, 157760\n" \
  "v_mov_b32 v59, s88\n" \
  "v_lshrrev_b32 v49, 3, v48\n" \
  "v_and_b32 v50, 7, v48\n" \
  "v_lshlrev_b32 v50, 4, v50\n" \
  "v_lshl_or_b32 v44, v49, 8, v50\n" \
  "v_and_b32 v50, 3, v48\n" \
  "v_lshl_add_u32 v40, v50, 2, s98\n" \
  "v_lshrrev_b32 v50, 4, v48\n" \
  "v_mad_u32_u24 v40, v50, s95, v40\n" \
  "v_and_b32 v49, 1, v49\n" \
  "v_lshl_add_u32 v40, v49, 6, v40\n" \
  "v_bfe_u32 v49, v48, 2, 1\n" \
  "v_lshl_add_u32 v40, v49, 7, v40\n" \
  "v_xor_b32 v41, 16, v40\n" \
  "v_xor_b32 v42, 32, v40\n" \
  "v_xor_b32 v43, 48, v40\n" \
  "s_add_i32 s80, s86, 0x32800000\n" \
  "s_add_i32 s81, s86, 0x33800000\n" \
  "s_add_i32 s88, s86, 0xb4000000\n" \
  "v_mov_b32 v46, s88\n" \
  "v_mov_b32 v47, 0\n" \
  "v_mov_b32 v11, 0\n" \
  "s_mov_b32 s89, s60\n" \
  "s_xor_b32 s60, s60, 0x80\n" \
  "s_bitcmp0_b32 s60, 7\n" \
  "s_cselect_b32 s88, 0x8000, 0\n" \
  "s_add_u32 s60, s60, s88\n" \
  "s_and_b32 s60, s60, 0xffff\n" \
  "s_add_u32 s58, s56, s60\n" \
  "s_addc_u32 s59, s57, 0\n" \
  "s_sub_u32 s71, s71, 1\n" \
  "s_mov_b32 s87, 0x40000\n" \
  "s_waitcnt vmcnt(0)\n" \
  "s_memrealtime s[38:39]\n" \
  ".LCA_CHK_%=:\n" \
  "v_cmp_eq_u32 vcc, 0, v52\n" \
  "v_readlane_b32 s88, v52, s79\n" \
  "s_cmp_eq_u64 vcc, 0\n" \
  "s_cbranch_scc1 .LCA_OKM_%=\n" \
  "s_sleep 4\n" \
  "buffer_load_dword v52, v5, s[0:3], s66 offen sc1\n" \
  "s_sub_u32 s87, s87, 1\n" \
  "s_cbranch_scc1 .LCA_END_%=\n" \
  "s_waitcnt vmcnt(0)\n" \
  "s_branch .LCA_CHK_%=\n" \
  ".LCA_OKM_%=:\n" \
  "v_subrev_u32 v52, s88, v52\n" \
  "s_nop 1\n" \
  "v_max_i32_dpp v52, v52, v52 row_ror:1 row_mask:0xf bank_mask:0xf\n" \
  "s_nop 1\n" \
  "v_max_i32_dpp v52, v52, v52 row_ror:2 row_mask:0xf bank_mask:0xf\n" \
  "s_nop 1\n" \
  "v_max_i32_dpp v52, v52, v52 row_ror:4 row_mask:0xf bank_mask:0xf\n" \
  "s_add_u32 s66, s66, 1024\n" \
  "v_readlane_b32 vcc_lo, v52, 0\n" \
  "v_readlane_b32 s88, v52, 16\n" \
  "s_max_i32 vcc_lo, vcc_lo, s88\n" \
  "v_readlane_b32 s88, v52, 32\n" \
  "s_max_i32 vcc_lo, vcc_lo, s88\n" \
  "v_readlane_b32 s88, v52, 48\n" \
  "s_max_i32 vcc_lo, vcc_lo, s88\n" \
  "s_waitcnt lgkmcnt(0)\n" \
  "s_add_u32 vcc_lo, vcc_lo, s38\n" \
  "s_add_u32 vcc_lo, vcc_lo, -6\n" \
  ".LCA_TW_%=:\n" \
  "s_memrealtime s[38:39]\n" \
  "s_waitcnt lgkmcnt(0)\n" \
  "s_sub_u32 s38, s38, vcc_lo\n" \
  "s_cmp_lt_i32 s38, 0\n" \
  "s_cbranch_scc1 .LCA_TW_%=\n" \
  ".LCA_POLL_%=:\n" \
  "buffer_load_dwordx4 v[24:27], v44, s[40:43], s89 offen sc1\n" \
  "buffer_load_dwordx4 v[28:31], v44, s[40:43], s89 offen offset:2048 sc1\n" \
  "s_sub_u32 s87, s87, 1\n" \
  "s_cbranch_scc1 .LCA_END_%=\n" \
  "s_waitcnt vmcnt(1)\n" \
  "v_bitop3_b32 v45, v24, v26, 32 bitop3:0x80\n" \
  "v_alignbit_b32 v32, v25, v24, 2\n" \
  "v_alignbit_b32 v34, v27, v26, 2\n" \
  "s_waitcnt vmcnt(0)\n" \
  "v_bitop3_b32 v45, v45, v28, v30 bitop3:0x80\n" \
  "v_cmp_eq_u32 vcc, 0, v45\n" \
  "v_cvt_f32_i32 v32, v32\n" \
  "v_cvt_f32_i32 v33, v25\n" \
  "s_cmp_eq_u64 vcc, 0\n" \
  "s_cbranch_scc0 .LCA_POLL_%=\n" \
  "v_cvt_f32_i32 v34, v34\n" \
  "v_cvt_f32_i32 v35, v27\n" \
  "v_pk_fma_f32 v[32:33], v[32:33], s[80:81], v[46:47]\n" \
  "v_alignbit_b32 v36, v29, v28, 2\n" \
  "v_pk_fma_f32 v[34:35], v[34:35], s[80:81], v[46:47]\n" \
  "v_alignbit_b32 v38, v31, v30, 2\n" \
  "v_cvt_pk_bf16_f32 v32, v32, v34\n" \
  "v_cvt_pk_bf16_f32 v33, v33, v35\n" \
  "v_cvt_f32_i32 v36, v36\n" \
  "ds_write_b32 v40, v32\n" \
  "v_cvt_f32_i32 v37, v29\n" \
  "ds_write_b32 v41, v33\n" \
  "v_cvt_f32_i32 v38, v38\n" \
  "v_cvt_f32_i32 v39, v31\n" \
  "v_pk_fma_f32 v[36:37], v[36:37], s[80:81], v[46:47]\n" \
  "v_pk_fma_f32 v[38:39], v[38:39], s[80:81], v[46:47]\n" \
  "v_dot2c_f32_bf16 v11, v32, v32\n" \
  "v_cvt_pk_bf16_f32 v36, v36, v38\n" \
  "v_cvt_pk_bf16_f32 v37, v37, v39\n" \
  "ds_write_b32 v42, v36\n" \
  "ds_write_b32 v43, v37\n" \
  "v_dot2c_f32_bf16 v11, v33, v33\n" \
  "v_dot2c_f32_bf16 v11, v36, v36\n" \
  "v_dot2c_f32_bf16 v11, v37, v37\n" \
  "s_nop 1\n" \
  "s_nop 0\n" \
  "v_cmpx_lt_f32 vcc, 2.0, v11\n" \
  "ds_max_u32 v59, v11\n" \
  "s_mov_b64 exec, -1\n" \
  "s_branch .LCA_TOP_%=\n" \
  ".LCA_LAST_%=:\n" \
  ".LCA_END_%=:\n" \
  "s_endpgm\n"
#define CHAIN_ASM_VO \
  "s_mov_b32 s92, 1152\n" \
  "s_mov_b32 s93, 576\n" \
  "s_mov_b32 s94, 2272\n" \
  "s_mov_b32 s95, 272\n" \
  "s_mov_b32 s96, 288\n" \
  "s_mov_b32 s91, 0x40000001\n" \
  "s_mov_b32 s84, 0x4b400000\n" \
  "s_mov_b32 s85, 0x4b400000\n" \
  "s_mov_b32 s78, 0x3e0293ee\n" \
  "s_mov_b32 s76, 0xff00ff00\n" \
  "s_mov_b32 s77, 0xff00ff00\n" \
  "s_add_u32 s58, s56, s60\n" \
  "s_addc_u32 s59, s57, 0\n" \
  "s_lshl_b32 s98, s73, 10\n" \
  "s_add_u32 s98, s98, s74\n" \
  "s_add_u32 s98, s98, 131072\n" \
  "s_lshl_b32 s97, s73, 5\n" \
  "s_add_u32 s97, s97, s74\n" \
  "s_add_u32 s97, s97, 160992\n" \
  "s_lshl_b32 s99, s73, 11\n" \
  "s_add_u32 s99, s99, s74\n" \
  "s_add_u32 s99, s99, 133120\n" \
  "s_lshl_b32 s94, s73, 2\n" \
  "s_add_u32 s94, s94, s74\n" \
  "s_add_u32 s94, s94, 157828\n" \
  "v_mbcnt_lo_u32_b32 v48, -1, 0\n" \
  "v_mbcnt_hi_u32_b32 v48, -1, v48\n" \
  "s_sub_u32 s88, s73, 4\n" \
  "v_and_b32 v51, 15, v48\n" \
  "v_lshl_add_u32 v4, v51, 1, s97\n" \
  "v_bfe_u32 v49, v48, 4, 1\n" \
  "v_mad_u32_u24 v4, v49, s92, v4\n" \
  "v_lshrrev_b32 v50, 5, v48\n" \
  "v_mad_u32_u24 v4, v50, s93, v4\n" \
  "v_lshlrev_b32 v8, 4, v49\n" \
  "v_lshl_add_u32 v8, v51, 5, v8\n" \
  "v_lshl_add_u32 v8, v50, 3, v8\n" \
  "s_lshl_b32 s89, s88, 8\n" \
  "s_add_u32 s89, s89, s74\n" \
  "s_add_u32 s89, s89, 139264\n" \
  "v_and_b32 v49, 31, v48\n" \
  "v_lshl_add_u32 v5, v49, 3, s89\n" \
  "v_lshl_add_u32 v5, v50, 10, v5\n" \
  "v_lshrrev_b32 v49, 2, v51\n" \
  "v_and_b32 v50, 5, v51\n" \
  "v_and_or_b32 v49, v49, 2, v50\n" \
  "s_lshl_b32 s89, s88, 6\n" \
  "s_add_u32 s89, s89, s74\n" \
  "s_add_u32 s89, s89, 160992\n" \
  "v_and_b32 v50, 48, v48\n" \
  "v_add_u32 v6, s89, v50\n" \
  "v_mad_u32_u24 v6, v49, s96, v6\n" \
  "v_lshlrev_b32 v7, 3, v50\n" \
  "v_lshl_add_u32 v7, v49, 4, v7\n" \
  "v_and_b32 v49, 2, v51\n" \
  "v_lshl_add_u32 v7, v49, 29, v7\n" \
  "v_lshlrev_b32 v10, 4, v48\n" \
  "v_mov_b64 v[12:13], 0\n" \
  "v_mov_b64 v[14:15], 0\n" \
  "v_mov_b64 v[16:17], 0\n" \
  "v_mov_b64 v[18:19], 0\n" \
  "v_mov_b64 v[20:21], 0\n" \
  "v_mov_b64 v[22:23], 0\n" \
  "s_add_u32 s88, s74, 157760\n" \
  "v_mov_b32 v59, s88\n" \
  "v_mov_b32 v24, 0x44870000\n" \
  "v_mul_f32 v24, s72, v24\n" \
  "s_nop 0\n" \
  "v_readfirstlane_b32 s86, v24\n" \
  "s_lshr_b32 s86, s86, 23\n" \
  "s_addk_i32 s86, 0xff83\n" \
  "s_ashr_i32 s86, s86, 1\n" \
  "s_max_i32 s86, s86, -20\n" \
  "s_min_i32 s86, s86, 100\n" \
  "s_mov_b32 s82, s86\n" \
  "s_lshr_b32 s83, s72, 23\n" \
  "s_sub_i32 s83, s83, 242\n" \
  "s_lshl_b32 s88, s73, 10\n" \
  "v_add_u32 v24, s88, v1\n" \
  "ds_read_b128 v[32:35], v24 offset:0\n" \
  "s_waitcnt lgkmcnt(0)\n" \
  "v_mov_b32 v11, 0\n" \
  "v_dot2c_f32_bf16 v11, v32, v32\n" \
  "v_dot2c_f32_bf16 v11, v33, v33\n" \
  "v_dot2c_f32_bf16 v11, v34, v34\n" \
  "v_dot2c_f32_bf16 v11, v35, v35\n" \
  "s_nop 1\n" \
  "s_nop 0\n" \
  "v_cmpx_lt_f32 vcc, 2.0, v11\n" \
  "ds_max_u32 v59, v11\n" \
  "s_mov_b64 exec, -1\n" \
  ".LCV_TOP_%=:\n" \
  "s_waitcnt lgkmcnt(0)\n" \
  "s_barrier\n" \
  "s_setprio 3\n" \
  "ds_read_b128 v[24:27], v1 offset:0\n" \
  "ds_read_b128 v[28:31], v1 offset:1024\n" \
  "ds_read_b128 v[32:35], v1 offset:2048\n" \
  "ds_read_b128 v[36:39], v1 offset:3072\n" \
  "ds_read_b128 v[40:43], v1 offset:4096\n" \
  "ds_read_b128 v[44:47], v1 offset:5120\n" \
  "ds_read_b128 v[48:51], v1 offset:6144\n" \
  "ds_read_b128 v[52:55], v1 offset:7168\n" \
  "buffer_load_dword v58, off, s[44:47], s68 sc1\n" \
  "s_waitcnt lgkmcnt(7)\n" \
  "v_smfmac_f32_16x16x64_bf16 v[12:15], v[24:27], v[64:71], v0\n" \
  "s_waitcnt lgkmcnt(6)\n" \
  "v_smfmac_f32_16x16x64_bf16 v[12:15], v[28:31], v[72:79], v0\n" \
  "s_waitcnt lgkmcnt(5)\n" \
  "v_smfmac_f32_16x16x64_bf16 v[12:15], v[32:35], v[80:87], v0\n" \
  "s_waitcnt lgkmcnt(4)\n" \
  "v_smfmac_f32_16x16x64_bf16 v[12:15], v[36:39], v[88:95], v0\n" \
  "s_waitcnt lgkmcnt(3)\n" \
  "v_smfmac_f32_16x16x64_bf16 v[12:15], v[40:43], v[96:103], v0\n" \
  "s_waitcnt lgkmcnt(2)\n" \
  "v_smfmac_f32_16x16x64_bf16 v[12:15], v[44:47], v[104:111], v0\n" \
  "s_waitcnt lgkmcnt(1)\n" \
  "v_smfmac_f32_16x16x64_bf16 v[12:15], v[48:51], v[112:119], v0\n" \
  "s_waitcnt lgkmcnt(0)\n" \
  "v_smfmac_f32_16x16x64_bf16 v[12:15], v[52:55], v[120:127], v0\n" \
  "s_memrealtime s[38:39]\n" \
  "ds_read_b32 v11, v1 offset:26688\n" \
  "s_nop 6\n" \
  "v_permlane32_swap_b32 v12, v14\n" \
  "v_permlane32_swap_b32 v13, v15\n" \
  "v_pk_add_f32 v[12:13], v[12:13], v[14:15]\n" \
  "s_nop 0\n" \
  "v_cvt_pk_bf16_f32 v12, v12, v13\n" \
  "ds_write_b16 v4, v12\n" \
  "ds_write_b16_d16_hi v4, v12 offset:288\n" \
  "s_setprio 0\n" \
  "v_mov_b64 v[12:13], 0\n" \
  "v_mov_b64 v[14:15], 0\n" \
  "s_waitcnt lgkmcnt(2)\n" \
  "v_readfirstlane_b32 s86, v11\n" \
  "s_lshr_b32 s86, s86, 23\n" \
  "s_add_i32 s86, s86, s83\n" \
  "s_ashr_i32 s86, s86, 1\n" \
  "s_max_i32 s86, s86, s82\n" \
  "s_min_i32 s86, s86, 100\n" \
  "s_lshl_b32 s86, s86, 23\n" \
  "s_sub_i32 s84, 0x4a800000, s86\n" \
  "s_cmp_eq_u32 s71, 0\n" \
  "s_waitcnt lgkmcnt(0)\n" \
  "s_barrier\n" \
  "ds_read_b128 v[60:63], v6 offset:0\n" \
  "v_smfmac_f32_16x16x64_bf16 v[16:19], v[24:27], v[128:135], v0\n" \
  "v_or_b32 v57, 1, s38\n" \
  "v_smfmac_f32_16x16x64_bf16 v[20:23], v[24:27], v[192:199], v0\n" \
  "v_add_u32 v56, s67, v10\n" \
  "v_smfmac_f32_16x16x64_bf16 v[16:19], v[28:31], v[136:143], v0\n" \
  "s_mov_b64 exec, 0x11\n" \
  "buffer_store_dword v57, v56, s[0:3], 0 offen sc1\n" \
  "s_mov_b64 exec, -1\n" \
  "v_smfmac_f32_16x16x64_bf16 v[20:23], v[28:31], v[200:207], v0\n" \
  "v_smfmac_f32_16x16x64_bf16 v[16:19], v[32:35], v[144:151], v0\n" \
  "s_waitcnt lgkmcnt(0)\n" \
  "v_max_u32 v60, 1, v60\n" \
  "v_smfmac_f32_16x16x64_bf16 v[20:23], v[32:35], v[208:215], v0\n" \
  "v_max_u32 v61, 1, v61\n" \
  "v_smfmac_f32_16x16x64_bf16 v[16:19], v[36:39], v[152:159], v0\n" \
  "v_max_u32 v62, 1, v62\n" \
  "v_smfmac_f32_16x16x64_bf16 v[20:23], v[36:39], v[216:223], v0\n" \
  "v_max_u32 v63, 1, v63\n" \
  "v_smfmac_f32_16x16x64_bf16 v[16:19], v[40:43], v[160:167], v0\n" \
  "buffer_store_dwordx4 v[60:63], v7, s[48:51], s69 offen sc1\n" \
  "v_smfmac_f32_16x16x64_bf16 v[20:23], v[40:43], v[224:231], v0\n" \
  "v_smfmac_f32_16x16x64_bf16 v[16:19], v[44:47], v[168:175], v0\n" \
  "v_smfmac_f32_16x16x64_bf16 v[20:23], v[44:47], v[232:239], v0\n" \
  "v_smfmac_f32_16x16x64_bf16 v[16:19], v[48:51], v[176:183], v0\n" \
  "v_smfmac_f32_16x16x64_bf16 v[20:23], v[48:51], v[240:247], v0\n" \
  "v_smfmac_f32_16x16x64_bf16 v[16:19], v[52:55], v[184:191], v0\n" \
  "v_smfmac_f32_16x16x64_bf16 v[20:23], v[52:55], v[248:255], v0\n" \
  "ds_read_b128 v[24:27], v3 offset:0\n" \
  "ds_read_b128 v[28:31], v3 offset:1024\n" \
  "ds_read_b128 v[32:35], v3 offset:2048\n" \
  "ds_read_b128 v[36:39], v3 offset:3072\n" \
  "ds_read_b128 v[40:43], v3 offset:8192\n" \
  "ds_read_b128 v[44:47], v3 offset:9216\n" \
  "s_nop 0\n" \
  "v_permlane32_swap_b32 v16, v18\n" \
  "v_permlane32_swap_b32 v17, v19\n" \
  "v_permlane32_swap_b32 v20, v22\n" \
  "v_permlane32_swap_b32 v21, v23\n" \
  "v_pk_add_f32 v[16:17], v[16:17], v[18:19]\n" \
  "v_pk_add_f32 v[20:21], v[20:21], v[22:23]\n" \
  "v_cvt_pk_bf16_f32 v16, v16, v17\n" \
  "v_cvt_pk_bf16_f32 v17, v20, v21\n" \
  "ds_write_b64 v5, v[16:17]\n" \
  "v_max_u32 v20, 1, v16\n" \
  "v_max_u32 v21, 1, v17\n" \
  "s_nop 1\n" \
  "v_permlane32_swap_b32 v20, v21\n" \
  "buffer_store_dwordx2 v[20:21], v8, s[52:55], s70 offen sc1\n" \
  "s_cbranch_scc1 .LCV_LAST_%=\n" \
  "s_waitcnt vmcnt(3)\n" \
  "v_cmp_eq_u32 vcc, 0, v58\n" \
  "s_cmp_eq_u64 vcc, 0\n" \
  "s_cselect_b32 s90, 1, 0\n" \
  "s_waitcnt lgkmcnt(0)\n" \
  "s_barrier\n" \
  "s_cbranch_scc0 .LCV_NOTOK_%=\n" \
  "s_mov_b32 m0, s99\n" \
  "s_add_u32 s88, s99, 8192\n" \
  "global_load_lds_dwordx4 v10, s[62:63] sc1\n" \
  "global_load_lds_dwordx4 v10, s[62:63] offset:1024 sc1\n" \
  "s_mov_b32 m0, s88\n" \
  "s_nop 0\n" \
  "global_load_lds_dwordx4 v10, s[64:65] sc1\n" \
  "global_load_lds_dwordx4 v10, s[64:65] offset:1024 sc1\n" \
  ".LCV_NOTOK_%=:\n" \
  "s_add_u32 s67, s67, 1024\n" \
  "ds_write_b32 v59, v12\n" \
  "ds_read_b128 v[48:51], v3 offset:10240\n" \
  "ds_read_b128 v[52:55], v3 offset:11264\n" \
  "v_mov_b64 v[18:19], 0\n" \
  "v_mov_b64 v[20:21], 0\n" \
  "v_mov_b64 v[22:23], 0\n" \
  "v_mov_b64 v[16:17], 0\n" \
  "ds_read_b128 v[56:59], v3 offset:4096\n" \
  "ds_read_b128 v[60:63], v3 offset:5120\n" \
  "s_cmp_eq_u32 s90, 1\n" \
  "s_cbranch_scc1 .LCV_NM0_%=\n" \
  "s_mov_b32 m0, s94\n" \
  "s_mov_b64 exec, 1\n" \
  "buffer_load_dword off, s[44:47], s68 sc1 lds\n" \
  "s_mov_b64 exec, -1\n" \
  ".LCV_NM0_%=:\n" \
  "s_waitcnt lgkmcnt(0)\n" \
  "s_barrier\n" \
  "ds_read_b128 v[4:7], v1 offset:8192\n" \
  "ds_read_b128 v[8:11], v1 offset:9216\n" \
  "s_waitcnt lgkmcnt(1)\n" \
  "v_smfmac_f32_16x16x64_bf16 v[12:15], v[4:7], v[24:31], v0\n" \
  "ds_read_b128 v[24:27], v3 offset:6144\n" \
  "ds_read_b128 v[28:31], v3 offset:7168\n" \
  "v_smfmac_f32_16x16x64_bf16 v[16:19], v[4:7], v[32:39], v0\n" \
  "ds_read_b128 v[32:35], v3 offset:12288\n" \
  "ds_read_b128 v[36:39], v3 offset:13312\n" \
  "s_waitcnt lgkmcnt(4)\n" \
  "v_smfmac_f32_16x16x64_bf16 v[12:15], v[8:11], v[40:47], v0\n" \
  "v_smfmac_f32_16x16x64_bf16 v[16:19], v[8:11], v[48:55], v0\n" \
  "v_smfmac_f32_16x16x64_bf16 v[20:23], v[4:7], v[56:63], v0\n" \
  "ds_read_b128 v[56:59], v3 offset:14336\n" \
  "ds_read_b128 v[60:63], v3 offset:15360\n" \
  "v_mov_b64 v[40:41], 0\n" \
  "v_mov_b64 v[42:43], 0\n" \
  "s_waitcnt lgkmcnt(2)\n" \
  "v_smfmac_f32_16x16x64_bf16 v[20:23], v[8:11], v[32:39], v0\n" \
  "s_nop 0\n" \
  "v_permlane32_swap_b32 v12, v17\n" \
  "v_permlane32_swap_b32 v13, v16\n" \
  "v_permlane32_swap_b32 v14, v19\n" \
  "v_permlane32_swap_b32 v15, v18\n" \
  "v_smfmac_f32_16x16x64_bf16 v[40:43], v[4:7], v[24:31], v0\n" \
  "v_pk_add_f32 v[12:13], v[12:13], v[16:17] op_sel:[0,1] op_sel_hi:[1,0]\n" \
  "v_pk_add_f32 v[14:15], v[14:15], v[18:19] op_sel:[0,1] op_sel_hi:[1,0]\n" \
  "v_pk_fma_f32 v[12:13], v[12:13], s[84:85], s[84:85] op_sel:[0,0,1] op_sel_hi:[1,0,1]\n" \
  "v_pk_fma_f32 v[14:15], v[14:15], s[84:85], s[84:85] op_sel:[0,0,1] op_sel_hi:[1,0,1]\n" \
  "s_waitcnt lgkmcnt(0)\n" \
  "v_smfmac_f32_16x16x64_bf16 v[40:43], v[8:11], v[56:63], v0\n" \
  "v_lshl_add_u32 v12, v12, 6, s91\n" \
  "v_lshl_add_u32 v13, v13, 2, s75\n" \
  "global_atomic_add_x2 v2, v[12:13], s[58:59] offset:0\n" \
  "v_lshl_add_u32 v14, v14, 6, s91\n" \
  "v_lshl_add_u32 v15, v15, 2, s75\n" \
  "global_atomic_add_x2 v2, v[14:15], s[58:59] offset:2048\n" \
  "s_nop 1\n" \
  "v_permlane32_swap_b32 v20, v41\n" \
  "v_permlane32_swap_b32 v21, v40\n" \
  "v_permlane32_swap_b32 v22, v43\n" \
  "v_permlane32_swap_b32 v23, v42\n" \
  "v_pk_add_f32 v[20:21], v[20:21], v[40:41] op_sel:[0,1] op_sel_hi:[1,0]\n" \
  "v_pk_add_f32 v[22:23], v[22:23], v[42:43] op_sel:[0,1] op_sel_hi:[1,0]\n" \
  "v_pk_fma_f32 v[20:21], v[20:21], s[84:85], s[84:85] op_sel:[0,0,1] op_sel_hi:[1,0,1]\n" \
  "v_pk_fma_f32 v[22:23], v[22:23], s[84:85], s[84:85] op_sel:[0,0,1] op_sel_hi:[1,0,1]\n" \
  "v_lshl_add_u32 v20, v20, 6, s91\n" \
  "v_lshl_add_u32 v21, v21, 2, s75\n" \
  "global_atomic_add_x2 v2, v[20:21], s[58:59] offset:3072\n" \
  "v_lshl_add_u32 v22, v22, 6, s91\n" \
  "v_lshl_add_u32 v23, v23, 2, s75\n" \
  "global_atomic_add_x2 v2, v[22:23], s[58:59] offset:1024\n" \
  "buffer_load_dword v52, v2, s[0:3], s66 offen sc1\n" \
  "v_mbcnt_lo_u32_b32 v48, -1, 0\n" \
  "v_mbcnt_hi_u32_b32 v48, -1, v48\n" \
  "s_sub_u32 s88, s73, 4\n" \
  "v_and_b32 v51, 15, v48\n" \
  "v_lshl_add_u32 v4, v51, 1, s97\n" \
  "v_bfe_u32 v49, v48, 4, 1\n" \
  "v_mad_u32_u24 v4, v49, s92, v4\n" \
  "v_lshrrev_b32 v50, 5, v48\n" \
  "v_mad_u32_u24 v4, v50, s93, v4\n" \
  "v_lshlrev_b32 v8, 4, v49\n" \
  "v_lshl_add_u32 v8, v51, 5, v8\n" \
  "v_lshl_add_u32 v8, v50, 3, v8\n" \
  "s_lshl_b32 s89, s88, 8\n" \
  "s_add_u32 s89, s89, s74\n" \
  "s_add_u32 s89, s89, 139264\n" \
  "v_and_b32 v49, 31, v48\n" \
  "v_lshl_add_u32 v5, v49, 3, s89\n" \
  "v_lshl_add_u32 v5, v50, 10, v5\n" \
  "v_lshrrev_b32 v49, 2, v51\n" \
  "v_and_b32 v50, 5, v51\n" \
  "v_and_or_b32 v49, v49, 2, v50\n" \
  "s_lshl_b32 s89, s88, 6\n" \
  "s_add_u32 s89, s89, s74\n" \
  "s_add_u32 s89, s89, 160992\n" \
  "v_and_b32 v50, 48, v48\n" \
  "v_add_u32 v6, s89, v50\n" \
  "v_mad_u32_u24 v6, v49, s96, v6\n" \
  "v_lshlrev_b32 v7, 3, v50\n" \
  "v_lshl_add_u32 v7, v49, 4, v7\n" \
  "v_and_b32 v49, 2, v51\n" \
  "v_lshl_add_u32 v7, v49, 29, v7\n" \
  "v_lshlrev_b32 v10, 4, v48\n" \
  "v_mov_b64 v[12:13], 0\n" \
  "v_mov_b64 v[14:15], 0\n" \
  "v_mov_b64 v[16:17], 0\n" \
  "v_mov_b64 v[18:19], 0\n" \
  "v_mov_b64 v[20:21], 0\n" \
  "v_mov_b64 v[22:23], 0\n" \
  "s_add_u32 s88, s74, 157760\n" \
  "v_mov_b32 v59, s88\n" \
  "s_cmp_eq_u32 s90, 1\n" \
  "s_cbranch_scc1 .LCV_NQF_%=\n" \
  "v_mov_b32 v58, s94\n" \
  "s_waitcnt vmcnt(5)\n" \
  "ds_read_b32 v58, v58\n" \
  "s_waitcnt lgkmcnt(0)\n" \
  "v_cmp_eq_u32 vcc, 0, v58\n" \
  "s_cmp_eq_u64 vcc, 0\n" \
  "s_cbranch_scc0 .LCV_NM1_%=\n" \
  "s_mov_b32 m0, s99\n" \
  "s_add_u32 s88, s99, 8192\n" \
  "global_load_lds_dwordx4 v10, s[62:63] sc1\n" \
  "global_load_lds_dwordx4 v10, s[62:63] offset:1024 sc1\n" \
  "s_mov_b32 m0, s88\n" \
  "s_nop 0\n" \
  "global_load_lds_dwordx4 v10, s[64:65] sc1\n" \
  "global_load_lds_dwordx4 v10, s[64:65] offset:1024 sc1\n" \
  "s_mov_b32 s90, 2\n" \
  "s_branch .LCV_NQF_%=\n" \
  ".LCV_NM1_%=:\n" \
  "buffer_load_dword v58, off, s[44:47], s68 sc1\n" \
  ".LCV_NQF_%=:\n" \
  "v_lshrrev_b32 v49, 3, v48\n" \
  "v_and_b32 v50, 7, v48\n" \
  "v_lshlrev_b32 v50, 4, v50\n" \
  "v_lshl_or_b32 v44, v49, 8, v50\n" \
  "v_and_b32 v50, 3, v48\n" \
  "v_lshl_add_u32 v40, v50, 2, s98\n" \
  "v_lshrrev_b32 v50, 4, v48\n" \
  "v_mad_u32_u24 v40, v50, s95, v40\n" \
  "v_and_b32 v49, 1, v49\n" \
  "v_lshl_add_u32 v40, v49, 6, v40\n" \
  "v_bfe_u32 v49, v48, 2, 1\n" \
  "v_lshl_add_u32 v40, v49, 7, v40\n" \
  "v_xor_b32 v41, 16, v40\n" \
  "v_xor_b32 v42, 32, v40\n" \
  "v_xor_b32 v43, 48, v40\n" \
  "s_add_i32 s80, s86, 0x32800000\n" \
  "s_add_i32 s81, s86, 0x33800000\n" \
  "s_add_i32 s88, s86, 0xb4000000\n" \
  "v_mov_b32 v46, s88\n" \
  "v_mov_b32 v47, 0\n" \
  "v_mov_b32 v11, 0\n" \
  "s_mov_b32 s89, s60\n" \
  "s_xor_b32 s60, s60, 0x80\n" \
  "s_bitcmp0_b32 s60, 7\n" \
  "s_cselect_b32 s88, 0x8000, 0\n" \
  "s_add_u32 s60, s60, s88\n" \
  "s_and_b32 s60, s60, 0xffff\n" \
  "s_add_u32 s58, s56, s60\n" \
  "s_addc_u32 s59, s57, 0\n" \
  "s_sub_u32 s71, s71, 1\n" \
  "s_mov_b32 s87, 0x40000\n" \
  "s_waitcnt vmcnt(4)\n" \
  "s_cmp_eq_u32 s90, 2\n" \
  "s_cbranch_scc1 .LCV_DR_%=\n" \
  "s_waitcnt vmcnt(0)\n" \
  ".LCV_DR_%=:\n" \
  "s_memrealtime s[38:39]\n" \
  "s_cmp_lg_u32 s90, 0\n" \
  "s_cbranch_scc1 .LCV_HAVE_%=\n" \
  ".LCV_LATE_%=:\n" \
  "v_cmp_eq_u32 vcc, 0, v58\n" \
  "s_cmp_eq_u64 vcc, 0\n" \
  "s_cbranch_scc1 .LCV_GOT_%=\n" \
  "s_sleep 12\n" \
  "buffer_load_dword v58, off, s[44:47], s68 sc1\n" \
  "s_sub_u32 s87, s87, 1\n" \
  "s_cbranch_scc1 .LCV_END_%=\n" \
  "s_waitcnt vmcnt(0)\n" \
  "s_branch .LCV_LATE_%=\n" \
  ".LCV_GOT_%=:\n" \
  "s_mov_b32 m0, s99\n" \
  "s_add_u32 s88, s99, 8192\n" \
  "global_load_lds_dwordx4 v10, s[62:63] sc1\n" \
  "global_load_lds_dwordx4 v10, s[62:63] offset:1024 sc1\n" \
  "s_mov_b32 m0, s88\n" \
  "s_nop 0\n" \
  "global_load_lds_dwordx4 v10, s[64:65] sc1\n" \
  "global_load_lds_dwordx4 v10, s[64:65] offset:1024 sc1\n" \
  ".LCV_HAVE_%=:\n" \
  "s_add_u32 s62, s62, 0x80000\n" \
  "s_addc_u32 s63, s63, 0\n" \
  "s_add_u32 s64, s64, 0x40000\n" \
  "s_addc_u32 s65, s65, 0\n" \
  "s_add_u32 s68, s68, 0x1000\n" \
  "s_add_u32 s69, s69, 0x10000\n" \
  "s_add_u32 s70, s70, 0x10000\n" \
  ".LCV_CHK_%=:\n" \
  "v_cmp_eq_u32 vcc, 0, v52\n" \
  "v_readlane_b32 s88, v52, s79\n" \
  "s_cmp_eq_u64 vcc, 0\n" \
  "s_cbranch_scc1 .LCV_OKM_%=\n" \
  "s_sleep 4\n" \
  "buffer_load_dword v52, v2, s[0:3], s66 offen sc1\n" \
  "s_sub_u32 s87, s87, 1\n" \
  "s_cbranch_scc1 .LCV_END_%=\n" \
  "s_waitcnt vmcnt(0)\n" \
  "s_branch .LCV_CHK_%=\n" \
  ".LCV_OKM_%=:\n" \
  "v_subrev_u32 v52, s88, v52\n" \
  "s_nop 1\n" \
  "v_max_i32_dpp v52, v52, v52 row_ror:1 row_mask:0xf bank_mask:0xf\n" \
  "s_nop 1\n" \
  "v_max_i32_dpp v52, v52, v52 row_ror:2 row_mask:0xf bank_mask:0xf\n" \
  "s_nop 1\n" \
  "v_max_i32_dpp v52, v52, v52 row_ror:4 row_mask:0xf bank_mask:0xf\n" \
  "s_add_u32 s66, s66, 1024\n" \
  "v_readlane_b32 vcc_lo, v52, 0\n" \
  "v_readlane_b32 s88, v52, 16\n" \
  "s_max_i32 vcc_lo, vcc_lo, s88\n" \
  "v_readlane_b32 s88, v52, 32\n" \
  "s_max_i32 vcc_lo, vcc_lo, s88\n" \
  "v_readlane_b32 s88, v52, 48\n" \
  "s_max_i32 vcc_lo, vcc_lo, s88\n" \
  "s_waitcnt lgkmcnt(0)\n" \
  "s_add_u32 vcc_lo, vcc_lo, s38\n" \
  "s_add_u32 vcc_lo, vcc_lo, -14\n" \
  ".LCV_TW_%=:\n" \
  "s_memrealtime s[38:39]\n" \
  "s_waitcnt lgkmcnt(0)\n" \
  "s_sub_u32 s38, s38, vcc_lo\n" \
  "s_cmp_lt_i32 s38, 0\n" \
  "s_cbranch_scc1 .LCV_TW_%=\n" \
  ".LCV_POLL_%=:\n" \
  "buffer_load_dwordx4 v[24:27], v44, s[40:43], s89 offen sc1\n" \
  "buffer_load_dwordx4 v[28:31], v44, s[40:43], s89 offen offset:2048 sc1\n" \
  "s_sub_u32 s87, s87, 1\n" \
  "s_cbranch_scc1 .LCV_END_%=\n" \
  "s_waitcnt vmcnt(1)\n" \
  "v_bitop3_b32 v45, v24, v26, 32 bitop3:0x80\n" \
  "v_alignbit_b32 v32, v25, v24, 2\n" \
  "v_alignbit_b32 v34, v27, v26, 2\n" \
  "s_waitcnt vmcnt(0)\n" \
  "v_bitop3_b32 v45, v45, v28, v30 bitop3:0x80\n" \
  "v_cmp_eq_u32 vcc, 0, v45\n" \
  "v_cvt_f32_i32 v32, v32\n" \
  "v_cvt_f32_i32 v33, v25\n" \
  "s_cmp_eq_u64 vcc, 0\n" \
  "s_cbranch_scc0 .LCV_POLL_%=\n" \
  "v_cvt_f32_i32 v34, v34\n" \
  "v_cvt_f32_i32 v35, v27\n" \
  "v_pk_fma_f32 v[32:33], v[32:33], s[80:81], v[46:47]\n" \
  "v_alignbit_b32 v36, v29, v28, 2\n" \
  "v_pk_fma_f32 v[34:35], v[34:35], s[80:81], v[46:47]\n" \
  "v_alignbit_b32 v38, v31, v30, 2\n" \
  "v_cvt_pk_bf16_f32 v32, v32, v34\n" \
  "v_cvt_pk_bf16_f32 v33, v33, v35\n" \
  "v_cvt_f32_i32 v36, v36\n" \
  "ds_write_b32 v40, v32\n" \
  "v_cvt_f32_i32 v37, v29\n" \
  "ds_write_b32 v41, v33\n" \
  "v_cvt_f32_i32 v38, v38\n" \
  "v_cvt_f32_i32 v39, v31\n" \
  "v_pk_fma_f32 v[36:37], v[36:37], s[80:81], v[46:47]\n" \
  "v_pk_fma_f32 v[38:39], v[38:39], s[80:81], v[46:47]\n" \
  "v_dot2c_f32_bf16 v11, v32, v32\n" \
  "v_cvt_pk_bf16_f32 v36, v36, v38\n" \
  "v_cvt_pk_bf16_f32 v37, v37, v39\n" \
  "ds_write_b32 v42, v36\n" \
  "ds_write_b32 v43, v37\n" \
  "v_dot2c_f32_bf16 v11, v33, v33\n" \
  "v_dot2c_f32_bf16 v11, v36, v36\n" \
  "v_dot2c_f32_bf16 v11, v37, v37\n" \
  "s_nop 1\n" \
  "s_nop 0\n" \
  "v_cmpx_lt_f32 vcc, 2.0, v11\n" \
  "ds_max_u32 v59, v11\n" \
  "s_mov_b64 exec, -1\n" \
  "s_branch .LCV_TOP_%=\n" \
  ".LCV_LAST_%=:\n" \
  "s_waitcnt vmcnt(0)\n" \
  ".LCV_END_%=:\n" \
  "s_endpgm\n"
__device__ __forceinline__ v4u mk_srd(const void* p, const unsigned bytes) {
  const unsigned long long a = (unsigned long long)p;
  return v4u{(unsigned)a, (unsigned)(a >> 32) & 0xffffu, bytes, 0x00020000u};
}
template <bool ATT>
__device__ __forceinline__ void role_chain_t(const MegaArgs& A, char* smem, const int L, const int h, const int rep) {
  u16* swo = reinterpret_cast<u16*>(smem);
  const int w = __builtin_amdgcn_readfirstlane(threadIdx.x >> 6);
  char* ws = A.ws;
  unsigned* FL = (unsigned*)(ws + OFF_FL);
  unsigned* TMO = FL + FL_TMO;
#define LANE_IDS()                         \
  const int l = lane_id();                 \
  const int c = l & 15, g = l >> 4;        \
  (void)c; (void)g;
  float* sgf = reinterpret_cast<float*>(smem + C_X);
  unsigned* FQ = FL + FL_FQ + L * 64 * 32 * 32;
  const int wb = (w + ROT_H) & 7;
  v8u w0[8], w1[8], w2[8];
  {
    LANE_IDS();
    const unsigned sb = lds_base(smem);
    const int q = (l >> 2) & 3, p = l & 3;
    const int f = (q & 1) | ((g & 1) << 1) | ((q >> 1) << 2);
    const unsigned ra = sb + (16 * (g >> 1) + 4 * (g & 1) + (q & 1) + 8 * (q >> 1)) * 256 + (w ^ f) * 32 + p * 8;
#define W_READ(W, hf, X)                                                       \
  {                                                                           \
    _Pragma("unroll") for (int t = 0; t < 4; ++t) {                           \
      const unsigned a_ = (ra ^ (X)) + (hf) * 8192 + t * 32768;               \
      const v2u r0 = trd(a_), r1 = trd(a_ + 512), r2 = trd(a_ + 16384), r3 = trd(a_ + 16384 + 512); \
      W[4 * (hf) + t] = v8u{r0.x, r0.y, r1.x, r1.y, r2.x, r2.y, r3.x, r3.y};  \
    }                                                                         \
    _Pragma("unroll") for (int t = 0; t < 4; ++t) asm volatile("" : "+v"(W[4 * (hf) + t])); \
  }
#define W_STAGE(M, hf, VM, NEXT)                                               \
  {                                                                           \
    WAIT_VM(VM);                                                              \
    RAW_BAR();                                                                \
    if ((M) == 0 && ATT) { W_READ(w0, hf, 0); W_READ(w2, hf, 128); }          \
    if ((M) == 1 && ATT) { W_READ(w1, hf, 0); }                               \
    if ((M) == 1 && !ATT) { W_READ(w0, hf, 0); }                              \
    if ((M) == 2 && !ATT) { W_READ(w1, hf, 128); W_READ(w2, hf, 0); }         \
    RAW_BAR();                                                                \
    NEXT;                                                                     \
  }
#define WO_ISSUE(hf)                                                          \
  {                                                                           \
    const char* base_ = (const char*)A.Wo + (size_t)(h * DH + 64 * (hf) + 8 * w) * (DM * 2); \
    _Pragma("unroll") for (int i = 0; i < 8; ++i) {                           \
      const int fd = (i & 1) | (((i >> 2) & 1) << 1) | ((w & 1) << 2);        \
      glds16(base_, (unsigned)(i * 1024 + ((l ^ (fd << 1)) << 4)), sb + w * 16384 + (hf) * 8192 + i * 1024); \
    }                                                                         \
  }
    wqkv_issue<true>(A.Wv, 0, h, w, l, sb);
    wqkv_issue<true>(A.Wv, 1, h, w, l, sb);
    tag_early(A);
    W_STAGE(2, 0, 8, wqkv_issue<true>(A.Wk, 0, h, w, l, sb));
    SST(4);
    W_STAGE(2, 1, 8, wqkv_issue<true>(A.Wk, 1, h, w, l, sb));
    SST(5);
    if (!ATT) {
      float f2 = 0.f;
#pragma unroll
      for (int t = 0; t < 8; ++t)
#pragma unroll
        for (int e = 0; e < 8; ++e) f2 = dot2sq(w2[t][e], dot2sq(w1[t][e], f2));
      f2 = wave_sum_dpp(f2);
      if (l == 0) { sgf[w] = f2; sgf[16] = 0.f; }
    }
    const __amdgpu_buffer_rsrc_t rg = MKRS(ws + OFF_G, (w == 0 && L == 0) ? 8192 : 0);
    W_STAGE(1, 0, 8,
            __builtin_amdgcn_raw_buffer_store_b32(__float_as_uint((sgf[4] + sgf[5]) + (sgf[6] + sgf[7])), rg, l * 1024 + h * 16 + 8, 0, AUX_SC1);
            wqkv_issue<true>(A.Wq, 0, h, w, l, sb));
    W_STAGE(1, 1, 8, wqkv_issue<true>(A.Wq, 1, h, w, l, sb); ST64(__builtin_bit_cast(v2u, call_tag(A)), rg, l * 1024 + h * 16, 0));
    SST(6);
    W_STAGE(0, 0, 9, WO_ISSUE(0));
    W_STAGE(0, 1, 8, WO_ISSUE(1));
    SST(7);
#undef W_STAGE
#undef W_READ
#undef WO_ISSUE
    const unsigned rb = sb + (2 * (g >> 1) + (q >> 1)) * 16384 + (4 * (g & 1) + (q & 1)) * 1024 + p * 8;
#pragma unroll
    for (int hf = 0; hf < 2; ++hf) {
      if (hf == 0) WAIT_VM(8); else WAIT_VM(0);
      RAW_BAR();
      v4u fr[8];
#pragma unroll
      for (int i = 0; i < 8; ++i) {
        const unsigned a_ = rb + hf * 8192 + (i & 1) * 65536 + ((4 * wb + (i >> 1)) ^ f) * 32;
        const v2u r0 = trd(a_), r1 = trd(a_ + 2048);
        fr[i] = v4u{r0.x, r0.y, r1.x, r1.y};
      }
#pragma unroll
      for (int i = 0; i < 8; ++i) asm volatile("" : "+v"(fr[i]));
      RAW_BAR();
#pragma unroll
      for (int i = 0; i < 8; ++i) *reinterpret_cast<v4u*>(swo + w * 8192 + hf * 4096 + i * 512 + l * 8) = fr[i];
    }
  }
  WSTAMP2();
  v4u m0r;
  {
    LANE_IDS();
    m0r = *reinterpret_cast<const v4u*>(A.mem0 + (c & 7) * DM + 64 * w + 16 * g + 8 * (c >> 3));
  }
  const unsigned sb = lds_base(smem);
  const char* gK = ws + OFF_QK + L * SZ_QK + ((size_t)h * 16 + 2 * w) * 1024;
  const char* gV = ws + OFF_VF + L * SZ_O + ((size_t)h * 8 + 2 * w - 8) * 1024;
  float gw2;
  {
    const __amdgpu_buffer_rsrc_t rg = MKRS(ws + OFF_G, 8192);
    const v2u tag = __builtin_bit_cast(v2u, call_tag(A));
    v4u gv;
    for (unsigned spins = 0;; ++spins) {
      gv = LD128(rg, lane_id() * 16, (blockIdx.x & 7) * 1024);
      if (__all(gv.x == tag.x && gv.y == tag.y)) break;
      if (spins > SPIN_CAP) return;
      __builtin_amdgcn_s_sleep(4);
      asm volatile("" ::: "memory");
    }
    const auto gx = __builtin_amdgcn_permlane32_swap(gv.z, gv.z, false, false);
    const float gm = wave_max_dpp(__uint_as_float(gx[0]) * __uint_as_float(gx[1]));
    asm volatile("s_nop 0\n\tv_readfirstlane_b32 %0, %1" : "=s"(gw2) : "v"(gm * 1.063f));
  }
  v8u pv;
  {
    LANE_IDS();
    *reinterpret_cast<v4u*>(smem + C_SM + w * 1024 + l * 16) = m0r;
    const unsigned voff = g * 256 + c * 8, wp = sb + w * 16384 + l * 16;
    pv[0] = (c & 8) ? 0xEEEE : 0x4444;
    pv[1] = sb + C_SM + l * 16;
    pv[2] = ATT ? sb + C_SQ + ((c & 7) * QS + 8 * g) * 2 : voff;
    pv[3] = ATT ? sb + C_SK + ((4 * ((c >> 2) & 1) + 2 * (c >> 3) + (c & 1)) * QS + 8 * g) * 2 : wp;
    pv[4] = sb + (c < 8 ? C_SO : C_SQ) + w * 256 + ((c & 7) + 8 * (g >> 1)) * 16 + 8 * (g & 1);
    pv[5] = voff;
    pv[6] = wp;
    pv[7] = sb + C_SO + (l >> 5) * 1024 + w * 256 + (l & 31) * 8;
  }
  if (!ATT) {
    if (!wait_n<4, 1>(FQ + h * 32, 1, TMO)) return;
    const unsigned lo = lane_id() * 16, d = sb + C_KT + (2 * w - 8) * 1024;
    glds16c(gK, lo, d);
    glds16c(gK + 1024, lo, d + 1024);
    glds16c(gV, lo, d + 8192);
    glds16c(gV + 1024, lo, d + 9216);
  }
  DRAIN();
  SST(8);
  const v4u rsA = mk_srd(ws + OFF_ACC + L * SZ_ACC, SZ_ACC);
  const v4u rsF = mk_srd(FQ, 64 * 32 * 32 * 4);
  const v4u rsMK = mk_srd(ws + OFF_MEMK + L * SZ_MEM, SZ_MEM);
  const v4u rsMV = mk_srd(ws + OFF_MEMV + L * SZ_MEM, SZ_MEM);
  const char* pAB = ws + OFF_ACC + L * SZ_ACC + (wb - w) * 4096;
  const char* pGK = gK + NH * 16 * 1024;
  const char* pGV = gV + NH * 8 * 1024;
  const v4u rsT = mk_srd(FL + FL_FK + L * 32, 64 * 1024 - 128);
  unsigned sc[8];
  sc[0] = (32 + h) * 128;
  sc[1] = (h * 4 + w - 4) * 512;
  sc[2] = (h * 4 + w - 4) * 512;
  sc[3] = NSEG - 1;
  sc[4] = __float_as_uint(gw2);
  sc[5] = w;
  sc[6] = sb;
  sc[7] = h == 0 ? 0xd4000000u - 2u - (1u << 29) : 0xd4000000u;
  const char* dbg = ws;
#define CHAIN_OPS                                                                                                          \
  ::"{v[64:71]}"(w0[0]), "{v[72:79]}"(w0[1]), "{v[80:87]}"(w0[2]), "{v[88:95]}"(w0[3]), "{v[96:103]}"(w0[4]),              \
      "{v[104:111]}"(w0[5]), "{v[112:119]}"(w0[6]), "{v[120:127]}"(w0[7]), "{v[128:135]}"(w1[0]), "{v[136:143]}"(w1[1]),    \
      "{v[144:151]}"(w1[2]), "{v[152:159]}"(w1[3]), "{v[160:167]}"(w1[4]), "{v[168:175]}"(w1[5]), "{v[176:183]}"(w1[6]),    \
      "{v[184:191]}"(w1[7]), "{v[192:199]}"(w2[0]), "{v[200:207]}"(w2[1]), "{v[208:215]}"(w2[2]), "{v[216:223]}"(w2[3]),    \
      "{v[224:231]}"(w2[4]), "{v[232:239]}"(w2[5]), "{v[240:247]}"(w2[6]), "{v[248:255]}"(w2[7]), "{v[0:7]}"(pv),          \
      "{s[40:43]}"(rsA), "{s[44:47]}"(rsF), "{s[48:51]}"(rsMK), "{s[52:55]}"(rsMV), "{s[56:57]}"(pAB), "{s60}"(w * 4096), "{s61}"(ATT ? h * 1024 : w != 4 ? 0x40000000 : L ? (PNL - 1) * 1024 - 128 : 128 - (PNL + 2) * 1024), \
      "{s[62:63]}"(pGK), "{s[64:65]}"(pGV), "{s66}"(0), "{s67}"(w == 4 ? (h >> 3) * 256 + (h & 7) * 8 : 0x40000000), "{s68}"(sc[0]), "{s69}"(sc[1]), "{s70}"(sc[2]),    \
      "{s71}"(sc[3]), "{s72}"(sc[4]), "{s73}"(sc[5]), "{s74}"(sc[6]), "{s75}"(sc[7]), "{s[36:37]}"(dbg),                   \
      "{s[0:3]}"(rsT), "{s79}"((h >> 3) * 16 + (h & 7))                                                                        \
      : "memory", "vcc", "scc", "s99", "v63"
  if (ATT) asm volatile("s_mov_b64 exec, -1\ns_nop 4\n" CHAIN_ASM_ATT CHAIN_OPS);
  else asm volatile("s_mov_b64 exec, -1\ns_nop 4\n" CHAIN_ASM_VO CHAIN_OPS);
#undef CHAIN_OPS
#undef LANE_IDS
}

__device__ __forceinline__ void role_chain(const MegaArgs& A, char* smem, const int L, const int h, const int rep) {
  if (__builtin_amdgcn_readfirstlane(threadIdx.x >> 6) < 4) role_chain_t<true>(A, smem, L, h, rep);
  else role_chain_t<false>(A, smem, L, h, rep);
}

constexpr int AW_SO = 0, AW_TK = 32768, AW_CNT = AW_TK + 98304;
__device__ __forceinline__ unsigned umin3(unsigned a, unsigned b, unsigned c) {
  return __builtin_elementwise_min(__builtin_elementwise_min(a, b), c);
}
#define AW_ASM_X \
  "s_mov_b32 s61, 0\n" \
  "s_mov_b32 s67, 1\n" \
  "s_mov_b32 s68, 0\n" \
  "s_mov_b32 s70, 0\n" \
  "s_mov_b32 s73, 0\n" \
  "s_mov_b32 s71, 0x3e0293ee\n" \
  "s_mov_b32 s72, 0xbe0293ee\n" \
  "s_mov_b32 s65, 49152\n" \
  "s_mov_b32 s66, 16384\n" \
  "v_mov_b32 v38, s58\n" \
  "s_memrealtime s[38:39]\n" \
  "s_waitcnt lgkmcnt(0)\n" \
  "s_mov_b32 s69, s38\n" \
  "s_setprio 3\n" \
  ".LCX_TOP_%=:\n" \
  "s_mov_b32 s62, 0x200000\n" \
  "s_cmp_ge_u32 s73, s67\n" \
  "s_cbranch_scc1 .LCX_TOK_%=\n" \
  "s_cmp_eq_u32 s61, 0\n" \
  "s_cbranch_scc1 .LCX_SPIN_%=\n" \
  "s_waitcnt vmcnt(0)\n" \
  "v_mov_b32 v46, s59\n" \
  "v_mov_b32 v47, 1\n" \
  "s_mov_b64 exec, 1\n" \
  "ds_add_rtn_u32 v46, v46, v47\n" \
  "s_mov_b64 exec, -1\n" \
  "s_xor_b32 s59, s59, 4\n" \
  "s_mov_b32 s61, 0\n" \
  "s_waitcnt lgkmcnt(0)\n" \
  "v_readfirstlane_b32 s63, v46\n" \
  "v_lshlrev_b32 v46, 5, v5\n" \
  "s_and_b32 s63, s63, 7\n" \
  "s_cmp_eq_u32 s63, 7\n" \
  "s_cbranch_scc0 .LCX_W1_%=\n" \
  "s_mov_b64 exec, 0xff\n" \
  "global_store_dword v46, v47, s[52:53] sc1\n" \
  "s_mov_b64 exec, -1\n" \
  ".LCX_W1_%=:\n" \
  "s_add_u32 s52, s52, 4096\n" \
  "s_addc_u32 s53, s53, 0\n" \
  ".LCX_SPIN_%=:\n" \
  "s_sleep 2\n" \
  "s_memrealtime s[38:39]\n" \
  "ds_read_b32 v37, v38\n" \
  "s_sub_u32 s62, s62, 1\n" \
  "s_cbranch_scc1 .LCX_END_%=\n" \
  "s_waitcnt lgkmcnt(0)\n" \
  "v_readfirstlane_b32 s73, v37\n" \
  "s_add_u32 s38, s38, 80\n" \
  "s_bfe_u32 s64, s73, 0x80008\n" \
  "s_and_b32 s73, s73, 0xff\n" \
  "s_min_u32 s73, s73, s64\n" \
  "s_cmp_ge_u32 s73, s67\n" \
  "s_cbranch_scc0 .LCX_SPIN_%=\n" \
  "s_cmp_eq_u32 s70, 0\n" \
  "s_cbranch_scc1 .LCX_TOK_%=\n" \
  "s_sub_u32 s63, s38, s69\n" \
  "s_max_i32 s63, s63, 0\n" \
  "s_add_u32 s69, s69, s63\n" \
  ".LCX_TOK_%=:\n" \
  "ds_read_b128 v[48:51], v6 offset:0\n" \
  "ds_read_b128 v[64:67], v0 offset:8192\n" \
  "ds_read_b128 v[80:83], v0 offset:12288\n" \
  "ds_read_b128 v[52:55], v6 offset:1024\n" \
  "ds_read_b128 v[68:71], v0 offset:9216\n" \
  "ds_read_b128 v[84:87], v0 offset:13312\n" \
  "ds_read_b128 v[56:59], v6 offset:2048\n" \
  "ds_read_b128 v[72:75], v0 offset:10240\n" \
  "ds_read_b128 v[88:91], v0 offset:14336\n" \
  "ds_read_b128 v[60:63], v6 offset:3072\n" \
  "ds_read_b128 v[76:79], v0 offset:11264\n" \
  "ds_read_b128 v[92:95], v0 offset:15360\n" \
  "s_waitcnt lgkmcnt(9)\n" \
  "v_mfma_f32_16x16x32_bf16 v[20:23], v[64:67], v[48:51], v[8:11]\n" \
  "v_mfma_f32_16x16x32_bf16 v[24:27], v[80:83], v[48:51], v[12:15]\n" \
  "ds_read_b128 v[96:99], v0 offset:16384\n" \
  "ds_read_b128 v[100:103], v0 offset:17408\n" \
  "s_waitcnt lgkmcnt(8)\n" \
  "v_mfma_f32_16x16x32_bf16 v[20:23], v[68:71], v[52:55], v[20:23]\n" \
  "v_mfma_f32_16x16x32_bf16 v[24:27], v[84:87], v[52:55], v[24:27]\n" \
  "ds_read_b128 v[104:107], v0 offset:18432\n" \
  "ds_read_b128 v[108:111], v0 offset:19456\n" \
  "s_waitcnt lgkmcnt(7)\n" \
  "v_mfma_f32_16x16x32_bf16 v[20:23], v[72:75], v[56:59], v[20:23]\n" \
  "v_mfma_f32_16x16x32_bf16 v[24:27], v[88:91], v[56:59], v[24:27]\n" \
  "ds_read_b128 v[112:115], v0 offset:20480\n" \
  "ds_read_b128 v[116:119], v0 offset:21504\n" \
  "s_waitcnt lgkmcnt(6)\n" \
  "v_mfma_f32_16x16x32_bf16 v[20:23], v[76:79], v[60:63], v[20:23]\n" \
  "v_mfma_f32_16x16x32_bf16 v[24:27], v[92:95], v[60:63], v[24:27]\n" \
  "ds_read_b128 v[120:123], v0 offset:22528\n" \
  "ds_read_b128 v[124:127], v0 offset:23552\n" \
  "s_add_u32 s67, s67, 1\n" \
  "v_add_u32 v0, s65, v0\n" \
  "v_add_u32 v6, s65, v6\n" \
  "s_sub_u32 s65, 0, s65\n" \
  "s_nop 1\n" \
  "v_max3_f32 v32, v20, v21, v22\n" \
  "v_max3_f32 v33, v23, v24, v25\n" \
  "v_max3_f32 v32, v32, v26, v27\n" \
  "v_max_f32 v32, v32, v33\n" \
  "v_mov_b32 v33, v32\n" \
  "s_nop 1\n" \
  "v_permlane16_swap_b32 v32, v33\n" \
  "v_max_f32 v32, v32, v33\n" \
  "v_mov_b32 v33, v32\n" \
  "s_nop 1\n" \
  "v_permlane32_swap_b32 v32, v33\n" \
  "v_max_f32 v32, v32, v33\n" \
  "v_mul_f32 v33, s72, v32\n" \
  "v_fma_f32 v20, v20, s71, v33\n" \
  "v_fma_f32 v21, v21, s71, v33\n" \
  "v_fma_f32 v22, v22, s71, v33\n" \
  "v_fma_f32 v23, v23, s71, v33\n" \
  "v_fma_f32 v24, v24, s71, v33\n" \
  "v_fma_f32 v25, v25, s71, v33\n" \
  "v_fma_f32 v26, v26, s71, v33\n" \
  "v_fma_f32 v27, v27, s71, v33\n" \
  "v_exp_f32 v20, v20\n" \
  "v_exp_f32 v21, v21\n" \
  "v_exp_f32 v22, v22\n" \
  "v_exp_f32 v23, v23\n" \
  "v_exp_f32 v24, v24\n" \
  "v_exp_f32 v25, v25\n" \
  "v_exp_f32 v26, v26\n" \
  "v_exp_f32 v27, v27\n" \
  "v_add_f32 v34, v20, v21\n" \
  "v_add_f32 v33, v22, v23\n" \
  "v_add_f32 v36, v24, v25\n" \
  "v_add_f32 v34, v34, v33\n" \
  "v_add_f32 v33, v26, v27\n" \
  "v_add_f32 v34, v34, v36\n" \
  "v_add_f32 v34, v34, v33\n" \
  "s_waitcnt vmcnt(0) lgkmcnt(0)\n" \
  "s_cmp_eq_u32 s61, 0\n" \
  "s_cbranch_scc1 .LCX_NP_%=\n" \
  "v_mov_b32 v46, s59\n" \
  "v_mov_b32 v47, 1\n" \
  "s_mov_b64 exec, 1\n" \
  "ds_add_rtn_u32 v46, v46, v47\n" \
  "s_mov_b64 exec, -1\n" \
  "s_xor_b32 s59, s59, 4\n" \
  "s_mov_b32 s61, 0\n" \
  "s_waitcnt lgkmcnt(0)\n" \
  "v_readfirstlane_b32 s63, v46\n" \
  "v_lshlrev_b32 v46, 5, v5\n" \
  "s_and_b32 s63, s63, 7\n" \
  "s_cmp_eq_u32 s63, 7\n" \
  "s_cbranch_scc0 .LCX_W2_%=\n" \
  "s_mov_b64 exec, 0xff\n" \
  "global_store_dword v46, v47, s[52:53] sc1\n" \
  "s_mov_b64 exec, -1\n" \
  ".LCX_W2_%=:\n" \
  "s_add_u32 s52, s52, 4096\n" \
  "s_addc_u32 s53, s53, 0\n" \
  ".LCX_NP_%=:\n" \
  "s_cmp_lg_u32 s68, 0\n" \
  "s_cbranch_scc1 .LCX_TEST_%=\n" \
  ".LCX_WF_%=:\n" \
  "s_memrealtime s[38:39]\n" \
  "s_waitcnt lgkmcnt(0)\n" \
  "s_sub_u32 s63, s69, s38\n" \
  "s_cmp_le_i32 s63, 0\n" \
  "s_cbranch_scc1 .LCX_GO_%=\n" \
  "s_cmp_lt_i32 s63, 12\n" \
  "s_cbranch_scc1 .LCX_WF_%=\n" \
  "s_sleep 3\n" \
  "s_branch .LCX_WF_%=\n" \
  ".LCX_GO_%=:\n" \
  "v_mov_b32 v35, 0\n" \
  "global_load_dwordx4 v[128:131], v2, s[48:49] offset:0 sc1\n" \
  "global_load_dwordx4 v[132:135], v2, s[48:49] offset:512 sc1\n" \
  "global_load_dwordx4 v[136:139], v2, s[48:49] offset:1024 sc1\n" \
  "global_load_dwordx4 v[140:143], v2, s[48:49] offset:1536 sc1\n" \
  "global_load_dwordx4 v[144:147], v3, s[48:49] offset:0 sc1\n" \
  "global_load_dwordx4 v[148:151], v3, s[48:49] offset:512 sc1\n" \
  "global_load_dwordx4 v[152:155], v3, s[48:49] offset:1024 sc1\n" \
  "global_load_dwordx4 v[156:159], v3, s[48:49] offset:1536 sc1\n" \
  "global_load_dword v35, v35, s[50:51] sc1\n" \
  "s_sub_u32 s62, s62, 8\n" \
  "s_cbranch_scc1 .LCX_END_%=\n" \
  "s_waitcnt vmcnt(0)\n" \
  ".LCX_TEST_%=:\n" \
  "v_mfma_f32_16x16x32_bf16 v[28:31], v[128:131], v[48:51], v[16:19]\n" \
  "v_min3_u32 v33, v128, v129, v130\n" \
  "v_min3_u32 v36, v131, v132, v133\n" \
  "v_min3_u32 v33, v33, v134, v135\n" \
  "v_min3_u32 v36, v36, v136, v137\n" \
  "v_mfma_f32_16x16x32_bf16 v[28:31], v[132:135], v[52:55], v[28:31]\n" \
  "v_min3_u32 v33, v33, v138, v139\n" \
  "v_min3_u32 v36, v36, v140, v141\n" \
  "v_min3_u32 v33, v33, v142, v143\n" \
  "v_min3_u32 v36, v36, v144, v145\n" \
  "v_mfma_f32_16x16x32_bf16 v[28:31], v[136:139], v[56:59], v[28:31]\n" \
  "v_min3_u32 v33, v33, v146, v147\n" \
  "v_min3_u32 v36, v36, v148, v149\n" \
  "v_min3_u32 v33, v33, v150, v151\n" \
  "v_min3_u32 v36, v36, v152, v153\n" \
  "v_mfma_f32_16x16x32_bf16 v[28:31], v[140:143], v[60:63], v[28:31]\n" \
  "v_min3_u32 v33, v33, v154, v155\n" \
  "v_min3_u32 v36, v36, v156, v157\n" \
  "v_min3_u32 v33, v33, v158, v159\n" \
  "v_min_u32 v33, v33, v36\n" \
  "v_cmp_eq_u32 vcc, 0, v33\n" \
  "v_readfirstlane_b32 s64, v35\n" \
  "s_mov_b32 s68, 0\n" \
  "s_cmp_eq_u64 vcc, 0\n" \
  "s_cbranch_scc0 .LCX_FAIL_%=\n" \
  "v_max3_f32 v36, v28, v29, v30\n" \
  "v_max3_f32 v36, v36, v31, v32\n" \
  "v_mov_b32 v33, v36\n" \
  "s_cmp_eq_u32 s64, 0\n" \
  "s_cselect_b32 s64, s38, s64\n" \
  "v_permlane16_swap_b32 v36, v33\n" \
  "v_max_f32 v36, v36, v33\n" \
  "v_mov_b32 v33, v36\n" \
  "s_sub_u32 s63, s64, s70\n" \
  "s_mov_b32 s70, s64\n" \
  "v_permlane32_swap_b32 v36, v33\n" \
  "v_max_f32 v36, v36, v33\n" \
  "v_mul_f32 v36, s72, v36\n" \
  "v_fma_f32 v32, v32, s71, v36\n" \
  "v_fma_f32 v28, v28, s71, v36\n" \
  "v_fma_f32 v29, v29, s71, v36\n" \
  "v_fma_f32 v30, v30, s71, v36\n" \
  "v_fma_f32 v31, v31, s71, v36\n" \
  "v_exp_f32 v32, v32\n" \
  "v_exp_f32 v28, v28\n" \
  "v_exp_f32 v29, v29\n" \
  "v_exp_f32 v30, v30\n" \
  "v_exp_f32 v31, v31\n" \
  "v_fma_f32 v34, v34, v32, v28\n" \
  "v_add_f32 v33, v29, v30\n" \
  "v_add_f32 v34, v34, v31\n" \
  "v_add_f32 v34, v34, v33\n" \
  "v_mov_b32 v33, v34\n" \
  "s_min_u32 s63, s63, 250\n" \
  "s_add_u32 s69, s64, s63\n" \
  "v_permlane16_swap_b32 v34, v33\n" \
  "v_add_f32 v34, v34, v33\n" \
  "v_mov_b32 v33, v34\n" \
  "s_add_u32 s69, s69, 68\n" \
  "s_add_u32 s48, s48, 0x10000\n" \
  "s_addc_u32 s49, s49, 0\n" \
  "v_permlane32_swap_b32 v34, v33\n" \
  "v_add_f32 v34, v34, v33\n" \
  "v_rcp_f32 v34, v34\n" \
  "s_add_u32 s50, s50, 1024\n" \
  "s_addc_u32 s51, s51, 0\n" \
  "v_mul_f32 v32, v32, v34\n" \
  "v_mul_f32 v20, v20, v32\n" \
  "v_mul_f32 v21, v21, v32\n" \
  "v_mul_f32 v22, v22, v32\n" \
  "v_mul_f32 v23, v23, v32\n" \
  "v_mul_f32 v24, v24, v32\n" \
  "v_mul_f32 v25, v25, v32\n" \
  "v_mul_f32 v26, v26, v32\n" \
  "v_mul_f32 v27, v27, v32\n" \
  "v_cvt_pk_bf16_f32 v40, v20, v21\n" \
  "v_cvt_pk_bf16_f32 v41, v22, v23\n" \
  "v_cvt_pk_bf16_f32 v42, v24, v25\n" \
  "v_cvt_pk_bf16_f32 v43, v26, v27\n" \
  "v_mul_f32 v28, v28, v34\n" \
  "v_mul_f32 v29, v29, v34\n" \
  "v_mfma_f32_16x16x32_bf16 v[64:67], v[96:99], v[40:43], 0\n" \
  "v_mul_f32 v30, v30, v34\n" \
  "v_mfma_f32_16x16x32_bf16 v[68:71], v[100:103], v[40:43], 0\n" \
  "v_mul_f32 v31, v31, v34\n" \
  "v_mfma_f32_16x16x32_bf16 v[72:75], v[104:107], v[40:43], 0\n" \
  "v_cvt_pk_bf16_f32 v44, v28, v29\n" \
  "v_mfma_f32_16x16x32_bf16 v[76:79], v[108:111], v[40:43], 0\n" \
  "v_cvt_pk_bf16_f32 v45, v30, v31\n" \
  "v_mfma_f32_16x16x32_bf16 v[80:83], v[112:115], v[40:43], 0\n" \
  "v_mfma_f32_16x16x32_bf16 v[84:87], v[116:119], v[40:43], 0\n" \
  "v_mfma_f32_16x16x32_bf16 v[88:91], v[120:123], v[40:43], 0\n" \
  "v_mfma_f32_16x16x32_bf16 v[92:95], v[124:127], v[40:43], 0\n" \
  "s_memrealtime s[38:39]\n" \
  "ds_read_b32 v37, v38\n" \
  "v_mfma_f32_16x16x16_bf16 v[64:67], v[144:145], v[44:45], v[64:67]\n" \
  "v_mfma_f32_16x16x16_bf16 v[68:71], v[148:149], v[44:45], v[68:71]\n" \
  "v_mfma_f32_16x16x16_bf16 v[72:75], v[152:153], v[44:45], v[72:75]\n" \
  "v_mfma_f32_16x16x16_bf16 v[76:79], v[156:157], v[44:45], v[76:79]\n" \
  "v_mfma_f32_16x16x16_bf16 v[80:83], v[146:147], v[44:45], v[80:83]\n" \
  "v_mfma_f32_16x16x16_bf16 v[84:87], v[150:151], v[44:45], v[84:87]\n" \
  "v_mfma_f32_16x16x16_bf16 v[88:91], v[154:155], v[44:45], v[88:91]\n" \
  "v_mfma_f32_16x16x16_bf16 v[92:95], v[158:159], v[44:45], v[92:95]\n" \
  "s_nop 0\n" \
  "v_cvt_pk_bf16_f32 v160, v64, v65\n" \
  "v_cvt_pk_bf16_f32 v161, v66, v67\n" \
  "v_cvt_pk_bf16_f32 v162, v68, v69\n" \
  "v_cvt_pk_bf16_f32 v163, v70, v71\n" \
  "ds_write_b128 v7, v[160:163] offset:0\n" \
  "v_cvt_pk_bf16_f32 v164, v72, v73\n" \
  "v_cvt_pk_bf16_f32 v165, v74, v75\n" \
  "v_cvt_pk_bf16_f32 v166, v76, v77\n" \
  "v_cvt_pk_bf16_f32 v167, v78, v79\n" \
  "ds_write_b128 v7, v[164:167] offset:1024\n" \
  "v_cvt_pk_bf16_f32 v168, v80, v81\n" \
  "v_cvt_pk_bf16_f32 v169, v82, v83\n" \
  "v_cvt_pk_bf16_f32 v170, v84, v85\n" \
  "v_cvt_pk_bf16_f32 v171, v86, v87\n" \
  "ds_write_b128 v7, v[168:171] offset:2048\n" \
  "v_cvt_pk_bf16_f32 v172, v88, v89\n" \
  "v_cvt_pk_bf16_f32 v173, v90, v91\n" \
  "v_cvt_pk_bf16_f32 v174, v92, v93\n" \
  "v_cvt_pk_bf16_f32 v175, v94, v95\n" \
  "ds_write_b128 v7, v[172:175] offset:3072\n" \
  "v_add_u32 v7, s66, v7\n" \
  "s_waitcnt lgkmcnt(0)\n" \
  "s_barrier\n" \
  "ds_read_b128 v[48:51], v1 offset:0\n" \
  "ds_read_b128 v[52:55], v1 offset:4096\n" \
  "ds_read_b128 v[56:59], v1 offset:1024\n" \
  "ds_read_b128 v[60:63], v1 offset:5120\n" \
  "ds_read_b128 v[64:67], v1 offset:2048\n" \
  "ds_read_b128 v[68:71], v1 offset:6144\n" \
  "ds_read_b128 v[72:75], v1 offset:3072\n" \
  "ds_read_b128 v[76:79], v1 offset:7168\n" \
  "ds_read_b128 v[80:83], v1 offset:8192\n" \
  "ds_read_b128 v[84:87], v1 offset:12288\n" \
  "ds_read_b128 v[88:91], v1 offset:9216\n" \
  "ds_read_b128 v[92:95], v1 offset:13312\n" \
  "ds_read_b128 v[96:99], v1 offset:10240\n" \
  "ds_read_b128 v[100:103], v1 offset:14336\n" \
  "v_readfirstlane_b32 s73, v37\n" \
  "s_sub_u32 s63, s38, s69\n" \
  "s_bfe_u32 s64, s73, 0x80010\n" \
  "s_sub_u32 s64, s64, s67\n" \
  "s_or_b32 s63, s63, s64\n" \
  "s_bfe_u32 s64, s73, 0x80008\n" \
  "s_and_b32 s73, s73, 0xff\n" \
  "s_min_u32 s73, s73, s64\n" \
  "s_or_b32 s63, s63, s68\n" \
  "s_cmp_lt_i32 s63, 0\n" \
  "s_cbranch_scc1 .LCX_E3_%=\n" \
  "v_mov_b32 v35, 0\n" \
  "global_load_dwordx4 v[128:131], v2, s[48:49] offset:0 sc1\n" \
  "global_load_dwordx4 v[132:135], v2, s[48:49] offset:512 sc1\n" \
  "global_load_dwordx4 v[136:139], v2, s[48:49] offset:1024 sc1\n" \
  "global_load_dwordx4 v[140:143], v2, s[48:49] offset:1536 sc1\n" \
  "global_load_dwordx4 v[144:147], v3, s[48:49] offset:0 sc1\n" \
  "global_load_dwordx4 v[148:151], v3, s[48:49] offset:512 sc1\n" \
  "global_load_dwordx4 v[152:155], v3, s[48:49] offset:1024 sc1\n" \
  "global_load_dwordx4 v[156:159], v3, s[48:49] offset:1536 sc1\n" \
  "global_load_dword v35, v35, s[50:51] sc1\n" \
  "s_mov_b32 s68, -1\n" \
  ".LCX_E3_%=:\n" \
  "s_waitcnt lgkmcnt(12)\n" \
  "v_mfma_f32_16x16x32_bf16 v[112:115], v[192:195], v[48:51], 0\n" \
  "v_mfma_f32_16x16x32_bf16 v[116:119], v[224:227], v[48:51], 0\n" \
  "v_mfma_f32_16x16x32_bf16 v[120:123], v[192:195], v[52:55], 0\n" \
  "v_mfma_f32_16x16x32_bf16 v[124:127], v[224:227], v[52:55], 0\n" \
  "ds_read_b128 v[104:107], v1 offset:11264\n" \
  "ds_read_b128 v[108:111], v1 offset:15360\n" \
  "s_waitcnt lgkmcnt(12)\n" \
  "v_mfma_f32_16x16x32_bf16 v[112:115], v[196:199], v[56:59], v[112:115]\n" \
  "v_mfma_f32_16x16x32_bf16 v[116:119], v[228:231], v[56:59], v[116:119]\n" \
  "v_mfma_f32_16x16x32_bf16 v[120:123], v[196:199], v[60:63], v[120:123]\n" \
  "v_mfma_f32_16x16x32_bf16 v[124:127], v[228:231], v[60:63], v[124:127]\n" \
  "s_waitcnt lgkmcnt(10)\n" \
  "v_mfma_f32_16x16x32_bf16 v[112:115], v[200:203], v[64:67], v[112:115]\n" \
  "v_mfma_f32_16x16x32_bf16 v[116:119], v[232:235], v[64:67], v[116:119]\n" \
  "v_mfma_f32_16x16x32_bf16 v[120:123], v[200:203], v[68:71], v[120:123]\n" \
  "v_mfma_f32_16x16x32_bf16 v[124:127], v[232:235], v[68:71], v[124:127]\n" \
  "s_waitcnt lgkmcnt(8)\n" \
  "v_mfma_f32_16x16x32_bf16 v[112:115], v[204:207], v[72:75], v[112:115]\n" \
  "v_mfma_f32_16x16x32_bf16 v[116:119], v[236:239], v[72:75], v[116:119]\n" \
  "v_mfma_f32_16x16x32_bf16 v[120:123], v[204:207], v[76:79], v[120:123]\n" \
  "v_mfma_f32_16x16x32_bf16 v[124:127], v[236:239], v[76:79], v[124:127]\n" \
  "s_waitcnt lgkmcnt(6)\n" \
  "v_mfma_f32_16x16x32_bf16 v[112:115], v[208:211], v[80:83], v[112:115]\n" \
  "v_mfma_f32_16x16x32_bf16 v[116:119], v[240:243], v[80:83], v[116:119]\n" \
  "v_mfma_f32_16x16x32_bf16 v[120:123], v[208:211], v[84:87], v[120:123]\n" \
  "v_mfma_f32_16x16x32_bf16 v[124:127], v[240:243], v[84:87], v[124:127]\n" \
  "s_waitcnt lgkmcnt(4)\n" \
  "v_mfma_f32_16x16x32_bf16 v[112:115], v[212:215], v[88:91], v[112:115]\n" \
  "v_mfma_f32_16x16x32_bf16 v[116:119], v[244:247], v[88:91], v[116:119]\n" \
  "v_mfma_f32_16x16x32_bf16 v[120:123], v[212:215], v[92:95], v[120:123]\n" \
  "v_mfma_f32_16x16x32_bf16 v[124:127], v[244:247], v[92:95], v[124:127]\n" \
  "s_memrealtime s[38:39]\n" \
  "ds_read_b32 v37, v38\n" \
  "s_waitcnt lgkmcnt(3)\n" \
  "v_mfma_f32_16x16x32_bf16 v[112:115], v[216:219], v[96:99], v[112:115]\n" \
  "v_mfma_f32_16x16x32_bf16 v[116:119], v[248:251], v[96:99], v[116:119]\n" \
  "v_mfma_f32_16x16x32_bf16 v[120:123], v[216:219], v[100:103], v[120:123]\n" \
  "v_mfma_f32_16x16x32_bf16 v[124:127], v[248:251], v[100:103], v[124:127]\n" \
  "s_waitcnt lgkmcnt(0)\n" \
  "v_mfma_f32_16x16x32_bf16 v[112:115], v[220:223], v[104:107], v[112:115]\n" \
  "v_mfma_f32_16x16x32_bf16 v[116:119], v[252:255], v[104:107], v[116:119]\n" \
  "v_mfma_f32_16x16x32_bf16 v[120:123], v[220:223], v[108:111], v[120:123]\n" \
  "v_mfma_f32_16x16x32_bf16 v[124:127], v[252:255], v[108:111], v[124:127]\n" \
  "v_add_u32 v1, s66, v1\n" \
  "s_sub_u32 s66, 0, s66\n" \
  "s_add_u32 s64, s54, 16384\n" \
  "s_mov_b32 s61, 1\n" \
  "s_nop 1\n" \
  "v_cvt_pk_bf16_f32 v20, v112, v113\n" \
  "v_cvt_pk_bf16_f32 v21, v114, v115\n" \
  "v_cvt_pk_bf16_f32 v22, v116, v117\n" \
  "v_cvt_pk_bf16_f32 v23, v118, v119\n" \
  "buffer_store_dwordx4 v[20:23], v4, s[40:43], s54 offen sc1\n" \
  "v_cvt_pk_bf16_f32 v24, v120, v121\n" \
  "v_cvt_pk_bf16_f32 v25, v122, v123\n" \
  "v_cvt_pk_bf16_f32 v26, v124, v125\n" \
  "v_cvt_pk_bf16_f32 v27, v126, v127\n" \
  "buffer_store_dwordx4 v[24:27], v4, s[40:43], s64 offen sc1\n" \
  "v_readfirstlane_b32 s73, v37\n" \
  "s_sub_u32 s63, s38, s69\n" \
  "s_bfe_u32 s64, s73, 0x80010\n" \
  "s_sub_u32 s64, s64, s67\n" \
  "s_or_b32 s63, s63, s64\n" \
  "s_bfe_u32 s64, s73, 0x80008\n" \
  "s_and_b32 s73, s73, 0xff\n" \
  "s_min_u32 s73, s73, s64\n" \
  "s_or_b32 s63, s63, s68\n" \
  "s_cmp_lt_i32 s63, 0\n" \
  "s_cbranch_scc1 .LCX_E4_%=\n" \
  "v_mov_b32 v35, 0\n" \
  "global_load_dwordx4 v[128:131], v2, s[48:49] offset:0 sc1\n" \
  "global_load_dwordx4 v[132:135], v2, s[48:49] offset:512 sc1\n" \
  "global_load_dwordx4 v[136:139], v2, s[48:49] offset:1024 sc1\n" \
  "global_load_dwordx4 v[140:143], v2, s[48:49] offset:1536 sc1\n" \
  "global_load_dwordx4 v[144:147], v3, s[48:49] offset:0 sc1\n" \
  "global_load_dwordx4 v[148:151], v3, s[48:49] offset:512 sc1\n" \
  "global_load_dwordx4 v[152:155], v3, s[48:49] offset:1024 sc1\n" \
  "global_load_dwordx4 v[156:159], v3, s[48:49] offset:1536 sc1\n" \
  "global_load_dword v35, v35, s[50:51] sc1\n" \
  "s_mov_b32 s68, -1\n" \
  ".LCX_E4_%=:\n" \
  "s_add_u32 s54, s54, 0x80000\n" \
  "s_sub_u32 s60, s60, 1\n" \
  "s_cmp_lg_u32 s60, 0\n" \
  "s_cbranch_scc1 .LCX_TOP_%=\n" \
  "s_waitcnt vmcnt(0)\n" \
  "v_mov_b32 v46, s59\n" \
  "v_mov_b32 v47, 1\n" \
  "s_mov_b64 exec, 1\n" \
  "ds_add_rtn_u32 v46, v46, v47\n" \
  "s_mov_b64 exec, -1\n" \
  "s_xor_b32 s59, s59, 4\n" \
  "s_mov_b32 s61, 0\n" \
  "s_waitcnt lgkmcnt(0)\n" \
  "v_readfirstlane_b32 s63, v46\n" \
  "v_lshlrev_b32 v46, 5, v5\n" \
  "s_and_b32 s63, s63, 7\n" \
  "s_cmp_eq_u32 s63, 7\n" \
  "s_cbranch_scc0 .LCX_W5_%=\n" \
  "s_mov_b64 exec, 0xff\n" \
  "global_store_dword v46, v47, s[52:53] sc1\n" \
  "s_mov_b64 exec, -1\n" \
  ".LCX_W5_%=:\n" \
  "s_add_u32 s52, s52, 4096\n" \
  "s_addc_u32 s53, s53, 0\n" \
  ".LCX_END_%=:\n" \
  "s_endpgm\n" \
  ".LCX_FAIL_%=:\n" \
  "s_add_u32 s69, s64, 68\n" \
  "s_cmp_lg_u32 s64, 0\n" \
  "s_cbranch_scc1 .LCX_WF_%=\n" \
  "s_sleep 4\n" \
  "global_load_dword v35, v35, s[50:51] sc1\n" \
  "s_sub_u32 s62, s62, 8\n" \
  "s_cbranch_scc1 .LCX_END_%=\n" \
  "s_waitcnt vmcnt(0)\n" \
  "v_readfirstlane_b32 s64, v35\n" \
  "s_branch .LCX_FAIL_%=\n"
#define AW_ASM_Y \
  "s_mov_b32 s67, 0\n" \
  "s_mov_b32 s68, 2\n" \
  "s_mov_b32 s69, 0\n" \
  "s_mov_b32 s65, 49152\n" \
  "s_mov_b32 s66, 16384\n" \
  "v_mov_b32 v5, s58\n" \
  "v_add_u32 v3, 4096, v2\n" \
  "s_mov_b32 s62, 0x40000\n" \
  "s_branch .LCY_ASK_%=\n" \
  ".LCY_BAR_%=:\n" \
  "s_barrier\n" \
  "ds_read_b128 v[8:11], v0 offset:0\n" \
  "ds_read_b128 v[12:15], v0 offset:1024\n" \
  "ds_read_b128 v[16:19], v0 offset:2048\n" \
  "s_add_u32 s68, s68, 1\n" \
  "s_add_u32 s64, s54, 16384\n" \
  "s_cmp_eq_u32 s69, 0\n" \
  "s_cbranch_scc1 .LCY_NI_%=\n" \
  "s_waitcnt vmcnt(1)\n" \
  "s_cmp_lg_u32 s61, 0\n" \
  "s_cbranch_scc1 .LCY_NJ_%=\n" \
  "s_waitcnt vmcnt(0)\n" \
  ".LCY_NJ_%=:\n" \
  "v_mov_b32 v6, s55\n" \
  "s_mov_b64 exec, 1\n" \
  "ds_add_u32 v5, v6\n" \
  "s_mov_b64 exec, -1\n" \
  "s_mov_b32 s69, 0\n" \
  ".LCY_NI_%=:\n" \
  "v_cmp_eq_u32 vcc, 0, v4\n" \
  "s_cmp_eq_u64 vcc, 0\n" \
  "s_cselect_b32 s70, 1, 0\n" \
  "s_cbranch_scc0 .LCY_NS_%=\n" \
  "v_lshlrev_b32 v6, 16, s55\n" \
  "s_mov_b64 exec, 1\n" \
  "ds_add_u32 v5, v6\n" \
  "s_mov_b64 exec, -1\n" \
  "s_mov_b32 m0, s56\n" \
  "s_add_u32 s71, s56, 4096\n" \
  "global_load_lds_dwordx4 v2, s[48:49] offset:0 sc1\n" \
  "global_load_lds_dwordx4 v2, s[48:49] offset:1024 sc1\n" \
  "global_load_lds_dwordx4 v2, s[48:49] offset:2048 sc1\n" \
  "global_load_lds_dwordx4 v2, s[48:49] offset:3072 sc1\n" \
  "s_mov_b32 m0, s71\n" \
  "s_add_u32 s56, s56, s65\n" \
  "global_load_lds_dwordx4 v3, s[48:49] offset:0 sc1\n" \
  "global_load_lds_dwordx4 v3, s[48:49] offset:1024 sc1\n" \
  ".LCY_NS_%=:\n" \
  "ds_read_b128 v[20:23], v0 offset:3072\n" \
  "s_waitcnt lgkmcnt(3)\n" \
  "v_mfma_f32_16x16x32_bf16 v[32:35], v[64:67], v[8:11], 0\n" \
  "v_mfma_f32_16x16x32_bf16 v[36:39], v[96:99], v[8:11], 0\n" \
  "v_mfma_f32_16x16x32_bf16 v[40:43], v[128:131], v[8:11], 0\n" \
  "v_mfma_f32_16x16x32_bf16 v[44:47], v[160:163], v[8:11], 0\n" \
  "v_mfma_f32_16x16x32_bf16 v[48:51], v[192:195], v[8:11], 0\n" \
  "v_mfma_f32_16x16x32_bf16 v[52:55], v[224:227], v[8:11], 0\n" \
  "ds_read_b128 v[8:11], v0 offset:8192\n" \
  "s_waitcnt lgkmcnt(3)\n" \
  "v_mfma_f32_16x16x32_bf16 v[32:35], v[68:71], v[12:15], v[32:35]\n" \
  "v_mfma_f32_16x16x32_bf16 v[36:39], v[100:103], v[12:15], v[36:39]\n" \
  "v_mfma_f32_16x16x32_bf16 v[40:43], v[132:135], v[12:15], v[40:43]\n" \
  "v_mfma_f32_16x16x32_bf16 v[44:47], v[164:167], v[12:15], v[44:47]\n" \
  "v_mfma_f32_16x16x32_bf16 v[48:51], v[196:199], v[12:15], v[48:51]\n" \
  "v_mfma_f32_16x16x32_bf16 v[52:55], v[228:231], v[12:15], v[52:55]\n" \
  "ds_read_b128 v[12:15], v0 offset:9216\n" \
  "s_waitcnt lgkmcnt(3)\n" \
  "v_mfma_f32_16x16x32_bf16 v[32:35], v[72:75], v[16:19], v[32:35]\n" \
  "v_mfma_f32_16x16x32_bf16 v[36:39], v[104:107], v[16:19], v[36:39]\n" \
  "v_mfma_f32_16x16x32_bf16 v[40:43], v[136:139], v[16:19], v[40:43]\n" \
  "v_mfma_f32_16x16x32_bf16 v[44:47], v[168:171], v[16:19], v[44:47]\n" \
  "v_mfma_f32_16x16x32_bf16 v[48:51], v[200:203], v[16:19], v[48:51]\n" \
  "v_mfma_f32_16x16x32_bf16 v[52:55], v[232:235], v[16:19], v[52:55]\n" \
  "ds_read_b128 v[16:19], v0 offset:10240\n" \
  "s_waitcnt lgkmcnt(3)\n" \
  "v_mfma_f32_16x16x32_bf16 v[32:35], v[76:79], v[20:23], v[32:35]\n" \
  "v_mfma_f32_16x16x32_bf16 v[36:39], v[108:111], v[20:23], v[36:39]\n" \
  "v_mfma_f32_16x16x32_bf16 v[40:43], v[140:143], v[20:23], v[40:43]\n" \
  "v_mfma_f32_16x16x32_bf16 v[44:47], v[172:175], v[20:23], v[44:47]\n" \
  "v_mfma_f32_16x16x32_bf16 v[48:51], v[204:207], v[20:23], v[48:51]\n" \
  "v_mfma_f32_16x16x32_bf16 v[52:55], v[236:239], v[20:23], v[52:55]\n" \
  "ds_read_b128 v[20:23], v0 offset:11264\n" \
  "s_waitcnt lgkmcnt(3)\n" \
  "v_mfma_f32_16x16x32_bf16 v[32:35], v[80:83], v[8:11], v[32:35]\n" \
  "v_mfma_f32_16x16x32_bf16 v[36:39], v[112:115], v[8:11], v[36:39]\n" \
  "v_mfma_f32_16x16x32_bf16 v[40:43], v[144:147], v[8:11], v[40:43]\n" \
  "v_mfma_f32_16x16x32_bf16 v[44:47], v[176:179], v[8:11], v[44:47]\n" \
  "v_mfma_f32_16x16x32_bf16 v[48:51], v[208:211], v[8:11], v[48:51]\n" \
  "v_mfma_f32_16x16x32_bf16 v[52:55], v[240:243], v[8:11], v[52:55]\n" \
  "ds_read_b128 v[8:11], v0 offset:4096\n" \
  "s_waitcnt lgkmcnt(3)\n" \
  "v_mfma_f32_16x16x32_bf16 v[32:35], v[84:87], v[12:15], v[32:35]\n" \
  "v_mfma_f32_16x16x32_bf16 v[36:39], v[116:119], v[12:15], v[36:39]\n" \
  "v_mfma_f32_16x16x32_bf16 v[40:43], v[148:151], v[12:15], v[40:43]\n" \
  "v_mfma_f32_16x16x32_bf16 v[44:47], v[180:183], v[12:15], v[44:47]\n" \
  "v_mfma_f32_16x16x32_bf16 v[48:51], v[212:215], v[12:15], v[48:51]\n" \
  "v_mfma_f32_16x16x32_bf16 v[52:55], v[244:247], v[12:15], v[52:55]\n" \
  "ds_read_b128 v[12:15], v0 offset:5120\n" \
  "s_waitcnt lgkmcnt(3)\n" \
  "v_mfma_f32_16x16x32_bf16 v[32:35], v[88:91], v[16:19], v[32:35]\n" \
  "v_mfma_f32_16x16x32_bf16 v[36:39], v[120:123], v[16:19], v[36:39]\n" \
  "v_mfma_f32_16x16x32_bf16 v[40:43], v[152:155], v[16:19], v[40:43]\n" \
  "v_mfma_f32_16x16x32_bf16 v[44:47], v[184:187], v[16:19], v[44:47]\n" \
  "v_mfma_f32_16x16x32_bf16 v[48:51], v[216:219], v[16:19], v[48:51]\n" \
  "v_mfma_f32_16x16x32_bf16 v[52:55], v[248:251], v[16:19], v[52:55]\n" \
  "ds_read_b128 v[16:19], v0 offset:6144\n" \
  "s_waitcnt lgkmcnt(3)\n" \
  "v_mfma_f32_16x16x32_bf16 v[32:35], v[92:95], v[20:23], v[32:35]\n" \
  "v_mfma_f32_16x16x32_bf16 v[36:39], v[124:127], v[20:23], v[36:39]\n" \
  "v_mfma_f32_16x16x32_bf16 v[40:43], v[156:159], v[20:23], v[40:43]\n" \
  "v_mfma_f32_16x16x32_bf16 v[44:47], v[188:191], v[20:23], v[44:47]\n" \
  "v_mfma_f32_16x16x32_bf16 v[48:51], v[220:223], v[20:23], v[48:51]\n" \
  "v_mfma_f32_16x16x32_bf16 v[52:55], v[252:255], v[20:23], v[52:55]\n" \
  "ds_read_b128 v[20:23], v0 offset:7168\n" \
  "s_waitcnt lgkmcnt(3)\n" \
  "s_nop 1\n" \
  "v_cvt_pk_bf16_f32 v24, v32, v33\n" \
  "v_cvt_pk_bf16_f32 v25, v34, v35\n" \
  "v_cvt_pk_bf16_f32 v26, v36, v37\n" \
  "v_cvt_pk_bf16_f32 v27, v38, v39\n" \
  "s_nop 0\n" \
  "v_mfma_f32_16x16x32_bf16 v[32:35], v[64:67], v[8:11], 0\n" \
  "v_mfma_f32_16x16x32_bf16 v[36:39], v[96:99], v[8:11], 0\n" \
  "buffer_store_dwordx4 v[24:27], v1, s[40:43], s54 offen offset:0 sc1\n" \
  "v_cvt_pk_bf16_f32 v28, v40, v41\n" \
  "v_cvt_pk_bf16_f32 v29, v42, v43\n" \
  "v_cvt_pk_bf16_f32 v30, v44, v45\n" \
  "v_cvt_pk_bf16_f32 v31, v46, v47\n" \
  "s_nop 0\n" \
  "v_mfma_f32_16x16x32_bf16 v[40:43], v[128:131], v[8:11], 0\n" \
  "v_mfma_f32_16x16x32_bf16 v[44:47], v[160:163], v[8:11], 0\n" \
  "buffer_store_dwordx4 v[28:31], v1, s[40:43], s54 offen offset:128 sc1\n" \
  "v_cvt_pk_bf16_f32 v56, v48, v49\n" \
  "v_cvt_pk_bf16_f32 v57, v50, v51\n" \
  "v_cvt_pk_bf16_f32 v58, v52, v53\n" \
  "v_cvt_pk_bf16_f32 v59, v54, v55\n" \
  "s_nop 0\n" \
  "v_mfma_f32_16x16x32_bf16 v[48:51], v[192:195], v[8:11], 0\n" \
  "v_mfma_f32_16x16x32_bf16 v[52:55], v[224:227], v[8:11], 0\n" \
  "buffer_store_dwordx4 v[56:59], v1, s[40:43], s54 offen offset:256 sc1\n" \
  "ds_read_b128 v[8:11], v0 offset:12288\n" \
  "s_waitcnt lgkmcnt(3)\n" \
  "v_mfma_f32_16x16x32_bf16 v[32:35], v[68:71], v[12:15], v[32:35]\n" \
  "v_mfma_f32_16x16x32_bf16 v[36:39], v[100:103], v[12:15], v[36:39]\n" \
  "v_mfma_f32_16x16x32_bf16 v[40:43], v[132:135], v[12:15], v[40:43]\n" \
  "v_mfma_f32_16x16x32_bf16 v[44:47], v[164:167], v[12:15], v[44:47]\n" \
  "v_mfma_f32_16x16x32_bf16 v[48:51], v[196:199], v[12:15], v[48:51]\n" \
  "v_mfma_f32_16x16x32_bf16 v[52:55], v[228:231], v[12:15], v[52:55]\n" \
  "ds_read_b128 v[12:15], v0 offset:13312\n" \
  "s_waitcnt lgkmcnt(3)\n" \
  "v_mfma_f32_16x16x32_bf16 v[32:35], v[72:75], v[16:19], v[32:35]\n" \
  "v_mfma_f32_16x16x32_bf16 v[36:39], v[104:107], v[16:19], v[36:39]\n" \
  "v_mfma_f32_16x16x32_bf16 v[40:43], v[136:139], v[16:19], v[40:43]\n" \
  "v_mfma_f32_16x16x32_bf16 v[44:47], v[168:171], v[16:19], v[44:47]\n" \
  "v_mfma_f32_16x16x32_bf16 v[48:51], v[200:203], v[16:19], v[48:51]\n" \
  "v_mfma_f32_16x16x32_bf16 v[52:55], v[232:235], v[16:19], v[52:55]\n" \
  "ds_read_b128 v[16:19], v0 offset:14336\n" \
  "s_waitcnt lgkmcnt(3)\n" \
  "v_mfma_f32_16x16x32_bf16 v[32:35], v[76:79], v[20:23], v[32:35]\n" \
  "v_mfma_f32_16x16x32_bf16 v[36:39], v[108:111], v[20:23], v[36:39]\n" \
  "v_mfma_f32_16x16x32_bf16 v[40:43], v[140:143], v[20:23], v[40:43]\n" \
  "v_mfma_f32_16x16x32_bf16 v[44:47], v[172:175], v[20:23], v[44:47]\n" \
  "v_mfma_f32_16x16x32_bf16 v[48:51], v[204:207], v[20:23], v[48:51]\n" \
  "v_mfma_f32_16x16x32_bf16 v[52:55], v[236:239], v[20:23], v[52:55]\n" \
  "ds_read_b128 v[20:23], v0 offset:15360\n" \
  "s_waitcnt lgkmcnt(3)\n" \
  "v_mfma_f32_16x16x32_bf16 v[32:35], v[80:83], v[8:11], v[32:35]\n" \
  "v_mfma_f32_16x16x32_bf16 v[36:39], v[112:115], v[8:11], v[36:39]\n" \
  "v_mfma_f32_16x16x32_bf16 v[40:43], v[144:147], v[8:11], v[40:43]\n" \
  "v_mfma_f32_16x16x32_bf16 v[44:47], v[176:179], v[8:11], v[44:47]\n" \
  "v_mfma_f32_16x16x32_bf16 v[48:51], v[208:211], v[8:11], v[48:51]\n" \
  "v_mfma_f32_16x16x32_bf16 v[52:55], v[240:243], v[8:11], v[52:55]\n" \
  "s_waitcnt lgkmcnt(2)\n" \
  "v_mfma_f32_16x16x32_bf16 v[32:35], v[84:87], v[12:15], v[32:35]\n" \
  "v_mfma_f32_16x16x32_bf16 v[36:39], v[116:119], v[12:15], v[36:39]\n" \
  "v_mfma_f32_16x16x32_bf16 v[40:43], v[148:151], v[12:15], v[40:43]\n" \
  "v_mfma_f32_16x16x32_bf16 v[44:47], v[180:183], v[12:15], v[44:47]\n" \
  "v_mfma_f32_16x16x32_bf16 v[48:51], v[212:215], v[12:15], v[48:51]\n" \
  "v_mfma_f32_16x16x32_bf16 v[52:55], v[244:247], v[12:15], v[52:55]\n" \
  "s_waitcnt lgkmcnt(1)\n" \
  "v_mfma_f32_16x16x32_bf16 v[32:35], v[88:91], v[16:19], v[32:35]\n" \
  "v_mfma_f32_16x16x32_bf16 v[36:39], v[120:123], v[16:19], v[36:39]\n" \
  "v_mfma_f32_16x16x32_bf16 v[40:43], v[152:155], v[16:19], v[40:43]\n" \
  "v_mfma_f32_16x16x32_bf16 v[44:47], v[184:187], v[16:19], v[44:47]\n" \
  "v_mfma_f32_16x16x32_bf16 v[48:51], v[216:219], v[16:19], v[48:51]\n" \
  "v_mfma_f32_16x16x32_bf16 v[52:55], v[248:251], v[16:19], v[52:55]\n" \
  "s_waitcnt lgkmcnt(0)\n" \
  "v_mfma_f32_16x16x32_bf16 v[32:35], v[92:95], v[20:23], v[32:35]\n" \
  "v_mfma_f32_16x16x32_bf16 v[36:39], v[124:127], v[20:23], v[36:39]\n" \
  "v_mfma_f32_16x16x32_bf16 v[40:43], v[156:159], v[20:23], v[40:43]\n" \
  "v_mfma_f32_16x16x32_bf16 v[44:47], v[188:191], v[20:23], v[44:47]\n" \
  "v_mfma_f32_16x16x32_bf16 v[48:51], v[220:223], v[20:23], v[48:51]\n" \
  "v_mfma_f32_16x16x32_bf16 v[52:55], v[252:255], v[20:23], v[52:55]\n" \
  "v_add_u32 v0, s66, v0\n" \
  "s_sub_u32 s66, 0, s66\n" \
  "s_add_u32 s54, s54, 0x80000\n" \
  "s_mov_b32 s62, 0x40000\n" \
  "v_cvt_pk_bf16_f32 v24, v32, v33\n" \
  "v_cvt_pk_bf16_f32 v25, v34, v35\n" \
  "v_cvt_pk_bf16_f32 v26, v36, v37\n" \
  "v_cvt_pk_bf16_f32 v27, v38, v39\n" \
  "buffer_store_dwordx4 v[24:27], v1, s[40:43], s64 offen offset:0 sc1\n" \
  "v_cvt_pk_bf16_f32 v28, v40, v41\n" \
  "v_cvt_pk_bf16_f32 v29, v42, v43\n" \
  "v_cvt_pk_bf16_f32 v30, v44, v45\n" \
  "v_cvt_pk_bf16_f32 v31, v46, v47\n" \
  "buffer_store_dwordx4 v[28:31], v1, s[40:43], s64 offen offset:128 sc1\n" \
  "v_cvt_pk_bf16_f32 v56, v48, v49\n" \
  "v_cvt_pk_bf16_f32 v57, v50, v51\n" \
  "v_cvt_pk_bf16_f32 v58, v52, v53\n" \
  "v_cvt_pk_bf16_f32 v59, v54, v55\n" \
  "buffer_store_dwordx4 v[56:59], v1, s[40:43], s64 offen offset:256 sc1\n" \
  "s_cmp_eq_u32 s70, 0\n" \
  "s_cbranch_scc1 .LCY_ND_%=\n" \
  "global_load_lds_dwordx4 v3, s[48:49] offset:2048 sc1\n" \
  "global_load_lds_dwordx4 v3, s[48:49] offset:3072 sc1\n" \
  "s_add_u32 s48, s48, 0x80000\n" \
  "s_addc_u32 s49, s49, 0\n" \
  "s_mov_b32 m0, s57\n" \
  "s_add_u32 s57, s57, s65\n" \
  "global_load_lds_dwordx4 v2, s[50:51] offset:0 sc1\n" \
  "global_load_lds_dwordx4 v2, s[50:51] offset:1024 sc1\n" \
  "s_sub_u32 s65, 0, s65\n" \
  "s_add_u32 s67, s67, 4096\n" \
  "global_load_lds_dwordx4 v2, s[50:51] offset:2048 sc1\n" \
  "s_sub_u32 s68, s68, 1\n" \
  "s_mov_b32 s69, 1\n" \
  "global_load_lds_dwordx4 v2, s[50:51] offset:3072 sc1\n" \
  "s_add_u32 s50, s50, 0x40000\n" \
  "s_addc_u32 s51, s51, 0\n" \
  "v_mov_b32 v4, 0\n" \
  "buffer_load_dword v4, v4, s[44:47], s67 offen sc1\n" \
  "s_waitcnt vmcnt(7)\n" \
  "s_branch .LCY_WC_%=\n" \
  ".LCY_ND_%=:\n" \
  "v_mov_b32 v4, 0\n" \
  "buffer_load_dword v4, v4, s[44:47], s67 offen sc1\n" \
  "s_waitcnt vmcnt(0)\n" \
  ".LCY_WC_%=:\n" \
  "v_mov_b32 v6, s59\n" \
  "v_mov_b32 v7, 1\n" \
  "s_mov_b64 exec, 1\n" \
  "ds_add_rtn_u32 v6, v6, v7\n" \
  "s_mov_b64 exec, -1\n" \
  "s_xor_b32 s59, s59, 4\n" \
  "s_mov_b32 s61, 0\n" \
  "s_waitcnt lgkmcnt(0)\n" \
  "v_readfirstlane_b32 s63, v6\n" \
  "v_lshlrev_b32 v6, 5, v2\n" \
  "s_and_b32 s63, s63, 7\n" \
  "s_cmp_eq_u32 s63, 7\n" \
  "s_cbranch_scc0 .LCY_W1_%=\n" \
  "s_mov_b64 exec, 0xff\n" \
  "global_store_dword v6, v7, s[52:53] sc1\n" \
  "s_mov_b64 exec, -1\n" \
  "s_mov_b32 s61, 1\n" \
  ".LCY_W1_%=:\n" \
  "s_add_u32 s52, s52, 4096\n" \
  "s_addc_u32 s53, s53, 0\n" \
  "s_sub_u32 s60, s60, 1\n" \
  "s_cmp_eq_u32 s60, 0\n" \
  "s_cbranch_scc1 .LCY_END_%=\n" \
  ".LCY_NEED_%=:\n" \
  "s_cmp_eq_u32 s68, 0\n" \
  "s_cbranch_scc1 .LCY_BAR_%=\n" \
  "v_cmp_eq_u32 vcc, 0, v4\n" \
  "s_cmp_eq_u64 vcc, 0\n" \
  "s_cbranch_scc1 .LCY_GET_%=\n" \
  "s_cmp_eq_u32 s68, 1\n" \
  "s_cbranch_scc1 .LCY_BAR_%=\n" \
  "s_cmp_ge_u32 s67, 0x40000\n" \
  "s_cbranch_scc1 .LCY_BAR_%=\n" \
  "s_sleep 12\n" \
  ".LCY_ASK_%=:\n" \
  "v_mov_b32 v4, 0\n" \
  "buffer_load_dword v4, v4, s[44:47], s67 offen sc1\n" \
  "s_sub_u32 s62, s62, 1\n" \
  "s_cbranch_scc1 .LCY_END_%=\n" \
  "s_waitcnt vmcnt(0)\n" \
  "s_branch .LCY_NEED_%=\n" \
  ".LCY_GET_%=:\n" \
  "v_lshlrev_b32 v6, 16, s55\n" \
  "s_mov_b64 exec, 1\n" \
  "ds_add_u32 v5, v6\n" \
  "s_mov_b64 exec, -1\n" \
  "s_mov_b32 m0, s56\n" \
  "s_add_u32 s71, s56, 4096\n" \
  "global_load_lds_dwordx4 v2, s[48:49] offset:0 sc1\n" \
  "global_load_lds_dwordx4 v2, s[48:49] offset:1024 sc1\n" \
  "global_load_lds_dwordx4 v2, s[48:49] offset:2048 sc1\n" \
  "global_load_lds_dwordx4 v2, s[48:49] offset:3072 sc1\n" \
  "s_mov_b32 m0, s71\n" \
  "s_add_u32 s56, s56, s65\n" \
  "global_load_lds_dwordx4 v3, s[48:49] offset:0 sc1\n" \
  "global_load_lds_dwordx4 v3, s[48:49] offset:1024 sc1\n" \
  "global_load_lds_dwordx4 v3, s[48:49] offset:2048 sc1\n" \
  "global_load_lds_dwordx4 v3, s[48:49] offset:3072 sc1\n" \
  "s_add_u32 s48, s48, 0x80000\n" \
  "s_addc_u32 s49, s49, 0\n" \
  "s_mov_b32 m0, s57\n" \
  "s_add_u32 s57, s57, s65\n" \
  "global_load_lds_dwordx4 v2, s[50:51] offset:0 sc1\n" \
  "global_load_lds_dwordx4 v2, s[50:51] offset:1024 sc1\n" \
  "s_sub_u32 s65, 0, s65\n" \
  "s_add_u32 s67, s67, 4096\n" \
  "global_load_lds_dwordx4 v2, s[50:51] offset:2048 sc1\n" \
  "s_sub_u32 s68, s68, 1\n" \
  "s_mov_b32 s69, 1\n" \
  "global_load_lds_dwordx4 v2, s[50:51] offset:3072 sc1\n" \
  "s_add_u32 s50, s50, 0x40000\n" \
  "s_addc_u32 s51, s51, 0\n" \
  "v_mov_b32 v4, 0\n" \
  "buffer_load_dword v4, v4, s[44:47], s67 offen sc1\n" \
  "s_waitcnt vmcnt(0)\n" \
  "v_mov_b32 v6, s55\n" \
  "s_mov_b64 exec, 1\n" \
  "ds_add_u32 v5, v6\n" \
  "s_mov_b64 exec, -1\n" \
  "s_mov_b32 s69, 0\n" \
  "s_branch .LCY_NEED_%=\n" \
  ".LCY_END_%=:\n" \
  "s_endpgm\n"
template <bool AT>
__device__ __forceinline__ void role_aw_t(const MegaArgs& A, char* smem, const int L, const int a) {
  const int w = __builtin_amdgcn_readfirstlane(threadIdx.x >> 6);
  const int l = threadIdx.x & 63, c = l & 15, g = l >> 4;
  const int qt = w & 1, hh = (w >> 1) & 1, h = 2 * a + hh;
  char* ws = A.ws;
  unsigned* FL = (unsigned*)(ws + OFF_FL);
  const unsigned sb = lds_base(smem);
  if (threadIdx.x < 4) reinterpret_cast<unsigned*>(smem + AW_CNT)[threadIdx.x] = 0u;
  constexpr int NP = AT ? 1 : 3;
  const int cp0 = AT ? w : 3 * w - 8;
  v8u wo[2 * NP][4];
  {
    const int q = (l >> 2) & 3, p = l & 3, f = q | ((g & 1) << 2);
    const unsigned ra = sb + (4 * g + q) * 256 + (p & 1) * 16;
#pragma unroll
    for (int r = 0; r < 2; ++r) {
#pragma unroll
      for (int k = 0; k < 2; ++k) {
        const char* base_ = (const char*)A.Wo + ((size_t)((2 * a + r) * DH + 64 * (w & 1) + 32 * k) * DM + 128 * (w >> 1)) * 2;
#pragma unroll
        for (int i = 0; i < 8; ++i) {
          const int fd = g | ((i & 1) << 2);
          glds16(base_, (unsigned)((4 * i + g) * (DM * 2) + ((c ^ (fd << 1)) << 4)), sb + w * 16384 + k * 8192 + i * 1024);
        }
      }
      SST(4 + 3 * r);
      if (L == 0 && r == 0) {
        ready_wait(A, ready_look(A));
        WAIT_VM(0);
        stats_flag(A, 16 + a);
      }
      WAIT_VM(0);
      RAW_BAR();
      SST(5 + 3 * r);
#pragma unroll
      for (int k2 = 0; k2 < 2; ++k2) {
#pragma unroll
        for (int ct = 0; ct < 2 * NP; ++ct) {
          const int cp = cp0 + (ct >> 1);
          const unsigned a_ = ra + (cp >> 2) * 32768 + k2 * 16384 + ((2 * (cp & 3) + (p >> 1)) ^ f) * 32 + (ct & 1) * 8;
          const v2u r0 = trd(a_), r1 = trd(a_ + 4096), r2 = trd(a_ + 8192), r3 = trd(a_ + 12288);
          wo[ct][2 * r + k2] = v8u{r0.x, r0.y, r1.x, r1.y, r2.x, r2.y, r3.x, r3.y};
          asm volatile("" : "+v"(wo[ct][2 * r + k2]));
        }
      }
      RAW_BAR();
      SST(6 + 3 * r);
    }
  }
  if (L == 0) {
    float* sn = reinterpret_cast<float*>(smem + AW_CNT + 64);
#pragma unroll
    for (int r = 0; r < 2; ++r) {
      float m2 = 0.f;
#pragma unroll
      for (int ct = 0; ct < 2 * NP; ++ct) {
        float t = 0.f;
#pragma unroll
        for (int e = 0; e < 8; ++e) t = dot2sq(wo[ct][2 * r][e], dot2sq(wo[ct][2 * r + 1][e], t));
        m2 = fmaxf(m2, xsum32(xsum16(t)));
      }
      m2 = wave_max_dpp(m2);
      if (l == 0) sn[8 * r + w] = m2;
    }
    RAW_BAR();
    if (w == 0) {
      float m2 = 0.f;
#pragma unroll
      for (int i = 0; i < 8; ++i) m2 = fmaxf(m2, sn[(l & 8) + i]);
      const __amdgpu_buffer_rsrc_t rg = MKRS(ws + OFF_G, 8192);
      const int o = (l & 7) * 1024 + (32 + 2 * a + ((l >> 3) & 1)) * 16 + (l >> 4) * 8192;
      __builtin_amdgcn_raw_buffer_store_b32(__float_as_uint(m2), rg, o + 8, 0, AUX_SC1);
      DRAIN();
      ST64(__builtin_bit_cast(v2u, call_tag(A)), rg, o, 0);
    }
  }
  WSTAMP2();
  const v4u rsW = mk_srd(ws + OFF_PW, SZ_PW);
  const v4u rsF = mk_srd(FL + FL_FQ + (L * 64 * 32 + h) * 32, (64 * 32 - h) * 128);
  const char* p0 = AT ? ws + OFF_MEMK + L * SZ_MEM + h * 2048 : ws + OFF_QK + L * SZ_QK + ((size_t)h * 16 + 8 * qt) * 1024;
  const char* p1 = AT ? (const char*)(FL + FL_FK + L * 32 + (h >> 3) * 64 + (h & 7) * 2) : ws + OFF_VF + L * SZ_O + ((size_t)h * 8 + 4 * qt) * 1024;
  const unsigned* pFW = FL + FL_FW + (L * 64 * 32 + (a >> 2)) * 32 + (a & 3);
  const unsigned tk = sb + AW_TK + hh * 24 * 1024, voff = (c >> 1) * 2048 + (c & 1) * 64 + g * 16;
  const char* dbg = ws + WS_TOTAL + A.vb * 64 * 8;
#define AW_SOPS                                                                                                                 \
  "{s[40:43]}"(rsW), "{s[44:47]}"(rsF), "{s[48:49]}"(p0), "{s[50:51]}"(p1), "{s[52:53]}"(pFW),                                   \
      "{s54}"((L * 64 * 16 + a) * 32768 + cp0 * 128), "{s55}"(1 << (8 * qt)), "{s56}"(tk + qt * 8192), "{s57}"(tk + 16384 + qt * 4096),                  \
      "{s58}"(sb + AW_CNT + 8 + 4 * hh), "{s59}"(sb + AW_CNT), "{s60}"(NSEG), "{s[36:37]}"(dbg)                                  \
      : "memory", "vcc", "scc", "s80"
  if constexpr (AT) {
    const v8u pv = {tk + l * 16, sb + AW_SO + l * 16, (unsigned)(g * 8 + (c & 7)) * 16, (unsigned)(OFF_MEMV - OFF_MEMK) + c * 32 + (g & 1) * 16,
                    voff, (unsigned)l * 16, tk + qt * 4096 + l * 16, sb + AW_SO + w * 4096 + l * 16};
    const unsigned neg = 0xff61b1e6u;
    v8u bt;
#pragma unroll
    for (int i = 0; i < 4; ++i) {
      const unsigned d = 4 * g + i > c ? neg : 0u;
      bt[i] = qt ? 0u : d;
      bt[4 + i] = qt ? d : neg;
    }
    const unsigned e = g >= 2 ? neg : 0u;
    const v4u bm = {e, e, e, e};
    asm volatile("s_mov_b64 exec, -1\ns_waitcnt vmcnt(0) lgkmcnt(0)\ns_nop 4\n" AW_ASM_X
                 ::"{v[192:199]}"(wo[0][0]), "{v[200:207]}"(wo[0][1]), "{v[208:215]}"(wo[0][2]), "{v[216:223]}"(wo[0][3]),
                 "{v[224:231]}"(wo[1][0]), "{v[232:239]}"(wo[1][1]), "{v[240:247]}"(wo[1][2]), "{v[248:255]}"(wo[1][3]),
                 "{v[0:7]}"(pv), "{v[8:15]}"(bt), "{v[16:19]}"(bm), AW_SOPS);
  } else {
    const v4u pv = {sb + AW_SO + l * 16, voff, (unsigned)l * 16, 0u};
    asm volatile("s_mov_b64 exec, -1\ns_waitcnt vmcnt(0) lgkmcnt(0)\ns_nop 4\n" AW_ASM_Y
                 ::"{v[64:71]}"(wo[0][0]), "{v[72:79]}"(wo[0][1]), "{v[80:87]}"(wo[0][2]), "{v[88:95]}"(wo[0][3]),
                 "{v[96:103]}"(wo[1][0]), "{v[104:111]}"(wo[1][1]), "{v[112:119]}"(wo[1][2]), "{v[120:127]}"(wo[1][3]),
                 "{v[128:135]}"(wo[2][0]), "{v[136:143]}"(wo[2][1]), "{v[144:151]}"(wo[2][2]), "{v[152:159]}"(wo[2][3]),
                 "{v[160:167]}"(wo[3][0]), "{v[168:175]}"(wo[3][1]), "{v[176:183]}"(wo[3][2]), "{v[184:191]}"(wo[3][3]),
                 "{v[192:199]}"(wo[4][0]), "{v[200:207]}"(wo[4][1]), "{v[208:215]}"(wo[4][2]), "{v[216:223]}"(wo[4][3]),
                 "{v[224:231]}"(wo[5][0]), "{v[232:239]}"(wo[5][1]), "{v[240:247]}"(wo[5][2]), "{v[248:255]}"(wo[5][3]),
                 "{v[0:3]}"(pv), AW_SOPS);
  }
#undef AW_SOPS
}
__device__ __forceinline__ void role_aw(const MegaArgs& A, char* smem, const int L, const int a) {
  if (__builtin_amdgcn_readfirstlane(threadIdx.x >> 6) < 4) role_aw_t<true>(A, smem, L, a);
  else role_aw_t<false>(A, smem, L, a);
}

constexpr int RG = 2, RW = 2;
__device__ __forceinline__ void role_red(const MegaArgs& A, char* smem, const int L, const int idx) {
  const int w = __builtin_amdgcn_readfirstlane(threadIdx.x >> 6);
  const int l = threadIdx.x & 63;
  const int gr = (w >> 1) & 1, r = idx * 2 + (w & 1);
  char* ws = A.ws;
  unsigned* FL = (unsigned*)(ws + OFF_FL);
  unsigned* TMO = FL + FL_TMO;
  const __amdgpu_buffer_rsrc_t rsW = MKRS(ws + OFF_PW, SZ_PW);
  const __amdgpu_buffer_rsrc_t rsX = MKRS(ws + (L ? OFF_X3 : OFF_XA), SZ_X);
  const __amdgpu_buffer_rsrc_t rsY = L ? MKRS(A.out, SZ_X) : MKRS(ws + OFF_X2, SZ_X);
  volatile unsigned* sw = reinterpret_cast<volatile unsigned*>(smem) + (w >> 1) * 32;
  if (threadIdx.x < 128) reinterpret_cast<volatile unsigned*>(smem)[threadIdx.x] = 0u;
  __syncthreads();
  if ((EXP_OFF & 2) && L == 0 && w < 4) return;
  if ((EXP_OFF & 64) && L == 1 && w < 4) return;
  if ((EXP_OFF & 8) && L == 1 && w >= 4) return;
  unsigned tl = now10();
  if (L == 0) {
    if (w < 4)
      for (int s = gr; s < NSEG; s += RG) ST128((v4u{0u, 0u, 0u, 0u}), rsY, l * 16, (s * SEG + r) * DM * 2);
    else
      __builtin_amdgcn_s_sleep(40);
    DRAIN();
    ready_wait(A, ready_look(A));
    stats_flag(A, idx);
    if (l < 8 && w < 4) set_flag(FL + FL_FZ + (2 * l + (idx >> 3)) * 32 + (idx & 7) * 4 + w);
  }
  if (w >= 4) return;
  for (int s = gr; s < NSEG; s += RG) {
    grp_wait<SLP_R, 4, GATE_R>((w & 1) == 0, sw, s + 1, FL + FL_FW + ((L * 64 + s) * 8 + (blockIdx.x & (NR_W - 1))) * 4 * 32, 16, TMO, tl);
    if (s >= NSEG - 6) tl = now10();
    TS(0, s, gr * RW * 64);
    TS(4, s, gr * RW * 64 + (RW - 1) * 64);
    v4u p[16];
    const v4u xr = LD128(rsX, l * 512, s * 32768 + r * 16);
#pragma unroll
    for (int k = 0; k < 16; ++k)
      p[k] = LD128(rsW, (l >> 2) * 128 + (l & 3) * 16, ((L * 64 + s) * 16 + k) * 32768 + (r >> 1) * 2048 + (r & 1) * 64);
    v4u xo;
#pragma unroll
    for (int e = 0; e < 4; ++e) {
      f32x2_t t[4];
#pragma unroll
      for (int k = 0; k < 4; ++k)
        t[k] = (f32x2_t{lo16(p[4 * k][e]), hi16(p[4 * k][e])} + f32x2_t{lo16(p[4 * k + 1][e]), hi16(p[4 * k + 1][e])}) +
               (f32x2_t{lo16(p[4 * k + 2][e]), hi16(p[4 * k + 2][e])} + f32x2_t{lo16(p[4 * k + 3][e]), hi16(p[4 * k + 3][e])});
      const f32x2_t u = (t[0] + t[1]) + (t[2] + t[3]);
      const unsigned a0 = pk2(u.x, u.y);
      xo[e] = pk2(lo16(xr[e]) + lo16(a0), hi16(xr[e]) + hi16(a0));
      if (L == 0) xo[e] = xo[e] ? xo[e] : 0x8000u;
    }
    ST128(xo, rsY, l * 16, (s * SEG + r) * DM * 2);
    TS(1, s, gr * RW * 64);
    TS(5, s, gr * RW * 64 + (RW - 1) * 64);
    if (L == 0) {
      const unsigned st = row_stats(xo);
      if (l < NR_R) __hip_atomic_store((gu32*)(FL + FL_FR + ((s * 8 + l) * 4 + (r >> 3)) * 32 + (r & 7)), st, RLX_AGENT);
    }
    TS(6, s, gr * RW * 64 + (RW - 1) * 64);
    TS(3, s, gr * RW * 64);
  }
  if (L == 1 && idx == 0 && threadIdx.x == 0) __hip_atomic_store((gu64*)(ws + OFF_EP), call_tag(A), RLX_AGENT);
}

constexpr int CUN = 2144;
__device__ __forceinline__ bf16x8 conv_bfrag(const unsigned* up, const unsigned sh) {
  const unsigned d0 = up[0], d1 = up[1], d2 = up[2], d3 = up[3], d4 = up[4];
  const v4u r = {__builtin_amdgcn_alignbit(d1, d0, sh), __builtin_amdgcn_alignbit(d2, d1, sh),
                 __builtin_amdgcn_alignbit(d3, d2, sh), __builtin_amdgcn_alignbit(d4, d3, sh)};
  return BF8(r);
}
__device__ __forceinline__ v2u part_stats(const v4u o) {
  float v[8];
#pragma unroll
  for (int e = 0; e < 4; ++e) { v[2 * e] = lo16(o[e]); v[2 * e + 1] = hi16(o[e]); }
  const float s = ((v[0] + v[1]) + (v[2] + v[3])) + ((v[4] + v[5]) + (v[6] + v[7])), m = s * 0.125f;
  float q0 = 0.f, q1 = 0.f;
#pragma unroll
  for (int e = 0; e < 4; ++e) {
    const float a = v[e] - m, b = v[4 + e] - m;
    q0 += a * a;
    q1 += b * b;
  }
  return v2u{__float_as_uint(s), __float_as_uint(q0 + q1)};
}
__device__ __forceinline__ void role_conv(const MegaArgs& A, char* smem, const int cv) {
  u16* Kr = reinterpret_cast<u16*>(smem);
  u16* Uc = Kr + 8 * 2 * CKN;
  u16* Xr = Uc + 8 * CUN;
  u16* To = Uc + 8 * 2 * CUN;
  u16* Su = To + 32 * 8;
  u16* Sx = Su + 32 * 8;
  const int tid = threadIdx.x;
  const int w = __builtin_amdgcn_readfirstlane(tid >> 6);
  const int l = tid & 63, c = l & 15, g = l >> 4;
  const int d = 8 * cv + w;
  char* ws = A.ws;
  unsigned* FL = (unsigned*)(ws + OFF_FL);
  unsigned* TMO = FL + FL_TMO;
  u16* kr = Kr + w * 2 * CKN;
  float* prm = reinterpret_cast<float*>(kr);
  v4u tx[4];
  {
    const unsigned pa = *reinterpret_cast<const unsigned*>(A.logAre + d * NST + 2 * l);
    const unsigned pb = *reinterpret_cast<const unsigned*>(A.Aim + d * NST + 2 * l);
    const unsigned pc = *reinterpret_cast<const unsigned*>(A.Cre + d * NST + 2 * l);
    const unsigned pd = *reinterpret_cast<const unsigned*>(A.Cim + d * NST + 2 * l);
    const u16 pe = A.logdt[d];
    if (w < 2) tx[0] = *reinterpret_cast<const v4u*>(A.x0 + (size_t)tid * DM + 8 * cv);
    tag_early(A);
    const float dt = bfr(expf(bf2f(pe)));
    if (l < 16) reinterpret_cast<unsigned*>(Uc + w * CUN)[l] = 0u;
    if (l < 32) reinterpret_cast<unsigned*>(Uc + w * CUN + 32 + L_SEQ)[l] = 0u;
    if (l < 32) reinterpret_cast<unsigned*>(Uc + w * CUN + 32 + 128)[l] = 0u;
    if (l < 50) {
      reinterpret_cast<v4u*>(kr)[l < 48 ? 228 + l : 146 + l] = v4u{0u, 0u, 0u, 0u};
      reinterpret_cast<v4u*>(kr + CKN)[l < 48 ? 228 + l : 146 + l] = v4u{0u, 0u, 0u, 0u};
    }
#pragma unroll
    for (int e = 0; e < 2; ++e) {
      const float ar = -bfr(expf(e ? hi16(pa) : lo16(pa))) * dt, ai = (e ? hi16(pb) : lo16(pb)) * dt;
      const float ex = expf(ar);
      float sn, cs;
      sincosf(ai, &sn, &cs);
      prm[2 * l + e] = ar;
      prm[NST + 2 * l + e] = ai;
      prm[2 * NST + 2 * l + e] = (e ? hi16(pc) : lo16(pc)) * dt;
      prm[3 * NST + 2 * l + e] = (e ? hi16(pd) : lo16(pd)) * dt;
      prm[4 * NST + 2 * l + e] = ex * cs;
      prm[5 * NST + 2 * l + e] = ex * sn;
    }
  }
  const float dsk = bf2f(A.Dskip[d]);
  const u16* ka = kr + (c >> 3) * CKN + 2080 - 16 * (c & 7) + 8 * g;
  const unsigned ush = (c & 1) * 16;
#define KFRAG(D0) (*reinterpret_cast<const bf16x8*>(ka - (D0)))
  f32x4 acc[16], far[9];
  v4u xf[3];
  const unsigned* ub1 = reinterpret_cast<const unsigned*>(Uc + w * CUN + (c & ~1) + 8 * g + 32);
  const __amdgpu_buffer_rsrc_t rz1 = MKRS(ws + OFF_XA, SZ_X), rps = MKRS(ws + OFF_H, SZ_X);
  const int t0 = 16 * (4 * (g & 1) + (g >> 1)) + c;
  {
    const u16* uc = Uc + w * CUN;
    const unsigned* ub = reinterpret_cast<const unsigned*>(uc + (c & ~1) + 8 * g + 32);
    const __amdgpu_buffer_rsrc_t rz = MKRS(ws + OFF_XA, SZ_X);
    const __amdgpu_buffer_rsrc_t rst = MKRS(ws + OFF_GT, L_SEQ * 4);
#define TILE_OUT(T)                                                                            \
  {                                                                                            \
    const float u0 = bf2f(uc[32 + 128 * (T) + t0]), u1 = bf2f(uc[64 + 128 * (T) + t0]);        \
    const float a0 = bfr(bfr(y0) + bfr(u0 * dsk)), a1 = bfr(bfr(y1) + bfr(u1 * dsk));          \
    u16* xp = Xr + (128 * (T) + t0) * 8 + w;                                                   \
    xp[0] = f2bf(bf2f(xp[0]) + a0);                                                            \
    xp[256] = f2bf(bf2f(xp[256]) + a1);                                                        \
  }
#define CHUNK_ST(ck)                                                                           \
  {                                                                                            \
    const v4u o = *reinterpret_cast<const v4u*>(Xr + (64 * (ck) + l) * 8);                     \
    ST128(o, rz, (l >> 5) * 32768 + (l & 31) * 16, (2 * (ck) * 64 + cv) * 512);                \
    ST64(part_stats(o), rps, (l >> 5) * 16384 + (l & 31) * 8, (2 * (ck) * 64 + cv) * 256);     \
  }
#define U_ROW(row)                                                                             \
  {                                                                                            \
    const unsigned st_ = __builtin_amdgcn_raw_buffer_load_b32(rst, (row) * 4, 0, AUX_SC1);     \
    const v4u xr_ = *reinterpret_cast<const v4u*>(Xr + (row) * 8);                             \
    const float mb_ = lo16(st_), ri_ = 1.0f / hi16(st_);                                       \
    _Pragma("unroll") for (int e = 0; e < 4; ++e) {                                            \
      const unsigned t_ = pk2(lo16(xr_[e]) - mb_, hi16(xr_[e]) - mb_);                         \
      const unsigned q_ = pk2(lo16(t_) * ri_, hi16(t_) * ri_);                                 \
      Uc[(2 * e) * CUN + 32 + (row)] = (u16)q_;                                                \
      Uc[(2 * e + 1) * CUN + 32 + (row)] = (u16)(q_ >> 16);                                    \
    }                                                                                          \
  }
    const unsigned* FS = FL + FL_FS + (blockIdx.x & 7) * 32 * 32;
    const int gi = 4 * (g * 8 + (l & 7));
    const int tl = 32 * ((l >> 3) & 1) + 8 * g + (l & 7);
    unsigned khl = 0u;
#pragma unroll 1
    for (int ps = 0; ps < 4; ++ps) {
      float v[32];
      {
        const f32x4 ar = *reinterpret_cast<const f32x4*>(prm + gi), ai = *reinterpret_cast<const f32x4*>(prm + NST + gi);
        const f32x4 cr = *reinterpret_cast<const f32x4*>(prm + 2 * NST + gi), ci = *reinterpret_cast<const f32x4*>(prm + 3 * NST + gi);
        const f32x4 wr = *reinterpret_cast<const f32x4*>(prm + 4 * NST + gi), wi = *reinterpret_cast<const f32x4*>(prm + 5 * NST + gi);
        const float p0 = (float)(64 * ps + 32 * ((l >> 3) & 1));
        float yr0[4], yi0[4];
#pragma unroll
        for (int k = 0; k < 4; ++k) {
          const float e = __expf(ar[k] * p0);
          float sn, cs;
          __sincosf(ai[k] * p0, &sn, &cs);
          const float zr = e * cs, zi = e * sn;
          yr0[k] = cr[k] * zr - ci[k] * zi;
          yi0[k] = cr[k] * zi + ci[k] * zr;
        }
        f32x2_t yra = {yr0[0], yr0[1]}, yia = {yi0[0], yi0[1]}, wra = {wr[0], wr[1]}, wia = {wi[0], wi[1]};
        f32x2_t yrb = {yr0[2], yr0[3]}, yib = {yi0[2], yi0[3]}, wrb = {wr[2], wr[3]}, wib = {wi[2], wi[3]};
#pragma unroll
        for (int i = 0; i < 32; ++i) {
          const f32x2_t sm2 = yra + yrb;
          v[i] = sm2.x + sm2.y;
          const f32x2_t ta = yra * wra - yia * wia, tb = yrb * wrb - yib * wib;
          yia = yra * wia + yia * wra;
          yib = yrb * wib + yib * wrb;
          yra = ta;
          yrb = tb;
        }
      }
#pragma unroll
      for (int i = 0; i < 16; ++i) v[i] = swap_add32(v[i], v[i + 16]);
#pragma unroll
      for (int i = 0; i < 8; ++i) {
        v[i] = swap_add16(v[i], v[i + 8]);
        v[i] += dppf<0xB1>(0.f, v[i]);
        v[i] += dppf<0x4E>(0.f, v[i]);
        v[i] += dppf<0x141>(0.f, v[i]);
      }
      float s0 = v[0], s1 = v[1], s2 = v[2], s3 = v[3], s4 = v[4], s5 = v[5], s6 = v[6], s7 = v[7];
      asm volatile("" : "+v"(s0), "+v"(s1), "+v"(s2), "+v"(s3), "+v"(s4), "+v"(s5), "+v"(s6), "+v"(s7));
      s0 = (l & 4) ? s4 : s0; s1 = (l & 4) ? s5 : s1; s2 = (l & 4) ? s6 : s2; s3 = (l & 4) ? s7 : s3;
      s0 = (l & 2) ? s2 : s0; s1 = (l & 2) ? s3 : s1;
      const float kv = 2.0f * ((l & 1) ? s1 : s0);
      khl = pk2(kv, 0.f);
      khl = pk2(kv, kv - lo16(khl));
      kr[2080 - 64 * ps - tl] = (u16)khl;
      kr[CKN + 2080 - 64 * ps - tl] = (u16)(khl >> 16);
      SST(ps);
      if (ps < 2) {
        if (ps == 0) {
          if (w < 2) {
            reinterpret_cast<v4u*>(Xr)[tid] = tx[0];
            const u64 tag = call_tag(A);
            const gu64* f0 = (gu64*)(ws + OFF_F0) + ((blockIdx.x & 7) * 8 + 4 * w + ((l >> 3) & 3)) * 16 + (l & 7);
            for (unsigned spins = 0; !__all(__hip_atomic_load(f0, RLX_AGENT) == tag) && spins < SPIN_CAP; ++spins) __builtin_amdgcn_s_sleep(2);
            asm volatile("" ::: "memory");
            U_ROW(tid);
          }
          __syncthreads();
        }
        f32x4 a0t = {};
#pragma unroll
        for (int b = -1; b < 4; ++b) a0t = MFMA16(KFRAG(-32 * b), conv_bfrag(ub + 16 * b, ush), a0t);
        const float y0 = swap_add32(a0t[0], a0t[1]), y1 = swap_add32(a0t[2], a0t[3]);
        if ((g & 1) == ps) TILE_OUT(0);
        __syncthreads();
        if (w == ps) {
          const u64 rv = ready_look(A);
          CHUNK_ST(w);
          DRAIN();
          ready_wait(A, rv);
          if (l < 16) set_flag(FL + FL_FC + (((2 * w + (l >> 3)) * 8 + (l & 7)) * 8 + (cv >> 3)) * 32 + (cv & 7));
        }
        SST(4 + ps);
      }
      if (ps == 2) {
        if (w >= 2) tx[0] = *reinterpret_cast<const v4u*>(A.x0 + (size_t)tid * DM + 8 * cv);
#pragma unroll
        for (int i = 1; i < 4; ++i) tx[i] = *reinterpret_cast<const v4u*>(A.x0 + (size_t)(tid + 512 * i) * DM + 8 * cv);
      }
    }
    if (w >= 2) reinterpret_cast<v4u*>(Xr)[tid] = tx[0];
#pragma unroll
    for (int i = 1; i < 4; ++i) reinterpret_cast<v4u*>(Xr)[tid + 512 * i] = tx[i];
    {
      unsigned spins = 0;
      while (!__all(__hip_atomic_load((gu32*)FS + (w + 8 * ((l >> 3) & 3)) * 32 + (l & 7), RLX_AGENT) != 0u)) {
        if (spin_timeout(++spins, TMO)) break;
        __builtin_amdgcn_s_sleep(4);
      }
      asm volatile("" ::: "memory");
    }
    if (w >= 2) U_ROW(tid);
#pragma unroll
    for (int i = 1; i < 4; ++i) U_ROW(tid + 512 * i);
    SST(6);
    const __amdgpu_buffer_rsrc_t rk = MKRS(ws + OFF_KF + (size_t)d * 2 * CKN * 2, 2 * CKN * 2);
#define TAPS(kc, U0, N, HOOK)                                                                  \
  {                                                                                            \
    (void)wait_n<8, 1>(FL + FL_KG + (cv * 3 + (kc)) * 32 + (w >> 1), 1 + (kc), TMO);            \
    HOOK                                                                                       \
    v4u t[2 * (((N) + 63) / 64)];                                                              \
    _Pragma("unroll") for (int i = 0; i < ((N) + 63) / 64; ++i) {                              \
      t[2 * i] = LD128(rk, ((U0) + 64 * i + l) * 16, 0);                                       \
      t[2 * i + 1] = LD128(rk, ((U0) + 64 * i + l) * 16, CKN * 2);                             \
    }                                                                                          \
    _Pragma("unroll") for (int i = 0; i < ((N) + 63) / 64; ++i)                                \
      if (64 * i + l < (N)) {                                                                  \
        reinterpret_cast<v4u*>(kr)[(U0) + 64 * i + l] = t[2 * i];                              \
        reinterpret_cast<v4u*>(kr + CKN)[(U0) + 64 * i + l] = t[2 * i + 1];                    \
      }                                                                                        \
  }
    TAPS(0, 196, 33, );
    SST(7);
    kr[2080 - 192 - tl] = (u16)khl;
    kr[CKN + 2080 - 192 - tl] = (u16)(khl >> 16);
    __syncthreads();
#define BULK(TA, TB, HOOK)                                                                      \
  {                                                                                            \
    const bf16x8 bm = conv_bfrag(ub - 16, ush);                                                \
    _Pragma("unroll") for (int T = (TA); T < (TB); ++T) acc[T] = MFMA16(KFRAG(128 * T + 32), bm, (f32x4{})); \
    _Pragma("unroll 1") for (int r = 0; r < 4; ++r) {                                          \
      bf16x8 bf[TB];                                                                           \
      _Pragma("unroll") for (int j = 0; j < (TB); ++j) bf[j] = conv_bfrag(ub + 16 * r + 64 * j, ush); \
      _Pragma("unroll") for (int e = 0; e < (TB); ++e) {                                       \
        const bf16x8 a = KFRAG(128 * e - 32 * r);                                              \
        _Pragma("unroll") for (int T = (e > (TA) ? e : (TA)); T < (TB); ++T) acc[T] = MFMA16(a, bf[T - e], acc[T]); \
      }                                                                                        \
      if (r == 0) { HOOK }                                                                     \
    }                                                                                          \
  }
    BULK(1, 4, );
#pragma unroll
    for (int T = 1; T < 4; ++T) {
      const float y0 = swap_add32(acc[T][0], acc[T][1]), y1 = swap_add32(acc[T][2], acc[T][3]);
      TILE_OUT(T);
    }
    __syncthreads();
    if (w < 6) CHUNK_ST(2 + w);
    SST(8);
    if (w < 6) {
      DRAIN();
      if (l < 16) set_flag(FL + FL_FC + (((4 + 2 * w + (l >> 3)) * 8 + (l & 7)) * 8 + (cv >> 3)) * 32 + (cv & 7));
    }
    TAPS(1, 0, 196, );
#undef TAPS
    BULK(4, 7, if (w == 1) (void)(wait_n<SLP_V, 32>(FL + FL_FZ + (blockIdx.x & 7) * 64, 64, TMO)););
#pragma unroll
    for (int T = 4; T < 7; ++T) {
      const float y0 = swap_add32(acc[T][0], acc[T][1]), y1 = swap_add32(acc[T][2], acc[T][3]);
      TILE_OUT(T);
    }
    __syncthreads();
    if (w >= 2) {
      CHUNK_ST(6 + w);
#pragma unroll
      for (int i = 0; i < 3; ++i) xf[i] = *reinterpret_cast<const v4u*>(Xr + (64 * (8 + 3 * w + i) + l) * 8);
    }
    SST(10);
    {
      const bf16x8 bm = conv_bfrag(ub - 16, ush);
#pragma unroll
      for (int T = 7; T < 16; ++T) far[T - 7] = MFMA16(KFRAG(128 * T + 32), bm, (f32x4{}));
    }
#define FAR_HALF(r, J0, J1)                                                                     \
  {                                                                                            \
    bf16x8 bf[16], af[16];                                                                     \
    _Pragma("unroll") for (int j = (J0); j < (J1); ++j) bf[j] = conv_bfrag(ub1 + 16 * (r) + 64 * j, ush); \
    _Pragma("unroll") for (int e = (8 - (J1) > 0 ? 8 - (J1) : 0); e < 16 - (J0); ++e) af[e] = KFRAG(128 * e - 32 * (r)); \
    __builtin_amdgcn_sched_barrier(0);                                                         \
    _Pragma("unroll") for (int j = (J0); j < (J1); ++j)                                        \
      _Pragma("unroll") for (int T = (j > 7 ? j : 7); T < 16; ++T) far[T - 7] = MFMA16(af[T - j], bf[j], far[T - 7]); \
  }
#define FAR_PIECE(p)                                                                            \
  {                                                                                            \
    if ((p) < 8) {                                                                             \
      const int r_ = 2 + ((p) >> 2);                                                           \
      if (((p) & 3) == 0) FAR_HALF(r_, 0, 3) else if (((p) & 3) == 1) FAR_HALF(r_, 3, 6)       \
      else if (((p) & 3) == 2) FAR_HALF(r_, 6, 9) else FAR_HALF(r_, 9, 16)                     \
    } else if ((p) == 8) {                                                                     \
                     \
      u16* ucw = Uc + w * CUN + 32 + t0;                                                       \
      _Pragma("unroll") for (int T = 7; T < 16; ++T) {                                         \
        const float y0 = swap_add32(far[T - 7][0], far[T - 7][1]), y1 = swap_add32(far[T - 7][2], far[T - 7][3]); \
        const float u0 = bf2f(ucw[128 * T]), u1 = bf2f(ucw[128 * T + 32]);                     \
        const unsigned yy = pk2(y0, y1), uu = pk2(u0 * dsk, u1 * dsk);                         \
        const unsigned aa = pk2(lo16(yy) + lo16(uu), hi16(yy) + hi16(uu));                     \
        ucw[128 * T] = (u16)aa;                                                                \
        ucw[128 * T + 32] = (u16)(aa >> 16);                                                   \
      }                                                                                        \
    } else if (w >= 2) {                                                                       \
      int l = tid & 63;                                                                        \
      asm volatile("" : "+v"(l), "+v"(xf[0]), "+v"(xf[1]), "+v"(xf[2]));                       \
      if ((p) == 9) {                                                                          \
        _Pragma("unroll") for (int i = 0; i < 3; ++i) {                                        \
          const int ck = 8 + 3 * w + i;                                                        \
          const u16* ur = Uc + 32 + 64 * ck + l;                                               \
          v4u o;                                                                               \
          _Pragma("unroll") for (int e = 0; e < 4; ++e)                                        \
            o[e] = pk2(lo16(xf[i][e]) + bf2f(ur[(2 * e) * CUN]), hi16(xf[i][e]) + bf2f(ur[(2 * e + 1) * CUN])); \
          ST128(o, rz1, (l >> 5) * 32768 + (l & 31) * 16, (2 * ck * 64 + cv) * 512);           \
          ST64(part_stats(o), rps, (l >> 5) * 16384 + (l & 31) * 8, (2 * ck * 64 + cv) * 256); \
        }                                                                                      \
      } else {                                                                                 \
        DRAIN();                                                                               \
        if (l < 48) set_flag(FL + FL_FC + (((16 + 6 * w + (l >> 3)) * 8 + (l & 7)) * 8 + (cv >> 3)) * 32 + (cv & 7)); \
      }                                                                                        \
    }                                                                                          \
  }
    if (w >= 2) {
      DRAIN();
      if (l < 16) set_flag(FL + FL_FC + (((12 + 2 * w + (l >> 3)) * 8 + (l & 7)) * 8 + (cv >> 3)) * 32 + (cv & 7));
    }
#undef BULK
    SST(9);
#undef TILE_OUT
#undef CHUNK_ST
#undef U_ROW
  }
  if (EXP_OFF & 4) {
    for (int p = -8; p < 11; ++p) {
      __syncthreads();
      FAR_PIECE(p);
    }
    return;
  }
  u16* uc = Uc + (8 + w) * CUN;
  const unsigned* ub = reinterpret_cast<const unsigned*>(uc + (c & ~1) + 8 * g + 32);
#pragma unroll
  for (int j = 0; j < 16; ++j) acc[j] = f32x4{};
  const __amdgpu_buffer_rsrc_t rx = MKRS(ws + OFF_X2, SZ_X), rz = MKRS(ws + OFF_X3, SZ_X);
  const __amdgpu_buffer_rsrc_t rf = MKRS(FL + FL_FR, 64 * 32 * 128);
  unsigned st = 0u;
  v2u xx = {0u, 0u};
#define X2_LOAD(ss, far)                                                                                                      \
  {                                                                                                                      \
    xx = LD64(rx, (l >> 1) * (DM * 2) + (l & 1) * 8, (far) ? (int)SZ_X : ((ss) * SEG * DM + 8 * cv) * 2);                 \
    st = __builtin_amdgcn_raw_buffer_load_b32(rf, (l >> 4) * 128 + ((l >> 1) & 7) * 4, ((ss) * 8 + (blockIdx.x & (NR_R - 1))) * 512, AUX_SC1); \
  }
  unsigned tl = now10();
  for (int s = -CVPRE; s < NSEG; ++s) {
   if (s >= 0) {
    if (w == 1) {
      for (unsigned spins = 0; !__all(umin3(xx.x, xx.y, st) != 0u); ++spins) {
        if (spin_timeout(spins, TMO)) break;
        if (GATE_V && spins == 0 && !__any(st != 0u)) gate(tl);
        if (spins && !__all(st != 0u)) __builtin_amdgcn_s_sleep(SLP_V);
        X2_LOAD(s, __builtin_popcountll(__ballot(st != 0u)) < CVTHR);
      }
      tl = now10() + GATE_V;
      TS(0, s, 64);
      const float mb = lo16(st), ri = 1.0f / hi16(st);
      const unsigned t0 = pk2(lo16(xx.x) - mb, hi16(xx.x) - mb), t1 = pk2(lo16(xx.y) - mb, hi16(xx.y) - mb);
      *reinterpret_cast<v2u*>(Su + l * 4) = v2u{pk2(lo16(t0) * ri, hi16(t0) * ri), pk2(lo16(t1) * ri, hi16(t1) * ri)};
      *reinterpret_cast<v2u*>(Sx + l * 4) = xx;
    }
    const int q = s & 3;
    const u16* kq = ka + 32 * q;
    const bf16x8 a0 = *reinterpret_cast<const bf16x8*>(kq - 32), a1 = *reinterpret_cast<const bf16x8*>(kq);
    __syncthreads();
    TS(1, s, 0);
    if (s == 0)
      for (int x = l; x < CUN / 2; x += 64) reinterpret_cast<unsigned*>(uc)[x] = 0u;
    if (l < 32) uc[32 + 32 * s + l] = Su[l * 8 + w];
    asm volatile("s_waitcnt lgkmcnt(0)" ::: "memory");
    const bf16x8 bp = conv_bfrag(ub + 16 * (s - 1), ush);
    acc[0] = MFMA16(a0, bp, (q == 0 ? acc[1] : acc[0]));
    const f32x4 tmp = MFMA16(a1, conv_bfrag(ub + 16 * s, ush), acc[0]);
    float y0 = (q & 1) ? tmp[2] : tmp[0], y1 = (q & 1) ? tmp[3] : tmp[1];
    y0 = xsum32(y0);
    y1 = xsum32(y1);
    if (g == (q >> 1)) {
      const float u0 = bf2f(Su[c * 8 + w]), u1 = bf2f(Su[(16 + c) * 8 + w]);
      const float a0 = bfr(bfr(y0) + bfr(u0 * dsk)), a1 = bfr(bfr(y1) + bfr(u1 * dsk));
      To[c * 8 + w] = f2bf(bf2f(Sx[c * 8 + w]) + a0);
      To[(16 + c) * 8 + w] = f2bf(bf2f(Sx[(16 + c) * 8 + w]) + a1);
    }
    __syncthreads();
    if (w == 0 && l < 32) {
      const v4u o = *reinterpret_cast<const v4u*>(To + l * 8);
      ST128(o, rz, l * 16, (s * 64 + cv) * 512);
      ST64(part_stats(o), rps, l * 8, ((64 + s) * 64 + cv) * 256);
    }
    if (w == 1) X2_LOAD(s + 1, CVTHR > 0);
    const int left = 15 - (s >> 2);
#define PUSH(J0, SH)                                                                                              \
  if (left >= (J0)) {                                                                                             \
    _Pragma("unroll") for (int j = (J0); j < (J0) + 4 && j < 16; ++j)                                             \
      acc[j] = MFMA16(*reinterpret_cast<const bf16x8*>(kq - 32 - 128 * j), bp, (j + (SH) < 16 ? acc[(j + (SH)) & 15] : f32x4{})); \
  }
    TS(2, s, 0);
    if (w == 0) {
      DRAIN();
      if (l < NR_C) set_flag(FL + FL_FC + (((64 + s) * 8 + l) * 8 + (cv >> 3)) * 32 + (cv & 7));
    }
    TS(3, s, 0);
    if (q == 0) { PUSH(1, 1) PUSH(5, 1) PUSH(9, 1) PUSH(13, 1) }
    else { PUSH(1, 0) PUSH(5, 0) PUSH(9, 0) PUSH(13, 0) }
#undef PUSH
    MSTAMP();
   }
    if (s < 19 - CVPRE) FAR_PIECE(s + CVPRE - 8);
  }
#undef KFRAG
#undef X2_LOAD
#undef FAR_PIECE
#undef FAR_HALF
}

__device__ __forceinline__ v4u ln_row_packed(const v4u xr) {
  float v[8];
#pragma unroll
  for (int e = 0; e < 4; ++e) { v[2 * e] = lo16(xr[e]); v[2 * e + 1] = hi16(xr[e]); }
  float s = ((v[0] + v[1]) + (v[2] + v[3])) + ((v[4] + v[5]) + (v[6] + v[7]));
  s = wave_sum(s);
  const float mean = s * (1.0f / DM);
  float q = 0.f;
#pragma unroll
  for (int j = 0; j < 8; ++j) { float t = v[j] - mean; q += t * t; }
  q = wave_sum(q);
  const float var = q * (1.0f / DM);
  const float mb = bfr(mean);
  const float sd = bfr(sqrtf(bfr(bfr(var) + 1e-5f)));
  const float ri = 1.0f / sd;
  v4u o;
#pragma unroll
  for (int e = 0; e < 4; ++e) {
    const unsigned t = pk2(v[2 * e] - mb, v[2 * e + 1] - mb);
    const float x0 = lo16(t), x1 = hi16(t);
    float q0 = x0 * ri, q1 = x1 * ri;
    q0 = __builtin_fmaf(__builtin_fmaf(-q0, sd, x0), ri, q0);
    q1 = __builtin_fmaf(__builtin_fmaf(-q1, sd, x1), ri, q1);
    o[e] = pk2(q0, q1);
  }
  return o;
}

__device__ __forceinline__ float bfr1(float x) { return lo16(pk2(x, x)); }
__device__ __forceinline__ void ln4_packed(v4u (&x)[4]) {
  f32x2_t v[16];
#pragma unroll
  for (int e = 0; e < 16; ++e) v[e] = f32x2_t{lo16(x[e >> 2][e & 3]), hi16(x[e >> 2][e & 3])};
  const f32x2_t t = (((v[0] + v[1]) + (v[2] + v[3])) + ((v[4] + v[5]) + (v[6] + v[7]))) +
                    (((v[8] + v[9]) + (v[10] + v[11])) + ((v[12] + v[13]) + (v[14] + v[15])));
  const float mean = red_mod4(t.x + t.y) * (1.0f / DM);
  const float mb = bfr1(mean);
  f32x2_t q0 = {0.f, 0.f}, q1 = {0.f, 0.f};
#pragma unroll
  for (int e = 0; e < 16; e += 2) {
    v[e] -= mb;
    v[e + 1] -= mb;
    q0 += v[e] * v[e];
    q1 += v[e + 1] * v[e + 1];
  }
  q0 += q1;
  const float dm = mean - mb;
  const float var = fmaxf(red_mod4(q0.x + q0.y) * (1.0f / DM) - dm * dm, 0.f);
  const float sd = bfr1(sqrtf(bfr1(bfr1(var) + 1e-5f)));
  const float ri = 1.0f / sd;
#pragma unroll
  for (int e = 0; e < 16; ++e) {
    const unsigned t2 = pk2(v[e].x, v[e].y);
    const f32x2_t q = f32x2_t{lo16(t2), hi16(t2)} * ri;
    x[e >> 2][e & 3] = pk2(q.x, q.y);
  }
}

#define Q_ASM \
  "s_mov_b32 s79, 0\n" \
  "s_mov_b32 s81, 0\n" \
  "s_mov_b32 s83, 0x8000\n" \
  "s_mov_b32 s76, 0\n" \
  "s_mov_b32 s77, -1\n" \
  "s_branch .LCQ_POLL_%=\n" \
  ".LCQ_TOP_%=:\n" \
  "s_waitcnt lgkmcnt(0)\n" \
  "s_barrier\n" \
  "s_sub_i32 s83, 0, s83\n" \
  "v_add_u32 v0, s83, v0\n" \
  "ds_read_b128 v[18:21], v0 offset:0\n" \
  "ds_read_b128 v[22:25], v0 offset:2048\n" \
  "ds_read_b128 v[26:29], v0 offset:4096\n" \
  "ds_read2_b32 v[6:7], v3 offset0:16 offset1:48\n" \
  "ds_read_b128 v[10:13], v4 offset:256\n" \
  "ds_read_b128 v[14:17], v4 offset:320\n" \
  "ds_read_b32 v8, v3 offset:384\n" \
  "s_add_u32 s71, s71, 0x2000\n" \
  "s_mov_b32 s78, s80\n" \
  "s_xor_b32 s74, s74, 2048\n" \
  "s_xor_b32 s75, s75, 2048\n" \
  "s_setprio 3\n" \
  "ds_read_b128 v[30:33], v0 offset:6144\n" \
  "s_waitcnt lgkmcnt(7)\n" \
  "v_mfma_f32_16x16x32_bf16 v[34:37], v[64:67], v[18:21], 0\n" \
  "v_mfma_f32_16x16x32_bf16 v[38:41], v[128:131], v[18:21], 0\n" \
  "v_mfma_f32_16x16x32_bf16 v[42:45], v[18:21], v[192:195], 0\n" \
  "s_cmp_eq_u32 s78, 1\n" \
  "s_cbranch_scc0 .LCQ_X1_%=\n" \
  "s_mov_b32 m0, s73\n" \
  "s_nop 0\n" \
  "global_load_lds_dwordx4 v2, s[62:63] offset:0 sc1\n" \
  "global_load_lds_dwordx4 v2, s[62:63] offset:1024 sc1\n" \
  "s_add_u32 s62, s62, 0x4000\n" \
  "s_addc_u32 s63, s63, 0\n" \
  ".LCQ_X1_%=:\n" \
  "ds_read_b128 v[18:21], v0 offset:8192\n" \
  "s_waitcnt lgkmcnt(7)\n" \
  "v_mfma_f32_16x16x32_bf16 v[34:37], v[68:71], v[22:25], v[34:37]\n" \
  "s_waitcnt lgkmcnt(4)\n" \
  "v_fma_f32 v48, -v6, v10, v48\n" \
  "v_fma_f32 v49, -v6, v11, v49\n" \
  "v_mfma_f32_16x16x32_bf16 v[38:41], v[132:135], v[22:25], v[38:41]\n" \
  "v_fma_f32 v50, -v6, v12, v50\n" \
  "v_fma_f32 v51, -v6, v13, v51\n" \
  "v_mfma_f32_16x16x32_bf16 v[42:45], v[22:25], v[196:199], v[42:45]\n" \
  "ds_read_b128 v[10:13], v4 offset:64\n" \
  "s_waitcnt lgkmcnt(4)\n" \
  "v_fma_f32 v54, -v6, v14, v54\n" \
  "v_fma_f32 v55, -v6, v15, v55\n" \
  "s_cmp_eq_u32 s78, 1\n" \
  "s_cbranch_scc0 .LCQ_X2_%=\n" \
  "s_mov_b32 m0, s68\n" \
  "s_xor_b32 s68, s68, 0x8000\n" \
  "global_load_lds_dwordx4 v2, s[60:61] offset:0 sc1\n" \
  "global_load_lds_dwordx4 v2, s[60:61] offset:1024 sc1\n" \
  ".LCQ_X2_%=:\n" \
  "ds_read_b128 v[22:25], v0 offset:10240\n" \
  "v_mfma_f32_16x16x32_bf16 v[34:37], v[72:75], v[26:29], v[34:37]\n" \
  "v_fma_f32 v56, -v6, v16, v56\n" \
  "v_fma_f32 v57, -v6, v17, v57\n" \
  "v_mfma_f32_16x16x32_bf16 v[38:41], v[136:139], v[26:29], v[38:41]\n" \
  "ds_read_b128 v[14:17], v4 offset:192\n" \
  "v_mul_f32 v48, v48, v7\n" \
  "v_mul_f32 v49, v49, v7\n" \
  "v_mfma_f32_16x16x32_bf16 v[42:45], v[26:29], v[200:203], v[42:45]\n" \
  "v_mul_f32 v50, v50, v7\n" \
  "v_mul_f32 v51, v51, v7\n" \
  "s_cmp_eq_u32 s78, 1\n" \
  "s_cbranch_scc0 .LCQ_X3_%=\n" \
  "global_load_lds_dwordx4 v2, s[60:61] offset:2048 sc1\n" \
  "global_load_lds_dwordx4 v2, s[60:61] offset:3072 sc1\n" \
  "s_add_u32 s60, s60, 0x8000\n" \
  "s_addc_u32 s61, s61, 0\n" \
  ".LCQ_X3_%=:\n" \
  "ds_read_b128 v[26:29], v0 offset:12288\n" \
  "s_waitcnt lgkmcnt(5)\n" \
  "v_mfma_f32_16x16x32_bf16 v[34:37], v[76:79], v[30:33], v[34:37]\n" \
  "v_cvt_pk_bf16_f32 v48, v48, v49\n" \
  "v_cvt_pk_bf16_f32 v49, v50, v51\n" \
  "v_mfma_f32_16x16x32_bf16 v[38:41], v[140:143], v[30:33], v[38:41]\n" \
  "v_mul_f32 v54, v54, v7\n" \
  "v_mul_f32 v55, v55, v7\n" \
  "v_mfma_f32_16x16x32_bf16 v[42:45], v[30:33], v[204:207], v[42:45]\n" \
  "v_mul_f32 v56, v56, v7\n" \
  "v_mul_f32 v57, v57, v7\n" \
  "s_cmp_eq_u32 s78, 1\n" \
  "s_cbranch_scc0 .LCQ_X4_%=\n" \
  "s_add_u32 s87, s71, 0x2000\n" \
  "v_bfe_u32 v5, v2, 2, 5\n" \
  "buffer_load_dword v5, v5, s[52:55], s87 offen sc1\n" \
  ".LCQ_X4_%=:\n" \
  "ds_read_b128 v[30:33], v0 offset:14336\n" \
  "s_waitcnt lgkmcnt(5)\n" \
  "v_mfma_f32_16x16x32_bf16 v[34:37], v[80:83], v[18:21], v[34:37]\n" \
  "v_cvt_pk_bf16_f32 v54, v54, v55\n" \
  "v_cvt_pk_bf16_f32 v55, v56, v57\n" \
  "v_mfma_f32_16x16x32_bf16 v[38:41], v[144:147], v[18:21], v[38:41]\n" \
  "s_waitcnt lgkmcnt(4)\n" \
  "v_fma_f32 v60, -v10, v8, v60\n" \
  "v_fma_f32 v61, -v11, v8, v61\n" \
  "v_mfma_f32_16x16x32_bf16 v[42:45], v[18:21], v[208:211], v[42:45]\n" \
  "v_fma_f32 v62, -v12, v8, v62\n" \
  "v_fma_f32 v63, -v13, v8, v63\n" \
  "ds_read_b128 v[18:21], v0 offset:16384\n" \
  "s_waitcnt lgkmcnt(4)\n" \
  "v_mfma_f32_16x16x32_bf16 v[34:37], v[84:87], v[22:25], v[34:37]\n" \
  "v_permlane16_swap_b32 v46, v48\n" \
  "v_mfma_f32_16x16x32_bf16 v[38:41], v[148:151], v[22:25], v[38:41]\n" \
  "v_permlane16_swap_b32 v47, v49\n" \
  "v_mfma_f32_16x16x32_bf16 v[42:45], v[22:25], v[212:215], v[42:45]\n" \
  "v_permlane16_swap_b32 v52, v54\n" \
  "ds_read_b128 v[22:25], v0 offset:18432\n" \
  "s_waitcnt lgkmcnt(3)\n" \
  "v_mfma_f32_16x16x32_bf16 v[34:37], v[88:91], v[26:29], v[34:37]\n" \
  "v_permlane16_swap_b32 v53, v55\n" \
  "v_mfma_f32_16x16x32_bf16 v[38:41], v[152:155], v[26:29], v[38:41]\n" \
  "v_mul_f32 v60, v60, v14\n" \
  "v_mul_f32 v61, v61, v15\n" \
  "v_mfma_f32_16x16x32_bf16 v[42:45], v[26:29], v[216:219], v[42:45]\n" \
  "v_mul_f32 v62, v62, v16\n" \
  "v_mul_f32 v63, v63, v17\n" \
  "ds_read_b128 v[26:29], v0 offset:20480\n" \
  "s_waitcnt lgkmcnt(3)\n" \
  "v_mfma_f32_16x16x32_bf16 v[34:37], v[92:95], v[30:33], v[34:37]\n" \
  "v_cvt_pk_bf16_f32 v60, v60, v61\n" \
  "v_cvt_pk_bf16_f32 v61, v62, v63\n" \
  "v_mfma_f32_16x16x32_bf16 v[38:41], v[156:159], v[30:33], v[38:41]\n" \
  "v_mov_b64 v[10:11], 0\n" \
  "v_lshrrev_b32 v12, 1, v2\n" \
  "v_mfma_f32_16x16x32_bf16 v[42:45], v[30:33], v[220:223], v[42:45]\n" \
  "s_cmp_eq_u32 s81, 0\n" \
  "s_cbranch_scc1 .LCQ_S5_%=\n" \
  "s_add_u32 s87, s65, 0x2000\n" \
  "buffer_store_dwordx4 v[52:55], v1, s[40:43], s87 offen sc1\n" \
  "buffer_store_dwordx4 v[58:61], v2, s[44:47], s66 offen sc1\n" \
  "buffer_store_dwordx4 v[46:49], v1, s[40:43], s65 offen sc1\n" \
  "buffer_store_dwordx2 v[10:11], v12, s[48:51], s67 offen sc1\n" \
  "s_add_u32 s65, s65, 0x80000\n" \
  "s_add_u32 s66, s66, 0x40000\n" \
  "s_add_u32 s67, s67, 0x10000\n" \
  "s_mov_b32 s84, s72\n" \
  "s_add_u32 s72, s72, 0x1000\n" \
  "s_mov_b32 s85, s70\n" \
  "s_xor_b32 s70, s70, 4\n" \
  "s_mov_b32 s79, 1\n" \
  ".LCQ_S5_%=:\n" \
  "s_setprio 2\n" \
  "ds_read_b128 v[30:33], v0 offset:22528\n" \
  "s_waitcnt lgkmcnt(3)\n" \
  "v_mfma_f32_16x16x32_bf16 v[34:37], v[96:99], v[18:21], v[34:37]\n" \
  "v_bfe_u32 v7, v2, 4, 5\n" \
  "v_and_b32 v8, 0x200, v2\n" \
  "v_mfma_f32_16x16x32_bf16 v[38:41], v[160:163], v[18:21], v[38:41]\n" \
  "v_lshl_add_u32 v6, v7, 3, s74\n" \
  "v_lshl_add_u32 v6, v8, 1, v6\n" \
  "v_mfma_f32_16x16x32_bf16 v[42:45], v[18:21], v[224:227], v[42:45]\n" \
  "ds_read2_b64 v[10:13], v6 offset0:0 offset1:32\n" \
  "ds_read2_b64 v[14:17], v6 offset0:64 offset1:96\n" \
  "v_lshrrev_b32 v7, 2, v2\n" \
  "s_andn2_b32 s82, s79, s81\n" \
  "s_cbranch_scc0 .LCQ_NPA_%=\n" \
  "s_waitcnt vmcnt(7)\n" \
  "s_cmp_lg_u32 s78, 0\n" \
  "s_cbranch_scc1 .LCQ_PA_%=\n" \
  "s_waitcnt vmcnt(0)\n" \
  ".LCQ_PA_%=:\n" \
  "v_mov_b32 v9, s85\n" \
  "s_mov_b64 exec, 2\n" \
  "ds_add_rtn_u32 v9, v9, v2\n" \
  "s_mov_b64 exec, -1\n" \
  ".LCQ_NPA_%=:\n" \
  "s_cmp_eq_u32 s78, 0\n" \
  "s_cbranch_scc0 .LCQ_X6_%=\n" \
  "v_bfe_u32 v5, v2, 2, 5\n" \
  "buffer_load_dword v5, v5, s[52:55], s71 offen sc1\n" \
  ".LCQ_X6_%=:\n" \
  "ds_read_b128 v[18:21], v0 offset:24576\n" \
  "s_waitcnt lgkmcnt(5)\n" \
  "v_mfma_f32_16x16x32_bf16 v[34:37], v[100:103], v[22:25], v[34:37]\n" \
  "s_waitcnt lgkmcnt(1)\n" \
  "v_add_f32 v6, v10, v12\n" \
  "v_add_f32 v8, v14, v16\n" \
  "v_mfma_f32_16x16x32_bf16 v[38:41], v[164:167], v[22:25], v[38:41]\n" \
  "v_add_f32 v6, v6, v8\n" \
  "v_mov_b32 v8, v6\n" \
  "v_mfma_f32_16x16x32_bf16 v[42:45], v[22:25], v[228:231], v[42:45]\n" \
  "v_add_f32 v11, v11, v13\n" \
  "v_add_f32 v15, v15, v17\n" \
  "ds_read_b128 v[22:25], v0 offset:26624\n" \
  "v_mfma_f32_16x16x32_bf16 v[34:37], v[104:107], v[26:29], v[34:37]\n" \
  "v_permlane32_swap_b32 v6, v8\n" \
  "v_mfma_f32_16x16x32_bf16 v[38:41], v[168:171], v[26:29], v[38:41]\n" \
  "v_add_f32 v6, v6, v8\n" \
  "v_fmamk_f32 v10, v6, 0xbe000000, v10\n" \
  "v_mfma_f32_16x16x32_bf16 v[42:45], v[26:29], v[232:235], v[42:45]\n" \
  "v_fmamk_f32 v12, v6, 0xbe000000, v12\n" \
  "v_fmamk_f32 v14, v6, 0xbe000000, v14\n" \
  "s_cmp_eq_u32 s82, 1\n" \
  "s_cbranch_scc0 .LCQ_X8_%=\n" \
  "s_waitcnt lgkmcnt(2)\n" \
  "v_readlane_b32 s87, v9, 1\n" \
  "s_mov_b32 s79, 0\n" \
  "s_and_b32 s87, s87, 0x70\n" \
  "s_cmp_eq_u32 s87, 0x70\n" \
  "s_cbranch_scc0 .LCQ_F7_%=\n" \
  "s_mov_b64 exec, 2\n" \
  "buffer_store_dword v9, off, s[56:59], s84 sc1\n" \
  "s_mov_b64 exec, -1\n" \
  ".LCQ_F7_%=:\n" \
  ".LCQ_X8_%=:\n" \
  "ds_read_b128 v[26:29], v0 offset:28672\n" \
  "v_mfma_f32_16x16x32_bf16 v[34:37], v[108:111], v[30:33], v[34:37]\n" \
  "v_fmamk_f32 v16, v6, 0xbe000000, v16\n" \
  "v_add_f32 v11, v11, v15\n" \
  "v_mfma_f32_16x16x32_bf16 v[38:41], v[172:175], v[30:33], v[38:41]\n" \
  "v_mul_f32 v10, v10, v10\n" \
  "v_fmac_f32 v10, v12, v12\n" \
  "v_mfma_f32_16x16x32_bf16 v[42:45], v[30:33], v[236:239], v[42:45]\n" \
  "v_fmac_f32 v10, v14, v14\n" \
  "v_fmac_f32 v10, v16, v16\n" \
  "ds_read_b128 v[30:33], v0 offset:30720\n" \
  "s_waitcnt lgkmcnt(3)\n" \
  "v_mfma_f32_16x16x32_bf16 v[34:37], v[112:115], v[18:21], v[34:37]\n" \
  "v_fmamk_f32 v11, v10, 0x3c800000, v11\n" \
  "v_mov_b32 v8, v11\n" \
  "v_mfma_f32_16x16x32_bf16 v[38:41], v[176:179], v[18:21], v[38:41]\n" \
  "v_mul_f32 v6, 0x3b000000, v6\n" \
  "v_add_u32 v7, s69, v7\n" \
  "v_mfma_f32_16x16x32_bf16 v[42:45], v[18:21], v[240:243], v[42:45]\n" \
  "v_permlane32_swap_b32 v11, v8\n" \
  "ds_read_b128 v[18:21], v0 offset:256\n" \
  "s_waitcnt lgkmcnt(3)\n" \
  "v_mfma_f32_16x16x32_bf16 v[34:37], v[116:119], v[22:25], v[34:37]\n" \
  "v_add_f32 v11, v11, v8\n" \
  "v_mul_f32 v10, 0x3b000000, v11\n" \
  "v_mfma_f32_16x16x32_bf16 v[38:41], v[180:183], v[22:25], v[38:41]\n" \
  "v_cvt_pk_bf16_f32 v6, v6, v10\n" \
  "v_and_b32 v10, 0xffff0000, v6\n" \
  "v_mfma_f32_16x16x32_bf16 v[42:45], v[22:25], v[244:247], v[42:45]\n" \
  "v_lshlrev_b32 v6, 16, v6\n" \
  "v_add_f32 v10, 0x3727c5ac, v10\n" \
  "ds_read_b128 v[22:25], v0 offset:2304\n" \
  "s_waitcnt lgkmcnt(3)\n" \
  "v_mfma_f32_16x16x32_bf16 v[34:37], v[120:123], v[26:29], v[34:37]\n" \
  "v_cvt_pk_bf16_f32 v10, v10, v10\n" \
  "v_lshlrev_b32 v10, 16, v10\n" \
  "v_mfma_f32_16x16x32_bf16 v[38:41], v[184:187], v[26:29], v[38:41]\n" \
  "v_sqrt_f32 v10, v10\n" \
  "s_nop 0\n" \
  "v_cvt_pk_bf16_f32 v10, v10, v10\n" \
  "v_mfma_f32_16x16x32_bf16 v[42:45], v[26:29], v[248:251], v[42:45]\n" \
  "v_lshlrev_b32 v10, 16, v10\n" \
  "v_rcp_f32 v10, v10\n" \
  "ds_read_b128 v[26:29], v0 offset:4352\n" \
  "s_waitcnt lgkmcnt(3)\n" \
  "v_mfma_f32_16x16x32_bf16 v[34:37], v[124:127], v[30:33], v[34:37]\n" \
  "s_nop 0\n" \
  "v_cndmask_b32 v10, v6, v10, s[76:77]\n" \
  "ds_write_b32 v7, v10\n" \
  "v_mfma_f32_16x16x32_bf16 v[38:41], v[188:191], v[30:33], v[38:41]\n" \
  "v_mfma_f32_16x16x32_bf16 v[42:45], v[30:33], v[252:255], v[42:45]\n" \
  "s_setprio 1\n" \
  "ds_read_b128 v[30:33], v0 offset:6400\n" \
  "s_waitcnt lgkmcnt(4)\n" \
  "v_mfma_f32_16x16x32_bf16 v[48:51], v[64:67], v[18:21], 0\n" \
  "ds_read2_b32 v[6:7], v3 offset0:0 offset1:32\n" \
  "ds_read_b128 v[10:13], v4 offset:256\n" \
  "ds_read_b128 v[14:17], v4 offset:320\n" \
  "ds_read_b32 v8, v3 offset:384\n" \
  "v_mfma_f32_16x16x32_bf16 v[54:57], v[128:131], v[18:21], 0\n" \
  "v_mfma_f32_16x16x32_bf16 v[60:63], v[18:21], v[192:195], 0\n" \
  "ds_read_b128 v[18:21], v0 offset:8448\n" \
  "s_waitcnt lgkmcnt(8)\n" \
  "v_mfma_f32_16x16x32_bf16 v[48:51], v[68:71], v[22:25], v[48:51]\n" \
  "s_waitcnt lgkmcnt(3)\n" \
  "v_fma_f32 v34, -v6, v10, v34\n" \
  "v_fma_f32 v35, -v6, v11, v35\n" \
  "v_mfma_f32_16x16x32_bf16 v[54:57], v[132:135], v[22:25], v[54:57]\n" \
  "v_fma_f32 v36, -v6, v12, v36\n" \
  "v_fma_f32 v37, -v6, v13, v37\n" \
  "v_mfma_f32_16x16x32_bf16 v[60:63], v[22:25], v[196:199], v[60:63]\n" \
  "ds_read_b128 v[10:13], v4 offset:0\n" \
  "s_waitcnt lgkmcnt(3)\n" \
  "v_fma_f32 v38, -v6, v14, v38\n" \
  "v_fma_f32 v39, -v6, v15, v39\n" \
  "s_mov_b32 s82, 0\n" \
  "s_cmp_eq_u32 s78, 0\n" \
  "s_cbranch_scc0 .LCQ_X9_%=\n" \
  "s_waitcnt vmcnt(0)\n" \
  "v_cmp_eq_u32 vcc, 0, v5\n" \
  "s_cmp_eq_u64 vcc, 0\n" \
  "s_cselect_b32 s78, 2, 0\n" \
  "s_mov_b32 s82, s79\n" \
  ".LCQ_X9_%=:\n" \
  "s_cmp_eq_u32 s82, 1\n" \
  "s_cbranch_scc0 .LCQ_X10_%=\n" \
  "v_mov_b32 v9, s85\n" \
  "s_mov_b64 exec, 2\n" \
  "ds_add_rtn_u32 v9, v9, v2\n" \
  "s_mov_b64 exec, -1\n" \
  ".LCQ_X10_%=:\n" \
  "s_cmp_eq_u32 s78, 2\n" \
  "s_cbranch_scc0 .LCQ_X11_%=\n" \
  "s_mov_b32 m0, s73\n" \
  "s_nop 0\n" \
  "global_load_lds_dwordx4 v2, s[62:63] offset:0 sc1\n" \
  "global_load_lds_dwordx4 v2, s[62:63] offset:1024 sc1\n" \
  "s_add_u32 s62, s62, 0x4000\n" \
  "s_addc_u32 s63, s63, 0\n" \
  ".LCQ_X11_%=:\n" \
  "ds_read_b128 v[22:25], v0 offset:10496\n" \
  "v_mfma_f32_16x16x32_bf16 v[48:51], v[72:75], v[26:29], v[48:51]\n" \
  "v_fma_f32 v40, -v6, v16, v40\n" \
  "v_fma_f32 v41, -v6, v17, v41\n" \
  "v_mfma_f32_16x16x32_bf16 v[54:57], v[136:139], v[26:29], v[54:57]\n" \
  "ds_read_b128 v[14:17], v4 offset:128\n" \
  "v_mul_f32 v34, v34, v7\n" \
  "v_mul_f32 v35, v35, v7\n" \
  "v_mfma_f32_16x16x32_bf16 v[60:63], v[26:29], v[200:203], v[60:63]\n" \
  "v_mul_f32 v36, v36, v7\n" \
  "v_mul_f32 v37, v37, v7\n" \
  "s_cmp_eq_u32 s78, 2\n" \
  "s_cbranch_scc0 .LCQ_X12_%=\n" \
  "s_mov_b32 m0, s68\n" \
  "s_xor_b32 s68, s68, 0x8000\n" \
  "global_load_lds_dwordx4 v2, s[60:61] offset:0 sc1\n" \
  "global_load_lds_dwordx4 v2, s[60:61] offset:1024 sc1\n" \
  ".LCQ_X12_%=:\n" \
  "ds_read_b128 v[26:29], v0 offset:12544\n" \
  "v_mfma_f32_16x16x32_bf16 v[48:51], v[76:79], v[30:33], v[48:51]\n" \
  "v_cvt_pk_bf16_f32 v46, v34, v35\n" \
  "v_cvt_pk_bf16_f32 v47, v36, v37\n" \
  "v_mfma_f32_16x16x32_bf16 v[54:57], v[140:143], v[30:33], v[54:57]\n" \
  "v_mul_f32 v38, v38, v7\n" \
  "v_mul_f32 v39, v39, v7\n" \
  "v_mfma_f32_16x16x32_bf16 v[60:63], v[30:33], v[204:207], v[60:63]\n" \
  "v_mul_f32 v40, v40, v7\n" \
  "v_mul_f32 v41, v41, v7\n" \
  "s_cmp_eq_u32 s82, 1\n" \
  "s_cbranch_scc0 .LCQ_X14_%=\n" \
  "s_waitcnt lgkmcnt(2)\n" \
  "v_readlane_b32 s87, v9, 1\n" \
  "s_mov_b32 s79, 0\n" \
  "s_and_b32 s87, s87, 0x70\n" \
  "s_cmp_eq_u32 s87, 0x70\n" \
  "s_cbranch_scc0 .LCQ_F13_%=\n" \
  "s_mov_b64 exec, 2\n" \
  "buffer_store_dword v9, off, s[56:59], s84 sc1\n" \
  "s_mov_b64 exec, -1\n" \
  ".LCQ_F13_%=:\n" \
  ".LCQ_X14_%=:\n" \
  "s_cmp_eq_u32 s78, 2\n" \
  "s_cbranch_scc0 .LCQ_X15_%=\n" \
  "global_load_lds_dwordx4 v2, s[60:61] offset:2048 sc1\n" \
  "global_load_lds_dwordx4 v2, s[60:61] offset:3072 sc1\n" \
  "s_add_u32 s60, s60, 0x8000\n" \
  "s_addc_u32 s61, s61, 0\n" \
  ".LCQ_X15_%=:\n" \
  "ds_read_b128 v[30:33], v0 offset:14592\n" \
  "s_waitcnt lgkmcnt(5)\n" \
  "v_mfma_f32_16x16x32_bf16 v[48:51], v[80:83], v[18:21], v[48:51]\n" \
  "v_cvt_pk_bf16_f32 v52, v38, v39\n" \
  "v_cvt_pk_bf16_f32 v53, v40, v41\n" \
  "v_mfma_f32_16x16x32_bf16 v[54:57], v[144:147], v[18:21], v[54:57]\n" \
  "s_waitcnt lgkmcnt(4)\n" \
  "v_fma_f32 v42, -v10, v8, v42\n" \
  "v_fma_f32 v43, -v11, v8, v43\n" \
  "v_mfma_f32_16x16x32_bf16 v[60:63], v[18:21], v[208:211], v[60:63]\n" \
  "v_fma_f32 v44, -v12, v8, v44\n" \
  "v_fma_f32 v45, -v13, v8, v45\n" \
  "s_cmp_eq_u32 s78, 2\n" \
  "s_cbranch_scc0 .LCQ_X16_%=\n" \
  "s_add_u32 s87, s71, 0x2000\n" \
  "v_bfe_u32 v5, v2, 2, 5\n" \
  "buffer_load_dword v5, v5, s[52:55], s87 offen sc1\n" \
  ".LCQ_X16_%=:\n" \
  "s_and_b32 s82, s79, s78\n" \
  "s_cmp_eq_u32 s78, 1\n" \
  "s_cbranch_scc0 .LCQ_X17_%=\n" \
  "s_waitcnt vmcnt(0)\n" \
  ".LCQ_X17_%=:\n" \
  "s_cmp_eq_u32 s82, 1\n" \
  "s_cbranch_scc0 .LCQ_X18_%=\n" \
  "v_mov_b32 v9, s85\n" \
  "s_mov_b64 exec, 2\n" \
  "ds_add_rtn_u32 v9, v9, v2\n" \
  "s_mov_b64 exec, -1\n" \
  ".LCQ_X18_%=:\n" \
  "ds_read_b128 v[18:21], v0 offset:16640\n" \
  "s_waitcnt lgkmcnt(4)\n" \
  "v_mfma_f32_16x16x32_bf16 v[48:51], v[84:87], v[22:25], v[48:51]\n" \
  "s_waitcnt lgkmcnt(3)\n" \
  "v_mul_f32 v42, v42, v14\n" \
  "v_mul_f32 v43, v43, v15\n" \
  "v_mfma_f32_16x16x32_bf16 v[54:57], v[148:151], v[22:25], v[54:57]\n" \
  "v_mul_f32 v44, v44, v16\n" \
  "v_mul_f32 v45, v45, v17\n" \
  "v_mfma_f32_16x16x32_bf16 v[60:63], v[22:25], v[212:215], v[60:63]\n" \
  "v_cvt_pk_bf16_f32 v58, v42, v43\n" \
  "v_cvt_pk_bf16_f32 v59, v44, v45\n" \
  "ds_read_b128 v[22:25], v0 offset:18688\n" \
  "s_waitcnt lgkmcnt(3)\n" \
  "v_mfma_f32_16x16x32_bf16 v[48:51], v[88:91], v[26:29], v[48:51]\n" \
  "v_mfma_f32_16x16x32_bf16 v[54:57], v[152:155], v[26:29], v[54:57]\n" \
  "v_mfma_f32_16x16x32_bf16 v[60:63], v[26:29], v[216:219], v[60:63]\n" \
  "s_cmp_eq_u32 s82, 1\n" \
  "s_cbranch_scc0 .LCQ_X20_%=\n" \
  "s_waitcnt lgkmcnt(2)\n" \
  "v_readlane_b32 s87, v9, 1\n" \
  "s_mov_b32 s79, 0\n" \
  "s_and_b32 s87, s87, 0x70\n" \
  "s_cmp_eq_u32 s87, 0x70\n" \
  "s_cbranch_scc0 .LCQ_F19_%=\n" \
  "s_mov_b64 exec, 2\n" \
  "buffer_store_dword v9, off, s[56:59], s84 sc1\n" \
  "s_mov_b64 exec, -1\n" \
  ".LCQ_F19_%=:\n" \
  ".LCQ_X20_%=:\n" \
  "s_cmp_eq_u32 s78, 0\n" \
  "s_cbranch_scc0 .LCQ_X21_%=\n" \
  "v_bfe_u32 v5, v2, 2, 5\n" \
  "buffer_load_dword v5, v5, s[52:55], s71 offen sc1\n" \
  ".LCQ_X21_%=:\n" \
  "ds_read_b128 v[26:29], v0 offset:20736\n" \
  "s_waitcnt lgkmcnt(3)\n" \
  "v_mfma_f32_16x16x32_bf16 v[48:51], v[92:95], v[30:33], v[48:51]\n" \
  "v_mfma_f32_16x16x32_bf16 v[54:57], v[156:159], v[30:33], v[54:57]\n" \
  "v_mfma_f32_16x16x32_bf16 v[60:63], v[30:33], v[220:223], v[60:63]\n" \
  "s_setprio 0\n" \
  "ds_read_b128 v[30:33], v0 offset:22784\n" \
  "s_waitcnt lgkmcnt(3)\n" \
  "v_mfma_f32_16x16x32_bf16 v[48:51], v[96:99], v[18:21], v[48:51]\n" \
  "v_mfma_f32_16x16x32_bf16 v[54:57], v[160:163], v[18:21], v[54:57]\n" \
  "v_mfma_f32_16x16x32_bf16 v[60:63], v[18:21], v[224:227], v[60:63]\n" \
  "ds_read_b128 v[18:21], v0 offset:24832\n" \
  "s_waitcnt lgkmcnt(3)\n" \
  "v_mfma_f32_16x16x32_bf16 v[48:51], v[100:103], v[22:25], v[48:51]\n" \
  "v_mfma_f32_16x16x32_bf16 v[54:57], v[164:167], v[22:25], v[54:57]\n" \
  "v_mfma_f32_16x16x32_bf16 v[60:63], v[22:25], v[228:231], v[60:63]\n" \
  "ds_read_b128 v[22:25], v0 offset:26880\n" \
  "s_waitcnt lgkmcnt(3)\n" \
  "v_mfma_f32_16x16x32_bf16 v[48:51], v[104:107], v[26:29], v[48:51]\n" \
  "v_bfe_u32 v7, v2, 4, 5\n" \
  "v_and_b32 v8, 0x200, v2\n" \
  "v_mfma_f32_16x16x32_bf16 v[54:57], v[168:171], v[26:29], v[54:57]\n" \
  "v_lshl_add_u32 v6, v7, 3, s73\n" \
  "v_lshl_add_u32 v6, v8, 1, v6\n" \
  "v_mfma_f32_16x16x32_bf16 v[60:63], v[26:29], v[232:235], v[60:63]\n" \
  "s_cmp_eq_u32 s78, 2\n" \
  "s_cbranch_scc0 .LCQ_X22_%=\n" \
  "s_waitcnt vmcnt(5)\n" \
  ".LCQ_X22_%=:\n" \
  "ds_read_b128 v[26:29], v0 offset:28928\n" \
  "s_waitcnt lgkmcnt(3)\n" \
  "v_mfma_f32_16x16x32_bf16 v[48:51], v[108:111], v[30:33], v[48:51]\n" \
  "ds_read2_b64 v[10:13], v6 offset0:0 offset1:32\n" \
  "ds_read2_b64 v[14:17], v6 offset0:64 offset1:96\n" \
  "v_lshl_add_u32 v7, v7, 3, s75\n" \
  "v_mfma_f32_16x16x32_bf16 v[54:57], v[172:175], v[30:33], v[54:57]\n" \
  "v_mfma_f32_16x16x32_bf16 v[60:63], v[30:33], v[236:239], v[60:63]\n" \
  "ds_read_b128 v[30:33], v0 offset:30976\n" \
  "s_waitcnt lgkmcnt(5)\n" \
  "v_mfma_f32_16x16x32_bf16 v[48:51], v[112:115], v[18:21], v[48:51]\n" \
  "s_waitcnt lgkmcnt(1)\n" \
  "v_add_f32 v6, v10, v12\n" \
  "v_add_f32 v8, v14, v16\n" \
  "v_mfma_f32_16x16x32_bf16 v[54:57], v[176:179], v[18:21], v[54:57]\n" \
  "v_add_f32 v6, v6, v8\n" \
  "v_mov_b32 v8, v6\n" \
  "v_mfma_f32_16x16x32_bf16 v[60:63], v[18:21], v[240:243], v[60:63]\n" \
  "v_add_f32 v11, v11, v13\n" \
  "v_add_f32 v15, v15, v17\n" \
  "v_mfma_f32_16x16x32_bf16 v[48:51], v[116:119], v[22:25], v[48:51]\n" \
  "v_permlane32_swap_b32 v6, v8\n" \
  "v_mfma_f32_16x16x32_bf16 v[54:57], v[180:183], v[22:25], v[54:57]\n" \
  "v_add_f32 v6, v6, v8\n" \
  "v_fmamk_f32 v10, v6, 0xbe000000, v10\n" \
  "v_mfma_f32_16x16x32_bf16 v[60:63], v[22:25], v[244:247], v[60:63]\n" \
  "v_fmamk_f32 v12, v6, 0xbe000000, v12\n" \
  "v_fmamk_f32 v14, v6, 0xbe000000, v14\n" \
  "v_mfma_f32_16x16x32_bf16 v[48:51], v[120:123], v[26:29], v[48:51]\n" \
  "v_fmamk_f32 v16, v6, 0xbe000000, v16\n" \
  "v_add_f32 v11, v11, v15\n" \
  "v_mfma_f32_16x16x32_bf16 v[54:57], v[184:187], v[26:29], v[54:57]\n" \
  "v_mul_f32 v10, v10, v10\n" \
  "v_fmac_f32 v10, v12, v12\n" \
  "v_mfma_f32_16x16x32_bf16 v[60:63], v[26:29], v[248:251], v[60:63]\n" \
  "v_fmac_f32 v10, v14, v14\n" \
  "v_fmac_f32 v10, v16, v16\n" \
  "s_waitcnt lgkmcnt(0)\n" \
  "v_mfma_f32_16x16x32_bf16 v[48:51], v[124:127], v[30:33], v[48:51]\n" \
  "v_fmamk_f32 v11, v10, 0x3e000000, v11\n" \
  "v_mov_b32 v8, v11\n" \
  "v_mfma_f32_16x16x32_bf16 v[54:57], v[188:191], v[30:33], v[54:57]\n" \
  "v_mov_b32 v10, v6\n" \
  "s_nop 0\n" \
  "v_permlane32_swap_b32 v11, v8\n" \
  "v_mfma_f32_16x16x32_bf16 v[60:63], v[30:33], v[252:255], v[60:63]\n" \
  "v_add_f32 v11, v11, v8\n" \
  "ds_write_b64 v7, v[10:11]\n" \
  "s_cmp_eq_u32 s78, 1\n" \
  "s_cbranch_scc1 .LCQ_NV_%=\n" \
  "s_waitcnt vmcnt(0)\n" \
  ".LCQ_NV_%=:\n" \
  "v_cmp_eq_u32 vcc, 0, v5\n" \
  "s_cmp_eq_u64 vcc, 0\n" \
  "s_cselect_b32 s80, 1, 0\n" \
  "s_sub_u32 s64, s64, 1\n" \
  "s_min_u32 s81, s78, 1\n" \
  "s_cmp_lg_u32 s78, 0\n" \
  "s_cbranch_scc1 .LCQ_TOP_%=\n" \
  "s_cmp_eq_u32 s80, 1\n" \
  "s_cbranch_scc0 .LCQ_X23_%=\n" \
  "s_mov_b32 m0, s73\n" \
  "s_nop 0\n" \
  "global_load_lds_dwordx4 v2, s[62:63] offset:0 sc1\n" \
  "global_load_lds_dwordx4 v2, s[62:63] offset:1024 sc1\n" \
  "s_add_u32 s62, s62, 0x4000\n" \
  "s_addc_u32 s63, s63, 0\n" \
  "s_mov_b32 m0, s68\n" \
  "s_xor_b32 s68, s68, 0x8000\n" \
  "global_load_lds_dwordx4 v2, s[60:61] offset:0 sc1\n" \
  "global_load_lds_dwordx4 v2, s[60:61] offset:1024 sc1\n" \
  "global_load_lds_dwordx4 v2, s[60:61] offset:2048 sc1\n" \
  "global_load_lds_dwordx4 v2, s[60:61] offset:3072 sc1\n" \
  "s_add_u32 s60, s60, 0x8000\n" \
  "s_addc_u32 s61, s61, 0\n" \
  "s_add_u32 s87, s71, 0x2000\n" \
  "v_bfe_u32 v5, v2, 2, 5\n" \
  "buffer_load_dword v5, v5, s[52:55], s87 offen sc1\n" \
  ".LCQ_X23_%=:\n" \
  "ds_read2_b32 v[6:7], v3 offset0:16 offset1:48\n" \
  "ds_read_b128 v[10:13], v4 offset:256\n" \
  "ds_read_b128 v[14:17], v4 offset:320\n" \
  "ds_read_b32 v8, v3 offset:384\n" \
  "s_waitcnt lgkmcnt(2)\n" \
  "v_fma_f32 v48, -v6, v10, v48\n" \
  "v_fma_f32 v49, -v6, v11, v49\n" \
  "v_fma_f32 v50, -v6, v12, v50\n" \
  "v_fma_f32 v51, -v6, v13, v51\n" \
  "ds_read_b128 v[10:13], v4 offset:64\n" \
  "s_waitcnt lgkmcnt(2)\n" \
  "v_fma_f32 v54, -v6, v14, v54\n" \
  "v_fma_f32 v55, -v6, v15, v55\n" \
  "v_fma_f32 v56, -v6, v16, v56\n" \
  "v_fma_f32 v57, -v6, v17, v57\n" \
  "ds_read_b128 v[14:17], v4 offset:192\n" \
  "v_mul_f32 v48, v48, v7\n" \
  "v_mul_f32 v49, v49, v7\n" \
  "v_mul_f32 v50, v50, v7\n" \
  "v_mul_f32 v51, v51, v7\n" \
  "v_cvt_pk_bf16_f32 v48, v48, v49\n" \
  "v_cvt_pk_bf16_f32 v49, v50, v51\n" \
  "v_mul_f32 v54, v54, v7\n" \
  "v_mul_f32 v55, v55, v7\n" \
  "v_mul_f32 v56, v56, v7\n" \
  "v_mul_f32 v57, v57, v7\n" \
  "v_cvt_pk_bf16_f32 v54, v54, v55\n" \
  "v_cvt_pk_bf16_f32 v55, v56, v57\n" \
  "s_waitcnt lgkmcnt(1)\n" \
  "v_fma_f32 v60, -v10, v8, v60\n" \
  "v_fma_f32 v61, -v11, v8, v61\n" \
  "v_fma_f32 v62, -v12, v8, v62\n" \
  "v_fma_f32 v63, -v13, v8, v63\n" \
  "v_permlane16_swap_b32 v46, v48\n" \
  "v_permlane16_swap_b32 v47, v49\n" \
  "v_permlane16_swap_b32 v52, v54\n" \
  "v_permlane16_swap_b32 v53, v55\n" \
  "s_waitcnt lgkmcnt(0)\n" \
  "v_mul_f32 v60, v60, v14\n" \
  "v_mul_f32 v61, v61, v15\n" \
  "v_mul_f32 v62, v62, v16\n" \
  "v_mul_f32 v63, v63, v17\n" \
  "v_cvt_pk_bf16_f32 v60, v60, v61\n" \
  "v_cvt_pk_bf16_f32 v61, v62, v63\n" \
  "v_mov_b64 v[10:11], 0\n" \
  "v_lshrrev_b32 v12, 1, v2\n" \
  "s_add_u32 s87, s65, 0x2000\n" \
  "buffer_store_dwordx4 v[52:55], v1, s[40:43], s87 offen sc1\n" \
  "buffer_store_dwordx4 v[58:61], v2, s[44:47], s66 offen sc1\n" \
  "buffer_store_dwordx4 v[46:49], v1, s[40:43], s65 offen sc1\n" \
  "buffer_store_dwordx2 v[10:11], v12, s[48:51], s67 offen sc1\n" \
  "s_add_u32 s65, s65, 0x80000\n" \
  "s_add_u32 s66, s66, 0x40000\n" \
  "s_add_u32 s67, s67, 0x10000\n" \
  "s_mov_b32 s84, s72\n" \
  "s_add_u32 s72, s72, 0x1000\n" \
  "s_mov_b32 s85, s70\n" \
  "s_xor_b32 s70, s70, 4\n" \
  "s_mov_b32 s79, 1\n" \
  "s_cmp_eq_u32 s80, 0\n" \
  "s_cbranch_scc1 .LCQ_POLL_%=\n" \
  "s_waitcnt vmcnt(4)\n" \
  "s_branch .LCQ_GOT_%=\n" \
  ".LCQ_POLL_%=:\n" \
  "s_mov_b32 s86, 0x40000\n" \
  "s_branch .LCQ_PW_%=\n" \
  ".LCQ_PV_%=:\n" \
  "s_sleep 12\n" \
  ".LCQ_PW_%=:\n" \
  "v_bfe_u32 v5, v2, 2, 5\n" \
  "buffer_load_dword v5, v5, s[52:55], s71 offen sc1\n" \
  "s_sub_u32 s86, s86, 1\n" \
  "s_cbranch_scc1 .LCQ_END_%=\n" \
  "s_waitcnt vmcnt(0)\n" \
  "s_cmp_eq_u32 s79, 0\n" \
  "s_cbranch_scc1 .LCQ_NPD_%=\n" \
  "v_mov_b32 v9, s85\n" \
  "s_mov_b64 exec, 2\n" \
  "ds_add_rtn_u32 v9, v9, v2\n" \
  "s_mov_b64 exec, -1\n" \
  "s_waitcnt lgkmcnt(0)\n" \
  "v_readlane_b32 s87, v9, 1\n" \
  "s_mov_b32 s79, 0\n" \
  "s_and_b32 s87, s87, 0x70\n" \
  "s_cmp_eq_u32 s87, 0x70\n" \
  "s_cbranch_scc0 .LCQ_F24_%=\n" \
  "s_mov_b64 exec, 2\n" \
  "buffer_store_dword v9, off, s[56:59], s84 sc1\n" \
  "s_mov_b64 exec, -1\n" \
  ".LCQ_F24_%=:\n" \
  "s_cmp_eq_u32 s64, 0\n" \
  "s_cbranch_scc1 .LCQ_END_%=\n" \
  ".LCQ_NPD_%=:\n" \
  "v_cmp_eq_u32 vcc, 0, v5\n" \
  "s_cmp_eq_u64 vcc, 0\n" \
  "s_cbranch_scc0 .LCQ_PV_%=\n" \
  "s_mov_b32 m0, s73\n" \
  "s_nop 0\n" \
  "global_load_lds_dwordx4 v2, s[62:63] offset:0 sc1\n" \
  "global_load_lds_dwordx4 v2, s[62:63] offset:1024 sc1\n" \
  "s_add_u32 s62, s62, 0x4000\n" \
  "s_addc_u32 s63, s63, 0\n" \
  "s_mov_b32 m0, s68\n" \
  "s_xor_b32 s68, s68, 0x8000\n" \
  "global_load_lds_dwordx4 v2, s[60:61] offset:0 sc1\n" \
  "global_load_lds_dwordx4 v2, s[60:61] offset:1024 sc1\n" \
  "global_load_lds_dwordx4 v2, s[60:61] offset:2048 sc1\n" \
  "global_load_lds_dwordx4 v2, s[60:61] offset:3072 sc1\n" \
  "s_add_u32 s60, s60, 0x8000\n" \
  "s_addc_u32 s61, s61, 0\n" \
  "s_add_u32 s87, s71, 0x2000\n" \
  "v_bfe_u32 v5, v2, 2, 5\n" \
  "buffer_load_dword v5, v5, s[52:55], s87 offen sc1\n" \
  "s_waitcnt vmcnt(0)\n" \
  ".LCQ_GOT_%=:\n" \
  "v_bfe_u32 v7, v2, 4, 5\n" \
  "v_and_b32 v8, 0x200, v2\n" \
  "v_lshl_add_u32 v6, v7, 3, s73\n" \
  "v_lshl_add_u32 v6, v8, 1, v6\n" \
  "ds_read2_b64 v[10:13], v6 offset0:0 offset1:32\n" \
  "ds_read2_b64 v[14:17], v6 offset0:64 offset1:96\n" \
  "v_lshl_add_u32 v7, v7, 3, s75\n" \
  "s_waitcnt lgkmcnt(0)\n" \
  "v_add_f32 v6, v10, v12\n" \
  "v_add_f32 v8, v14, v16\n" \
  "v_add_f32 v6, v6, v8\n" \
  "v_mov_b32 v8, v6\n" \
  "v_add_f32 v11, v11, v13\n" \
  "v_add_f32 v15, v15, v17\n" \
  "v_permlane32_swap_b32 v6, v8\n" \
  "v_add_f32 v6, v6, v8\n" \
  "v_fmamk_f32 v10, v6, 0xbe000000, v10\n" \
  "v_fmamk_f32 v12, v6, 0xbe000000, v12\n" \
  "v_fmamk_f32 v14, v6, 0xbe000000, v14\n" \
  "v_fmamk_f32 v16, v6, 0xbe000000, v16\n" \
  "v_add_f32 v11, v11, v15\n" \
  "v_mul_f32 v10, v10, v10\n" \
  "v_fmac_f32 v10, v12, v12\n" \
  "v_fmac_f32 v10, v14, v14\n" \
  "v_fmac_f32 v10, v16, v16\n" \
  "v_fmamk_f32 v11, v10, 0x3e000000, v11\n" \
  "v_mov_b32 v8, v11\n" \
  "v_mov_b32 v10, v6\n" \
  "s_nop 0\n" \
  "v_permlane32_swap_b32 v11, v8\n" \
  "v_add_f32 v11, v11, v8\n" \
  "ds_write_b64 v7, v[10:11]\n" \
  "v_cmp_eq_u32 vcc, 0, v5\n" \
  "s_cmp_eq_u64 vcc, 0\n" \
  "s_cselect_b32 s80, 1, 0\n" \
  "s_branch .LCQ_TOP_%=\n" \
  ".LCQ_END_%=:\n" \
  "s_endpgm\n"
__device__ __forceinline__ void role_qkv(const MegaArgs& A, char* smem, const int L, const int h) {
  const int w = __builtin_amdgcn_readfirstlane(threadIdx.x >> 6);
  char* ws = A.ws;
  unsigned* FL = (unsigned*)(ws + OFF_FL);
  const int l = lane_id();
  const int c = l & 15, g = l >> 4;
  const unsigned sb = lds_base(smem);
  v8u wq[8], wk[8], wv[8];
  {
    const int q = (l >> 2) & 3, p = l & 3, f = q | ((g & 1) << 2);
    const unsigned ra = sb + (8 * g + q) * 256 + (w ^ f) * 32 + p * 8;
#define W_STAGE(W, hf, VM, NEXT)                                               \
  {                                                                           \
    WAIT_VM(VM);                                                              \
    RAW_BAR();                                                                \
    _Pragma("unroll") for (int t = 0; t < 4; ++t) {                           \
      const unsigned a_ = ra + (hf) * 8192 + t * 32768;                       \
      const v2u r0 = trd(a_), r1 = trd(a_ + 1024), r2 = trd(a_ + 16384), r3 = trd(a_ + 16384 + 1024); \
      W[4 * (hf) + t] = v8u{r0.x, r0.y, r1.x, r1.y, r2.x, r2.y, r3.x, r3.y};  \
    }                                                                         \
    _Pragma("unroll") for (int t = 0; t < 4; ++t) asm volatile("" : "+v"(W[4 * (hf) + t])); \
    RAW_BAR();                                                                \
    NEXT;                                                                     \
  }
    wqkv_issue<false>(A.Wq, 0, h, w, l, sb);
    wqkv_issue<false>(A.Wq, 1, h, w, l, sb);
    tag_early(A);
    W_STAGE(wq, 0, 8, wqkv_issue<false>(A.Wk, 0, h, w, l, sb));
    SST(4);
    W_STAGE(wq, 1, 8, wqkv_issue<false>(A.Wk, 1, h, w, l, sb));
    SST(5);
    W_STAGE(wk, 0, 8, wqkv_issue<false>(A.Wv, 0, h, w, l, sb));
    W_STAGE(wk, 1, 8, wqkv_issue<false>(A.Wv, 1, h, w, l, sb));
    SST(6);
    u64 rv;
    W_STAGE(wv, 0, 8, rv = ready_look(A));
    W_STAGE(wv, 1, 0, (void)0);
    ready_wait(A, rv);
#undef W_STAGE
  }
  WSTAMP2();
  constexpr int Q_ST = 65536, Q_PR = Q_ST + 8192, Q_WP = Q_PR + 16384, Q_CN = Q_WP + 4096;
  {
    const v4u one = {0x3f803f80u, 0x3f803f80u, 0x3f803f80u, 0x3f803f80u};
    f32x4 csq = {}, csk = {}, t = {};
#define HALF(x, i) BF8(((i) ? __builtin_shufflevector(x, x, 4, 5, 6, 7) : __builtin_shufflevector(x, x, 0, 1, 2, 3)))
#pragma unroll
    for (int ks = 0; ks < 16; ++ks) {
      csq = MFMA16(HALF(wq[ks >> 1], ks & 1), BF8(one), csq);
      csk = MFMA16(HALF(wk[ks >> 1], ks & 1), BF8(one), csk);
      t = MFMA16(BF8(one), HALF(wv[ks >> 1], ks & 1), t);
    }
#undef HALF
    float* cp = reinterpret_cast<float*>(smem + Q_ST + w * 1024 + 256);
    *reinterpret_cast<f32x4*>(cp + 4 * g) = csq;
    *reinterpret_cast<f32x4*>(cp + 16 + 4 * g) = csk;
    cp[32 + c] = t[0];
  }
  if (threadIdx.x < 2) reinterpret_cast<unsigned*>(smem + Q_CN)[threadIdx.x] = 0u;
  const int rc = blockIdx.x & (NR_C - 1);
  const v4u rsQ = mk_srd(ws + OFF_QK + L * SZ_QK, SZ_QK);
  const v4u rsV = mk_srd(ws + OFF_VF + L * SZ_O, SZ_O);
  const v4u rsM = mk_srd(ws + (w >= 4 ? OFF_MEMV : OFF_MEMK) + L * SZ_MEM, SZ_MEM);
  const v4u rsF = mk_srd(FL + FL_FC + (L * 64 * 8 + rc) * 8 * 32 + w * 32, (64 * 64 - rc * 8 - w) * 128);
  const v4u rsFQ = mk_srd(FL + FL_FQ + (L * 64 * 32 + h) * 32, 64 * 4096 - h * 128);
  const char* Xb = ws + (L ? OFF_X3 : OFF_XA) + w * 4096;
  const char* Sb = ws + OFF_H + L * (SZ_X / 2) + w * 2048;
  v8u pv;
  pv[0] = sb + 32768 + g * 512 + c * 16;
  pv[1] = (c + 16 * (2 * (w & 1) + (g >> 1))) * 16 + 4096 * (g & 1);
  pv[2] = l * 16;
  pv[3] = sb + Q_ST + w * 1024 + c * 4;
  pv[4] = sb + Q_ST + w * 1024 + g * 16;
  pv[5] = pv[6] = pv[7] = 0u;
  const char* dbg = nullptr;
  asm volatile("s_mov_b64 exec, -1\ns_waitcnt vmcnt(0) lgkmcnt(0)\ns_nop 4\n" Q_ASM
               ::"{v[64:71]}"(wq[0]), "{v[72:79]}"(wq[1]), "{v[80:87]}"(wq[2]), "{v[88:95]}"(wq[3]), "{v[96:103]}"(wq[4]),
               "{v[104:111]}"(wq[5]), "{v[112:119]}"(wq[6]), "{v[120:127]}"(wq[7]), "{v[128:135]}"(wk[0]), "{v[136:143]}"(wk[1]),
               "{v[144:151]}"(wk[2]), "{v[152:159]}"(wk[3]), "{v[160:167]}"(wk[4]), "{v[168:175]}"(wk[5]), "{v[176:183]}"(wk[6]),
               "{v[184:191]}"(wk[7]), "{v[192:199]}"(wv[0]), "{v[200:207]}"(wv[1]), "{v[208:215]}"(wv[2]), "{v[216:223]}"(wv[3]),
               "{v[224:231]}"(wv[4]), "{v[232:239]}"(wv[5]), "{v[240:247]}"(wv[6]), "{v[248:255]}"(wv[7]), "{v[0:7]}"(pv),
               "{s[40:43]}"(rsQ), "{s[44:47]}"(rsV), "{s[48:51]}"(rsM), "{s[52:55]}"(rsF), "{s[56:59]}"(rsFQ), "{s[60:61]}"(Xb),
               "{s[62:63]}"(Sb), "{s64}"(NSEG), "{s65}"((h * 16 + (w >> 1)) * 1024), "{s66}"((h * 512 + w * 64) * 16),
               "{s67}"(h * 2048 + (w & 3) * 512), "{s68}"(sb + w * 4096), "{s69}"(sb + Q_ST + w * 1024), "{s70}"(sb + Q_CN),
               "{s71}"(0), "{s72}"(0), "{s73}"(sb + Q_PR + w * 2048), "{s74}"(sb + Q_WP + 2048), "{s75}"(sb + Q_WP + w * 256), "{s[36:37]}"(dbg)
               : "memory", "vcc", "scc", "s88", "v63");
}

constexpr int MEGA_WGS = 256;
constexpr size_t MEGA_LDS = (size_t)(32 * 4 * 64 * 8 + 8 * SMS + 15 * QS + 128 + 16 * 64 * 8 + 8 * SOS + 512) * 2;
static_assert(MEGA_LDS <= 163840, "lds");
static_assert(MEGA_LDS >= (size_t)AW_CNT + 128, "lds");
static_assert(MEGA_LDS >= (size_t)(8 * 2 * CKN + 8 * 2 * CUN + 4 * 32 * 8) * 2, "lds");
static_assert(MEGA_LDS >= (size_t)2 * 32 * SMS * 2 && MEGA_LDS >= 131072, "lds");
static_assert(MEGA_LDS >= (size_t)16 * (6 * NST + 9 * 132) * 4, "lds");
__global__ __launch_bounds__(512, 2) void mega_kernel(const u16* mem0, const u16* Dskip, char* ws, u16* out, const u16* x0,
                                                      const u16* logAre, const u16* Aim, const u16* Cre, const u16* Cim,
                                                      const u16* logdt, const u16* Wq, const u16* Wk, const u16* Wv,
                                                      const u16* Wo) {
  extern __shared__ __attribute__((aligned(16))) char smem[];
  const MegaArgs A{mem0, Dskip, ws, out, x0, logAre, Aim, Cre, Cim, logdt, Wq, Wk, Wv, Wo, vbid()};
  const int b = A.vb;
  {
    const unsigned t0 = now10();
    const int kc = (b >= 32 && b < 64) ? 2 - KC0 : (b >= 200 && b < 232) ? 1 : ((b >= 80 && b < 96) || b >= 240) ? KC0 : -1;
    if (kc >= 0) start_kgen(A, smem, kc, b >= 240 ? b - 224 : b >= 200 ? b - 200 : b >= 80 ? b - 80 : b - 32, b < 64 ? b - 32 : kc == 1 ? b - 168 : -1);
    else if ((b >= 64 && b < 104) || b >= 232) start_stats(A, b >= 232 ? b - 224 : b >= 96 ? b - 96 : b - 48);
    const unsigned dl = (b >= 32 && b < 64) ? DL_C : (b >= 80 && b < 96) ? DL_A : (b >= 200 && b < 232) ? DL_Q : 0u;
    while (now10() - t0 < dl) __builtin_amdgcn_s_sleep(16);
  }
  if ((EXP_OFF & 1) && b >= 64 && b < 80) return;
  if ((EXP_OFF & 8) && b >= 200 && b < 232) return;
  if ((EXP_OFF & 16) && b >= 32 && b < 64) return;
  if ((EXP_OFF & 32) && b >= 80 && b < 96) return;
  if (b < 64) role_chain(A, smem, b >> 5, b & 31, 0);
  else if (b < 96) role_aw(A, smem, (b >> 4) & 1, b & 15);
  else if (b < 104) role_red(A, smem, 0, b - 96);
  else if (b < 168) role_conv(A, smem, b - 104);
  else if (b < 232) role_qkv(A, smem, (b - 168) >> 5, (b - 168) & 31);
  else if (b < 240) role_red(A, smem, 0, b - 224);
  else role_red(A, smem, 1, b - 240);
}

extern "C" void kernel_launch(void* const* d_in, const int* in_sizes, int n_in, void* d_out, int out_size, void* d_ws,
                              size_t ws_size, hipStream_t stream) {
  MegaArgs A;
  A.x0 = (const u16*)d_in[0];
  A.logAre = (const u16*)d_in[1];
  A.Aim = (const u16*)d_in[2];
  A.Cre = (const u16*)d_in[3];
  A.Cim = (const u16*)d_in[4];
  A.logdt = (const u16*)d_in[5];
  A.Dskip = (const u16*)d_in[6];
  A.mem0 = (const u16*)d_in[8];
  A.Wq = (const u16*)d_in[9];
  A.Wk = (const u16*)d_in[10];
  A.Wv = (const u16*)d_in[11];
  A.Wo = (const u16*)d_in[12];
  A.ws = (char*)d_ws;
  A.out = (u16*)d_out;

  (void)hipFuncSetAttribute((const void*)mega_kernel, hipFuncAttributeMaxDynamicSharedMemorySize, (int)MEGA_LDS);
  mega_kernel<<<MEGA_WGS, 512, MEGA_LDS, stream>>>(A.mem0, A.Dskip, A.ws, A.out, A.x0, A.logAre, A.Aim, A.Cre, A.Cim, A.logdt,
                                                   A.Wq, A.Wk, A.Wv, A.Wo);
}
```

```cpp
#include <hip/hip_runtime.h>
#include <hip/hip_bf16.h>
#include <stdint.h>

constexpr int L_SEQ = 2048;
constexpr int DM    = 512;
constexpr int NST   = 128;
constexpr int NH    = 32;
constexpr int DH    = 128;
constexpr int HD    = NH * DH;
constexpr int MM    = 8;
constexpr int SEG   = 32;
constexpr int NSEG  = L_SEQ / SEG;
constexpr int LK    = MM + SEG;

typedef unsigned short u16;
using bf16x8 = __attribute__((ext_vector_type(8))) short;
using f32x4  = __attribute__((ext_vector_type(4))) float;
using f32x16 = __attribute__((ext_vector_type(16))) float;

static __device__ __forceinline__ u16 f2bf(float f) {
  unsigned u = __float_as_uint(f);
  u += 0x7fffu + ((u >> 16) & 1u);
  return (u16)(u >> 16);
}
static __device__ __forceinline__ float bf2f(u16 h) { return __uint_as_float(((unsigned)h) << 16); }
static __device__ __forceinline__ float bfr(float f) { return bf2f(f2bf(f)); }

constexpr size_t SZ_X      = (size_t)L_SEQ * DM * 2;
constexpr size_t OFF_KF    = 0;
constexpr size_t OFF_UT    = OFF_KF + (size_t)DM * L_SEQ * 4;
constexpr size_t OFF_YT    = OFF_UT + SZ_X;
constexpr size_t OFF_XA    = OFF_YT + SZ_X;
constexpr size_t OFF_H     = OFF_XA + SZ_X;
constexpr size_t OFF_X2    = OFF_H + SZ_X;
constexpr size_t OFF_U2    = OFF_X2 + SZ_X;
constexpr size_t OFF_X3    = OFF_U2 + SZ_X;
constexpr size_t SZ_QK     = (size_t)L_SEQ * 2 * HD * 2;
constexpr size_t OFF_QK    = OFF_X3 + SZ_X;
constexpr size_t SZ_O      = (size_t)L_SEQ * HD * 2;
constexpr size_t OFF_VF    = OFF_QK + 2 * SZ_QK;
constexpr size_t SZ_PW     = (size_t)2 * NSEG * 16 * SEG * DM * 2;
constexpr size_t OFF_PW    = OFF_VF + 2 * SZ_O;
constexpr size_t SZ_MEM    = (size_t)NSEG * MM * HD * 2;
constexpr size_t OFF_MEMK  = OFF_PW + SZ_PW;
constexpr size_t OFF_MEMV  = OFF_MEMK + 2 * SZ_MEM;
constexpr size_t OFF_GT    = OFF_MEMV + 2 * SZ_MEM;
constexpr size_t OFF_EP    = OFF_GT + 65536;
constexpr size_t OFF_RD    = OFF_EP + 4096;
constexpr size_t OFF_FS    = OFF_RD + 4096;
constexpr size_t OFF_KG    = OFF_FS + 32768;
constexpr size_t OFF_G     = OFF_KG + 8192;
constexpr size_t SZ_ACC    = (size_t)2 * 32768;
constexpr size_t OFF_Z     = OFF_GT + (size_t)64 * DM * 4;
constexpr size_t OFF_ACC   = OFF_Z;
constexpr size_t OFF_FL    = OFF_ACC + 2 * SZ_ACC;
constexpr int FL_FK  = 0;
constexpr int FL_FW  = FL_FK + 64 * 8 * 32;
constexpr int FL_FR  = FL_FW + 2 * 64 * 16 * 32;
constexpr int FL_FC  = FL_FR + 64 * 16 * 32;
constexpr int FL_FQ  = FL_FC + 2 * 64 * 32 * 32;
constexpr int FL_FZ  = FL_FQ + 2 * 64 * 32 * 32;
constexpr int FL_TMO = FL_FZ + 16 * 32;
constexpr int FL_END = FL_TMO + 64;
constexpr size_t Z_BYTES   = OFF_FL + (size_t)FL_END * 4 - OFF_Z;
constexpr size_t WS_TOTAL  = OFF_Z + Z_BYTES;
static_assert(Z_BYTES % 16 == 0 && OFF_Z % 256 == 0 && Z_BYTES <= 4 * 64 * 8192 && OFF_G + 4096 <= OFF_Z, "zero region");
static_assert(WS_TOTAL + (4 << 20) <= (size_t)256 * 1024 * 1024, "workspace too large");

static_assert(WS_TOTAL - OFF_XA <= (size_t)384 * 8 * 65536, "translation warm-up");
constexpr int CKN = 2208;
static_assert((size_t)DM * 2 * CKN * 2 <= (size_t)DM * L_SEQ * 4 + SZ_X, "K buffer");
__device__ __forceinline__ float pos_of_index(int p) {
  if (p < 256) return (float)p;
  if (p < 384) return (float)(256 + 2 * (p - 256));
  if (p < 512) return (float)(512 + 4 * (p - 384));
  if (p < 640) return (float)(1024 + 8 * (p - 512));
  return 2048.0f;
}
__device__ __forceinline__ int index_of_l(int l) {
  float pf = bfr((float)l);
  int p = (int)pf;
  if (p < 256) return p;
  if (p < 512) return 256 + ((p - 256) >> 1);
  if (p < 1024) return 384 + ((p - 512) >> 2);
  if (p < 2048) return 512 + ((p - 1024) >> 3);
  return 640;
}

template <int CTRL> __device__ __forceinline__ float dppr(float x) {
  return __builtin_bit_cast(float, __builtin_amdgcn_update_dpp(0, __builtin_bit_cast(int, x), CTRL, 0xf, 0xf, false));
}
__device__ __forceinline__ float wave_sum(float v) {
  v += dppr<0x121>(v);
  v += dppr<0x122>(v);
  v += dppr<0x124>(v);
  v += dppr<0x128>(v);
  {
    auto r = __builtin_amdgcn_permlane16_swap(__float_as_uint(v), __float_as_uint(v), false, false);
    v = __uint_as_float(r[0]) + __uint_as_float(r[1]);
  }
  {
    auto r = __builtin_amdgcn_permlane32_swap(__float_as_uint(v), __float_as_uint(v), false, false);
    v = __uint_as_float(r[0]) + __uint_as_float(r[1]);
  }
  return v;
}
__device__ __forceinline__ void ln_row8(float (&v)[8]) {
  float s = 0.f;
#pragma unroll
  for (int j = 0; j < 8; ++j) s += v[j];
  s = wave_sum(s);
  const float mean = s * (1.0f / DM);
  float q = 0.f;
#pragma unroll
  for (int j = 0; j < 8; ++j) { float t = v[j] - mean; q += t * t; }
  q = wave_sum(q);
  const float var = q * (1.0f / DM);
  const float mb = bfr(mean);
  const float sd = bfr(sqrtf(bfr(bfr(var) + 1e-5f)));
#pragma unroll
  for (int j = 0; j < 8; ++j) v[j] = bfr(bfr(v[j] - mb) / sd);
}

#define RLX_AGENT __ATOMIC_RELAXED, __HIP_MEMORY_SCOPE_AGENT
typedef __attribute__((address_space(1))) unsigned gu32;
typedef unsigned v4u __attribute__((ext_vector_type(4)));
constexpr int AUX_SC1 = 16;
constexpr unsigned SPIN_CAP = 1u << 18;

__device__ __forceinline__ bool wait_flags(const unsigned* flags, int lane, int nfl, unsigned want, unsigned* tmo) {
  for (unsigned spins = 0;; ++spins) {
    unsigned v = want;
    if (lane < nfl) v = __hip_atomic_load((gu32*)flags + lane * 32, RLX_AGENT);
    if (__all(v >= want)) break;
    if (spins > SPIN_CAP) {
      if (lane == 0) __hip_atomic_store((gu32*)tmo, 1u, RLX_AGENT);
      return false;
    }
  }
  __builtin_amdgcn_fence(__ATOMIC_ACQUIRE, "wavefront");
  return true;
}

#define STAMP(i)
#define STAMP_DUMP()
#define WSTAMP(i)
template <int CTRL> __device__ __forceinline__ float dppf(float old, float x) {
  return __builtin_bit_cast(float, __builtin_amdgcn_update_dpp(__builtin_bit_cast(int, old), __builtin_bit_cast(int, x), CTRL, 0xf, 0xf, false));
}
__device__ __forceinline__ float wave_max_dpp(float x) {
  const float ninf = -3e38f;
  x = fmaxf(x, dppf<0x111>(ninf, x));
  x = fmaxf(x, dppf<0x112>(ninf, x));
  x = fmaxf(x, dppf<0x114>(ninf, x));
  x = fmaxf(x, dppf<0x118>(ninf, x));
  x = fmaxf(x, dppf<0x142>(ninf, x));
  x = fmaxf(x, dppf<0x143>(ninf, x));
  return __builtin_bit_cast(float, __builtin_amdgcn_readlane(__builtin_bit_cast(int, x), 63));
}
__device__ __forceinline__ float wave_sum_dpp(float x) {
  x += dppf<0x111>(0.f, x);
  x += dppf<0x112>(0.f, x);
  x += dppf<0x114>(0.f, x);
  x += dppf<0x118>(0.f, x);
  x += dppf<0x142>(0.f, x);
  x += dppf<0x143>(0.f, x);
  return __builtin_bit_cast(float, __builtin_amdgcn_readlane(__builtin_bit_cast(int, x), 63));
}
constexpr int SMS = 528;
constexpr int QS = 144;
constexpr int VS = 80;
constexpr int SOS = 144;
#define VAR_NREP 4
constexpr int NREP = VAR_NREP;
__device__ __forceinline__ float xsum16(float x) {
  auto r = __builtin_amdgcn_permlane16_swap(__float_as_uint(x), __float_as_uint(x), false, false);
  return __uint_as_float(r[0]) + __uint_as_float(r[1]);
}
__device__ __forceinline__ float xsum32(float x) {
  auto r = __builtin_amdgcn_permlane32_swap(__float_as_uint(x), __float_as_uint(x), false, false);
  return __uint_as_float(r[0]) + __uint_as_float(r[1]);
}
__device__ __forceinline__ float xmax16(float x) {
  auto r = __builtin_amdgcn_permlane16_swap(__float_as_uint(x), __float_as_uint(x), false, false);
  return fmaxf(__uint_as_float(r[0]), __uint_as_float(r[1]));
}
__device__ __forceinline__ float xmax32(float x) {
  auto r = __builtin_amdgcn_permlane32_swap(__float_as_uint(x), __float_as_uint(x), false, false);
  return fmaxf(__uint_as_float(r[0]), __uint_as_float(r[1]));
}
__device__ __forceinline__ float red_mod4(float x) {
  x += dppf<0x124>(0.f, x);
  x += dppf<0x128>(0.f, x);
  return xsum32(xsum16(x));
}
typedef __bf16 bf16x2_t __attribute__((ext_vector_type(2)));
typedef float f32x2_t __attribute__((ext_vector_type(2)));
__device__ __forceinline__ unsigned pk2(float a, float b) {
  f32x2_t v = {a, b};
  bf16x2_t r = __builtin_convertvector(v, bf16x2_t);
  return __builtin_bit_cast(unsigned, r);
}
__device__ __forceinline__ float one(float x) {
  asm("" : "+v"(x));
  return x;
}
__device__ __forceinline__ unsigned pk2s(float a, float b) { return pk2(one(a), one(b)); }
__device__ __forceinline__ float dot2sq(const unsigned x, const float acc) {
  return __builtin_amdgcn_fdot2_f32_bf16(__builtin_bit_cast(bf16x2_t, x), __builtin_bit_cast(bf16x2_t, x), acc, false);
}
__device__ __forceinline__ float lo16(unsigned u) { return __uint_as_float(u << 16); }
__device__ __forceinline__ float hi16(unsigned u) { return __uint_as_float(u & 0xffff0000u); }
__device__ __forceinline__ void scale_pair(float a, float b, float scale, float& oa, float& ob) {
  unsigned u = pk2(a, b);
  unsigned v = pk2s(lo16(u) * scale, hi16(u) * scale);
  oa = lo16(v);
  ob = hi16(v);
}

#define XMAP 2
__device__ __forceinline__ int vbid() {
  const int b = blockIdx.x, x = b & 7, t = b >> 3;
  if (XMAP == 0) {
    if (b < 64) return (b & 32) + 4 * x + (t & 3);
    if (b < 96) return (b & ~15) + 2 * x + (t & 1);
    if (b >= 104 && b < 168) return 104 + 8 * x + t - 13;
    if (b >= 168 && b < 232) return 168 + ((b - 168) & 32) + 4 * x + ((t - 21) & 3);
    return b;
  }
  if (XMAP == 2) {
    if (x < 4) return t < 16 ? 4 * (t & 8) + 8 * x + (t & 7) : 104 + 16 * x + t - 16;
    const int y = x - 4;
    if (t >= 24) return (t >= 28 ? 240 : y < 2 ? 96 : 224) + 4 * y + (t & 3);
    if (t < 16) return 168 + 4 * (t & 8) + 8 * y + (t & 7);
    return 64 + 4 * (t & 4) + 4 * y + (t & 3);
  }
  if (x < 2) return 2 * (t & 16) + 16 * x + (t & 15);
  const int i = 16 * (x - 2) + (t & 15);
  if (t < 16) {
    const int k = (i * 171) >> 9, r = i - 3 * k;
    return r == 0 ? 168 + k : r == 1 ? 200 + k : 64 + 16 * (k & 1) + (k >> 1);
  }
  const int g = i >> 3, e = i & 7;
  if (g == 3) return 96 + e;
  if (g == 7) return 232 + e;
  if (g == 9) return 240 + e;
  if (g == 11) return 248 + e;
  return 104 + 8 * (g - (g > 3) - (g > 7) - (g > 9)) + e;
}
#define MSTAMP()
#define WSTAMP2()
#define SST(k)
#define TS(k, ss, t)
#define LS(k, ss)
#define LS_DUMP()
struct MegaArgs {
  const u16* mem0;
  const u16* Dskip;
  char* ws;
  u16* out;
  const u16* x0;
  const u16 *logAre, *Aim, *Cre, *Cim, *logdt;
  const u16 *Wq, *Wk, *Wv, *Wo;
  int vb;
};

__device__ __forceinline__ int lane_id() {
  int l;
  asm volatile("v_mbcnt_lo_u32_b32 %0, -1, 0\n\tv_mbcnt_hi_u32_b32 %0, -1, %0" : "=v"(l));
  return l;
}
__device__ __forceinline__ bool spin_timeout(unsigned spins, unsigned* tmo) {
  if (spins > SPIN_CAP) {
    if (lane_id() == 0) __hip_atomic_store((gu32*)tmo, 1u, RLX_AGENT);
    return true;
  }
  return false;
}
#define ROT_H (4 * (h & 1))
#define PNL 8
#define GSL 12
#define AWDIR 1
#define AWAT 0
#define AWRP 0
#define PEPS 4
#define FGATE 0
#define DL_C 0
#define DL_A 800
#define DL_R 900
#define DL_Q 0
#define KC0 0
#define CPRIO 1
#define P1PRIO 3
#define QLEAD 2
#define POLL_SLEEP 12
#define SLP_C POLL_SLEEP
#define SLP_A POLL_SLEEP
#define SLP_W POLL_SLEEP
#define SLP_R POLL_SLEEP
#define SLP_V POLL_SLEEP
#define SLP_Q POLL_SLEEP
#define SLP_M 4
#define AWPRIO 2
#define SLP_S 8
#define SPOS 4
#define CVPRE 4
#define QPD 3
#define QPRI 2
#define TLBW 1
#define QHV 0
#define QCHK 0
#define LEADN 99
#define GATE_V 170
#define GATE_R 380
#define GATE_N 380
#define AWAHEAD 1
#define GATE_M 150
#define PRI_A 3
#define PRI_WA 2
#define PRI_WB 1
#define AWSKIP 3
#define PRI_0 3
#define AWDEF 0
#define SLP_F 2
#define EXP_OFF 0
#define AWSPEC 1
#define CVTHR 48
#define NR_C 8
#define NR_R 8
#define NR_W 8
#define NR_N 2
#define QAHEAD 0
#define NS0 4
#define SLP_N POLL_SLEEP
template <int SLP, int WPL>
__device__ __forceinline__ bool wait_n(const unsigned* f, int n, unsigned* tmo) {
  const int l = lane_id();
  for (unsigned spins = 0;; ++spins) {
    unsigned v = 1u;
    if (l < n) v = __hip_atomic_load((gu32*)f + (l / WPL) * 32 + l % WPL, RLX_AGENT);
    if (__all(v != 0u)) break;
    if (spin_timeout(spins, tmo)) return false;
    __builtin_amdgcn_s_sleep(SLP);
  }
  asm volatile("" ::: "memory");
  return true;
}
__device__ __forceinline__ unsigned now10() { return (unsigned)__builtin_amdgcn_s_memrealtime(); }
__device__ __forceinline__ void gate(const unsigned t) {
  for (int i = 0; i < 64; ++i) {
    const int d = (int)(t - now10());
    if (d <= 0) break;
    if (d > 100) __builtin_amdgcn_s_sleep(32);
    else if (d > 25) __builtin_amdgcn_s_sleep(8);
    else __builtin_amdgcn_s_sleep(2);
  }
}
template <int SLP, int WPL, int GT>
__device__ __forceinline__ void grp_wait(const bool leader, volatile unsigned* sw, const unsigned val, const unsigned* f, int n,
                                         unsigned* tmo, unsigned& tl) {
  if (leader) {
    const int l = lane_id();
    for (unsigned spins = 0;; ++spins) {
      unsigned v = 1u;
      if (l < n) v = __hip_atomic_load((gu32*)f + (l / WPL) * 32 + l % WPL, RLX_AGENT);
      if (__all(v != 0u) || spin_timeout(spins, tmo)) break;
      if (GT && spins == 0) gate(tl);
      else __builtin_amdgcn_s_sleep(SLP);
    }
    if (GT) tl = now10() + GT;
    if ((threadIdx.x & 63) == 0) *sw = val;
  } else {
    for (unsigned spins = 0; *sw < val; ++spins) {
      if (spins > 64u * SPIN_CAP) break;
      if (FGATE && GT && spins == 0) gate(tl);
      __builtin_amdgcn_s_sleep(SLP_F);
    }
    if (FGATE && GT) tl = now10() + GT;
  }
  asm volatile("" ::: "memory");
}
template <int SLP, int WPL>
__device__ __forceinline__ bool wg_wait(const unsigned* f, int n, unsigned* tmo) {
  if (threadIdx.x < 64) (void)wait_n<SLP, WPL>(f, n, tmo);
  __syncthreads();
  asm volatile("" ::: "memory");
  return true;
}
__device__ __forceinline__ void set_flag(unsigned* f) { __hip_atomic_store((gu32*)f, 1u, RLX_AGENT); }
#define DRAIN() asm volatile("s_waitcnt vmcnt(0)" ::: "memory")
#define MKRS(ptr, bytes) __builtin_amdgcn_make_buffer_rsrc((void*)(ptr), 0, (int)(bytes), 0x00020000)
typedef unsigned v2u __attribute__((ext_vector_type(2)));
#define LD128(rs, voff, soff) __builtin_amdgcn_raw_buffer_load_b128(rs, voff, soff, AUX_SC1)
#define LD64(rs, voff, soff) __builtin_amdgcn_raw_buffer_load_b64(rs, voff, soff, AUX_SC1)
#define ST128(v, rs, voff, soff)                                              \
  do {                                                                        \
    const v4u st_d_ = (v);                                                    \
    __builtin_amdgcn_raw_buffer_store_b128(st_d_, rs, voff, soff, AUX_SC1);   \
    asm volatile("s_nop 1" ::"v"(st_d_) : "memory");                          \
  } while (0)
#define ST64(v, rs, voff, soff) __builtin_amdgcn_raw_buffer_store_b64(v, rs, voff, soff, AUX_SC1)
#define BF8(x) __builtin_bit_cast(bf16x8, x)
#define MFMA16(a, b, c) __builtin_amdgcn_mfma_f32_16x16x32_bf16(a, b, c, 0, 0, 0)
typedef short bf16x4 __attribute__((ext_vector_type(4)));
#define MFMA16K(a, b, c) \
  __builtin_amdgcn_mfma_f32_16x16x16bf16_1k(__builtin_bit_cast(bf16x4, a), __builtin_bit_cast(bf16x4, b), c, 0, 0, 0)
__device__ __forceinline__ float fmax_nn(float a, float b) { return __builtin_elementwise_maximum(a, b); }
typedef __bf16 bfv8 __attribute__((ext_vector_type(8)));
typedef __bf16 bfv16 __attribute__((ext_vector_type(16)));
typedef unsigned v8u __attribute__((ext_vector_type(8)));
#define SMF(a, b, c, idx) \
  __builtin_amdgcn_smfmac_f32_16x16x64_bf16(__builtin_bit_cast(bfv8, a), __builtin_bit_cast(bfv16, b), c, idx, 0, 0)
__device__ __forceinline__ float swap_add32(float a, float b) {
  auto r = __builtin_amdgcn_permlane32_swap(__float_as_uint(a), __float_as_uint(b), false, false);
  return __uint_as_float(r[0]) + __uint_as_float(r[1]);
}
__device__ __forceinline__ float swap_add16(float a, float b) {
  auto r = __builtin_amdgcn_permlane16_swap(__float_as_uint(a), __float_as_uint(b), false, false);
  return __uint_as_float(r[0]) + __uint_as_float(r[1]);
}
__device__ __forceinline__ v4u smf_fix(const v4u d) {
  auto x = __builtin_amdgcn_permlane16_swap(d.x, d.z, false, false);
  auto y = __builtin_amdgcn_permlane16_swap(d.y, d.w, false, false);
  return v4u{x[0], x[1], y[0], y[1]};
}
__device__ __forceinline__ v8u smf_bfrag(const u16* p) {
  const v2u a = *reinterpret_cast<const v2u*>(p), b = *reinterpret_cast<const v2u*>(p + 8);
  const v2u c = *reinterpret_cast<const v2u*>(p + 32), d = *reinterpret_cast<const v2u*>(p + 40);
  return v8u{a.x, b.x, a.y, b.y, c.x, d.x, c.y, d.y};
}

#define VAR_PPA 0
#define VAR_PPB 0
#define VAR_THR 0
#define VAR_THR2 0
#define CV_LEAD 100
#define VAR_EARLY 1
#define VAR_DRAIN 1
#define VAR_D1 0
#define VAR_D2 0
#define VAR_DR2 1
#define VAR_S1 0
#define VAR_S2 0
typedef __attribute__((address_space(1))) unsigned long long gu64;
typedef __attribute__((address_space(1))) char gch;
__device__ __forceinline__ int cvt_rpi(float x) {
  int r;
  asm("v_cvt_rpi_i32_f32 %0, %1" : "=v"(r) : "v"(x));
  return r;
}
typedef unsigned long long u64;
__device__ __forceinline__ u64 call_tag(const MegaArgs& A) {
  u64 t = *(const __attribute__((address_space(4))) u64*)(A.ws + OFF_EP) * 6364136223846793005ull + 1442695040888963407ull;
  asm volatile("" : "+s"(t)::"memory");
  return t;
}
__device__ __forceinline__ u64 ready_look(const MegaArgs& A) {
  return __hip_atomic_load((gu64*)(A.ws + OFF_RD) + (blockIdx.x & 7) * 64 + lane_id(), RLX_AGENT);
}
__device__ __forceinline__ void ready_wait(const MegaArgs& A, const u64 tag, u64 v) {
  for (unsigned spins = 0; !__all(v == tag) && spins < SPIN_CAP; ++spins) {
    __builtin_amdgcn_s_sleep(8);
    v = ready_look(A);
  }
  asm volatile("" ::: "memory");
}
typedef short v4s __attribute__((ext_vector_type(4)));
typedef __attribute__((address_space(3))) v4s* lp4s;
__device__ __forceinline__ void glds16(const void* sbase, const unsigned voff, const unsigned lds) {
  unsigned keep;
  asm volatile("s_nop 4\n\ts_mov_b32 %0, m0\n\ts_mov_b32 m0, %3\n\ts_nop 0\n\tglobal_load_lds_dwordx4 %1, %2\n\ts_mov_b32 m0, %0"
               : "=&s"(keep)
               : "v"(voff), "s"(sbase), "s"(lds)
               : "memory");
}
__device__ __forceinline__ void glds16c(const void* sbase, const unsigned voff, const unsigned lds) {
  unsigned keep;
  asm volatile("s_nop 4\n\ts_mov_b32 %0, m0\n\ts_mov_b32 m0, %3\n\ts_nop 0\n\tglobal_load_lds_dwordx4 %1, %2 sc1\n\ts_mov_b32 m0, %0"
               : "=&s"(keep)
               : "v"(voff), "s"(sbase), "s"(lds)
               : "memory");
}
__device__ __forceinline__ v2u trd(const unsigned a) {
  return __builtin_bit_cast(v2u, __builtin_amdgcn_ds_read_tr16_b64_v4i16((lp4s)(unsigned long)a));
}
__device__ __forceinline__ unsigned lds_base(char* smem) { return (unsigned)(unsigned long)(__attribute__((address_space(3))) char*)smem; }
#define RAW_BAR()                                           \
  do {                                                      \
    asm volatile("s_waitcnt lgkmcnt(0)" ::: "memory");      \
    __builtin_amdgcn_s_barrier();                           \
    asm volatile("" ::: "memory");                          \
  } while (0)
#define WAIT_VM(n) asm volatile("s_waitcnt vmcnt(" #n ")" ::: "memory")
template <bool SP>
__device__ __forceinline__ void wqkv_issue(const u16* Wm, const int hf, const int h, const int w, const int l, const unsigned sb) {
  const char* base = (const char*)Wm + (size_t)(256 * hf + 32 * w) * (HD * 2) + h * (DH * 2);
#pragma unroll
  for (int i = 0; i < 8; ++i) {
    const int r = l >> 4;
    const int f = SP ? ((r & 1) | ((i & 1) << 1) | (((i >> 1) & 1) << 2)) : (r | (((i >> 1) & 1) << 2));
    glds16(base, (unsigned)((4 * i + r) * (HD * 2) + (((l & 15) ^ (f << 1)) << 4)), sb + w * 16384 + hf * 8192 + i * 1024);
  }
}

__device__ __forceinline__ unsigned row_stats(const v4u raw) {
  float v[8];
#pragma unroll
  for (int e = 0; e < 4; ++e) { v[2 * e] = lo16(raw[e]); v[2 * e + 1] = hi16(raw[e]); }
  float s = 0.f;
#pragma unroll
  for (int e = 0; e < 8; ++e) s += v[e];
  const float mean = wave_sum(s) * (1.0f / DM);
  float q = 0.f;
#pragma unroll
  for (int e = 0; e < 8; ++e) { const float t = v[e] - mean; q += t * t; }
  const float var = wave_sum(q) * (1.0f / DM);
  const float sd = bfr(sqrtf(bfr(bfr(var) + 1e-5f)));
  return (unsigned)f2bf(mean) | ((unsigned)f2bf(sd) << 16);
}
__device__ __forceinline__ void ask_slow(const MegaArgs& A, const int k, unsigned (&t)[3]) {
  const int l = lane_id(), x = blockIdx.x & 7;
  const char *wq = (const char*)A.Wq, *wv = (const char*)A.Wv;
  asm("" : "+s"(wq), "+s"(wv));
#pragma unroll
  for (int m = 0; m < 3; ++m) {
    const char* W = m == 1 ? (const char*)A.Wk : ((m == 0) == (x < 4)) ? wv : wq;
    const int a = (int)((unsigned long)W >> 8) + 8 * (x & 3), h = k < 32 ? ((1 - a) & 3) + 4 * (k & 1) : (31 - a) & 31;
    const int r = k < 32 ? k >> 1 : k - 32;
    t[m] = __builtin_amdgcn_raw_buffer_load_b32(MKRS(W, (unsigned)k < 48u && h < 8 ? DM * HD * 2 : 0), (l >> 1) * (HD * 2) + (l & 1) * 128,
                                                (32 * (r & 15)) * (HD * 2) + (8 * (x & 3) + (h & 7)) * 256, 0);
  }
}
#define ASKED(t) asm volatile("" ::"v"(t[0]), "v"(t[1]), "v"(t[2]))
__device__ __forceinline__ u64 start_stats(const MegaArgs& A, const int idx) {
  const int w = __builtin_amdgcn_readfirstlane(threadIdx.x >> 6), l = threadIdx.x & 63;
  const int r0 = 32 * idx + 4 * w;
  unsigned ta[3];
  v4u raw[4];
#pragma unroll
  for (int i = 0; i < 4; ++i) raw[i] = *reinterpret_cast<const v4u*>(A.x0 + (size_t)(r0 + i) * DM + l * 8);
  asm volatile("" ::: "memory");
  ask_slow(A, 16 * w + (idx & 3) + (idx >> 4) * 4, ta);
  const u64 tag = call_tag(A);
  unsigned stw = 0u;
#pragma unroll
  for (int i = 0; i < 4; ++i) {
    const unsigned t = row_stats(raw[i]);
    if (l == i) stw = t;
  }
  if (l < 4) __hip_atomic_store((gu32*)(A.ws + OFF_GT) + r0 + l, stw, RLX_AGENT);
  DRAIN();
  ASKED(ta);
  if (l < 4) __hip_atomic_store((gu64*)(A.ws + OFF_FS) + (l * 64 + idx) * 16 + w, tag, RLX_AGENT);
  SST(0);
  return tag;
}
struct Cx {
  f32x2_t r, i;
};
__device__ __forceinline__ Cx cmul(const Cx a, const Cx b) { return Cx{a.r * b.r - a.i * b.i, a.r * b.i + a.i * b.r}; }
struct KGen {
  unsigned pa, pb, pc, pd;
  u16 pe;
  f32x2_t ar, ai;
  Cx C;
  char* st;
  v4u bh[8], bl[8];
  __device__ __forceinline__ void load(const MegaArgs& A, const int d, const int l) {
    pa = *reinterpret_cast<const unsigned*>(A.logAre + d * NST + 2 * l);
    pb = *reinterpret_cast<const unsigned*>(A.Aim + d * NST + 2 * l);
    pc = *reinterpret_cast<const unsigned*>(A.Cre + d * NST + 2 * l);
    pd = *reinterpret_cast<const unsigned*>(A.Cim + d * NST + 2 * l);
    pe = A.logdt[d];
  }
  __device__ __forceinline__ void setup() {
    const float dt = bfr(expf(bf2f(pe)));
    ar = f32x2_t{-bfr(expf(lo16(pa))) * dt, -bfr(expf(hi16(pa))) * dt};
    ai = f32x2_t{lo16(pb) * dt, hi16(pb) * dt};
    C = Cx{f32x2_t{lo16(pc) * dt, hi16(pc) * dt}, f32x2_t{lo16(pd) * dt, hi16(pd) * dt}};
  }
  __device__ __forceinline__ Cx power(const float p) const {
    Cx z;
#pragma unroll
    for (int e = 0; e < 2; ++e) {
      const float x = ai[e] * p;
      const float f = __builtin_fmaf(x, 6.4206382e-9f, __builtin_fmaf(x, 0.15915494f, -__builtin_rintf(x * 0.15915494f)));
      const float m = __expf(ar[e] * p);
      z.r[e] = m * __builtin_amdgcn_cosf(f);
      z.i[e] = m * __builtin_amdgcn_sinf(f);
    }
    return z;
  }
  __device__ __forceinline__ char* rows(char* p, Cx y, const Cx s, const int n) const {
#pragma unroll 1
    for (int i = 0; i < n; i += 2) {
#pragma unroll
      for (int k = 0; k < 2; ++k) {
        const unsigned h0 = pk2(y.r.x, y.r.y), h1 = pk2(y.i.x, y.i.y);
        const f32x2_t a = y.r - f32x2_t{lo16(h0), hi16(h0)}, b = y.i - f32x2_t{lo16(h1), hi16(h1)};
        *reinterpret_cast<v2u*>(p + 1040 * k) = v2u{h0, h1};
        *reinterpret_cast<v2u*>(p + 1040 * k + 512) = v2u{pk2(a.x, a.y), pk2(b.x, b.y)};
        y = cmul(y, s);
      }
      p += 2080;
    }
    return p;
  }
  __device__ __forceinline__ void get(v4u (&h)[8], v4u (&o)[8], const int l) const {
    const char* p = st + (l & 15) * 1040 + (l >> 4) * 16;
    asm volatile("" ::: "memory");
#pragma unroll
    for (int k = 0; k < 8; ++k) {
      h[k] = *reinterpret_cast<const v4u*>(p + 64 * k);
      o[k] = *reinterpret_cast<const v4u*>(p + 512 + 64 * k);
    }
    asm volatile("" ::: "memory");
  }
  __device__ __forceinline__ void columns(const int l) {
    Cx s = power(16.f);
    s.i = -s.i;
    rows(st + l * 8, Cx{f32x2_t{1.f, 1.f}, f32x2_t{0.f, 0.f}}, s, 16);
    get(bh, bl, l);
  }
  __device__ __forceinline__ v4u tile(const int l) const {
    v4u h[8], o[8];
    get(h, o, l);
    f32x4 d = {}, e = {}, f = {};
#pragma unroll
    for (int k = 0; k < 8; ++k) {
      d = MFMA16(BF8(h[k]), BF8(bh[k]), d);
      e = MFMA16(BF8(o[k]), BF8(bh[k]), e);
      f = MFMA16(BF8(h[k]), BF8(bl[k]), f);
    }
    d = (d + (e + f)) * 2.0f;
    v4u r;
#pragma unroll
    for (int i = 0; i < 4; ++i) r[i] = pk2(d[i], d[i] - lo16(pk2(d[i], d[i])));
    return r;
  }
};
__device__ __forceinline__ void start_kgen(const MegaArgs& A, char* smem, const int kg) {
  const int tid = threadIdx.x, w = __builtin_amdgcn_readfirstlane(tid >> 6), l = tid & 63, c = l & 15, g = l >> 4;
  KGen G;
  G.load(A, 8 * kg + w, l);
  {
    const __amdgpu_buffer_rsrc_t rz = MKRS(A.ws + OFF_Z, Z_BYTES);
#pragma unroll
    for (int i = 0; i < 4; ++i) ST128((v4u{0u, 0u, 0u, 0u}), rz, tid * 16, (i * 64 + kg) * 8192);
  }
  const u64 tag = call_tag(A);
  DRAIN();
  __syncthreads();
  if (tid < 8) __hip_atomic_store((gu64*)(A.ws + OFF_RD) + tid * 64 + kg, tag, RLX_AGENT);
  asm volatile("" : "+v"(G.pa), "+v"(G.pb), "+v"(G.pe));
  G.setup();
  G.st = smem + w * 16640;
  const __amdgpu_buffer_rsrc_t rs = MKRS(A.ws + OFF_KF + (8 * kg + w) * 2048, 386 * 4);
  SST(1);
  unsigned tw = 0u;
  if (TLBW && tid < 384) {
    const unsigned o = (unsigned)OFF_XA + (unsigned)(tid * 8 + ((blockIdx.x >> 3) & 7)) * 65536u;
    if (o < WS_TOTAL) asm volatile("global_load_dword %0, %1, %2" : "=v"(tw) : "v"(o), "s"(A.ws) : "memory");
  }
  G.columns(l);
  const Cx zb = G.power(256.f);
  Cx b = G.C, z = G.power(2.f);
#pragma unroll 1
  for (int f = 1; f < 9;) {
    char* p = G.st + l * 8;
    const int fe = f == 1 ? 4 : 9;
#pragma unroll 1
    for (; f < fe; ++f) {
      b = cmul(b, zb);
      if (f == 2 || f == 4) z = cmul(z, z);
      p = G.rows(p, b, z, f == 1 ? 8 : f < 4 ? 4 : 2);
    }
    const v4u r = G.tile(l);
    if (f == 4) {
      ST128(r, rs, (g < 2 ? 8 * c + 4 * g : 64 * g + 4 * c) * 4, 0);
    } else {
      ST64((v2u{r.x, r.y}), rs, (256 + 64 * g + 2 * c) * 4, 0);
      ST64((v2u{r.z, r.w}), rs, (288 + 64 * g + 2 * c) * 4, 0);
    }
  }
  SST(2);
  DRAIN();
  asm volatile("" ::"v"(tw));
  if (l == 0) __hip_atomic_store((gu64*)(A.ws + OFF_KG) + kg * 16 + w, tag, RLX_AGENT);
  __syncthreads();
  SST(3);
}

constexpr int C_SM = 131072, C_SO = C_SM + 8192, C_KT = C_SO + 2048, C_X = C_KT + 16384, C_SQ = C_X + 1024;
constexpr int C_SK = C_SQ + (7 * QS + 128) * 2;
static_assert(C_SK + 8 * QS * 2 <= 163840, "lds");
#define TDSLOT 0
#define CHAIN_ASM_ATT \
  "s_mov_b32 s92, 1152\n" \
  "s_mov_b32 s93, 576\n" \
  "s_mov_b32 s94, 2272\n" \
  "s_mov_b32 s95, 272\n" \
  "s_mov_b32 s96, 288\n" \
  "s_mov_b32 s91, 0x40000001\n" \
  "s_mov_b32 s84, 0x4b400000\n" \
  "s_mov_b32 s85, 0x4b400000\n" \
  "s_mov_b32 s78, 0x3e0293ee\n" \
  "s_mov_b32 s76, 0xff00ff00\n" \
  "s_mov_b32 s77, 0xff00ff00\n" \
  "s_add_u32 s58, s56, s60\n" \
  "s_addc_u32 s59, s57, 0\n" \
  "s_lshl_b32 s98, s73, 10\n" \
  "s_add_u32 s98, s98, s74\n" \
  "s_add_u32 s98, s98, 131072\n" \
  "s_lshl_b32 s97, s73, 5\n" \
  "s_add_u32 s97, s97, s74\n" \
  "s_add_u32 s97, s97, 158720\n" \
  "s_lshl_b32 s99, s73, 10\n" \
  "v_mbcnt_lo_u32_b32 v48, -1, 0\n" \
  "v_mbcnt_hi_u32_b32 v48, -1, v48\n" \
  "v_and_b32 v49, 15, v48\n" \
  "v_lshl_add_u32 v8, v49, 1, s97\n" \
  "v_bfe_u32 v49, v48, 4, 1\n" \
  "v_mad_u32_u24 v8, v49, s92, v8\n" \
  "v_lshrrev_b32 v49, 5, v48\n" \
  "v_mad_u32_u24 v9, v49, s93, v8\n" \
  "v_mad_u32_u24 v8, v49, s94, v8\n" \
  "v_add_u32 v10, s99, v1\n" \
  "s_lshr_b32 s88, s99, 2\n" \
  "s_add_u32 s88, s88, s74\n" \
  "s_add_u32 s88, s88, 139264\n" \
  "v_lshl_add_u32 v7, v49, 10, s88\n" \
  "v_and_b32 v49, 31, v48\n" \
  "v_lshl_add_u32 v7, v49, 3, v7\n" \
  "v_mov_b64 v[12:13], 0\n" \
  "v_mov_b64 v[14:15], 0\n" \
  "v_mov_b64 v[16:17], 0\n" \
  "v_mov_b64 v[18:19], 0\n" \
  "v_mov_b64 v[20:21], 0\n" \
  "v_mov_b64 v[22:23], 0\n" \
  "s_add_u32 s88, s74, 157760\n" \
  "v_mov_b32 v59, s88\n" \
  "v_mov_b32 v24, 0x44870000\n" \
  "v_mul_f32 v24, s72, v24\n" \
  "s_nop 0\n" \
  "v_readfirstlane_b32 s86, v24\n" \
  "s_lshr_b32 s86, s86, 23\n" \
  "s_addk_i32 s86, 0xff83\n" \
  "s_ashr_i32 s86, s86, 1\n" \
  "s_max_i32 s86, s86, -20\n" \
  "s_min_i32 s86, s86, 100\n" \
  "s_mov_b32 s82, s86\n" \
  "s_lshr_b32 s83, s72, 23\n" \
  "s_sub_i32 s83, s83, 242\n" \
  "s_lshl_b32 s88, s73, 10\n" \
  "v_add_u32 v24, s88, v1\n" \
  "ds_read_b128 v[32:35], v24 offset:0\n" \
  "s_waitcnt lgkmcnt(0)\n" \
  "v_mov_b32 v11, 0\n" \
  "v_dot2c_f32_bf16 v11, v32, v32\n" \
  "v_dot2c_f32_bf16 v11, v33, v33\n" \
  "v_dot2c_f32_bf16 v11, v34, v34\n" \
  "v_dot2c_f32_bf16 v11, v35, v35\n" \
  "s_nop 1\n" \
  "s_nop 0\n" \
  "v_cmpx_lt_f32 vcc, 2.0, v11\n" \
  "ds_max_u32 v59, v11\n" \
  "s_mov_b64 exec, -1\n" \
  ".LCA_TOP_%=:\n" \
  "s_waitcnt lgkmcnt(0)\n" \
  "s_barrier\n" \
  "ds_read_b128 v[40:43], v1 offset:0\n" \
  "ds_read_b128 v[44:47], v1 offset:1024\n" \
  "ds_read_b128 v[48:51], v1 offset:2048\n" \
  "ds_read_b128 v[52:55], v1 offset:3072\n" \
  "ds_read_b128 v[56:59], v1 offset:4096\n" \
  "ds_read_b128 v[60:63], v1 offset:5120\n" \
  "s_cmp_lg_u32 s73, 0\n" \
  "s_cbranch_scc1 .LCA_NOZ_%=\n" \
  "s_xor_b32 s88, s60, 0x8000\n" \
  "s_add_u32 s88, s88, s61\n" \
  "buffer_store_dwordx2 v[12:13], v5, s[40:43], s88 offen sc1\n" \
  ".LCA_NOZ_%=:\n" \
  "s_waitcnt lgkmcnt(5)\n" \
  "v_smfmac_f32_16x16x64_bf16 v[20:23], v[40:43], v[192:199], v0\n" \
  "v_smfmac_f32_16x16x64_bf16 v[12:15], v[40:43], v[64:71], v0\n" \
  "v_smfmac_f32_16x16x64_bf16 v[16:19], v[40:43], v[128:135], v0\n" \
  "ds_read_b128 v[40:43], v1 offset:6144\n" \
  "s_waitcnt lgkmcnt(5)\n" \
  "v_smfmac_f32_16x16x64_bf16 v[20:23], v[44:47], v[200:207], v0\n" \
  "v_smfmac_f32_16x16x64_bf16 v[12:15], v[44:47], v[72:79], v0\n" \
  "v_smfmac_f32_16x16x64_bf16 v[16:19], v[44:47], v[136:143], v0\n" \
  "ds_read_b128 v[44:47], v1 offset:7168\n" \
  "s_waitcnt lgkmcnt(5)\n" \
  "v_smfmac_f32_16x16x64_bf16 v[20:23], v[48:51], v[208:215], v0\n" \
  "v_smfmac_f32_16x16x64_bf16 v[12:15], v[48:51], v[80:87], v0\n" \
  "v_smfmac_f32_16x16x64_bf16 v[16:19], v[48:51], v[144:151], v0\n" \
  "ds_read_b128 v[24:27], v1 offset:10240\n" \
  "ds_read_b128 v[28:31], v1 offset:14336\n" \
  "s_waitcnt lgkmcnt(6)\n" \
  "v_smfmac_f32_16x16x64_bf16 v[20:23], v[52:55], v[216:223], v0\n" \
  "v_smfmac_f32_16x16x64_bf16 v[12:15], v[52:55], v[88:95], v0\n" \
  "v_smfmac_f32_16x16x64_bf16 v[16:19], v[52:55], v[152:159], v0\n" \
  "ds_read_b128 v[32:35], v1 offset:11264\n" \
  "ds_read_b128 v[36:39], v1 offset:15360\n" \
  "s_waitcnt lgkmcnt(7)\n" \
  "v_smfmac_f32_16x16x64_bf16 v[20:23], v[56:59], v[224:231], v0\n" \
  "v_smfmac_f32_16x16x64_bf16 v[12:15], v[56:59], v[96:103], v0\n" \
  "v_smfmac_f32_16x16x64_bf16 v[16:19], v[56:59], v[160:167], v0\n" \
  "s_waitcnt lgkmcnt(6)\n" \
  "v_smfmac_f32_16x16x64_bf16 v[20:23], v[60:63], v[232:239], v0\n" \
  "v_smfmac_f32_16x16x64_bf16 v[12:15], v[60:63], v[104:111], v0\n" \
  "v_smfmac_f32_16x16x64_bf16 v[16:19], v[60:63], v[168:175], v0\n" \
  "s_waitcnt lgkmcnt(5)\n" \
  "v_smfmac_f32_16x16x64_bf16 v[20:23], v[40:43], v[240:247], v0\n" \
  "v_smfmac_f32_16x16x64_bf16 v[12:15], v[40:43], v[112:119], v0\n" \
  "v_smfmac_f32_16x16x64_bf16 v[16:19], v[40:43], v[176:183], v0\n" \
  "s_waitcnt lgkmcnt(4)\n" \
  "v_smfmac_f32_16x16x64_bf16 v[20:23], v[44:47], v[248:255], v0\n" \
  "v_smfmac_f32_16x16x64_bf16 v[12:15], v[44:47], v[120:127], v0\n" \
  "v_smfmac_f32_16x16x64_bf16 v[16:19], v[44:47], v[184:191], v0\n" \
  "s_cmp_eq_u32 s71, 0\n" \
  "s_nop 4\n" \
  "v_permlane32_swap_b32 v20, v22\n" \
  "v_permlane32_swap_b32 v21, v23\n" \
  "v_pk_add_f32 v[20:21], v[20:21], v[22:23]\n" \
  "v_permlane32_swap_b32 v12, v16\n" \
  "v_permlane32_swap_b32 v13, v17\n" \
  "v_cvt_pk_bf16_f32 v20, v20, v21\n" \
  "v_pk_add_f32 v[12:13], v[12:13], v[16:17]\n" \
  "v_permlane32_swap_b32 v14, v18\n" \
  "v_permlane32_swap_b32 v15, v19\n" \
  "ds_write_b16 v9, v20 offset:128\n" \
  "v_cvt_pk_bf16_f32 v12, v12, v13\n" \
  "v_pk_add_f32 v[14:15], v[14:15], v[18:19]\n" \
  "ds_write_b16_d16_hi v9, v20 offset:416\n" \
  "ds_write_b16 v8, v12\n" \
  "v_cvt_pk_bf16_f32 v14, v14, v15\n" \
  "ds_write_b16_d16_hi v8, v12 offset:288\n" \
  "ds_write_b16 v8, v14 offset:576\n" \
  "ds_write_b16_d16_hi v8, v14 offset:864\n" \
  "s_setprio 3\n" \
  "s_waitcnt lgkmcnt(0)\n" \
  "s_barrier\n" \
  "s_cbranch_scc1 .LCA_LAST_%=\n" \
  "ds_read_b32 v11, v1 offset:26688\n" \
  "ds_read_b128 v[40:43], v2 offset:0\n" \
  "ds_read_b128 v[44:47], v3 offset:0\n" \
  "ds_read_b128 v[48:51], v2 offset:64\n" \
  "ds_read_b128 v[52:55], v3 offset:64\n" \
  "ds_read_b128 v[56:59], v2 offset:128\n" \
  "ds_read_b128 v[60:63], v3 offset:128\n" \
  "s_waitcnt lgkmcnt(5)\n" \
  "v_mfma_f32_16x16x32_bf16 v[12:15], v[24:27], v[40:43], 0\n" \
  "v_mfma_f32_16x16x32_bf16 v[16:19], v[28:31], v[40:43], 0\n" \
  "v_readfirstlane_b32 s86, v11\n" \
  "ds_read_b128 v[24:27], v1 offset:12288\n" \
  "ds_read_b128 v[28:31], v1 offset:16384\n" \
  "s_waitcnt lgkmcnt(6)\n" \
  "v_mfma_f32_16x16x32_bf16 v[20:23], v[44:47], v[40:43], 0\n" \
  "s_lshr_b32 s86, s86, 23\n" \
  "s_add_i32 s86, s86, s83\n" \
  "ds_read_b128 v[40:43], v2 offset:192\n" \
  "ds_read_b128 v[44:47], v3 offset:192\n" \
  "s_waitcnt lgkmcnt(7)\n" \
  "v_mfma_f32_16x16x32_bf16 v[12:15], v[32:35], v[48:51], v[12:15]\n" \
  "v_mfma_f32_16x16x32_bf16 v[16:19], v[36:39], v[48:51], v[16:19]\n" \
  "s_ashr_i32 s86, s86, 1\n" \
  "s_max_i32 s86, s86, s82\n" \
  "ds_read_b128 v[32:35], v1 offset:13312\n" \
  "ds_read_b128 v[36:39], v1 offset:17408\n" \
  "s_waitcnt lgkmcnt(8)\n" \
  "v_mfma_f32_16x16x32_bf16 v[20:23], v[52:55], v[48:51], v[20:23]\n" \
  "s_min_i32 s86, s86, 100\n" \
  "s_lshl_b32 s86, s86, 23\n" \
  "s_sub_i32 s84, 0x4a800000, s86\n" \
  "ds_read_b128 v[48:51], v10 offset:18432\n" \
  "ds_read_b128 v[52:55], v10 offset:22528\n" \
  "s_waitcnt lgkmcnt(8)\n" \
  "v_mfma_f32_16x16x32_bf16 v[20:23], v[60:63], v[56:59], v[20:23]\n" \
  "s_waitcnt lgkmcnt(6)\n" \
  "v_mfma_f32_16x16x32_bf16 v[12:15], v[24:27], v[56:59], v[12:15]\n" \
  "v_mfma_f32_16x16x32_bf16 v[16:19], v[28:31], v[56:59], v[16:19]\n" \
  "ds_read_b128 v[56:59], v6 offset:0\n" \
  "ds_read_b128 v[60:63], v6 offset:1024\n" \
  "s_waitcnt lgkmcnt(6)\n" \
  "v_mfma_f32_16x16x32_bf16 v[20:23], v[44:47], v[40:43], v[20:23]\n" \
  "ds_read_b128 v[24:27], v6 offset:2048\n" \
  "ds_read_b128 v[28:31], v6 offset:3072\n" \
  "s_waitcnt lgkmcnt(6)\n" \
  "v_mfma_f32_16x16x32_bf16 v[12:15], v[32:35], v[40:43], v[12:15]\n" \
  "v_mfma_f32_16x16x32_bf16 v[16:19], v[36:39], v[40:43], v[16:19]\n" \
  "ds_read_b128 v[32:35], v6 offset:8192\n" \
  "ds_read_b128 v[36:39], v6 offset:9216\n" \
  "s_waitcnt lgkmcnt(6)\n" \
  "v_cndmask_b32 v20, v20, v21, s[76:77]\n" \
  "v_mov_b32 v41, 0\n" \
  "v_mov_b32 v42, 0\n" \
  "s_nop 1\n" \
  "v_cndmask_b32 v12, v12, v16, s[76:77]\n" \
  "v_cndmask_b32 v13, v13, v17, s[76:77]\n" \
  "v_cndmask_b32 v14, v14, v18, s[76:77]\n" \
  "v_cndmask_b32 v15, v15, v19, s[76:77]\n" \
  "v_maximum3_f32 v16, v20, v12, v13\n" \
  "v_maximum3_f32 v16, v16, v14, v15\n" \
  "s_nop 1\n" \
  "v_max_f32_dpp v16, v16, v16 row_ror:8 row_mask:0xf bank_mask:0xf\n" \
  "v_mov_b32 v17, v16\n" \
  "s_nop 1\n" \
  "v_permlane16_swap_b32 v16, v17\n" \
  "v_max_f32 v16, v16, v17\n" \
  "v_mov_b32 v17, v16\n" \
  "s_nop 1\n" \
  "v_permlane32_swap_b32 v16, v17\n" \
  "v_max_f32 v16, v16, v17\n" \
  "v_mul_f32 v16, s78, v16\n" \
  "v_pk_fma_f32 v[12:13], v[12:13], s[78:79], v[16:17] op_sel_hi:[1,0,0] neg_lo:[0,0,1] neg_hi:[0,0,1]\n" \
  "v_pk_fma_f32 v[14:15], v[14:15], s[78:79], v[16:17] op_sel_hi:[1,0,0] neg_lo:[0,0,1] neg_hi:[0,0,1]\n" \
  "v_fma_f32 v20, v20, s78, -v16\n" \
  "v_exp_f32 v12, v12\n" \
  "v_exp_f32 v13, v13\n" \
  "v_exp_f32 v14, v14\n" \
  "v_exp_f32 v15, v15\n" \
  "v_exp_f32 v20, v20\n" \
  "s_barrier\n" \
  "ds_read_b64 v[22:23], v7 offset:0\n" \
  "v_cvt_pk_bf16_f32 v44, v12, v13\n" \
  "v_cvt_pk_bf16_f32 v45, v14, v15\n" \
  "v_pk_add_f32 v[8:9], v[12:13], v[14:15]\n" \
  "v_mov_b32_dpp v46, v44 row_ror:8 row_mask:0xf bank_mask:0xf\n" \
  "v_mov_b32_dpp v17, v20 row_ror:8 row_mask:0xf bank_mask:0xf\n" \
  "v_mov_b32_dpp v47, v45 row_ror:8 row_mask:0xf bank_mask:0xf\n" \
  "v_add_f32 v10, v8, v20\n" \
  "v_cvt_pk_bf16_f32 v40, v20, v17\n" \
  "v_add_f32 v10, v10, v9\n" \
  "v_mfma_f32_16x16x32_bf16 v[12:15], v[48:51], v[44:47], 0\n" \
  "v_cvt_pk_bf16_f32 v43, v20, v17\n" \
  "v_add_f32_dpp v10, v10, v10 row_ror:8 row_mask:0xf bank_mask:0xf\n" \
  "v_mfma_f32_16x16x32_bf16 v[16:19], v[52:55], v[44:47], 0\n" \
  "v_mov_b32 v11, v10\n" \
  "s_waitcnt lgkmcnt(0)\n" \
  "v_mfma_f32_16x16x16_bf16 v[12:15], v[22:23], v[40:41], v[12:15]\n" \
  "v_mfma_f32_16x16x16_bf16 v[16:19], v[22:23], v[42:43], v[16:19]\n" \
  "v_permlane16_swap_b32 v10, v11\n" \
  "v_add_f32 v10, v10, v11\n" \
  "ds_read_b128 v[48:51], v6 offset:10240\n" \
  "v_mov_b32 v11, v10\n" \
  "ds_read_b128 v[52:55], v6 offset:11264\n" \
  "v_mov_b64 v[20:21], 0\n" \
  "v_mov_b64 v[22:23], 0\n" \
  "v_permlane32_swap_b32 v10, v11\n" \
  "v_add_f32 v10, v10, v11\n" \
  "v_rcp_f32 v10, v10\n" \
  "s_nop 0\n" \
  "v_pk_mul_f32 v[12:13], v[12:13], v[10:11] op_sel_hi:[1,0]\n" \
  "v_pk_mul_f32 v[14:15], v[14:15], v[10:11] op_sel_hi:[1,0]\n" \
  "v_pk_mul_f32 v[16:17], v[16:17], v[10:11] op_sel_hi:[1,0]\n" \
  "v_pk_mul_f32 v[18:19], v[18:19], v[10:11] op_sel_hi:[1,0]\n" \
  "v_cvt_pk_bf16_f32 v40, v12, v13\n" \
  "v_cvt_pk_bf16_f32 v41, v14, v15\n" \
  "v_cvt_pk_bf16_f32 v42, v16, v17\n" \
  "ds_write_b64 v4, v[40:41]\n" \
  "v_cvt_pk_bf16_f32 v43, v18, v19\n" \
  "ds_write_b64 v4, v[42:43] offset:1024\n" \
  "v_mov_b64 v[12:13], 0\n" \
  "v_mov_b64 v[14:15], 0\n" \
  "v_mov_b64 v[16:17], 0\n" \
  "v_mov_b64 v[18:19], 0\n" \
  "v_mov_b64 v[8:9], 0\n" \
  "v_mov_b64 v[10:11], 0\n" \
  "s_setprio 0\n" \
  "s_waitcnt lgkmcnt(0)\n" \
  "s_barrier\n" \
  "ds_read_b128 v[40:43], v1 offset:8192\n" \
  "ds_read_b128 v[44:47], v1 offset:9216\n" \
  "s_waitcnt lgkmcnt(1)\n" \
  "v_smfmac_f32_16x16x64_bf16 v[12:15], v[40:43], v[56:63], v0\n" \
  "ds_read_b128 v[56:59], v6 offset:4096\n" \
  "ds_read_b128 v[60:63], v6 offset:5120\n" \
  "v_smfmac_f32_16x16x64_bf16 v[16:19], v[40:43], v[24:31], v0\n" \
  "ds_read_b128 v[24:27], v6 offset:6144\n" \
  "ds_read_b128 v[28:31], v6 offset:7168\n" \
  "s_waitcnt lgkmcnt(4)\n" \
  "v_smfmac_f32_16x16x64_bf16 v[12:15], v[44:47], v[32:39], v0\n" \
  "ds_read_b128 v[32:35], v6 offset:12288\n" \
  "ds_read_b128 v[36:39], v6 offset:13312\n" \
  "v_smfmac_f32_16x16x64_bf16 v[16:19], v[44:47], v[48:55], v0\n" \
  "ds_read_b128 v[48:51], v6 offset:14336\n" \
  "ds_read_b128 v[52:55], v6 offset:15360\n" \
  "s_nop 0\n" \
  "s_waitcnt lgkmcnt(6)\n" \
  "v_smfmac_f32_16x16x64_bf16 v[20:23], v[40:43], v[56:63], v0\n" \
  "s_nop 0\n" \
  "s_waitcnt lgkmcnt(4)\n" \
  "v_smfmac_f32_16x16x64_bf16 v[8:11], v[40:43], v[24:31], v0\n" \
  "v_permlane32_swap_b32 v12, v17\n" \
  "v_permlane32_swap_b32 v13, v16\n" \
  "v_permlane32_swap_b32 v14, v19\n" \
  "v_permlane32_swap_b32 v15, v18\n" \
  "s_waitcnt lgkmcnt(2)\n" \
  "v_smfmac_f32_16x16x64_bf16 v[20:23], v[44:47], v[32:39], v0\n" \
  "v_pk_add_f32 v[12:13], v[12:13], v[16:17] op_sel:[0,1] op_sel_hi:[1,0]\n" \
  "v_pk_add_f32 v[14:15], v[14:15], v[18:19] op_sel:[0,1] op_sel_hi:[1,0]\n" \
  "v_pk_fma_f32 v[12:13], v[12:13], s[84:85], s[84:85] op_sel:[0,0,1] op_sel_hi:[1,0,1]\n" \
  "v_pk_fma_f32 v[14:15], v[14:15], s[84:85], s[84:85] op_sel:[0,0,1] op_sel_hi:[1,0,1]\n" \
  "s_waitcnt lgkmcnt(0)\n" \
  "v_smfmac_f32_16x16x64_bf16 v[8:11], v[44:47], v[48:55], v0\n" \
  "v_lshl_add_u32 v12, v12, 6, s91\n" \
  "v_lshl_add_u32 v13, v13, 2, s75\n" \
  "global_atomic_add_x2 v5, v[12:13], s[58:59] offset:0\n" \
  "v_lshl_add_u32 v14, v14, 6, s91\n" \
  "v_lshl_add_u32 v15, v15, 2, s75\n" \
  "global_atomic_add_x2 v5, v[14:15], s[58:59] offset:2048\n" \
  "s_nop 1\n" \
  "v_permlane32_swap_b32 v20, v9\n" \
  "v_permlane32_swap_b32 v21, v8\n" \
  "v_permlane32_swap_b32 v22, v11\n" \
  "v_permlane32_swap_b32 v23, v10\n" \
  "v_pk_add_f32 v[20:21], v[20:21], v[8:9] op_sel:[0,1] op_sel_hi:[1,0]\n" \
  "v_pk_add_f32 v[22:23], v[22:23], v[10:11] op_sel:[0,1] op_sel_hi:[1,0]\n" \
  "v_pk_fma_f32 v[20:21], v[20:21], s[84:85], s[84:85] op_sel:[0,0,1] op_sel_hi:[1,0,1]\n" \
  "v_pk_fma_f32 v[22:23], v[22:23], s[84:85], s[84:85] op_sel:[0,0,1] op_sel_hi:[1,0,1]\n" \
  "v_lshl_add_u32 v20, v20, 6, s91\n" \
  "v_lshl_add_u32 v21, v21, 2, s75\n" \
  "global_atomic_add_x2 v5, v[20:21], s[58:59] offset:3072\n" \
  "v_lshl_add_u32 v22, v22, 6, s91\n" \
  "v_lshl_add_u32 v23, v23, 2, s75\n" \
  "global_atomic_add_x2 v5, v[22:23], s[58:59] offset:1024\n" \
  "buffer_load_dword v52, v5, s[0:3], s66 offen sc1\n" \
  "v_mbcnt_lo_u32_b32 v48, -1, 0\n" \
  "v_mbcnt_hi_u32_b32 v48, -1, v48\n" \
  "v_and_b32 v49, 15, v48\n" \
  "v_lshl_add_u32 v8, v49, 1, s97\n" \
  "v_bfe_u32 v49, v48, 4, 1\n" \
  "v_mad_u32_u24 v8, v49, s92, v8\n" \
  "v_lshrrev_b32 v49, 5, v48\n" \
  "v_mad_u32_u24 v9, v49, s93, v8\n" \
  "v_mad_u32_u24 v8, v49, s94, v8\n" \
  "v_add_u32 v10, s99, v1\n" \
  "s_lshr_b32 s88, s99, 2\n" \
  "s_add_u32 s88, s88, s74\n" \
  "s_add_u32 s88, s88, 139264\n" \
  "v_lshl_add_u32 v7, v49, 10, s88\n" \
  "v_and_b32 v49, 31, v48\n" \
  "v_lshl_add_u32 v7, v49, 3, v7\n" \
  "v_mov_b64 v[12:13], 0\n" \
  "v_mov_b64 v[14:15], 0\n" \
  "v_mov_b64 v[16:17], 0\n" \
  "v_mov_b64 v[18:19], 0\n" \
  "v_mov_b64 v[20:21], 0\n" \
  "v_mov_b64 v[22:23], 0\n" \
  "s_add_u32 s88, s74, 157760\n" \
  "v_mov_b32 v59, s88\n" \
  "v_lshrrev_b32 v49, 3, v48\n" \
  "v_and_b32 v50, 7, v48\n" \
  "v_lshlrev_b32 v50, 4, v50\n" \
  "v_lshl_or_b32 v44, v49, 8, v50\n" \
  "v_and_b32 v50, 3, v48\n" \
  "v_lshl_add_u32 v40, v50, 2, s98\n" \
  "v_lshrrev_b32 v50, 4, v48\n" \
  "v_mad_u32_u24 v40, v50, s95, v40\n" \
  "v_and_b32 v49, 1, v49\n" \
  "v_lshl_add_u32 v40, v49, 6, v40\n" \
  "v_bfe_u32 v49, v48, 2, 1\n" \
  "v_lshl_add_u32 v40, v49, 7, v40\n" \
  "v_xor_b32 v41, 16, v40\n" \
  "v_xor_b32 v42, 32, v40\n" \
  "v_xor_b32 v43, 48, v40\n" \
  "s_add_i32 s80, s86, 0x32800000\n" \
  "s_add_i32 s81, s86, 0x33800000\n" \
  "s_add_i32 s88, s86, 0xb4000000\n" \
  "v_mov_b32 v46, s88\n" \
  "v_mov_b32 v47, 0\n" \
  "v_mov_b32 v11, 0\n" \
  "s_mov_b32 s89, s60\n" \
  "s_xor_b32 s60, s60, 0x80\n" \
  "s_bitcmp0_b32 s60, 7\n" \
  "s_cselect_b32 s88, 0x8000, 0\n" \
  "s_add_u32 s60, s60, s88\n" \
  "s_and_b32 s60, s60, 0xffff\n" \
  "s_add_u32 s58, s56, s60\n" \
  "s_addc_u32 s59, s57, 0\n" \
  "s_sub_u32 s71, s71, 1\n" \
  "s_mov_b32 s87, 0x40000\n" \
  "s_waitcnt vmcnt(0)\n" \
  "s_memrealtime s[38:39]\n" \
  ".LCA_CHK_%=:\n" \
  "v_cmp_eq_u32 vcc, 0, v52\n" \
  "v_readlane_b32 s88, v52, s79\n" \
  "s_cmp_eq_u64 vcc, 0\n" \
  "s_cbranch_scc1 .LCA_OKM_%=\n" \
  "s_sleep 4\n" \
  "buffer_load_dword v52, v5, s[0:3], s66 offen sc1\n" \
  "s_sub_u32 s87, s87, 1\n" \
  "s_cbranch_scc1 .LCA_END_%=\n" \
  "s_waitcnt vmcnt(0)\n" \
  "s_branch .LCA_CHK_%=\n" \
  ".LCA_OKM_%=:\n" \
  "v_subrev_u32 v52, s88, v52\n" \
  "s_nop 1\n" \
  "v_max_i32_dpp v52, v52, v52 row_ror:1 row_mask:0xf bank_mask:0xf\n" \
  "s_nop 1\n" \
  "v_max_i32_dpp v52, v52, v52 row_ror:2 row_mask:0xf bank_mask:0xf\n" \
  "s_nop 1\n" \
  "v_max_i32_dpp v52, v52, v52 row_ror:4 row_mask:0xf bank_mask:0xf\n" \
  "s_add_u32 s66, s66, 1024\n" \
  "v_readlane_b32 vcc_lo, v52, 0\n" \
  "v_readlane_b32 s88, v52, 16\n" \
  "s_max_i32 vcc_lo, vcc_lo, s88\n" \
  "v_readlane_b32 s88, v52, 32\n" \
  "s_max_i32 vcc_lo, vcc_lo, s88\n" \
  "v_readlane_b32 s88, v52, 48\n" \
  "s_max_i32 vcc_lo, vcc_lo, s88\n" \
  "s_waitcnt lgkmcnt(0)\n" \
  "s_add_u32 vcc_lo, vcc_lo, s38\n" \
  "s_add_u32 vcc_lo, vcc_lo, -6\n" \
  ".LCA_TW_%=:\n" \
  "s_memrealtime s[38:39]\n" \
  "s_waitcnt lgkmcnt(0)\n" \
  "s_sub_u32 s38, s38, vcc_lo\n" \
  "s_cmp_lt_i32 s38, 0\n" \
  "s_cbranch_scc1 .LCA_TW_%=\n" \
  ".LCA_POLL_%=:\n" \
  "buffer_load_dwordx4 v[24:27], v44, s[40:43], s89 offen sc1\n" \
  "buffer_load_dwordx4 v[28:31], v44, s[40:43], s89 offen offset:2048 sc1\n" \
  "s_sub_u32 s87, s87, 1\n" \
  "s_cbranch_scc1 .LCA_END_%=\n" \
  "s_waitcnt vmcnt(1)\n" \
  "v_bitop3_b32 v45, v24, v26, 32 bitop3:0x80\n" \
  "v_alignbit_b32 v32, v25, v24, 2\n" \
  "v_alignbit_b32 v34, v27, v26, 2\n" \
  "s_waitcnt vmcnt(0)\n" \
  "v_bitop3_b32 v45, v45, v28, v30 bitop3:0x80\n" \
  "v_cmp_eq_u32 vcc, 0, v45\n" \
  "v_cvt_f32_i32 v32, v32\n" \
  "v_cvt_f32_i32 v33, v25\n" \
  "s_cmp_eq_u64 vcc, 0\n" \
  "s_cbranch_scc0 .LCA_POLL_%=\n" \
  "v_cvt_f32_i32 v34, v34\n" \
  "v_cvt_f32_i32 v35, v27\n" \
  "v_pk_fma_f32 v[32:33], v[32:33], s[80:81], v[46:47]\n" \
  "v_alignbit_b32 v36, v29, v28, 2\n" \
  "v_pk_fma_f32 v[34:35], v[34:35], s[80:81], v[46:47]\n" \
  "v_alignbit_b32 v38, v31, v30, 2\n" \
  "v_cvt_pk_bf16_f32 v32, v32, v34\n" \
  "v_cvt_pk_bf16_f32 v33, v33, v35\n" \
  "v_cvt_f32_i32 v36, v36\n" \
  "ds_write_b32 v40, v32\n" \
  "v_cvt_f32_i32 v37, v29\n" \
  "ds_write_b32 v41, v33\n" \
  "v_cvt_f32_i32 v38, v38\n" \
  "v_cvt_f32_i32 v39, v31\n" \
  "v_pk_fma_f32 v[36:37], v[36:37], s[80:81], v[46:47]\n" \
  "v_pk_fma_f32 v[38:39], v[38:39], s[80:81], v[46:47]\n" \
  "v_dot2c_f32_bf16 v11, v32, v32\n" \
  "v_cvt_pk_bf16_f32 v36, v36, v38\n" \
  "v_cvt_pk_bf16_f32 v37, v37, v39\n" \
  "ds_write_b32 v42, v36\n" \
  "ds_write_b32 v43, v37\n" \
  "v_dot2c_f32_bf16 v11, v33, v33\n" \
  "v_dot2c_f32_bf16 v11, v36, v36\n" \
  "v_dot2c_f32_bf16 v11, v37, v37\n" \
  "s_nop 1\n" \
  "s_nop 0\n" \
  "v_cmpx_lt_f32 vcc, 2.0, v11\n" \
  "ds_max_u32 v59, v11\n" \
  "s_mov_b64 exec, -1\n" \
  "s_branch .LCA_TOP_%=\n" \
  ".LCA_LAST_%=:\n" \
  ".LCA_END_%=:\n" \
  "s_endpgm\n"
#define CHAIN_ASM_VO \
  "s_mov_b32 s92, 1152\n" \
  "s_mov_b32 s93, 576\n" \
  "s_mov_b32 s94, 2272\n" \
  "s_mov_b32 s95, 272\n" \
  "s_mov_b32 s96, 288\n" \
  "s_mov_b32 s91, 0x40000001\n" \
  "s_mov_b32 s84, 0x4b400000\n" \
  "s_mov_b32 s85, 0x4b400000\n" \
  "s_mov_b32 s78, 0x3e0293ee\n" \
  "s_mov_b32 s76, 0xff00ff00\n" \
  "s_mov_b32 s77, 0xff00ff00\n" \
  "s_add_u32 s58, s56, s60\n" \
  "s_addc_u32 s59, s57, 0\n" \
  "s_lshl_b32 s98, s73, 10\n" \
  "s_add_u32 s98, s98, s74\n" \
  "s_add_u32 s98, s98, 131072\n" \
  "s_lshl_b32 s97, s73, 5\n" \
  "s_add_u32 s97, s97, s74\n" \
  "s_add_u32 s97, s97, 160992\n" \
  "s_lshl_b32 s99, s73, 11\n" \
  "s_add_u32 s99, s99, s74\n" \
  "s_add_u32 s99, s99, 133120\n" \
  "s_lshl_b32 s94, s73, 2\n" \
  "s_add_u32 s94, s94, s74\n" \
  "s_add_u32 s94, s94, 157828\n" \
  "v_mbcnt_lo_u32_b32 v48, -1, 0\n" \
  "v_mbcnt_hi_u32_b32 v48, -1, v48\n" \
  "s_sub_u32 s88, s73, 4\n" \
  "v_and_b32 v51, 15, v48\n" \
  "v_lshl_add_u32 v4, v51, 1, s97\n" \
  "v_bfe_u32 v49, v48, 4, 1\n" \
  "v_mad_u32_u24 v4, v49, s92, v4\n" \
  "v_lshrrev_b32 v50, 5, v48\n" \
  "v_mad_u32_u24 v4, v50, s93, v4\n" \
  "v_lshlrev_b32 v8, 4, v49\n" \
  "v_lshl_add_u32 v8, v51, 5, v8\n" \
  "v_lshl_add_u32 v8, v50, 3, v8\n" \
  "s_lshl_b32 s89, s88, 8\n" \
  "s_add_u32 s89, s89, s74\n" \
  "s_add_u32 s89, s89, 139264\n" \
  "v_and_b32 v49, 31, v48\n" \
  "v_lshl_add_u32 v5, v49, 3, s89\n" \
  "v_lshl_add_u32 v5, v50, 10, v5\n" \
  "v_lshrrev_b32 v49, 2, v51\n" \
  "v_and_b32 v50, 5, v51\n" \
  "v_and_or_b32 v49, v49, 2, v50\n" \
  "s_lshl_b32 s89, s88, 6\n" \
  "s_add_u32 s89, s89, s74\n" \
  "s_add_u32 s89, s89, 160992\n" \
  "v_and_b32 v50, 48, v48\n" \
  "v_add_u32 v6, s89, v50\n" \
  "v_mad_u32_u24 v6, v49, s96, v6\n" \
  "v_lshlrev_b32 v7, 3, v50\n" \
  "v_lshl_add_u32 v7, v49, 4, v7\n" \
  "v_and_b32 v49, 2, v51\n" \
  "v_lshl_add_u32 v7, v49, 29, v7\n" \
  "v_lshlrev_b32 v10, 4, v48\n" \
  "v_mov_b64 v[12:13], 0\n" \
  "v_mov_b64 v[14:15], 0\n" \
  "v_mov_b64 v[16:17], 0\n" \
  "v_mov_b64 v[18:19], 0\n" \
  "v_mov_b64 v[20:21], 0\n" \
  "v_mov_b64 v[22:23], 0\n" \
  "s_add_u32 s88, s74, 157760\n" \
  "v_mov_b32 v59, s88\n" \
  "v_mov_b32 v24, 0x44870000\n" \
  "v_mul_f32 v24, s72, v24\n" \
  "s_nop 0\n" \
  "v_readfirstlane_b32 s86, v24\n" \
  "s_lshr_b32 s86, s86, 23\n" \
  "s_addk_i32 s86, 0xff83\n" \
  "s_ashr_i32 s86, s86, 1\n" \
  "s_max_i32 s86, s86, -20\n" \
  "s_min_i32 s86, s86, 100\n" \
  "s_mov_b32 s82, s86\n" \
  "s_lshr_b32 s83, s72, 23\n" \
  "s_sub_i32 s83, s83, 242\n" \
  "s_lshl_b32 s88, s73, 10\n" \
  "v_add_u32 v24, s88, v1\n" \
  "ds_read_b128 v[32:35], v24 offset:0\n" \
  "s_waitcnt lgkmcnt(0)\n" \
  "v_mov_b32 v11, 0\n" \
  "v_dot2c_f32_bf16 v11, v32, v32\n" \
  "v_dot2c_f32_bf16 v11, v33, v33\n" \
  "v_dot2c_f32_bf16 v11, v34, v34\n" \
  "v_dot2c_f32_bf16 v11, v35, v35\n" \
  "s_nop 1\n" \
  "s_nop 0\n" \
  "v_cmpx_lt_f32 vcc, 2.0, v11\n" \
  "ds_max_u32 v59, v11\n" \
  "s_mov_b64 exec, -1\n" \
  ".LCV_TOP_%=:\n" \
  "s_waitcnt lgkmcnt(0)\n" \
  "s_barrier\n" \
  "s_setprio 3\n" \
  "ds_read_b128 v[24:27], v1 offset:0\n" \
  "ds_read_b128 v[28:31], v1 offset:1024\n" \
  "ds_read_b128 v[32:35], v1 offset:2048\n" \
  "ds_read_b128 v[36:39], v1 offset:3072\n" \
  "ds_read_b128 v[40:43], v1 offset:4096\n" \
  "ds_read_b128 v[44:47], v1 offset:5120\n" \
  "ds_read_b128 v[48:51], v1 offset:6144\n" \
  "ds_read_b128 v[52:55], v1 offset:7168\n" \
  "buffer_load_dword v58, off, s[44:47], s68 sc1\n" \
  "s_waitcnt lgkmcnt(7)\n" \
  "v_smfmac_f32_16x16x64_bf16 v[12:15], v[24:27], v[64:71], v0\n" \
  "s_waitcnt lgkmcnt(6)\n" \
  "v_smfmac_f32_16x16x64_bf16 v[12:15], v[28:31], v[72:79], v0\n" \
  "s_waitcnt lgkmcnt(5)\n" \
  "v_smfmac_f32_16x16x64_bf16 v[12:15], v[32:35], v[80:87], v0\n" \
  "s_waitcnt lgkmcnt(4)\n" \
  "v_smfmac_f32_16x16x64_bf16 v[12:15], v[36:39], v[88:95], v0\n" \
  "s_waitcnt lgkmcnt(3)\n" \
  "v_smfmac_f32_16x16x64_bf16 v[12:15], v[40:43], v[96:103], v0\n" \
  "s_waitcnt lgkmcnt(2)\n" \
  "v_smfmac_f32_16x16x64_bf16 v[12:15], v[44:47], v[104:111], v0\n" \
  "s_waitcnt lgkmcnt(1)\n" \
  "v_smfmac_f32_16x16x64_bf16 v[12:15], v[48:51], v[112:119], v0\n" \
  "s_waitcnt lgkmcnt(0)\n" \
  "v_smfmac_f32_16x16x64_bf16 v[12:15], v[52:55], v[120:127], v0\n" \
  "s_memrealtime s[38:39]\n" \
  "ds_read_b32 v11, v1 offset:26688\n" \
  "s_nop 6\n" \
  "v_permlane32_swap_b32 v12, v14\n" \
  "v_permlane32_swap_b32 v13, v15\n" \
  "v_pk_add_f32 v[12:13], v[12:13], v[14:15]\n" \
  "s_nop 0\n" \
  "v_cvt_pk_bf16_f32 v12, v12, v13\n" \
  "ds_write_b16 v4, v12\n" \
  "ds_write_b16_d16_hi v4, v12 offset:288\n" \
  "s_setprio 0\n" \
  "v_mov_b64 v[12:13], 0\n" \
  "v_mov_b64 v[14:15], 0\n" \
  "s_waitcnt lgkmcnt(2)\n" \
  "v_readfirstlane_b32 s86, v11\n" \
  "s_lshr_b32 s86, s86, 23\n" \
  "s_add_i32 s86, s86, s83\n" \
  "s_ashr_i32 s86, s86, 1\n" \
  "s_max_i32 s86, s86, s82\n" \
  "s_min_i32 s86, s86, 100\n" \
  "s_lshl_b32 s86, s86, 23\n" \
  "s_sub_i32 s84, 0x4a800000, s86\n" \
  "s_cmp_eq_u32 s71, 0\n" \
  "s_waitcnt lgkmcnt(0)\n" \
  "s_barrier\n" \
  "ds_read_b128 v[60:63], v6 offset:0\n" \
  "v_smfmac_f32_16x16x64_bf16 v[16:19], v[24:27], v[128:135], v0\n" \
  "v_or_b32 v57, 1, s38\n" \
  "v_smfmac_f32_16x16x64_bf16 v[20:23], v[24:27], v[192:199], v0\n" \
  "v_add_u32 v56, s67, v10\n" \
  "v_smfmac_f32_16x16x64_bf16 v[16:19], v[28:31], v[136:143], v0\n" \
  "s_mov_b64 exec, 0x11\n" \
  "buffer_store_dword v57, v56, s[0:3], 0 offen sc1\n" \
  "s_mov_b64 exec, -1\n" \
  "v_smfmac_f32_16x16x64_bf16 v[20:23], v[28:31], v[200:207], v0\n" \
  "v_smfmac_f32_16x16x64_bf16 v[16:19], v[32:35], v[144:151], v0\n" \
  "s_waitcnt lgkmcnt(0)\n" \
  "v_max_u32 v60, 1, v60\n" \
  "v_smfmac_f32_16x16x64_bf16 v[20:23], v[32:35], v[208:215], v0\n" \
  "v_max_u32 v61, 1, v61\n" \
  "v_smfmac_f32_16x16x64_bf16 v[16:19], v[36:39], v[152:159], v0\n" \
  "v_max_u32 v62, 1, v62\n" \
  "v_smfmac_f32_16x16x64_bf16 v[20:23], v[36:39], v[216:223], v0\n" \
  "v_max_u32 v63, 1, v63\n" \
  "v_smfmac_f32_16x16x64_bf16 v[16:19], v[40:43], v[160:167], v0\n" \
  "buffer_store_dwordx4 v[60:63], v7, s[48:51], s69 offen sc1\n" \
  "v_smfmac_f32_16x16x64_bf16 v[20:23], v[40:43], v[224:231], v0\n" \
  "v_smfmac_f32_16x16x64_bf16 v[16:19], v[44:47], v[168:175], v0\n" \
  "v_smfmac_f32_16x16x64_bf16 v[20:23], v[44:47], v[232:239], v0\n" \
  "v_smfmac_f32_16x16x64_bf16 v[16:19], v[48:51], v[176:183], v0\n" \
  "v_smfmac_f32_16x16x64_bf16 v[20:23], v[48:51], v[240:247], v0\n" \
  "v_smfmac_f32_16x16x64_bf16 v[16:19], v[52:55], v[184:191], v0\n" \
  "v_smfmac_f32_16x16x64_bf16 v[20:23], v[52:55], v[248:255], v0\n" \
  "ds_read_b128 v[24:27], v3 offset:0\n" \
  "ds_read_b128 v[28:31], v3 offset:1024\n" \
  "ds_read_b128 v[32:35], v3 offset:2048\n" \
  "ds_read_b128 v[36:39], v3 offset:3072\n" \
  "ds_read_b128 v[40:43], v3 offset:8192\n" \
  "ds_read_b128 v[44:47], v3 offset:9216\n" \
  "s_nop 0\n" \
  "v_permlane32_swap_b32 v16, v18\n" \
  "v_permlane32_swap_b32 v17, v19\n" \
  "v_permlane32_swap_b32 v20, v22\n" \
  "v_permlane32_swap_b32 v21, v23\n" \
  "v_pk_add_f32 v[16:17], v[16:17], v[18:19]\n" \
  "v_pk_add_f32 v[20:21], v[20:21], v[22:23]\n" \
  "v_cvt_pk_bf16_f32 v16, v16, v17\n" \
  "v_cvt_pk_bf16_f32 v17, v20, v21\n" \
  "ds_write_b64 v5, v[16:17]\n" \
  "v_max_u32 v20, 1, v16\n" \
  "v_max_u32 v21, 1, v17\n" \
  "s_nop 1\n" \
  "v_permlane32_swap_b32 v20, v21\n" \
  "buffer_store_dwordx2 v[20:21], v8, s[52:55], s70 offen sc1\n" \
  "s_cbranch_scc1 .LCV_LAST_%=\n" \
  "s_waitcnt vmcnt(3)\n" \
  "v_cmp_eq_u32 vcc, 0, v58\n" \
  "s_cmp_eq_u64 vcc, 0\n" \
  "s_cselect_b32 s90, 1, 0\n" \
  "s_waitcnt lgkmcnt(0)\n" \
  "s_barrier\n" \
  "s_cbranch_scc0 .LCV_NOTOK_%=\n" \
  "s_mov_b32 m0, s99\n" \
  "s_add_u32 s88, s99, 8192\n" \
  "global_load_lds_dwordx4 v10, s[62:63] sc1\n" \
  "global_load_lds_dwordx4 v10, s[62:63] offset:1024 sc1\n" \
  "s_mov_b32 m0, s88\n" \
  "s_nop 0\n" \
  "global_load_lds_dwordx4 v10, s[64:65] sc1\n" \
  "global_load_lds_dwordx4 v10, s[64:65] offset:1024 sc1\n" \
  ".LCV_NOTOK_%=:\n" \
  "s_add_u32 s67, s67, 1024\n" \
  "ds_write_b32 v59, v12\n" \
  "ds_read_b128 v[48:51], v3 offset:10240\n" \
  "ds_read_b128 v[52:55], v3 offset:11264\n" \
  "v_mov_b64 v[18:19], 0\n" \
  "v_mov_b64 v[20:21], 0\n" \
  "v_mov_b64 v[22:23], 0\n" \
  "v_mov_b64 v[16:17], 0\n" \
  "ds_read_b128 v[56:59], v3 offset:4096\n" \
  "ds_read_b128 v[60:63], v3 offset:5120\n" \
  "s_cmp_eq_u32 s90, 1\n" \
  "s_cbranch_scc1 .LCV_NM0_%=\n" \
  "s_mov_b32 m0, s94\n" \
  "s_mov_b64 exec, 1\n" \
  "buffer_load_dword off, s[44:47], s68 sc1 lds\n" \
  "s_mov_b64 exec, -1\n" \
  ".LCV_NM0_%=:\n" \
  "s_waitcnt lgkmcnt(0)\n" \
  "s_barrier\n" \
  "ds_read_b128 v[4:7], v1 offset:8192\n" \
  "ds_read_b128 v[8:11], v1 offset:9216\n" \
  "s_waitcnt lgkmcnt(1)\n" \
  "v_smfmac_f32_16x16x64_bf16 v[12:15], v[4:7], v[24:31], v0\n" \
  "ds_read_b128 v[24:27], v3 offset:6144\n" \
  "ds_read_b128 v[28:31], v3 offset:7168\n" \
  "v_smfmac_f32_16x16x64_bf16 v[16:19], v[4:7], v[32:39], v0\n" \
  "ds_read_b128 v[32:35], v3 offset:12288\n" \
  "ds_read_b128 v[36:39], v3 offset:13312\n" \
  "s_waitcnt lgkmcnt(4)\n" \
  "v_smfmac_f32_16x16x64_bf16 v[12:15], v[8:11], v[40:47], v0\n" \
  "v_smfmac_f32_16x16x64_bf16 v[16:19], v[8:11], v[48:55], v0\n" \
  "v_smfmac_f32_16x16x64_bf16 v[20:23], v[4:7], v[56:63], v0\n" \
  "ds_read_b128 v[56:59], v3 offset:14336\n" \
  "ds_read_b128 v[60:63], v3 offset:15360\n" \
  "v_mov_b64 v[40:41], 0\n" \
  "v_mov_b64 v[42:43], 0\n" \
  "s_waitcnt lgkmcnt(2)\n" \
  "v_smfmac_f32_16x16x64_bf16 v[20:23], v[8:11], v[32:39], v0\n" \
  "s_nop 0\n" \
  "v_permlane32_swap_b32 v12, v17\n" \
  "v_permlane32_swap_b32 v13, v16\n" \
  "v_permlane32_swap_b32 v14, v19\n" \
  "v_permlane32_swap_b32 v15, v18\n" \
  "v_smfmac_f32_16x16x64_bf16 v[40:43], v[4:7], v[24:31], v0\n" \
  "v_pk_add_f32 v[12:13], v[12:13], v[16:17] op_sel:[0,1] op_sel_hi:[1,0]\n" \
  "v_pk_add_f32 v[14:15], v[14:15], v[18:19] op_sel:[0,1] op_sel_hi:[1,0]\n" \
  "v_pk_fma_f32 v[12:13], v[12:13], s[84:85], s[84:85] op_sel:[0,0,1] op_sel_hi:[1,0,1]\n" \
  "v_pk_fma_f32 v[14:15], v[14:15], s[84:85], s[84:85] op_sel:[0,0,1] op_sel_hi:[1,0,1]\n" \
  "s_waitcnt lgkmcnt(0)\n" \
  "v_smfmac_f32_16x16x64_bf16 v[40:43], v[8:11], v[56:63], v0\n" \
  "v_lshl_add_u32 v12, v12, 6, s91\n" \
  "v_lshl_add_u32 v13, v13, 2, s75\n" \
  "global_atomic_add_x2 v2, v[12:13], s[58:59] offset:0\n" \
  "v_lshl_add_u32 v14, v14, 6, s91\n" \
  "v_lshl_add_u32 v15, v15, 2, s75\n" \
  "global_atomic_add_x2 v2, v[14:15], s[58:59] offset:2048\n" \
  "s_nop 1\n" \
  "v_permlane32_swap_b32 v20, v41\n" \
  "v_permlane32_swap_b32 v21, v40\n" \
  "v_permlane32_swap_b32 v22, v43\n" \
  "v_permlane32_swap_b32 v23, v42\n" \
  "v_pk_add_f32 v[20:21], v[20:21], v[40:41] op_sel:[0,1] op_sel_hi:[1,0]\n" \
  "v_pk_add_f32 v[22:23], v[22:23], v[42:43] op_sel:[0,1] op_sel_hi:[1,0]\n" \
  "v_pk_fma_f32 v[20:21], v[20:21], s[84:85], s[84:85] op_sel:[0,0,1] op_sel_hi:[1,0,1]\n" \
  "v_pk_fma_f32 v[22:23], v[22:23], s[84:85], s[84:85] op_sel:[0,0,1] op_sel_hi:[1,0,1]\n" \
  "v_lshl_add_u32 v20, v20, 6, s91\n" \
  "v_lshl_add_u32 v21, v21, 2, s75\n" \
  "global_atomic_add_x2 v2, v[20:21], s[58:59] offset:3072\n" \
  "v_lshl_add_u32 v22, v22, 6, s91\n" \
  "v_lshl_add_u32 v23, v23, 2, s75\n" \
  "global_atomic_add_x2 v2, v[22:23], s[58:59] offset:1024\n" \
  "buffer_load_dword v52, v2, s[0:3], s66 offen sc1\n" \
  "v_mbcnt_lo_u32_b32 v48, -1, 0\n" \
  "v_mbcnt_hi_u32_b32 v48, -1, v48\n" \
  "s_sub_u32 s88, s73, 4\n" \
  "v_and_b32 v51, 15, v48\n" \
  "v_lshl_add_u32 v4, v51, 1, s97\n" \
  "v_bfe_u32 v49, v48, 4, 1\n" \
  "v_mad_u32_u24 v4, v49, s92, v4\n" \
  "v_lshrrev_b32 v50, 5, v48\n" \
  "v_mad_u32_u24 v4, v50, s93, v4\n" \
  "v_lshlrev_b32 v8, 4, v49\n" \
  "v_lshl_add_u32 v8, v51, 5, v8\n" \
  "v_lshl_add_u32 v8, v50, 3, v8\n" \
  "s_lshl_b32 s89, s88, 8\n" \
  "s_add_u32 s89, s89, s74\n" \
  "s_add_u32 s89, s89, 139264\n" \
  "v_and_b32 v49, 31, v48\n" \
  "v_lshl_add_u32 v5, v49, 3, s89\n" \
  "v_lshl_add_u32 v5, v50, 10, v5\n" \
  "v_lshrrev_b32 v49, 2, v51\n" \
  "v_and_b32 v50, 5, v51\n" \
  "v_and_or_b32 v49, v49, 2, v50\n" \
  "s_lshl_b32 s89, s88, 6\n" \
  "s_add_u32 s89, s89, s74\n" \
  "s_add_u32 s89, s89, 160992\n" \
  "v_and_b32 v50, 48, v48\n" \
  "v_add_u32 v6, s89, v50\n" \
  "v_mad_u32_u24 v6, v49, s96, v6\n" \
  "v_lshlrev_b32 v7, 3, v50\n" \
  "v_lshl_add_u32 v7, v49, 4, v7\n" \
  "v_and_b32 v49, 2, v51\n" \
  "v_lshl_add_u32 v7, v49, 29, v7\n" \
  "v_lshlrev_b32 v10, 4, v48\n" \
  "v_mov_b64 v[12:13], 0\n" \
  "v_mov_b64 v[14:15], 0\n" \
  "v_mov_b64 v[16:17], 0\n" \
  "v_mov_b64 v[18:19], 0\n" \
  "v_mov_b64 v[20:21], 0\n" \
  "v_mov_b64 v[22:23], 0\n" \
  "s_add_u32 s88, s74, 157760\n" \
  "v_mov_b32 v59, s88\n" \
  "s_cmp_eq_u32 s90, 1\n" \
  "s_cbranch_scc1 .LCV_NQF_%=\n" \
  "v_mov_b32 v58, s94\n" \
  "s_waitcnt vmcnt(5)\n" \
  "ds_read_b32 v58, v58\n" \
  "s_waitcnt lgkmcnt(0)\n" \
  "v_cmp_eq_u32 vcc, 0, v58\n" \
  "s_cmp_eq_u64 vcc, 0\n" \
  "s_cbranch_scc0 .LCV_NM1_%=\n" \
  "s_mov_b32 m0, s99\n" \
  "s_add_u32 s88, s99, 8192\n" \
  "global_load_lds_dwordx4 v10, s[62:63] sc1\n" \
  "global_load_lds_dwordx4 v10, s[62:63] offset:1024 sc1\n" \
  "s_mov_b32 m0, s88\n" \
  "s_nop 0\n" \
  "global_load_lds_dwordx4 v10, s[64:65] sc1\n" \
  "global_load_lds_dwordx4 v10, s[64:65] offset:1024 sc1\n" \
  "s_mov_b32 s90, 2\n" \
  "s_branch .LCV_NQF_%=\n" \
  ".LCV_NM1_%=:\n" \
  "buffer_load_dword v58, off, s[44:47], s68 sc1\n" \
  ".LCV_NQF_%=:\n" \
  "v_lshrrev_b32 v49, 3, v48\n" \
  "v_and_b32 v50, 7, v48\n" \
  "v_lshlrev_b32 v50, 4, v50\n" \
  "v_lshl_or_b32 v44, v49, 8, v50\n" \
  "v_and_b32 v50, 3, v48\n" \
  "v_lshl_add_u32 v40, v50, 2, s98\n" \
  "v_lshrrev_b32 v50, 4, v48\n" \
  "v_mad_u32_u24 v40, v50, s95, v40\n" \
  "v_and_b32 v49, 1, v49\n" \
  "v_lshl_add_u32 v40, v49, 6, v40\n" \
  "v_bfe_u32 v49, v48, 2, 1\n" \
  "v_lshl_add_u32 v40, v49, 7, v40\n" \
  "v_xor_b32 v41, 16, v40\n" \
  "v_xor_b32 v42, 32, v40\n" \
  "v_xor_b32 v43, 48, v40\n" \
  "s_add_i32 s80, s86, 0x32800000\n" \
  "s_add_i32 s81, s86, 0x33800000\n" \
  "s_add_i32 s88, s86, 0xb4000000\n" \
  "v_mov_b32 v46, s88\n" \
  "v_mov_b32 v47, 0\n" \
  "v_mov_b32 v11, 0\n" \
  "s_mov_b32 s89, s60\n" \
  "s_xor_b32 s60, s60, 0x80\n" \
  "s_bitcmp0_b32 s60, 7\n" \
  "s_cselect_b32 s88, 0x8000, 0\n" \
  "s_add_u32 s60, s60, s88\n" \
  "s_and_b32 s60, s60, 0xffff\n" \
  "s_add_u32 s58, s56, s60\n" \
  "s_addc_u32 s59, s57, 0\n" \
  "s_sub_u32 s71, s71, 1\n" \
  "s_mov_b32 s87, 0x40000\n" \
  "s_waitcnt vmcnt(4)\n" \
  "s_cmp_eq_u32 s90, 2\n" \
  "s_cbranch_scc1 .LCV_DR_%=\n" \
  "s_waitcnt vmcnt(0)\n" \
  ".LCV_DR_%=:\n" \
  "s_memrealtime s[38:39]\n" \
  "s_cmp_lg_u32 s90, 0\n" \
  "s_cbranch_scc1 .LCV_HAVE_%=\n" \
  ".LCV_LATE_%=:\n" \
  "v_cmp_eq_u32 vcc, 0, v58\n" \
  "s_cmp_eq_u64 vcc, 0\n" \
  "s_cbranch_scc1 .LCV_GOT_%=\n" \
  "s_sleep 12\n" \
  "buffer_load_dword v58, off, s[44:47], s68 sc1\n" \
  "s_sub_u32 s87, s87, 1\n" \
  "s_cbranch_scc1 .LCV_END_%=\n" \
  "s_waitcnt vmcnt(0)\n" \
  "s_branch .LCV_LATE_%=\n" \
  ".LCV_GOT_%=:\n" \
  "s_mov_b32 m0, s99\n" \
  "s_add_u32 s88, s99, 8192\n" \
  "global_load_lds_dwordx4 v10, s[62:63] sc1\n" \
  "global_load_lds_dwordx4 v10, s[62:63] offset:1024 sc1\n" \
  "s_mov_b32 m0, s88\n" \
  "s_nop 0\n" \
  "global_load_lds_dwordx4 v10, s[64:65] sc1\n" \
  "global_load_lds_dwordx4 v10, s[64:65] offset:1024 sc1\n" \
  ".LCV_HAVE_%=:\n" \
  "s_add_u32 s62, s62, 0x80000\n" \
  "s_addc_u32 s63, s63, 0\n" \
  "s_add_u32 s64, s64, 0x40000\n" \
  "s_addc_u32 s65, s65, 0\n" \
  "s_add_u32 s68, s68, 0x1000\n" \
  "s_add_u32 s69, s69, 0x10000\n" \
  "s_add_u32 s70, s70, 0x10000\n" \
  ".LCV_CHK_%=:\n" \
  "v_cmp_eq_u32 vcc, 0, v52\n" \
  "v_readlane_b32 s88, v52, s79\n" \
  "s_cmp_eq_u64 vcc, 0\n" \
  "s_cbranch_scc1 .LCV_OKM_%=\n" \
  "s_sleep 4\n" \
  "buffer_load_dword v52, v2, s[0:3], s66 offen sc1\n" \
  "s_sub_u32 s87, s87, 1\n" \
  "s_cbranch_scc1 .LCV_END_%=\n" \
  "s_waitcnt vmcnt(0)\n" \
  "s_branch .LCV_CHK_%=\n" \
  ".LCV_OKM_%=:\n" \
  "v_subrev_u32 v52, s88, v52\n" \
  "s_nop 1\n" \
  "v_max_i32_dpp v52, v52, v52 row_ror:1 row_mask:0xf bank_mask:0xf\n" \
  "s_nop 1\n" \
  "v_max_i32_dpp v52, v52, v52 row_ror:2 row_mask:0xf bank_mask:0xf\n" \
  "s_nop 1\n" \
  "v_max_i32_dpp v52, v52, v52 row_ror:4 row_mask:0xf bank_mask:0xf\n" \
  "s_add_u32 s66, s66, 1024\n" \
  "v_readlane_b32 vcc_lo, v52, 0\n" \
  "v_readlane_b32 s88, v52, 16\n" \
  "s_max_i32 vcc_lo, vcc_lo, s88\n" \
  "v_readlane_b32 s88, v52, 32\n" \
  "s_max_i32 vcc_lo, vcc_lo, s88\n" \
  "v_readlane_b32 s88, v52, 48\n" \
  "s_max_i32 vcc_lo, vcc_lo, s88\n" \
  "s_waitcnt lgkmcnt(0)\n" \
  "s_add_u32 vcc_lo, vcc_lo, s38\n" \
  "s_add_u32 vcc_lo, vcc_lo, -14\n" \
  ".LCV_TW_%=:\n" \
  "s_memrealtime s[38:39]\n" \
  "s_waitcnt lgkmcnt(0)\n" \
  "s_sub_u32 s38, s38, vcc_lo\n" \
  "s_cmp_lt_i32 s38, 0\n" \
  "s_cbranch_scc1 .LCV_TW_%=\n" \
  ".LCV_POLL_%=:\n" \
  "buffer_load_dwordx4 v[24:27], v44, s[40:43], s89 offen sc1\n" \
  "buffer_load_dwordx4 v[28:31], v44, s[40:43], s89 offen offset:2048 sc1\n" \
  "s_sub_u32 s87, s87, 1\n" \
  "s_cbranch_scc1 .LCV_END_%=\n" \
  "s_waitcnt vmcnt(1)\n" \
  "v_bitop3_b32 v45, v24, v26, 32 bitop3:0x80\n" \
  "v_alignbit_b32 v32, v25, v24, 2\n" \
  "v_alignbit_b32 v34, v27, v26, 2\n" \
  "s_waitcnt vmcnt(0)\n" \
  "v_bitop3_b32 v45, v45, v28, v30 bitop3:0x80\n" \
  "v_cmp_eq_u32 vcc, 0, v45\n" \
  "v_cvt_f32_i32 v32, v32\n" \
  "v_cvt_f32_i32 v33, v25\n" \
  "s_cmp_eq_u64 vcc, 0\n" \
  "s_cbranch_scc0 .LCV_POLL_%=\n" \
  "v_cvt_f32_i32 v34, v34\n" \
  "v_cvt_f32_i32 v35, v27\n" \
  "v_pk_fma_f32 v[32:33], v[32:33], s[80:81], v[46:47]\n" \
  "v_alignbit_b32 v36, v29, v28, 2\n" \
  "v_pk_fma_f32 v[34:35], v[34:35], s[80:81], v[46:47]\n" \
  "v_alignbit_b32 v38, v31, v30, 2\n" \
  "v_cvt_pk_bf16_f32 v32, v32, v34\n" \
  "v_cvt_pk_bf16_f32 v33, v33, v35\n" \
  "v_cvt_f32_i32 v36, v36\n" \
  "ds_write_b32 v40, v32\n" \
  "v_cvt_f32_i32 v37, v29\n" \
  "ds_write_b32 v41, v33\n" \
  "v_cvt_f32_i32 v38, v38\n" \
  "v_cvt_f32_i32 v39, v31\n" \
  "v_pk_fma_f32 v[36:37], v[36:37], s[80:81], v[46:47]\n" \
  "v_pk_fma_f32 v[38:39], v[38:39], s[80:81], v[46:47]\n" \
  "v_dot2c_f32_bf16 v11, v32, v32\n" \
  "v_cvt_pk_bf16_f32 v36, v36, v38\n" \
  "v_cvt_pk_bf16_f32 v37, v37, v39\n" \
  "ds_write_b32 v42, v36\n" \
  "ds_write_b32 v43, v37\n" \
  "v_dot2c_f32_bf16 v11, v33, v33\n" \
  "v_dot2c_f32_bf16 v11, v36, v36\n" \
  "v_dot2c_f32_bf16 v11, v37, v37\n" \
  "s_nop 1\n" \
  "s_nop 0\n" \
  "v_cmpx_lt_f32 vcc, 2.0, v11\n" \
  "ds_max_u32 v59, v11\n" \
  "s_mov_b64 exec, -1\n" \
  "s_branch .LCV_TOP_%=\n" \
  ".LCV_LAST_%=:\n" \
  "s_waitcnt vmcnt(0)\n" \
  ".LCV_END_%=:\n" \
  "s_endpgm\n"
__device__ __forceinline__ v4u mk_srd(const void* p, const unsigned bytes) {
  const unsigned long long a = (unsigned long long)p;
  return v4u{(unsigned)a, (unsigned)(a >> 32) & 0xffffu, bytes, 0x00020000u};
}
template <bool ATT>
__device__ __forceinline__ void role_chain_t(const MegaArgs& A, char* smem, const int L, const int h, const int rep) {
  u16* swo = reinterpret_cast<u16*>(smem);
  const int w = __builtin_amdgcn_readfirstlane(threadIdx.x >> 6);
  char* ws = A.ws;
  unsigned* FL = (unsigned*)(ws + OFF_FL);
  unsigned* TMO = FL + FL_TMO;
#define LANE_IDS()                         \
  const int l = lane_id();                 \
  const int c = l & 15, g = l >> 4;        \
  (void)c; (void)g;
  float* sgf = reinterpret_cast<float*>(smem + C_X);
  unsigned* FQ = FL + FL_FQ + L * 64 * 32 * 32;
  const int wb = (w + ROT_H) & 7;
  v8u w0[8], w1[8], w2[8];
  u64 tag;
  {
    LANE_IDS();
    const unsigned sb = lds_base(smem);
    const int q = (l >> 2) & 3, p = l & 3;
    const int f = (q & 1) | ((g & 1) << 1) | ((q >> 1) << 2);
    const unsigned ra = sb + (16 * (g >> 1) + 4 * (g & 1) + (q & 1) + 8 * (q >> 1)) * 256 + (w ^ f) * 32 + p * 8;
#define W_READ(W, hf, X)                                                       \
  {                                                                           \
    _Pragma("unroll") for (int t = 0; t < 4; ++t) {                           \
      const unsigned a_ = (ra ^ (X)) + (hf) * 8192 + t * 32768;               \
      const v2u r0 = trd(a_), r1 = trd(a_ + 512), r2 = trd(a_ + 16384), r3 = trd(a_ + 16384 + 512); \
      W[4 * (hf) + t] = v8u{r0.x, r0.y, r1.x, r1.y, r2.x, r2.y, r3.x, r3.y};  \
    }                                                                         \
    _Pragma("unroll") for (int t = 0; t < 4; ++t) asm volatile("" : "+v"(W[4 * (hf) + t])); \
  }
#define W_STAGE(M, hf, VM, NEXT)                                               \
  {                                                                           \
    WAIT_VM(VM);                                                              \
    RAW_BAR();                                                                \
    if ((M) == 0 && ATT) { W_READ(w0, hf, 0); W_READ(w2, hf, 128); }          \
    if ((M) == 1 && ATT) { W_READ(w1, hf, 0); }                               \
    if ((M) == 1 && !ATT) { W_READ(w0, hf, 0); }                              \
    if ((M) == 2 && !ATT) { W_READ(w1, hf, 128); W_READ(w2, hf, 0); }         \
    if ((M) == 3) {                                                           \
      _Pragma("unroll") for (int i = 0; i < 8; ++i) {                         \
        const unsigned a_ = rb + (hf) * 8192 + (i & 1) * 65536 + ((4 * wb + (i >> 1)) ^ f) * 32; \
        const v2u r0 = trd(a_), r1 = trd(a_ + 2048);                          \
        fr[i] = v4u{r0.x, r0.y, r1.x, r1.y};                                  \
      }                                                                       \
      _Pragma("unroll") for (int i = 0; i < 8; ++i) asm volatile("" : "+v"(fr[i])); \
      _Pragma("unroll") for (int e = 0; e < 32; ++e) cn[e & 3] = dot2sq(fr[2 * (e & 3) + (e >> 4)][(e >> 2) & 3], cn[e & 3]); \
      if (hf) {                                                               \
        const float m2 = wave_max_dpp(swap_add32(swap_add16(cn[0], cn[1]), swap_add16(cn[2], cn[3]))); \
        if (l == 0) sgf[8 + w] = m2;                                          \
      }                                                                       \
    }                                                                         \
    RAW_BAR();                                                                \
    NEXT;                                                                     \
  }
#define WO_ISSUE(hf)                                                          \
  {                                                                           \
    const char* base_ = (const char*)A.Wo + (size_t)(h * DH + 64 * (hf) + 8 * w) * (DM * 2); \
    _Pragma("unroll") for (int i = 0; i < 8; ++i) {                           \
      const int fd = (i & 1) | (((i >> 2) & 1) << 1) | ((w & 1) << 2);        \
      glds16(base_, (unsigned)(i * 1024 + ((l ^ (fd << 1)) << 4)), sb + w * 16384 + (hf) * 8192 + i * 1024); \
    }                                                                         \
  }
    const unsigned rb = sb + (2 * (g >> 1) + (q >> 1)) * 16384 + (4 * (g & 1) + (q & 1)) * 1024 + p * 8;
    v4u fr[8];
    float cn[4] = {0.f, 0.f, 0.f, 0.f};
#define WO_PUT(hf) \
  _Pragma("unroll") for (int i = 0; i < 8; ++i) *reinterpret_cast<v4u*>(swo + w * 8192 + (hf) * 4096 + i * 512 + l * 8) = fr[i];
    glds16(A.mem0, (unsigned)((c & 7) * DM + 64 * w + 16 * g + 8 * (c >> 3)) * 2, sb + C_SM + w * 1024);
    wqkv_issue<true>(A.Wv, 0, h, w, l, sb);
    wqkv_issue<true>(A.Wv, 1, h, w, l, sb);
    tag = call_tag(A);
    W_STAGE(2, 0, 8, wqkv_issue<true>(A.Wk, 0, h, w, l, sb));
    W_STAGE(2, 1, 8, wqkv_issue<true>(A.Wk, 1, h, w, l, sb));
    SST(4);
    if (!ATT) {
      float f2 = 0.f;
#pragma unroll
      for (int t = 0; t < 8; ++t)
#pragma unroll
        for (int e = 0; e < 8; ++e) f2 = dot2sq(w2[t][e], dot2sq(w1[t][e], f2));
      f2 = wave_sum_dpp(f2);
      if (l == 0) { sgf[w] = f2; sgf[16] = 0.f; }
    }
    W_STAGE(1, 0, 8, wqkv_issue<true>(A.Wq, 0, h, w, l, sb));
    W_STAGE(1, 1, 8, wqkv_issue<true>(A.Wq, 1, h, w, l, sb));
    SST(5);
    W_STAGE(0, 0, 8, WO_ISSUE(0));
    W_STAGE(0, 1, 8, WO_ISSUE(1));
    SST(6);
    W_STAGE(3, 0, 8, WO_PUT(0));
    W_STAGE(3, 1, 0, (void)0);
    const __amdgpu_buffer_rsrc_t rg = MKRS(ws + OFF_G, 4096);
    if (w == 0 && L == 0) {
      const f32x4 a = *reinterpret_cast<const f32x4*>(sgf + 4), b = *reinterpret_cast<const f32x4*>(sgf + 8);
      const f32x4 d = *reinterpret_cast<const f32x4*>(sgf + 12);
      const float m2 = fmaxf(fmaxf(fmaxf(b.x, b.y), fmaxf(b.z, b.w)), fmaxf(fmaxf(d.x, d.y), fmaxf(d.z, d.w)));
      __builtin_amdgcn_raw_buffer_store_b32(__float_as_uint(((a.x + a.y) + (a.z + a.w)) * m2), rg, l * 512 + h * 16 + 8, 0, AUX_SC1);
    }
    WO_PUT(1);
    if (w == 0 && L == 0) {
      DRAIN();
      ST64(__builtin_bit_cast(v2u, tag), rg, l * 512 + h * 16, 0);
    }
    SST(7);
#undef WO_PUT
#undef W_STAGE
#undef W_READ
#undef WO_ISSUE
  }
  WSTAMP2();
  const unsigned sb = lds_base(smem);
  const char* gK = ws + OFF_QK + L * SZ_QK + ((size_t)h * 16 + 2 * w) * 1024;
  const char* gV = ws + OFF_VF + L * SZ_O + ((size_t)h * 8 + 2 * w - 8) * 1024;
  v8u pv;
  {
    LANE_IDS();
    const unsigned voff = g * 256 + c * 8, wp = sb + w * 16384 + l * 16;
    pv[0] = (c & 8) ? 0xEEEE : 0x4444;
    pv[1] = sb + C_SM + l * 16;
    pv[2] = ATT ? sb + C_SQ + ((c & 7) * QS + 8 * g) * 2 : voff;
    pv[3] = ATT ? sb + C_SK + ((4 * ((c >> 2) & 1) + 2 * (c >> 3) + (c & 1)) * QS + 8 * g) * 2 : wp;
    pv[4] = sb + (c < 8 ? C_SO : C_SQ) + w * 256 + ((c & 7) + 8 * (g >> 1)) * 16 + 8 * (g & 1);
    pv[5] = voff;
    pv[6] = wp;
    pv[7] = sb + C_SO + (l >> 5) * 1024 + w * 256 + (l & 31) * 8;
  }
  ready_wait(A, tag, ready_look(A));
  if (!ATT) {
    if (!wait_n<4, 1>(FQ + h * 32, 1, TMO)) return;
    const unsigned lo = lane_id() * 16, d = sb + C_KT + (2 * w - 8) * 1024;
    glds16c(gK, lo, d);
    glds16c(gK + 1024, lo, d + 1024);
    glds16c(gV, lo, d + 8192);
    glds16c(gV + 1024, lo, d + 9216);
  }
  float gw2;
  {
    const __amdgpu_buffer_rsrc_t rg = MKRS(ws + OFF_G, 4096);
    v4u gv;
    for (unsigned spins = 0;; ++spins) {
      gv = LD128(rg, (lane_id() & 31) * 16, (blockIdx.x & 7) * 512);
      if (__all(gv.x == (unsigned)tag && gv.y == (unsigned)(tag >> 32))) break;
      if (spins > SPIN_CAP) return;
      __builtin_amdgcn_s_sleep(GSL);
      asm volatile("" ::: "memory");
    }
    const float gm = wave_max_dpp(__uint_as_float(gv.z));
    asm volatile("s_nop 0\n\tv_readfirstlane_b32 %0, %1" : "=s"(gw2) : "v"(gm * 1.063f));
  }
  DRAIN();
  SST(8);
  const v4u rsA = mk_srd(ws + OFF_ACC + L * SZ_ACC, SZ_ACC);
  const v4u rsF = mk_srd(FQ, 64 * 32 * 32 * 4);
  const v4u rsMK = mk_srd(ws + OFF_MEMK + L * SZ_MEM, SZ_MEM);
  const v4u rsMV = mk_srd(ws + OFF_MEMV + L * SZ_MEM, SZ_MEM);
  const char* pAB = ws + OFF_ACC + L * SZ_ACC + (wb - w) * 4096;
  const char* pGK = gK + NH * 16 * 1024;
  const char* pGV = gV + NH * 8 * 1024;
  const v4u rsT = mk_srd(FL + FL_FK + L * 32, 64 * 1024 - 128);
  unsigned sc[8];
  sc[0] = (32 + h) * 128;
  sc[1] = (h * 4 + w - 4) * 512;
  sc[2] = (h * 4 + w - 4) * 512;
  sc[3] = NSEG - 1;
  sc[4] = __float_as_uint(gw2);
  sc[5] = w;
  sc[6] = sb;
  sc[7] = h == 0 ? 0xd4000000u - 2u - (1u << 29) : 0xd4000000u;
  const char* dbg = ws;
#define CHAIN_OPS                                                                                                          \
  ::"{v[64:71]}"(w0[0]), "{v[72:79]}"(w0[1]), "{v[80:87]}"(w0[2]), "{v[88:95]}"(w0[3]), "{v[96:103]}"(w0[4]),              \
      "{v[104:111]}"(w0[5]), "{v[112:119]}"(w0[6]), "{v[120:127]}"(w0[7]), "{v[128:135]}"(w1[0]), "{v[136:143]}"(w1[1]),    \
      "{v[144:151]}"(w1[2]), "{v[152:159]}"(w1[3]), "{v[160:167]}"(w1[4]), "{v[168:175]}"(w1[5]), "{v[176:183]}"(w1[6]),    \
      "{v[184:191]}"(w1[7]), "{v[192:199]}"(w2[0]), "{v[200:207]}"(w2[1]), "{v[208:215]}"(w2[2]), "{v[216:223]}"(w2[3]),    \
      "{v[224:231]}"(w2[4]), "{v[232:239]}"(w2[5]), "{v[240:247]}"(w2[6]), "{v[248:255]}"(w2[7]), "{v[0:7]}"(pv),          \
      "{s[40:43]}"(rsA), "{s[44:47]}"(rsF), "{s[48:51]}"(rsMK), "{s[52:55]}"(rsMV), "{s[56:57]}"(pAB), "{s60}"(w * 4096), "{s61}"(ATT ? h * 1024 : w != 4 ? 0x40000000 : L ? (PNL - 1) * 1024 - 128 : 128 - (PNL + 2) * 1024), \
      "{s[62:63]}"(pGK), "{s[64:65]}"(pGV), "{s66}"(0), "{s67}"(w == 4 ? (h >> 3) * 256 + (h & 7) * 8 : 0x40000000), "{s68}"(sc[0]), "{s69}"(sc[1]), "{s70}"(sc[2]),    \
      "{s71}"(sc[3]), "{s72}"(sc[4]), "{s73}"(sc[5]), "{s74}"(sc[6]), "{s75}"(sc[7]), "{s[36:37]}"(dbg),                   \
      "{s[0:3]}"(rsT), "{s79}"((h >> 3) * 16 + (h & 7))                                                                        \
      : "memory", "vcc", "scc", "s99", "v63"
  if (ATT) asm volatile("s_mov_b64 exec, -1\ns_nop 4\n" CHAIN_ASM_ATT CHAIN_OPS);
  else asm volatile("s_mov_b64 exec, -1\ns_nop 4\n" CHAIN_ASM_VO CHAIN_OPS);
#undef CHAIN_OPS
#undef LANE_IDS
}

__device__ __forceinline__ void role_chain(const MegaArgs& A, char* smem, const int L, const int h, const int rep) {
  if (__builtin_amdgcn_readfirstlane(threadIdx.x >> 6) < 4) role_chain_t<true>(A, smem, L, h, rep);
  else role_chain_t<false>(A, smem, L, h, rep);
}

constexpr int AW_SO = 0, AW_TK = 32768, AW_CNT = AW_TK + 98304;
__device__ __forceinline__ unsigned umin3(unsigned a, unsigned b, unsigned c) {
  return __builtin_elementwise_min(__builtin_elementwise_min(a, b), c);
}
#define AW_ASM_X \
  "s_mov_b32 s61, 0\n" \
  "s_mov_b32 s67, 1\n" \
  "s_mov_b32 s68, 0\n" \
  "s_mov_b32 s70, 0\n" \
  "s_mov_b32 s73, 0\n" \
  "s_mov_b32 s71, 0x3e0293ee\n" \
  "s_mov_b32 s72, 0xbe0293ee\n" \
  "s_mov_b32 s65, 49152\n" \
  "s_mov_b32 s66, 16384\n" \
  "v_mov_b32 v38, s58\n" \
  "s_memrealtime s[38:39]\n" \
  "s_waitcnt lgkmcnt(0)\n" \
  "s_mov_b32 s69, s38\n" \
  "s_setprio 3\n" \
  ".LCX_TOP_%=:\n" \
  "s_mov_b32 s62, 0x200000\n" \
  "s_cmp_ge_u32 s73, s67\n" \
  "s_cbranch_scc1 .LCX_TOK_%=\n" \
  "s_cmp_eq_u32 s61, 0\n" \
  "s_cbranch_scc1 .LCX_SPIN_%=\n" \
  "s_waitcnt vmcnt(0)\n" \
  "v_mov_b32 v46, s59\n" \
  "v_mov_b32 v47, 1\n" \
  "s_mov_b64 exec, 1\n" \
  "ds_add_rtn_u32 v46, v46, v47\n" \
  "s_mov_b64 exec, -1\n" \
  "s_xor_b32 s59, s59, 4\n" \
  "s_mov_b32 s61, 0\n" \
  "s_waitcnt lgkmcnt(0)\n" \
  "v_readfirstlane_b32 s63, v46\n" \
  "v_lshlrev_b32 v46, 5, v5\n" \
  "s_and_b32 s63, s63, 7\n" \
  "s_cmp_eq_u32 s63, 7\n" \
  "s_cbranch_scc0 .LCX_W1_%=\n" \
  "s_mov_b64 exec, 15\n" \
  "global_store_dword v46, v47, s[52:53] sc1\n" \
  "s_mov_b64 exec, -1\n" \
  ".LCX_W1_%=:\n" \
  "s_add_u32 s52, s52, 2048\n" \
  "s_addc_u32 s53, s53, 0\n" \
  ".LCX_SPIN_%=:\n" \
  "s_sleep 2\n" \
  "s_memrealtime s[38:39]\n" \
  "ds_read_b32 v37, v38\n" \
  "s_sub_u32 s62, s62, 1\n" \
  "s_cbranch_scc1 .LCX_END_%=\n" \
  "s_waitcnt lgkmcnt(0)\n" \
  "v_readfirstlane_b32 s73, v37\n" \
  "s_add_u32 s38, s38, 80\n" \
  "s_bfe_u32 s64, s73, 0x80008\n" \
  "s_and_b32 s73, s73, 0xff\n" \
  "s_min_u32 s73, s73, s64\n" \
  "s_cmp_ge_u32 s73, s67\n" \
  "s_cbranch_scc0 .LCX_SPIN_%=\n" \
  "s_cmp_eq_u32 s70, 0\n" \
  "s_cbranch_scc1 .LCX_TOK_%=\n" \
  "s_sub_u32 s63, s38, s69\n" \
  "s_max_i32 s63, s63, 0\n" \
  "s_add_u32 s69, s69, s63\n" \
  ".LCX_TOK_%=:\n" \
  "ds_read_b128 v[48:51], v6 offset:0\n" \
  "ds_read_b128 v[64:67], v0 offset:8192\n" \
  "ds_read_b128 v[80:83], v0 offset:12288\n" \
  "ds_read_b128 v[52:55], v6 offset:1024\n" \
  "ds_read_b128 v[68:71], v0 offset:9216\n" \
  "ds_read_b128 v[84:87], v0 offset:13312\n" \
  "ds_read_b128 v[56:59], v6 offset:2048\n" \
  "ds_read_b128 v[72:75], v0 offset:10240\n" \
  "ds_read_b128 v[88:91], v0 offset:14336\n" \
  "ds_read_b128 v[60:63], v6 offset:3072\n" \
  "ds_read_b128 v[76:79], v0 offset:11264\n" \
  "ds_read_b128 v[92:95], v0 offset:15360\n" \
  "s_waitcnt lgkmcnt(9)\n" \
  "v_mfma_f32_16x16x32_bf16 v[20:23], v[64:67], v[48:51], v[8:11]\n" \
  "v_mfma_f32_16x16x32_bf16 v[24:27], v[80:83], v[48:51], v[12:15]\n" \
  "ds_read_b128 v[96:99], v0 offset:16384\n" \
  "ds_read_b128 v[100:103], v0 offset:17408\n" \
  "s_waitcnt lgkmcnt(8)\n" \
  "v_mfma_f32_16x16x32_bf16 v[20:23], v[68:71], v[52:55], v[20:23]\n" \
  "v_mfma_f32_16x16x32_bf16 v[24:27], v[84:87], v[52:55], v[24:27]\n" \
  "ds_read_b128 v[104:107], v0 offset:18432\n" \
  "ds_read_b128 v[108:111], v0 offset:19456\n" \
  "s_waitcnt lgkmcnt(7)\n" \
  "v_mfma_f32_16x16x32_bf16 v[20:23], v[72:75], v[56:59], v[20:23]\n" \
  "v_mfma_f32_16x16x32_bf16 v[24:27], v[88:91], v[56:59], v[24:27]\n" \
  "ds_read_b128 v[112:115], v0 offset:20480\n" \
  "ds_read_b128 v[116:119], v0 offset:21504\n" \
  "s_waitcnt lgkmcnt(6)\n" \
  "v_mfma_f32_16x16x32_bf16 v[20:23], v[76:79], v[60:63], v[20:23]\n" \
  "v_mfma_f32_16x16x32_bf16 v[24:27], v[92:95], v[60:63], v[24:27]\n" \
  "ds_read_b128 v[120:123], v0 offset:22528\n" \
  "ds_read_b128 v[124:127], v0 offset:23552\n" \
  "s_add_u32 s67, s67, 1\n" \
  "v_add_u32 v0, s65, v0\n" \
  "v_add_u32 v6, s65, v6\n" \
  "s_sub_u32 s65, 0, s65\n" \
  "s_nop 1\n" \
  "v_max3_f32 v32, v20, v21, v22\n" \
  "v_max3_f32 v33, v23, v24, v25\n" \
  "v_max3_f32 v32, v32, v26, v27\n" \
  "v_max_f32 v32, v32, v33\n" \
  "v_mov_b32 v33, v32\n" \
  "s_nop 1\n" \
  "v_permlane16_swap_b32 v32, v33\n" \
  "v_max_f32 v32, v32, v33\n" \
  "v_mov_b32 v33, v32\n" \
  "s_nop 1\n" \
  "v_permlane32_swap_b32 v32, v33\n" \
  "v_max_f32 v32, v32, v33\n" \
  "v_mul_f32 v33, s72, v32\n" \
  "v_fma_f32 v20, v20, s71, v33\n" \
  "v_fma_f32 v21, v21, s71, v33\n" \
  "v_fma_f32 v22, v22, s71, v33\n" \
  "v_fma_f32 v23, v23, s71, v33\n" \
  "v_fma_f32 v24, v24, s71, v33\n" \
  "v_fma_f32 v25, v25, s71, v33\n" \
  "v_fma_f32 v26, v26, s71, v33\n" \
  "v_fma_f32 v27, v27, s71, v33\n" \
  "v_exp_f32 v20, v20\n" \
  "v_exp_f32 v21, v21\n" \
  "v_exp_f32 v22, v22\n" \
  "v_exp_f32 v23, v23\n" \
  "v_exp_f32 v24, v24\n" \
  "v_exp_f32 v25, v25\n" \
  "v_exp_f32 v26, v26\n" \
  "v_exp_f32 v27, v27\n" \
  "v_add_f32 v34, v20, v21\n" \
  "v_add_f32 v33, v22, v23\n" \
  "v_add_f32 v36, v24, v25\n" \
  "v_add_f32 v34, v34, v33\n" \
  "v_add_f32 v33, v26, v27\n" \
  "v_add_f32 v34, v34, v36\n" \
  "v_add_f32 v34, v34, v33\n" \
  "s_waitcnt vmcnt(0) lgkmcnt(0)\n" \
  "s_cmp_eq_u32 s61, 0\n" \
  "s_cbranch_scc1 .LCX_NP_%=\n" \
  "v_mov_b32 v46, s59\n" \
  "v_mov_b32 v47, 1\n" \
  "s_mov_b64 exec, 1\n" \
  "ds_add_rtn_u32 v46, v46, v47\n" \
  "s_mov_b64 exec, -1\n" \
  "s_xor_b32 s59, s59, 4\n" \
  "s_mov_b32 s61, 0\n" \
  "s_waitcnt lgkmcnt(0)\n" \
  "v_readfirstlane_b32 s63, v46\n" \
  "v_lshlrev_b32 v46, 5, v5\n" \
  "s_and_b32 s63, s63, 7\n" \
  "s_cmp_eq_u32 s63, 7\n" \
  "s_cbranch_scc0 .LCX_W2_%=\n" \
  "s_mov_b64 exec, 15\n" \
  "global_store_dword v46, v47, s[52:53] sc1\n" \
  "s_mov_b64 exec, -1\n" \
  ".LCX_W2_%=:\n" \
  "s_add_u32 s52, s52, 2048\n" \
  "s_addc_u32 s53, s53, 0\n" \
  ".LCX_NP_%=:\n" \
  "s_cmp_lg_u32 s68, 0\n" \
  "s_cbranch_scc1 .LCX_TEST_%=\n" \
  ".LCX_WF_%=:\n" \
  "s_memrealtime s[38:39]\n" \
  "s_waitcnt lgkmcnt(0)\n" \
  "s_sub_u32 s63, s69, s38\n" \
  "s_cmp_le_i32 s63, 0\n" \
  "s_cbranch_scc1 .LCX_GO_%=\n" \
  "s_cmp_lt_i32 s63, 12\n" \
  "s_cbranch_scc1 .LCX_WF_%=\n" \
  "s_sleep 3\n" \
  "s_branch .LCX_WF_%=\n" \
  ".LCX_GO_%=:\n" \
  "v_mov_b32 v35, 0\n" \
  "global_load_dwordx4 v[128:131], v2, s[48:49] offset:0 sc1\n" \
  "global_load_dwordx4 v[132:135], v2, s[48:49] offset:512 sc1\n" \
  "global_load_dwordx4 v[136:139], v2, s[48:49] offset:1024 sc1\n" \
  "global_load_dwordx4 v[140:143], v2, s[48:49] offset:1536 sc1\n" \
  "global_load_dwordx4 v[144:147], v3, s[48:49] offset:0 sc1\n" \
  "global_load_dwordx4 v[148:151], v3, s[48:49] offset:512 sc1\n" \
  "global_load_dwordx4 v[152:155], v3, s[48:49] offset:1024 sc1\n" \
  "global_load_dwordx4 v[156:159], v3, s[48:49] offset:1536 sc1\n" \
  "global_load_dword v35, v35, s[50:51] sc1\n" \
  "s_sub_u32 s62, s62, 8\n" \
  "s_cbranch_scc1 .LCX_END_%=\n" \
  "s_waitcnt vmcnt(0)\n" \
  ".LCX_TEST_%=:\n" \
  "v_mfma_f32_16x16x32_bf16 v[28:31], v[128:131], v[48:51], v[16:19]\n" \
  "v_min3_u32 v33, v128, v129, v130\n" \
  "v_min3_u32 v36, v131, v132, v133\n" \
  "v_min3_u32 v33, v33, v134, v135\n" \
  "v_min3_u32 v36, v36, v136, v137\n" \
  "v_mfma_f32_16x16x32_bf16 v[28:31], v[132:135], v[52:55], v[28:31]\n" \
  "v_min3_u32 v33, v33, v138, v139\n" \
  "v_min3_u32 v36, v36, v140, v141\n" \
  "v_min3_u32 v33, v33, v142, v143\n" \
  "v_min3_u32 v36, v36, v144, v145\n" \
  "v_mfma_f32_16x16x32_bf16 v[28:31], v[136:139], v[56:59], v[28:31]\n" \
  "v_min3_u32 v33, v33, v146, v147\n" \
  "v_min3_u32 v36, v36, v148, v149\n" \
  "v_min3_u32 v33, v33, v150, v151\n" \
  "v_min3_u32 v36, v36, v152, v153\n" \
  "v_mfma_f32_16x16x32_bf16 v[28:31], v[140:143], v[60:63], v[28:31]\n" \
  "v_min3_u32 v33, v33, v154, v155\n" \
  "v_min3_u32 v36, v36, v156, v157\n" \
  "v_min3_u32 v33, v33, v158, v159\n" \
  "v_min_u32 v33, v33, v36\n" \
  "v_cmp_eq_u32 vcc, 0, v33\n" \
  "v_readfirstlane_b32 s64, v35\n" \
  "s_mov_b32 s68, 0\n" \
  "s_cmp_eq_u64 vcc, 0\n" \
  "s_cbranch_scc0 .LCX_FAIL_%=\n" \
  "v_max3_f32 v36, v28, v29, v30\n" \
  "v_max3_f32 v36, v36, v31, v32\n" \
  "v_mov_b32 v33, v36\n" \
  "s_cmp_eq_u32 s64, 0\n" \
  "s_cselect_b32 s64, s38, s64\n" \
  "v_permlane16_swap_b32 v36, v33\n" \
  "v_max_f32 v36, v36, v33\n" \
  "v_mov_b32 v33, v36\n" \
  "s_sub_u32 s63, s64, s70\n" \
  "s_mov_b32 s70, s64\n" \
  "v_permlane32_swap_b32 v36, v33\n" \
  "v_max_f32 v36, v36, v33\n" \
  "v_mul_f32 v36, s72, v36\n" \
  "v_fma_f32 v32, v32, s71, v36\n" \
  "v_fma_f32 v28, v28, s71, v36\n" \
  "v_fma_f32 v29, v29, s71, v36\n" \
  "v_fma_f32 v30, v30, s71, v36\n" \
  "v_fma_f32 v31, v31, s71, v36\n" \
  "v_exp_f32 v32, v32\n" \
  "v_exp_f32 v28, v28\n" \
  "v_exp_f32 v29, v29\n" \
  "v_exp_f32 v30, v30\n" \
  "v_exp_f32 v31, v31\n" \
  "v_fma_f32 v34, v34, v32, v28\n" \
  "v_add_f32 v33, v29, v30\n" \
  "v_add_f32 v34, v34, v31\n" \
  "v_add_f32 v34, v34, v33\n" \
  "v_mov_b32 v33, v34\n" \
  "s_min_u32 s63, s63, 250\n" \
  "s_add_u32 s69, s64, s63\n" \
  "v_permlane16_swap_b32 v34, v33\n" \
  "v_add_f32 v34, v34, v33\n" \
  "v_mov_b32 v33, v34\n" \
  "s_add_u32 s69, s69, 68\n" \
  "s_add_u32 s48, s48, 0x10000\n" \
  "s_addc_u32 s49, s49, 0\n" \
  "v_permlane32_swap_b32 v34, v33\n" \
  "v_add_f32 v34, v34, v33\n" \
  "v_rcp_f32 v34, v34\n" \
  "s_add_u32 s50, s50, 1024\n" \
  "s_addc_u32 s51, s51, 0\n" \
  "v_mul_f32 v32, v32, v34\n" \
  "v_mul_f32 v20, v20, v32\n" \
  "v_mul_f32 v21, v21, v32\n" \
  "v_mul_f32 v22, v22, v32\n" \
  "v_mul_f32 v23, v23, v32\n" \
  "v_mul_f32 v24, v24, v32\n" \
  "v_mul_f32 v25, v25, v32\n" \
  "v_mul_f32 v26, v26, v32\n" \
  "v_mul_f32 v27, v27, v32\n" \
  "v_cvt_pk_bf16_f32 v40, v20, v21\n" \
  "v_cvt_pk_bf16_f32 v41, v22, v23\n" \
  "v_cvt_pk_bf16_f32 v42, v24, v25\n" \
  "v_cvt_pk_bf16_f32 v43, v26, v27\n" \
  "v_mul_f32 v28, v28, v34\n" \
  "v_mul_f32 v29, v29, v34\n" \
  "v_mfma_f32_16x16x32_bf16 v[64:67], v[96:99], v[40:43], 0\n" \
  "v_mul_f32 v30, v30, v34\n" \
  "v_mfma_f32_16x16x32_bf16 v[68:71], v[100:103], v[40:43], 0\n" \
  "v_mul_f32 v31, v31, v34\n" \
  "v_mfma_f32_16x16x32_bf16 v[72:75], v[104:107], v[40:43], 0\n" \
  "v_cvt_pk_bf16_f32 v44, v28, v29\n" \
  "v_mfma_f32_16x16x32_bf16 v[76:79], v[108:111], v[40:43], 0\n" \
  "v_cvt_pk_bf16_f32 v45, v30, v31\n" \
  "v_mfma_f32_16x16x32_bf16 v[80:83], v[112:115], v[40:43], 0\n" \
  "v_mfma_f32_16x16x32_bf16 v[84:87], v[116:119], v[40:43], 0\n" \
  "v_mfma_f32_16x16x32_bf16 v[88:91], v[120:123], v[40:43], 0\n" \
  "v_mfma_f32_16x16x32_bf16 v[92:95], v[124:127], v[40:43], 0\n" \
  "s_memrealtime s[38:39]\n" \
  "ds_read_b32 v37, v38\n" \
  "v_mfma_f32_16x16x16_bf16 v[64:67], v[144:145], v[44:45], v[64:67]\n" \
  "v_mfma_f32_16x16x16_bf16 v[68:71], v[148:149], v[44:45], v[68:71]\n" \
  "v_mfma_f32_16x16x16_bf16 v[72:75], v[152:153], v[44:45], v[72:75]\n" \
  "v_mfma_f32_16x16x16_bf16 v[76:79], v[156:157], v[44:45], v[76:79]\n" \
  "v_mfma_f32_16x16x16_bf16 v[80:83], v[146:147], v[44:45], v[80:83]\n" \
  "v_mfma_f32_16x16x16_bf16 v[84:87], v[150:151], v[44:45], v[84:87]\n" \
  "v_mfma_f32_16x16x16_bf16 v[88:91], v[154:155], v[44:45], v[88:91]\n" \
  "v_mfma_f32_16x16x16_bf16 v[92:95], v[158:159], v[44:45], v[92:95]\n" \
  "s_nop 0\n" \
  "v_cvt_pk_bf16_f32 v160, v64, v65\n" \
  "v_cvt_pk_bf16_f32 v161, v66, v67\n" \
  "v_cvt_pk_bf16_f32 v162, v68, v69\n" \
  "v_cvt_pk_bf16_f32 v163, v70, v71\n" \
  "ds_write_b128 v7, v[160:163] offset:0\n" \
  "v_cvt_pk_bf16_f32 v164, v72, v73\n" \
  "v_cvt_pk_bf16_f32 v165, v74, v75\n" \
  "v_cvt_pk_bf16_f32 v166, v76, v77\n" \
  "v_cvt_pk_bf16_f32 v167, v78, v79\n" \
  "ds_write_b128 v7, v[164:167] offset:1024\n" \
  "v_cvt_pk_bf16_f32 v168, v80, v81\n" \
  "v_cvt_pk_bf16_f32 v169, v82, v83\n" \
  "v_cvt_pk_bf16_f32 v170, v84, v85\n" \
  "v_cvt_pk_bf16_f32 v171, v86, v87\n" \
  "ds_write_b128 v7, v[168:171] offset:2048\n" \
  "v_cvt_pk_bf16_f32 v172, v88, v89\n" \
  "v_cvt_pk_bf16_f32 v173, v90, v91\n" \
  "v_cvt_pk_bf16_f32 v174, v92, v93\n" \
  "v_cvt_pk_bf16_f32 v175, v94, v95\n" \
  "ds_write_b128 v7, v[172:175] offset:3072\n" \
  "v_add_u32 v7, s66, v7\n" \
  "s_waitcnt lgkmcnt(0)\n" \
  "s_barrier\n" \
  "ds_read_b128 v[48:51], v1 offset:0\n" \
  "ds_read_b128 v[52:55], v1 offset:4096\n" \
  "ds_read_b128 v[56:59], v1 offset:1024\n" \
  "ds_read_b128 v[60:63], v1 offset:5120\n" \
  "ds_read_b128 v[64:67], v1 offset:2048\n" \
  "ds_read_b128 v[68:71], v1 offset:6144\n" \
  "ds_read_b128 v[72:75], v1 offset:3072\n" \
  "ds_read_b128 v[76:79], v1 offset:7168\n" \
  "ds_read_b128 v[80:83], v1 offset:8192\n" \
  "ds_read_b128 v[84:87], v1 offset:12288\n" \
  "ds_read_b128 v[88:91], v1 offset:9216\n" \
  "ds_read_b128 v[92:95], v1 offset:13312\n" \
  "ds_read_b128 v[96:99], v1 offset:10240\n" \
  "ds_read_b128 v[100:103], v1 offset:14336\n" \
  "v_readfirstlane_b32 s73, v37\n" \
  "s_sub_u32 s63, s38, s69\n" \
  "s_bfe_u32 s64, s73, 0x80010\n" \
  "s_sub_u32 s64, s64, s67\n" \
  "s_or_b32 s63, s63, s64\n" \
  "s_bfe_u32 s64, s73, 0x80008\n" \
  "s_and_b32 s73, s73, 0xff\n" \
  "s_min_u32 s73, s73, s64\n" \
  "s_or_b32 s63, s63, s68\n" \
  "s_cmp_lt_i32 s63, 0\n" \
  "s_cbranch_scc1 .LCX_E3_%=\n" \
  "v_mov_b32 v35, 0\n" \
  "global_load_dwordx4 v[128:131], v2, s[48:49] offset:0 sc1\n" \
  "global_load_dwordx4 v[132:135], v2, s[48:49] offset:512 sc1\n" \
  "global_load_dwordx4 v[136:139], v2, s[48:49] offset:1024 sc1\n" \
  "global_load_dwordx4 v[140:143], v2, s[48:49] offset:1536 sc1\n" \
  "global_load_dwordx4 v[144:147], v3, s[48:49] offset:0 sc1\n" \
  "global_load_dwordx4 v[148:151], v3, s[48:49] offset:512 sc1\n" \
  "global_load_dwordx4 v[152:155], v3, s[48:49] offset:1024 sc1\n" \
  "global_load_dwordx4 v[156:159], v3, s[48:49] offset:1536 sc1\n" \
  "global_load_dword v35, v35, s[50:51] sc1\n" \
  "s_mov_b32 s68, -1\n" \
  ".LCX_E3_%=:\n" \
  "s_waitcnt lgkmcnt(12)\n" \
  "v_mfma_f32_16x16x32_bf16 v[112:115], v[192:195], v[48:51], 0\n" \
  "v_mfma_f32_16x16x32_bf16 v[116:119], v[224:227], v[48:51], 0\n" \
  "v_mfma_f32_16x16x32_bf16 v[120:123], v[192:195], v[52:55], 0\n" \
  "v_mfma_f32_16x16x32_bf16 v[124:127], v[224:227], v[52:55], 0\n" \
  "ds_read_b128 v[104:107], v1 offset:11264\n" \
  "ds_read_b128 v[108:111], v1 offset:15360\n" \
  "s_waitcnt lgkmcnt(12)\n" \
  "v_mfma_f32_16x16x32_bf16 v[112:115], v[196:199], v[56:59], v[112:115]\n" \
  "v_mfma_f32_16x16x32_bf16 v[116:119], v[228:231], v[56:59], v[116:119]\n" \
  "v_mfma_f32_16x16x32_bf16 v[120:123], v[196:199], v[60:63], v[120:123]\n" \
  "v_mfma_f32_16x16x32_bf16 v[124:127], v[228:231], v[60:63], v[124:127]\n" \
  "s_waitcnt lgkmcnt(10)\n" \
  "v_mfma_f32_16x16x32_bf16 v[112:115], v[200:203], v[64:67], v[112:115]\n" \
  "v_mfma_f32_16x16x32_bf16 v[116:119], v[232:235], v[64:67], v[116:119]\n" \
  "v_mfma_f32_16x16x32_bf16 v[120:123], v[200:203], v[68:71], v[120:123]\n" \
  "v_mfma_f32_16x16x32_bf16 v[124:127], v[232:235], v[68:71], v[124:127]\n" \
  "s_waitcnt lgkmcnt(8)\n" \
  "v_mfma_f32_16x16x32_bf16 v[112:115], v[204:207], v[72:75], v[112:115]\n" \
  "v_mfma_f32_16x16x32_bf16 v[116:119], v[236:239], v[72:75], v[116:119]\n" \
  "v_mfma_f32_16x16x32_bf16 v[120:123], v[204:207], v[76:79], v[120:123]\n" \
  "v_mfma_f32_16x16x32_bf16 v[124:127], v[236:239], v[76:79], v[124:127]\n" \
  "s_waitcnt lgkmcnt(6)\n" \
  "v_mfma_f32_16x16x32_bf16 v[112:115], v[208:211], v[80:83], v[112:115]\n" \
  "v_mfma_f32_16x16x32_bf16 v[116:119], v[240:243], v[80:83], v[116:119]\n" \
  "v_mfma_f32_16x16x32_bf16 v[120:123], v[208:211], v[84:87], v[120:123]\n" \
  "v_mfma_f32_16x16x32_bf16 v[124:127], v[240:243], v[84:87], v[124:127]\n" \
  "s_waitcnt lgkmcnt(4)\n" \
  "v_mfma_f32_16x16x32_bf16 v[112:115], v[212:215], v[88:91], v[112:115]\n" \
  "v_mfma_f32_16x16x32_bf16 v[116:119], v[244:247], v[88:91], v[116:119]\n" \
  "v_mfma_f32_16x16x32_bf16 v[120:123], v[212:215], v[92:95], v[120:123]\n" \
  "v_mfma_f32_16x16x32_bf16 v[124:127], v[244:247], v[92:95], v[124:127]\n" \
  "s_memrealtime s[38:39]\n" \
  "ds_read_b32 v37, v38\n" \
  "s_waitcnt lgkmcnt(3)\n" \
  "v_mfma_f32_16x16x32_bf16 v[112:115], v[216:219], v[96:99], v[112:115]\n" \
  "v_mfma_f32_16x16x32_bf16 v[116:119], v[248:251], v[96:99], v[116:119]\n" \
  "v_mfma_f32_16x16x32_bf16 v[120:123], v[216:219], v[100:103], v[120:123]\n" \
  "v_mfma_f32_16x16x32_bf16 v[124:127], v[248:251], v[100:103], v[124:127]\n" \
  "s_waitcnt lgkmcnt(0)\n" \
  "v_mfma_f32_16x16x32_bf16 v[112:115], v[220:223], v[104:107], v[112:115]\n" \
  "v_mfma_f32_16x16x32_bf16 v[116:119], v[252:255], v[104:107], v[116:119]\n" \
  "v_mfma_f32_16x16x32_bf16 v[120:123], v[220:223], v[108:111], v[120:123]\n" \
  "v_mfma_f32_16x16x32_bf16 v[124:127], v[252:255], v[108:111], v[124:127]\n" \
  "v_add_u32 v1, s66, v1\n" \
  "s_sub_u32 s66, 0, s66\n" \
  "s_add_u32 s64, s54, 16384\n" \
  "s_mov_b32 s61, 1\n" \
  "s_nop 1\n" \
  "v_cvt_pk_bf16_f32 v20, v112, v113\n" \
  "v_cvt_pk_bf16_f32 v21, v114, v115\n" \
  "v_cvt_pk_bf16_f32 v22, v116, v117\n" \
  "v_cvt_pk_bf16_f32 v23, v118, v119\n" \
  "buffer_store_dwordx4 v[20:23], v4, s[40:43], s54 offen sc1\n" \
  "v_cvt_pk_bf16_f32 v24, v120, v121\n" \
  "v_cvt_pk_bf16_f32 v25, v122, v123\n" \
  "v_cvt_pk_bf16_f32 v26, v124, v125\n" \
  "v_cvt_pk_bf16_f32 v27, v126, v127\n" \
  "buffer_store_dwordx4 v[24:27], v4, s[40:43], s64 offen sc1\n" \
  "v_readfirstlane_b32 s73, v37\n" \
  "s_sub_u32 s63, s38, s69\n" \
  "s_bfe_u32 s64, s73, 0x80010\n" \
  "s_sub_u32 s64, s64, s67\n" \
  "s_or_b32 s63, s63, s64\n" \
  "s_bfe_u32 s64, s73, 0x80008\n" \
  "s_and_b32 s73, s73, 0xff\n" \
  "s_min_u32 s73, s73, s64\n" \
  "s_or_b32 s63, s63, s68\n" \
  "s_cmp_lt_i32 s63, 0\n" \
  "s_cbranch_scc1 .LCX_E4_%=\n" \
  "v_mov_b32 v35, 0\n" \
  "global_load_dwordx4 v[128:131], v2, s[48:49] offset:0 sc1\n" \
  "global_load_dwordx4 v[132:135], v2, s[48:49] offset:512 sc1\n" \
  "global_load_dwordx4 v[136:139], v2, s[48:49] offset:1024 sc1\n" \
  "global_load_dwordx4 v[140:143], v2, s[48:49] offset:1536 sc1\n" \
  "global_load_dwordx4 v[144:147], v3, s[48:49] offset:0 sc1\n" \
  "global_load_dwordx4 v[148:151], v3, s[48:49] offset:512 sc1\n" \
  "global_load_dwordx4 v[152:155], v3, s[48:49] offset:1024 sc1\n" \
  "global_load_dwordx4 v[156:159], v3, s[48:49] offset:1536 sc1\n" \
  "global_load_dword v35, v35, s[50:51] sc1\n" \
  "s_mov_b32 s68, -1\n" \
  ".LCX_E4_%=:\n" \
  "s_add_u32 s54, s54, 0x80000\n" \
  "s_sub_u32 s60, s60, 1\n" \
  "s_cmp_lg_u32 s60, 0\n" \
  "s_cbranch_scc1 .LCX_TOP_%=\n" \
  "s_waitcnt vmcnt(0)\n" \
  "v_mov_b32 v46, s59\n" \
  "v_mov_b32 v47, 1\n" \
  "s_mov_b64 exec, 1\n" \
  "ds_add_rtn_u32 v46, v46, v47\n" \
  "s_mov_b64 exec, -1\n" \
  "s_xor_b32 s59, s59, 4\n" \
  "s_mov_b32 s61, 0\n" \
  "s_waitcnt lgkmcnt(0)\n" \
  "v_readfirstlane_b32 s63, v46\n" \
  "v_lshlrev_b32 v46, 5, v5\n" \
  "s_and_b32 s63, s63, 7\n" \
  "s_cmp_eq_u32 s63, 7\n" \
  "s_cbranch_scc0 .LCX_W5_%=\n" \
  "s_mov_b64 exec, 15\n" \
  "global_store_dword v46, v47, s[52:53] sc1\n" \
  "s_mov_b64 exec, -1\n" \
  ".LCX_W5_%=:\n" \
  "s_add_u32 s52, s52, 2048\n" \
  "s_addc_u32 s53, s53, 0\n" \
  ".LCX_END_%=:\n" \
  "s_endpgm\n" \
  ".LCX_FAIL_%=:\n" \
  "s_add_u32 s69, s64, 68\n" \
  "s_cmp_lg_u32 s64, 0\n" \
  "s_cbranch_scc1 .LCX_WF_%=\n" \
  "s_sleep 4\n" \
  "global_load_dword v35, v35, s[50:51] sc1\n" \
  "s_sub_u32 s62, s62, 8\n" \
  "s_cbranch_scc1 .LCX_END_%=\n" \
  "s_waitcnt vmcnt(0)\n" \
  "v_readfirstlane_b32 s64, v35\n" \
  "s_branch .LCX_FAIL_%=\n"
#define AW_ASM_Y \
  "s_mov_b32 s67, 0\n" \
  "s_mov_b32 s68, 2\n" \
  "s_mov_b32 s69, 0\n" \
  "s_mov_b32 s65, 49152\n" \
  "s_mov_b32 s66, 16384\n" \
  "v_mov_b32 v5, s58\n" \
  "v_add_u32 v3, 4096, v2\n" \
  "s_mov_b32 s62, 0x40000\n" \
  "s_branch .LCY_ASK_%=\n" \
  ".LCY_BAR_%=:\n" \
  "s_barrier\n" \
  "ds_read_b128 v[8:11], v0 offset:0\n" \
  "ds_read_b128 v[12:15], v0 offset:1024\n" \
  "ds_read_b128 v[16:19], v0 offset:2048\n" \
  "s_add_u32 s68, s68, 1\n" \
  "s_add_u32 s64, s54, 16384\n" \
  "s_cmp_eq_u32 s69, 0\n" \
  "s_cbranch_scc1 .LCY_NI_%=\n" \
  "s_waitcnt vmcnt(1)\n" \
  "s_cmp_lg_u32 s61, 0\n" \
  "s_cbranch_scc1 .LCY_NJ_%=\n" \
  "s_waitcnt vmcnt(0)\n" \
  ".LCY_NJ_%=:\n" \
  "v_mov_b32 v6, s55\n" \
  "s_mov_b64 exec, 1\n" \
  "ds_add_u32 v5, v6\n" \
  "s_mov_b64 exec, -1\n" \
  "s_mov_b32 s69, 0\n" \
  ".LCY_NI_%=:\n" \
  "v_cmp_eq_u32 vcc, 0, v4\n" \
  "s_cmp_eq_u64 vcc, 0\n" \
  "s_cselect_b32 s70, 1, 0\n" \
  "s_cbranch_scc0 .LCY_NS_%=\n" \
  "v_lshlrev_b32 v6, 16, s55\n" \
  "s_mov_b64 exec, 1\n" \
  "ds_add_u32 v5, v6\n" \
  "s_mov_b64 exec, -1\n" \
  "s_mov_b32 m0, s56\n" \
  "s_add_u32 s71, s56, 4096\n" \
  "global_load_lds_dwordx4 v2, s[48:49] offset:0 sc1\n" \
  "global_load_lds_dwordx4 v2, s[48:49] offset:1024 sc1\n" \
  "global_load_lds_dwordx4 v2, s[48:49] offset:2048 sc1\n" \
  "global_load_lds_dwordx4 v2, s[48:49] offset:3072 sc1\n" \
  "s_mov_b32 m0, s71\n" \
  "s_add_u32 s56, s56, s65\n" \
  "global_load_lds_dwordx4 v3, s[48:49] offset:0 sc1\n" \
  "global_load_lds_dwordx4 v3, s[48:49] offset:1024 sc1\n" \
  ".LCY_NS_%=:\n" \
  "ds_read_b128 v[20:23], v0 offset:3072\n" \
  "s_waitcnt lgkmcnt(3)\n" \
  "v_mfma_f32_16x16x32_bf16 v[32:35], v[64:67], v[8:11], 0\n" \
  "v_mfma_f32_16x16x32_bf16 v[36:39], v[96:99], v[8:11], 0\n" \
  "v_mfma_f32_16x16x32_bf16 v[40:43], v[128:131], v[8:11], 0\n" \
  "v_mfma_f32_16x16x32_bf16 v[44:47], v[160:163], v[8:11], 0\n" \
  "v_mfma_f32_16x16x32_bf16 v[48:51], v[192:195], v[8:11], 0\n" \
  "v_mfma_f32_16x16x32_bf16 v[52:55], v[224:227], v[8:11], 0\n" \
  "ds_read_b128 v[8:11], v0 offset:8192\n" \
  "s_waitcnt lgkmcnt(3)\n" \
  "v_mfma_f32_16x16x32_bf16 v[32:35], v[68:71], v[12:15], v[32:35]\n" \
  "v_mfma_f32_16x16x32_bf16 v[36:39], v[100:103], v[12:15], v[36:39]\n" \
  "v_mfma_f32_16x16x32_bf16 v[40:43], v[132:135], v[12:15], v[40:43]\n" \
  "v_mfma_f32_16x16x32_bf16 v[44:47], v[164:167], v[12:15], v[44:47]\n" \
  "v_mfma_f32_16x16x32_bf16 v[48:51], v[196:199], v[12:15], v[48:51]\n" \
  "v_mfma_f32_16x16x32_bf16 v[52:55], v[228:231], v[12:15], v[52:55]\n" \
  "ds_read_b128 v[12:15], v0 offset:9216\n" \
  "s_waitcnt lgkmcnt(3)\n" \
  "v_mfma_f32_16x16x32_bf16 v[32:35], v[72:75], v[16:19], v[32:35]\n" \
  "v_mfma_f32_16x16x32_bf16 v[36:39], v[104:107], v[16:19], v[36:39]\n" \
  "v_mfma_f32_16x16x32_bf16 v[40:43], v[136:139], v[16:19], v[40:43]\n" \
  "v_mfma_f32_16x16x32_bf16 v[44:47], v[168:171], v[16:19], v[44:47]\n" \
  "v_mfma_f32_16x16x32_bf16 v[48:51], v[200:203], v[16:19], v[48:51]\n" \
  "v_mfma_f32_16x16x32_bf16 v[52:55], v[232:235], v[16:19], v[52:55]\n" \
  "ds_read_b128 v[16:19], v0 offset:10240\n" \
  "s_waitcnt lgkmcnt(3)\n" \
  "v_mfma_f32_16x16x32_bf16 v[32:35], v[76:79], v[20:23], v[32:35]\n" \
  "v_mfma_f32_16x16x32_bf16 v[36:39], v[108:111], v[20:23], v[36:39]\n" \
  "v_mfma_f32_16x16x32_bf16 v[40:43], v[140:143], v[20:23], v[40:43]\n" \
  "v_mfma_f32_16x16x32_bf16 v[44:47], v[172:175], v[20:23], v[44:47]\n" \
  "v_mfma_f32_16x16x32_bf16 v[48:51], v[204:207], v[20:23], v[48:51]\n" \
  "v_mfma_f32_16x16x32_bf16 v[52:55], v[236:239], v[20:23], v[52:55]\n" \
  "ds_read_b128 v[20:23], v0 offset:11264\n" \
  "s_waitcnt lgkmcnt(3)\n" \
  "v_mfma_f32_16x16x32_bf16 v[32:35], v[80:83], v[8:11], v[32:35]\n" \
  "v_mfma_f32_16x16x32_bf16 v[36:39], v[112:115], v[8:11], v[36:39]\n" \
  "v_mfma_f32_16x16x32_bf16 v[40:43], v[144:147], v[8:11], v[40:43]\n" \
  "v_mfma_f32_16x16x32_bf16 v[44:47], v[176:179], v[8:11], v[44:47]\n" \
  "v_mfma_f32_16x16x32_bf16 v[48:51], v[208:211], v[8:11], v[48:51]\n" \
  "v_mfma_f32_16x16x32_bf16 v[52:55], v[240:243], v[8:11], v[52:55]\n" \
  "ds_read_b128 v[8:11], v0 offset:4096\n" \
  "s_waitcnt lgkmcnt(3)\n" \
  "v_mfma_f32_16x16x32_bf16 v[32:35], v[84:87], v[12:15], v[32:35]\n" \
  "v_mfma_f32_16x16x32_bf16 v[36:39], v[116:119], v[12:15], v[36:39]\n" \
  "v_mfma_f32_16x16x32_bf16 v[40:43], v[148:151], v[12:15], v[40:43]\n" \
  "v_mfma_f32_16x16x32_bf16 v[44:47], v[180:183], v[12:15], v[44:47]\n" \
  "v_mfma_f32_16x16x32_bf16 v[48:51], v[212:215], v[12:15], v[48:51]\n" \
  "v_mfma_f32_16x16x32_bf16 v[52:55], v[244:247], v[12:15], v[52:55]\n" \
  "ds_read_b128 v[12:15], v0 offset:5120\n" \
  "s_waitcnt lgkmcnt(3)\n" \
  "v_mfma_f32_16x16x32_bf16 v[32:35], v[88:91], v[16:19], v[32:35]\n" \
  "v_mfma_f32_16x16x32_bf16 v[36:39], v[120:123], v[16:19], v[36:39]\n" \
  "v_mfma_f32_16x16x32_bf16 v[40:43], v[152:155], v[16:19], v[40:43]\n" \
  "v_mfma_f32_16x16x32_bf16 v[44:47], v[184:187], v[16:19], v[44:47]\n" \
  "v_mfma_f32_16x16x32_bf16 v[48:51], v[216:219], v[16:19], v[48:51]\n" \
  "v_mfma_f32_16x16x32_bf16 v[52:55], v[248:251], v[16:19], v[52:55]\n" \
  "ds_read_b128 v[16:19], v0 offset:6144\n" \
  "s_waitcnt lgkmcnt(3)\n" \
  "v_mfma_f32_16x16x32_bf16 v[32:35], v[92:95], v[20:23], v[32:35]\n" \
  "v_mfma_f32_16x16x32_bf16 v[36:39], v[124:127], v[20:23], v[36:39]\n" \
  "v_mfma_f32_16x16x32_bf16 v[40:43], v[156:159], v[20:23], v[40:43]\n" \
  "v_mfma_f32_16x16x32_bf16 v[44:47], v[188:191], v[20:23], v[44:47]\n" \
  "v_mfma_f32_16x16x32_bf16 v[48:51], v[220:223], v[20:23], v[48:51]\n" \
  "v_mfma_f32_16x16x32_bf16 v[52:55], v[252:255], v[20:23], v[52:55]\n" \
  "ds_read_b128 v[20:23], v0 offset:7168\n" \
  "s_waitcnt lgkmcnt(3)\n" \
  "s_nop 1\n" \
  "v_cvt_pk_bf16_f32 v24, v32, v33\n" \
  "v_cvt_pk_bf16_f32 v25, v34, v35\n" \
  "v_cvt_pk_bf16_f32 v26, v36, v37\n" \
  "v_cvt_pk_bf16_f32 v27, v38, v39\n" \
  "s_nop 0\n" \
  "v_mfma_f32_16x16x32_bf16 v[32:35], v[64:67], v[8:11], 0\n" \
  "v_mfma_f32_16x16x32_bf16 v[36:39], v[96:99], v[8:11], 0\n" \
  "buffer_store_dwordx4 v[24:27], v1, s[40:43], s54 offen offset:0 sc1\n" \
  "v_cvt_pk_bf16_f32 v28, v40, v41\n" \
  "v_cvt_pk_bf16_f32 v29, v42, v43\n" \
  "v_cvt_pk_bf16_f32 v30, v44, v45\n" \
  "v_cvt_pk_bf16_f32 v31, v46, v47\n" \
  "s_nop 0\n" \
  "v_mfma_f32_16x16x32_bf16 v[40:43], v[128:131], v[8:11], 0\n" \
  "v_mfma_f32_16x16x32_bf16 v[44:47], v[160:163], v[8:11], 0\n" \
  "buffer_store_dwordx4 v[28:31], v1, s[40:43], s54 offen offset:128 sc1\n" \
  "v_cvt_pk_bf16_f32 v56, v48, v49\n" \
  "v_cvt_pk_bf16_f32 v57, v50, v51\n" \
  "v_cvt_pk_bf16_f32 v58, v52, v53\n" \
  "v_cvt_pk_bf16_f32 v59, v54, v55\n" \
  "s_nop 0\n" \
  "v_mfma_f32_16x16x32_bf16 v[48:51], v[192:195], v[8:11], 0\n" \
  "v_mfma_f32_16x16x32_bf16 v[52:55], v[224:227], v[8:11], 0\n" \
  "buffer_store_dwordx4 v[56:59], v1, s[40:43], s54 offen offset:256 sc1\n" \
  "ds_read_b128 v[8:11], v0 offset:12288\n" \
  "s_waitcnt lgkmcnt(3)\n" \
  "v_mfma_f32_16x16x32_bf16 v[32:35], v[68:71], v[12:15], v[32:35]\n" \
  "v_mfma_f32_16x16x32_bf16 v[36:39], v[100:103], v[12:15], v[36:39]\n" \
  "v_mfma_f32_16x16x32_bf16 v[40:43], v[132:135], v[12:15], v[40:43]\n" \
  "v_mfma_f32_16x16x32_bf16 v[44:47], v[164:167], v[12:15], v[44:47]\n" \
  "v_mfma_f32_16x16x32_bf16 v[48:51], v[196:199], v[12:15], v[48:51]\n" \
  "v_mfma_f32_16x16x32_bf16 v[52:55], v[228:231], v[12:15], v[52:55]\n" \
  "ds_read_b128 v[12:15], v0 offset:13312\n" \
  "s_waitcnt lgkmcnt(3)\n" \
  "v_mfma_f32_16x16x32_bf16 v[32:35], v[72:75], v[16:19], v[32:35]\n" \
  "v_mfma_f32_16x16x32_bf16 v[36:39], v[104:107], v[16:19], v[36:39]\n" \
  "v_mfma_f32_16x16x32_bf16 v[40:43], v[136:139], v[16:19], v[40:43]\n" \
  "v_mfma_f32_16x16x32_bf16 v[44:47], v[168:171], v[16:19], v[44:47]\n" \
  "v_mfma_f32_16x16x32_bf16 v[48:51], v[200:203], v[16:19], v[48:51]\n" \
  "v_mfma_f32_16x16x32_bf16 v[52:55], v[232:235], v[16:19], v[52:55]\n" \
  "ds_read_b128 v[16:19], v0 offset:14336\n" \
  "s_waitcnt lgkmcnt(3)\n" \
  "v_mfma_f32_16x16x32_bf16 v[32:35], v[76:79], v[20:23], v[32:35]\n" \
  "v_mfma_f32_16x16x32_bf16 v[36:39], v[108:111], v[20:23], v[36:39]\n" \
  "v_mfma_f32_16x16x32_bf16 v[40:43], v[140:143], v[20:23], v[40:43]\n" \
  "v_mfma_f32_16x16x32_bf16 v[44:47], v[172:175], v[20:23], v[44:47]\n" \
  "v_mfma_f32_16x16x32_bf16 v[48:51], v[204:207], v[20:23], v[48:51]\n" \
  "v_mfma_f32_16x16x32_bf16 v[52:55], v[236:239], v[20:23], v[52:55]\n" \
  "ds_read_b128 v[20:23], v0 offset:15360\n" \
  "s_waitcnt lgkmcnt(3)\n" \
  "v_mfma_f32_16x16x32_bf16 v[32:35], v[80:83], v[8:11], v[32:35]\n" \
  "v_mfma_f32_16x16x32_bf16 v[36:39], v[112:115], v[8:11], v[36:39]\n" \
  "v_mfma_f32_16x16x32_bf16 v[40:43], v[144:147], v[8:11], v[40:43]\n" \
  "v_mfma_f32_16x16x32_bf16 v[44:47], v[176:179], v[8:11], v[44:47]\n" \
  "v_mfma_f32_16x16x32_bf16 v[48:51], v[208:211], v[8:11], v[48:51]\n" \
  "v_mfma_f32_16x16x32_bf16 v[52:55], v[240:243], v[8:11], v[52:55]\n" \
  "s_waitcnt lgkmcnt(2)\n" \
  "v_mfma_f32_16x16x32_bf16 v[32:35], v[84:87], v[12:15], v[32:35]\n" \
  "v_mfma_f32_16x16x32_bf16 v[36:39], v[116:119], v[12:15], v[36:39]\n" \
  "v_mfma_f32_16x16x32_bf16 v[40:43], v[148:151], v[12:15], v[40:43]\n" \
  "v_mfma_f32_16x16x32_bf16 v[44:47], v[180:183], v[12:15], v[44:47]\n" \
  "v_mfma_f32_16x16x32_bf16 v[48:51], v[212:215], v[12:15], v[48:51]\n" \
  "v_mfma_f32_16x16x32_bf16 v[52:55], v[244:247], v[12:15], v[52:55]\n" \
  "s_waitcnt lgkmcnt(1)\n" \
  "v_mfma_f32_16x16x32_bf16 v[32:35], v[88:91], v[16:19], v[32:35]\n" \
  "v_mfma_f32_16x16x32_bf16 v[36:39], v[120:123], v[16:19], v[36:39]\n" \
  "v_mfma_f32_16x16x32_bf16 v[40:43], v[152:155], v[16:19], v[40:43]\n" \
  "v_mfma_f32_16x16x32_bf16 v[44:47], v[184:187], v[16:19], v[44:47]\n" \
  "v_mfma_f32_16x16x32_bf16 v[48:51], v[216:219], v[16:19], v[48:51]\n" \
  "v_mfma_f32_16x16x32_bf16 v[52:55], v[248:251], v[16:19], v[52:55]\n" \
  "s_waitcnt lgkmcnt(0)\n" \
  "v_mfma_f32_16x16x32_bf16 v[32:35], v[92:95], v[20:23], v[32:35]\n" \
  "v_mfma_f32_16x16x32_bf16 v[36:39], v[124:127], v[20:23], v[36:39]\n" \
  "v_mfma_f32_16x16x32_bf16 v[40:43], v[156:159], v[20:23], v[40:43]\n" \
  "v_mfma_f32_16x16x32_bf16 v[44:47], v[188:191], v[20:23], v[44:47]\n" \
  "v_mfma_f32_16x16x32_bf16 v[48:51], v[220:223], v[20:23], v[48:51]\n" \
  "v_mfma_f32_16x16x32_bf16 v[52:55], v[252:255], v[20:23], v[52:55]\n" \
  "v_add_u32 v0, s66, v0\n" \
  "s_sub_u32 s66, 0, s66\n" \
  "s_add_u32 s54, s54, 0x80000\n" \
  "s_mov_b32 s62, 0x40000\n" \
  "v_cvt_pk_bf16_f32 v24, v32, v33\n" \
  "v_cvt_pk_bf16_f32 v25, v34, v35\n" \
  "v_cvt_pk_bf16_f32 v26, v36, v37\n" \
  "v_cvt_pk_bf16_f32 v27, v38, v39\n" \
  "buffer_store_dwordx4 v[24:27], v1, s[40:43], s64 offen offset:0 sc1\n" \
  "v_cvt_pk_bf16_f32 v28, v40, v41\n" \
  "v_cvt_pk_bf16_f32 v29, v42, v43\n" \
  "v_cvt_pk_bf16_f32 v30, v44, v45\n" \
  "v_cvt_pk_bf16_f32 v31, v46, v47\n" \
  "buffer_store_dwordx4 v[28:31], v1, s[40:43], s64 offen offset:128 sc1\n" \
  "v_cvt_pk_bf16_f32 v56, v48, v49\n" \
  "v_cvt_pk_bf16_f32 v57, v50, v51\n" \
  "v_cvt_pk_bf16_f32 v58, v52, v53\n" \
  "v_cvt_pk_bf16_f32 v59, v54, v55\n" \
  "buffer_store_dwordx4 v[56:59], v1, s[40:43], s64 offen offset:256 sc1\n" \
  "s_cmp_eq_u32 s70, 0\n" \
  "s_cbranch_scc1 .LCY_ND_%=\n" \
  "global_load_lds_dwordx4 v3, s[48:49] offset:2048 sc1\n" \
  "global_load_lds_dwordx4 v3, s[48:49] offset:3072 sc1\n" \
  "s_add_u32 s48, s48, 0x80000\n" \
  "s_addc_u32 s49, s49, 0\n" \
  "s_mov_b32 m0, s57\n" \
  "s_add_u32 s57, s57, s65\n" \
  "global_load_lds_dwordx4 v2, s[50:51] offset:0 sc1\n" \
  "global_load_lds_dwordx4 v2, s[50:51] offset:1024 sc1\n" \
  "s_sub_u32 s65, 0, s65\n" \
  "s_add_u32 s67, s67, 4096\n" \
  "global_load_lds_dwordx4 v2, s[50:51] offset:2048 sc1\n" \
  "s_sub_u32 s68, s68, 1\n" \
  "s_mov_b32 s69, 1\n" \
  "global_load_lds_dwordx4 v2, s[50:51] offset:3072 sc1\n" \
  "s_add_u32 s50, s50, 0x40000\n" \
  "s_addc_u32 s51, s51, 0\n" \
  "v_mov_b32 v4, 0\n" \
  "buffer_load_dword v4, v4, s[44:47], s67 offen sc1\n" \
  "s_waitcnt vmcnt(7)\n" \
  "s_branch .LCY_WC_%=\n" \
  ".LCY_ND_%=:\n" \
  "v_mov_b32 v4, 0\n" \
  "buffer_load_dword v4, v4, s[44:47], s67 offen sc1\n" \
  "s_waitcnt vmcnt(0)\n" \
  ".LCY_WC_%=:\n" \
  "v_mov_b32 v6, s59\n" \
  "v_mov_b32 v7, 1\n" \
  "s_mov_b64 exec, 1\n" \
  "ds_add_rtn_u32 v6, v6, v7\n" \
  "s_mov_b64 exec, -1\n" \
  "s_xor_b32 s59, s59, 4\n" \
  "s_mov_b32 s61, 0\n" \
  "s_waitcnt lgkmcnt(0)\n" \
  "v_readfirstlane_b32 s63, v6\n" \
  "v_lshlrev_b32 v6, 5, v2\n" \
  "s_and_b32 s63, s63, 7\n" \
  "s_cmp_eq_u32 s63, 7\n" \
  "s_cbranch_scc0 .LCY_W1_%=\n" \
  "s_mov_b64 exec, 15\n" \
  "global_store_dword v6, v7, s[52:53] sc1\n" \
  "s_mov_b64 exec, -1\n" \
  "s_mov_b32 s61, 1\n" \
  ".LCY_W1_%=:\n" \
  "s_add_u32 s52, s52, 2048\n" \
  "s_addc_u32 s53, s53, 0\n" \
  "s_sub_u32 s60, s60, 1\n" \
  "s_cmp_eq_u32 s60, 0\n" \
  "s_cbranch_scc1 .LCY_END_%=\n" \
  ".LCY_NEED_%=:\n" \
  "s_cmp_eq_u32 s68, 0\n" \
  "s_cbranch_scc1 .LCY_BAR_%=\n" \
  "v_cmp_eq_u32 vcc, 0, v4\n" \
  "s_cmp_eq_u64 vcc, 0\n" \
  "s_cbranch_scc1 .LCY_GET_%=\n" \
  "s_cmp_eq_u32 s68, 1\n" \
  "s_cbranch_scc1 .LCY_BAR_%=\n" \
  "s_cmp_ge_u32 s67, 0x40000\n" \
  "s_cbranch_scc1 .LCY_BAR_%=\n" \
  "s_sleep 12\n" \
  ".LCY_ASK_%=:\n" \
  "v_mov_b32 v4, 0\n" \
  "buffer_load_dword v4, v4, s[44:47], s67 offen sc1\n" \
  "s_sub_u32 s62, s62, 1\n" \
  "s_cbranch_scc1 .LCY_END_%=\n" \
  "s_waitcnt vmcnt(0)\n" \
  "s_branch .LCY_NEED_%=\n" \
  ".LCY_GET_%=:\n" \
  "v_lshlrev_b32 v6, 16, s55\n" \
  "s_mov_b64 exec, 1\n" \
  "ds_add_u32 v5, v6\n" \
  "s_mov_b64 exec, -1\n" \
  "s_mov_b32 m0, s56\n" \
  "s_add_u32 s71, s56, 4096\n" \
  "global_load_lds_dwordx4 v2, s[48:49] offset:0 sc1\n" \
  "global_load_lds_dwordx4 v2, s[48:49] offset:1024 sc1\n" \
  "global_load_lds_dwordx4 v2, s[48:49] offset:2048 sc1\n" \
  "global_load_lds_dwordx4 v2, s[48:49] offset:3072 sc1\n" \
  "s_mov_b32 m0, s71\n" \
  "s_add_u32 s56, s56, s65\n" \
  "global_load_lds_dwordx4 v3, s[48:49] offset:0 sc1\n" \
  "global_load_lds_dwordx4 v3, s[48:49] offset:1024 sc1\n" \
  "global_load_lds_dwordx4 v3, s[48:49] offset:2048 sc1\n" \
  "global_load_lds_dwordx4 v3, s[48:49] offset:3072 sc1\n" \
  "s_add_u32 s48, s48, 0x80000\n" \
  "s_addc_u32 s49, s49, 0\n" \
  "s_mov_b32 m0, s57\n" \
  "s_add_u32 s57, s57, s65\n" \
  "global_load_lds_dwordx4 v2, s[50:51] offset:0 sc1\n" \
  "global_load_lds_dwordx4 v2, s[50:51] offset:1024 sc1\n" \
  "s_sub_u32 s65, 0, s65\n" \
  "s_add_u32 s67, s67, 4096\n" \
  "global_load_lds_dwordx4 v2, s[50:51] offset:2048 sc1\n" \
  "s_sub_u32 s68, s68, 1\n" \
  "s_mov_b32 s69, 1\n" \
  "global_load_lds_dwordx4 v2, s[50:51] offset:3072 sc1\n" \
  "s_add_u32 s50, s50, 0x40000\n" \
  "s_addc_u32 s51, s51, 0\n" \
  "v_mov_b32 v4, 0\n" \
  "buffer_load_dword v4, v4, s[44:47], s67 offen sc1\n" \
  "s_waitcnt vmcnt(0)\n" \
  "v_mov_b32 v6, s55\n" \
  "s_mov_b64 exec, 1\n" \
  "ds_add_u32 v5, v6\n" \
  "s_mov_b64 exec, -1\n" \
  "s_mov_b32 s69, 0\n" \
  "s_branch .LCY_NEED_%=\n" \
  ".LCY_END_%=:\n" \
  "s_endpgm\n"
template <bool AT>
__device__ __forceinline__ void role_aw_t(const MegaArgs& A, char* smem, const int L, const int a, const u64 tag) {
  const int w = __builtin_amdgcn_readfirstlane(threadIdx.x >> 6);
  const int l = threadIdx.x & 63, c = l & 15, g = l >> 4;
  const int qt = w & 1, hh = (w >> 1) & 1, h = 2 * a + hh;
  char* ws = A.ws;
  unsigned* FL = (unsigned*)(ws + OFF_FL);
  const unsigned sb = lds_base(smem);
  if (threadIdx.x < 4) reinterpret_cast<unsigned*>(smem + AW_CNT)[threadIdx.x] = 0u;
  constexpr int NP = AT ? 1 : 3;
  const int cp0 = AT ? w : 3 * w - 8;
  v8u wo[2 * NP][4];
  {
    const int q = (l >> 2) & 3, p = l & 3, f = q | ((g & 1) << 2);
    const unsigned ra = sb + (4 * g + q) * 256 + (p & 1) * 16;
#pragma unroll
    for (int r = 0; r < 2; ++r) {
#pragma unroll
      for (int k = 0; k < 2; ++k) {
        const char* base_ = (const char*)A.Wo + ((size_t)((2 * a + r) * DH + 64 * (w & 1) + 32 * k) * DM + 128 * (w >> 1)) * 2;
#pragma unroll
        for (int i = 0; i < 8; ++i) {
          const int fd = g | ((i & 1) << 2);
          glds16(base_, (unsigned)((4 * i + g) * (DM * 2) + ((c ^ (fd << 1)) << 4)), sb + w * 16384 + k * 8192 + i * 1024);
        }
      }
      SST(4 + 3 * r);
      if (r == 1) ready_wait(A, tag, ready_look(A));
      WAIT_VM(0);
      RAW_BAR();
      SST(5 + 3 * r);
#pragma unroll
      for (int k2 = 0; k2 < 2; ++k2) {
#pragma unroll
        for (int ct = 0; ct < 2 * NP; ++ct) {
          const int cp = cp0 + (ct >> 1);
          const unsigned a_ = ra + (cp >> 2) * 32768 + k2 * 16384 + ((2 * (cp & 3) + (p >> 1)) ^ f) * 32 + (ct & 1) * 8;
          const v2u r0 = trd(a_), r1 = trd(a_ + 4096), r2 = trd(a_ + 8192), r3 = trd(a_ + 12288);
          wo[ct][2 * r + k2] = v8u{r0.x, r0.y, r1.x, r1.y, r2.x, r2.y, r3.x, r3.y};
          asm volatile("" : "+v"(wo[ct][2 * r + k2]));
        }
      }
      RAW_BAR();
      SST(6 + 3 * r);
    }
  }
  WSTAMP2();
  const v4u rsW = mk_srd(ws + OFF_PW, SZ_PW);
  const v4u rsF = mk_srd(FL + FL_FQ + (L * 64 * 32 + h) * 32, (64 * 32 - h) * 128);
  const char* p0 = AT ? ws + OFF_MEMK + L * SZ_MEM + h * 2048 : ws + OFF_QK + L * SZ_QK + ((size_t)h * 16 + 8 * qt) * 1024;
  const char* p1 = AT ? (const char*)(FL + FL_FK + L * 32 + (h >> 3) * 64 + (h & 7) * 2) : ws + OFF_VF + L * SZ_O + ((size_t)h * 8 + 4 * qt) * 1024;
  const unsigned* pFW = FL + FL_FW + (L * 64 * 16 + (a >> 2)) * 32 + (a & 3);
  const unsigned tk = sb + AW_TK + hh * 24 * 1024, voff = (c >> 1) * 2048 + (c & 1) * 64 + g * 16;
  const char* dbg = ws + WS_TOTAL + A.vb * 64 * 8;
#define AW_SOPS                                                                                                                 \
  "{s[40:43]}"(rsW), "{s[44:47]}"(rsF), "{s[48:49]}"(p0), "{s[50:51]}"(p1), "{s[52:53]}"(pFW),                                   \
      "{s54}"((L * 64 * 16 + a) * 32768 + cp0 * 128), "{s55}"(1 << (8 * qt)), "{s56}"(tk + qt * 8192), "{s57}"(tk + 16384 + qt * 4096),                  \
      "{s58}"(sb + AW_CNT + 8 + 4 * hh), "{s59}"(sb + AW_CNT), "{s60}"(NSEG), "{s[36:37]}"(dbg)                                  \
      : "memory", "vcc", "scc", "s80"
  if constexpr (AT) {
    const v8u pv = {tk + l * 16, sb + AW_SO + l * 16, (unsigned)(g * 8 + (c & 7)) * 16, (unsigned)(OFF_MEMV - OFF_MEMK) + c * 32 + (g & 1) * 16,
                    voff, (unsigned)l * 16, tk + qt * 4096 + l * 16, sb + AW_SO + w * 4096 + l * 16};
    const unsigned neg = 0xff61b1e6u;
    v8u bt;
#pragma unroll
    for (int i = 0; i < 4; ++i) {
      const unsigned d = 4 * g + i > c ? neg : 0u;
      bt[i] = qt ? 0u : d;
      bt[4 + i] = qt ? d : neg;
    }
    const unsigned e = g >= 2 ? neg : 0u;
    const v4u bm = {e, e, e, e};
    asm volatile("s_mov_b64 exec, -1\ns_waitcnt vmcnt(0) lgkmcnt(0)\ns_nop 4\n" AW_ASM_X
                 ::"{v[192:199]}"(wo[0][0]), "{v[200:207]}"(wo[0][1]), "{v[208:215]}"(wo[0][2]), "{v[216:223]}"(wo[0][3]),
                 "{v[224:231]}"(wo[1][0]), "{v[232:239]}"(wo[1][1]), "{v[240:247]}"(wo[1][2]), "{v[248:255]}"(wo[1][3]),
                 "{v[0:7]}"(pv), "{v[8:15]}"(bt), "{v[16:19]}"(bm), AW_SOPS);
  } else {
    const v4u pv = {sb + AW_SO + l * 16, voff, (unsigned)l * 16, 0u};
    asm volatile("s_mov_b64 exec, -1\ns_waitcnt vmcnt(0) lgkmcnt(0)\ns_nop 4\n" AW_ASM_Y
                 ::"{v[64:71]}"(wo[0][0]), "{v[72:79]}"(wo[0][1]), "{v[80:87]}"(wo[0][2]), "{v[88:95]}"(wo[0][3]),
                 "{v[96:103]}"(wo[1][0]), "{v[104:111]}"(wo[1][1]), "{v[112:119]}"(wo[1][2]), "{v[120:127]}"(wo[1][3]),
                 "{v[128:135]}"(wo[2][0]), "{v[136:143]}"(wo[2][1]), "{v[144:151]}"(wo[2][2]), "{v[152:159]}"(wo[2][3]),
                 "{v[160:167]}"(wo[3][0]), "{v[168:175]}"(wo[3][1]), "{v[176:183]}"(wo[3][2]), "{v[184:191]}"(wo[3][3]),
                 "{v[192:199]}"(wo[4][0]), "{v[200:207]}"(wo[4][1]), "{v[208:215]}"(wo[4][2]), "{v[216:223]}"(wo[4][3]),
                 "{v[224:231]}"(wo[5][0]), "{v[232:239]}"(wo[5][1]), "{v[240:247]}"(wo[5][2]), "{v[248:255]}"(wo[5][3]),
                 "{v[0:3]}"(pv), AW_SOPS);
  }
#undef AW_SOPS
}
__device__ __forceinline__ void role_aw(const MegaArgs& A, char* smem, const int L, const int a, const u64 tag) {
  if (__builtin_amdgcn_readfirstlane(threadIdx.x >> 6) < 4) role_aw_t<true>(A, smem, L, a, tag);
  else role_aw_t<false>(A, smem, L, a, tag);
}

constexpr int RG = 2, RW = 2;
__device__ __forceinline__ void role_red(const MegaArgs& A, char* smem, const int L, const int idx, const u64 tag) {
  const int w = __builtin_amdgcn_readfirstlane(threadIdx.x >> 6);
  const int l = threadIdx.x & 63;
  const int gr = (w >> 1) & 1, r = idx * 2 + (w & 1);
  char* ws = A.ws;
  unsigned* FL = (unsigned*)(ws + OFF_FL);
  unsigned* TMO = FL + FL_TMO;
  const __amdgpu_buffer_rsrc_t rsW = MKRS(ws + OFF_PW, SZ_PW);
  const __amdgpu_buffer_rsrc_t rsX = MKRS(ws + (L ? OFF_X3 : OFF_XA), SZ_X);
  const __amdgpu_buffer_rsrc_t rsY = L ? MKRS(A.out, SZ_X) : MKRS(ws + OFF_X2, SZ_X);
  volatile unsigned* sw = reinterpret_cast<volatile unsigned*>(smem) + (w >> 1) * 32;
  if (threadIdx.x < 128) reinterpret_cast<volatile unsigned*>(smem)[threadIdx.x] = 0u;
  __syncthreads();
  if ((EXP_OFF & 2) && L == 0 && w < 4) return;
  if ((EXP_OFF & 64) && L == 1 && w < 4) return;
  if ((EXP_OFF & 8) && L == 1 && w >= 4) return;
  unsigned tl = now10();
  if (w >= 4) return;
  ready_wait(A, tag, ready_look(A));
  if (L == 0) {
    for (int s = gr; s < NSEG; s += RG) ST128((v4u{0u, 0u, 0u, 0u}), rsY, l * 16, (s * SEG + r) * DM * 2);
    DRAIN();
    if (l < 8) set_flag(FL + FL_FZ + (2 * l + (idx >> 3)) * 32 + (idx & 7) * 4 + w);
  }
  for (int s = gr; s < NSEG; s += RG) {
    grp_wait<SLP_R, 4, GATE_R>((w & 1) == 0, sw, s + 1, FL + FL_FW + ((L * 64 + s) * 4 + (blockIdx.x & 3)) * 4 * 32, 16, TMO, tl);
    if (s >= NSEG - 6) tl = now10();
    TS(0, s, gr * RW * 64);
    TS(4, s, gr * RW * 64 + (RW - 1) * 64);
    v4u p[16];
    const v4u xr = LD128(rsX, l * 512, s * 32768 + r * 16);
#pragma unroll
    for (int k = 0; k < 16; ++k)
      p[k] = LD128(rsW, (l >> 2) * 128 + (l & 3) * 16, ((L * 64 + s) * 16 + k) * 32768 + (r >> 1) * 2048 + (r & 1) * 64);
    v4u xo;
#pragma unroll
    for (int e = 0; e < 4; ++e) {
      f32x2_t t[4];
#pragma unroll
      for (int k = 0; k < 4; ++k)
        t[k] = (f32x2_t{lo16(p[4 * k][e]), hi16(p[4 * k][e])} + f32x2_t{lo16(p[4 * k + 1][e]), hi16(p[4 * k + 1][e])}) +
               (f32x2_t{lo16(p[4 * k + 2][e]), hi16(p[4 * k + 2][e])} + f32x2_t{lo16(p[4 * k + 3][e]), hi16(p[4 * k + 3][e])});
      const f32x2_t u = (t[0] + t[1]) + (t[2] + t[3]);
      const unsigned a0 = pk2(u.x, u.y);
      xo[e] = pk2(lo16(xr[e]) + lo16(a0), hi16(xr[e]) + hi16(a0));
      if (L == 0) xo[e] = xo[e] ? xo[e] : 0x8000u;
    }
    ST128(xo, rsY, l * 16, (s * SEG + r) * DM * 2);
    TS(1, s, gr * RW * 64);
    TS(5, s, gr * RW * 64 + (RW - 1) * 64);
    if (L == 0) {
      const unsigned st = row_stats(xo);
      if (l < 4) __hip_atomic_store((gu32*)(FL + FL_FR + ((s * 4 + l) * 4 + (r >> 3)) * 32 + (r & 7)), st, RLX_AGENT);
    }
    TS(6, s, gr * RW * 64 + (RW - 1) * 64);
    TS(3, s, gr * RW * 64);
  }
  if (L == 1 && idx == 0 && threadIdx.x == 0) __hip_atomic_store((gu64*)(ws + OFF_EP), tag, RLX_AGENT);
}

constexpr int CUN = 2144;
__device__ __forceinline__ v2u part_stats(const v4u o) {
  float v[8];
#pragma unroll
  for (int e = 0; e < 4; ++e) { v[2 * e] = lo16(o[e]); v[2 * e + 1] = hi16(o[e]); }
  const float s = ((v[0] + v[1]) + (v[2] + v[3])) + ((v[4] + v[5]) + (v[6] + v[7])), m = s * 0.125f;
  float q0 = 0.f, q1 = 0.f;
#pragma unroll
  for (int e = 0; e < 4; ++e) {
    const float a = v[e] - m, b = v[4 + e] - m;
    q0 += a * a;
    q1 += b * b;
  }
  return v2u{__float_as_uint(s), __float_as_uint(q0 + q1)};
}
constexpr int CRN = 176;
constexpr int CV_LDS = (8 * 2 * CKN + 8 * 2 * CUN + 64 + 2 * 32 * 8 + 16 * CRN + 64 + 256 * 8) * 2 + 8 * 1568;
__device__ __forceinline__ void role_conv(const MegaArgs& A, char* smem, const int cv) {
  u16* Kr = reinterpret_cast<u16*>(smem);
  u16* Uc = Kr + 8 * 2 * CKN;
  u16* Us = Uc + 8 * CUN + 63;
  u16* To = Uc + 8 * 2 * CUN + 64;
  u16* Sx = To + 32 * 8;
  u16* Rc = Sx + 32 * 8;
  u16* Rs = Rc + 8 * CRN + 63;
  u16* Ta = Rc + 16 * CRN + 64;
  const int tid = threadIdx.x;
  const int w = __builtin_amdgcn_readfirstlane(tid >> 6);
  const int l = tid & 63, c = l & 15, g = l >> 4;
  const int d = 8 * cv + w;
  char* ws = A.ws;
  unsigned* FL = (unsigned*)(ws + OFF_FL);
  unsigned* TMO = FL + FL_TMO;
  u16* kr = Kr + w * 2 * CKN;
  const u16* ka = kr + (c >> 3) * CKN + 2080 - 16 * (c & 7) + 8 * g;
#define KFRAG(D0) (*reinterpret_cast<const bf16x8*>(ka - (D0)))
  typedef __attribute__((address_space(3))) const unsigned* lpu;
#define BFRAG(a) BF8((v4u{((lpu)(unsigned long)(a))[0], ((lpu)(unsigned long)(a))[1], ((lpu)(unsigned long)(a))[2], ((lpu)(unsigned long)(a))[3]}))
  f32x4 acc[16], far[8];
  v4u tx[4];
  const __amdgpu_buffer_rsrc_t rz1 = MKRS(ws + OFF_XA, SZ_X), rps = MKRS(ws + OFF_H, SZ_X);
  u16* uc = Uc + w * CUN;
  const unsigned ub = lds_base(reinterpret_cast<char*>(((c & 1) ? Us : Uc) + w * CUN + c + 8 * g + 32));
  const int t0 = 16 * (4 * (g & 1) + (g >> 1)) + c;
  float dsk;
#define SKIP(T) pk2(bf2f(uc[32 + 128 * (T) + t0]) * dsk, bf2f(uc[64 + 128 * (T) + t0]) * dsk)
#define TILE_OUT(T, a, uu)                                                                     \
  {                                                                                            \
    const unsigned yy = pk2(swap_add32(a[0], a[1]), swap_add32(a[2], a[3]));                   \
    const unsigned v_ = pk2(lo16(yy) + lo16(uu), hi16(yy) + hi16(uu));                         \
    Ta[(128 * ((T) & 7) + t0) * 8 + w] = (u16)v_;                                              \
    Ta[(128 * ((T) & 7) + t0 + 32) * 8 + w] = (u16)(v_ >> 16);                                 \
  }
#define CHUNK_ST(i)                                                                            \
  {                                                                                            \
    const v4u a_ = *reinterpret_cast<const v4u*>(Ta + (64 * w + l + 512 * ((i) & 1)) * 8);     \
    v4u o;                                                                                     \
    _Pragma("unroll") for (int e = 0; e < 4; ++e) o[e] = pk2(lo16(tx[i][e]) + lo16(a_[e]), hi16(tx[i][e]) + hi16(a_[e])); \
    ST128(o, rz1, (l >> 5) * 32768 + (l & 31) * 16, (2 * (w + 8 * (i)) * 64 + cv) * 512);      \
    ST64(part_stats(o), rps, (l >> 5) * 16384 + (l & 31) * 8, (2 * (w + 8 * (i)) * 64 + cv) * 256); \
  }
#define CHUNK_FLAG(i) set_flag(FL + FL_FC + (((2 * w + 16 * ((i) + (l >> 3)) + ((l >> 2) & 1)) * 4 + (l & 3)) * 8 + (cv >> 3)) * 32 + (cv & 7))
  {
    unsigned ta[3], k0p;
    v4u kn;
    u64 tag;
    {
      KGen G;
      G.load(A, d, l);
      const u16 pk = A.Dskip[d];
      asm volatile("" ::: "memory");
      tx[0] = *reinterpret_cast<const v4u*>(A.x0 + (size_t)tid * DM + 8 * cv);
      ask_slow(A, 16 * (w - 4) + (cv & 15), ta);
      tag = call_tag(A);
      G.setup();
      dsk = bf2f(pk);
      G.st = smem + w * 16640;
      SST(0);
      G.columns(l);
      const Cx z = G.power(1.f);
      G.rows(G.st + l * 8, cmul(G.C, z), z, 16);
      kn = G.tile(l);
      const float k0 = 2.0f * wave_sum(G.C.r.x + G.C.r.y);
      k0p = l == 0 ? pk2(k0, k0 - lo16(pk2(k0, k0))) : 0u;
    }
    SST(1);
    const __amdgpu_buffer_rsrc_t rst = MKRS(ws + OFF_GT, L_SEQ * 4);
#define U_VAL(i)                                                                               \
  {                                                                                            \
    const float mb_ = lo16(st[i]), ri_ = 1.0f / hi16(st[i]);                                   \
    _Pragma("unroll") for (int e = 0; e < 4; ++e) {                                            \
      const unsigned t_ = pk2(lo16(tx[i][e]) - mb_, hi16(tx[i][e]) - mb_);                     \
      uv[e] = pk2(lo16(t_) * ri_, hi16(t_) * ri_);                                             \
    }                                                                                          \
  }
#define U_PUT(i)                                                                               \
  _Pragma("unroll") for (int e = 0; e < 4; ++e) {                                              \
    ((lph)(unsigned long)up0)[(2 * e) * CUN + 512 * (i)] = (u16)uv[e];                         \
    ((lph)(unsigned long)up0)[(2 * e + 1) * CUN + 512 * (i)] = (u16)(uv[e] >> 16);             \
    ((lph)(unsigned long)up1)[(2 * e) * CUN + 512 * (i)] = (u16)uv[e];                         \
    ((lph)(unsigned long)up1)[(2 * e + 1) * CUN + 512 * (i)] = (u16)(uv[e] >> 16);             \
  }
#define U_ROW(i) { U_VAL(i) U_PUT(i) }
    typedef __attribute__((address_space(3))) u16* lph;
    unsigned st[4], up0 = lds_base((char*)(Uc + 32 + tid)), up1 = lds_base((char*)(Us + 32 + tid));
    asm volatile("" : "+v"(up0), "+v"(up1), "+v"(kn), "+v"(k0p));
    v4u uv;
    const gu64* fs = (gu64*)(ws + OFF_FS) + ((blockIdx.x & 3) * 64 + 2 * w + 16 * g + ((l >> 3) & 1)) * 16 + (l & 7);
#define ST_GET()                                                                                                              \
  {                                                                                                                           \
    for (unsigned spins = 0; !__all(fv == tag) && spins < SPIN_CAP; ++spins) {                                                \
      __builtin_amdgcn_s_sleep(4);                                                                                            \
      fv = __hip_atomic_load(fs, RLX_AGENT);                                                                                  \
    }                                                                                                                         \
    asm volatile("" ::: "memory");                                                                                            \
    _Pragma("unroll") for (int i = 0; i < 4; ++i) st[i] = __builtin_amdgcn_raw_buffer_load_b32(rst, (tid + 512 * i) * 4, 0, AUX_SC1); \
    asm volatile("" ::: "memory");                                                                                            \
  }
    u64 fv = __hip_atomic_load(fs, RLX_AGENT);
    if (w < 4) {
      ST_GET();
      U_VAL(0);
      asm volatile("" : "+v"(uv));
    }
#pragma unroll
    for (int i = 1; i < 4; ++i) tx[i] = *reinterpret_cast<const v4u*>(A.x0 + (size_t)(tid + 512 * i) * DM + 8 * cv);
    RAW_BAR();
    SST(2);
    if (w < 4) { U_PUT(0) }
    *reinterpret_cast<v2u*>(kr + 2076 - 16 * c - 4 * g) = v2u{__builtin_amdgcn_perm(kn.z, kn.w, 0x05040100), __builtin_amdgcn_perm(kn.x, kn.y, 0x05040100)};
    *reinterpret_cast<v2u*>(kr + CKN + 2076 - 16 * c - 4 * g) = v2u{__builtin_amdgcn_perm(kn.z, kn.w, 0x07060302), __builtin_amdgcn_perm(kn.x, kn.y, 0x07060302)};
    if (l < 18) {
      reinterpret_cast<v4u*>(kr)[l < 16 ? 260 + l : 210 + l] = v4u{k0p & 0xffffu, 0u, 0u, 0u};
      reinterpret_cast<v4u*>(kr + CKN)[l < 16 ? 260 + l : 210 + l] = v4u{k0p >> 16, 0u, 0u, 0u};
    }
    (l < 32 ? uc : Us + w * CUN - 32)[l] = 0;
    uc[32 + L_SEQ + l] = Us[w * CUN + 32 + L_SEQ + l] = 0;
    uc[32 + 256 + l] = Us[w * CUN + 32 + 256 + l] = 0;
    RAW_BAR();
    {
      f32x4 a0 = {}, a1 = {}, a2 = {};
      bf16x8 bf[9], af[9];
#pragma unroll
      for (int b = 0; b < 9; ++b) {
        af[(b + 4) % 9] = KFRAG(160 - 32 * ((b + 4) % 9));
        bf[b] = BFRAG(ub + 64 * (b - 1));
      }
      const unsigned k0 = SKIP(0), k1 = SKIP(1);
      __builtin_amdgcn_sched_barrier(0);
#pragma unroll
      for (int b = 0; b < 5; ++b) a0 = MFMA16(af[b + 4], bf[b], a0);
#pragma unroll
      for (int b = 0; b < 4; ++b) {
        a2 = MFMA16(af[b + 4], bf[b + 4], a2);
        a1 = MFMA16(af[b], bf[b], a1);
      }
      a2 = MFMA16(af[8], bf[8], a2);
      TILE_OUT(0, a0, k0);
      a1 += a2;
      TILE_OUT(1, a1, k1);
    }
    const u64 rv = ready_look(A);
    RAW_BAR();
    SST(3);
    if (w < 4) {
      CHUNK_ST(0);
      asm volatile("" : "+v"(tx[1]));
      U_ROW(1);
      DRAIN();
      ready_wait(A, tag, rv);
      if (l < 8) CHUNK_FLAG(0);
    } else {
      ST_GET();
      ready_wait(A, tag, rv);
      asm volatile("" : "+v"(tx[0]), "+v"(tx[1]));
      U_ROW(0);
      U_ROW(1);
    }
    ASKED(ta);
    if (tid < (16 * CRN + 64) / 8) reinterpret_cast<v4u*>(Rc)[tid] = v4u{0u, 0u, 0u, 0u};
    SST(4);
    const gu64* fk = (gu64*)(ws + OFF_KG) + cv * 16 + w;
    u64 kg = __hip_atomic_load(fk, RLX_AGENT);
    U_ROW(2);
    for (unsigned spins = 0; !__all(kg == tag) && spins < SPIN_CAP; ++spins) {
      __builtin_amdgcn_s_sleep(8);
      kg = __hip_atomic_load(fk, RLX_AGENT);
    }
    asm volatile("" : "+v"(tx[3]), "+v"(st[3])::"memory");
    SST(5);
    const unsigned* kd = reinterpret_cast<const unsigned*>(Ta + 256 * 8) + w * 392;
    glds16c(ws + OFF_KF + d * 2048, l * 16, lds_base((char*)kd));
    if (l < 34) glds16c(ws + OFF_KF + d * 2048 + 1024, l * 16, lds_base((char*)kd) + 1024);
    U_ROW(3);
    DRAIN();
    SST(6);
#pragma unroll 1
    for (int un = l; un < 228; un += 64) {
      const int kc = un < 132 ? 2 : un < 196 ? 1 : 0;
      v4u oh, ol;
#pragma unroll
      for (int e = 0; e < 4; ++e) {
        unsigned kv[2];
#pragma unroll
        for (int k = 0; k < 2; ++k) {
          const int lag = 2080 - (8 * un + 2 * e + k);
          const int q = (lag + (1 << kc) - 1 + ((lag >> (kc + 1)) & 1)) >> (kc + 1);
          kv[k] = kd[128 * kc - 128 + q];
          if (lag >= L_SEQ) kv[k] = 0u;
        }
        oh[e] = __builtin_amdgcn_perm(kv[1], kv[0], 0x05040100);
        ol[e] = __builtin_amdgcn_perm(kv[1], kv[0], 0x07060302);
      }
      reinterpret_cast<v4u*>(kr)[un] = oh;
      reinterpret_cast<v4u*>(kr + CKN)[un] = ol;
    }
    RAW_BAR();
    SST(7);
    {
      unsigned uq[2] = {ub, ub + 1024};
      bf16x8 bf[8], a[4];
      const bf16x8 bm = BFRAG(ub - 64);
      asm volatile("" : "+v"(uq[0]), "+v"(uq[1]));
#pragma unroll
      for (int j = 1; j < 8; ++j) bf[j] = BFRAG(uq[j >> 2] + 256 * (j & 3));
#pragma unroll
      for (int T = 2; T < 16; ++T) (T < 8 ? acc[T] : far[T & 7]) = MFMA16(KFRAG(128 * T + 32), bm, (f32x4{}));
#pragma unroll
      for (int e = 0; e < 3; ++e) a[e] = KFRAG(128 * e);
      const u16* kq = ka - 128 * 7;
#pragma unroll 1
      for (int r = 0; r < 4; ++r) {
#pragma unroll
        for (int e = 0; e < 8; ++e) {
          a[(e + 3) & 3] = *reinterpret_cast<const bf16x8*>(kq + 128 * (7 - ((e + 3) & 7)) + (e >= 5 ? 32 : 0));
          const int j = (8 - e) & 7;
          bf[j] = BFRAG(uq[j >> 2] + 256 * (j & 3) + (e ? 64 : 0));
          __builtin_amdgcn_sched_barrier(0);
#pragma unroll
          for (int T = 7; T >= (e > 2 ? e : 2); --T) acc[T] = MFMA16(a[e & 3], bf[T - e], acc[T]);
          __builtin_amdgcn_sched_barrier(0);
        }
        kq += 32;
        uq[0] += 64;
        uq[1] += 64;
        asm volatile("" : "+v"(uq[0]), "+v"(uq[1]));
      }
    }
    SST(8);
    {
      unsigned k[8];
#pragma unroll
      for (int T = 2; T < 8; ++T) k[T] = SKIP(T);
#pragma unroll
      for (int T = 2; T < 8; ++T) TILE_OUT(T, acc[T], k[T]);
    }
    RAW_BAR();
    SST(10);
    if (w >= 4) CHUNK_ST(0);
    CHUNK_ST(1);
    if (w == 1) (void)wait_n<SLP_V, 32>(FL + FL_FZ + (blockIdx.x & 7) * 64, 64, TMO);
    DRAIN();
    if (l < 16) CHUNK_FLAG(0);
    SST(9);
#undef U_ROW
#undef U_VAL
#undef U_PUT
#undef ST_GET
  }
#define FAR_HALF(r, J0, J1)                                                                     \
  {                                                                                            \
    bf16x8 bf[16], af[16];                                                                     \
    unsigned u_ = ub + 64 * (r) + 256 * (J0), v_ = u_ + 1024;                                  \
    asm volatile("" : "+v"(u_), "+v"(v_));                                                     \
    _Pragma("unroll") for (int j = (J0); j < (J1); ++j) bf[j] = BFRAG((j - (J0) < 4 ? u_ : v_) + 256 * ((j - (J0)) & 3)); \
    _Pragma("unroll") for (int e = (9 - (J1) > 0 ? 9 - (J1) : 0); e < 16 - (J0); ++e) af[e] = KFRAG(128 * e - 32 * (r)); \
    __builtin_amdgcn_sched_barrier(0);                                                         \
    _Pragma("unroll") for (int j = (J1) - 1; j >= (J0); --j)                                   \
      _Pragma("unroll") for (int T = (j > 8 ? j : 8); T < 16; ++T) far[T - 8] = MFMA16(af[T - j], bf[j], far[T - 8]); \
  }
#define FAR_PIECE(p)                                                                            \
  {                                                                                            \
    int l = tid & 63;                                                                          \
    asm volatile("" : "+v"(l), "+v"(tx[2]), "+v"(tx[3]));                                      \
    if ((p) < 16) {                                                                            \
      const int r_ = (p) >> 2;                                                                 \
      if (((p) & 3) == 0) FAR_HALF(r_, 0, 3) else if (((p) & 3) == 1) FAR_HALF(r_, 3, 6)       \
      else if (((p) & 3) == 2) FAR_HALF(r_, 6, 9) else FAR_HALF(r_, 9, 16)                     \
    } else if ((p) == 16) {                                                                    \
      unsigned k_[8];                                                                          \
      _Pragma("unroll") for (int T = 0; T < 8; ++T) k_[T] = SKIP(8 + T);                       \
      _Pragma("unroll") for (int T = 0; T < 8; ++T) TILE_OUT(T, far[T], k_[T]);                \
    } else if ((p) == 17) {                                                                    \
      CHUNK_ST(2);                                                                             \
    } else if ((p) == 18) {                                                                    \
      CHUNK_ST(3);                                                                             \
    } else {                                                                                   \
      DRAIN();                                                                                 \
      if (l < 16) CHUNK_FLAG(2);                                                               \
    }                                                                                          \
  }
  static_assert(CVPRE <= 16, "the later pieces need the barriers of the segments");
  const unsigned rb = lds_base(reinterpret_cast<char*>(((c & 1) ? Rs : Rc) + w * CRN + c + 8 * g));
#pragma unroll
  for (int j = 0; j < 16; ++j) acc[j] = f32x4{};
  const __amdgpu_buffer_rsrc_t rx = MKRS(ws + OFF_X2, SZ_X), rz = MKRS(ws + OFF_X3, SZ_X);
  const __amdgpu_buffer_rsrc_t rf = MKRS(FL + FL_FR, 64 * 16 * 128);
  unsigned st = 0u;
  v2u xx = {0u, 0u};
#define X2_LOAD(ss, far)                                                                                                      \
  {                                                                                                                      \
    xx = LD64(rx, (l >> 1) * (DM * 2) + (l & 1) * 8, (far) ? (int)SZ_X : ((ss) * SEG * DM + 8 * cv) * 2);                 \
    st = __builtin_amdgcn_raw_buffer_load_b32(rf, (l >> 4) * 128 + ((l >> 1) & 7) * 4, ((ss) * 4 + (blockIdx.x & 3)) * 512, AUX_SC1); \
  }
  unsigned tl = now10();
  for (int s = -CVPRE; s < NSEG; ++s) {
   if (s >= 0) {
    const int q = s & 3;
    if (w == 1) {
      for (unsigned spins = 0; !__all(umin3(xx.x, xx.y, st) != 0u); ++spins) {
        if (spin_timeout(spins, TMO)) break;
        if (GATE_V && spins == 0 && !__any(st != 0u)) gate(tl);
        if (spins && !__all(st != 0u)) __builtin_amdgcn_s_sleep(SLP_V);
        X2_LOAD(s, __builtin_popcountll(__ballot(st != 0u)) < CVTHR);
      }
      tl = now10() + GATE_V;
      TS(0, s, 64);
      const float mb = lo16(st), ri = 1.0f / hi16(st);
      const unsigned t0 = pk2(lo16(xx.x) - mb, hi16(xx.x) - mb), t1 = pk2(lo16(xx.y) - mb, hi16(xx.y) - mb);
      const unsigned u0 = pk2(lo16(t0) * ri, hi16(t0) * ri), u1 = pk2(lo16(t1) * ri, hi16(t1) * ri);
      u16* rp = Rc + (l & 1) * 4 * CRN + 32 * q + (l >> 1);
#define R_PUT(o) { rp[o] = (u16)u0; rp[(o) + CRN] = (u16)(u0 >> 16); rp[(o) + 2 * CRN] = (u16)u1; rp[(o) + 3 * CRN] = (u16)(u1 >> 16); }
      R_PUT(0) R_PUT(8 * CRN + 63)
      if (q == 0) { R_PUT(128) R_PUT(8 * CRN + 63 + 128) }
#undef R_PUT
      *reinterpret_cast<v2u*>(Sx + l * 4) = xx;
    }
    const u16* kq = ka + 32 * q;
    const bf16x8 a0 = *reinterpret_cast<const bf16x8*>(kq - 32), a1 = *reinterpret_cast<const bf16x8*>(kq);
    __syncthreads();
    TS(1, s, 0);
    const bf16x8 bp = BFRAG(rb + 64 * ((q + 3) & 3)), bc = BFRAG(rb + 64 * q);
    const float u0 = bf2f(Rc[w * CRN + 32 * q + c]), u1 = bf2f(Rc[w * CRN + 32 * q + 16 + c]);
    const float x0 = bf2f(Sx[c * 8 + w]), x1 = bf2f(Sx[(16 + c) * 8 + w]);
    acc[0] = MFMA16(a0, bp, (q == 0 ? acc[1] : acc[0]));
    const f32x4 tmp = MFMA16(a1, bc, acc[0]);
    const unsigned uu = pk2(u0 * dsk, u1 * dsk);
    const unsigned yy = pk2(xsum32((q & 1) ? tmp[2] : tmp[0]), xsum32((q & 1) ? tmp[3] : tmp[1]));
    const unsigned aa = pk2(lo16(yy) + lo16(uu), hi16(yy) + hi16(uu)), oo = pk2(x0 + lo16(aa), x1 + hi16(aa));
    if (g == (q >> 1)) {
      To[c * 8 + w] = (u16)oo;
      To[(16 + c) * 8 + w] = (u16)(oo >> 16);
    }
    __syncthreads();
    if (w == 0 && l < 32) {
      const v4u o = *reinterpret_cast<const v4u*>(To + l * 8);
      ST128(o, rz, l * 16, (s * 64 + cv) * 512);
      ST64(part_stats(o), rps, l * 8, ((64 + s) * 64 + cv) * 256);
    }
    if (w == 1) X2_LOAD(s + 1, CVTHR > 0);
    const int left = 15 - (s >> 2);
#define PUSH(J0, SH)                                                                                              \
  if (left >= (J0)) {                                                                                             \
    _Pragma("unroll") for (int j = (J0); j < (J0) + 4 && j < 16; ++j)                                             \
      acc[j] = MFMA16(*reinterpret_cast<const bf16x8*>(kq - 32 - 128 * j), bp, (j + (SH) < 16 ? acc[(j + (SH)) & 15] : f32x4{})); \
  }
    TS(2, s, 0);
    if (w == 0) {
      DRAIN();
      if (l < 4) set_flag(FL + FL_FC + (((64 + s) * 4 + l) * 8 + (cv >> 3)) * 32 + (cv & 7));
    }
    TS(3, s, 0);
    if (q == 0) { PUSH(1, 1) PUSH(5, 1) PUSH(9, 1) PUSH(13, 1) }
    else { PUSH(1, 0) PUSH(5, 0) PUSH(9, 0) PUSH(13, 0) }
#undef PUSH
    MSTAMP();
   }
    if (s < 20 - CVPRE) FAR_PIECE(s + CVPRE);
  }
#undef KFRAG
#undef BFRAG
#undef X2_LOAD
#undef FAR_PIECE
#undef FAR_HALF
#undef TILE_OUT
#undef SKIP
#undef CHUNK_ST
#undef CHUNK_FLAG
}

__device__ __forceinline__ v4u ln_row_packed(const v4u xr) {
  float v[8];
#pragma unroll
  for (int e = 0; e < 4; ++e) { v[2 * e] = lo16(xr[e]); v[2 * e + 1] = hi16(xr[e]); }
  float s = ((v[0] + v[1]) + (v[2] + v[3])) + ((v[4] + v[5]) + (v[6] + v[7]));
  s = wave_sum(s);
  const float mean = s * (1.0f / DM);
  float q = 0.f;
#pragma unroll
  for (int j = 0; j < 8; ++j) { float t = v[j] - mean; q += t * t; }
  q = wave_sum(q);
  const float var = q * (1.0f / DM);
  const float mb = bfr(mean);
  const float sd = bfr(sqrtf(bfr(bfr(var) + 1e-5f)));
  const float ri = 1.0f / sd;
  v4u o;
#pragma unroll
  for (int e = 0; e < 4; ++e) {
    const unsigned t = pk2(v[2 * e] - mb, v[2 * e + 1] - mb);
    const float x0 = lo16(t), x1 = hi16(t);
    float q0 = x0 * ri, q1 = x1 * ri;
    q0 = __builtin_fmaf(__builtin_fmaf(-q0, sd, x0), ri, q0);
    q1 = __builtin_fmaf(__builtin_fmaf(-q1, sd, x1), ri, q1);
    o[e] = pk2(q0, q1);
  }
  return o;
}

__device__ __forceinline__ float bfr1(float x) { return lo16(pk2(x, x)); }
__device__ __forceinline__ void ln4_packed(v4u (&x)[4]) {
  f32x2_t v[16];
#pragma unroll
  for (int e = 0; e < 16; ++e) v[e] = f32x2_t{lo16(x[e >> 2][e & 3]), hi16(x[e >> 2][e & 3])};
  const f32x2_t t = (((v[0] + v[1]) + (v[2] + v[3])) + ((v[4] + v[5]) + (v[6] + v[7]))) +
                    (((v[8] + v[9]) + (v[10] + v[11])) + ((v[12] + v[13]) + (v[14] + v[15])));
  const float mean = red_mod4(t.x + t.y) * (1.0f / DM);
  const float mb = bfr1(mean);
  f32x2_t q0 = {0.f, 0.f}, q1 = {0.f, 0.f};
#pragma unroll
  for (int e = 0; e < 16; e += 2) {
    v[e] -= mb;
    v[e + 1] -= mb;
    q0 += v[e] * v[e];
    q1 += v[e + 1] * v[e + 1];
  }
  q0 += q1;
  const float dm = mean - mb;
  const float var = fmaxf(red_mod4(q0.x + q0.y) * (1.0f / DM) - dm * dm, 0.f);
  const float sd = bfr1(sqrtf(bfr1(bfr1(var) + 1e-5f)));
  const float ri = 1.0f / sd;
#pragma unroll
  for (int e = 0; e < 16; ++e) {
    const unsigned t2 = pk2(v[e].x, v[e].y);
    const f32x2_t q = f32x2_t{lo16(t2), hi16(t2)} * ri;
    x[e >> 2][e & 3] = pk2(q.x, q.y);
  }
}

#define Q_ASM \
  "s_mov_b32 s79, 0\n" \
  "s_mov_b32 s81, 0\n" \
  "s_mov_b32 s83, 0x10000\n" \
  "s_mov_b32 s76, 0\n" \
  "s_mov_b32 s77, -1\n" \
  "s_cmp_lg_u32 s78, 0\n" \
  "s_cbranch_scc1 .LCQ_GOT_%=\n" \
  "s_branch .LCQ_POLL_%=\n" \
  ".LCQ_TOP_%=:\n" \
  "s_waitcnt lgkmcnt(0)\n" \
  "s_barrier\n" \
  "s_sub_i32 s83, 0, s83\n" \
  "v_add_u32 v0, s83, v0\n" \
  "ds_read_b128 v[18:21], v0 offset:0\n" \
  "ds_read_b128 v[22:25], v0 offset:2048\n" \
  "ds_read_b128 v[26:29], v0 offset:4096\n" \
  "ds_read2_b32 v[6:7], v3 offset0:16 offset1:48\n" \
  "ds_read_b128 v[10:13], v4 offset:256\n" \
  "ds_read_b128 v[14:17], v4 offset:320\n" \
  "ds_read_b32 v8, v3 offset:384\n" \
  "s_add_u32 s71, s71, 0x1000\n" \
  "s_mov_b32 s78, s80\n" \
  "s_xor_b32 s74, s74, 2048\n" \
  "s_xor_b32 s75, s75, 2048\n" \
  "s_setprio 3\n" \
  "ds_read_b128 v[30:33], v0 offset:6144\n" \
  "s_waitcnt lgkmcnt(7)\n" \
  "v_mfma_f32_16x16x32_bf16 v[34:37], v[64:67], v[18:21], 0\n" \
  "v_mfma_f32_16x16x32_bf16 v[38:41], v[128:131], v[18:21], 0\n" \
  "v_mfma_f32_16x16x32_bf16 v[42:45], v[18:21], v[192:195], 0\n" \
  "s_cmp_eq_u32 s78, 1\n" \
  "s_cbranch_scc0 .LCQ_X1_%=\n" \
  "s_mov_b32 m0, s73\n" \
  "s_nop 0\n" \
  "global_load_lds_dwordx4 v2, s[62:63] offset:0 sc1\n" \
  "global_load_lds_dwordx4 v2, s[62:63] offset:1024 sc1\n" \
  "s_add_u32 s62, s62, 0x4000\n" \
  "s_addc_u32 s63, s63, 0\n" \
  ".LCQ_X1_%=:\n" \
  "ds_read_b128 v[18:21], v0 offset:16384\n" \
  "s_waitcnt lgkmcnt(7)\n" \
  "v_mfma_f32_16x16x32_bf16 v[34:37], v[68:71], v[22:25], v[34:37]\n" \
  "s_waitcnt lgkmcnt(4)\n" \
  "v_fma_f32 v48, -v6, v10, v48\n" \
  "v_fma_f32 v49, -v6, v11, v49\n" \
  "v_mfma_f32_16x16x32_bf16 v[38:41], v[132:135], v[22:25], v[38:41]\n" \
  "v_fma_f32 v50, -v6, v12, v50\n" \
  "v_fma_f32 v51, -v6, v13, v51\n" \
  "v_mfma_f32_16x16x32_bf16 v[42:45], v[22:25], v[196:199], v[42:45]\n" \
  "ds_read_b128 v[10:13], v4 offset:64\n" \
  "s_waitcnt lgkmcnt(4)\n" \
  "v_fma_f32 v54, -v6, v14, v54\n" \
  "v_fma_f32 v55, -v6, v15, v55\n" \
  "s_cmp_eq_u32 s78, 1\n" \
  "s_cbranch_scc0 .LCQ_X2_%=\n" \
  "s_mov_b32 m0, s68\n" \
  "s_xor_b32 s68, s68, 0x10000\n" \
  "global_load_lds_dwordx4 v2, s[60:61] offset:0 sc1\n" \
  "global_load_lds_dwordx4 v2, s[60:61] offset:1024 sc1\n" \
  ".LCQ_X2_%=:\n" \
  "ds_read_b128 v[22:25], v0 offset:18432\n" \
  "v_mfma_f32_16x16x32_bf16 v[34:37], v[72:75], v[26:29], v[34:37]\n" \
  "v_fma_f32 v56, -v6, v16, v56\n" \
  "v_fma_f32 v57, -v6, v17, v57\n" \
  "v_mfma_f32_16x16x32_bf16 v[38:41], v[136:139], v[26:29], v[38:41]\n" \
  "ds_read_b128 v[14:17], v4 offset:192\n" \
  "v_mul_f32 v48, v48, v7\n" \
  "v_mul_f32 v49, v49, v7\n" \
  "v_mfma_f32_16x16x32_bf16 v[42:45], v[26:29], v[200:203], v[42:45]\n" \
  "v_mul_f32 v50, v50, v7\n" \
  "v_mul_f32 v51, v51, v7\n" \
  "s_cmp_eq_u32 s78, 1\n" \
  "s_cbranch_scc0 .LCQ_X3_%=\n" \
  "global_load_lds_dwordx4 v2, s[60:61] offset:2048 sc1\n" \
  "global_load_lds_dwordx4 v2, s[60:61] offset:3072 sc1\n" \
  "s_add_u32 s60, s60, 0x8000\n" \
  "s_addc_u32 s61, s61, 0\n" \
  ".LCQ_X3_%=:\n" \
  "ds_read_b128 v[26:29], v0 offset:20480\n" \
  "s_waitcnt lgkmcnt(5)\n" \
  "v_mfma_f32_16x16x32_bf16 v[34:37], v[76:79], v[30:33], v[34:37]\n" \
  "v_cvt_pk_bf16_f32 v48, v48, v49\n" \
  "v_cvt_pk_bf16_f32 v49, v50, v51\n" \
  "v_mfma_f32_16x16x32_bf16 v[38:41], v[140:143], v[30:33], v[38:41]\n" \
  "v_mul_f32 v54, v54, v7\n" \
  "v_mul_f32 v55, v55, v7\n" \
  "v_mfma_f32_16x16x32_bf16 v[42:45], v[30:33], v[204:207], v[42:45]\n" \
  "v_mul_f32 v56, v56, v7\n" \
  "v_mul_f32 v57, v57, v7\n" \
  "s_cmp_eq_u32 s78, 1\n" \
  "s_cbranch_scc0 .LCQ_X4_%=\n" \
  "s_add_u32 s87, s71, 0x1000\n" \
  "v_bfe_u32 v5, v2, 2, 5\n" \
  "buffer_load_dword v5, v5, s[52:55], s87 offen sc1\n" \
  ".LCQ_X4_%=:\n" \
  "ds_read_b128 v[30:33], v0 offset:22528\n" \
  "s_waitcnt lgkmcnt(5)\n" \
  "v_mfma_f32_16x16x32_bf16 v[34:37], v[80:83], v[18:21], v[34:37]\n" \
  "v_cvt_pk_bf16_f32 v54, v54, v55\n" \
  "v_cvt_pk_bf16_f32 v55, v56, v57\n" \
  "v_mfma_f32_16x16x32_bf16 v[38:41], v[144:147], v[18:21], v[38:41]\n" \
  "s_waitcnt lgkmcnt(4)\n" \
  "v_fma_f32 v60, -v10, v8, v60\n" \
  "v_fma_f32 v61, -v11, v8, v61\n" \
  "v_mfma_f32_16x16x32_bf16 v[42:45], v[18:21], v[208:211], v[42:45]\n" \
  "v_fma_f32 v62, -v12, v8, v62\n" \
  "v_fma_f32 v63, -v13, v8, v63\n" \
  "ds_read_b128 v[18:21], v0 offset:32768\n" \
  "s_waitcnt lgkmcnt(4)\n" \
  "v_mfma_f32_16x16x32_bf16 v[34:37], v[84:87], v[22:25], v[34:37]\n" \
  "v_permlane16_swap_b32 v46, v48\n" \
  "v_mfma_f32_16x16x32_bf16 v[38:41], v[148:151], v[22:25], v[38:41]\n" \
  "v_permlane16_swap_b32 v47, v49\n" \
  "v_mfma_f32_16x16x32_bf16 v[42:45], v[22:25], v[212:215], v[42:45]\n" \
  "v_permlane16_swap_b32 v52, v54\n" \
  "ds_read_b128 v[22:25], v0 offset:34816\n" \
  "s_waitcnt lgkmcnt(3)\n" \
  "v_mfma_f32_16x16x32_bf16 v[34:37], v[88:91], v[26:29], v[34:37]\n" \
  "v_permlane16_swap_b32 v53, v55\n" \
  "v_mfma_f32_16x16x32_bf16 v[38:41], v[152:155], v[26:29], v[38:41]\n" \
  "v_mul_f32 v60, v60, v14\n" \
  "v_mul_f32 v61, v61, v15\n" \
  "v_mfma_f32_16x16x32_bf16 v[42:45], v[26:29], v[216:219], v[42:45]\n" \
  "v_mul_f32 v62, v62, v16\n" \
  "v_mul_f32 v63, v63, v17\n" \
  "ds_read_b128 v[26:29], v0 offset:36864\n" \
  "s_waitcnt lgkmcnt(3)\n" \
  "v_mfma_f32_16x16x32_bf16 v[34:37], v[92:95], v[30:33], v[34:37]\n" \
  "v_cvt_pk_bf16_f32 v60, v60, v61\n" \
  "v_cvt_pk_bf16_f32 v61, v62, v63\n" \
  "v_mfma_f32_16x16x32_bf16 v[38:41], v[156:159], v[30:33], v[38:41]\n" \
  "v_mov_b64 v[10:11], 0\n" \
  "v_lshrrev_b32 v12, 1, v2\n" \
  "v_mfma_f32_16x16x32_bf16 v[42:45], v[30:33], v[220:223], v[42:45]\n" \
  "s_cmp_eq_u32 s81, 0\n" \
  "s_cbranch_scc1 .LCQ_S5_%=\n" \
  "s_add_u32 s87, s65, 0x2000\n" \
  "buffer_store_dwordx4 v[52:55], v1, s[40:43], s87 offen sc1\n" \
  "buffer_store_dwordx4 v[58:61], v2, s[44:47], s66 offen sc1\n" \
  "buffer_store_dwordx4 v[46:49], v1, s[40:43], s65 offen sc1\n" \
  "buffer_store_dwordx2 v[10:11], v12, s[48:51], s67 offen sc1\n" \
  "s_add_u32 s65, s65, 0x80000\n" \
  "s_add_u32 s66, s66, 0x40000\n" \
  "s_add_u32 s67, s67, 0x10000\n" \
  "s_mov_b32 s84, s72\n" \
  "s_add_u32 s72, s72, 0x1000\n" \
  "s_mov_b32 s85, s70\n" \
  "s_xor_b32 s70, s70, 4\n" \
  "s_mov_b32 s79, 1\n" \
  ".LCQ_S5_%=:\n" \
  "s_setprio 2\n" \
  "ds_read_b128 v[30:33], v0 offset:38912\n" \
  "s_waitcnt lgkmcnt(3)\n" \
  "v_mfma_f32_16x16x32_bf16 v[34:37], v[96:99], v[18:21], v[34:37]\n" \
  "v_bfe_u32 v7, v2, 4, 5\n" \
  "v_and_b32 v8, 0x200, v2\n" \
  "v_mfma_f32_16x16x32_bf16 v[38:41], v[160:163], v[18:21], v[38:41]\n" \
  "v_lshl_add_u32 v6, v7, 3, s74\n" \
  "v_lshl_add_u32 v6, v8, 1, v6\n" \
  "v_mfma_f32_16x16x32_bf16 v[42:45], v[18:21], v[224:227], v[42:45]\n" \
  "ds_read2_b64 v[10:13], v6 offset0:0 offset1:32\n" \
  "ds_read2_b64 v[14:17], v6 offset0:64 offset1:96\n" \
  "v_lshrrev_b32 v7, 2, v2\n" \
  "s_andn2_b32 s82, s79, s81\n" \
  "s_cbranch_scc0 .LCQ_NPA_%=\n" \
  "s_waitcnt vmcnt(7)\n" \
  "s_cmp_lg_u32 s78, 0\n" \
  "s_cbranch_scc1 .LCQ_PA_%=\n" \
  "s_waitcnt vmcnt(0)\n" \
  ".LCQ_PA_%=:\n" \
  "v_mov_b32 v9, s85\n" \
  "s_mov_b64 exec, 2\n" \
  "ds_add_rtn_u32 v9, v9, v2\n" \
  "s_mov_b64 exec, -1\n" \
  ".LCQ_NPA_%=:\n" \
  "s_cmp_eq_u32 s78, 0\n" \
  "s_cbranch_scc0 .LCQ_X6_%=\n" \
  "v_bfe_u32 v5, v2, 2, 5\n" \
  "buffer_load_dword v5, v5, s[52:55], s71 offen sc1\n" \
  ".LCQ_X6_%=:\n" \
  "ds_read_b128 v[18:21], v0 offset:49152\n" \
  "s_waitcnt lgkmcnt(5)\n" \
  "v_mfma_f32_16x16x32_bf16 v[34:37], v[100:103], v[22:25], v[34:37]\n" \
  "s_waitcnt lgkmcnt(1)\n" \
  "v_add_f32 v6, v10, v12\n" \
  "v_add_f32 v8, v14, v16\n" \
  "v_mfma_f32_16x16x32_bf16 v[38:41], v[164:167], v[22:25], v[38:41]\n" \
  "v_add_f32 v6, v6, v8\n" \
  "v_mov_b32 v8, v6\n" \
  "v_mfma_f32_16x16x32_bf16 v[42:45], v[22:25], v[228:231], v[42:45]\n" \
  "v_add_f32 v11, v11, v13\n" \
  "v_add_f32 v15, v15, v17\n" \
  "ds_read_b128 v[22:25], v0 offset:51200\n" \
  "v_mfma_f32_16x16x32_bf16 v[34:37], v[104:107], v[26:29], v[34:37]\n" \
  "v_permlane32_swap_b32 v6, v8\n" \
  "v_mfma_f32_16x16x32_bf16 v[38:41], v[168:171], v[26:29], v[38:41]\n" \
  "v_add_f32 v6, v6, v8\n" \
  "v_fmamk_f32 v10, v6, 0xbe000000, v10\n" \
  "v_mfma_f32_16x16x32_bf16 v[42:45], v[26:29], v[232:235], v[42:45]\n" \
  "v_fmamk_f32 v12, v6, 0xbe000000, v12\n" \
  "v_fmamk_f32 v14, v6, 0xbe000000, v14\n" \
  "s_cmp_eq_u32 s82, 1\n" \
  "s_cbranch_scc0 .LCQ_X8_%=\n" \
  "s_waitcnt lgkmcnt(2)\n" \
  "v_readlane_b32 s87, v9, 1\n" \
  "s_mov_b32 s79, 0\n" \
  "s_and_b32 s87, s87, 0x70\n" \
  "s_cmp_eq_u32 s87, 0x70\n" \
  "s_cbranch_scc0 .LCQ_F7_%=\n" \
  "s_mov_b64 exec, 2\n" \
  "buffer_store_dword v9, off, s[56:59], s84 sc1\n" \
  "s_mov_b64 exec, -1\n" \
  ".LCQ_F7_%=:\n" \
  ".LCQ_X8_%=:\n" \
  "ds_read_b128 v[26:29], v0 offset:53248\n" \
  "v_mfma_f32_16x16x32_bf16 v[34:37], v[108:111], v[30:33], v[34:37]\n" \
  "v_fmamk_f32 v16, v6, 0xbe000000, v16\n" \
  "v_add_f32 v11, v11, v15\n" \
  "v_mfma_f32_16x16x32_bf16 v[38:41], v[172:175], v[30:33], v[38:41]\n" \
  "v_mul_f32 v10, v10, v10\n" \
  "v_fmac_f32 v10, v12, v12\n" \
  "v_mfma_f32_16x16x32_bf16 v[42:45], v[30:33], v[236:239], v[42:45]\n" \
  "v_fmac_f32 v10, v14, v14\n" \
  "v_fmac_f32 v10, v16, v16\n" \
  "ds_read_b128 v[30:33], v0 offset:55296\n" \
  "s_waitcnt lgkmcnt(3)\n" \
  "v_mfma_f32_16x16x32_bf16 v[34:37], v[112:115], v[18:21], v[34:37]\n" \
  "v_fmamk_f32 v11, v10, 0x3c800000, v11\n" \
  "v_mov_b32 v8, v11\n" \
  "v_mfma_f32_16x16x32_bf16 v[38:41], v[176:179], v[18:21], v[38:41]\n" \
  "v_mul_f32 v6, 0x3b000000, v6\n" \
  "v_add_u32 v7, s69, v7\n" \
  "v_mfma_f32_16x16x32_bf16 v[42:45], v[18:21], v[240:243], v[42:45]\n" \
  "v_permlane32_swap_b32 v11, v8\n" \
  "ds_read_b128 v[18:21], v0 offset:256\n" \
  "s_waitcnt lgkmcnt(3)\n" \
  "v_mfma_f32_16x16x32_bf16 v[34:37], v[116:119], v[22:25], v[34:37]\n" \
  "v_add_f32 v11, v11, v8\n" \
  "v_mul_f32 v10, 0x3b000000, v11\n" \
  "v_mfma_f32_16x16x32_bf16 v[38:41], v[180:183], v[22:25], v[38:41]\n" \
  "v_cvt_pk_bf16_f32 v6, v6, v10\n" \
  "v_and_b32 v10, 0xffff0000, v6\n" \
  "v_mfma_f32_16x16x32_bf16 v[42:45], v[22:25], v[244:247], v[42:45]\n" \
  "v_lshlrev_b32 v6, 16, v6\n" \
  "v_add_f32 v10, 0x3727c5ac, v10\n" \
  "ds_read_b128 v[22:25], v0 offset:2304\n" \
  "s_waitcnt lgkmcnt(3)\n" \
  "v_mfma_f32_16x16x32_bf16 v[34:37], v[120:123], v[26:29], v[34:37]\n" \
  "v_cvt_pk_bf16_f32 v10, v10, v10\n" \
  "v_lshlrev_b32 v10, 16, v10\n" \
  "v_mfma_f32_16x16x32_bf16 v[38:41], v[184:187], v[26:29], v[38:41]\n" \
  "v_sqrt_f32 v10, v10\n" \
  "s_nop 0\n" \
  "v_cvt_pk_bf16_f32 v10, v10, v10\n" \
  "v_mfma_f32_16x16x32_bf16 v[42:45], v[26:29], v[248:251], v[42:45]\n" \
  "v_lshlrev_b32 v10, 16, v10\n" \
  "v_rcp_f32 v10, v10\n" \
  "ds_read_b128 v[26:29], v0 offset:4352\n" \
  "s_waitcnt lgkmcnt(3)\n" \
  "v_mfma_f32_16x16x32_bf16 v[34:37], v[124:127], v[30:33], v[34:37]\n" \
  "s_nop 0\n" \
  "v_cndmask_b32 v10, v6, v10, s[76:77]\n" \
  "ds_write_b32 v7, v10\n" \
  "v_mfma_f32_16x16x32_bf16 v[38:41], v[188:191], v[30:33], v[38:41]\n" \
  "v_mfma_f32_16x16x32_bf16 v[42:45], v[30:33], v[252:255], v[42:45]\n" \
  "s_setprio 1\n" \
  "ds_read_b128 v[30:33], v0 offset:6400\n" \
  "s_waitcnt lgkmcnt(4)\n" \
  "v_mfma_f32_16x16x32_bf16 v[48:51], v[64:67], v[18:21], 0\n" \
  "ds_read2_b32 v[6:7], v3 offset0:0 offset1:32\n" \
  "ds_read_b128 v[10:13], v4 offset:256\n" \
  "ds_read_b128 v[14:17], v4 offset:320\n" \
  "ds_read_b32 v8, v3 offset:384\n" \
  "v_mfma_f32_16x16x32_bf16 v[54:57], v[128:131], v[18:21], 0\n" \
  "v_mfma_f32_16x16x32_bf16 v[60:63], v[18:21], v[192:195], 0\n" \
  "ds_read_b128 v[18:21], v0 offset:16640\n" \
  "s_waitcnt lgkmcnt(8)\n" \
  "v_mfma_f32_16x16x32_bf16 v[48:51], v[68:71], v[22:25], v[48:51]\n" \
  "s_waitcnt lgkmcnt(3)\n" \
  "v_fma_f32 v34, -v6, v10, v34\n" \
  "v_fma_f32 v35, -v6, v11, v35\n" \
  "v_mfma_f32_16x16x32_bf16 v[54:57], v[132:135], v[22:25], v[54:57]\n" \
  "v_fma_f32 v36, -v6, v12, v36\n" \
  "v_fma_f32 v37, -v6, v13, v37\n" \
  "v_mfma_f32_16x16x32_bf16 v[60:63], v[22:25], v[196:199], v[60:63]\n" \
  "ds_read_b128 v[10:13], v4 offset:0\n" \
  "s_waitcnt lgkmcnt(3)\n" \
  "v_fma_f32 v38, -v6, v14, v38\n" \
  "v_fma_f32 v39, -v6, v15, v39\n" \
  "s_mov_b32 s82, 0\n" \
  "s_cmp_eq_u32 s78, 0\n" \
  "s_cbranch_scc0 .LCQ_X9_%=\n" \
  "s_waitcnt vmcnt(0)\n" \
  "v_cmp_eq_u32 vcc, 0, v5\n" \
  "s_cmp_eq_u64 vcc, 0\n" \
  "s_cselect_b32 s78, 2, 0\n" \
  "s_mov_b32 s82, s79\n" \
  ".LCQ_X9_%=:\n" \
  "s_cmp_eq_u32 s82, 1\n" \
  "s_cbranch_scc0 .LCQ_X10_%=\n" \
  "v_mov_b32 v9, s85\n" \
  "s_mov_b64 exec, 2\n" \
  "ds_add_rtn_u32 v9, v9, v2\n" \
  "s_mov_b64 exec, -1\n" \
  ".LCQ_X10_%=:\n" \
  "s_cmp_eq_u32 s78, 2\n" \
  "s_cbranch_scc0 .LCQ_X11_%=\n" \
  "s_mov_b32 m0, s73\n" \
  "s_nop 0\n" \
  "global_load_lds_dwordx4 v2, s[62:63] offset:0 sc1\n" \
  "global_load_lds_dwordx4 v2, s[62:63] offset:1024 sc1\n" \
  "s_add_u32 s62, s62, 0x4000\n" \
  "s_addc_u32 s63, s63, 0\n" \
  ".LCQ_X11_%=:\n" \
  "ds_read_b128 v[22:25], v0 offset:18688\n" \
  "v_mfma_f32_16x16x32_bf16 v[48:51], v[72:75], v[26:29], v[48:51]\n" \
  "v_fma_f32 v40, -v6, v16, v40\n" \
  "v_fma_f32 v41, -v6, v17, v41\n" \
  "v_mfma_f32_16x16x32_bf16 v[54:57], v[136:139], v[26:29], v[54:57]\n" \
  "ds_read_b128 v[14:17], v4 offset:128\n" \
  "v_mul_f32 v34, v34, v7\n" \
  "v_mul_f32 v35, v35, v7\n" \
  "v_mfma_f32_16x16x32_bf16 v[60:63], v[26:29], v[200:203], v[60:63]\n" \
  "v_mul_f32 v36, v36, v7\n" \
  "v_mul_f32 v37, v37, v7\n" \
  "s_cmp_eq_u32 s78, 2\n" \
  "s_cbranch_scc0 .LCQ_X12_%=\n" \
  "s_mov_b32 m0, s68\n" \
  "s_xor_b32 s68, s68, 0x10000\n" \
  "global_load_lds_dwordx4 v2, s[60:61] offset:0 sc1\n" \
  "global_load_lds_dwordx4 v2, s[60:61] offset:1024 sc1\n" \
  ".LCQ_X12_%=:\n" \
  "ds_read_b128 v[26:29], v0 offset:20736\n" \
  "v_mfma_f32_16x16x32_bf16 v[48:51], v[76:79], v[30:33], v[48:51]\n" \
  "v_cvt_pk_bf16_f32 v46, v34, v35\n" \
  "v_cvt_pk_bf16_f32 v47, v36, v37\n" \
  "v_mfma_f32_16x16x32_bf16 v[54:57], v[140:143], v[30:33], v[54:57]\n" \
  "v_mul_f32 v38, v38, v7\n" \
  "v_mul_f32 v39, v39, v7\n" \
  "v_mfma_f32_16x16x32_bf16 v[60:63], v[30:33], v[204:207], v[60:63]\n" \
  "v_mul_f32 v40, v40, v7\n" \
  "v_mul_f32 v41, v41, v7\n" \
  "s_cmp_eq_u32 s82, 1\n" \
  "s_cbranch_scc0 .LCQ_X14_%=\n" \
  "s_waitcnt lgkmcnt(2)\n" \
  "v_readlane_b32 s87, v9, 1\n" \
  "s_mov_b32 s79, 0\n" \
  "s_and_b32 s87, s87, 0x70\n" \
  "s_cmp_eq_u32 s87, 0x70\n" \
  "s_cbranch_scc0 .LCQ_F13_%=\n" \
  "s_mov_b64 exec, 2\n" \
  "buffer_store_dword v9, off, s[56:59], s84 sc1\n" \
  "s_mov_b64 exec, -1\n" \
  ".LCQ_F13_%=:\n" \
  ".LCQ_X14_%=:\n" \
  "s_cmp_eq_u32 s78, 2\n" \
  "s_cbranch_scc0 .LCQ_X15_%=\n" \
  "global_load_lds_dwordx4 v2, s[60:61] offset:2048 sc1\n" \
  "global_load_lds_dwordx4 v2, s[60:61] offset:3072 sc1\n" \
  "s_add_u32 s60, s60, 0x8000\n" \
  "s_addc_u32 s61, s61, 0\n" \
  ".LCQ_X15_%=:\n" \
  "ds_read_b128 v[30:33], v0 offset:22784\n" \
  "s_waitcnt lgkmcnt(5)\n" \
  "v_mfma_f32_16x16x32_bf16 v[48:51], v[80:83], v[18:21], v[48:51]\n" \
  "v_cvt_pk_bf16_f32 v52, v38, v39\n" \
  "v_cvt_pk_bf16_f32 v53, v40, v41\n" \
  "v_mfma_f32_16x16x32_bf16 v[54:57], v[144:147], v[18:21], v[54:57]\n" \
  "s_waitcnt lgkmcnt(4)\n" \
  "v_fma_f32 v42, -v10, v8, v42\n" \
  "v_fma_f32 v43, -v11, v8, v43\n" \
  "v_mfma_f32_16x16x32_bf16 v[60:63], v[18:21], v[208:211], v[60:63]\n" \
  "v_fma_f32 v44, -v12, v8, v44\n" \
  "v_fma_f32 v45, -v13, v8, v45\n" \
  "s_cmp_eq_u32 s78, 2\n" \
  "s_cbranch_scc0 .LCQ_X16_%=\n" \
  "s_add_u32 s87, s71, 0x1000\n" \
  "v_bfe_u32 v5, v2, 2, 5\n" \
  "buffer_load_dword v5, v5, s[52:55], s87 offen sc1\n" \
  ".LCQ_X16_%=:\n" \
  "s_and_b32 s82, s79, s78\n" \
  "s_cmp_eq_u32 s78, 1\n" \
  "s_cbranch_scc0 .LCQ_X17_%=\n" \
  "s_waitcnt vmcnt(0)\n" \
  ".LCQ_X17_%=:\n" \
  "s_cmp_eq_u32 s82, 1\n" \
  "s_cbranch_scc0 .LCQ_X18_%=\n" \
  "v_mov_b32 v9, s85\n" \
  "s_mov_b64 exec, 2\n" \
  "ds_add_rtn_u32 v9, v9, v2\n" \
  "s_mov_b64 exec, -1\n" \
  ".LCQ_X18_%=:\n" \
  "ds_read_b128 v[18:21], v0 offset:33024\n" \
  "s_waitcnt lgkmcnt(4)\n" \
  "v_mfma_f32_16x16x32_bf16 v[48:51], v[84:87], v[22:25], v[48:51]\n" \
  "s_waitcnt lgkmcnt(3)\n" \
  "v_mul_f32 v42, v42, v14\n" \
  "v_mul_f32 v43, v43, v15\n" \
  "v_mfma_f32_16x16x32_bf16 v[54:57], v[148:151], v[22:25], v[54:57]\n" \
  "v_mul_f32 v44, v44, v16\n" \
  "v_mul_f32 v45, v45, v17\n" \
  "v_mfma_f32_16x16x32_bf16 v[60:63], v[22:25], v[212:215], v[60:63]\n" \
  "v_cvt_pk_bf16_f32 v58, v42, v43\n" \
  "v_cvt_pk_bf16_f32 v59, v44, v45\n" \
  "ds_read_b128 v[22:25], v0 offset:35072\n" \
  "s_waitcnt lgkmcnt(3)\n" \
  "v_mfma_f32_16x16x32_bf16 v[48:51], v[88:91], v[26:29], v[48:51]\n" \
  "v_mfma_f32_16x16x32_bf16 v[54:57], v[152:155], v[26:29], v[54:57]\n" \
  "v_mfma_f32_16x16x32_bf16 v[60:63], v[26:29], v[216:219], v[60:63]\n" \
  "s_cmp_eq_u32 s82, 1\n" \
  "s_cbranch_scc0 .LCQ_X20_%=\n" \
  "s_waitcnt lgkmcnt(2)\n" \
  "v_readlane_b32 s87, v9, 1\n" \
  "s_mov_b32 s79, 0\n" \
  "s_and_b32 s87, s87, 0x70\n" \
  "s_cmp_eq_u32 s87, 0x70\n" \
  "s_cbranch_scc0 .LCQ_F19_%=\n" \
  "s_mov_b64 exec, 2\n" \
  "buffer_store_dword v9, off, s[56:59], s84 sc1\n" \
  "s_mov_b64 exec, -1\n" \
  ".LCQ_F19_%=:\n" \
  ".LCQ_X20_%=:\n" \
  "s_cmp_eq_u32 s78, 0\n" \
  "s_cbranch_scc0 .LCQ_X21_%=\n" \
  "v_bfe_u32 v5, v2, 2, 5\n" \
  "buffer_load_dword v5, v5, s[52:55], s71 offen sc1\n" \
  ".LCQ_X21_%=:\n" \
  "ds_read_b128 v[26:29], v0 offset:37120\n" \
  "s_waitcnt lgkmcnt(3)\n" \
  "v_mfma_f32_16x16x32_bf16 v[48:51], v[92:95], v[30:33], v[48:51]\n" \
  "v_mfma_f32_16x16x32_bf16 v[54:57], v[156:159], v[30:33], v[54:57]\n" \
  "v_mfma_f32_16x16x32_bf16 v[60:63], v[30:33], v[220:223], v[60:63]\n" \
  "s_setprio 0\n" \
  "ds_read_b128 v[30:33], v0 offset:39168\n" \
  "s_waitcnt lgkmcnt(3)\n" \
  "v_mfma_f32_16x16x32_bf16 v[48:51], v[96:99], v[18:21], v[48:51]\n" \
  "v_mfma_f32_16x16x32_bf16 v[54:57], v[160:163], v[18:21], v[54:57]\n" \
  "v_mfma_f32_16x16x32_bf16 v[60:63], v[18:21], v[224:227], v[60:63]\n" \
  "ds_read_b128 v[18:21], v0 offset:49408\n" \
  "s_waitcnt lgkmcnt(3)\n" \
  "v_mfma_f32_16x16x32_bf16 v[48:51], v[100:103], v[22:25], v[48:51]\n" \
  "v_mfma_f32_16x16x32_bf16 v[54:57], v[164:167], v[22:25], v[54:57]\n" \
  "v_mfma_f32_16x16x32_bf16 v[60:63], v[22:25], v[228:231], v[60:63]\n" \
  "ds_read_b128 v[22:25], v0 offset:51456\n" \
  "s_waitcnt lgkmcnt(3)\n" \
  "v_mfma_f32_16x16x32_bf16 v[48:51], v[104:107], v[26:29], v[48:51]\n" \
  "v_bfe_u32 v7, v2, 4, 5\n" \
  "v_and_b32 v8, 0x200, v2\n" \
  "v_mfma_f32_16x16x32_bf16 v[54:57], v[168:171], v[26:29], v[54:57]\n" \
  "v_lshl_add_u32 v6, v7, 3, s73\n" \
  "v_lshl_add_u32 v6, v8, 1, v6\n" \
  "v_mfma_f32_16x16x32_bf16 v[60:63], v[26:29], v[232:235], v[60:63]\n" \
  "s_cmp_eq_u32 s78, 2\n" \
  "s_cbranch_scc0 .LCQ_X22_%=\n" \
  "s_waitcnt vmcnt(5)\n" \
  ".LCQ_X22_%=:\n" \
  "ds_read_b128 v[26:29], v0 offset:53504\n" \
  "s_waitcnt lgkmcnt(3)\n" \
  "v_mfma_f32_16x16x32_bf16 v[48:51], v[108:111], v[30:33], v[48:51]\n" \
  "ds_read2_b64 v[10:13], v6 offset0:0 offset1:32\n" \
  "ds_read2_b64 v[14:17], v6 offset0:64 offset1:96\n" \
  "v_lshl_add_u32 v7, v7, 3, s75\n" \
  "v_mfma_f32_16x16x32_bf16 v[54:57], v[172:175], v[30:33], v[54:57]\n" \
  "v_mfma_f32_16x16x32_bf16 v[60:63], v[30:33], v[236:239], v[60:63]\n" \
  "ds_read_b128 v[30:33], v0 offset:55552\n" \
  "s_waitcnt lgkmcnt(5)\n" \
  "v_mfma_f32_16x16x32_bf16 v[48:51], v[112:115], v[18:21], v[48:51]\n" \
  "s_waitcnt lgkmcnt(1)\n" \
  "v_add_f32 v6, v10, v12\n" \
  "v_add_f32 v8, v14, v16\n" \
  "v_mfma_f32_16x16x32_bf16 v[54:57], v[176:179], v[18:21], v[54:57]\n" \
  "v_add_f32 v6, v6, v8\n" \
  "v_mov_b32 v8, v6\n" \
  "v_mfma_f32_16x16x32_bf16 v[60:63], v[18:21], v[240:243], v[60:63]\n" \
  "v_add_f32 v11, v11, v13\n" \
  "v_add_f32 v15, v15, v17\n" \
  "v_mfma_f32_16x16x32_bf16 v[48:51], v[116:119], v[22:25], v[48:51]\n" \
  "v_permlane32_swap_b32 v6, v8\n" \
  "v_mfma_f32_16x16x32_bf16 v[54:57], v[180:183], v[22:25], v[54:57]\n" \
  "v_add_f32 v6, v6, v8\n" \
  "v_fmamk_f32 v10, v6, 0xbe000000, v10\n" \
  "v_mfma_f32_16x16x32_bf16 v[60:63], v[22:25], v[244:247], v[60:63]\n" \
  "v_fmamk_f32 v12, v6, 0xbe000000, v12\n" \
  "v_fmamk_f32 v14, v6, 0xbe000000, v14\n" \
  "v_mfma_f32_16x16x32_bf16 v[48:51], v[120:123], v[26:29], v[48:51]\n" \
  "v_fmamk_f32 v16, v6, 0xbe000000, v16\n" \
  "v_add_f32 v11, v11, v15\n" \
  "v_mfma_f32_16x16x32_bf16 v[54:57], v[184:187], v[26:29], v[54:57]\n" \
  "v_mul_f32 v10, v10, v10\n" \
  "v_fmac_f32 v10, v12, v12\n" \
  "v_mfma_f32_16x16x32_bf16 v[60:63], v[26:29], v[248:251], v[60:63]\n" \
  "v_fmac_f32 v10, v14, v14\n" \
  "v_fmac_f32 v10, v16, v16\n" \
  "s_waitcnt lgkmcnt(0)\n" \
  "v_mfma_f32_16x16x32_bf16 v[48:51], v[124:127], v[30:33], v[48:51]\n" \
  "v_fmamk_f32 v11, v10, 0x3e000000, v11\n" \
  "v_mov_b32 v8, v11\n" \
  "v_mfma_f32_16x16x32_bf16 v[54:57], v[188:191], v[30:33], v[54:57]\n" \
  "v_mov_b32 v10, v6\n" \
  "s_nop 0\n" \
  "v_permlane32_swap_b32 v11, v8\n" \
  "v_mfma_f32_16x16x32_bf16 v[60:63], v[30:33], v[252:255], v[60:63]\n" \
  "v_add_f32 v11, v11, v8\n" \
  "ds_write_b64 v7, v[10:11]\n" \
  "s_cmp_eq_u32 s78, 1\n" \
  "s_cbranch_scc1 .LCQ_NV_%=\n" \
  "s_waitcnt vmcnt(0)\n" \
  ".LCQ_NV_%=:\n" \
  "v_cmp_eq_u32 vcc, 0, v5\n" \
  "s_cmp_eq_u64 vcc, 0\n" \
  "s_cselect_b32 s80, 1, 0\n" \
  "s_sub_u32 s64, s64, 1\n" \
  "s_cmp_lt_u32 s64, 62\n" \
  "s_cselect_b32 s81, s78, 0\n" \
  "s_min_u32 s81, s81, 1\n" \
  "s_cmp_lg_u32 s81, 0\n" \
  "s_cbranch_scc1 .LCQ_TOP_%=\n" \
  "s_cmp_lg_u32 s78, 0\n" \
  "s_cbranch_scc1 .LCQ_NA_%=\n" \
  "s_cmp_eq_u32 s80, 1\n" \
  "s_cbranch_scc0 .LCQ_X23_%=\n" \
  "s_mov_b32 m0, s73\n" \
  "s_nop 0\n" \
  "global_load_lds_dwordx4 v2, s[62:63] offset:0 sc1\n" \
  "global_load_lds_dwordx4 v2, s[62:63] offset:1024 sc1\n" \
  "s_add_u32 s62, s62, 0x4000\n" \
  "s_addc_u32 s63, s63, 0\n" \
  "s_mov_b32 m0, s68\n" \
  "s_xor_b32 s68, s68, 0x10000\n" \
  "global_load_lds_dwordx4 v2, s[60:61] offset:0 sc1\n" \
  "global_load_lds_dwordx4 v2, s[60:61] offset:1024 sc1\n" \
  "global_load_lds_dwordx4 v2, s[60:61] offset:2048 sc1\n" \
  "global_load_lds_dwordx4 v2, s[60:61] offset:3072 sc1\n" \
  "s_add_u32 s60, s60, 0x8000\n" \
  "s_addc_u32 s61, s61, 0\n" \
  "s_add_u32 s87, s71, 0x1000\n" \
  "v_bfe_u32 v5, v2, 2, 5\n" \
  "buffer_load_dword v5, v5, s[52:55], s87 offen sc1\n" \
  ".LCQ_X23_%=:\n" \
  ".LCQ_NA_%=:\n" \
  "ds_read2_b32 v[6:7], v3 offset0:16 offset1:48\n" \
  "ds_read_b128 v[10:13], v4 offset:256\n" \
  "ds_read_b128 v[14:17], v4 offset:320\n" \
  "ds_read_b32 v8, v3 offset:384\n" \
  "s_waitcnt lgkmcnt(2)\n" \
  "v_fma_f32 v48, -v6, v10, v48\n" \
  "v_fma_f32 v49, -v6, v11, v49\n" \
  "v_fma_f32 v50, -v6, v12, v50\n" \
  "v_fma_f32 v51, -v6, v13, v51\n" \
  "ds_read_b128 v[10:13], v4 offset:64\n" \
  "s_waitcnt lgkmcnt(2)\n" \
  "v_fma_f32 v54, -v6, v14, v54\n" \
  "v_fma_f32 v55, -v6, v15, v55\n" \
  "v_fma_f32 v56, -v6, v16, v56\n" \
  "v_fma_f32 v57, -v6, v17, v57\n" \
  "ds_read_b128 v[14:17], v4 offset:192\n" \
  "v_mul_f32 v48, v48, v7\n" \
  "v_mul_f32 v49, v49, v7\n" \
  "v_mul_f32 v50, v50, v7\n" \
  "v_mul_f32 v51, v51, v7\n" \
  "v_cvt_pk_bf16_f32 v48, v48, v49\n" \
  "v_cvt_pk_bf16_f32 v49, v50, v51\n" \
  "v_mul_f32 v54, v54, v7\n" \
  "v_mul_f32 v55, v55, v7\n" \
  "v_mul_f32 v56, v56, v7\n" \
  "v_mul_f32 v57, v57, v7\n" \
  "v_cvt_pk_bf16_f32 v54, v54, v55\n" \
  "v_cvt_pk_bf16_f32 v55, v56, v57\n" \
  "s_waitcnt lgkmcnt(1)\n" \
  "v_fma_f32 v60, -v10, v8, v60\n" \
  "v_fma_f32 v61, -v11, v8, v61\n" \
  "v_fma_f32 v62, -v12, v8, v62\n" \
  "v_fma_f32 v63, -v13, v8, v63\n" \
  "v_permlane16_swap_b32 v46, v48\n" \
  "v_permlane16_swap_b32 v47, v49\n" \
  "v_permlane16_swap_b32 v52, v54\n" \
  "v_permlane16_swap_b32 v53, v55\n" \
  "s_waitcnt lgkmcnt(0)\n" \
  "v_mul_f32 v60, v60, v14\n" \
  "v_mul_f32 v61, v61, v15\n" \
  "v_mul_f32 v62, v62, v16\n" \
  "v_mul_f32 v63, v63, v17\n" \
  "v_cvt_pk_bf16_f32 v60, v60, v61\n" \
  "v_cvt_pk_bf16_f32 v61, v62, v63\n" \
  "v_mov_b64 v[10:11], 0\n" \
  "v_lshrrev_b32 v12, 1, v2\n" \
  "s_add_u32 s87, s65, 0x2000\n" \
  "buffer_store_dwordx4 v[52:55], v1, s[40:43], s87 offen sc1\n" \
  "buffer_store_dwordx4 v[58:61], v2, s[44:47], s66 offen sc1\n" \
  "buffer_store_dwordx4 v[46:49], v1, s[40:43], s65 offen sc1\n" \
  "buffer_store_dwordx2 v[10:11], v12, s[48:51], s67 offen sc1\n" \
  "s_add_u32 s65, s65, 0x80000\n" \
  "s_add_u32 s66, s66, 0x40000\n" \
  "s_add_u32 s67, s67, 0x10000\n" \
  "s_mov_b32 s84, s72\n" \
  "s_add_u32 s72, s72, 0x1000\n" \
  "s_mov_b32 s85, s70\n" \
  "s_xor_b32 s70, s70, 4\n" \
  "s_mov_b32 s79, 1\n" \
  "s_cmp_lg_u32 s78, 0\n" \
  "s_cbranch_scc1 .LCQ_TOP_%=\n" \
  "s_cmp_eq_u32 s80, 0\n" \
  "s_cbranch_scc1 .LCQ_POLL_%=\n" \
  "s_waitcnt vmcnt(4)\n" \
  "s_branch .LCQ_GOT_%=\n" \
  ".LCQ_POLL_%=:\n" \
  "s_mov_b32 s86, 0x40000\n" \
  "s_branch .LCQ_PW_%=\n" \
  ".LCQ_PV_%=:\n" \
  "s_sleep 12\n" \
  ".LCQ_PW_%=:\n" \
  "v_bfe_u32 v5, v2, 2, 5\n" \
  "buffer_load_dword v5, v5, s[52:55], s71 offen sc1\n" \
  "s_sub_u32 s86, s86, 1\n" \
  "s_cbranch_scc1 .LCQ_END_%=\n" \
  "s_waitcnt vmcnt(0)\n" \
  "s_cmp_eq_u32 s79, 0\n" \
  "s_cbranch_scc1 .LCQ_NPD_%=\n" \
  "v_mov_b32 v9, s85\n" \
  "s_mov_b64 exec, 2\n" \
  "ds_add_rtn_u32 v9, v9, v2\n" \
  "s_mov_b64 exec, -1\n" \
  "s_waitcnt lgkmcnt(0)\n" \
  "v_readlane_b32 s87, v9, 1\n" \
  "s_mov_b32 s79, 0\n" \
  "s_and_b32 s87, s87, 0x70\n" \
  "s_cmp_eq_u32 s87, 0x70\n" \
  "s_cbranch_scc0 .LCQ_F24_%=\n" \
  "s_mov_b64 exec, 2\n" \
  "buffer_store_dword v9, off, s[56:59], s84 sc1\n" \
  "s_mov_b64 exec, -1\n" \
  ".LCQ_F24_%=:\n" \
  "s_cmp_eq_u32 s64, 0\n" \
  "s_cbranch_scc1 .LCQ_END_%=\n" \
  ".LCQ_NPD_%=:\n" \
  "v_cmp_eq_u32 vcc, 0, v5\n" \
  "s_cmp_eq_u64 vcc, 0\n" \
  "s_cbranch_scc0 .LCQ_PV_%=\n" \
  "s_mov_b32 m0, s73\n" \
  "s_nop 0\n" \
  "global_load_lds_dwordx4 v2, s[62:63] offset:0 sc1\n" \
  "global_load_lds_dwordx4 v2, s[62:63] offset:1024 sc1\n" \
  "s_add_u32 s62, s62, 0x4000\n" \
  "s_addc_u32 s63, s63, 0\n" \
  "s_mov_b32 m0, s68\n" \
  "s_xor_b32 s68, s68, 0x10000\n" \
  "global_load_lds_dwordx4 v2, s[60:61] offset:0 sc1\n" \
  "global_load_lds_dwordx4 v2, s[60:61] offset:1024 sc1\n" \
  "global_load_lds_dwordx4 v2, s[60:61] offset:2048 sc1\n" \
  "global_load_lds_dwordx4 v2, s[60:61] offset:3072 sc1\n" \
  "s_add_u32 s60, s60, 0x8000\n" \
  "s_addc_u32 s61, s61, 0\n" \
  "s_add_u32 s87, s71, 0x1000\n" \
  "v_bfe_u32 v5, v2, 2, 5\n" \
  "buffer_load_dword v5, v5, s[52:55], s87 offen sc1\n" \
  "s_waitcnt vmcnt(0)\n" \
  ".LCQ_GOT_%=:\n" \
  "v_bfe_u32 v7, v2, 4, 5\n" \
  "v_and_b32 v8, 0x200, v2\n" \
  "v_lshl_add_u32 v6, v7, 3, s73\n" \
  "v_lshl_add_u32 v6, v8, 1, v6\n" \
  "ds_read2_b64 v[10:13], v6 offset0:0 offset1:32\n" \
  "ds_read2_b64 v[14:17], v6 offset0:64 offset1:96\n" \
  "v_lshl_add_u32 v7, v7, 3, s75\n" \
  "s_waitcnt lgkmcnt(0)\n" \
  "v_add_f32 v6, v10, v12\n" \
  "v_add_f32 v8, v14, v16\n" \
  "v_add_f32 v6, v6, v8\n" \
  "v_mov_b32 v8, v6\n" \
  "v_add_f32 v11, v11, v13\n" \
  "v_add_f32 v15, v15, v17\n" \
  "v_permlane32_swap_b32 v6, v8\n" \
  "v_add_f32 v6, v6, v8\n" \
  "v_fmamk_f32 v10, v6, 0xbe000000, v10\n" \
  "v_fmamk_f32 v12, v6, 0xbe000000, v12\n" \
  "v_fmamk_f32 v14, v6, 0xbe000000, v14\n" \
  "v_fmamk_f32 v16, v6, 0xbe000000, v16\n" \
  "v_add_f32 v11, v11, v15\n" \
  "v_mul_f32 v10, v10, v10\n" \
  "v_fmac_f32 v10, v12, v12\n" \
  "v_fmac_f32 v10, v14, v14\n" \
  "v_fmac_f32 v10, v16, v16\n" \
  "v_fmamk_f32 v11, v10, 0x3e000000, v11\n" \
  "v_mov_b32 v8, v11\n" \
  "v_mov_b32 v10, v6\n" \
  "s_nop 0\n" \
  "v_permlane32_swap_b32 v11, v8\n" \
  "v_add_f32 v11, v11, v8\n" \
  "ds_write_b64 v7, v[10:11]\n" \
  "v_cmp_eq_u32 vcc, 0, v5\n" \
  "s_cmp_eq_u64 vcc, 0\n" \
  "s_cselect_b32 s80, 1, 0\n" \
  "s_branch .LCQ_TOP_%=\n" \
  ".LCQ_END_%=:\n" \
  "s_endpgm\n"
__device__ __forceinline__ void role_qkv(const MegaArgs& A, char* smem, const int L, const int h) {
  const int w = __builtin_amdgcn_readfirstlane(threadIdx.x >> 6);
  char* ws = A.ws;
  unsigned* FL = (unsigned*)(ws + OFF_FL);
  const int l = lane_id();
  const int c = l & 15, g = l >> 4;
  const unsigned sb = lds_base(smem);
  v8u wq[8], wk[8], wv[8];
  constexpr int Q_ST = 131072, Q_PR = Q_ST + 8192, Q_WP = Q_PR + 16384, Q_CN = Q_WP + 4096;
  const int rc = blockIdx.x & 3;
  const char* Xb = ws + (L ? OFF_X3 : OFF_XA);
  const char* Sb = ws + OFF_H + L * (SZ_X / 2);
  {
    const int q = (l >> 2) & 3, p = l & 3, f = q | ((g & 1) << 2);
    const unsigned ra = sb + (8 * g + q) * 256 + (w ^ f) * 32 + p * 8;
#define W_READ(W, hf)                                                          \
  {                                                                           \
    _Pragma("unroll") for (int t = 0; t < 4; ++t) {                           \
      const unsigned a_ = ra + (hf) * 8192 + t * 32768;                       \
      const v2u r0 = trd(a_), r1 = trd(a_ + 1024), r2 = trd(a_ + 16384), r3 = trd(a_ + 16384 + 1024); \
      W[4 * (hf) + t] = v8u{r0.x, r0.y, r1.x, r1.y, r2.x, r2.y, r3.x, r3.y};  \
    }                                                                         \
    _Pragma("unroll") for (int t = 0; t < 4; ++t) asm volatile("" : "+v"(W[4 * (hf) + t])); \
  }
#define W_STAGE(W, hf, WAIT, NEXT)                                             \
  {                                                                           \
    WAIT;                                                                     \
    RAW_BAR();                                                                \
    W_READ(W, hf);                                                            \
    RAW_BAR();                                                                \
    NEXT;                                                                     \
  }
    wqkv_issue<false>(A.Wq, 0, h, w, l, sb);
    wqkv_issue<false>(A.Wq, 1, h, w, l, sb);
    const u64 tag = call_tag(A);
    const v4u one = {0x3f803f80u, 0x3f803f80u, 0x3f803f80u, 0x3f803f80u};
    float* cp = reinterpret_cast<float*>(smem + Q_ST + w * 1024 + 256);
    f32x4 cs = {};
#define HALF(x, i) BF8(((i) ? __builtin_shufflevector(x, x, 4, 5, 6, 7) : __builtin_shufflevector(x, x, 0, 1, 2, 3)))
    if (threadIdx.x < 8) reinterpret_cast<unsigned*>(smem + Q_CN)[threadIdx.x] = 0u;
    W_STAGE(wq, 0, WAIT_VM(8), wqkv_issue<false>(A.Wk, 0, h, w, l, sb));
    SST(4);
    W_STAGE(wq, 1, WAIT_VM(8), wqkv_issue<false>(A.Wk, 1, h, w, l, sb));
    SST(5);
#pragma unroll
    for (int ks = 0; ks < 16; ++ks) cs = MFMA16(HALF(wq[ks >> 1], ks & 1), BF8(one), cs);
    *reinterpret_cast<f32x4*>(cp + 4 * g) = cs;
    W_STAGE(wk, 0, WAIT_VM(8), wqkv_issue<false>(A.Wv, 0, h, w, l, sb));
    W_STAGE(wk, 1, WAIT_VM(8), if (w < 4) {
      wqkv_issue<false>(A.Wv, 1, h, w, l, sb);
      wqkv_issue<false>(A.Wv, 1, h, w + 4, l, sb);
    });
    SST(6);
    cs = f32x4{};
#pragma unroll
    for (int ks = 0; ks < 16; ++ks) cs = MFMA16(HALF(wk[ks >> 1], ks & 1), BF8(one), cs);
    *reinterpret_cast<f32x4*>(cp + 16 + 4 * g) = cs;
    W_STAGE(wv, 0, if (w < 4) WAIT_VM(16); else { ready_wait(A, tag, ready_look(A)); WAIT_VM(0); }, (void)0);
    if (w >= 4 && __all(__hip_atomic_load((gu32*)FL + FL_FC + ((L * 64 * 4 + rc) * 8 + 2 * w - 8 + ((l >> 3) & 1)) * 32 + (l & 7), RLX_AGENT) != 0u)) {
      reinterpret_cast<unsigned*>(smem + Q_CN)[w] = 1u;
#pragma unroll
      for (int i = 0; i < 4; ++i) glds16c(Sb + (4 * w - 16 + i) * 1024, l * 16, sb + Q_PR + (4 * w - 16 + i) * 1024);
#pragma unroll
      for (int i = 0; i < 8; ++i) glds16c(Xb + (8 * w - 32 + i) * 1024, l * 16, sb + (w - 4) * 16384 + i * 1024);
    }
    cs = f32x4{};
#pragma unroll
    for (int ks = 0; ks < 8; ++ks) cs = MFMA16(BF8(one), HALF(wv[ks >> 1], ks & 1), cs);
    asm volatile("" : "+v"(cs));
    SST(7);
    WAIT_VM(0);
    RAW_BAR();
    SST(8);
    W_READ(wv, 1);
#pragma unroll
    for (int ks = 8; ks < 16; ++ks) cs = MFMA16(BF8(one), HALF(wv[ks >> 1], ks & 1), cs);
    cp[32 + c] = cs[0];
#undef HALF
#undef W_STAGE
#undef W_READ
  }
  SST(9);
  const int got = __builtin_amdgcn_readfirstlane(reinterpret_cast<unsigned*>(smem + Q_CN)[4 + (w >> 1)]);
  const v4u rsQ = mk_srd(ws + OFF_QK + L * SZ_QK, SZ_QK);
  const v4u rsV = mk_srd(ws + OFF_VF + L * SZ_O, SZ_O);
  const v4u rsM = mk_srd(ws + (w >= 4 ? OFF_MEMV : OFF_MEMK) + L * SZ_MEM, SZ_MEM);
  const v4u rsF = mk_srd(FL + FL_FC + (L * 64 * 4 + rc) * 8 * 32 + w * 32, (64 * 32 - rc * 8 - w) * 128);
  const v4u rsFQ = mk_srd(FL + FL_FQ + (L * 64 * 32 + h) * 32, 64 * 4096 - h * 128);
  v8u pv;
  pv[0] = sb + 65536 + g * 512 + c * 16;
  pv[1] = (c + 16 * (2 * (w & 1) + (g >> 1))) * 16 + 4096 * (g & 1);
  pv[2] = l * 16;
  pv[3] = sb + Q_ST + w * 1024 + c * 4;
  pv[4] = sb + Q_ST + w * 1024 + g * 16;
  pv[5] = pv[6] = pv[7] = 0u;
  const char* dbg = nullptr;
  asm volatile("s_mov_b64 exec, -1\ns_waitcnt vmcnt(0) lgkmcnt(0)\ns_nop 4\n" Q_ASM
               ::"{v[64:71]}"(wq[0]), "{v[72:79]}"(wq[1]), "{v[80:87]}"(wq[2]), "{v[88:95]}"(wq[3]), "{v[96:103]}"(wq[4]),
               "{v[104:111]}"(wq[5]), "{v[112:119]}"(wq[6]), "{v[120:127]}"(wq[7]), "{v[128:135]}"(wk[0]), "{v[136:143]}"(wk[1]),
               "{v[144:151]}"(wk[2]), "{v[152:159]}"(wk[3]), "{v[160:167]}"(wk[4]), "{v[168:175]}"(wk[5]), "{v[176:183]}"(wk[6]),
               "{v[184:191]}"(wk[7]), "{v[192:199]}"(wv[0]), "{v[200:207]}"(wv[1]), "{v[208:215]}"(wv[2]), "{v[216:223]}"(wv[3]),
               "{v[224:231]}"(wv[4]), "{v[232:239]}"(wv[5]), "{v[240:247]}"(wv[6]), "{v[248:255]}"(wv[7]), "{v[0:7]}"(pv),
               "{s[40:43]}"(rsQ), "{s[44:47]}"(rsV), "{s[48:51]}"(rsM), "{s[52:55]}"(rsF), "{s[56:59]}"(rsFQ), "{s[60:61]}"(Xb + got * 32768 + w * 4096),
               "{s[62:63]}"(Sb + got * 16384 + w * 2048), "{s64}"(NSEG), "{s65}"((h * 16 + (w >> 1)) * 1024), "{s66}"((h * 512 + w * 64) * 16),
               "{s67}"(h * 2048 + (w & 3) * 512), "{s68}"(sb + got * 65536 + (w >> 1) * 16384 + (w & 1) * 4096), "{s69}"(sb + Q_ST + w * 1024), "{s70}"(sb + Q_CN),
               "{s71}"(0), "{s72}"(0), "{s73}"(sb + Q_PR + w * 2048), "{s74}"(sb + Q_WP + 2048), "{s75}"(sb + Q_WP + w * 256), "{s78}"(got), "{s[36:37]}"(dbg)
               : "memory", "vcc", "scc", "s88", "v63");
}

constexpr int MEGA_WGS = 256;
constexpr size_t MEGA_LDS = (size_t)(32 * 4 * 64 * 8 + 8 * SMS + 15 * QS + 128 + 16 * 64 * 8 + 8 * SOS + 512) * 2;
static_assert(MEGA_LDS <= 163840, "lds");
static_assert(MEGA_LDS >= (size_t)AW_CNT + 128, "lds");
static_assert(MEGA_LDS >= (size_t)CV_LDS && 8 * 1568 >= 768 * 16, "lds");
static_assert(MEGA_LDS >= (size_t)2 * 32 * SMS * 2 && MEGA_LDS >= 131072, "lds");
static_assert(MEGA_LDS >= (size_t)8 * 16640, "lds");
__global__ __launch_bounds__(512, 2) void mega_kernel(const u16* mem0, const u16* Dskip, char* ws, u16* out, const u16* x0,
                                                      const u16* logAre, const u16* Aim, const u16* Cre, const u16* Cim,
                                                      const u16* logdt, const u16* Wq, const u16* Wk, const u16* Wv,
                                                      const u16* Wo) {
  extern __shared__ __attribute__((aligned(16))) char smem[];
  const MegaArgs A{mem0, Dskip, ws, out, x0, logAre, Aim, Cre, Cim, logdt, Wq, Wk, Wv, Wo, vbid()};
  const int b = A.vb;
  u64 tag = 0;
  {
    const unsigned t0 = now10();
    if ((b >= 32 && b < 64) || (b >= 200 && b < 232)) start_kgen(A, smem, b < 64 ? b - 32 : b - 168);
    else if ((b >= 64 && b < 104) || b >= 232) tag = start_stats(A, b >= 232 ? b - 192 : b - 64);
    const unsigned dl = (b >= 32 && b < 64) ? DL_C : (b >= 64 && b < 96) ? DL_A : ((b >= 96 && b < 104) || b >= 232) ? DL_R : (b >= 200 && b < 232) ? DL_Q : 0u;
    while (now10() - t0 < dl) __builtin_amdgcn_s_sleep(16);
  }
  if ((EXP_OFF & 1) && b >= 64 && b < 80) return;
  if ((EXP_OFF & 8) && b >= 200 && b < 232) return;
  if ((EXP_OFF & 16) && b >= 32 && b < 64) return;
  if ((EXP_OFF & 32) && b >= 80 && b < 96) return;
  if (b < 64) role_chain(A, smem, b >> 5, b & 31, 0);
  else if (b < 96) role_aw(A, smem, (b >> 4) & 1, b & 15, tag);
  else if (b >= 104 && b < 168) role_conv(A, smem, b - 104);
  else if (b >= 168 && b < 232) role_qkv(A, smem, (b - 168) >> 5, (b - 168) & 31);
  else if (b < 240) role_red(A, smem, 0, b & 15, tag);
  else role_red(A, smem, 1, b - 240, tag);
}

extern "C" void kernel_launch(void* const* d_in, const int* in_sizes, int n_in, void* d_out, int out_size, void* d_ws,
                              size_t ws_size, hipStream_t stream) {
  MegaArgs A;
  A.x0 = (const u16*)d_in[0];
  A.logAre = (const u16*)d_in[1];
  A.Aim = (const u16*)d_in[2];
  A.Cre = (const u16*)d_in[3];
  A.Cim = (const u16*)d_in[4];
  A.logdt = (const u16*)d_in[5];
  A.Dskip = (const u16*)d_in[6];
  A.mem0 = (const u16*)d_in[8];
  A.Wq = (const u16*)d_in[9];
  A.Wk = (const u16*)d_in[10];
  A.Wv = (const u16*)d_in[11];
  A.Wo = (const u16*)d_in[12];
  A.ws = (char*)d_ws;
  A.out = (u16*)d_out;

  (void)hipFuncSetAttribute((const void*)mega_kernel, hipFuncAttributeMaxDynamicSharedMemorySize, (int)MEGA_LDS);
  mega_kernel<<<MEGA_WGS, 512, MEGA_LDS, stream>>>(A.mem0, A.Dskip, A.ws, A.out, A.x0, A.logAre, A.Aim, A.Cre, A.Cim, A.logdt,
                                                   A.Wq, A.Wk, A.Wv, A.Wo);
}
```

```cpp
#include <hip/hip_runtime.h>
#include <hip/hip_bf16.h>
#include <stdint.h>

constexpr int L_SEQ = 2048;
constexpr int DM    = 512;
constexpr int NST   = 128;
constexpr int NH    = 32;
constexpr int DH    = 128;
constexpr int HD    = NH * DH;
constexpr int MM    = 8;
constexpr int SEG   = 32;
constexpr int NSEG  = L_SEQ / SEG;
constexpr int LK    = MM + SEG;

typedef unsigned short u16;
using bf16x8 = __attribute__((ext_vector_type(8))) short;
using f32x4  = __attribute__((ext_vector_type(4))) float;
using f32x16 = __attribute__((ext_vector_type(16))) float;

static __device__ __forceinline__ u16 f2bf(float f) {
  unsigned u = __float_as_uint(f);
  u += 0x7fffu + ((u >> 16) & 1u);
  return (u16)(u >> 16);
}
static __device__ __forceinline__ float bf2f(u16 h) { return __uint_as_float(((unsigned)h) << 16); }
static __device__ __forceinline__ float bfr(float f) { return bf2f(f2bf(f)); }

constexpr size_t SZ_X      = (size_t)L_SEQ * DM * 2;
constexpr size_t OFF_KF    = 0;
constexpr size_t OFF_UT    = OFF_KF + (size_t)DM * L_SEQ * 4;
constexpr size_t OFF_YT    = OFF_UT + SZ_X;
constexpr size_t OFF_XA    = OFF_YT + SZ_X;
constexpr size_t OFF_H     = OFF_XA + SZ_X;
constexpr size_t OFF_X2    = OFF_H + SZ_X;
constexpr size_t OFF_U2    = OFF_X2 + SZ_X;
constexpr size_t OFF_X3    = OFF_U2 + SZ_X;
constexpr size_t SZ_QK     = (size_t)L_SEQ * 2 * HD * 2;
constexpr size_t OFF_QK    = OFF_X3 + SZ_X;
constexpr size_t SZ_O      = (size_t)L_SEQ * HD * 2;
constexpr size_t OFF_VF    = OFF_QK + 2 * SZ_QK;
constexpr size_t SZ_PW     = (size_t)2 * NSEG * 16 * SEG * DM * 2;
constexpr size_t OFF_PW    = OFF_VF + 2 * SZ_O;
constexpr size_t SZ_MEM    = (size_t)NSEG * MM * HD * 2;
constexpr size_t OFF_MEMK  = OFF_PW + SZ_PW;
constexpr size_t OFF_MEMV  = OFF_MEMK + 2 * SZ_MEM;
constexpr size_t OFF_GT    = OFF_MEMV + 2 * SZ_MEM;
constexpr size_t OFF_EP    = OFF_GT + 65536;
constexpr size_t OFF_RD    = OFF_EP + 4096;
constexpr size_t OFF_KG    = OFF_RD + 4096;
constexpr size_t OFF_G     = OFF_KG + 8192;
constexpr size_t SZ_ACC    = (size_t)2 * 32768;
constexpr size_t OFF_Z     = OFF_GT + (size_t)64 * DM * 4;
constexpr size_t OFF_ACC   = OFF_Z;
constexpr size_t OFF_FL    = OFF_ACC + 2 * SZ_ACC;
constexpr int FL_FK  = 0;
constexpr int FL_FW  = FL_FK + 64 * 8 * 32;
constexpr int FL_FR  = FL_FW + 2 * 64 * 16 * 32;
constexpr int FL_FC  = FL_FR + 64 * 16 * 32;
constexpr int FL_FQ  = FL_FC + 2 * 64 * 32 * 32;
constexpr int FL_FZ  = FL_FQ + 2 * 64 * 32 * 32;
constexpr int FL_TMO = FL_FZ + 16 * 32;
constexpr int FL_END = FL_TMO + 64;
constexpr size_t Z_BYTES   = OFF_FL + (size_t)FL_END * 4 - OFF_Z;
constexpr size_t WS_TOTAL  = OFF_Z + Z_BYTES;
static_assert(Z_BYTES % 16 == 0 && OFF_Z % 256 == 0 && Z_BYTES <= 4 * 64 * 8192 && OFF_G + 2048 <= OFF_Z, "zero region");
static_assert(WS_TOTAL + (4 << 20) <= (size_t)256 * 1024 * 1024, "workspace too large");

static_assert(WS_TOTAL - OFF_XA <= (size_t)384 * 8 * 65536, "translation warm-up");
constexpr int CKN = 2208;
static_assert((size_t)DM * 2 * CKN * 2 <= (size_t)DM * L_SEQ * 4 + SZ_X, "K buffer");
__device__ __forceinline__ float pos_of_index(int p) {
  if (p < 256) return (float)p;
  if (p < 384) return (float)(256 + 2 * (p - 256));
  if (p < 512) return (float)(512 + 4 * (p - 384));
  if (p < 640) return (float)(1024 + 8 * (p - 512));
  return 2048.0f;
}
__device__ __forceinline__ int index_of_l(int l) {
  float pf = bfr((float)l);
  int p = (int)pf;
  if (p < 256) return p;
  if (p < 512) return 256 + ((p - 256) >> 1);
  if (p < 1024) return 384 + ((p - 512) >> 2);
  if (p < 2048) return 512 + ((p - 1024) >> 3);
  return 640;
}

template <int CTRL> __device__ __forceinline__ float dppr(float x) {
  return __builtin_bit_cast(float, __builtin_amdgcn_update_dpp(0, __builtin_bit_cast(int, x), CTRL, 0xf, 0xf, false));
}
__device__ __forceinline__ float wave_sum(float v) {
  v += dppr<0x121>(v);
  v += dppr<0x122>(v);
  v += dppr<0x124>(v);
  v += dppr<0x128>(v);
  {
    auto r = __builtin_amdgcn_permlane16_swap(__float_as_uint(v), __float_as_uint(v), false, false);
    v = __uint_as_float(r[0]) + __uint_as_float(r[1]);
  }
  {
    auto r = __builtin_amdgcn_permlane32_swap(__float_as_uint(v), __float_as_uint(v), false, false);
    v = __uint_as_float(r[0]) + __uint_as_float(r[1]);
  }
  return v;
}
__device__ __forceinline__ void ln_row8(float (&v)[8]) {
  float s = 0.f;
#pragma unroll
  for (int j = 0; j < 8; ++j) s += v[j];
  s = wave_sum(s);
  const float mean = s * (1.0f / DM);
  float q = 0.f;
#pragma unroll
  for (int j = 0; j < 8; ++j) { float t = v[j] - mean; q += t * t; }
  q = wave_sum(q);
  const float var = q * (1.0f / DM);
  const float mb = bfr(mean);
  const float sd = bfr(sqrtf(bfr(bfr(var) + 1e-5f)));
#pragma unroll
  for (int j = 0; j < 8; ++j) v[j] = bfr(bfr(v[j] - mb) / sd);
}

#define RLX_AGENT __ATOMIC_RELAXED, __HIP_MEMORY_SCOPE_AGENT
typedef __attribute__((address_space(1))) unsigned gu32;
typedef unsigned v4u __attribute__((ext_vector_type(4)));
constexpr int AUX_SC1 = 16;
constexpr unsigned SPIN_CAP = 1u << 18;

__device__ __forceinline__ bool wait_flags(const unsigned* flags, int lane, int nfl, unsigned want, unsigned* tmo) {
  for (unsigned spins = 0;; ++spins) {
    unsigned v = want;
    if (lane < nfl) v = __hip_atomic_load((gu32*)flags + lane * 32, RLX_AGENT);
    if (__all(v >= want)) break;
    if (spins > SPIN_CAP) {
      if (lane == 0) __hip_atomic_store((gu32*)tmo, 1u, RLX_AGENT);
      return false;
    }
  }
  __builtin_amdgcn_fence(__ATOMIC_ACQUIRE, "wavefront");
  return true;
}

#define STAMP(i)
#define STAMP_DUMP()
#define WSTAMP(i)
template <int CTRL> __device__ __forceinline__ float dppf(float old, float x) {
  return __builtin_bit_cast(float, __builtin_amdgcn_update_dpp(__builtin_bit_cast(int, old), __builtin_bit_cast(int, x), CTRL, 0xf, 0xf, false));
}
__device__ __forceinline__ float wave_max_dpp(float x) {
  const float ninf = -3e38f;
  x = fmaxf(x, dppf<0x111>(ninf, x));
  x = fmaxf(x, dppf<0x112>(ninf, x));
  x = fmaxf(x, dppf<0x114>(ninf, x));
  x = fmaxf(x, dppf<0x118>(ninf, x));
  x = fmaxf(x, dppf<0x142>(ninf, x));
  x = fmaxf(x, dppf<0x143>(ninf, x));
  return __builtin_bit_cast(float, __builtin_amdgcn_readlane(__builtin_bit_cast(int, x), 63));
}
__device__ __forceinline__ float wave_sum_dpp(float x) {
  x += dppf<0x111>(0.f, x);
  x += dppf<0x112>(0.f, x);
  x += dppf<0x114>(0.f, x);
  x += dppf<0x118>(0.f, x);
  x += dppf<0x142>(0.f, x);
  x += dppf<0x143>(0.f, x);
  return __builtin_bit_cast(float, __builtin_amdgcn_readlane(__builtin_bit_cast(int, x), 63));
}
constexpr int SMS = 528;
constexpr int QS = 144;
constexpr int VS = 80;
constexpr int SOS = 144;
#define VAR_NREP 4
constexpr int NREP = VAR_NREP;
__device__ __forceinline__ float xsum16(float x) {
  auto r = __builtin_amdgcn_permlane16_swap(__float_as_uint(x), __float_as_uint(x), false, false);
  return __uint_as_float(r[0]) + __uint_as_float(r[1]);
}
__device__ __forceinline__ float xsum32(float x) {
  auto r = __builtin_amdgcn_permlane32_swap(__float_as_uint(x), __float_as_uint(x), false, false);
  return __uint_as_float(r[0]) + __uint_as_float(r[1]);
}
__device__ __forceinline__ float xmax16(float x) {
  auto r = __builtin_amdgcn_permlane16_swap(__float_as_uint(x), __float_as_uint(x), false, false);
  return fmaxf(__uint_as_float(r[0]), __uint_as_float(r[1]));
}
__device__ __forceinline__ float xmax32(float x) {
  auto r = __builtin_amdgcn_permlane32_swap(__float_as_uint(x), __float_as_uint(x), false, false);
  return fmaxf(__uint_as_float(r[0]), __uint_as_float(r[1]));
}
__device__ __forceinline__ float red_mod4(float x) {
  x += dppf<0x124>(0.f, x);
  x += dppf<0x128>(0.f, x);
  return xsum32(xsum16(x));
}
typedef __bf16 bf16x2_t __attribute__((ext_vector_type(2)));
typedef float f32x2_t __attribute__((ext_vector_type(2)));
__device__ __forceinline__ unsigned pk2(float a, float b) {
  f32x2_t v = {a, b};
  bf16x2_t r = __builtin_convertvector(v, bf16x2_t);
  return __builtin_bit_cast(unsigned, r);
}
__device__ __forceinline__ float one(float x) {
  asm("" : "+v"(x));
  return x;
}
__device__ __forceinline__ unsigned pk2s(float a, float b) { return pk2(one(a), one(b)); }
__device__ __forceinline__ float dot2sq(const unsigned x, const float acc) {
  return __builtin_amdgcn_fdot2_f32_bf16(__builtin_bit_cast(bf16x2_t, x), __builtin_bit_cast(bf16x2_t, x), acc, false);
}
__device__ __forceinline__ float lo16(unsigned u) { return __uint_as_float(u << 16); }
__device__ __forceinline__ float hi16(unsigned u) { return __uint_as_float(u & 0xffff0000u); }
__device__ __forceinline__ void scale_pair(float a, float b, float scale, float& oa, float& ob) {
  unsigned u = pk2(a, b);
  unsigned v = pk2s(lo16(u) * scale, hi16(u) * scale);
  oa = lo16(v);
  ob = hi16(v);
}

#define XMAP 2
__device__ __forceinline__ int vbid() {
  const int b = blockIdx.x, x = b & 7, t = b >> 3;
  if (XMAP == 0) {
    if (b < 64) return (b & 32) + 4 * x + (t & 3);
    if (b < 96) return (b & ~15) + 2 * x + (t & 1);
    if (b >= 104 && b < 168) return 104 + 8 * x + t - 13;
    if (b >= 168 && b < 232) return 168 + ((b - 168) & 32) + 4 * x + ((t - 21) & 3);
    return b;
  }
  if (XMAP == 2) {
    if (x < 4) return t < 16 ? 4 * (t & 8) + 8 * x + (t & 7) : 104 + 16 * x + t - 16;
    const int y = x - 4;
    if (t >= 24) return (t >= 28 ? 240 : y < 2 ? 96 : 224) + 4 * y + (t & 3);
    if (t < 16) return 168 + 4 * (t & 8) + 8 * y + (t & 7);
    return 64 + 4 * (t & 4) + 4 * y + (t & 3);
  }
  if (x < 2) return 2 * (t & 16) + 16 * x + (t & 15);
  const int i = 16 * (x - 2) + (t & 15);
  if (t < 16) {
    const int k = (i * 171) >> 9, r = i - 3 * k;
    return r == 0 ? 168 + k : r == 1 ? 200 + k : 64 + 16 * (k & 1) + (k >> 1);
  }
  const int g = i >> 3, e = i & 7;
  if (g == 3) return 96 + e;
  if (g == 7) return 232 + e;
  if (g == 9) return 240 + e;
  if (g == 11) return 248 + e;
  return 104 + 8 * (g - (g > 3) - (g > 7) - (g > 9)) + e;
}
#define MSTAMP()
#define WSTAMP2()
#define SST(k)
#define TS(k, ss, t)
#define LS(k, ss)
#define LS_DUMP()
struct MegaArgs {
  const u16* mem0;
  const u16* Dskip;
  char* ws;
  u16* out;
  const u16* x0;
  const u16 *logAre, *Aim, *Cre, *Cim, *logdt;
  const u16 *Wq, *Wk, *Wv, *Wo;
  int vb, wv;
};

__device__ __forceinline__ int lane_id() {
  int l;
  asm volatile("v_mbcnt_lo_u32_b32 %0, -1, 0\n\tv_mbcnt_hi_u32_b32 %0, -1, %0" : "=v"(l));
  __builtin_assume(l >= 0 && l < 64);
  return l;
}
__device__ __forceinline__ bool spin_timeout(unsigned spins, unsigned* tmo) {
  if (spins > SPIN_CAP) {
    if (lane_id() == 0) __hip_atomic_store((gu32*)tmo, 1u, RLX_AGENT);
    return true;
  }
  return false;
}
#define ROT_H (4 * (h & 1))
#define PNL 8
#define GSL 12
#define AWDIR 1
#define AWAT 0
#define AWRP 0
#define PEPS 4
#define FGATE 0
#define DL_C 0
#define DL_A 800
#define DL_R 900
#define DL_Q 0
#define KC0 0
#define CPRIO 1
#define P1PRIO 3
#define QLEAD 2
#define POLL_SLEEP 12
#define SLP_C POLL_SLEEP
#define SLP_A POLL_SLEEP
#define SLP_W POLL_SLEEP
#define SLP_R POLL_SLEEP
#define SLP_V POLL_SLEEP
#define SLP_Q POLL_SLEEP
#define SLP_M 4
#define AWPRIO 2
#define SLP_S 8
#define SPOS 4
#define CVPRE 4
#define QPD 3
#define QPRI 2
#define TLBW 1
#define QHV 0
#define QCHK 0
#define LEADN 99
#define GATE_V 170
#define GATE_R 380
#define GATE_N 380
#define AWAHEAD 1
#define GATE_M 150
#define PRI_A 3
#define PRI_WA 2
#define PRI_WB 1
#define AWSKIP 3
#define PRI_0 3
#define AWDEF 0
#define SLP_F 2
#define EXP_OFF 0
#define AWSPEC 1
#define CVTHR 48
#define NR_C 8
#define NR_R 8
#define NR_W 8
#define NR_N 2
#define QAHEAD 0
#define NS0 4
#define SLP_N POLL_SLEEP
template <int SLP, int WPL>
__device__ __forceinline__ bool wait_n(const unsigned* f, int n, unsigned* tmo) {
  const int l = lane_id();
  for (unsigned spins = 0;; ++spins) {
    unsigned v = 1u;
    if (l < n) v = __hip_atomic_load((gu32*)f + (l / WPL) * 32 + l % WPL, RLX_AGENT);
    if (__all(v != 0u)) break;
    if (spin_timeout(spins, tmo)) return false;
    __builtin_amdgcn_s_sleep(SLP);
  }
  asm volatile("" ::: "memory");
  return true;
}
__device__ __forceinline__ unsigned now10() { return (unsigned)__builtin_amdgcn_s_memrealtime(); }
__device__ __forceinline__ void gate(const unsigned t) {
  for (int i = 0; i < 64; ++i) {
    const int d = (int)(t - now10());
    if (d <= 0) break;
    if (d > 100) __builtin_amdgcn_s_sleep(32);
    else if (d > 25) __builtin_amdgcn_s_sleep(8);
    else __builtin_amdgcn_s_sleep(2);
  }
}
template <int SLP, int WPL, int GT>
__device__ __forceinline__ void grp_wait(const bool leader, volatile unsigned* sw, const unsigned val, const unsigned* f, int n,
                                         unsigned* tmo, unsigned& tl) {
  if (leader) {
    const int l = lane_id();
    for (unsigned spins = 0;; ++spins) {
      unsigned v = 1u;
      if (l < n) v = __hip_atomic_load((gu32*)f + (l / WPL) * 32 + l % WPL, RLX_AGENT);
      if (__all(v != 0u) || spin_timeout(spins, tmo)) break;
      if (GT && spins == 0) gate(tl);
      else __builtin_amdgcn_s_sleep(SLP);
    }
    if (GT) tl = now10() + GT;
    if (l == 0) *sw = val;
  } else {
    for (unsigned spins = 0; *sw < val; ++spins) {
      if (spins > 64u * SPIN_CAP) break;
      if (FGATE && GT && spins == 0) gate(tl);
      __builtin_amdgcn_s_sleep(SLP_F);
    }
    if (FGATE && GT) tl = now10() + GT;
  }
  asm volatile("" ::: "memory");
}
template <int SLP, int WPL>
__device__ __forceinline__ bool wg_wait(const unsigned* f, int n, unsigned* tmo) {
  if (threadIdx.x < 64) (void)wait_n<SLP, WPL>(f, n, tmo);
  __syncthreads();
  asm volatile("" ::: "memory");
  return true;
}
__device__ __forceinline__ void set_flag(unsigned* f) { __hip_atomic_store((gu32*)f, 1u, RLX_AGENT); }
#define DRAIN() asm volatile("s_waitcnt vmcnt(0)" ::: "memory")
#define MKRS(ptr, bytes) __builtin_amdgcn_make_buffer_rsrc((void*)(ptr), 0, (int)(bytes), 0x00020000)
typedef unsigned v2u __attribute__((ext_vector_type(2)));
#define LD128(rs, voff, soff) __builtin_amdgcn_raw_buffer_load_b128(rs, voff, soff, AUX_SC1)
#define LD64(rs, voff, soff) __builtin_amdgcn_raw_buffer_load_b64(rs, voff, soff, AUX_SC1)
#define ST128(v, rs, voff, soff)                                              \
  do {                                                                        \
    const v4u st_d_ = (v);                                                    \
    __builtin_amdgcn_raw_buffer_store_b128(st_d_, rs, voff, soff, AUX_SC1);   \
    asm volatile("s_nop 1" ::"v"(st_d_) : "memory");                          \
  } while (0)
#define ST64(v, rs, voff, soff) __builtin_amdgcn_raw_buffer_store_b64(v, rs, voff, soff, AUX_SC1)
#define BF8(x) __builtin_bit_cast(bf16x8, x)
#define MFMA16(a, b, c) __builtin_amdgcn_mfma_f32_16x16x32_bf16(a, b, c, 0, 0, 0)
typedef short bf16x4 __attribute__((ext_vector_type(4)));
#define MFMA16K(a, b, c) \
  __builtin_amdgcn_mfma_f32_16x16x16bf16_1k(__builtin_bit_cast(bf16x4, a), __builtin_bit_cast(bf16x4, b), c, 0, 0, 0)
__device__ __forceinline__ float fmax_nn(float a, float b) { return __builtin_elementwise_maximum(a, b); }
typedef __bf16 bfv8 __attribute__((ext_vector_type(8)));
typedef __bf16 bfv16 __attribute__((ext_vector_type(16)));
typedef unsigned v8u __attribute__((ext_vector_type(8)));
#define SMF(a, b, c, idx) \
  __builtin_amdgcn_smfmac_f32_16x16x64_bf16(__builtin_bit_cast(bfv8, a), __builtin_bit_cast(bfv16, b), c, idx, 0, 0)
__device__ __forceinline__ float swap_add32(float a, float b) {
  auto r = __builtin_amdgcn_permlane32_swap(__float_as_uint(a), __float_as_uint(b), false, false);
  return __uint_as_float(r[0]) + __uint_as_float(r[1]);
}
__device__ __forceinline__ float swap_add16(float a, float b) {
  auto r = __builtin_amdgcn_permlane16_swap(__float_as_uint(a), __float_as_uint(b), false, false);
  return __uint_as_float(r[0]) + __uint_as_float(r[1]);
}
__device__ __forceinline__ v4u smf_fix(const v4u d) {
  auto x = __builtin_amdgcn_permlane16_swap(d.x, d.z, false, false);
  auto y = __builtin_amdgcn_permlane16_swap(d.y, d.w, false, false);
  return v4u{x[0], x[1], y[0], y[1]};
}
__device__ __forceinline__ v8u smf_bfrag(const u16* p) {
  const v2u a = *reinterpret_cast<const v2u*>(p), b = *reinterpret_cast<const v2u*>(p + 8);
  const v2u c = *reinterpret_cast<const v2u*>(p + 32), d = *reinterpret_cast<const v2u*>(p + 40);
  return v8u{a.x, b.x, a.y, b.y, c.x, d.x, c.y, d.y};
}

#define VAR_PPA 0
#define VAR_PPB 0
#define VAR_THR 0
#define VAR_THR2 0
#define CV_LEAD 100
#define VAR_EARLY 1
#define VAR_DRAIN 1
#define VAR_D1 0
#define VAR_D2 0
#define VAR_DR2 1
#define VAR_S1 0
#define VAR_S2 0
typedef __attribute__((address_space(1))) unsigned long long gu64;
typedef __attribute__((address_space(1))) char gch;
__device__ __forceinline__ int cvt_rpi(float x) {
  int r;
  asm("v_cvt_rpi_i32_f32 %0, %1" : "=v"(r) : "v"(x));
  return r;
}
typedef unsigned long long u64;
__device__ __forceinline__ u64 call_tag(const MegaArgs& A) {
  u64 t = *(const __attribute__((address_space(4))) u64*)(A.ws + OFF_EP) * 6364136223846793005ull + 1442695040888963407ull;
  asm volatile("" : "+s"(t)::"memory");
  return t;
}
__device__ __forceinline__ u64 tagged(const u64 tag, const unsigned v) { return (tag & 0xffffffffffffull) | (u64)(v >> 16) << 48; }
__device__ __forceinline__ bool has_tag(const u64 tag, const unsigned lo, const unsigned hi) {
  return lo == (unsigned)tag && (hi & 0xffffu) == ((unsigned)(tag >> 32) & 0xffffu);
}
__device__ __forceinline__ u64 ready_look(const MegaArgs& A) {
  return __hip_atomic_load((gu64*)(A.ws + OFF_RD) + (blockIdx.x & 7) * 64 + lane_id(), RLX_AGENT);
}
__device__ __forceinline__ void ready_wait(const MegaArgs& A, const u64 tag, u64 v) {
  for (unsigned spins = 0; !__all(v == tag) && spins < SPIN_CAP; ++spins) {
    __builtin_amdgcn_s_sleep(8);
    v = ready_look(A);
  }
  asm volatile("" ::: "memory");
}
typedef short v4s __attribute__((ext_vector_type(4)));
typedef __attribute__((address_space(3))) v4s* lp4s;
__device__ __forceinline__ void glds16(const void* sbase, const unsigned voff, const unsigned lds) {
  unsigned keep;
  asm volatile("s_nop 4\n\ts_mov_b32 %0, m0\n\ts_mov_b32 m0, %3\n\ts_nop 0\n\tglobal_load_lds_dwordx4 %1, %2\n\ts_mov_b32 m0, %0"
               : "=&s"(keep)
               : "v"(voff), "s"(sbase), "s"(lds)
               : "memory");
}
__device__ __forceinline__ void glds16c(const void* sbase, const unsigned voff, const unsigned lds) {
  unsigned keep;
  asm volatile("s_nop 4\n\ts_mov_b32 %0, m0\n\ts_mov_b32 m0, %3\n\ts_nop 0\n\tglobal_load_lds_dwordx4 %1, %2 sc1\n\ts_mov_b32 m0, %0"
               : "=&s"(keep)
               : "v"(voff), "s"(sbase), "s"(lds)
               : "memory");
}
__device__ __forceinline__ v2u trd(const unsigned a) {
  return __builtin_bit_cast(v2u, __builtin_amdgcn_ds_read_tr16_b64_v4i16((lp4s)(unsigned long)a));
}
__device__ __forceinline__ unsigned lds_base(char* smem) { return (unsigned)(unsigned long)(__attribute__((address_space(3))) char*)smem; }
#define RAW_BAR()                                           \
  do {                                                      \
    asm volatile("s_waitcnt lgkmcnt(0)" ::: "memory");      \
    __builtin_amdgcn_s_barrier();                           \
    asm volatile("" ::: "memory");                          \
  } while (0)
#define WAIT_VM(n) asm volatile("s_waitcnt vmcnt(" #n ")" ::: "memory")
template <bool SP>
__device__ __forceinline__ void wqkv_issue(const u16* Wm, const int hf, const int h, const int w, const int l, const unsigned sb) {
  const char* base = (const char*)Wm + (size_t)(256 * hf + 32 * w) * (HD * 2) + h * (DH * 2);
#pragma unroll
  for (int i = 0; i < 8; ++i) {
    const int r = l >> 4;
    const int f = SP ? ((r & 1) | ((i & 1) << 1) | (((i >> 1) & 1) << 2)) : (r | (((i >> 1) & 1) << 2));
    glds16(base, (unsigned)((4 * i + r) * (HD * 2) + (((l & 15) ^ (f << 1)) << 4)), sb + w * 16384 + hf * 8192 + i * 1024);
  }
}

__device__ __forceinline__ unsigned row_stats(const v4u raw) {
  float v[8];
#pragma unroll
  for (int e = 0; e < 4; ++e) { v[2 * e] = lo16(raw[e]); v[2 * e + 1] = hi16(raw[e]); }
  float s = 0.f;
#pragma unroll
  for (int e = 0; e < 8; ++e) s += v[e];
  const float mean = wave_sum(s) * (1.0f / DM);
  float q = 0.f;
#pragma unroll
  for (int e = 0; e < 8; ++e) { const float t = v[e] - mean; q += t * t; }
  const float var = wave_sum(q) * (1.0f / DM);
  const float sd = bfr(sqrtf(bfr(bfr(var) + 1e-5f)));
  return (unsigned)f2bf(mean) | ((unsigned)f2bf(sd) << 16);
}
__device__ __forceinline__ void ask_slow(const MegaArgs& A, const int k, unsigned (&t)[3]) {
  const int l = lane_id(), x = blockIdx.x & 7;
  const char *wq = (const char*)A.Wq, *wv = (const char*)A.Wv;
  asm("" : "+s"(wq), "+s"(wv));
#pragma unroll
  for (int m = 0; m < 3; ++m) {
    const char* W = m == 1 ? (const char*)A.Wk : ((m == 0) == (x < 4)) ? wv : wq;
    const int a = (int)((unsigned long)W >> 8) + 8 * (x & 3), h = k < 32 ? ((1 - a) & 3) + 4 * (k & 1) : (31 - a) & 31;
    const int r = k < 32 ? k >> 1 : k - 32;
    t[m] = __builtin_amdgcn_raw_buffer_load_b32(MKRS(W, (unsigned)k < 48u && h < 8 ? DM * HD * 2 : 0), (l >> 1) * (HD * 2) + (l & 1) * 128,
                                                (32 * (r & 15)) * (HD * 2) + (8 * (x & 3) + (h & 7)) * 256, 0);
  }
}
#define ASKED(t) asm volatile("" ::"v"(t[0]), "v"(t[1]), "v"(t[2]))
__device__ __forceinline__ u64 start_stats(const MegaArgs& A, const int idx) {
  const int w = A.wv, l = lane_id();
  const int r0 = 32 * idx + 4 * w;
  unsigned ta[3];
  v4u raw[4];
#pragma unroll
  for (int i = 0; i < 4; ++i) raw[i] = *reinterpret_cast<const v4u*>(A.x0 + (size_t)(r0 + i) * DM + l * 8);
  asm volatile("" ::: "memory");
  ask_slow(A, 16 * w + (idx & 3) + (idx >> 4) * 4, ta);
  const u64 tag = call_tag(A);
  unsigned stw = 0u;
#pragma unroll
  for (int i = 0; i < 4; ++i) {
    const unsigned t = row_stats(raw[i]);
    if ((l >> 1) == i) stw = t;
  }
  if (l < 8) __hip_atomic_store((gu64*)(A.ws + OFF_GT) + 2 * r0 + l, tagged(tag, (l & 1) ? stw : stw << 16), RLX_AGENT);
  SST(0);
  ASKED(ta);
  return tag;
}
struct Cx {
  f32x2_t r, i;
};
__device__ __forceinline__ Cx cmul(const Cx a, const Cx b) { return Cx{a.r * b.r - a.i * b.i, a.r * b.i + a.i * b.r}; }
struct KGen {
  unsigned pa, pb, pc, pd;
  u16 pe;
  f32x2_t ar, ai;
  Cx C;
  char* st;
  v4u bh[8], bl[8];
  __device__ __forceinline__ void load(const MegaArgs& A, const int d, const int l) {
    pa = *reinterpret_cast<const unsigned*>(A.logAre + d * NST + 2 * l);
    pb = *reinterpret_cast<const unsigned*>(A.Aim + d * NST + 2 * l);
    pc = *reinterpret_cast<const unsigned*>(A.Cre + d * NST + 2 * l);
    pd = *reinterpret_cast<const unsigned*>(A.Cim + d * NST + 2 * l);
    pe = A.logdt[d];
  }
  __device__ __forceinline__ void setup() {
    const float dt = bfr(expf(bf2f(pe)));
    ar = f32x2_t{-bfr(expf(lo16(pa))) * dt, -bfr(expf(hi16(pa))) * dt};
    ai = f32x2_t{lo16(pb) * dt, hi16(pb) * dt};
    C = Cx{f32x2_t{lo16(pc) * dt, hi16(pc) * dt}, f32x2_t{lo16(pd) * dt, hi16(pd) * dt}};
  }
  __device__ __forceinline__ Cx power(const float p) const {
    Cx z;
#pragma unroll
    for (int e = 0; e < 2; ++e) {
      const float x = ai[e] * p;
      const float f = __builtin_fmaf(x, 6.4206382e-9f, __builtin_fmaf(x, 0.15915494f, -__builtin_rintf(x * 0.15915494f)));
      const float m = __expf(ar[e] * p);
      z.r[e] = m * __builtin_amdgcn_cosf(f);
      z.i[e] = m * __builtin_amdgcn_sinf(f);
    }
    return z;
  }
  __device__ __forceinline__ char* rows(char* p, Cx y, const Cx s, const int n) const {
#pragma unroll 1
    for (int i = 0; i < n; i += 2) {
#pragma unroll
      for (int k = 0; k < 2; ++k) {
        const unsigned h0 = pk2(y.r.x, y.r.y), h1 = pk2(y.i.x, y.i.y);
        const f32x2_t a = y.r - f32x2_t{lo16(h0), hi16(h0)}, b = y.i - f32x2_t{lo16(h1), hi16(h1)};
        *reinterpret_cast<v2u*>(p + 1040 * k) = v2u{h0, h1};
        *reinterpret_cast<v2u*>(p + 1040 * k + 512) = v2u{pk2(a.x, a.y), pk2(b.x, b.y)};
        y = cmul(y, s);
      }
      p += 2080;
    }
    return p;
  }
  __device__ __forceinline__ void get(v4u (&h)[8], v4u (&o)[8], const int l) const {
    const char* p = st + (l & 15) * 1040 + (l >> 4) * 16;
    asm volatile("" ::: "memory");
#pragma unroll
    for (int k = 0; k < 8; ++k) {
      h[k] = *reinterpret_cast<const v4u*>(p + 64 * k);
      o[k] = *reinterpret_cast<const v4u*>(p + 512 + 64 * k);
    }
    asm volatile("" ::: "memory");
  }
  __device__ __forceinline__ void columns(const int l) {
    Cx s = power(16.f);
    s.i = -s.i;
    rows(st + l * 8, Cx{f32x2_t{1.f, 1.f}, f32x2_t{0.f, 0.f}}, s, 16);
    get(bh, bl, l);
  }
  __device__ __forceinline__ v4u tile(const int l) const {
    v4u h[8], o[8];
    get(h, o, l);
    f32x4 d = {}, e = {}, f = {};
#pragma unroll
    for (int k = 0; k < 8; ++k) {
      d = MFMA16(BF8(h[k]), BF8(bh[k]), d);
      e = MFMA16(BF8(o[k]), BF8(bh[k]), e);
      f = MFMA16(BF8(h[k]), BF8(bl[k]), f);
    }
    d = (d + (e + f)) * 2.0f;
    v4u r;
#pragma unroll
    for (int i = 0; i < 4; ++i) r[i] = pk2(d[i], d[i] - lo16(pk2(d[i], d[i])));
    return r;
  }
};
__device__ __forceinline__ void start_kgen(const MegaArgs& A, char* smem, const int kg) {
  const int w = A.wv, l = lane_id(), tid = 64 * w + l, c = l & 15, g = l >> 4;
  KGen G;
  G.load(A, 8 * kg + w, l);
  {
    const __amdgpu_buffer_rsrc_t rz = MKRS(A.ws + OFF_Z, Z_BYTES);
#pragma unroll
    for (int i = 0; i < 4; ++i) ST128((v4u{0u, 0u, 0u, 0u}), rz, tid * 16, (i * 64 + kg) * 8192);
  }
  const u64 tag = call_tag(A);
  DRAIN();
  __syncthreads();
  if (tid < 8) __hip_atomic_store((gu64*)(A.ws + OFF_RD) + tid * 64 + kg, tag, RLX_AGENT);
  asm volatile("" : "+v"(G.pa), "+v"(G.pb), "+v"(G.pe));
  G.setup();
  G.st = smem + w * 16640;
  const __amdgpu_buffer_rsrc_t rs = MKRS(A.ws + OFF_KF + (8 * kg + w) * 2048, 386 * 4);
  SST(1);
  unsigned tw = 0u;
  if (TLBW && tid < 384) {
    const unsigned o = (unsigned)OFF_XA + (unsigned)(tid * 8 + ((blockIdx.x >> 3) & 7)) * 65536u;
    if (o < WS_TOTAL) asm volatile("global_load_dword %0, %1, %2" : "=v"(tw) : "v"(o), "s"(A.ws) : "memory");
  }
  G.columns(l);
  const Cx zb = G.power(256.f);
  Cx b = G.C, z = G.power(2.f);
#pragma unroll 1
  for (int f = 1; f < 9;) {
    char* p = G.st + l * 8;
    const int fe = f == 1 ? 4 : 9;
#pragma unroll 1
    for (; f < fe; ++f) {
      b = cmul(b, zb);
      if (f == 2 || f == 4) z = cmul(z, z);
      p = G.rows(p, b, z, f == 1 ? 8 : f < 4 ? 4 : 2);
    }
    const v4u r = G.tile(l);
    if (f == 4) {
      ST128(r, rs, (g < 2 ? 8 * c + 4 * g : 64 * g + 4 * c) * 4, 0);
    } else {
      ST64((v2u{r.x, r.y}), rs, (256 + 64 * g + 2 * c) * 4, 0);
      ST64((v2u{r.z, r.w}), rs, (288 + 64 * g + 2 * c) * 4, 0);
    }
  }
  SST(2);
  DRAIN();
  asm volatile("" ::"v"(tw));
  if (l == 0) __hip_atomic_store((gu64*)(A.ws + OFF_KG) + kg * 16 + w, tag, RLX_AGENT);
  __syncthreads();
  SST(3);
}

constexpr int C_SM = 131072, C_SO = C_SM + 8192, C_KT = C_SO + 2048, C_X = C_KT + 16384, C_SQ = C_X + 1024;
constexpr int C_SK = C_SQ + (7 * QS + 128) * 2;
static_assert(C_SK + 8 * QS * 2 <= 163840, "lds");
#define TDSLOT 0
#define CHAIN_ASM_ATT \
  "s_mov_b32 s92, 1152\n" \
  "s_mov_b32 s93, 576\n" \
  "s_mov_b32 s94, 2272\n" \
  "s_mov_b32 s95, 272\n" \
  "s_mov_b32 s96, 288\n" \
  "s_mov_b32 s91, 0x40000001\n" \
  "s_mov_b32 s84, 0x4b400000\n" \
  "s_mov_b32 s85, 0x4b400000\n" \
  "s_mov_b32 s78, 0x3e0293ee\n" \
  "s_mov_b32 s76, 0xff00ff00\n" \
  "s_mov_b32 s77, 0xff00ff00\n" \
  "s_add_u32 s58, s56, s60\n" \
  "s_addc_u32 s59, s57, 0\n" \
  "s_lshl_b32 s98, s73, 10\n" \
  "s_add_u32 s98, s98, s74\n" \
  "s_add_u32 s98, s98, 131072\n" \
  "s_lshl_b32 s97, s73, 5\n" \
  "s_add_u32 s97, s97, s74\n" \
  "s_add_u32 s97, s97, 158720\n" \
  "s_lshl_b32 s99, s73, 10\n" \
  "v_mbcnt_lo_u32_b32 v48, -1, 0\n" \
  "v_mbcnt_hi_u32_b32 v48, -1, v48\n" \
  "s_add_u32 s88, s74, 157760\n" \
  "v_mov_b32 v59, s88\n" \
  "v_and_b32 v49, 15, v48\n" \
  "v_lshl_add_u32 v8, v49, 1, s97\n" \
  "v_bfe_u32 v49, v48, 4, 1\n" \
  "v_mad_u32_u24 v8, v49, s92, v8\n" \
  "v_lshrrev_b32 v49, 5, v48\n" \
  "v_mad_u32_u24 v9, v49, s93, v8\n" \
  "v_mad_u32_u24 v8, v49, s94, v8\n" \
  "v_add_u32 v10, s99, v1\n" \
  "s_lshr_b32 s88, s99, 2\n" \
  "s_add_u32 s88, s88, s74\n" \
  "s_add_u32 s88, s88, 139264\n" \
  "v_lshl_add_u32 v7, v49, 10, s88\n" \
  "v_and_b32 v49, 31, v48\n" \
  "v_lshl_add_u32 v7, v49, 3, v7\n" \
  "v_mov_b64 v[12:13], 0\n" \
  "v_mov_b64 v[14:15], 0\n" \
  "v_mov_b64 v[16:17], 0\n" \
  "v_mov_b64 v[18:19], 0\n" \
  "v_mov_b64 v[20:21], 0\n" \
  "v_mov_b64 v[22:23], 0\n" \
  "v_mov_b32 v24, 0x44870000\n" \
  "v_mul_f32 v24, s72, v24\n" \
  "s_nop 0\n" \
  "v_readfirstlane_b32 s86, v24\n" \
  "s_lshr_b32 s86, s86, 23\n" \
  "s_addk_i32 s86, 0xff83\n" \
  "s_ashr_i32 s86, s86, 1\n" \
  "s_max_i32 s86, s86, -20\n" \
  "s_min_i32 s86, s86, 100\n" \
  "s_mov_b32 s82, s86\n" \
  "s_lshr_b32 s83, s72, 23\n" \
  "s_sub_i32 s83, s83, 242\n" \
  "s_lshl_b32 s88, s73, 10\n" \
  "v_add_u32 v24, s88, v1\n" \
  "ds_read_b64 v[32:33], v24 offset:0\n" \
  "ds_read_b64 v[36:37], v24 offset:8\n" \
  "s_waitcnt lgkmcnt(0)\n" \
  "v_mov_b32 v11, 0\n" \
  "v_dot2c_f32_bf16 v11, v32, v32\n" \
  "v_dot2c_f32_bf16 v11, v33, v33\n" \
  "v_dot2c_f32_bf16 v11, v36, v36\n" \
  "v_dot2c_f32_bf16 v11, v37, v37\n" \
  "s_nop 1\n" \
  "s_nop 0\n" \
  "v_cmpx_lt_f32 vcc, 2.0, v11\n" \
  "ds_max_u32 v59, v11\n" \
  "s_mov_b64 exec, -1\n" \
  ".LCA_TOP_%=:\n" \
  "s_waitcnt lgkmcnt(0)\n" \
  "s_barrier\n" \
  "ds_read_b128 v[40:43], v1 offset:0\n" \
  "ds_read_b128 v[44:47], v1 offset:1024\n" \
  "ds_read_b128 v[48:51], v1 offset:2048\n" \
  "ds_read_b128 v[52:55], v1 offset:3072\n" \
  "ds_read_b128 v[56:59], v1 offset:4096\n" \
  "ds_read_b128 v[60:63], v1 offset:5120\n" \
  "s_cmp_lg_u32 s73, 0\n" \
  "s_cbranch_scc1 .LCA_NOZ_%=\n" \
  "s_xor_b32 s88, s60, 0x8000\n" \
  "s_add_u32 s88, s88, s61\n" \
  "buffer_store_dwordx2 v[12:13], v5, s[40:43], s88 offen sc1\n" \
  ".LCA_NOZ_%=:\n" \
  "s_waitcnt lgkmcnt(5)\n" \
  "v_smfmac_f32_16x16x64_bf16 v[20:23], v[40:43], v[192:199], v0\n" \
  "v_smfmac_f32_16x16x64_bf16 v[12:15], v[40:43], v[64:71], v0\n" \
  "v_smfmac_f32_16x16x64_bf16 v[16:19], v[40:43], v[128:135], v0\n" \
  "ds_read_b128 v[40:43], v1 offset:6144\n" \
  "s_waitcnt lgkmcnt(5)\n" \
  "v_smfmac_f32_16x16x64_bf16 v[20:23], v[44:47], v[200:207], v0\n" \
  "v_smfmac_f32_16x16x64_bf16 v[12:15], v[44:47], v[72:79], v0\n" \
  "v_smfmac_f32_16x16x64_bf16 v[16:19], v[44:47], v[136:143], v0\n" \
  "ds_read_b128 v[44:47], v1 offset:7168\n" \
  "s_waitcnt lgkmcnt(5)\n" \
  "v_smfmac_f32_16x16x64_bf16 v[20:23], v[48:51], v[208:215], v0\n" \
  "v_smfmac_f32_16x16x64_bf16 v[12:15], v[48:51], v[80:87], v0\n" \
  "v_smfmac_f32_16x16x64_bf16 v[16:19], v[48:51], v[144:151], v0\n" \
  "ds_read_b128 v[24:27], v1 offset:10240\n" \
  "ds_read_b128 v[28:31], v1 offset:14336\n" \
  "s_waitcnt lgkmcnt(6)\n" \
  "v_smfmac_f32_16x16x64_bf16 v[20:23], v[52:55], v[216:223], v0\n" \
  "v_smfmac_f32_16x16x64_bf16 v[12:15], v[52:55], v[88:95], v0\n" \
  "v_smfmac_f32_16x16x64_bf16 v[16:19], v[52:55], v[152:159], v0\n" \
  "ds_read_b128 v[32:35], v1 offset:11264\n" \
  "ds_read_b128 v[36:39], v1 offset:15360\n" \
  "s_waitcnt lgkmcnt(7)\n" \
  "v_smfmac_f32_16x16x64_bf16 v[20:23], v[56:59], v[224:231], v0\n" \
  "v_smfmac_f32_16x16x64_bf16 v[12:15], v[56:59], v[96:103], v0\n" \
  "v_smfmac_f32_16x16x64_bf16 v[16:19], v[56:59], v[160:167], v0\n" \
  "s_waitcnt lgkmcnt(6)\n" \
  "v_smfmac_f32_16x16x64_bf16 v[20:23], v[60:63], v[232:239], v0\n" \
  "v_smfmac_f32_16x16x64_bf16 v[12:15], v[60:63], v[104:111], v0\n" \
  "v_smfmac_f32_16x16x64_bf16 v[16:19], v[60:63], v[168:175], v0\n" \
  "s_waitcnt lgkmcnt(5)\n" \
  "v_smfmac_f32_16x16x64_bf16 v[20:23], v[40:43], v[240:247], v0\n" \
  "v_smfmac_f32_16x16x64_bf16 v[12:15], v[40:43], v[112:119], v0\n" \
  "v_smfmac_f32_16x16x64_bf16 v[16:19], v[40:43], v[176:183], v0\n" \
  "s_waitcnt lgkmcnt(4)\n" \
  "v_smfmac_f32_16x16x64_bf16 v[20:23], v[44:47], v[248:255], v0\n" \
  "v_smfmac_f32_16x16x64_bf16 v[12:15], v[44:47], v[120:127], v0\n" \
  "v_smfmac_f32_16x16x64_bf16 v[16:19], v[44:47], v[184:191], v0\n" \
  "s_cmp_eq_u32 s71, 0\n" \
  "s_nop 4\n" \
  "v_permlane32_swap_b32 v20, v22\n" \
  "v_permlane32_swap_b32 v21, v23\n" \
  "v_pk_add_f32 v[20:21], v[20:21], v[22:23]\n" \
  "v_permlane32_swap_b32 v12, v16\n" \
  "v_permlane32_swap_b32 v13, v17\n" \
  "v_cvt_pk_bf16_f32 v20, v20, v21\n" \
  "v_pk_add_f32 v[12:13], v[12:13], v[16:17]\n" \
  "v_permlane32_swap_b32 v14, v18\n" \
  "v_permlane32_swap_b32 v15, v19\n" \
  "ds_write_b16 v9, v20 offset:128\n" \
  "v_cvt_pk_bf16_f32 v12, v12, v13\n" \
  "v_pk_add_f32 v[14:15], v[14:15], v[18:19]\n" \
  "ds_write_b16_d16_hi v9, v20 offset:416\n" \
  "ds_write_b16 v8, v12\n" \
  "v_cvt_pk_bf16_f32 v14, v14, v15\n" \
  "ds_write_b16_d16_hi v8, v12 offset:288\n" \
  "ds_write_b16 v8, v14 offset:576\n" \
  "ds_write_b16_d16_hi v8, v14 offset:864\n" \
  "s_setprio 3\n" \
  "s_waitcnt lgkmcnt(0)\n" \
  "s_barrier\n" \
  "s_cbranch_scc1 .LCA_LAST_%=\n" \
  "ds_read_b32 v11, v1 offset:26688\n" \
  "ds_read_b128 v[40:43], v2 offset:0\n" \
  "ds_read_b128 v[44:47], v3 offset:0\n" \
  "ds_read_b128 v[48:51], v2 offset:64\n" \
  "ds_read_b128 v[52:55], v3 offset:64\n" \
  "ds_read_b128 v[56:59], v2 offset:128\n" \
  "ds_read_b128 v[60:63], v3 offset:128\n" \
  "s_waitcnt lgkmcnt(5)\n" \
  "v_mfma_f32_16x16x32_bf16 v[12:15], v[24:27], v[40:43], 0\n" \
  "v_mfma_f32_16x16x32_bf16 v[16:19], v[28:31], v[40:43], 0\n" \
  "v_readfirstlane_b32 s86, v11\n" \
  "ds_read_b128 v[24:27], v1 offset:12288\n" \
  "ds_read_b128 v[28:31], v1 offset:16384\n" \
  "s_waitcnt lgkmcnt(6)\n" \
  "v_mfma_f32_16x16x32_bf16 v[20:23], v[44:47], v[40:43], 0\n" \
  "s_lshr_b32 s86, s86, 23\n" \
  "s_add_i32 s86, s86, s83\n" \
  "ds_read_b128 v[40:43], v2 offset:192\n" \
  "ds_read_b128 v[44:47], v3 offset:192\n" \
  "s_waitcnt lgkmcnt(7)\n" \
  "v_mfma_f32_16x16x32_bf16 v[12:15], v[32:35], v[48:51], v[12:15]\n" \
  "v_mfma_f32_16x16x32_bf16 v[16:19], v[36:39], v[48:51], v[16:19]\n" \
  "s_ashr_i32 s86, s86, 1\n" \
  "s_max_i32 s86, s86, s82\n" \
  "ds_read_b128 v[32:35], v1 offset:13312\n" \
  "ds_read_b128 v[36:39], v1 offset:17408\n" \
  "s_waitcnt lgkmcnt(8)\n" \
  "v_mfma_f32_16x16x32_bf16 v[20:23], v[52:55], v[48:51], v[20:23]\n" \
  "s_min_i32 s86, s86, 100\n" \
  "s_lshl_b32 s86, s86, 23\n" \
  "s_sub_i32 s84, 0x4a800000, s86\n" \
  "ds_read_b128 v[48:51], v10 offset:18432\n" \
  "ds_read_b128 v[52:55], v10 offset:22528\n" \
  "s_waitcnt lgkmcnt(8)\n" \
  "v_mfma_f32_16x16x32_bf16 v[20:23], v[60:63], v[56:59], v[20:23]\n" \
  "s_waitcnt lgkmcnt(6)\n" \
  "v_mfma_f32_16x16x32_bf16 v[12:15], v[24:27], v[56:59], v[12:15]\n" \
  "v_mfma_f32_16x16x32_bf16 v[16:19], v[28:31], v[56:59], v[16:19]\n" \
  "ds_read_b128 v[56:59], v6 offset:0\n" \
  "ds_read_b128 v[60:63], v6 offset:1024\n" \
  "s_waitcnt lgkmcnt(6)\n" \
  "v_mfma_f32_16x16x32_bf16 v[20:23], v[44:47], v[40:43], v[20:23]\n" \
  "ds_read_b128 v[24:27], v6 offset:2048\n" \
  "ds_read_b128 v[28:31], v6 offset:3072\n" \
  "s_waitcnt lgkmcnt(6)\n" \
  "v_mfma_f32_16x16x32_bf16 v[12:15], v[32:35], v[40:43], v[12:15]\n" \
  "v_mfma_f32_16x16x32_bf16 v[16:19], v[36:39], v[40:43], v[16:19]\n" \
  "ds_read_b128 v[32:35], v6 offset:8192\n" \
  "ds_read_b128 v[36:39], v6 offset:9216\n" \
  "s_waitcnt lgkmcnt(6)\n" \
  "v_cndmask_b32 v20, v20, v21, s[76:77]\n" \
  "v_mov_b32 v41, 0\n" \
  "v_mov_b32 v42, 0\n" \
  "s_nop 1\n" \
  "v_cndmask_b32 v12, v12, v16, s[76:77]\n" \
  "v_cndmask_b32 v13, v13, v17, s[76:77]\n" \
  "v_cndmask_b32 v14, v14, v18, s[76:77]\n" \
  "v_cndmask_b32 v15, v15, v19, s[76:77]\n" \
  "v_maximum3_f32 v16, v20, v12, v13\n" \
  "v_maximum3_f32 v16, v16, v14, v15\n" \
  "s_nop 1\n" \
  "v_max_f32_dpp v16, v16, v16 row_ror:8 row_mask:0xf bank_mask:0xf\n" \
  "v_mov_b32 v17, v16\n" \
  "s_nop 1\n" \
  "v_permlane16_swap_b32 v16, v17\n" \
  "v_max_f32 v16, v16, v17\n" \
  "v_mov_b32 v17, v16\n" \
  "s_nop 1\n" \
  "v_permlane32_swap_b32 v16, v17\n" \
  "v_max_f32 v16, v16, v17\n" \
  "v_mul_f32 v16, s78, v16\n" \
  "v_pk_fma_f32 v[12:13], v[12:13], s[78:79], v[16:17] op_sel_hi:[1,0,0] neg_lo:[0,0,1] neg_hi:[0,0,1]\n" \
  "v_pk_fma_f32 v[14:15], v[14:15], s[78:79], v[16:17] op_sel_hi:[1,0,0] neg_lo:[0,0,1] neg_hi:[0,0,1]\n" \
  "v_fma_f32 v20, v20, s78, -v16\n" \
  "v_exp_f32 v12, v12\n" \
  "v_exp_f32 v13, v13\n" \
  "v_exp_f32 v14, v14\n" \
  "v_exp_f32 v15, v15\n" \
  "v_exp_f32 v20, v20\n" \
  "s_barrier\n" \
  "ds_read_b64 v[22:23], v7 offset:0\n" \
  "v_cvt_pk_bf16_f32 v44, v12, v13\n" \
  "v_cvt_pk_bf16_f32 v45, v14, v15\n" \
  "v_pk_add_f32 v[8:9], v[12:13], v[14:15]\n" \
  "v_mov_b32_dpp v46, v44 row_ror:8 row_mask:0xf bank_mask:0xf\n" \
  "v_mov_b32_dpp v17, v20 row_ror:8 row_mask:0xf bank_mask:0xf\n" \
  "v_mov_b32_dpp v47, v45 row_ror:8 row_mask:0xf bank_mask:0xf\n" \
  "v_add_f32 v10, v8, v20\n" \
  "v_cvt_pk_bf16_f32 v40, v20, v17\n" \
  "v_add_f32 v10, v10, v9\n" \
  "v_mfma_f32_16x16x32_bf16 v[12:15], v[48:51], v[44:47], 0\n" \
  "v_cvt_pk_bf16_f32 v43, v20, v17\n" \
  "v_add_f32_dpp v10, v10, v10 row_ror:8 row_mask:0xf bank_mask:0xf\n" \
  "v_mfma_f32_16x16x32_bf16 v[16:19], v[52:55], v[44:47], 0\n" \
  "v_mov_b32 v11, v10\n" \
  "s_waitcnt lgkmcnt(0)\n" \
  "v_mfma_f32_16x16x16_bf16 v[12:15], v[22:23], v[40:41], v[12:15]\n" \
  "v_mfma_f32_16x16x16_bf16 v[16:19], v[22:23], v[42:43], v[16:19]\n" \
  "v_permlane16_swap_b32 v10, v11\n" \
  "v_add_f32 v10, v10, v11\n" \
  "ds_read_b128 v[48:51], v6 offset:10240\n" \
  "v_mov_b32 v11, v10\n" \
  "ds_read_b128 v[52:55], v6 offset:11264\n" \
  "v_mov_b64 v[20:21], 0\n" \
  "v_mov_b64 v[22:23], 0\n" \
  "v_permlane32_swap_b32 v10, v11\n" \
  "v_add_f32 v10, v10, v11\n" \
  "v_rcp_f32 v10, v10\n" \
  "s_nop 0\n" \
  "v_pk_mul_f32 v[12:13], v[12:13], v[10:11] op_sel_hi:[1,0]\n" \
  "v_pk_mul_f32 v[14:15], v[14:15], v[10:11] op_sel_hi:[1,0]\n" \
  "v_pk_mul_f32 v[16:17], v[16:17], v[10:11] op_sel_hi:[1,0]\n" \
  "v_pk_mul_f32 v[18:19], v[18:19], v[10:11] op_sel_hi:[1,0]\n" \
  "v_cvt_pk_bf16_f32 v40, v12, v13\n" \
  "v_cvt_pk_bf16_f32 v41, v14, v15\n" \
  "v_cvt_pk_bf16_f32 v42, v16, v17\n" \
  "ds_write_b64 v4, v[40:41]\n" \
  "v_cvt_pk_bf16_f32 v43, v18, v19\n" \
  "ds_write_b64 v4, v[42:43] offset:1024\n" \
  "v_mov_b64 v[12:13], 0\n" \
  "v_mov_b64 v[14:15], 0\n" \
  "v_mov_b64 v[16:17], 0\n" \
  "v_mov_b64 v[18:19], 0\n" \
  "v_mov_b64 v[8:9], 0\n" \
  "v_mov_b64 v[10:11], 0\n" \
  "s_setprio 0\n" \
  "s_waitcnt lgkmcnt(0)\n" \
  "s_barrier\n" \
  "ds_read_b128 v[40:43], v1 offset:8192\n" \
  "ds_read_b128 v[44:47], v1 offset:9216\n" \
  "s_waitcnt lgkmcnt(1)\n" \
  "v_smfmac_f32_16x16x64_bf16 v[12:15], v[40:43], v[56:63], v0\n" \
  "ds_read_b128 v[56:59], v6 offset:4096\n" \
  "ds_read_b128 v[60:63], v6 offset:5120\n" \
  "v_smfmac_f32_16x16x64_bf16 v[16:19], v[40:43], v[24:31], v0\n" \
  "ds_read_b128 v[24:27], v6 offset:6144\n" \
  "ds_read_b128 v[28:31], v6 offset:7168\n" \
  "s_waitcnt lgkmcnt(4)\n" \
  "v_smfmac_f32_16x16x64_bf16 v[12:15], v[44:47], v[32:39], v0\n" \
  "ds_read_b128 v[32:35], v6 offset:12288\n" \
  "ds_read_b128 v[36:39], v6 offset:13312\n" \
  "v_smfmac_f32_16x16x64_bf16 v[16:19], v[44:47], v[48:55], v0\n" \
  "ds_read_b128 v[48:51], v6 offset:14336\n" \
  "ds_read_b128 v[52:55], v6 offset:15360\n" \
  "s_nop 0\n" \
  "s_waitcnt lgkmcnt(6)\n" \
  "v_smfmac_f32_16x16x64_bf16 v[20:23], v[40:43], v[56:63], v0\n" \
  "s_nop 0\n" \
  "s_waitcnt lgkmcnt(4)\n" \
  "v_smfmac_f32_16x16x64_bf16 v[8:11], v[40:43], v[24:31], v0\n" \
  "v_permlane32_swap_b32 v12, v17\n" \
  "v_permlane32_swap_b32 v13, v16\n" \
  "v_permlane32_swap_b32 v14, v19\n" \
  "v_permlane32_swap_b32 v15, v18\n" \
  "s_waitcnt lgkmcnt(2)\n" \
  "v_smfmac_f32_16x16x64_bf16 v[20:23], v[44:47], v[32:39], v0\n" \
  "v_pk_add_f32 v[12:13], v[12:13], v[16:17] op_sel:[0,1] op_sel_hi:[1,0]\n" \
  "v_pk_add_f32 v[14:15], v[14:15], v[18:19] op_sel:[0,1] op_sel_hi:[1,0]\n" \
  "v_pk_fma_f32 v[12:13], v[12:13], s[84:85], s[84:85] op_sel:[0,0,1] op_sel_hi:[1,0,1]\n" \
  "v_pk_fma_f32 v[14:15], v[14:15], s[84:85], s[84:85] op_sel:[0,0,1] op_sel_hi:[1,0,1]\n" \
  "s_waitcnt lgkmcnt(0)\n" \
  "v_smfmac_f32_16x16x64_bf16 v[8:11], v[44:47], v[48:55], v0\n" \
  "v_lshl_add_u32 v12, v12, 6, s91\n" \
  "v_lshl_add_u32 v13, v13, 2, s75\n" \
  "global_atomic_add_x2 v5, v[12:13], s[58:59] offset:0\n" \
  "v_lshl_add_u32 v14, v14, 6, s91\n" \
  "v_lshl_add_u32 v15, v15, 2, s75\n" \
  "global_atomic_add_x2 v5, v[14:15], s[58:59] offset:2048\n" \
  "s_nop 1\n" \
  "v_permlane32_swap_b32 v20, v9\n" \
  "v_permlane32_swap_b32 v21, v8\n" \
  "v_permlane32_swap_b32 v22, v11\n" \
  "v_permlane32_swap_b32 v23, v10\n" \
  "v_pk_add_f32 v[20:21], v[20:21], v[8:9] op_sel:[0,1] op_sel_hi:[1,0]\n" \
  "v_pk_add_f32 v[22:23], v[22:23], v[10:11] op_sel:[0,1] op_sel_hi:[1,0]\n" \
  "v_pk_fma_f32 v[20:21], v[20:21], s[84:85], s[84:85] op_sel:[0,0,1] op_sel_hi:[1,0,1]\n" \
  "v_pk_fma_f32 v[22:23], v[22:23], s[84:85], s[84:85] op_sel:[0,0,1] op_sel_hi:[1,0,1]\n" \
  "v_lshl_add_u32 v20, v20, 6, s91\n" \
  "v_lshl_add_u32 v21, v21, 2, s75\n" \
  "global_atomic_add_x2 v5, v[20:21], s[58:59] offset:3072\n" \
  "v_lshl_add_u32 v22, v22, 6, s91\n" \
  "v_lshl_add_u32 v23, v23, 2, s75\n" \
  "global_atomic_add_x2 v5, v[22:23], s[58:59] offset:1024\n" \
  "s_memrealtime s[38:39]\n" \
  "v_mbcnt_lo_u32_b32 v48, -1, 0\n" \
  "v_mbcnt_hi_u32_b32 v48, -1, v48\n" \
  "s_add_u32 s88, s74, 157760\n" \
  "v_mov_b32 v59, s88\n" \
  "v_lshl_add_u32 v52, v48, 2, v59\n" \
  "ds_read_b32 v52, v52 offset:192\n" \
  "v_lshrrev_b32 v49, 3, v48\n" \
  "v_and_b32 v50, 7, v48\n" \
  "v_lshlrev_b32 v50, 4, v50\n" \
  "v_lshl_or_b32 v44, v49, 8, v50\n" \
  "s_mov_b32 s87, 0x40000\n" \
  "v_and_b32 v49, 15, v48\n" \
  "v_lshl_add_u32 v8, v49, 1, s97\n" \
  "v_bfe_u32 v49, v48, 4, 1\n" \
  "v_mad_u32_u24 v8, v49, s92, v8\n" \
  "v_lshrrev_b32 v49, 5, v48\n" \
  "v_mad_u32_u24 v9, v49, s93, v8\n" \
  "v_mad_u32_u24 v8, v49, s94, v8\n" \
  "v_add_u32 v10, s99, v1\n" \
  "s_lshr_b32 s88, s99, 2\n" \
  "s_add_u32 s88, s88, s74\n" \
  "s_add_u32 s88, s88, 139264\n" \
  "v_lshl_add_u32 v7, v49, 10, s88\n" \
  "v_and_b32 v49, 31, v48\n" \
  "v_lshl_add_u32 v7, v49, 3, v7\n" \
  "v_mov_b64 v[12:13], 0\n" \
  "v_mov_b64 v[14:15], 0\n" \
  "v_mov_b64 v[16:17], 0\n" \
  "v_mov_b64 v[18:19], 0\n" \
  "v_mov_b64 v[20:21], 0\n" \
  "v_mov_b64 v[22:23], 0\n" \
  "v_and_b32 v50, 3, v48\n" \
  "v_lshl_add_u32 v40, v50, 2, s98\n" \
  "v_lshrrev_b32 v50, 4, v48\n" \
  "v_mad_u32_u24 v40, v50, s95, v40\n" \
  "v_bfe_u32 v49, v48, 3, 1\n" \
  "v_lshl_add_u32 v40, v49, 6, v40\n" \
  "v_bfe_u32 v49, v48, 2, 1\n" \
  "v_lshl_add_u32 v40, v49, 7, v40\n" \
  "v_xor_b32 v41, 16, v40\n" \
  "v_xor_b32 v42, 32, v40\n" \
  "v_xor_b32 v43, 48, v40\n" \
  "s_add_i32 s80, s86, 0x32800000\n" \
  "s_add_i32 s81, s86, 0x33800000\n" \
  "s_add_i32 s88, s86, 0xb4000000\n" \
  "v_mov_b32 v46, s88\n" \
  "v_mov_b32 v47, 0\n" \
  "v_mov_b32 v11, 0\n" \
  "s_mov_b32 s89, s60\n" \
  "s_xor_b32 s60, s60, 0x80\n" \
  "s_bitcmp0_b32 s60, 7\n" \
  "s_cselect_b32 s88, 0x8000, 0\n" \
  "s_add_u32 s60, s60, s88\n" \
  "s_and_b32 s60, s60, 0xffff\n" \
  "s_add_u32 s58, s56, s60\n" \
  "s_addc_u32 s59, s57, 0\n" \
  "s_sub_u32 s71, s71, 1\n" \
  "s_waitcnt lgkmcnt(0)\n" \
  "s_sub_u32 s39, s38, 100\n" \
  "v_subrev_u32 v52, s39, v52\n" \
  "v_cmp_lt_u32 vcc, 0x1000, v52\n" \
  "s_cmp_eq_u64 vcc, 0\n" \
  "s_cbranch_scc1 .LCA_OKM_%=\n" \
  ".LCA_AGN_%=:\n" \
  "buffer_load_dword v52, v5, s[0:3], s66 offen sc1\n" \
  "s_sub_u32 s87, s87, 1\n" \
  "s_cbranch_scc1 .LCA_END_%=\n" \
  "s_waitcnt vmcnt(0)\n" \
  "v_cmp_eq_u32 vcc, 0, v52\n" \
  "v_subrev_u32 v52, s39, v52\n" \
  "s_cmp_eq_u64 vcc, 0\n" \
  "s_cbranch_scc1 .LCA_OKM_%=\n" \
  "s_sleep 4\n" \
  "s_branch .LCA_AGN_%=\n" \
  ".LCA_OKM_%=:\n" \
  "s_nop 1\n" \
  "v_max_i32_dpp v52, v52, v52 row_ror:1 row_mask:0xf bank_mask:0xf\n" \
  "s_nop 1\n" \
  "v_max_i32_dpp v52, v52, v52 row_ror:2 row_mask:0xf bank_mask:0xf\n" \
  "s_nop 1\n" \
  "v_max_i32_dpp v52, v52, v52 row_ror:4 row_mask:0xf bank_mask:0xf\n" \
  "s_movk_i32 vcc_lo, 100\n" \
  "v_readlane_b32 s88, v52, 0\n" \
  "s_max_i32 vcc_lo, vcc_lo, s88\n" \
  "v_readlane_b32 s88, v52, 16\n" \
  "s_max_i32 vcc_lo, vcc_lo, s88\n" \
  "v_readlane_b32 s88, v52, 32\n" \
  "s_max_i32 vcc_lo, vcc_lo, s88\n" \
  "v_readlane_b32 s88, v52, 48\n" \
  "s_max_i32 vcc_lo, vcc_lo, s88\n" \
  "s_add_u32 s66, s66, 1024\n" \
  "s_add_u32 vcc_lo, vcc_lo, s39\n" \
  "s_add_u32 vcc_lo, vcc_lo, 24\n" \
  ".LCA_TW_%=:\n" \
  "s_memrealtime s[38:39]\n" \
  "s_waitcnt lgkmcnt(0)\n" \
  "s_sub_u32 s38, s38, vcc_lo\n" \
  "s_cmp_lt_i32 s38, 0\n" \
  "s_cbranch_scc1 .LCA_TW_%=\n" \
  "buffer_load_dwordx4 v[24:27], v44, s[40:43], s89 offen sc1\n" \
  "buffer_load_dwordx4 v[28:31], v44, s[40:43], s89 offen offset:2048 sc1\n" \
  ".LCA_SEE_%=:\n" \
  "s_waitcnt vmcnt(1)\n" \
  "v_bitop3_b32 v45, v24, v26, 32 bitop3:0x80\n" \
  "v_alignbit_b32 v32, v25, v24, 2\n" \
  "v_alignbit_b32 v34, v27, v26, 2\n" \
  "s_waitcnt vmcnt(0)\n" \
  "v_bitop3_b32 v45, v45, v28, v30 bitop3:0x80\n" \
  "v_cmp_eq_u32 vcc, 0, v45\n" \
  "v_cvt_f32_i32 v32, v32\n" \
  "v_cvt_f32_i32 v33, v25\n" \
  "s_cmp_eq_u64 vcc, 0\n" \
  "s_cbranch_scc0 .LCA_POLL_%=\n" \
  "v_cvt_f32_i32 v34, v34\n" \
  "v_cvt_f32_i32 v35, v27\n" \
  "v_pk_fma_f32 v[32:33], v[32:33], s[80:81], v[46:47]\n" \
  "v_alignbit_b32 v36, v29, v28, 2\n" \
  "v_pk_fma_f32 v[34:35], v[34:35], s[80:81], v[46:47]\n" \
  "v_alignbit_b32 v38, v31, v30, 2\n" \
  "v_cvt_pk_bf16_f32 v32, v32, v34\n" \
  "v_cvt_pk_bf16_f32 v33, v33, v35\n" \
  "v_cvt_f32_i32 v36, v36\n" \
  "ds_write_b32 v40, v32\n" \
  "v_cvt_f32_i32 v37, v29\n" \
  "ds_write_b32 v41, v33\n" \
  "v_cvt_f32_i32 v38, v38\n" \
  "v_cvt_f32_i32 v39, v31\n" \
  "v_pk_fma_f32 v[36:37], v[36:37], s[80:81], v[46:47]\n" \
  "v_pk_fma_f32 v[38:39], v[38:39], s[80:81], v[46:47]\n" \
  "v_dot2c_f32_bf16 v11, v32, v32\n" \
  "v_cvt_pk_bf16_f32 v36, v36, v38\n" \
  "v_cvt_pk_bf16_f32 v37, v37, v39\n" \
  "ds_write_b32 v42, v36\n" \
  "ds_write_b32 v43, v37\n" \
  "v_dot2c_f32_bf16 v11, v33, v33\n" \
  "v_dot2c_f32_bf16 v11, v36, v36\n" \
  "v_dot2c_f32_bf16 v11, v37, v37\n" \
  "s_nop 1\n" \
  "s_nop 0\n" \
  "v_cmpx_lt_f32 vcc, 2.0, v11\n" \
  "ds_max_u32 v59, v11\n" \
  "s_mov_b64 exec, -1\n" \
  "s_branch .LCA_TOP_%=\n" \
  ".LCA_POLL_%=:\n" \
  "buffer_load_dwordx4 v[24:27], v44, s[40:43], s89 offen sc1\n" \
  "buffer_load_dwordx4 v[28:31], v44, s[40:43], s89 offen offset:2048 sc1\n" \
  "s_sub_u32 s87, s87, 1\n" \
  "s_cbranch_scc1 .LCA_END_%=\n" \
  "s_branch .LCA_SEE_%=\n" \
  ".LCA_LAST_%=:\n" \
  ".LCA_END_%=:\n" \
  "s_endpgm\n"
#define CHAIN_ASM_VO \
  "s_mov_b32 s92, 1152\n" \
  "s_mov_b32 s93, 576\n" \
  "s_mov_b32 s94, 2272\n" \
  "s_mov_b32 s95, 272\n" \
  "s_mov_b32 s96, 288\n" \
  "s_mov_b32 s91, 0x40000001\n" \
  "s_mov_b32 s84, 0x4b400000\n" \
  "s_mov_b32 s85, 0x4b400000\n" \
  "s_mov_b32 s78, 0x3e0293ee\n" \
  "s_mov_b32 s76, 0xff00ff00\n" \
  "s_mov_b32 s77, 0xff00ff00\n" \
  "s_add_u32 s58, s56, s60\n" \
  "s_addc_u32 s59, s57, 0\n" \
  "s_lshl_b32 s98, s73, 10\n" \
  "s_add_u32 s98, s98, s74\n" \
  "s_add_u32 s98, s98, 131072\n" \
  "s_lshl_b32 s97, s73, 5\n" \
  "s_add_u32 s97, s97, s74\n" \
  "s_add_u32 s97, s97, 160992\n" \
  "s_lshl_b32 s99, s73, 11\n" \
  "s_add_u32 s99, s99, s74\n" \
  "s_add_u32 s99, s99, 133120\n" \
  "s_lshl_b32 s94, s73, 2\n" \
  "s_add_u32 s94, s94, s74\n" \
  "s_add_u32 s94, s94, 157828\n" \
  "v_mbcnt_lo_u32_b32 v48, -1, 0\n" \
  "v_mbcnt_hi_u32_b32 v48, -1, v48\n" \
  "s_add_u32 s88, s74, 157760\n" \
  "v_mov_b32 v59, s88\n" \
  "s_sub_u32 s88, s73, 4\n" \
  "v_and_b32 v51, 15, v48\n" \
  "v_lshl_add_u32 v4, v51, 1, s97\n" \
  "v_bfe_u32 v49, v48, 4, 1\n" \
  "v_mad_u32_u24 v4, v49, s92, v4\n" \
  "v_lshrrev_b32 v50, 5, v48\n" \
  "v_mad_u32_u24 v4, v50, s93, v4\n" \
  "v_lshlrev_b32 v8, 4, v49\n" \
  "v_lshl_add_u32 v8, v51, 5, v8\n" \
  "v_lshl_add_u32 v8, v50, 3, v8\n" \
  "s_lshl_b32 s89, s88, 8\n" \
  "s_add_u32 s89, s89, s74\n" \
  "s_add_u32 s89, s89, 139264\n" \
  "v_and_b32 v49, 31, v48\n" \
  "v_lshl_add_u32 v5, v49, 3, s89\n" \
  "v_lshl_add_u32 v5, v50, 10, v5\n" \
  "v_lshrrev_b32 v49, 2, v51\n" \
  "v_and_b32 v50, 5, v51\n" \
  "v_and_or_b32 v49, v49, 2, v50\n" \
  "s_lshl_b32 s89, s88, 6\n" \
  "s_add_u32 s89, s89, s74\n" \
  "s_add_u32 s89, s89, 160992\n" \
  "v_and_b32 v50, 48, v48\n" \
  "v_add_u32 v6, s89, v50\n" \
  "v_mad_u32_u24 v6, v49, s96, v6\n" \
  "v_lshlrev_b32 v7, 3, v50\n" \
  "v_lshl_add_u32 v7, v49, 4, v7\n" \
  "v_and_b32 v49, 2, v51\n" \
  "v_lshl_add_u32 v7, v49, 29, v7\n" \
  "v_mov_b64 v[12:13], 0\n" \
  "v_mov_b64 v[14:15], 0\n" \
  "v_mov_b64 v[16:17], 0\n" \
  "v_mov_b64 v[18:19], 0\n" \
  "v_mov_b64 v[20:21], 0\n" \
  "v_mov_b64 v[22:23], 0\n" \
  "v_lshlrev_b32 v10, 4, v48\n" \
  "s_add_u32 s78, s74, 157952\n" \
  "s_cmp_eq_u32 s73, 5\n" \
  "s_cselect_b64 s[76:77], -1, 0\n" \
  "v_lshl_add_u32 v52, v48, 2, v59\n" \
  "ds_write_b32 v52, v12 offset:192\n" \
  "s_movk_i32 s79, 136\n" \
  "v_mov_b32 v24, 0x44870000\n" \
  "v_mul_f32 v24, s72, v24\n" \
  "s_nop 0\n" \
  "v_readfirstlane_b32 s86, v24\n" \
  "s_lshr_b32 s86, s86, 23\n" \
  "s_addk_i32 s86, 0xff83\n" \
  "s_ashr_i32 s86, s86, 1\n" \
  "s_max_i32 s86, s86, -20\n" \
  "s_min_i32 s86, s86, 100\n" \
  "s_mov_b32 s82, s86\n" \
  "s_lshr_b32 s83, s72, 23\n" \
  "s_sub_i32 s83, s83, 242\n" \
  "s_lshl_b32 s88, s73, 10\n" \
  "v_add_u32 v24, s88, v1\n" \
  "ds_read_b64 v[32:33], v24 offset:0\n" \
  "ds_read_b64 v[36:37], v24 offset:8\n" \
  "s_waitcnt lgkmcnt(0)\n" \
  "v_mov_b32 v11, 0\n" \
  "v_dot2c_f32_bf16 v11, v32, v32\n" \
  "v_dot2c_f32_bf16 v11, v33, v33\n" \
  "v_dot2c_f32_bf16 v11, v36, v36\n" \
  "v_dot2c_f32_bf16 v11, v37, v37\n" \
  "s_nop 1\n" \
  "s_nop 0\n" \
  "v_cmpx_lt_f32 vcc, 2.0, v11\n" \
  "ds_max_u32 v59, v11\n" \
  "s_mov_b64 exec, -1\n" \
  ".LCV_TOP_%=:\n" \
  "s_waitcnt lgkmcnt(0)\n" \
  "s_barrier\n" \
  "s_setprio 3\n" \
  "ds_read_b128 v[24:27], v1 offset:0\n" \
  "ds_read_b128 v[28:31], v1 offset:1024\n" \
  "ds_read_b128 v[32:35], v1 offset:2048\n" \
  "ds_read_b128 v[36:39], v1 offset:3072\n" \
  "ds_read_b128 v[40:43], v1 offset:4096\n" \
  "ds_read_b128 v[44:47], v1 offset:5120\n" \
  "ds_read_b128 v[48:51], v1 offset:6144\n" \
  "ds_read_b128 v[52:55], v1 offset:7168\n" \
  "s_memrealtime s[38:39]\n" \
  "buffer_load_dword v58, off, s[44:47], s68 sc1\n" \
  "s_waitcnt lgkmcnt(7)\n" \
  "v_smfmac_f32_16x16x64_bf16 v[12:15], v[24:27], v[64:71], v0\n" \
  "s_waitcnt lgkmcnt(6)\n" \
  "v_smfmac_f32_16x16x64_bf16 v[12:15], v[28:31], v[72:79], v0\n" \
  "s_waitcnt lgkmcnt(5)\n" \
  "v_smfmac_f32_16x16x64_bf16 v[12:15], v[32:35], v[80:87], v0\n" \
  "s_waitcnt lgkmcnt(4)\n" \
  "v_smfmac_f32_16x16x64_bf16 v[12:15], v[36:39], v[88:95], v0\n" \
  "s_waitcnt lgkmcnt(3)\n" \
  "v_smfmac_f32_16x16x64_bf16 v[12:15], v[40:43], v[96:103], v0\n" \
  "s_waitcnt lgkmcnt(2)\n" \
  "v_smfmac_f32_16x16x64_bf16 v[12:15], v[44:47], v[104:111], v0\n" \
  "s_waitcnt lgkmcnt(1)\n" \
  "v_smfmac_f32_16x16x64_bf16 v[12:15], v[48:51], v[112:119], v0\n" \
  "s_waitcnt lgkmcnt(0)\n" \
  "v_smfmac_f32_16x16x64_bf16 v[12:15], v[52:55], v[120:127], v0\n" \
  "ds_read_b32 v11, v1 offset:26688\n" \
  "s_mov_b32 s61, s38\n" \
  "s_add_u32 s38, s38, s79\n" \
  "v_or_b32 v57, 1, s38\n" \
  "v_add_u32 v56, s67, v10\n" \
  "s_mov_b64 exec, 0x11\n" \
  "buffer_store_dword v57, v56, s[0:3], 0 offen sc1\n" \
  "s_mov_b64 exec, -1\n" \
  "v_permlane32_swap_b32 v12, v14\n" \
  "v_permlane32_swap_b32 v13, v15\n" \
  "v_pk_add_f32 v[12:13], v[12:13], v[14:15]\n" \
  "s_nop 0\n" \
  "v_cvt_pk_bf16_f32 v12, v12, v13\n" \
  "ds_write_b16 v4, v12\n" \
  "ds_write_b16_d16_hi v4, v12 offset:288\n" \
  "s_setprio 0\n" \
  "v_mov_b64 v[12:13], 0\n" \
  "v_mov_b64 v[14:15], 0\n" \
  "s_waitcnt lgkmcnt(2)\n" \
  "v_readfirstlane_b32 s86, v11\n" \
  "s_lshr_b32 s86, s86, 23\n" \
  "s_add_i32 s86, s86, s83\n" \
  "s_ashr_i32 s86, s86, 1\n" \
  "s_max_i32 s86, s86, s82\n" \
  "s_min_i32 s86, s86, 100\n" \
  "s_lshl_b32 s86, s86, 23\n" \
  "s_sub_i32 s84, 0x4a800000, s86\n" \
  "s_cmp_eq_u32 s71, 0\n" \
  "s_waitcnt lgkmcnt(0)\n" \
  "s_barrier\n" \
  "ds_read_b128 v[60:63], v6 offset:0\n" \
  "v_smfmac_f32_16x16x64_bf16 v[16:19], v[24:27], v[128:135], v0\n" \
  "v_smfmac_f32_16x16x64_bf16 v[20:23], v[24:27], v[192:199], v0\n" \
  "v_smfmac_f32_16x16x64_bf16 v[16:19], v[28:31], v[136:143], v0\n" \
  "v_smfmac_f32_16x16x64_bf16 v[20:23], v[28:31], v[200:207], v0\n" \
  "v_smfmac_f32_16x16x64_bf16 v[16:19], v[32:35], v[144:151], v0\n" \
  "s_waitcnt lgkmcnt(0)\n" \
  "v_max_u32 v60, 1, v60\n" \
  "v_smfmac_f32_16x16x64_bf16 v[20:23], v[32:35], v[208:215], v0\n" \
  "v_max_u32 v61, 1, v61\n" \
  "v_smfmac_f32_16x16x64_bf16 v[16:19], v[36:39], v[152:159], v0\n" \
  "v_max_u32 v62, 1, v62\n" \
  "v_smfmac_f32_16x16x64_bf16 v[20:23], v[36:39], v[216:223], v0\n" \
  "v_max_u32 v63, 1, v63\n" \
  "v_smfmac_f32_16x16x64_bf16 v[16:19], v[40:43], v[160:167], v0\n" \
  "buffer_store_dwordx4 v[60:63], v7, s[48:51], s69 offen sc1\n" \
  "v_smfmac_f32_16x16x64_bf16 v[20:23], v[40:43], v[224:231], v0\n" \
  "v_smfmac_f32_16x16x64_bf16 v[16:19], v[44:47], v[168:175], v0\n" \
  "s_mov_b32 m0, s78\n" \
  "s_mov_b64 exec, s[76:77]\n" \
  "buffer_load_dword v2, s[0:3], s66 offen sc1 lds\n" \
  "s_mov_b64 exec, -1\n" \
  "v_smfmac_f32_16x16x64_bf16 v[20:23], v[44:47], v[232:239], v0\n" \
  "v_smfmac_f32_16x16x64_bf16 v[16:19], v[48:51], v[176:183], v0\n" \
  "v_smfmac_f32_16x16x64_bf16 v[20:23], v[48:51], v[240:247], v0\n" \
  "v_smfmac_f32_16x16x64_bf16 v[16:19], v[52:55], v[184:191], v0\n" \
  "v_smfmac_f32_16x16x64_bf16 v[20:23], v[52:55], v[248:255], v0\n" \
  "ds_read_b128 v[24:27], v3 offset:0\n" \
  "ds_read_b128 v[28:31], v3 offset:1024\n" \
  "ds_read_b128 v[32:35], v3 offset:2048\n" \
  "ds_read_b128 v[36:39], v3 offset:3072\n" \
  "ds_read_b128 v[40:43], v3 offset:8192\n" \
  "ds_read_b128 v[44:47], v3 offset:9216\n" \
  "s_nop 0\n" \
  "v_permlane32_swap_b32 v16, v18\n" \
  "v_permlane32_swap_b32 v17, v19\n" \
  "v_permlane32_swap_b32 v20, v22\n" \
  "v_permlane32_swap_b32 v21, v23\n" \
  "v_pk_add_f32 v[16:17], v[16:17], v[18:19]\n" \
  "v_pk_add_f32 v[20:21], v[20:21], v[22:23]\n" \
  "v_cvt_pk_bf16_f32 v16, v16, v17\n" \
  "v_cvt_pk_bf16_f32 v17, v20, v21\n" \
  "ds_write_b64 v5, v[16:17]\n" \
  "v_max_u32 v20, 1, v16\n" \
  "v_max_u32 v21, 1, v17\n" \
  "s_nop 1\n" \
  "v_permlane32_swap_b32 v20, v21\n" \
  "buffer_store_dwordx2 v[20:21], v8, s[52:55], s70 offen sc1\n" \
  "s_cbranch_scc1 .LCV_LAST_%=\n" \
  "s_waitcnt vmcnt(3)\n" \
  "v_cmp_eq_u32 vcc, 0, v58\n" \
  "s_cmp_eq_u64 vcc, 0\n" \
  "s_cselect_b32 s90, 1, 0\n" \
  "s_waitcnt lgkmcnt(0)\n" \
  "s_barrier\n" \
  "s_cbranch_scc0 .LCV_NOTOK_%=\n" \
  "s_mov_b32 m0, s99\n" \
  "s_add_u32 s88, s99, 8192\n" \
  "global_load_lds_dwordx4 v10, s[62:63] sc1\n" \
  "global_load_lds_dwordx4 v10, s[62:63] offset:1024 sc1\n" \
  "s_mov_b32 m0, s88\n" \
  "s_nop 0\n" \
  "global_load_lds_dwordx4 v10, s[64:65] sc1\n" \
  "global_load_lds_dwordx4 v10, s[64:65] offset:1024 sc1\n" \
  ".LCV_NOTOK_%=:\n" \
  "s_add_u32 s67, s67, 1024\n" \
  "ds_write_b32 v59, v12\n" \
  "ds_read_b128 v[48:51], v3 offset:10240\n" \
  "ds_read_b128 v[52:55], v3 offset:11264\n" \
  "v_mov_b64 v[18:19], 0\n" \
  "v_mov_b64 v[20:21], 0\n" \
  "v_mov_b64 v[22:23], 0\n" \
  "v_mov_b64 v[16:17], 0\n" \
  "ds_read_b128 v[56:59], v3 offset:4096\n" \
  "ds_read_b128 v[60:63], v3 offset:5120\n" \
  "s_cmp_eq_u32 s90, 1\n" \
  "s_cbranch_scc1 .LCV_NM0_%=\n" \
  "s_mov_b32 m0, s94\n" \
  "s_mov_b64 exec, 1\n" \
  "buffer_load_dword off, s[44:47], s68 sc1 lds\n" \
  "s_mov_b64 exec, -1\n" \
  ".LCV_NM0_%=:\n" \
  "s_waitcnt lgkmcnt(0)\n" \
  "s_barrier\n" \
  "ds_read_b128 v[4:7], v1 offset:8192\n" \
  "ds_read_b128 v[8:11], v1 offset:9216\n" \
  "s_waitcnt lgkmcnt(1)\n" \
  "v_smfmac_f32_16x16x64_bf16 v[12:15], v[4:7], v[24:31], v0\n" \
  "ds_read_b128 v[24:27], v3 offset:6144\n" \
  "ds_read_b128 v[28:31], v3 offset:7168\n" \
  "v_smfmac_f32_16x16x64_bf16 v[16:19], v[4:7], v[32:39], v0\n" \
  "ds_read_b128 v[32:35], v3 offset:12288\n" \
  "ds_read_b128 v[36:39], v3 offset:13312\n" \
  "s_waitcnt lgkmcnt(4)\n" \
  "v_smfmac_f32_16x16x64_bf16 v[12:15], v[8:11], v[40:47], v0\n" \
  "v_smfmac_f32_16x16x64_bf16 v[16:19], v[8:11], v[48:55], v0\n" \
  "v_smfmac_f32_16x16x64_bf16 v[20:23], v[4:7], v[56:63], v0\n" \
  "ds_read_b128 v[56:59], v3 offset:14336\n" \
  "ds_read_b128 v[60:63], v3 offset:15360\n" \
  "v_mov_b64 v[40:41], 0\n" \
  "v_mov_b64 v[42:43], 0\n" \
  "s_waitcnt lgkmcnt(2)\n" \
  "v_smfmac_f32_16x16x64_bf16 v[20:23], v[8:11], v[32:39], v0\n" \
  "s_nop 0\n" \
  "v_permlane32_swap_b32 v12, v17\n" \
  "v_permlane32_swap_b32 v13, v16\n" \
  "v_permlane32_swap_b32 v14, v19\n" \
  "v_permlane32_swap_b32 v15, v18\n" \
  "v_smfmac_f32_16x16x64_bf16 v[40:43], v[4:7], v[24:31], v0\n" \
  "v_pk_add_f32 v[12:13], v[12:13], v[16:17] op_sel:[0,1] op_sel_hi:[1,0]\n" \
  "v_pk_add_f32 v[14:15], v[14:15], v[18:19] op_sel:[0,1] op_sel_hi:[1,0]\n" \
  "v_pk_fma_f32 v[12:13], v[12:13], s[84:85], s[84:85] op_sel:[0,0,1] op_sel_hi:[1,0,1]\n" \
  "v_pk_fma_f32 v[14:15], v[14:15], s[84:85], s[84:85] op_sel:[0,0,1] op_sel_hi:[1,0,1]\n" \
  "s_waitcnt lgkmcnt(0)\n" \
  "v_smfmac_f32_16x16x64_bf16 v[40:43], v[8:11], v[56:63], v0\n" \
  "v_lshl_add_u32 v12, v12, 6, s91\n" \
  "v_lshl_add_u32 v13, v13, 2, s75\n" \
  "global_atomic_add_x2 v2, v[12:13], s[58:59] offset:0\n" \
  "v_lshl_add_u32 v14, v14, 6, s91\n" \
  "v_lshl_add_u32 v15, v15, 2, s75\n" \
  "global_atomic_add_x2 v2, v[14:15], s[58:59] offset:2048\n" \
  "s_nop 1\n" \
  "v_permlane32_swap_b32 v20, v41\n" \
  "v_permlane32_swap_b32 v21, v40\n" \
  "v_permlane32_swap_b32 v22, v43\n" \
  "v_permlane32_swap_b32 v23, v42\n" \
  "v_pk_add_f32 v[20:21], v[20:21], v[40:41] op_sel:[0,1] op_sel_hi:[1,0]\n" \
  "v_pk_add_f32 v[22:23], v[22:23], v[42:43] op_sel:[0,1] op_sel_hi:[1,0]\n" \
  "v_pk_fma_f32 v[20:21], v[20:21], s[84:85], s[84:85] op_sel:[0,0,1] op_sel_hi:[1,0,1]\n" \
  "v_pk_fma_f32 v[22:23], v[22:23], s[84:85], s[84:85] op_sel:[0,0,1] op_sel_hi:[1,0,1]\n" \
  "v_lshl_add_u32 v20, v20, 6, s91\n" \
  "v_lshl_add_u32 v21, v21, 2, s75\n" \
  "global_atomic_add_x2 v2, v[20:21], s[58:59] offset:3072\n" \
  "v_lshl_add_u32 v22, v22, 6, s91\n" \
  "v_lshl_add_u32 v23, v23, 2, s75\n" \
  "global_atomic_add_x2 v2, v[22:23], s[58:59] offset:1024\n" \
  "s_memrealtime s[38:39]\n" \
  "v_mbcnt_lo_u32_b32 v48, -1, 0\n" \
  "v_mbcnt_hi_u32_b32 v48, -1, v48\n" \
  "s_add_u32 s88, s74, 157760\n" \
  "v_mov_b32 v59, s88\n" \
  "v_lshl_add_u32 v52, v48, 2, v59\n" \
  "ds_read_b32 v52, v52 offset:192\n" \
  "v_lshrrev_b32 v49, 3, v48\n" \
  "v_and_b32 v50, 7, v48\n" \
  "v_lshlrev_b32 v50, 4, v50\n" \
  "v_lshl_or_b32 v44, v49, 8, v50\n" \
  "s_mov_b32 s87, 0x40000\n" \
  "v_lshlrev_b32 v10, 4, v48\n" \
  "s_cmp_eq_u32 s90, 1\n" \
  "s_cbranch_scc1 .LCV_HAVE_%=\n" \
  "v_mov_b32 v58, s94\n" \
  "s_waitcnt vmcnt(4)\n" \
  "ds_read_b32 v58, v58\n" \
  "s_waitcnt lgkmcnt(0)\n" \
  "v_cmp_eq_u32 vcc, 0, v58\n" \
  "s_cmp_eq_u64 vcc, 0\n" \
  "s_cbranch_scc1 .LCV_GOT_%=\n" \
  "s_branch .LCV_ASK_%=\n" \
  ".LCV_LATE_%=:\n" \
  "v_cmp_eq_u32 vcc, 0, v58\n" \
  "s_cmp_eq_u64 vcc, 0\n" \
  "s_cbranch_scc1 .LCV_GOT_%=\n" \
  "s_sleep 12\n" \
  ".LCV_ASK_%=:\n" \
  "buffer_load_dword v58, off, s[44:47], s68 sc1\n" \
  "s_sub_u32 s87, s87, 1\n" \
  "s_cbranch_scc1 .LCV_END_%=\n" \
  "s_waitcnt vmcnt(0)\n" \
  "s_branch .LCV_LATE_%=\n" \
  ".LCV_GOT_%=:\n" \
  "s_mov_b32 m0, s99\n" \
  "s_add_u32 s88, s99, 8192\n" \
  "global_load_lds_dwordx4 v10, s[62:63] sc1\n" \
  "global_load_lds_dwordx4 v10, s[62:63] offset:1024 sc1\n" \
  "s_mov_b32 m0, s88\n" \
  "s_nop 0\n" \
  "global_load_lds_dwordx4 v10, s[64:65] sc1\n" \
  "global_load_lds_dwordx4 v10, s[64:65] offset:1024 sc1\n" \
  ".LCV_HAVE_%=:\n" \
  "s_waitcnt lgkmcnt(0)\n" \
  "s_sub_u32 s61, s38, s61\n" \
  "s_add_u32 s79, s79, 2\n" \
  "s_min_u32 s79, s79, s61\n" \
  "s_sub_u32 s39, s38, 100\n" \
  "v_subrev_u32 v52, s39, v52\n" \
  "v_cmp_lt_u32 vcc, 0x1000, v52\n" \
  "s_cmp_eq_u64 vcc, 0\n" \
  "s_cbranch_scc1 .LCV_OKM_%=\n" \
  ".LCV_AGN_%=:\n" \
  "buffer_load_dword v52, v2, s[0:3], s66 offen sc1\n" \
  "s_sub_u32 s87, s87, 1\n" \
  "s_cbranch_scc1 .LCV_END_%=\n" \
  "s_waitcnt vmcnt(0)\n" \
  "v_cmp_eq_u32 vcc, 0, v52\n" \
  "v_subrev_u32 v52, s39, v52\n" \
  "s_cmp_eq_u64 vcc, 0\n" \
  "s_cbranch_scc1 .LCV_OKM_%=\n" \
  "s_sleep 4\n" \
  "s_branch .LCV_AGN_%=\n" \
  ".LCV_OKM_%=:\n" \
  "s_nop 1\n" \
  "v_max_i32_dpp v52, v52, v52 row_ror:1 row_mask:0xf bank_mask:0xf\n" \
  "s_nop 1\n" \
  "v_max_i32_dpp v52, v52, v52 row_ror:2 row_mask:0xf bank_mask:0xf\n" \
  "s_nop 1\n" \
  "v_max_i32_dpp v52, v52, v52 row_ror:4 row_mask:0xf bank_mask:0xf\n" \
  "s_movk_i32 vcc_lo, 100\n" \
  "v_readlane_b32 s88, v52, 0\n" \
  "s_max_i32 vcc_lo, vcc_lo, s88\n" \
  "v_readlane_b32 s88, v52, 16\n" \
  "s_max_i32 vcc_lo, vcc_lo, s88\n" \
  "v_readlane_b32 s88, v52, 32\n" \
  "s_max_i32 vcc_lo, vcc_lo, s88\n" \
  "v_readlane_b32 s88, v52, 48\n" \
  "s_max_i32 vcc_lo, vcc_lo, s88\n" \
  "s_add_u32 s66, s66, 1024\n" \
  "s_add_u32 vcc_lo, vcc_lo, s39\n" \
  "s_add_u32 vcc_lo, vcc_lo, 24\n" \
  ".LCV_TW_%=:\n" \
  "s_memrealtime s[38:39]\n" \
  "s_waitcnt lgkmcnt(0)\n" \
  "s_sub_u32 s38, s38, vcc_lo\n" \
  "s_cmp_lt_i32 s38, 0\n" \
  "s_cbranch_scc1 .LCV_TW_%=\n" \
  "buffer_load_dwordx4 v[24:27], v44, s[40:43], s60 offen sc1\n" \
  "buffer_load_dwordx4 v[28:31], v44, s[40:43], s60 offen offset:2048 sc1\n" \
  "s_add_u32 s62, s62, 0x80000\n" \
  "s_addc_u32 s63, s63, 0\n" \
  "s_add_u32 s64, s64, 0x40000\n" \
  "s_addc_u32 s65, s65, 0\n" \
  "s_add_u32 s68, s68, 0x1000\n" \
  "s_add_u32 s69, s69, 0x10000\n" \
  "s_add_u32 s70, s70, 0x10000\n" \
  "s_sub_u32 s88, s73, 4\n" \
  "v_and_b32 v51, 15, v48\n" \
  "v_lshl_add_u32 v4, v51, 1, s97\n" \
  "v_bfe_u32 v49, v48, 4, 1\n" \
  "v_mad_u32_u24 v4, v49, s92, v4\n" \
  "v_lshrrev_b32 v50, 5, v48\n" \
  "v_mad_u32_u24 v4, v50, s93, v4\n" \
  "v_lshlrev_b32 v8, 4, v49\n" \
  "v_lshl_add_u32 v8, v51, 5, v8\n" \
  "v_lshl_add_u32 v8, v50, 3, v8\n" \
  "s_lshl_b32 s89, s88, 8\n" \
  "s_add_u32 s89, s89, s74\n" \
  "s_add_u32 s89, s89, 139264\n" \
  "v_and_b32 v49, 31, v48\n" \
  "v_lshl_add_u32 v5, v49, 3, s89\n" \
  "v_lshl_add_u32 v5, v50, 10, v5\n" \
  "v_lshrrev_b32 v49, 2, v51\n" \
  "v_and_b32 v50, 5, v51\n" \
  "v_and_or_b32 v49, v49, 2, v50\n" \
  "s_lshl_b32 s89, s88, 6\n" \
  "s_add_u32 s89, s89, s74\n" \
  "s_add_u32 s89, s89, 160992\n" \
  "v_and_b32 v50, 48, v48\n" \
  "v_add_u32 v6, s89, v50\n" \
  "v_mad_u32_u24 v6, v49, s96, v6\n" \
  "v_lshlrev_b32 v7, 3, v50\n" \
  "v_lshl_add_u32 v7, v49, 4, v7\n" \
  "v_and_b32 v49, 2, v51\n" \
  "v_lshl_add_u32 v7, v49, 29, v7\n" \
  "v_mov_b64 v[12:13], 0\n" \
  "v_mov_b64 v[14:15], 0\n" \
  "v_mov_b64 v[16:17], 0\n" \
  "v_mov_b64 v[18:19], 0\n" \
  "v_mov_b64 v[20:21], 0\n" \
  "v_mov_b64 v[22:23], 0\n" \
  "v_and_b32 v50, 3, v48\n" \
  "v_lshl_add_u32 v40, v50, 2, s98\n" \
  "v_lshrrev_b32 v50, 4, v48\n" \
  "v_mad_u32_u24 v40, v50, s95, v40\n" \
  "v_bfe_u32 v49, v48, 3, 1\n" \
  "v_lshl_add_u32 v40, v49, 6, v40\n" \
  "v_bfe_u32 v49, v48, 2, 1\n" \
  "v_lshl_add_u32 v40, v49, 7, v40\n" \
  "v_xor_b32 v41, 16, v40\n" \
  "v_xor_b32 v42, 32, v40\n" \
  "v_xor_b32 v43, 48, v40\n" \
  "s_add_i32 s80, s86, 0x32800000\n" \
  "s_add_i32 s81, s86, 0x33800000\n" \
  "s_add_i32 s88, s86, 0xb4000000\n" \
  "v_mov_b32 v46, s88\n" \
  "v_mov_b32 v47, 0\n" \
  "v_mov_b32 v11, 0\n" \
  "s_mov_b32 s89, s60\n" \
  "s_xor_b32 s60, s60, 0x80\n" \
  "s_bitcmp0_b32 s60, 7\n" \
  "s_cselect_b32 s88, 0x8000, 0\n" \
  "s_add_u32 s60, s60, s88\n" \
  "s_and_b32 s60, s60, 0xffff\n" \
  "s_add_u32 s58, s56, s60\n" \
  "s_addc_u32 s59, s57, 0\n" \
  "s_sub_u32 s71, s71, 1\n" \
  ".LCV_SEE_%=:\n" \
  "s_waitcnt vmcnt(1)\n" \
  "v_bitop3_b32 v45, v24, v26, 32 bitop3:0x80\n" \
  "v_alignbit_b32 v32, v25, v24, 2\n" \
  "v_alignbit_b32 v34, v27, v26, 2\n" \
  "s_waitcnt vmcnt(0)\n" \
  "v_bitop3_b32 v45, v45, v28, v30 bitop3:0x80\n" \
  "v_cmp_eq_u32 vcc, 0, v45\n" \
  "v_cvt_f32_i32 v32, v32\n" \
  "v_cvt_f32_i32 v33, v25\n" \
  "s_cmp_eq_u64 vcc, 0\n" \
  "s_cbranch_scc0 .LCV_POLL_%=\n" \
  "v_cvt_f32_i32 v34, v34\n" \
  "v_cvt_f32_i32 v35, v27\n" \
  "v_pk_fma_f32 v[32:33], v[32:33], s[80:81], v[46:47]\n" \
  "v_alignbit_b32 v36, v29, v28, 2\n" \
  "v_pk_fma_f32 v[34:35], v[34:35], s[80:81], v[46:47]\n" \
  "v_alignbit_b32 v38, v31, v30, 2\n" \
  "v_cvt_pk_bf16_f32 v32, v32, v34\n" \
  "v_cvt_pk_bf16_f32 v33, v33, v35\n" \
  "v_cvt_f32_i32 v36, v36\n" \
  "ds_write_b32 v40, v32\n" \
  "v_cvt_f32_i32 v37, v29\n" \
  "ds_write_b32 v41, v33\n" \
  "v_cvt_f32_i32 v38, v38\n" \
  "v_cvt_f32_i32 v39, v31\n" \
  "v_pk_fma_f32 v[36:37], v[36:37], s[80:81], v[46:47]\n" \
  "v_pk_fma_f32 v[38:39], v[38:39], s[80:81], v[46:47]\n" \
  "v_dot2c_f32_bf16 v11, v32, v32\n" \
  "v_cvt_pk_bf16_f32 v36, v36, v38\n" \
  "v_cvt_pk_bf16_f32 v37, v37, v39\n" \
  "ds_write_b32 v42, v36\n" \
  "ds_write_b32 v43, v37\n" \
  "v_dot2c_f32_bf16 v11, v33, v33\n" \
  "v_dot2c_f32_bf16 v11, v36, v36\n" \
  "v_dot2c_f32_bf16 v11, v37, v37\n" \
  "s_nop 1\n" \
  "s_nop 0\n" \
  "v_cmpx_lt_f32 vcc, 2.0, v11\n" \
  "ds_max_u32 v59, v11\n" \
  "s_mov_b64 exec, -1\n" \
  "s_branch .LCV_TOP_%=\n" \
  ".LCV_POLL_%=:\n" \
  "buffer_load_dwordx4 v[24:27], v44, s[40:43], s89 offen sc1\n" \
  "buffer_load_dwordx4 v[28:31], v44, s[40:43], s89 offen offset:2048 sc1\n" \
  "s_sub_u32 s87, s87, 1\n" \
  "s_cbranch_scc1 .LCV_END_%=\n" \
  "s_branch .LCV_SEE_%=\n" \
  ".LCV_LAST_%=:\n" \
  "s_waitcnt vmcnt(0)\n" \
  ".LCV_END_%=:\n" \
  "s_endpgm\n"
__device__ __forceinline__ v4u mk_srd(const void* p, const unsigned bytes) {
  const unsigned long long a = (unsigned long long)p;
  return v4u{(unsigned)a, (unsigned)(a >> 32) & 0xffffu, bytes, 0x00020000u};
}
template <int R>
__device__ __forceinline__ void pin8(v8u& x) {
#define PIN_(a, b) if constexpr (R == a) asm volatile("" : "+{v[" #a ":" #b "]}"(x));
  PIN_(64, 71)  PIN_(72, 79)  PIN_(80, 87)  PIN_(88, 95)  PIN_(96, 103)  PIN_(104, 111)  PIN_(112, 119)  PIN_(120, 127)
  PIN_(128, 135)  PIN_(136, 143)  PIN_(144, 151)  PIN_(152, 159)  PIN_(160, 167)  PIN_(168, 175)  PIN_(176, 183)  PIN_(184, 191)
  PIN_(192, 199)  PIN_(200, 207)  PIN_(208, 215)  PIN_(216, 223)  PIN_(224, 231)  PIN_(232, 239)  PIN_(240, 247)  PIN_(248, 255)
#undef PIN_
}
template <int R>
__device__ __forceinline__ void pin4(v4u& x) {
#define PIN_(a, b) if constexpr (R == a) asm volatile("" : "+{v[" #a ":" #b "]}"(x));
  PIN_(32, 35)  PIN_(36, 39)  PIN_(40, 43)  PIN_(44, 47)  PIN_(48, 51)  PIN_(52, 55)  PIN_(56, 59)  PIN_(60, 63)
  PIN_(96, 99)  PIN_(100, 103)  PIN_(104, 107)  PIN_(108, 111)  PIN_(112, 115)  PIN_(116, 119)  PIN_(120, 123)  PIN_(124, 127)
  PIN_(160, 163)  PIN_(164, 167)  PIN_(168, 171)  PIN_(172, 175)  PIN_(176, 179)  PIN_(180, 183)  PIN_(184, 187)  PIN_(188, 191)
#undef PIN_
}
template <bool ATT>
__device__ __forceinline__ void role_chain_t(const MegaArgs& A, char* smem, const int L, const int h, const int rep) {
  u16* swo = reinterpret_cast<u16*>(smem);
  const int w = A.wv;
  char* ws = A.ws;
  unsigned* FL = (unsigned*)(ws + OFF_FL);
  unsigned* TMO = FL + FL_TMO;
#define LANE_IDS()                         \
  const int l = lane_id();                 \
  const int c = l & 15, g = l >> 4;        \
  (void)c; (void)g;
  float* sgf = reinterpret_cast<float*>(smem + C_X);
  unsigned* FQ = FL + FL_FQ + L * 64 * 32 * 32;
  const int wb = (w + ROT_H) & 7;
  v8u w0[8], w1[8], w2[8];
  u64 tag;
  {
    LANE_IDS();
    const unsigned sb = lds_base(smem);
    const int q = (l >> 2) & 3, p = l & 3;
    const int f = (q & 1) | ((g & 1) << 1) | ((q >> 1) << 2);
    const unsigned ra = sb + (16 * (g >> 1) + 4 * (g & 1) + (q & 1) + 8 * (q >> 1)) * 256 + (w ^ f) * 32 + p * 8;
#define W_READ(N, hf, X)                                                       \
  {                                                                           \
    _Pragma("unroll") for (int t = 0; t < 4; ++t) {                           \
      const unsigned a_ = (ra ^ (X)) + (hf) * 8192 + t * 32768;               \
      const v2u r0 = trd(a_), r1 = trd(a_ + 512), r2 = trd(a_ + 16384), r3 = trd(a_ + 16384 + 512); \
      w##N[4 * (hf) + t] = v8u{r0.x, r0.y, r1.x, r1.y, r2.x, r2.y, r3.x, r3.y}; \
    }                                                                         \
    pin8<64 * N + 64 + 32 * hf>(w##N[4 * hf]);                                \
    pin8<64 * N + 72 + 32 * hf>(w##N[4 * hf + 1]);                            \
    pin8<64 * N + 80 + 32 * hf>(w##N[4 * hf + 2]);                            \
    pin8<64 * N + 88 + 32 * hf>(w##N[4 * hf + 3]);                            \
  }
#define W_STAGE(M, hf, WAIT, NEXT)                                             \
  {                                                                           \
    WAIT;                                                                     \
    RAW_BAR();                                                                \
    if ((M) == 0 && ATT) { W_READ(0, hf, 0); W_READ(2, hf, 128); }            \
    if ((M) == 1 && ATT) { W_READ(1, hf, 0); }                                \
    if ((M) == 1 && !ATT) { W_READ(0, hf, 0); }                               \
    if ((M) == 2 && !ATT) { W_READ(1, hf, 128); W_READ(2, hf, 0); }           \
    if ((M) == 3) {                                                           \
      _Pragma("unroll") for (int i = 0; i < 8; ++i) {                         \
        const unsigned a_ = rb + (hf) * 8192 + (i & 1) * 65536 + ((4 * wb + (i >> 1)) ^ f) * 32; \
        const v2u r0 = trd(a_), r1 = trd(a_ + 2048);                          \
        fr[hf][i] = v4u{r0.x, r0.y, r1.x, r1.y};                              \
      }                                                                       \
      constexpr int R_ = hf ? 32 : ATT ? 160 : 96;                            \
      pin4<R_>(fr[hf][0]); pin4<R_ + 4>(fr[hf][1]); pin4<R_ + 8>(fr[hf][2]); pin4<R_ + 12>(fr[hf][3]); \
      pin4<R_ + 16>(fr[hf][4]); pin4<R_ + 20>(fr[hf][5]); pin4<R_ + 24>(fr[hf][6]); pin4<R_ + 28>(fr[hf][7]); \
    }                                                                         \
    RAW_BAR();                                                                \
    NEXT;                                                                     \
  }
#define WO_ISSUE(hf)                                                          \
  {                                                                           \
    const char* base_ = (const char*)A.Wo + (size_t)(h * DH + 64 * (hf) + 8 * w) * (DM * 2); \
    _Pragma("unroll") for (int i = 0; i < 8; ++i) {                           \
      const int fd = (i & 1) | (((i >> 2) & 1) << 1) | ((w & 1) << 2);        \
      glds16(base_, (unsigned)(i * 1024 + ((l ^ (fd << 1)) << 4)), sb + w * 16384 + (hf) * 8192 + i * 1024); \
    }                                                                         \
  }
    const unsigned rb = sb + (2 * (g >> 1) + (q >> 1)) * 16384 + (4 * (g & 1) + (q & 1)) * 1024 + p * 8;
    v4u fr[2][8];
#define WO_PUT(hf) \
  _Pragma("unroll") for (int i = 0; i < 8; ++i) *reinterpret_cast<v4u*>(swo + w * 8192 + (hf) * 4096 + i * 512 + l * 8) = fr[hf][i];
    __builtin_amdgcn_s_waitcnt(0x0f70);
    glds16(A.mem0, (unsigned)((c & 7) * DM + 64 * w + 16 * g + 8 * (c >> 3)) * 2, sb + C_SM + w * 1024);
    wqkv_issue<true>(A.Wv, 0, h, w, l, sb);
    wqkv_issue<true>(A.Wv, 1, h, w, l, sb);
    tag = call_tag(A);
    W_STAGE(2, 0, WAIT_VM(8), WO_ISSUE(0));
    W_STAGE(2, 1, WAIT_VM(8), WO_ISSUE(1));
    SST(4);
    if (!ATT) {
      float f2 = 0.f;
      asm volatile("" : "+v"(f2));
#pragma unroll
      for (int t = 0; t < 8; ++t)
#pragma unroll
        for (int e = 0; e < 8; ++e) f2 = dot2sq(w2[t][e], dot2sq(w1[t][e], f2));
      f2 = wave_sum_dpp(f2);
      if (l == 0) { sgf[w] = f2; sgf[16] = 0.f; }
    }
    W_STAGE(3, 0, WAIT_VM(8), wqkv_issue<true>(A.Wq, 0, h, w, l, sb));
    W_STAGE(3, 1, WAIT_VM(8), wqkv_issue<true>(A.Wq, 1, h, w, l, sb));
    SST(5);
    {
      float cn[4] = {0.f, 0.f, 0.f, 0.f};
      asm volatile("" : "+v"(cn[0]), "+v"(cn[1]), "+v"(cn[2]), "+v"(cn[3]));
#pragma unroll
      for (int e = 0; e < 64; ++e) cn[e & 3] = dot2sq(fr[e >> 5][2 * (e & 3) + ((e >> 4) & 1)][(e >> 2) & 3], cn[e & 3]);
      const float m2 = wave_max_dpp(swap_add32(swap_add16(cn[0], cn[1]), swap_add16(cn[2], cn[3])));
      if (l == 0) sgf[8 + w] = m2;
    }
#define G_VALUE()                                                                                                   \
  if (w == 4 && L == 0) {                                                                                           \
    const f32x4 a = *reinterpret_cast<const f32x4*>(sgf + 4), b = *reinterpret_cast<const f32x4*>(sgf + 8);         \
    const f32x4 d = *reinterpret_cast<const f32x4*>(sgf + 12);                                                      \
    const float m2 = fmaxf(fmaxf(fmaxf(b.x, b.y), fmaxf(b.z, b.w)), fmaxf(fmaxf(d.x, d.y), fmaxf(d.z, d.w)));       \
    if (l < 8) __hip_atomic_store((gu64*)(ws + OFF_G) + l * 32 + h, tagged(tag, __float_as_uint(((a.x + a.y) + (a.z + a.w)) * m2 * 1.01f)), RLX_AGENT); \
  }
    W_STAGE(0, 0, WAIT_VM(8), G_VALUE(); wqkv_issue<true>(A.Wk, 0, h, w, l, sb));
    W_STAGE(0, 1, WAIT_VM(8), if (ATT) {
      wqkv_issue<true>(A.Wk, 1, h, w, l, sb);
      wqkv_issue<true>(A.Wk, 1, h, w + 4, l, sb);
    });
    SST(6);
    W_STAGE(1, 0, if (ATT) WAIT_VM(16); else { ready_wait(A, tag, ready_look(A)); __builtin_amdgcn_s_waitcnt(0x0f70); }, WO_PUT(0));
    if (w == 4) {
      u64 gv;
      for (unsigned spins = 0;; ++spins) {
        gv = __hip_atomic_load((gu64*)(ws + OFF_G) + (blockIdx.x & 7) * 32 + (l & 31), RLX_AGENT);
        if (__all(has_tag(tag, (unsigned)gv, (unsigned)(gv >> 32))) || spins > SPIN_CAP) break;
        __builtin_amdgcn_s_sleep(GSL);
      }
      const float gm = wave_max_dpp(hi16((unsigned)(gv >> 32)));
      if (l == 0) sgf[0] = gm * 1.063f;
    }
    W_STAGE(1, 1, WAIT_VM(0), WO_PUT(1));
    SST(7);
#undef G_VALUE
#undef WO_PUT
#undef W_STAGE
#undef W_READ
#undef WO_ISSUE
  }
  WSTAMP2();
  const unsigned sb = lds_base(smem);
  const char* gK = ws + OFF_QK + L * SZ_QK + ((size_t)h * 16 + 2 * w) * 1024;
  const char* gV = ws + OFF_VF + L * SZ_O + ((size_t)h * 8 + 2 * w - 8) * 1024;
  v8u pv;
  {
    LANE_IDS();
    const unsigned voff = g * 256 + c * 8, wp = sb + w * 16384 + l * 16;
    pv[0] = (c & 8) ? 0xEEEE : 0x4444;
    pv[1] = sb + C_SM + l * 16;
    pv[2] = ATT ? sb + C_SQ + ((c & 7) * QS + 8 * g) * 2 : voff;
    pv[3] = ATT ? sb + C_SK + ((4 * ((c >> 2) & 1) + 2 * (c >> 3) + (c & 1)) * QS + 8 * g) * 2 : wp;
    pv[4] = sb + (c < 8 ? C_SO : C_SQ) + w * 256 + ((c & 7) + 8 * (g >> 1)) * 16 + 8 * (g & 1);
    pv[5] = voff;
    pv[6] = wp;
    pv[7] = sb + C_SO + (l >> 5) * 1024 + w * 256 + (l & 31) * 8;
  }
  if (!ATT) {
    if (!wait_n<4, 1>(FQ + h * 32, 1, TMO)) return;
    const unsigned lo = lane_id() * 16, d = sb + C_KT + (2 * w - 8) * 1024;
    glds16c(gK, lo, d);
    glds16c(gK + 1024, lo, d + 1024);
    glds16c(gV, lo, d + 8192);
    glds16c(gV + 1024, lo, d + 9216);
  }
  const unsigned gw2 = __builtin_amdgcn_readfirstlane(__float_as_uint(sgf[0]));
  DRAIN();
  SST(8);
  const v4u rsA = mk_srd(ws + OFF_ACC + L * SZ_ACC, SZ_ACC);
  const v4u rsF = mk_srd(FQ, 64 * 32 * 32 * 4);
  const v4u rsMK = mk_srd(ws + OFF_MEMK + L * SZ_MEM, SZ_MEM);
  const v4u rsMV = mk_srd(ws + OFF_MEMV + L * SZ_MEM, SZ_MEM);
  const char* pAB = ws + OFF_ACC + L * SZ_ACC + (wb - w) * 4096;
  const char* pGK = gK + NH * 16 * 1024;
  const char* pGV = gV + NH * 8 * 1024;
  const v4u rsT = mk_srd(FL + FL_FK + L * 32, 64 * 1024 - 128);
  unsigned sc[8];
  sc[0] = (32 + h) * 128;
  sc[1] = (h * 4 + w - 4) * 512;
  sc[2] = (h * 4 + w - 4) * 512;
  sc[3] = NSEG - 1;
  sc[4] = gw2;
  sc[5] = w;
  sc[6] = sb;
  sc[7] = h == 0 ? 0xd4000000u - 2u - (1u << 29) : 0xd4000000u;
  const char* dbg = ws;
#define CHAIN_OPS                                                                                                          \
  ::"{v[64:71]}"(w0[0]), "{v[72:79]}"(w0[1]), "{v[80:87]}"(w0[2]), "{v[88:95]}"(w0[3]), "{v[96:103]}"(w0[4]),              \
      "{v[104:111]}"(w0[5]), "{v[112:119]}"(w0[6]), "{v[120:127]}"(w0[7]), "{v[128:135]}"(w1[0]), "{v[136:143]}"(w1[1]),    \
      "{v[144:151]}"(w1[2]), "{v[152:159]}"(w1[3]), "{v[160:167]}"(w1[4]), "{v[168:175]}"(w1[5]), "{v[176:183]}"(w1[6]),    \
      "{v[184:191]}"(w1[7]), "{v[192:199]}"(w2[0]), "{v[200:207]}"(w2[1]), "{v[208:215]}"(w2[2]), "{v[216:223]}"(w2[3]),    \
      "{v[224:231]}"(w2[4]), "{v[232:239]}"(w2[5]), "{v[240:247]}"(w2[6]), "{v[248:255]}"(w2[7]), "{v[0:7]}"(pv),          \
      "{s[40:43]}"(rsA), "{s[44:47]}"(rsF), "{s[48:51]}"(rsMK), "{s[52:55]}"(rsMV), "{s[56:57]}"(pAB), "{s60}"(w * 4096), "{s61}"(ATT ? h * 1024 : w != 4 ? 0x40000000 : L ? (PNL - 1) * 1024 - 128 : 128 - (PNL + 2) * 1024), \
      "{s[62:63]}"(pGK), "{s[64:65]}"(pGV), "{s66}"(0), "{s67}"(w == 4 ? (h >> 3) * 256 + (h & 7) * 8 : 0x40000000), "{s68}"(sc[0]), "{s69}"(sc[1]), "{s70}"(sc[2]),    \
      "{s71}"(sc[3]), "{s72}"(sc[4]), "{s73}"(sc[5]), "{s74}"(sc[6]), "{s75}"(sc[7]), "{s[36:37]}"(dbg),                   \
      "{s[0:3]}"(rsT), "{s79}"((h >> 3) * 16 + (h & 7))                                                                        \
      : "memory", "vcc", "scc", "s99", "v63"
  if (ATT) asm volatile("s_mov_b64 exec, -1\ns_nop 4\n" CHAIN_ASM_ATT CHAIN_OPS);
  else asm volatile("s_mov_b64 exec, -1\ns_nop 4\n" CHAIN_ASM_VO CHAIN_OPS);
#undef CHAIN_OPS
#undef LANE_IDS
}

__device__ __forceinline__ void role_chain(const MegaArgs& A, char* smem, const int L, const int h, const int rep) {
  if (A.wv < 4) role_chain_t<true>(A, smem, L, h, rep);
  else role_chain_t<false>(A, smem, L, h, rep);
}

constexpr int AW_SO = 0, AW_TK = 32768, AW_CNT = AW_TK + 98304;
__device__ __forceinline__ unsigned umin3(unsigned a, unsigned b, unsigned c) {
  return __builtin_elementwise_min(__builtin_elementwise_min(a, b), c);
}
#define AW_ASM_X \
  "s_mov_b32 s61, 0\n" \
  "s_mov_b32 s67, 1\n" \
  "s_mov_b32 s68, 0\n" \
  "s_mov_b32 s70, 0\n" \
  "s_mov_b32 s73, 0\n" \
  "s_mov_b32 s71, 0x3e0293ee\n" \
  "s_mov_b32 s72, 0xbe0293ee\n" \
  "s_mov_b32 s65, 49152\n" \
  "s_mov_b32 s66, 16384\n" \
  "v_mov_b32 v38, s58\n" \
  "s_memrealtime s[38:39]\n" \
  "s_waitcnt lgkmcnt(0)\n" \
  "s_mov_b32 s69, s38\n" \
  "s_setprio 3\n" \
  ".LCX_TOP_%=:\n" \
  "s_mov_b32 s62, 0x200000\n" \
  "s_cmp_ge_u32 s73, s67\n" \
  "s_cbranch_scc1 .LCX_TOK_%=\n" \
  "s_cmp_eq_u32 s61, 0\n" \
  "s_cbranch_scc1 .LCX_SPIN_%=\n" \
  "s_waitcnt vmcnt(0)\n" \
  "v_mov_b32 v46, s59\n" \
  "v_mov_b32 v47, 1\n" \
  "s_mov_b64 exec, 1\n" \
  "ds_add_rtn_u32 v46, v46, v47\n" \
  "s_mov_b64 exec, -1\n" \
  "s_xor_b32 s59, s59, 4\n" \
  "s_mov_b32 s61, 0\n" \
  "s_waitcnt lgkmcnt(0)\n" \
  "v_readfirstlane_b32 s63, v46\n" \
  "v_lshlrev_b32 v46, 5, v5\n" \
  "s_and_b32 s63, s63, 7\n" \
  "s_cmp_eq_u32 s63, 7\n" \
  "s_cbranch_scc0 .LCX_W1_%=\n" \
  "s_mov_b64 exec, 15\n" \
  "global_store_dword v46, v47, s[52:53] sc1\n" \
  "s_mov_b64 exec, -1\n" \
  ".LCX_W1_%=:\n" \
  "s_add_u32 s52, s52, 2048\n" \
  "s_addc_u32 s53, s53, 0\n" \
  ".LCX_SPIN_%=:\n" \
  "s_sleep 2\n" \
  "s_memrealtime s[38:39]\n" \
  "ds_read_b32 v37, v38\n" \
  "s_sub_u32 s62, s62, 1\n" \
  "s_cbranch_scc1 .LCX_END_%=\n" \
  "s_waitcnt lgkmcnt(0)\n" \
  "v_readfirstlane_b32 s73, v37\n" \
  "s_add_u32 s38, s38, 80\n" \
  "s_bfe_u32 s64, s73, 0x80008\n" \
  "s_and_b32 s73, s73, 0xff\n" \
  "s_min_u32 s73, s73, s64\n" \
  "s_cmp_ge_u32 s73, s67\n" \
  "s_cbranch_scc0 .LCX_SPIN_%=\n" \
  "s_cmp_eq_u32 s70, 0\n" \
  "s_cbranch_scc1 .LCX_TOK_%=\n" \
  "s_sub_u32 s63, s38, s69\n" \
  "s_max_i32 s63, s63, 0\n" \
  "s_add_u32 s69, s69, s63\n" \
  ".LCX_TOK_%=:\n" \
  "ds_read_b128 v[48:51], v6 offset:0\n" \
  "ds_read_b128 v[64:67], v0 offset:8192\n" \
  "ds_read_b128 v[80:83], v0 offset:12288\n" \
  "ds_read_b128 v[52:55], v6 offset:1024\n" \
  "ds_read_b128 v[68:71], v0 offset:9216\n" \
  "ds_read_b128 v[84:87], v0 offset:13312\n" \
  "ds_read_b128 v[56:59], v6 offset:2048\n" \
  "ds_read_b128 v[72:75], v0 offset:10240\n" \
  "ds_read_b128 v[88:91], v0 offset:14336\n" \
  "ds_read_b128 v[60:63], v6 offset:3072\n" \
  "ds_read_b128 v[76:79], v0 offset:11264\n" \
  "ds_read_b128 v[92:95], v0 offset:15360\n" \
  "s_waitcnt lgkmcnt(9)\n" \
  "v_mfma_f32_16x16x32_bf16 v[20:23], v[64:67], v[48:51], v[8:11]\n" \
  "v_mfma_f32_16x16x32_bf16 v[24:27], v[80:83], v[48:51], v[12:15]\n" \
  "ds_read_b128 v[96:99], v0 offset:16384\n" \
  "ds_read_b128 v[100:103], v0 offset:17408\n" \
  "s_waitcnt lgkmcnt(8)\n" \
  "v_mfma_f32_16x16x32_bf16 v[20:23], v[68:71], v[52:55], v[20:23]\n" \
  "v_mfma_f32_16x16x32_bf16 v[24:27], v[84:87], v[52:55], v[24:27]\n" \
  "ds_read_b128 v[104:107], v0 offset:18432\n" \
  "ds_read_b128 v[108:111], v0 offset:19456\n" \
  "s_waitcnt lgkmcnt(7)\n" \
  "v_mfma_f32_16x16x32_bf16 v[20:23], v[72:75], v[56:59], v[20:23]\n" \
  "v_mfma_f32_16x16x32_bf16 v[24:27], v[88:91], v[56:59], v[24:27]\n" \
  "ds_read_b128 v[112:115], v0 offset:20480\n" \
  "ds_read_b128 v[116:119], v0 offset:21504\n" \
  "s_waitcnt lgkmcnt(6)\n" \
  "v_mfma_f32_16x16x32_bf16 v[20:23], v[76:79], v[60:63], v[20:23]\n" \
  "v_mfma_f32_16x16x32_bf16 v[24:27], v[92:95], v[60:63], v[24:27]\n" \
  "ds_read_b128 v[120:123], v0 offset:22528\n" \
  "ds_read_b128 v[124:127], v0 offset:23552\n" \
  "s_add_u32 s67, s67, 1\n" \
  "v_add_u32 v0, s65, v0\n" \
  "v_add_u32 v6, s65, v6\n" \
  "s_sub_u32 s65, 0, s65\n" \
  "s_nop 1\n" \
  "v_max3_f32 v32, v20, v21, v22\n" \
  "v_max3_f32 v33, v23, v24, v25\n" \
  "v_max3_f32 v32, v32, v26, v27\n" \
  "v_max_f32 v32, v32, v33\n" \
  "v_mov_b32 v33, v32\n" \
  "s_nop 1\n" \
  "v_permlane16_swap_b32 v32, v33\n" \
  "v_max_f32 v32, v32, v33\n" \
  "v_mov_b32 v33, v32\n" \
  "s_nop 1\n" \
  "v_permlane32_swap_b32 v32, v33\n" \
  "v_max_f32 v32, v32, v33\n" \
  "v_mul_f32 v33, s72, v32\n" \
  "v_fma_f32 v20, v20, s71, v33\n" \
  "v_fma_f32 v21, v21, s71, v33\n" \
  "v_fma_f32 v22, v22, s71, v33\n" \
  "v_fma_f32 v23, v23, s71, v33\n" \
  "v_fma_f32 v24, v24, s71, v33\n" \
  "v_fma_f32 v25, v25, s71, v33\n" \
  "v_fma_f32 v26, v26, s71, v33\n" \
  "v_fma_f32 v27, v27, s71, v33\n" \
  "v_exp_f32 v20, v20\n" \
  "v_exp_f32 v21, v21\n" \
  "v_exp_f32 v22, v22\n" \
  "v_exp_f32 v23, v23\n" \
  "v_exp_f32 v24, v24\n" \
  "v_exp_f32 v25, v25\n" \
  "v_exp_f32 v26, v26\n" \
  "v_exp_f32 v27, v27\n" \
  "v_add_f32 v34, v20, v21\n" \
  "v_add_f32 v33, v22, v23\n" \
  "v_add_f32 v36, v24, v25\n" \
  "v_add_f32 v34, v34, v33\n" \
  "v_add_f32 v33, v26, v27\n" \
  "v_add_f32 v34, v34, v36\n" \
  "v_add_f32 v34, v34, v33\n" \
  "s_waitcnt vmcnt(0) lgkmcnt(0)\n" \
  "s_cmp_eq_u32 s61, 0\n" \
  "s_cbranch_scc1 .LCX_NP_%=\n" \
  "v_mov_b32 v46, s59\n" \
  "v_mov_b32 v47, 1\n" \
  "s_mov_b64 exec, 1\n" \
  "ds_add_rtn_u32 v46, v46, v47\n" \
  "s_mov_b64 exec, -1\n" \
  "s_xor_b32 s59, s59, 4\n" \
  "s_mov_b32 s61, 0\n" \
  "s_waitcnt lgkmcnt(0)\n" \
  "v_readfirstlane_b32 s63, v46\n" \
  "v_lshlrev_b32 v46, 5, v5\n" \
  "s_and_b32 s63, s63, 7\n" \
  "s_cmp_eq_u32 s63, 7\n" \
  "s_cbranch_scc0 .LCX_W2_%=\n" \
  "s_mov_b64 exec, 15\n" \
  "global_store_dword v46, v47, s[52:53] sc1\n" \
  "s_mov_b64 exec, -1\n" \
  ".LCX_W2_%=:\n" \
  "s_add_u32 s52, s52, 2048\n" \
  "s_addc_u32 s53, s53, 0\n" \
  ".LCX_NP_%=:\n" \
  "s_cmp_lg_u32 s68, 0\n" \
  "s_cbranch_scc1 .LCX_TEST_%=\n" \
  ".LCX_WF_%=:\n" \
  "s_memrealtime s[38:39]\n" \
  "s_waitcnt lgkmcnt(0)\n" \
  "s_sub_u32 s63, s69, s38\n" \
  "s_cmp_le_i32 s63, 0\n" \
  "s_cbranch_scc1 .LCX_GO_%=\n" \
  "s_cmp_lt_i32 s63, 12\n" \
  "s_cbranch_scc1 .LCX_WF_%=\n" \
  "s_sleep 3\n" \
  "s_branch .LCX_WF_%=\n" \
  ".LCX_GO_%=:\n" \
  "v_mov_b32 v35, 0\n" \
  "global_load_dwordx4 v[128:131], v2, s[48:49] offset:0 sc1\n" \
  "global_load_dwordx4 v[132:135], v2, s[48:49] offset:512 sc1\n" \
  "global_load_dwordx4 v[136:139], v2, s[48:49] offset:1024 sc1\n" \
  "global_load_dwordx4 v[140:143], v2, s[48:49] offset:1536 sc1\n" \
  "global_load_dwordx4 v[144:147], v3, s[48:49] offset:0 sc1\n" \
  "global_load_dwordx4 v[148:151], v3, s[48:49] offset:512 sc1\n" \
  "global_load_dwordx4 v[152:155], v3, s[48:49] offset:1024 sc1\n" \
  "global_load_dwordx4 v[156:159], v3, s[48:49] offset:1536 sc1\n" \
  "global_load_dword v35, v35, s[50:51] sc1\n" \
  "s_sub_u32 s62, s62, 8\n" \
  "s_cbranch_scc1 .LCX_END_%=\n" \
  "s_waitcnt vmcnt(0)\n" \
  ".LCX_TEST_%=:\n" \
  "v_mfma_f32_16x16x32_bf16 v[28:31], v[128:131], v[48:51], v[16:19]\n" \
  "v_min3_u32 v33, v128, v129, v130\n" \
  "v_min3_u32 v36, v131, v132, v133\n" \
  "v_min3_u32 v33, v33, v134, v135\n" \
  "v_min3_u32 v36, v36, v136, v137\n" \
  "v_mfma_f32_16x16x32_bf16 v[28:31], v[132:135], v[52:55], v[28:31]\n" \
  "v_min3_u32 v33, v33, v138, v139\n" \
  "v_min3_u32 v36, v36, v140, v141\n" \
  "v_min3_u32 v33, v33, v142, v143\n" \
  "v_min3_u32 v36, v36, v144, v145\n" \
  "v_mfma_f32_16x16x32_bf16 v[28:31], v[136:139], v[56:59], v[28:31]\n" \
  "v_min3_u32 v33, v33, v146, v147\n" \
  "v_min3_u32 v36, v36, v148, v149\n" \
  "v_min3_u32 v33, v33, v150, v151\n" \
  "v_min3_u32 v36, v36, v152, v153\n" \
  "v_mfma_f32_16x16x32_bf16 v[28:31], v[140:143], v[60:63], v[28:31]\n" \
  "v_min3_u32 v33, v33, v154, v155\n" \
  "v_min3_u32 v36, v36, v156, v157\n" \
  "v_min3_u32 v33, v33, v158, v159\n" \
  "v_min_u32 v33, v33, v36\n" \
  "v_cmp_eq_u32 vcc, 0, v33\n" \
  "v_readfirstlane_b32 s64, v35\n" \
  "s_mov_b32 s68, 0\n" \
  "s_cmp_eq_u64 vcc, 0\n" \
  "s_cbranch_scc0 .LCX_FAIL_%=\n" \
  "v_max3_f32 v36, v28, v29, v30\n" \
  "v_max3_f32 v36, v36, v31, v32\n" \
  "v_mov_b32 v33, v36\n" \
  "s_cmp_eq_u32 s64, 0\n" \
  "s_cselect_b32 s64, s38, s64\n" \
  "v_permlane16_swap_b32 v36, v33\n" \
  "v_max_f32 v36, v36, v33\n" \
  "v_mov_b32 v33, v36\n" \
  "s_sub_u32 s63, s64, s70\n" \
  "s_mov_b32 s70, s64\n" \
  "v_permlane32_swap_b32 v36, v33\n" \
  "v_max_f32 v36, v36, v33\n" \
  "v_mul_f32 v36, s72, v36\n" \
  "v_fma_f32 v32, v32, s71, v36\n" \
  "v_fma_f32 v28, v28, s71, v36\n" \
  "v_fma_f32 v29, v29, s71, v36\n" \
  "v_fma_f32 v30, v30, s71, v36\n" \
  "v_fma_f32 v31, v31, s71, v36\n" \
  "v_exp_f32 v32, v32\n" \
  "v_exp_f32 v28, v28\n" \
  "v_exp_f32 v29, v29\n" \
  "v_exp_f32 v30, v30\n" \
  "v_exp_f32 v31, v31\n" \
  "v_fma_f32 v34, v34, v32, v28\n" \
  "v_add_f32 v33, v29, v30\n" \
  "v_add_f32 v34, v34, v31\n" \
  "v_add_f32 v34, v34, v33\n" \
  "v_mov_b32 v33, v34\n" \
  "s_min_u32 s63, s63, 250\n" \
  "s_add_u32 s69, s64, s63\n" \
  "v_permlane16_swap_b32 v34, v33\n" \
  "v_add_f32 v34, v34, v33\n" \
  "v_mov_b32 v33, v34\n" \
  "s_add_u32 s69, s69, -56\n" \
  "s_add_u32 s48, s48, 0x10000\n" \
  "s_addc_u32 s49, s49, 0\n" \
  "v_permlane32_swap_b32 v34, v33\n" \
  "v_add_f32 v34, v34, v33\n" \
  "v_rcp_f32 v34, v34\n" \
  "s_add_u32 s50, s50, 1024\n" \
  "s_addc_u32 s51, s51, 0\n" \
  "v_mul_f32 v32, v32, v34\n" \
  "v_mul_f32 v20, v20, v32\n" \
  "v_mul_f32 v21, v21, v32\n" \
  "v_mul_f32 v22, v22, v32\n" \
  "v_mul_f32 v23, v23, v32\n" \
  "v_mul_f32 v24, v24, v32\n" \
  "v_mul_f32 v25, v25, v32\n" \
  "v_mul_f32 v26, v26, v32\n" \
  "v_mul_f32 v27, v27, v32\n" \
  "v_cvt_pk_bf16_f32 v40, v20, v21\n" \
  "v_cvt_pk_bf16_f32 v41, v22, v23\n" \
  "v_cvt_pk_bf16_f32 v42, v24, v25\n" \
  "v_cvt_pk_bf16_f32 v43, v26, v27\n" \
  "v_mul_f32 v28, v28, v34\n" \
  "v_mul_f32 v29, v29, v34\n" \
  "v_mfma_f32_16x16x32_bf16 v[64:67], v[96:99], v[40:43], 0\n" \
  "v_mul_f32 v30, v30, v34\n" \
  "v_mfma_f32_16x16x32_bf16 v[68:71], v[100:103], v[40:43], 0\n" \
  "v_mul_f32 v31, v31, v34\n" \
  "v_mfma_f32_16x16x32_bf16 v[72:75], v[104:107], v[40:43], 0\n" \
  "v_cvt_pk_bf16_f32 v44, v28, v29\n" \
  "v_mfma_f32_16x16x32_bf16 v[76:79], v[108:111], v[40:43], 0\n" \
  "v_cvt_pk_bf16_f32 v45, v30, v31\n" \
  "v_mfma_f32_16x16x32_bf16 v[80:83], v[112:115], v[40:43], 0\n" \
  "v_mfma_f32_16x16x32_bf16 v[84:87], v[116:119], v[40:43], 0\n" \
  "v_mfma_f32_16x16x32_bf16 v[88:91], v[120:123], v[40:43], 0\n" \
  "v_mfma_f32_16x16x32_bf16 v[92:95], v[124:127], v[40:43], 0\n" \
  "s_memrealtime s[38:39]\n" \
  "ds_read_b32 v37, v38\n" \
  "v_mfma_f32_16x16x16_bf16 v[64:67], v[144:145], v[44:45], v[64:67]\n" \
  "v_mfma_f32_16x16x16_bf16 v[68:71], v[148:149], v[44:45], v[68:71]\n" \
  "v_mfma_f32_16x16x16_bf16 v[72:75], v[152:153], v[44:45], v[72:75]\n" \
  "v_mfma_f32_16x16x16_bf16 v[76:79], v[156:157], v[44:45], v[76:79]\n" \
  "v_mfma_f32_16x16x16_bf16 v[80:83], v[146:147], v[44:45], v[80:83]\n" \
  "v_mfma_f32_16x16x16_bf16 v[84:87], v[150:151], v[44:45], v[84:87]\n" \
  "v_mfma_f32_16x16x16_bf16 v[88:91], v[154:155], v[44:45], v[88:91]\n" \
  "v_mfma_f32_16x16x16_bf16 v[92:95], v[158:159], v[44:45], v[92:95]\n" \
  "s_nop 0\n" \
  "v_cvt_pk_bf16_f32 v160, v64, v65\n" \
  "v_cvt_pk_bf16_f32 v161, v66, v67\n" \
  "v_cvt_pk_bf16_f32 v162, v68, v69\n" \
  "v_cvt_pk_bf16_f32 v163, v70, v71\n" \
  "ds_write_b128 v7, v[160:163] offset:0\n" \
  "v_cvt_pk_bf16_f32 v164, v72, v73\n" \
  "v_cvt_pk_bf16_f32 v165, v74, v75\n" \
  "v_cvt_pk_bf16_f32 v166, v76, v77\n" \
  "v_cvt_pk_bf16_f32 v167, v78, v79\n" \
  "ds_write_b128 v7, v[164:167] offset:1024\n" \
  "v_cvt_pk_bf16_f32 v168, v80, v81\n" \
  "v_cvt_pk_bf16_f32 v169, v82, v83\n" \
  "v_cvt_pk_bf16_f32 v170, v84, v85\n" \
  "v_cvt_pk_bf16_f32 v171, v86, v87\n" \
  "ds_write_b128 v7, v[168:171] offset:2048\n" \
  "v_cvt_pk_bf16_f32 v172, v88, v89\n" \
  "v_cvt_pk_bf16_f32 v173, v90, v91\n" \
  "v_cvt_pk_bf16_f32 v174, v92, v93\n" \
  "v_cvt_pk_bf16_f32 v175, v94, v95\n" \
  "ds_write_b128 v7, v[172:175] offset:3072\n" \
  "v_add_u32 v7, s66, v7\n" \
  "s_waitcnt lgkmcnt(0)\n" \
  "s_barrier\n" \
  "ds_read_b128 v[48:51], v1 offset:0\n" \
  "ds_read_b128 v[52:55], v1 offset:4096\n" \
  "ds_read_b128 v[56:59], v1 offset:1024\n" \
  "ds_read_b128 v[60:63], v1 offset:5120\n" \
  "ds_read_b128 v[64:67], v1 offset:2048\n" \
  "ds_read_b128 v[68:71], v1 offset:6144\n" \
  "ds_read_b128 v[72:75], v1 offset:3072\n" \
  "ds_read_b128 v[76:79], v1 offset:7168\n" \
  "ds_read_b128 v[80:83], v1 offset:8192\n" \
  "ds_read_b128 v[84:87], v1 offset:12288\n" \
  "ds_read_b128 v[88:91], v1 offset:9216\n" \
  "ds_read_b128 v[92:95], v1 offset:13312\n" \
  "ds_read_b128 v[96:99], v1 offset:10240\n" \
  "ds_read_b128 v[100:103], v1 offset:14336\n" \
  "v_readfirstlane_b32 s73, v37\n" \
  "s_sub_u32 s63, s38, s69\n" \
  "s_bfe_u32 s64, s73, 0x80010\n" \
  "s_sub_u32 s64, s64, s67\n" \
  "s_or_b32 s63, s63, s64\n" \
  "s_bfe_u32 s64, s73, 0x80008\n" \
  "s_and_b32 s73, s73, 0xff\n" \
  "s_min_u32 s73, s73, s64\n" \
  "s_or_b32 s63, s63, s68\n" \
  "s_cmp_lt_i32 s63, 0\n" \
  "s_cbranch_scc1 .LCX_E3_%=\n" \
  "v_mov_b32 v35, 0\n" \
  "global_load_dwordx4 v[128:131], v2, s[48:49] offset:0 sc1\n" \
  "global_load_dwordx4 v[132:135], v2, s[48:49] offset:512 sc1\n" \
  "global_load_dwordx4 v[136:139], v2, s[48:49] offset:1024 sc1\n" \
  "global_load_dwordx4 v[140:143], v2, s[48:49] offset:1536 sc1\n" \
  "global_load_dwordx4 v[144:147], v3, s[48:49] offset:0 sc1\n" \
  "global_load_dwordx4 v[148:151], v3, s[48:49] offset:512 sc1\n" \
  "global_load_dwordx4 v[152:155], v3, s[48:49] offset:1024 sc1\n" \
  "global_load_dwordx4 v[156:159], v3, s[48:49] offset:1536 sc1\n" \
  "global_load_dword v35, v35, s[50:51] sc1\n" \
  "s_mov_b32 s68, -1\n" \
  ".LCX_E3_%=:\n" \
  "s_waitcnt lgkmcnt(12)\n" \
  "v_mfma_f32_16x16x32_bf16 v[112:115], v[192:195], v[48:51], 0\n" \
  "v_mfma_f32_16x16x32_bf16 v[116:119], v[224:227], v[48:51], 0\n" \
  "v_mfma_f32_16x16x32_bf16 v[120:123], v[192:195], v[52:55], 0\n" \
  "v_mfma_f32_16x16x32_bf16 v[124:127], v[224:227], v[52:55], 0\n" \
  "ds_read_b128 v[104:107], v1 offset:11264\n" \
  "ds_read_b128 v[108:111], v1 offset:15360\n" \
  "s_waitcnt lgkmcnt(12)\n" \
  "v_mfma_f32_16x16x32_bf16 v[112:115], v[196:199], v[56:59], v[112:115]\n" \
  "v_mfma_f32_16x16x32_bf16 v[116:119], v[228:231], v[56:59], v[116:119]\n" \
  "v_mfma_f32_16x16x32_bf16 v[120:123], v[196:199], v[60:63], v[120:123]\n" \
  "v_mfma_f32_16x16x32_bf16 v[124:127], v[228:231], v[60:63], v[124:127]\n" \
  "s_waitcnt lgkmcnt(10)\n" \
  "v_mfma_f32_16x16x32_bf16 v[112:115], v[200:203], v[64:67], v[112:115]\n" \
  "v_mfma_f32_16x16x32_bf16 v[116:119], v[232:235], v[64:67], v[116:119]\n" \
  "v_mfma_f32_16x16x32_bf16 v[120:123], v[200:203], v[68:71], v[120:123]\n" \
  "v_mfma_f32_16x16x32_bf16 v[124:127], v[232:235], v[68:71], v[124:127]\n" \
  "s_waitcnt lgkmcnt(8)\n" \
  "v_mfma_f32_16x16x32_bf16 v[112:115], v[204:207], v[72:75], v[112:115]\n" \
  "v_mfma_f32_16x16x32_bf16 v[116:119], v[236:239], v[72:75], v[116:119]\n" \
  "v_mfma_f32_16x16x32_bf16 v[120:123], v[204:207], v[76:79], v[120:123]\n" \
  "v_mfma_f32_16x16x32_bf16 v[124:127], v[236:239], v[76:79], v[124:127]\n" \
  "s_waitcnt lgkmcnt(6)\n" \
  "v_mfma_f32_16x16x32_bf16 v[112:115], v[208:211], v[80:83], v[112:115]\n" \
  "v_mfma_f32_16x16x32_bf16 v[116:119], v[240:243], v[80:83], v[116:119]\n" \
  "v_mfma_f32_16x16x32_bf16 v[120:123], v[208:211], v[84:87], v[120:123]\n" \
  "v_mfma_f32_16x16x32_bf16 v[124:127], v[240:243], v[84:87], v[124:127]\n" \
  "s_waitcnt lgkmcnt(4)\n" \
  "v_mfma_f32_16x16x32_bf16 v[112:115], v[212:215], v[88:91], v[112:115]\n" \
  "v_mfma_f32_16x16x32_bf16 v[116:119], v[244:247], v[88:91], v[116:119]\n" \
  "v_mfma_f32_16x16x32_bf16 v[120:123], v[212:215], v[92:95], v[120:123]\n" \
  "v_mfma_f32_16x16x32_bf16 v[124:127], v[244:247], v[92:95], v[124:127]\n" \
  "s_memrealtime s[38:39]\n" \
  "ds_read_b32 v37, v38\n" \
  "s_waitcnt lgkmcnt(3)\n" \
  "v_mfma_f32_16x16x32_bf16 v[112:115], v[216:219], v[96:99], v[112:115]\n" \
  "v_mfma_f32_16x16x32_bf16 v[116:119], v[248:251], v[96:99], v[116:119]\n" \
  "v_mfma_f32_16x16x32_bf16 v[120:123], v[216:219], v[100:103], v[120:123]\n" \
  "v_mfma_f32_16x16x32_bf16 v[124:127], v[248:251], v[100:103], v[124:127]\n" \
  "s_waitcnt lgkmcnt(0)\n" \
  "v_mfma_f32_16x16x32_bf16 v[112:115], v[220:223], v[104:107], v[112:115]\n" \
  "v_mfma_f32_16x16x32_bf16 v[116:119], v[252:255], v[104:107], v[116:119]\n" \
  "v_mfma_f32_16x16x32_bf16 v[120:123], v[220:223], v[108:111], v[120:123]\n" \
  "v_mfma_f32_16x16x32_bf16 v[124:127], v[252:255], v[108:111], v[124:127]\n" \
  "v_add_u32 v1, s66, v1\n" \
  "s_sub_u32 s66, 0, s66\n" \
  "s_add_u32 s64, s54, 16384\n" \
  "s_mov_b32 s61, 1\n" \
  "s_nop 1\n" \
  "v_cvt_pk_bf16_f32 v20, v112, v113\n" \
  "v_cvt_pk_bf16_f32 v21, v114, v115\n" \
  "v_cvt_pk_bf16_f32 v22, v116, v117\n" \
  "v_cvt_pk_bf16_f32 v23, v118, v119\n" \
  "buffer_store_dwordx4 v[20:23], v4, s[40:43], s54 offen sc1\n" \
  "v_cvt_pk_bf16_f32 v24, v120, v121\n" \
  "v_cvt_pk_bf16_f32 v25, v122, v123\n" \
  "v_cvt_pk_bf16_f32 v26, v124, v125\n" \
  "v_cvt_pk_bf16_f32 v27, v126, v127\n" \
  "buffer_store_dwordx4 v[24:27], v4, s[40:43], s64 offen sc1\n" \
  "v_readfirstlane_b32 s73, v37\n" \
  "s_sub_u32 s63, s38, s69\n" \
  "s_bfe_u32 s64, s73, 0x80010\n" \
  "s_sub_u32 s64, s64, s67\n" \
  "s_or_b32 s63, s63, s64\n" \
  "s_bfe_u32 s64, s73, 0x80008\n" \
  "s_and_b32 s73, s73, 0xff\n" \
  "s_min_u32 s73, s73, s64\n" \
  "s_or_b32 s63, s63, s68\n" \
  "s_cmp_lt_i32 s63, 0\n" \
  "s_cbranch_scc1 .LCX_E4_%=\n" \
  "v_mov_b32 v35, 0\n" \
  "global_load_dwordx4 v[128:131], v2, s[48:49] offset:0 sc1\n" \
  "global_load_dwordx4 v[132:135], v2, s[48:49] offset:512 sc1\n" \
  "global_load_dwordx4 v[136:139], v2, s[48:49] offset:1024 sc1\n" \
  "global_load_dwordx4 v[140:143], v2, s[48:49] offset:1536 sc1\n" \
  "global_load_dwordx4 v[144:147], v3, s[48:49] offset:0 sc1\n" \
  "global_load_dwordx4 v[148:151], v3, s[48:49] offset:512 sc1\n" \
  "global_load_dwordx4 v[152:155], v3, s[48:49] offset:1024 sc1\n" \
  "global_load_dwordx4 v[156:159], v3, s[48:49] offset:1536 sc1\n" \
  "global_load_dword v35, v35, s[50:51] sc1\n" \
  "s_mov_b32 s68, -1\n" \
  ".LCX_E4_%=:\n" \
  "s_add_u32 s54, s54, 0x80000\n" \
  "s_sub_u32 s60, s60, 1\n" \
  "s_cmp_lg_u32 s60, 0\n" \
  "s_cbranch_scc1 .LCX_TOP_%=\n" \
  "s_waitcnt vmcnt(0)\n" \
  "v_mov_b32 v46, s59\n" \
  "v_mov_b32 v47, 1\n" \
  "s_mov_b64 exec, 1\n" \
  "ds_add_rtn_u32 v46, v46, v47\n" \
  "s_mov_b64 exec, -1\n" \
  "s_xor_b32 s59, s59, 4\n" \
  "s_mov_b32 s61, 0\n" \
  "s_waitcnt lgkmcnt(0)\n" \
  "v_readfirstlane_b32 s63, v46\n" \
  "v_lshlrev_b32 v46, 5, v5\n" \
  "s_and_b32 s63, s63, 7\n" \
  "s_cmp_eq_u32 s63, 7\n" \
  "s_cbranch_scc0 .LCX_W5_%=\n" \
  "s_mov_b64 exec, 15\n" \
  "global_store_dword v46, v47, s[52:53] sc1\n" \
  "s_mov_b64 exec, -1\n" \
  ".LCX_W5_%=:\n" \
  "s_add_u32 s52, s52, 2048\n" \
  "s_addc_u32 s53, s53, 0\n" \
  ".LCX_END_%=:\n" \
  "s_endpgm\n" \
  ".LCX_FAIL_%=:\n" \
  "s_add_u32 s69, s64, -56\n" \
  "s_cmp_lg_u32 s64, 0\n" \
  "s_cbranch_scc1 .LCX_WF_%=\n" \
  "s_sleep 4\n" \
  "global_load_dword v35, v35, s[50:51] sc1\n" \
  "s_sub_u32 s62, s62, 8\n" \
  "s_cbranch_scc1 .LCX_END_%=\n" \
  "s_waitcnt vmcnt(0)\n" \
  "v_readfirstlane_b32 s64, v35\n" \
  "s_branch .LCX_FAIL_%=\n"
#define AW_ASM_Y \
  "s_mov_b32 s67, 0\n" \
  "s_mov_b32 s68, 2\n" \
  "s_mov_b32 s69, 0\n" \
  "s_mov_b32 s65, 49152\n" \
  "s_mov_b32 s66, 16384\n" \
  "v_mov_b32 v5, s58\n" \
  "v_add_u32 v3, 4096, v2\n" \
  "s_mov_b32 s62, 0x40000\n" \
  "s_branch .LCY_ASK_%=\n" \
  ".LCY_BAR_%=:\n" \
  "s_barrier\n" \
  "ds_read_b128 v[8:11], v0 offset:0\n" \
  "ds_read_b128 v[12:15], v0 offset:1024\n" \
  "ds_read_b128 v[16:19], v0 offset:2048\n" \
  "s_add_u32 s68, s68, 1\n" \
  "s_add_u32 s64, s54, 16384\n" \
  "s_cmp_eq_u32 s69, 0\n" \
  "s_cbranch_scc1 .LCY_NI_%=\n" \
  "s_waitcnt vmcnt(1)\n" \
  "s_cmp_lg_u32 s61, 0\n" \
  "s_cbranch_scc1 .LCY_NJ_%=\n" \
  "s_waitcnt vmcnt(0)\n" \
  ".LCY_NJ_%=:\n" \
  "v_mov_b32 v6, s55\n" \
  "s_mov_b64 exec, 1\n" \
  "ds_add_u32 v5, v6\n" \
  "s_mov_b64 exec, -1\n" \
  "s_mov_b32 s69, 0\n" \
  ".LCY_NI_%=:\n" \
  "v_cmp_eq_u32 vcc, 0, v4\n" \
  "s_cmp_eq_u64 vcc, 0\n" \
  "s_cselect_b32 s70, 1, 0\n" \
  "s_cbranch_scc0 .LCY_NS_%=\n" \
  "v_lshlrev_b32 v6, 16, s55\n" \
  "s_mov_b64 exec, 1\n" \
  "ds_add_u32 v5, v6\n" \
  "s_mov_b64 exec, -1\n" \
  "s_mov_b32 m0, s56\n" \
  "s_add_u32 s71, s56, 4096\n" \
  "global_load_lds_dwordx4 v2, s[48:49] offset:0 sc1\n" \
  "global_load_lds_dwordx4 v2, s[48:49] offset:1024 sc1\n" \
  "global_load_lds_dwordx4 v2, s[48:49] offset:2048 sc1\n" \
  "global_load_lds_dwordx4 v2, s[48:49] offset:3072 sc1\n" \
  "s_mov_b32 m0, s71\n" \
  "s_add_u32 s56, s56, s65\n" \
  "global_load_lds_dwordx4 v3, s[48:49] offset:0 sc1\n" \
  "global_load_lds_dwordx4 v3, s[48:49] offset:1024 sc1\n" \
  ".LCY_NS_%=:\n" \
  "ds_read_b128 v[20:23], v0 offset:3072\n" \
  "s_waitcnt lgkmcnt(3)\n" \
  "v_mfma_f32_16x16x32_bf16 v[32:35], v[64:67], v[8:11], 0\n" \
  "v_mfma_f32_16x16x32_bf16 v[36:39], v[96:99], v[8:11], 0\n" \
  "v_mfma_f32_16x16x32_bf16 v[40:43], v[128:131], v[8:11], 0\n" \
  "v_mfma_f32_16x16x32_bf16 v[44:47], v[160:163], v[8:11], 0\n" \
  "v_mfma_f32_16x16x32_bf16 v[48:51], v[192:195], v[8:11], 0\n" \
  "v_mfma_f32_16x16x32_bf16 v[52:55], v[224:227], v[8:11], 0\n" \
  "ds_read_b128 v[8:11], v0 offset:8192\n" \
  "s_waitcnt lgkmcnt(3)\n" \
  "v_mfma_f32_16x16x32_bf16 v[32:35], v[68:71], v[12:15], v[32:35]\n" \
  "v_mfma_f32_16x16x32_bf16 v[36:39], v[100:103], v[12:15], v[36:39]\n" \
  "v_mfma_f32_16x16x32_bf16 v[40:43], v[132:135], v[12:15], v[40:43]\n" \
  "v_mfma_f32_16x16x32_bf16 v[44:47], v[164:167], v[12:15], v[44:47]\n" \
  "v_mfma_f32_16x16x32_bf16 v[48:51], v[196:199], v[12:15], v[48:51]\n" \
  "v_mfma_f32_16x16x32_bf16 v[52:55], v[228:231], v[12:15], v[52:55]\n" \
  "ds_read_b128 v[12:15], v0 offset:9216\n" \
  "s_waitcnt lgkmcnt(3)\n" \
  "v_mfma_f32_16x16x32_bf16 v[32:35], v[72:75], v[16:19], v[32:35]\n" \
  "v_mfma_f32_16x16x32_bf16 v[36:39], v[104:107], v[16:19], v[36:39]\n" \
  "v_mfma_f32_16x16x32_bf16 v[40:43], v[136:139], v[16:19], v[40:43]\n" \
  "v_mfma_f32_16x16x32_bf16 v[44:47], v[168:171], v[16:19], v[44:47]\n" \
  "v_mfma_f32_16x16x32_bf16 v[48:51], v[200:203], v[16:19], v[48:51]\n" \
  "v_mfma_f32_16x16x32_bf16 v[52:55], v[232:235], v[16:19], v[52:55]\n" \
  "ds_read_b128 v[16:19], v0 offset:10240\n" \
  "s_waitcnt lgkmcnt(3)\n" \
  "v_mfma_f32_16x16x32_bf16 v[32:35], v[76:79], v[20:23], v[32:35]\n" \
  "v_mfma_f32_16x16x32_bf16 v[36:39], v[108:111], v[20:23], v[36:39]\n" \
  "v_mfma_f32_16x16x32_bf16 v[40:43], v[140:143], v[20:23], v[40:43]\n" \
  "v_mfma_f32_16x16x32_bf16 v[44:47], v[172:175], v[20:23], v[44:47]\n" \
  "v_mfma_f32_16x16x32_bf16 v[48:51], v[204:207], v[20:23], v[48:51]\n" \
  "v_mfma_f32_16x16x32_bf16 v[52:55], v[236:239], v[20:23], v[52:55]\n" \
  "ds_read_b128 v[20:23], v0 offset:11264\n" \
  "s_waitcnt lgkmcnt(3)\n" \
  "v_mfma_f32_16x16x32_bf16 v[32:35], v[80:83], v[8:11], v[32:35]\n" \
  "v_mfma_f32_16x16x32_bf16 v[36:39], v[112:115], v[8:11], v[36:39]\n" \
  "v_mfma_f32_16x16x32_bf16 v[40:43], v[144:147], v[8:11], v[40:43]\n" \
  "v_mfma_f32_16x16x32_bf16 v[44:47], v[176:179], v[8:11], v[44:47]\n" \
  "v_mfma_f32_16x16x32_bf16 v[48:51], v[208:211], v[8:11], v[48:51]\n" \
  "v_mfma_f32_16x16x32_bf16 v[52:55], v[240:243], v[8:11], v[52:55]\n" \
  "ds_read_b128 v[8:11], v0 offset:4096\n" \
  "s_waitcnt lgkmcnt(3)\n" \
  "v_mfma_f32_16x16x32_bf16 v[32:35], v[84:87], v[12:15], v[32:35]\n" \
  "v_mfma_f32_16x16x32_bf16 v[36:39], v[116:119], v[12:15], v[36:39]\n" \
  "v_mfma_f32_16x16x32_bf16 v[40:43], v[148:151], v[12:15], v[40:43]\n" \
  "v_mfma_f32_16x16x32_bf16 v[44:47], v[180:183], v[12:15], v[44:47]\n" \
  "v_mfma_f32_16x16x32_bf16 v[48:51], v[212:215], v[12:15], v[48:51]\n" \
  "v_mfma_f32_16x16x32_bf16 v[52:55], v[244:247], v[12:15], v[52:55]\n" \
  "ds_read_b128 v[12:15], v0 offset:5120\n" \
  "s_waitcnt lgkmcnt(3)\n" \
  "v_mfma_f32_16x16x32_bf16 v[32:35], v[88:91], v[16:19], v[32:35]\n" \
  "v_mfma_f32_16x16x32_bf16 v[36:39], v[120:123], v[16:19], v[36:39]\n" \
  "v_mfma_f32_16x16x32_bf16 v[40:43], v[152:155], v[16:19], v[40:43]\n" \
  "v_mfma_f32_16x16x32_bf16 v[44:47], v[184:187], v[16:19], v[44:47]\n" \
  "v_mfma_f32_16x16x32_bf16 v[48:51], v[216:219], v[16:19], v[48:51]\n" \
  "v_mfma_f32_16x16x32_bf16 v[52:55], v[248:251], v[16:19], v[52:55]\n" \
  "ds_read_b128 v[16:19], v0 offset:6144\n" \
  "s_waitcnt lgkmcnt(3)\n" \
  "v_mfma_f32_16x16x32_bf16 v[32:35], v[92:95], v[20:23], v[32:35]\n" \
  "v_mfma_f32_16x16x32_bf16 v[36:39], v[124:127], v[20:23], v[36:39]\n" \
  "v_mfma_f32_16x16x32_bf16 v[40:43], v[156:159], v[20:23], v[40:43]\n" \
  "v_mfma_f32_16x16x32_bf16 v[44:47], v[188:191], v[20:23], v[44:47]\n" \
  "v_mfma_f32_16x16x32_bf16 v[48:51], v[220:223], v[20:23], v[48:51]\n" \
  "v_mfma_f32_16x16x32_bf16 v[52:55], v[252:255], v[20:23], v[52:55]\n" \
  "ds_read_b128 v[20:23], v0 offset:7168\n" \
  "s_waitcnt lgkmcnt(3)\n" \
  "s_nop 1\n" \
  "v_cvt_pk_bf16_f32 v24, v32, v33\n" \
  "v_cvt_pk_bf16_f32 v25, v34, v35\n" \
  "v_cvt_pk_bf16_f32 v26, v36, v37\n" \
  "v_cvt_pk_bf16_f32 v27, v38, v39\n" \
  "s_nop 0\n" \
  "v_mfma_f32_16x16x32_bf16 v[32:35], v[64:67], v[8:11], 0\n" \
  "v_mfma_f32_16x16x32_bf16 v[36:39], v[96:99], v[8:11], 0\n" \
  "buffer_store_dwordx4 v[24:27], v1, s[40:43], s54 offen offset:0 sc1\n" \
  "v_cvt_pk_bf16_f32 v28, v40, v41\n" \
  "v_cvt_pk_bf16_f32 v29, v42, v43\n" \
  "v_cvt_pk_bf16_f32 v30, v44, v45\n" \
  "v_cvt_pk_bf16_f32 v31, v46, v47\n" \
  "s_nop 0\n" \
  "v_mfma_f32_16x16x32_bf16 v[40:43], v[128:131], v[8:11], 0\n" \
  "v_mfma_f32_16x16x32_bf16 v[44:47], v[160:163], v[8:11], 0\n" \
  "buffer_store_dwordx4 v[28:31], v1, s[40:43], s54 offen offset:128 sc1\n" \
  "v_cvt_pk_bf16_f32 v56, v48, v49\n" \
  "v_cvt_pk_bf16_f32 v57, v50, v51\n" \
  "v_cvt_pk_bf16_f32 v58, v52, v53\n" \
  "v_cvt_pk_bf16_f32 v59, v54, v55\n" \
  "s_nop 0\n" \
  "v_mfma_f32_16x16x32_bf16 v[48:51], v[192:195], v[8:11], 0\n" \
  "v_mfma_f32_16x16x32_bf16 v[52:55], v[224:227], v[8:11], 0\n" \
  "buffer_store_dwordx4 v[56:59], v1, s[40:43], s54 offen offset:256 sc1\n" \
  "ds_read_b128 v[8:11], v0 offset:12288\n" \
  "s_waitcnt lgkmcnt(3)\n" \
  "v_mfma_f32_16x16x32_bf16 v[32:35], v[68:71], v[12:15], v[32:35]\n" \
  "v_mfma_f32_16x16x32_bf16 v[36:39], v[100:103], v[12:15], v[36:39]\n" \
  "v_mfma_f32_16x16x32_bf16 v[40:43], v[132:135], v[12:15], v[40:43]\n" \
  "v_mfma_f32_16x16x32_bf16 v[44:47], v[164:167], v[12:15], v[44:47]\n" \
  "v_mfma_f32_16x16x32_bf16 v[48:51], v[196:199], v[12:15], v[48:51]\n" \
  "v_mfma_f32_16x16x32_bf16 v[52:55], v[228:231], v[12:15], v[52:55]\n" \
  "ds_read_b128 v[12:15], v0 offset:13312\n" \
  "s_waitcnt lgkmcnt(3)\n" \
  "v_mfma_f32_16x16x32_bf16 v[32:35], v[72:75], v[16:19], v[32:35]\n" \
  "v_mfma_f32_16x16x32_bf16 v[36:39], v[104:107], v[16:19], v[36:39]\n" \
  "v_mfma_f32_16x16x32_bf16 v[40:43], v[136:139], v[16:19], v[40:43]\n" \
  "v_mfma_f32_16x16x32_bf16 v[44:47], v[168:171], v[16:19], v[44:47]\n" \
  "v_mfma_f32_16x16x32_bf16 v[48:51], v[200:203], v[16:19], v[48:51]\n" \
  "v_mfma_f32_16x16x32_bf16 v[52:55], v[232:235], v[16:19], v[52:55]\n" \
  "ds_read_b128 v[16:19], v0 offset:14336\n" \
  "s_waitcnt lgkmcnt(3)\n" \
  "v_mfma_f32_16x16x32_bf16 v[32:35], v[76:79], v[20:23], v[32:35]\n" \
  "v_mfma_f32_16x16x32_bf16 v[36:39], v[108:111], v[20:23], v[36:39]\n" \
  "v_mfma_f32_16x16x32_bf16 v[40:43], v[140:143], v[20:23], v[40:43]\n" \
  "v_mfma_f32_16x16x32_bf16 v[44:47], v[172:175], v[20:23], v[44:47]\n" \
  "v_mfma_f32_16x16x32_bf16 v[48:51], v[204:207], v[20:23], v[48:51]\n" \
  "v_mfma_f32_16x16x32_bf16 v[52:55], v[236:239], v[20:23], v[52:55]\n" \
  "ds_read_b128 v[20:23], v0 offset:15360\n" \
  "s_waitcnt lgkmcnt(3)\n" \
  "v_mfma_f32_16x16x32_bf16 v[32:35], v[80:83], v[8:11], v[32:35]\n" \
  "v_mfma_f32_16x16x32_bf16 v[36:39], v[112:115], v[8:11], v[36:39]\n" \
  "v_mfma_f32_16x16x32_bf16 v[40:43], v[144:147], v[8:11], v[40:43]\n" \
  "v_mfma_f32_16x16x32_bf16 v[44:47], v[176:179], v[8:11], v[44:47]\n" \
  "v_mfma_f32_16x16x32_bf16 v[48:51], v[208:211], v[8:11], v[48:51]\n" \
  "v_mfma_f32_16x16x32_bf16 v[52:55], v[240:243], v[8:11], v[52:55]\n" \
  "s_waitcnt lgkmcnt(2)\n" \
  "v_mfma_f32_16x16x32_bf16 v[32:35], v[84:87], v[12:15], v[32:35]\n" \
  "v_mfma_f32_16x16x32_bf16 v[36:39], v[116:119], v[12:15], v[36:39]\n" \
  "v_mfma_f32_16x16x32_bf16 v[40:43], v[148:151], v[12:15], v[40:43]\n" \
  "v_mfma_f32_16x16x32_bf16 v[44:47], v[180:183], v[12:15], v[44:47]\n" \
  "v_mfma_f32_16x16x32_bf16 v[48:51], v[212:215], v[12:15], v[48:51]\n" \
  "v_mfma_f32_16x16x32_bf16 v[52:55], v[244:247], v[12:15], v[52:55]\n" \
  "s_waitcnt lgkmcnt(1)\n" \
  "v_mfma_f32_16x16x32_bf16 v[32:35], v[88:91], v[16:19], v[32:35]\n" \
  "v_mfma_f32_16x16x32_bf16 v[36:39], v[120:123], v[16:19], v[36:39]\n" \
  "v_mfma_f32_16x16x32_bf16 v[40:43], v[152:155], v[16:19], v[40:43]\n" \
  "v_mfma_f32_16x16x32_bf16 v[44:47], v[184:187], v[16:19], v[44:47]\n" \
  "v_mfma_f32_16x16x32_bf16 v[48:51], v[216:219], v[16:19], v[48:51]\n" \
  "v_mfma_f32_16x16x32_bf16 v[52:55], v[248:251], v[16:19], v[52:55]\n" \
  "s_waitcnt lgkmcnt(0)\n" \
  "v_mfma_f32_16x16x32_bf16 v[32:35], v[92:95], v[20:23], v[32:35]\n" \
  "v_mfma_f32_16x16x32_bf16 v[36:39], v[124:127], v[20:23], v[36:39]\n" \
  "v_mfma_f32_16x16x32_bf16 v[40:43], v[156:159], v[20:23], v[40:43]\n" \
  "v_mfma_f32_16x16x32_bf16 v[44:47], v[188:191], v[20:23], v[44:47]\n" \
  "v_mfma_f32_16x16x32_bf16 v[48:51], v[220:223], v[20:23], v[48:51]\n" \
  "v_mfma_f32_16x16x32_bf16 v[52:55], v[252:255], v[20:23], v[52:55]\n" \
  "v_add_u32 v0, s66, v0\n" \
  "s_sub_u32 s66, 0, s66\n" \
  "s_add_u32 s54, s54, 0x80000\n" \
  "s_mov_b32 s62, 0x40000\n" \
  "v_cvt_pk_bf16_f32 v24, v32, v33\n" \
  "v_cvt_pk_bf16_f32 v25, v34, v35\n" \
  "v_cvt_pk_bf16_f32 v26, v36, v37\n" \
  "v_cvt_pk_bf16_f32 v27, v38, v39\n" \
  "buffer_store_dwordx4 v[24:27], v1, s[40:43], s64 offen offset:0 sc1\n" \
  "v_cvt_pk_bf16_f32 v28, v40, v41\n" \
  "v_cvt_pk_bf16_f32 v29, v42, v43\n" \
  "v_cvt_pk_bf16_f32 v30, v44, v45\n" \
  "v_cvt_pk_bf16_f32 v31, v46, v47\n" \
  "buffer_store_dwordx4 v[28:31], v1, s[40:43], s64 offen offset:128 sc1\n" \
  "v_cvt_pk_bf16_f32 v56, v48, v49\n" \
  "v_cvt_pk_bf16_f32 v57, v50, v51\n" \
  "v_cvt_pk_bf16_f32 v58, v52, v53\n" \
  "v_cvt_pk_bf16_f32 v59, v54, v55\n" \
  "buffer_store_dwordx4 v[56:59], v1, s[40:43], s64 offen offset:256 sc1\n" \
  "s_cmp_eq_u32 s70, 0\n" \
  "s_cbranch_scc1 .LCY_ND_%=\n" \
  "global_load_lds_dwordx4 v3, s[48:49] offset:2048 sc1\n" \
  "global_load_lds_dwordx4 v3, s[48:49] offset:3072 sc1\n" \
  "s_add_u32 s48, s48, 0x80000\n" \
  "s_addc_u32 s49, s49, 0\n" \
  "s_mov_b32 m0, s57\n" \
  "s_add_u32 s57, s57, s65\n" \
  "global_load_lds_dwordx4 v2, s[50:51] offset:0 sc1\n" \
  "global_load_lds_dwordx4 v2, s[50:51] offset:1024 sc1\n" \
  "s_sub_u32 s65, 0, s65\n" \
  "s_add_u32 s67, s67, 4096\n" \
  "global_load_lds_dwordx4 v2, s[50:51] offset:2048 sc1\n" \
  "s_sub_u32 s68, s68, 1\n" \
  "s_mov_b32 s69, 1\n" \
  "global_load_lds_dwordx4 v2, s[50:51] offset:3072 sc1\n" \
  "s_add_u32 s50, s50, 0x40000\n" \
  "s_addc_u32 s51, s51, 0\n" \
  "v_mov_b32 v4, 0\n" \
  "buffer_load_dword v4, v4, s[44:47], s67 offen sc1\n" \
  "s_waitcnt vmcnt(7)\n" \
  "s_branch .LCY_WC_%=\n" \
  ".LCY_ND_%=:\n" \
  "v_mov_b32 v4, 0\n" \
  "buffer_load_dword v4, v4, s[44:47], s67 offen sc1\n" \
  "s_waitcnt vmcnt(0)\n" \
  ".LCY_WC_%=:\n" \
  "v_mov_b32 v6, s59\n" \
  "v_mov_b32 v7, 1\n" \
  "s_mov_b64 exec, 1\n" \
  "ds_add_rtn_u32 v6, v6, v7\n" \
  "s_mov_b64 exec, -1\n" \
  "s_xor_b32 s59, s59, 4\n" \
  "s_mov_b32 s61, 0\n" \
  "s_waitcnt lgkmcnt(0)\n" \
  "v_readfirstlane_b32 s63, v6\n" \
  "v_lshlrev_b32 v6, 5, v2\n" \
  "s_and_b32 s63, s63, 7\n" \
  "s_cmp_eq_u32 s63, 7\n" \
  "s_cbranch_scc0 .LCY_W1_%=\n" \
  "s_mov_b64 exec, 15\n" \
  "global_store_dword v6, v7, s[52:53] sc1\n" \
  "s_mov_b64 exec, -1\n" \
  "s_mov_b32 s61, 1\n" \
  ".LCY_W1_%=:\n" \
  "s_add_u32 s52, s52, 2048\n" \
  "s_addc_u32 s53, s53, 0\n" \
  "s_sub_u32 s60, s60, 1\n" \
  "s_cmp_eq_u32 s60, 0\n" \
  "s_cbranch_scc1 .LCY_END_%=\n" \
  ".LCY_NEED_%=:\n" \
  "s_cmp_eq_u32 s68, 0\n" \
  "s_cbranch_scc1 .LCY_BAR_%=\n" \
  "v_cmp_eq_u32 vcc, 0, v4\n" \
  "s_cmp_eq_u64 vcc, 0\n" \
  "s_cbranch_scc1 .LCY_GET_%=\n" \
  "s_cmp_eq_u32 s68, 1\n" \
  "s_cbranch_scc1 .LCY_BAR_%=\n" \
  "s_cmp_ge_u32 s67, 0x40000\n" \
  "s_cbranch_scc1 .LCY_BAR_%=\n" \
  "s_sleep 12\n" \
  ".LCY_ASK_%=:\n" \
  "v_mov_b32 v4, 0\n" \
  "buffer_load_dword v4, v4, s[44:47], s67 offen sc1\n" \
  "s_sub_u32 s62, s62, 1\n" \
  "s_cbranch_scc1 .LCY_END_%=\n" \
  "s_waitcnt vmcnt(0)\n" \
  "s_branch .LCY_NEED_%=\n" \
  ".LCY_GET_%=:\n" \
  "v_lshlrev_b32 v6, 16, s55\n" \
  "s_mov_b64 exec, 1\n" \
  "ds_add_u32 v5, v6\n" \
  "s_mov_b64 exec, -1\n" \
  "s_mov_b32 m0, s56\n" \
  "s_add_u32 s71, s56, 4096\n" \
  "global_load_lds_dwordx4 v2, s[48:49] offset:0 sc1\n" \
  "global_load_lds_dwordx4 v2, s[48:49] offset:1024 sc1\n" \
  "global_load_lds_dwordx4 v2, s[48:49] offset:2048 sc1\n" \
  "global_load_lds_dwordx4 v2, s[48:49] offset:3072 sc1\n" \
  "s_mov_b32 m0, s71\n" \
  "s_add_u32 s56, s56, s65\n" \
  "global_load_lds_dwordx4 v3, s[48:49] offset:0 sc1\n" \
  "global_load_lds_dwordx4 v3, s[48:49] offset:1024 sc1\n" \
  "global_load_lds_dwordx4 v3, s[48:49] offset:2048 sc1\n" \
  "global_load_lds_dwordx4 v3, s[48:49] offset:3072 sc1\n" \
  "s_add_u32 s48, s48, 0x80000\n" \
  "s_addc_u32 s49, s49, 0\n" \
  "s_mov_b32 m0, s57\n" \
  "s_add_u32 s57, s57, s65\n" \
  "global_load_lds_dwordx4 v2, s[50:51] offset:0 sc1\n" \
  "global_load_lds_dwordx4 v2, s[50:51] offset:1024 sc1\n" \
  "s_sub_u32 s65, 0, s65\n" \
  "s_add_u32 s67, s67, 4096\n" \
  "global_load_lds_dwordx4 v2, s[50:51] offset:2048 sc1\n" \
  "s_sub_u32 s68, s68, 1\n" \
  "s_mov_b32 s69, 1\n" \
  "global_load_lds_dwordx4 v2, s[50:51] offset:3072 sc1\n" \
  "s_add_u32 s50, s50, 0x40000\n" \
  "s_addc_u32 s51, s51, 0\n" \
  "v_mov_b32 v4, 0\n" \
  "buffer_load_dword v4, v4, s[44:47], s67 offen sc1\n" \
  "s_waitcnt vmcnt(0)\n" \
  "v_mov_b32 v6, s55\n" \
  "s_mov_b64 exec, 1\n" \
  "ds_add_u32 v5, v6\n" \
  "s_mov_b64 exec, -1\n" \
  "s_mov_b32 s69, 0\n" \
  "s_branch .LCY_NEED_%=\n" \
  ".LCY_END_%=:\n" \
  "s_endpgm\n"
template <bool AT>
__device__ __forceinline__ void role_aw_t(const MegaArgs& A, char* smem, const int L, const int a, const u64 tag) {
  const int w = A.wv;
  const int l = lane_id(), c = l & 15, g = l >> 4;
  const int qt = w & 1, hh = (w >> 1) & 1, h = 2 * a + hh;
  char* ws = A.ws;
  unsigned* FL = (unsigned*)(ws + OFF_FL);
  const unsigned sb = lds_base(smem);
  if (w == 0 && l < 4) reinterpret_cast<unsigned*>(smem + AW_CNT)[l] = 0u;
  constexpr int NP = AT ? 1 : 3;
  const int cp0 = AT ? w : 3 * w - 8;
  v8u wo[2 * NP][4];
  {
    const int q = (l >> 2) & 3, p = l & 3, f = q | ((g & 1) << 2);
    const unsigned ra = sb + (4 * g + q) * 256 + (p & 1) * 16;
#pragma unroll
    for (int r = 0; r < 2; ++r) {
#pragma unroll
      for (int k = 0; k < 2; ++k) {
        const char* base_ = (const char*)A.Wo + ((size_t)((2 * a + r) * DH + 64 * (w & 1) + 32 * k) * DM + 128 * (w >> 1)) * 2;
#pragma unroll
        for (int i = 0; i < 8; ++i) {
          const int fd = g | ((i & 1) << 2);
          glds16(base_, (unsigned)((4 * i + g) * (DM * 2) + ((c ^ (fd << 1)) << 4)), sb + w * 16384 + k * 8192 + i * 1024);
        }
      }
      SST(4 + 3 * r);
      if (r == 1) ready_wait(A, tag, ready_look(A));
      WAIT_VM(0);
      RAW_BAR();
      SST(5 + 3 * r);
#pragma unroll
      for (int k2 = 0; k2 < 2; ++k2) {
#pragma unroll
        for (int ct = 0; ct < 2 * NP; ++ct) {
          const int cp = cp0 + (ct >> 1);
          const unsigned a_ = ra + (cp >> 2) * 32768 + k2 * 16384 + ((2 * (cp & 3) + (p >> 1)) ^ f) * 32 + (ct & 1) * 8;
          const v2u r0 = trd(a_), r1 = trd(a_ + 4096), r2 = trd(a_ + 8192), r3 = trd(a_ + 12288);
          wo[ct][2 * r + k2] = v8u{r0.x, r0.y, r1.x, r1.y, r2.x, r2.y, r3.x, r3.y};
          asm volatile("" : "+v"(wo[ct][2 * r + k2]));
        }
      }
      RAW_BAR();
      SST(6 + 3 * r);
    }
  }
  WSTAMP2();
  const v4u rsW = mk_srd(ws + OFF_PW, SZ_PW);
  const v4u rsF = mk_srd(FL + FL_FQ + (L * 64 * 32 + h) * 32, (64 * 32 - h) * 128);
  const char* p0 = AT ? ws + OFF_MEMK + L * SZ_MEM + h * 2048 : ws + OFF_QK + L * SZ_QK + ((size_t)h * 16 + 8 * qt) * 1024;
  const char* p1 = AT ? (const char*)(FL + FL_FK + L * 32 + (h >> 3) * 64 + (h & 7) * 2) : ws + OFF_VF + L * SZ_O + ((size_t)h * 8 + 4 * qt) * 1024;
  const unsigned* pFW = FL + FL_FW + (L * 64 * 16 + (a >> 2)) * 32 + (a & 3);
  const unsigned tk = sb + AW_TK + hh * 24 * 1024, voff = (c >> 1) * 2048 + (c & 1) * 64 + g * 16;
  const char* dbg = ws + WS_TOTAL + A.vb * 64 * 8;
#define AW_SOPS                                                                                                                 \
  "{s[40:43]}"(rsW), "{s[44:47]}"(rsF), "{s[48:49]}"(p0), "{s[50:51]}"(p1), "{s[52:53]}"(pFW),                                   \
      "{s54}"((L * 64 * 16 + a) * 32768 + cp0 * 128), "{s55}"(1 << (8 * qt)), "{s56}"(tk + qt * 8192), "{s57}"(tk + 16384 + qt * 4096),                  \
      "{s58}"(sb + AW_CNT + 8 + 4 * hh), "{s59}"(sb + AW_CNT), "{s60}"(NSEG), "{s[36:37]}"(dbg)                                  \
      : "memory", "vcc", "scc", "s80"
  if constexpr (AT) {
    const v8u pv = {tk + l * 16, sb + AW_SO + l * 16, (unsigned)(g * 8 + (c & 7)) * 16, (unsigned)(OFF_MEMV - OFF_MEMK) + c * 32 + (g & 1) * 16,
                    voff, (unsigned)l * 16, tk + qt * 4096 + l * 16, sb + AW_SO + w * 4096 + l * 16};
    const unsigned neg = 0xff61b1e6u;
    v8u bt;
#pragma unroll
    for (int i = 0; i < 4; ++i) {
      const unsigned d = 4 * g + i > c ? neg : 0u;
      bt[i] = qt ? 0u : d;
      bt[4 + i] = qt ? d : neg;
    }
    const unsigned e = g >= 2 ? neg : 0u;
    const v4u bm = {e, e, e, e};
    asm volatile("s_mov_b64 exec, -1\ns_waitcnt vmcnt(0) lgkmcnt(0)\ns_nop 4\n" AW_ASM_X
                 ::"{v[192:199]}"(wo[0][0]), "{v[200:207]}"(wo[0][1]), "{v[208:215]}"(wo[0][2]), "{v[216:223]}"(wo[0][3]),
                 "{v[224:231]}"(wo[1][0]), "{v[232:239]}"(wo[1][1]), "{v[240:247]}"(wo[1][2]), "{v[248:255]}"(wo[1][3]),
                 "{v[0:7]}"(pv), "{v[8:15]}"(bt), "{v[16:19]}"(bm), AW_SOPS);
  } else {
    const v4u pv = {sb + AW_SO + l * 16, voff, (unsigned)l * 16, 0u};
    asm volatile("s_mov_b64 exec, -1\ns_waitcnt vmcnt(0) lgkmcnt(0)\ns_nop 4\n" AW_ASM_Y
                 ::"{v[64:71]}"(wo[0][0]), "{v[72:79]}"(wo[0][1]), "{v[80:87]}"(wo[0][2]), "{v[88:95]}"(wo[0][3]),
                 "{v[96:103]}"(wo[1][0]), "{v[104:111]}"(wo[1][1]), "{v[112:119]}"(wo[1][2]), "{v[120:127]}"(wo[1][3]),
                 "{v[128:135]}"(wo[2][0]), "{v[136:143]}"(wo[2][1]), "{v[144:151]}"(wo[2][2]), "{v[152:159]}"(wo[2][3]),
                 "{v[160:167]}"(wo[3][0]), "{v[168:175]}"(wo[3][1]), "{v[176:183]}"(wo[3][2]), "{v[184:191]}"(wo[3][3]),
                 "{v[192:199]}"(wo[4][0]), "{v[200:207]}"(wo[4][1]), "{v[208:215]}"(wo[4][2]), "{v[216:223]}"(wo[4][3]),
                 "{v[224:231]}"(wo[5][0]), "{v[232:239]}"(wo[5][1]), "{v[240:247]}"(wo[5][2]), "{v[248:255]}"(wo[5][3]),
                 "{v[0:3]}"(pv), AW_SOPS);
  }
#undef AW_SOPS
}
__device__ __forceinline__ void role_aw(const MegaArgs& A, char* smem, const int L, const int a, const u64 tag) {
  if (A.wv < 4) role_aw_t<true>(A, smem, L, a, tag);
  else role_aw_t<false>(A, smem, L, a, tag);
}

constexpr int RG = 2, RW = 2;
__device__ __forceinline__ void role_red(const MegaArgs& A, char* smem, const int L, const int idx, const u64 tag) {
  const int w = A.wv;
  const int l = lane_id();
  const int gr = (w >> 1) & 1, r = idx * 2 + (w & 1);
  char* ws = A.ws;
  unsigned* FL = (unsigned*)(ws + OFF_FL);
  unsigned* TMO = FL + FL_TMO;
  const __amdgpu_buffer_rsrc_t rsW = MKRS(ws + OFF_PW, SZ_PW);
  const __amdgpu_buffer_rsrc_t rsX = MKRS(ws + (L ? OFF_X3 : OFF_XA), SZ_X);
  const __amdgpu_buffer_rsrc_t rsY = L ? MKRS(A.out, SZ_X) : MKRS(ws + OFF_X2, SZ_X);
  volatile unsigned* sw = reinterpret_cast<volatile unsigned*>(smem) + (w >> 1) * 32;
  if (w < 2) reinterpret_cast<volatile unsigned*>(smem)[64 * w + l] = 0u;
  __syncthreads();
  if ((EXP_OFF & 2) && L == 0 && w < 4) return;
  if ((EXP_OFF & 64) && L == 1 && w < 4) return;
  if ((EXP_OFF & 8) && L == 1 && w >= 4) return;
  unsigned tl = now10();
  if (w >= 4) return;
  ready_wait(A, tag, ready_look(A));
  if (L == 0) {
    for (int s = gr; s < NSEG; s += RG) ST128((v4u{0u, 0u, 0u, 0u}), rsY, l * 16, (s * SEG + r) * DM * 2);
    DRAIN();
    if (l < 8) set_flag(FL + FL_FZ + (2 * l + (idx >> 3)) * 32 + (idx & 7) * 4 + w);
  }
  for (int s = gr; s < NSEG; s += RG) {
    grp_wait<SLP_R, 4, GATE_R>((w & 1) == 0, sw, s + 1, FL + FL_FW + ((L * 64 + s) * 4 + (blockIdx.x & 3)) * 4 * 32, 16, TMO, tl);
    if (s >= NSEG - 6) tl = now10();
    TS(0, s, gr * RW * 64);
    TS(4, s, gr * RW * 64 + (RW - 1) * 64);
    v4u p[16];
    const v4u xr = LD128(rsX, l * 512, s * 32768 + r * 16);
#pragma unroll
    for (int k = 0; k < 16; ++k)
      p[k] = LD128(rsW, (l >> 2) * 128 + (l & 3) * 16, ((L * 64 + s) * 16 + k) * 32768 + (r >> 1) * 2048 + (r & 1) * 64);
    v4u xo;
#pragma unroll
    for (int e = 0; e < 4; ++e) {
      f32x2_t t[4];
#pragma unroll
      for (int k = 0; k < 4; ++k)
        t[k] = (f32x2_t{lo16(p[4 * k][e]), hi16(p[4 * k][e])} + f32x2_t{lo16(p[4 * k + 1][e]), hi16(p[4 * k + 1][e])}) +
               (f32x2_t{lo16(p[4 * k + 2][e]), hi16(p[4 * k + 2][e])} + f32x2_t{lo16(p[4 * k + 3][e]), hi16(p[4 * k + 3][e])});
      const f32x2_t u = (t[0] + t[1]) + (t[2] + t[3]);
      const unsigned a0 = pk2(u.x, u.y);
      xo[e] = pk2(lo16(xr[e]) + lo16(a0), hi16(xr[e]) + hi16(a0));
      if (L == 0) xo[e] = xo[e] ? xo[e] : 0x8000u;
    }
    ST128(xo, rsY, l * 16, (s * SEG + r) * DM * 2);
    TS(1, s, gr * RW * 64);
    TS(5, s, gr * RW * 64 + (RW - 1) * 64);
    if (L == 0) {
      const unsigned st = row_stats(xo);
      if (l < 4) __hip_atomic_store((gu32*)(FL + FL_FR + ((s * 4 + l) * 4 + (r >> 3)) * 32 + (r & 7)), st, RLX_AGENT);
    }
    TS(6, s, gr * RW * 64 + (RW - 1) * 64);
    TS(3, s, gr * RW * 64);
  }
  if (L == 1 && idx == 0 && w == 0 && l == 0) __hip_atomic_store((gu64*)(ws + OFF_EP), tag, RLX_AGENT);
}

constexpr int CUN = 2144;
__device__ __forceinline__ v2u part_stats(const v4u o) {
  float v[8];
#pragma unroll
  for (int e = 0; e < 4; ++e) { v[2 * e] = lo16(o[e]); v[2 * e + 1] = hi16(o[e]); }
  const float s = ((v[0] + v[1]) + (v[2] + v[3])) + ((v[4] + v[5]) + (v[6] + v[7])), m = s * 0.125f;
  float q0 = 0.f, q1 = 0.f;
#pragma unroll
  for (int e = 0; e < 4; ++e) {
    const float a = v[e] - m, b = v[4 + e] - m;
    q0 += a * a;
    q1 += b * b;
  }
  return v2u{__float_as_uint(s), __float_as_uint(q0 + q1)};
}
constexpr int CRN = 176;
constexpr int CV_LDS = (8 * 2 * CKN + 8 * 2 * CUN + 64 + 2 * 32 * 8 + 16 * CRN + 64 + 256 * 8) * 2 + 8 * 1568;
__device__ __forceinline__ void role_conv(const MegaArgs& A, char* smem, const int cv) {
  u16* Kr = reinterpret_cast<u16*>(smem);
  u16* Uc = Kr + 8 * 2 * CKN;
  u16* Us = Uc + 8 * CUN + 63;
  u16* To = Uc + 8 * 2 * CUN + 64;
  u16* Sx = To + 32 * 8;
  u16* Rc = Sx + 32 * 8;
  u16* Rs = Rc + 8 * CRN + 63;
  u16* Ta = Rc + 16 * CRN + 64;
  const int w = A.wv;
  const int l = lane_id(), tid = 64 * w + l, c = l & 15, g = l >> 4;
  const int d = 8 * cv + w;
  char* ws = A.ws;
  unsigned* FL = (unsigned*)(ws + OFF_FL);
  unsigned* TMO = FL + FL_TMO;
  u16* kr = Kr + w * 2 * CKN;
  const u16* ka = kr + (c >> 3) * CKN + 2080 - 16 * (c & 7) + 8 * g;
#define KFRAG(D0) (*reinterpret_cast<const bf16x8*>(ka - (D0)))
  typedef __attribute__((address_space(3))) const unsigned* lpu;
#define BFRAG(a) BF8((v4u{((lpu)(unsigned long)(a))[0], ((lpu)(unsigned long)(a))[1], ((lpu)(unsigned long)(a))[2], ((lpu)(unsigned long)(a))[3]}))
  f32x4 acc[16], far[8];
  v4u tx[4];
  const __amdgpu_buffer_rsrc_t rz1 = MKRS(ws + OFF_XA, SZ_X), rps = MKRS(ws + OFF_H, SZ_X);
  u16* uc = Uc + w * CUN;
  const unsigned ub = lds_base(reinterpret_cast<char*>(((c & 1) ? Us : Uc) + w * CUN + c + 8 * g + 32));
  const int t0 = 16 * (4 * (g & 1) + (g >> 1)) + c;
  float dsk;
#define SKIP(T) pk2(bf2f(uc[32 + 128 * (T) + t0]) * dsk, bf2f(uc[64 + 128 * (T) + t0]) * dsk)
#define TILE_OUT(T, a, uu)                                                                     \
  {                                                                                            \
    const unsigned yy = pk2(swap_add32(a[0], a[1]), swap_add32(a[2], a[3]));                   \
    const unsigned v_ = pk2(lo16(yy) + lo16(uu), hi16(yy) + hi16(uu));                         \
    Ta[(128 * ((T) & 7) + t0) * 8 + w] = (u16)v_;                                              \
    Ta[(128 * ((T) & 7) + t0 + 32) * 8 + w] = (u16)(v_ >> 16);                                 \
  }
#define CHUNK_ST(i)                                                                            \
  {                                                                                            \
    const v4u a_ = *reinterpret_cast<const v4u*>(Ta + (64 * w + l + 512 * ((i) & 1)) * 8);     \
    v4u o;                                                                                     \
    _Pragma("unroll") for (int e = 0; e < 4; ++e) o[e] = pk2(lo16(tx[i][e]) + lo16(a_[e]), hi16(tx[i][e]) + hi16(a_[e])); \
    ST128(o, rz1, (l >> 5) * 32768 + (l & 31) * 16, (2 * (w + 8 * (i)) * 64 + cv) * 512);      \
    ST64(part_stats(o), rps, (l >> 5) * 16384 + (l & 31) * 8, (2 * (w + 8 * (i)) * 64 + cv) * 256); \
  }
#define CHUNK_FLAG(i) set_flag(FL + FL_FC + (((2 * w + 16 * ((i) + (l >> 3)) + ((l >> 2) & 1)) * 4 + (l & 3)) * 8 + (cv >> 3)) * 32 + (cv & 7))
  {
    unsigned ta[3], k0p;
    v4u kn;
    u64 tag;
    {
      KGen G;
      G.load(A, d, l);
      const u16 pk = A.Dskip[d];
      asm volatile("" ::: "memory");
      tx[0] = *reinterpret_cast<const v4u*>(A.x0 + (size_t)tid * DM + 8 * cv);
      ask_slow(A, 16 * (w - 4) + (cv & 15), ta);
      tag = call_tag(A);
      G.setup();
      dsk = bf2f(pk);
      G.st = smem + w * 16640;
      SST(0);
      G.columns(l);
      const Cx z = G.power(1.f);
      G.rows(G.st + l * 8, cmul(G.C, z), z, 16);
      kn = G.tile(l);
      const float k0 = 2.0f * wave_sum(G.C.r.x + G.C.r.y);
      k0p = l == 0 ? pk2(k0, k0 - lo16(pk2(k0, k0))) : 0u;
    }
    SST(1);
    const __amdgpu_buffer_rsrc_t rst = MKRS(ws + OFF_GT, L_SEQ * 16);
#define U_VAL(i)                                                                               \
  {                                                                                            \
    const float mb_ = lo16(st[i]), ri_ = 1.0f / hi16(st[i]);                                   \
    _Pragma("unroll") for (int e = 0; e < 4; ++e) {                                            \
      const unsigned t_ = pk2(lo16(tx[i][e]) - mb_, hi16(tx[i][e]) - mb_);                     \
      uv[e] = pk2(lo16(t_) * ri_, hi16(t_) * ri_);                                             \
    }                                                                                          \
  }
#define U_PUT(i)                                                                               \
  _Pragma("unroll") for (int e = 0; e < 4; ++e) {                                              \
    ((lph)(unsigned long)up0)[(2 * e) * CUN + 512 * (i)] = (u16)uv[e];                         \
    ((lph)(unsigned long)up0)[(2 * e + 1) * CUN + 512 * (i)] = (u16)(uv[e] >> 16);             \
    ((lph)(unsigned long)up1)[(2 * e) * CUN + 512 * (i)] = (u16)uv[e];                         \
    ((lph)(unsigned long)up1)[(2 * e + 1) * CUN + 512 * (i)] = (u16)(uv[e] >> 16);             \
  }
#define U_ROW(i) { U_VAL(i) U_PUT(i) }
    typedef __attribute__((address_space(3))) u16* lph;
    unsigned st[4], up0 = lds_base((char*)(Uc + 32 + tid)), up1 = lds_base((char*)(Us + 32 + tid));
    asm volatile("" : "+v"(up0), "+v"(up1), "+v"(kn), "+v"(k0p));
    v4u uv;
#define ST_GET(I0, I1)                                                                                                        \
  for (unsigned spins = 0; spins < SPIN_CAP; ++spins) {                                                                       \
    bool ok = true;                                                                                                           \
    v4u t_[4];                                                                                                                \
    _Pragma("unroll") for (int i = I0; i < I1; ++i) t_[i] = LD128(rst, (tid + 512 * i) * 16, 0);                              \
    _Pragma("unroll") for (int i = I0; i < I1; ++i) {                                                                         \
      ok = ok && has_tag(tag, t_[i].x, t_[i].y) && has_tag(tag, t_[i].z, t_[i].w);                                            \
      st[i] = __builtin_amdgcn_perm(t_[i].w, t_[i].y, 0x07060302);                                                            \
    }                                                                                                                         \
    if (__all(ok)) break;                                                                                                     \
    __builtin_amdgcn_s_sleep(4);                                                                                              \
    asm volatile("" ::: "memory");                                                                                            \
  }
    if (w < 4) {
      ST_GET(0, 4);
      U_VAL(0);
      asm volatile("" : "+v"(uv));
    }
#pragma unroll
    for (int i = 1; i < 4; ++i) tx[i] = *reinterpret_cast<const v4u*>(A.x0 + (size_t)(tid + 512 * i) * DM + 8 * cv);
    RAW_BAR();
    SST(2);
    if (w < 4) { U_PUT(0) }
    *reinterpret_cast<v2u*>(kr + 2076 - 16 * c - 4 * g) = v2u{__builtin_amdgcn_perm(kn.z, kn.w, 0x05040100), __builtin_amdgcn_perm(kn.x, kn.y, 0x05040100)};
    *reinterpret_cast<v2u*>(kr + CKN + 2076 - 16 * c - 4 * g) = v2u{__builtin_amdgcn_perm(kn.z, kn.w, 0x07060302), __builtin_amdgcn_perm(kn.x, kn.y, 0x07060302)};
    if (l < 18) {
      reinterpret_cast<v4u*>(kr)[l < 16 ? 260 + l : 210 + l] = v4u{k0p & 0xffffu, 0u, 0u, 0u};
      reinterpret_cast<v4u*>(kr + CKN)[l < 16 ? 260 + l : 210 + l] = v4u{k0p >> 16, 0u, 0u, 0u};
    }
    (l < 32 ? uc : Us + w * CUN - 32)[l] = 0;
    uc[32 + L_SEQ + l] = Us[w * CUN + 32 + L_SEQ + l] = 0;
    uc[32 + 256 + l] = Us[w * CUN + 32 + 256 + l] = 0;
    RAW_BAR();
    SST(11);
    {
      f32x4 a0 = {}, a1 = {}, a2 = {};
      bf16x8 bf[9], af[9];
#pragma unroll
      for (int b = 0; b < 9; ++b) {
        af[(b + 4) % 9] = KFRAG(160 - 32 * ((b + 4) % 9));
        bf[b] = BFRAG(ub + 64 * (b - 1));
      }
      const unsigned k0 = SKIP(0), k1 = SKIP(1);
      __builtin_amdgcn_sched_barrier(0);
#pragma unroll
      for (int b = 0; b < 5; ++b) a0 = MFMA16(af[b + 4], bf[b], a0);
#pragma unroll
      for (int b = 0; b < 4; ++b) {
        a2 = MFMA16(af[b + 4], bf[b + 4], a2);
        a1 = MFMA16(af[b], bf[b], a1);
      }
      a2 = MFMA16(af[8], bf[8], a2);
      TILE_OUT(0, a0, k0);
      a1 += a2;
      TILE_OUT(1, a1, k1);
    }
    const u64 rv = ready_look(A);
    RAW_BAR();
    SST(3);
    if (w < 4) {
      CHUNK_ST(0);
      asm volatile("" : "+v"(tx[1]));
      U_ROW(1);
      DRAIN();
      ready_wait(A, tag, rv);
      if (l < 8) CHUNK_FLAG(0);
    } else {
      ST_GET(0, 4);
      ready_wait(A, tag, rv);
      asm volatile("" : "+v"(tx[0]), "+v"(tx[1]));
      U_ROW(0);
      U_ROW(1);
    }
    ASKED(ta);
    if (tid < (16 * CRN + 64) / 8) reinterpret_cast<v4u*>(Rc)[tid] = v4u{0u, 0u, 0u, 0u};
    SST(4);
    const gu64* fk = (gu64*)(ws + OFF_KG) + cv * 16 + w;
    u64 kg = __hip_atomic_load(fk, RLX_AGENT);
    U_ROW(2);
    for (unsigned spins = 0; !__all(kg == tag) && spins < SPIN_CAP; ++spins) {
      __builtin_amdgcn_s_sleep(8);
      kg = __hip_atomic_load(fk, RLX_AGENT);
    }
    asm volatile("" : "+v"(tx[3]), "+v"(st[3])::"memory");
    SST(5);
    const unsigned* kd = reinterpret_cast<const unsigned*>(Ta + 256 * 8) + w * 392;
    glds16c(ws + OFF_KF + d * 2048, l * 16, lds_base((char*)kd));
    if (l < 34) glds16c(ws + OFF_KF + d * 2048 + 1024, l * 16, lds_base((char*)kd) + 1024);
    U_ROW(3);
    DRAIN();
    SST(6);
#pragma unroll 1
    for (int un = l; un < 228; un += 64) {
      const int kc = un < 132 ? 2 : un < 196 ? 1 : 0;
      v4u oh, ol;
#pragma unroll
      for (int e = 0; e < 4; ++e) {
        unsigned kv[2];
#pragma unroll
        for (int k = 0; k < 2; ++k) {
          const int lag = 2080 - (8 * un + 2 * e + k);
          const int q = (lag + (1 << kc) - 1 + ((lag >> (kc + 1)) & 1)) >> (kc + 1);
          kv[k] = kd[128 * kc - 128 + q];
          if (lag >= L_SEQ) kv[k] = 0u;
        }
        oh[e] = __builtin_amdgcn_perm(kv[1], kv[0], 0x05040100);
        ol[e] = __builtin_amdgcn_perm(kv[1], kv[0], 0x07060302);
      }
      reinterpret_cast<v4u*>(kr)[un] = oh;
      reinterpret_cast<v4u*>(kr + CKN)[un] = ol;
    }
    RAW_BAR();
    SST(7);
    {
      unsigned uq[2] = {ub, ub + 1024};
      bf16x8 bf[8], a[4];
      const bf16x8 bm = BFRAG(ub - 64);
      asm volatile("" : "+v"(uq[0]), "+v"(uq[1]));
#pragma unroll
      for (int j = 1; j < 8; ++j) bf[j] = BFRAG(uq[j >> 2] + 256 * (j & 3));
#pragma unroll
      for (int T = 2; T < 16; ++T) (T < 8 ? acc[T] : far[T & 7]) = MFMA16(KFRAG(128 * T + 32), bm, (f32x4{}));
#pragma unroll
      for (int e = 0; e < 3; ++e) a[e] = KFRAG(128 * e);
      const u16* kq = ka - 128 * 7;
#pragma unroll 1
      for (int r = 0; r < 4; ++r) {
#pragma unroll
        for (int e = 0; e < 8; ++e) {
          a[(e + 3) & 3] = *reinterpret_cast<const bf16x8*>(kq + 128 * (7 - ((e + 3) & 7)) + (e >= 5 ? 32 : 0));
          const int j = (8 - e) & 7;
          bf[j] = BFRAG(uq[j >> 2] + 256 * (j & 3) + (e ? 64 : 0));
          __builtin_amdgcn_sched_barrier(0);
#pragma unroll
          for (int T = 7; T >= (e > 2 ? e : 2); --T) acc[T] = MFMA16(a[e & 3], bf[T - e], acc[T]);
          __builtin_amdgcn_sched_barrier(0);
        }
        kq += 32;
        uq[0] += 64;
        uq[1] += 64;
        asm volatile("" : "+v"(uq[0]), "+v"(uq[1]));
      }
    }
    SST(8);
    {
      unsigned k[8];
#pragma unroll
      for (int T = 2; T < 8; ++T) k[T] = SKIP(T);
#pragma unroll
      for (int T = 2; T < 8; ++T) TILE_OUT(T, acc[T], k[T]);
    }
    RAW_BAR();
    SST(10);
    if (w >= 4) CHUNK_ST(0);
    CHUNK_ST(1);
    if (w == 1) (void)wait_n<SLP_V, 32>(FL + FL_FZ + (blockIdx.x & 7) * 64, 64, TMO);
    DRAIN();
    if (l < 16) CHUNK_FLAG(0);
    SST(9);
#undef U_ROW
#undef U_VAL
#undef U_PUT
#undef ST_GET
  }
#define FAR_HALF(r, J0, J1)                                                                     \
  {                                                                                            \
    bf16x8 bf[16], af[16];                                                                     \
    unsigned u_ = ub + 64 * (r) + 256 * (J0), v_ = u_ + 1024;                                  \
    asm volatile("" : "+v"(u_), "+v"(v_));                                                     \
    _Pragma("unroll") for (int j = (J0); j < (J1); ++j) bf[j] = BFRAG((j - (J0) < 4 ? u_ : v_) + 256 * ((j - (J0)) & 3)); \
    _Pragma("unroll") for (int e = (9 - (J1) > 0 ? 9 - (J1) : 0); e < 16 - (J0); ++e) af[e] = KFRAG(128 * e - 32 * (r)); \
    __builtin_amdgcn_sched_barrier(0);                                                         \
    _Pragma("unroll") for (int j = (J1) - 1; j >= (J0); --j)                                   \
      _Pragma("unroll") for (int T = (j > 8 ? j : 8); T < 16; ++T) far[T - 8] = MFMA16(af[T - j], bf[j], far[T - 8]); \
  }
#define FAR_PIECE(p)                                                                            \
  {                                                                                            \
    int l = tid & 63;                                                                          \
    asm volatile("" : "+v"(l), "+v"(tx[2]), "+v"(tx[3]));                                      \
    if ((p) < 16) {                                                                            \
      const int r_ = (p) >> 2;                                                                 \
      if (((p) & 3) == 0) FAR_HALF(r_, 0, 3) else if (((p) & 3) == 1) FAR_HALF(r_, 3, 6)       \
      else if (((p) & 3) == 2) FAR_HALF(r_, 6, 9) else FAR_HALF(r_, 9, 16)                     \
    } else if ((p) == 16) {                                                                    \
      unsigned k_[8];                                                                          \
      _Pragma("unroll") for (int T = 0; T < 8; ++T) k_[T] = SKIP(8 + T);                       \
      _Pragma("unroll") for (int T = 0; T < 8; ++T) TILE_OUT(T, far[T], k_[T]);                \
    } else if ((p) == 17) {                                                                    \
      CHUNK_ST(2);                                                                             \
    } else if ((p) == 18) {                                                                    \
      CHUNK_ST(3);                                                                             \
    } else {                                                                                   \
      DRAIN();                                                                                 \
      if (l < 16) CHUNK_FLAG(2);                                                               \
    }                                                                                          \
  }
  static_assert(CVPRE <= 16, "the later pieces need the barriers of the segments");
  const unsigned rb = lds_base(reinterpret_cast<char*>(((c & 1) ? Rs : Rc) + w * CRN + c + 8 * g));
#pragma unroll
  for (int j = 0; j < 16; ++j) acc[j] = f32x4{};
  const __amdgpu_buffer_rsrc_t rx = MKRS(ws + OFF_X2, SZ_X), rz = MKRS(ws + OFF_X3, SZ_X);
  const __amdgpu_buffer_rsrc_t rf = MKRS(FL + FL_FR, 64 * 16 * 128);
  unsigned st = 0u;
  v2u xx = {0u, 0u};
#define X2_LOAD(ss, far)                                                                                                      \
  {                                                                                                                      \
    xx = LD64(rx, (l >> 1) * (DM * 2) + (l & 1) * 8, (far) ? (int)SZ_X : ((ss) * SEG * DM + 8 * cv) * 2);                 \
    st = __builtin_amdgcn_raw_buffer_load_b32(rf, (l >> 4) * 128 + ((l >> 1) & 7) * 4, ((ss) * 4 + (blockIdx.x & 3)) * 512, AUX_SC1); \
  }
  unsigned tl = now10();
  for (int s = -CVPRE; s < NSEG; ++s) {
   if (s >= 0) {
    const int q = s & 3;
    if (w == 1) {
      for (unsigned spins = 0; !__all(umin3(xx.x, xx.y, st) != 0u); ++spins) {
        if (spin_timeout(spins, TMO)) break;
        if (GATE_V && spins == 0 && !__any(st != 0u)) gate(tl);
        if (spins && !__all(st != 0u)) __builtin_amdgcn_s_sleep(SLP_V);
        X2_LOAD(s, __builtin_popcountll(__ballot(st != 0u)) < CVTHR);
      }
      tl = now10() + GATE_V;
      TS(0, s, 64);
      const float mb = lo16(st), ri = 1.0f / hi16(st);
      const unsigned t0 = pk2(lo16(xx.x) - mb, hi16(xx.x) - mb), t1 = pk2(lo16(xx.y) - mb, hi16(xx.y) - mb);
      const unsigned u0 = pk2(lo16(t0) * ri, hi16(t0) * ri), u1 = pk2(lo16(t1) * ri, hi16(t1) * ri);
      u16* rp = Rc + (l & 1) * 4 * CRN + 32 * q + (l >> 1);
#define R_PUT(o) { rp[o] = (u16)u0; rp[(o) + CRN] = (u16)(u0 >> 16); rp[(o) + 2 * CRN] = (u16)u1; rp[(o) + 3 * CRN] = (u16)(u1 >> 16); }
      R_PUT(0) R_PUT(8 * CRN + 63)
      if (q == 0) { R_PUT(128) R_PUT(8 * CRN + 63 + 128) }
#undef R_PUT
      *reinterpret_cast<v2u*>(Sx + l * 4) = xx;
    }
    const u16* kq = ka + 32 * q;
    const bf16x8 a0 = *reinterpret_cast<const bf16x8*>(kq - 32), a1 = *reinterpret_cast<const bf16x8*>(kq);
    __syncthreads();
    TS(1, s, 0);
    const bf16x8 bp = BFRAG(rb + 64 * ((q + 3) & 3)), bc = BFRAG(rb + 64 * q);
    const float u0 = bf2f(Rc[w * CRN + 32 * q + c]), u1 = bf2f(Rc[w * CRN + 32 * q + 16 + c]);
    const float x0 = bf2f(Sx[c * 8 + w]), x1 = bf2f(Sx[(16 + c) * 8 + w]);
    acc[0] = MFMA16(a0, bp, (q == 0 ? acc[1] : acc[0]));
    const f32x4 tmp = MFMA16(a1, bc, acc[0]);
    const unsigned uu = pk2(u0 * dsk, u1 * dsk);
    const unsigned yy = pk2(xsum32((q & 1) ? tmp[2] : tmp[0]), xsum32((q & 1) ? tmp[3] : tmp[1]));
    const unsigned aa = pk2(lo16(yy) + lo16(uu), hi16(yy) + hi16(uu)), oo = pk2(x0 + lo16(aa), x1 + hi16(aa));
    if (g == (q >> 1)) {
      To[c * 8 + w] = (u16)oo;
      To[(16 + c) * 8 + w] = (u16)(oo >> 16);
    }
    __syncthreads();
    if (w == 0 && l < 32) {
      const v4u o = *reinterpret_cast<const v4u*>(To + l * 8);
      ST128(o, rz, l * 16, (s * 64 + cv) * 512);
      ST64(part_stats(o), rps, l * 8, ((64 + s) * 64 + cv) * 256);
    }
    if (w == 1) X2_LOAD(s + 1, CVTHR > 0);
    const int left = 15 - (s >> 2);
#define PUSH(J0, SH)                                                                                              \
  if (left >= (J0)) {                                                                                             \
    _Pragma("unroll") for (int j = (J0); j < (J0) + 4 && j < 16; ++j)                                             \
      acc[j] = MFMA16(*reinterpret_cast<const bf16x8*>(kq - 32 - 128 * j), bp, (j + (SH) < 16 ? acc[(j + (SH)) & 15] : f32x4{})); \
  }
    TS(2, s, 0);
    if (w == 0) {
      DRAIN();
      if (l < 4) set_flag(FL + FL_FC + (((64 + s) * 4 + l) * 8 + (cv >> 3)) * 32 + (cv & 7));
    }
    TS(3, s, 0);
    if (q == 0) { PUSH(1, 1) PUSH(5, 1) PUSH(9, 1) PUSH(13, 1) }
    else { PUSH(1, 0) PUSH(5, 0) PUSH(9, 0) PUSH(13, 0) }
#undef PUSH
    MSTAMP();
   }
    if (s < 20 - CVPRE) FAR_PIECE(s + CVPRE);
  }
#undef KFRAG
#undef BFRAG
#undef X2_LOAD
#undef FAR_PIECE
#undef FAR_HALF
#undef TILE_OUT
#undef SKIP
#undef CHUNK_ST
#undef CHUNK_FLAG
}

__device__ __forceinline__ v4u ln_row_packed(const v4u xr) {
  float v[8];
#pragma unroll
  for (int e = 0; e < 4; ++e) { v[2 * e] = lo16(xr[e]); v[2 * e + 1] = hi16(xr[e]); }
  float s = ((v[0] + v[1]) + (v[2] + v[3])) + ((v[4] + v[5]) + (v[6] + v[7]));
  s = wave_sum(s);
  const float mean = s * (1.0f / DM);
  float q = 0.f;
#pragma unroll
  for (int j = 0; j < 8; ++j) { float t = v[j] - mean; q += t * t; }
  q = wave_sum(q);
  const float var = q * (1.0f / DM);
  const float mb = bfr(mean);
  const float sd = bfr(sqrtf(bfr(bfr(var) + 1e-5f)));
  const float ri = 1.0f / sd;
  v4u o;
#pragma unroll
  for (int e = 0; e < 4; ++e) {
    const unsigned t = pk2(v[2 * e] - mb, v[2 * e + 1] - mb);
    const float x0 = lo16(t), x1 = hi16(t);
    float q0 = x0 * ri, q1 = x1 * ri;
    q0 = __builtin_fmaf(__builtin_fmaf(-q0, sd, x0), ri, q0);
    q1 = __builtin_fmaf(__builtin_fmaf(-q1, sd, x1), ri, q1);
    o[e] = pk2(q0, q1);
  }
  return o;
}

__device__ __forceinline__ float bfr1(float x) { return lo16(pk2(x, x)); }
__device__ __forceinline__ void ln4_packed(v4u (&x)[4]) {
  f32x2_t v[16];
#pragma unroll
  for (int e = 0; e < 16; ++e) v[e] = f32x2_t{lo16(x[e >> 2][e & 3]), hi16(x[e >> 2][e & 3])};
  const f32x2_t t = (((v[0] + v[1]) + (v[2] + v[3])) + ((v[4] + v[5]) + (v[6] + v[7]))) +
                    (((v[8] + v[9]) + (v[10] + v[11])) + ((v[12] + v[13]) + (v[14] + v[15])));
  const float mean = red_mod4(t.x + t.y) * (1.0f / DM);
  const float mb = bfr1(mean);
  f32x2_t q0 = {0.f, 0.f}, q1 = {0.f, 0.f};
#pragma unroll
  for (int e = 0; e < 16; e += 2) {
    v[e] -= mb;
    v[e + 1] -= mb;
    q0 += v[e] * v[e];
    q1 += v[e + 1] * v[e + 1];
  }
  q0 += q1;
  const float dm = mean - mb;
  const float var = fmaxf(red_mod4(q0.x + q0.y) * (1.0f / DM) - dm * dm, 0.f);
  const float sd = bfr1(sqrtf(bfr1(bfr1(var) + 1e-5f)));
  const float ri = 1.0f / sd;
#pragma unroll
  for (int e = 0; e < 16; ++e) {
    const unsigned t2 = pk2(v[e].x, v[e].y);
    const f32x2_t q = f32x2_t{lo16(t2), hi16(t2)} * ri;
    x[e >> 2][e & 3] = pk2(q.x, q.y);
  }
}

#define Q_ASM \
  "v_readfirstlane_b32 s78, v6\n" \
  "s_mov_b32 s79, 0\n" \
  "s_mov_b32 s81, 0\n" \
  "s_mov_b32 s83, 0x10000\n" \
  "s_mov_b32 s76, 0\n" \
  "s_mov_b32 s77, -1\n" \
  "s_cmp_lg_u32 s78, 0\n" \
  "s_cbranch_scc1 .LCQ_GOT_%=\n" \
  "s_branch .LCQ_POLL_%=\n" \
  ".LCQ_TOP_%=:\n" \
  "s_waitcnt lgkmcnt(0)\n" \
  "s_barrier\n" \
  "s_sub_i32 s83, 0, s83\n" \
  "v_add_u32 v0, s83, v0\n" \
  "ds_read_b128 v[18:21], v0 offset:0\n" \
  "ds_read_b128 v[22:25], v0 offset:2048\n" \
  "ds_read_b128 v[26:29], v0 offset:4096\n" \
  "ds_read2_b32 v[6:7], v3 offset0:16 offset1:48\n" \
  "ds_read_b128 v[10:13], v4 offset:256\n" \
  "ds_read_b128 v[14:17], v4 offset:320\n" \
  "ds_read_b32 v8, v3 offset:384\n" \
  "s_add_u32 s71, s71, 0x1000\n" \
  "s_mov_b32 s78, s80\n" \
  "s_xor_b32 s74, s74, 2048\n" \
  "s_xor_b32 s75, s75, 2048\n" \
  "s_setprio 3\n" \
  "ds_read_b128 v[30:33], v0 offset:6144\n" \
  "s_waitcnt lgkmcnt(7)\n" \
  "v_mfma_f32_16x16x32_bf16 v[34:37], v[64:67], v[18:21], 0\n" \
  "v_mfma_f32_16x16x32_bf16 v[38:41], v[128:131], v[18:21], 0\n" \
  "v_mfma_f32_16x16x32_bf16 v[42:45], v[18:21], v[192:195], 0\n" \
  "s_cmp_eq_u32 s78, 1\n" \
  "s_cbranch_scc0 .LCQ_X1_%=\n" \
  "s_mov_b32 m0, s73\n" \
  "s_nop 0\n" \
  "global_load_lds_dwordx4 v2, s[62:63] offset:0 sc1\n" \
  "global_load_lds_dwordx4 v2, s[62:63] offset:1024 sc1\n" \
  "s_add_u32 s62, s62, 0x4000\n" \
  "s_addc_u32 s63, s63, 0\n" \
  ".LCQ_X1_%=:\n" \
  "ds_read_b128 v[18:21], v0 offset:16384\n" \
  "s_waitcnt lgkmcnt(7)\n" \
  "v_mfma_f32_16x16x32_bf16 v[34:37], v[68:71], v[22:25], v[34:37]\n" \
  "s_waitcnt lgkmcnt(4)\n" \
  "v_fma_f32 v48, -v6, v10, v48\n" \
  "v_fma_f32 v49, -v6, v11, v49\n" \
  "v_mfma_f32_16x16x32_bf16 v[38:41], v[132:135], v[22:25], v[38:41]\n" \
  "v_fma_f32 v50, -v6, v12, v50\n" \
  "v_fma_f32 v51, -v6, v13, v51\n" \
  "v_mfma_f32_16x16x32_bf16 v[42:45], v[22:25], v[196:199], v[42:45]\n" \
  "ds_read_b128 v[10:13], v4 offset:64\n" \
  "s_waitcnt lgkmcnt(4)\n" \
  "v_fma_f32 v54, -v6, v14, v54\n" \
  "v_fma_f32 v55, -v6, v15, v55\n" \
  "s_cmp_eq_u32 s78, 1\n" \
  "s_cbranch_scc0 .LCQ_X2_%=\n" \
  "s_mov_b32 m0, s68\n" \
  "s_xor_b32 s68, s68, 0x10000\n" \
  "global_load_lds_dwordx4 v2, s[60:61] offset:0 sc1\n" \
  "global_load_lds_dwordx4 v2, s[60:61] offset:1024 sc1\n" \
  ".LCQ_X2_%=:\n" \
  "ds_read_b128 v[22:25], v0 offset:18432\n" \
  "v_mfma_f32_16x16x32_bf16 v[34:37], v[72:75], v[26:29], v[34:37]\n" \
  "v_fma_f32 v56, -v6, v16, v56\n" \
  "v_fma_f32 v57, -v6, v17, v57\n" \
  "v_mfma_f32_16x16x32_bf16 v[38:41], v[136:139], v[26:29], v[38:41]\n" \
  "ds_read_b128 v[14:17], v4 offset:192\n" \
  "v_mul_f32 v48, v48, v7\n" \
  "v_mul_f32 v49, v49, v7\n" \
  "v_mfma_f32_16x16x32_bf16 v[42:45], v[26:29], v[200:203], v[42:45]\n" \
  "v_mul_f32 v50, v50, v7\n" \
  "v_mul_f32 v51, v51, v7\n" \
  "s_cmp_eq_u32 s78, 1\n" \
  "s_cbranch_scc0 .LCQ_X3_%=\n" \
  "global_load_lds_dwordx4 v2, s[60:61] offset:2048 sc1\n" \
  "global_load_lds_dwordx4 v2, s[60:61] offset:3072 sc1\n" \
  "s_add_u32 s60, s60, 0x8000\n" \
  "s_addc_u32 s61, s61, 0\n" \
  ".LCQ_X3_%=:\n" \
  "ds_read_b128 v[26:29], v0 offset:20480\n" \
  "s_waitcnt lgkmcnt(5)\n" \
  "v_mfma_f32_16x16x32_bf16 v[34:37], v[76:79], v[30:33], v[34:37]\n" \
  "v_cvt_pk_bf16_f32 v48, v48, v49\n" \
  "v_cvt_pk_bf16_f32 v49, v50, v51\n" \
  "v_mfma_f32_16x16x32_bf16 v[38:41], v[140:143], v[30:33], v[38:41]\n" \
  "v_mul_f32 v54, v54, v7\n" \
  "v_mul_f32 v55, v55, v7\n" \
  "v_mfma_f32_16x16x32_bf16 v[42:45], v[30:33], v[204:207], v[42:45]\n" \
  "v_mul_f32 v56, v56, v7\n" \
  "v_mul_f32 v57, v57, v7\n" \
  "s_cmp_eq_u32 s78, 1\n" \
  "s_cbranch_scc0 .LCQ_X4_%=\n" \
  "s_add_u32 s87, s71, 0x1000\n" \
  "v_bfe_u32 v5, v2, 2, 5\n" \
  "buffer_load_dword v5, v5, s[52:55], s87 offen sc1\n" \
  ".LCQ_X4_%=:\n" \
  "ds_read_b128 v[30:33], v0 offset:22528\n" \
  "s_waitcnt lgkmcnt(5)\n" \
  "v_mfma_f32_16x16x32_bf16 v[34:37], v[80:83], v[18:21], v[34:37]\n" \
  "v_cvt_pk_bf16_f32 v54, v54, v55\n" \
  "v_cvt_pk_bf16_f32 v55, v56, v57\n" \
  "v_mfma_f32_16x16x32_bf16 v[38:41], v[144:147], v[18:21], v[38:41]\n" \
  "s_waitcnt lgkmcnt(4)\n" \
  "v_fma_f32 v60, -v10, v8, v60\n" \
  "v_fma_f32 v61, -v11, v8, v61\n" \
  "v_mfma_f32_16x16x32_bf16 v[42:45], v[18:21], v[208:211], v[42:45]\n" \
  "v_fma_f32 v62, -v12, v8, v62\n" \
  "v_fma_f32 v63, -v13, v8, v63\n" \
  "ds_read_b128 v[18:21], v0 offset:32768\n" \
  "s_waitcnt lgkmcnt(4)\n" \
  "v_mfma_f32_16x16x32_bf16 v[34:37], v[84:87], v[22:25], v[34:37]\n" \
  "v_permlane16_swap_b32 v46, v48\n" \
  "v_mfma_f32_16x16x32_bf16 v[38:41], v[148:151], v[22:25], v[38:41]\n" \
  "v_permlane16_swap_b32 v47, v49\n" \
  "v_mfma_f32_16x16x32_bf16 v[42:45], v[22:25], v[212:215], v[42:45]\n" \
  "v_permlane16_swap_b32 v52, v54\n" \
  "ds_read_b128 v[22:25], v0 offset:34816\n" \
  "s_waitcnt lgkmcnt(3)\n" \
  "v_mfma_f32_16x16x32_bf16 v[34:37], v[88:91], v[26:29], v[34:37]\n" \
  "v_permlane16_swap_b32 v53, v55\n" \
  "v_mfma_f32_16x16x32_bf16 v[38:41], v[152:155], v[26:29], v[38:41]\n" \
  "v_mul_f32 v60, v60, v14\n" \
  "v_mul_f32 v61, v61, v15\n" \
  "v_mfma_f32_16x16x32_bf16 v[42:45], v[26:29], v[216:219], v[42:45]\n" \
  "v_mul_f32 v62, v62, v16\n" \
  "v_mul_f32 v63, v63, v17\n" \
  "ds_read_b128 v[26:29], v0 offset:36864\n" \
  "s_waitcnt lgkmcnt(3)\n" \
  "v_mfma_f32_16x16x32_bf16 v[34:37], v[92:95], v[30:33], v[34:37]\n" \
  "v_cvt_pk_bf16_f32 v60, v60, v61\n" \
  "v_cvt_pk_bf16_f32 v61, v62, v63\n" \
  "v_mfma_f32_16x16x32_bf16 v[38:41], v[156:159], v[30:33], v[38:41]\n" \
  "v_mov_b64 v[10:11], 0\n" \
  "v_lshrrev_b32 v12, 1, v2\n" \
  "v_mfma_f32_16x16x32_bf16 v[42:45], v[30:33], v[220:223], v[42:45]\n" \
  "s_cmp_eq_u32 s81, 0\n" \
  "s_cbranch_scc1 .LCQ_S5_%=\n" \
  "s_add_u32 s87, s65, 0x2000\n" \
  "buffer_store_dwordx4 v[52:55], v1, s[40:43], s87 offen sc1\n" \
  "buffer_store_dwordx4 v[58:61], v2, s[44:47], s66 offen sc1\n" \
  "buffer_store_dwordx4 v[46:49], v1, s[40:43], s65 offen sc1\n" \
  "buffer_store_dwordx2 v[10:11], v12, s[48:51], s67 offen sc1\n" \
  "s_add_u32 s65, s65, 0x80000\n" \
  "s_add_u32 s66, s66, 0x40000\n" \
  "s_add_u32 s67, s67, 0x10000\n" \
  "s_mov_b32 s84, s72\n" \
  "s_add_u32 s72, s72, 0x1000\n" \
  "s_mov_b32 s85, s70\n" \
  "s_xor_b32 s70, s70, 4\n" \
  "s_mov_b32 s79, 1\n" \
  ".LCQ_S5_%=:\n" \
  "s_setprio 2\n" \
  "ds_read_b128 v[30:33], v0 offset:38912\n" \
  "s_waitcnt lgkmcnt(3)\n" \
  "v_mfma_f32_16x16x32_bf16 v[34:37], v[96:99], v[18:21], v[34:37]\n" \
  "v_bfe_u32 v7, v2, 4, 5\n" \
  "v_and_b32 v8, 0x200, v2\n" \
  "v_mfma_f32_16x16x32_bf16 v[38:41], v[160:163], v[18:21], v[38:41]\n" \
  "v_lshl_add_u32 v6, v7, 3, s74\n" \
  "v_lshl_add_u32 v6, v8, 1, v6\n" \
  "v_mfma_f32_16x16x32_bf16 v[42:45], v[18:21], v[224:227], v[42:45]\n" \
  "ds_read2_b64 v[10:13], v6 offset0:0 offset1:32\n" \
  "ds_read2_b64 v[14:17], v6 offset0:64 offset1:96\n" \
  "v_lshrrev_b32 v7, 2, v2\n" \
  "s_andn2_b32 s82, s79, s81\n" \
  "s_cbranch_scc0 .LCQ_NPA_%=\n" \
  "s_waitcnt vmcnt(7)\n" \
  "s_cmp_lg_u32 s78, 0\n" \
  "s_cbranch_scc1 .LCQ_PA_%=\n" \
  "s_waitcnt vmcnt(0)\n" \
  ".LCQ_PA_%=:\n" \
  "v_mov_b32 v9, s85\n" \
  "s_mov_b64 exec, 2\n" \
  "ds_add_rtn_u32 v9, v9, v2\n" \
  "s_mov_b64 exec, -1\n" \
  ".LCQ_NPA_%=:\n" \
  "s_cmp_eq_u32 s78, 0\n" \
  "s_cbranch_scc0 .LCQ_X6_%=\n" \
  "v_bfe_u32 v5, v2, 2, 5\n" \
  "buffer_load_dword v5, v5, s[52:55], s71 offen sc1\n" \
  ".LCQ_X6_%=:\n" \
  "ds_read_b128 v[18:21], v0 offset:49152\n" \
  "s_waitcnt lgkmcnt(5)\n" \
  "v_mfma_f32_16x16x32_bf16 v[34:37], v[100:103], v[22:25], v[34:37]\n" \
  "s_waitcnt lgkmcnt(1)\n" \
  "v_add_f32 v6, v10, v12\n" \
  "v_add_f32 v8, v14, v16\n" \
  "v_mfma_f32_16x16x32_bf16 v[38:41], v[164:167], v[22:25], v[38:41]\n" \
  "v_add_f32 v6, v6, v8\n" \
  "v_mov_b32 v8, v6\n" \
  "v_mfma_f32_16x16x32_bf16 v[42:45], v[22:25], v[228:231], v[42:45]\n" \
  "v_add_f32 v11, v11, v13\n" \
  "v_add_f32 v15, v15, v17\n" \
  "ds_read_b128 v[22:25], v0 offset:51200\n" \
  "v_mfma_f32_16x16x32_bf16 v[34:37], v[104:107], v[26:29], v[34:37]\n" \
  "v_permlane32_swap_b32 v6, v8\n" \
  "v_mfma_f32_16x16x32_bf16 v[38:41], v[168:171], v[26:29], v[38:41]\n" \
  "v_add_f32 v6, v6, v8\n" \
  "v_fmamk_f32 v10, v6, 0xbe000000, v10\n" \
  "v_mfma_f32_16x16x32_bf16 v[42:45], v[26:29], v[232:235], v[42:45]\n" \
  "v_fmamk_f32 v12, v6, 0xbe000000, v12\n" \
  "v_fmamk_f32 v14, v6, 0xbe000000, v14\n" \
  "s_cmp_eq_u32 s82, 1\n" \
  "s_cbranch_scc0 .LCQ_X8_%=\n" \
  "s_waitcnt lgkmcnt(2)\n" \
  "v_readlane_b32 s87, v9, 1\n" \
  "s_mov_b32 s79, 0\n" \
  "s_and_b32 s87, s87, 0x70\n" \
  "s_cmp_eq_u32 s87, 0x70\n" \
  "s_cbranch_scc0 .LCQ_F7_%=\n" \
  "s_mov_b64 exec, 2\n" \
  "buffer_store_dword v9, off, s[56:59], s84 sc1\n" \
  "s_mov_b64 exec, -1\n" \
  ".LCQ_F7_%=:\n" \
  ".LCQ_X8_%=:\n" \
  "ds_read_b128 v[26:29], v0 offset:53248\n" \
  "v_mfma_f32_16x16x32_bf16 v[34:37], v[108:111], v[30:33], v[34:37]\n" \
  "v_fmamk_f32 v16, v6, 0xbe000000, v16\n" \
  "v_add_f32 v11, v11, v15\n" \
  "v_mfma_f32_16x16x32_bf16 v[38:41], v[172:175], v[30:33], v[38:41]\n" \
  "v_mul_f32 v10, v10, v10\n" \
  "v_fmac_f32 v10, v12, v12\n" \
  "v_mfma_f32_16x16x32_bf16 v[42:45], v[30:33], v[236:239], v[42:45]\n" \
  "v_fmac_f32 v10, v14, v14\n" \
  "v_fmac_f32 v10, v16, v16\n" \
  "ds_read_b128 v[30:33], v0 offset:55296\n" \
  "s_waitcnt lgkmcnt(3)\n" \
  "v_mfma_f32_16x16x32_bf16 v[34:37], v[112:115], v[18:21], v[34:37]\n" \
  "v_fmamk_f32 v11, v10, 0x3c800000, v11\n" \
  "v_mov_b32 v8, v11\n" \
  "v_mfma_f32_16x16x32_bf16 v[38:41], v[176:179], v[18:21], v[38:41]\n" \
  "v_mul_f32 v6, 0x3b000000, v6\n" \
  "v_add_u32 v7, s69, v7\n" \
  "v_mfma_f32_16x16x32_bf16 v[42:45], v[18:21], v[240:243], v[42:45]\n" \
  "v_permlane32_swap_b32 v11, v8\n" \
  "ds_read_b128 v[18:21], v0 offset:256\n" \
  "s_waitcnt lgkmcnt(3)\n" \
  "v_mfma_f32_16x16x32_bf16 v[34:37], v[116:119], v[22:25], v[34:37]\n" \
  "v_add_f32 v11, v11, v8\n" \
  "v_mul_f32 v10, 0x3b000000, v11\n" \
  "v_mfma_f32_16x16x32_bf16 v[38:41], v[180:183], v[22:25], v[38:41]\n" \
  "v_cvt_pk_bf16_f32 v6, v6, v10\n" \
  "v_and_b32 v10, 0xffff0000, v6\n" \
  "v_mfma_f32_16x16x32_bf16 v[42:45], v[22:25], v[244:247], v[42:45]\n" \
  "v_lshlrev_b32 v6, 16, v6\n" \
  "v_add_f32 v10, 0x3727c5ac, v10\n" \
  "ds_read_b128 v[22:25], v0 offset:2304\n" \
  "s_waitcnt lgkmcnt(3)\n" \
  "v_mfma_f32_16x16x32_bf16 v[34:37], v[120:123], v[26:29], v[34:37]\n" \
  "v_cvt_pk_bf16_f32 v10, v10, v10\n" \
  "v_lshlrev_b32 v10, 16, v10\n" \
  "v_mfma_f32_16x16x32_bf16 v[38:41], v[184:187], v[26:29], v[38:41]\n" \
  "v_sqrt_f32 v10, v10\n" \
  "s_nop 0\n" \
  "v_cvt_pk_bf16_f32 v10, v10, v10\n" \
  "v_mfma_f32_16x16x32_bf16 v[42:45], v[26:29], v[248:251], v[42:45]\n" \
  "v_lshlrev_b32 v10, 16, v10\n" \
  "v_rcp_f32 v10, v10\n" \
  "ds_read_b128 v[26:29], v0 offset:4352\n" \
  "s_waitcnt lgkmcnt(3)\n" \
  "v_mfma_f32_16x16x32_bf16 v[34:37], v[124:127], v[30:33], v[34:37]\n" \
  "s_nop 0\n" \
  "v_cndmask_b32 v10, v6, v10, s[76:77]\n" \
  "ds_write_b32 v7, v10\n" \
  "v_mfma_f32_16x16x32_bf16 v[38:41], v[188:191], v[30:33], v[38:41]\n" \
  "v_mfma_f32_16x16x32_bf16 v[42:45], v[30:33], v[252:255], v[42:45]\n" \
  "s_setprio 1\n" \
  "ds_read_b128 v[30:33], v0 offset:6400\n" \
  "s_waitcnt lgkmcnt(4)\n" \
  "v_mfma_f32_16x16x32_bf16 v[48:51], v[64:67], v[18:21], 0\n" \
  "ds_read2_b32 v[6:7], v3 offset0:0 offset1:32\n" \
  "ds_read_b128 v[10:13], v4 offset:256\n" \
  "ds_read_b128 v[14:17], v4 offset:320\n" \
  "ds_read_b32 v8, v3 offset:384\n" \
  "v_mfma_f32_16x16x32_bf16 v[54:57], v[128:131], v[18:21], 0\n" \
  "v_mfma_f32_16x16x32_bf16 v[60:63], v[18:21], v[192:195], 0\n" \
  "ds_read_b128 v[18:21], v0 offset:16640\n" \
  "s_waitcnt lgkmcnt(8)\n" \
  "v_mfma_f32_16x16x32_bf16 v[48:51], v[68:71], v[22:25], v[48:51]\n" \
  "s_waitcnt lgkmcnt(3)\n" \
  "v_fma_f32 v34, -v6, v10, v34\n" \
  "v_fma_f32 v35, -v6, v11, v35\n" \
  "v_mfma_f32_16x16x32_bf16 v[54:57], v[132:135], v[22:25], v[54:57]\n" \
  "v_fma_f32 v36, -v6, v12, v36\n" \
  "v_fma_f32 v37, -v6, v13, v37\n" \
  "v_mfma_f32_16x16x32_bf16 v[60:63], v[22:25], v[196:199], v[60:63]\n" \
  "ds_read_b128 v[10:13], v4 offset:0\n" \
  "s_waitcnt lgkmcnt(3)\n" \
  "v_fma_f32 v38, -v6, v14, v38\n" \
  "v_fma_f32 v39, -v6, v15, v39\n" \
  "s_mov_b32 s82, 0\n" \
  "s_cmp_eq_u32 s78, 0\n" \
  "s_cbranch_scc0 .LCQ_X9_%=\n" \
  "s_waitcnt vmcnt(0)\n" \
  "v_cmp_eq_u32 vcc, 0, v5\n" \
  "s_cmp_eq_u64 vcc, 0\n" \
  "s_cselect_b32 s78, 2, 0\n" \
  "s_mov_b32 s82, s79\n" \
  ".LCQ_X9_%=:\n" \
  "s_cmp_eq_u32 s82, 1\n" \
  "s_cbranch_scc0 .LCQ_X10_%=\n" \
  "v_mov_b32 v9, s85\n" \
  "s_mov_b64 exec, 2\n" \
  "ds_add_rtn_u32 v9, v9, v2\n" \
  "s_mov_b64 exec, -1\n" \
  ".LCQ_X10_%=:\n" \
  "s_cmp_eq_u32 s78, 2\n" \
  "s_cbranch_scc0 .LCQ_X11_%=\n" \
  "s_mov_b32 m0, s73\n" \
  "s_nop 0\n" \
  "global_load_lds_dwordx4 v2, s[62:63] offset:0 sc1\n" \
  "global_load_lds_dwordx4 v2, s[62:63] offset:1024 sc1\n" \
  "s_add_u32 s62, s62, 0x4000\n" \
  "s_addc_u32 s63, s63, 0\n" \
  ".LCQ_X11_%=:\n" \
  "ds_read_b128 v[22:25], v0 offset:18688\n" \
  "v_mfma_f32_16x16x32_bf16 v[48:51], v[72:75], v[26:29], v[48:51]\n" \
  "v_fma_f32 v40, -v6, v16, v40\n" \
  "v_fma_f32 v41, -v6, v17, v41\n" \
  "v_mfma_f32_16x16x32_bf16 v[54:57], v[136:139], v[26:29], v[54:57]\n" \
  "ds_read_b128 v[14:17], v4 offset:128\n" \
  "v_mul_f32 v34, v34, v7\n" \
  "v_mul_f32 v35, v35, v7\n" \
  "v_mfma_f32_16x16x32_bf16 v[60:63], v[26:29], v[200:203], v[60:63]\n" \
  "v_mul_f32 v36, v36, v7\n" \
  "v_mul_f32 v37, v37, v7\n" \
  "s_cmp_eq_u32 s78, 2\n" \
  "s_cbranch_scc0 .LCQ_X12_%=\n" \
  "s_mov_b32 m0, s68\n" \
  "s_xor_b32 s68, s68, 0x10000\n" \
  "global_load_lds_dwordx4 v2, s[60:61] offset:0 sc1\n" \
  "global_load_lds_dwordx4 v2, s[60:61] offset:1024 sc1\n" \
  ".LCQ_X12_%=:\n" \
  "ds_read_b128 v[26:29], v0 offset:20736\n" \
  "v_mfma_f32_16x16x32_bf16 v[48:51], v[76:79], v[30:33], v[48:51]\n" \
  "v_cvt_pk_bf16_f32 v46, v34, v35\n" \
  "v_cvt_pk_bf16_f32 v47, v36, v37\n" \
  "v_mfma_f32_16x16x32_bf16 v[54:57], v[140:143], v[30:33], v[54:57]\n" \
  "v_mul_f32 v38, v38, v7\n" \
  "v_mul_f32 v39, v39, v7\n" \
  "v_mfma_f32_16x16x32_bf16 v[60:63], v[30:33], v[204:207], v[60:63]\n" \
  "v_mul_f32 v40, v40, v7\n" \
  "v_mul_f32 v41, v41, v7\n" \
  "s_cmp_eq_u32 s82, 1\n" \
  "s_cbranch_scc0 .LCQ_X14_%=\n" \
  "s_waitcnt lgkmcnt(2)\n" \
  "v_readlane_b32 s87, v9, 1\n" \
  "s_mov_b32 s79, 0\n" \
  "s_and_b32 s87, s87, 0x70\n" \
  "s_cmp_eq_u32 s87, 0x70\n" \
  "s_cbranch_scc0 .LCQ_F13_%=\n" \
  "s_mov_b64 exec, 2\n" \
  "buffer_store_dword v9, off, s[56:59], s84 sc1\n" \
  "s_mov_b64 exec, -1\n" \
  ".LCQ_F13_%=:\n" \
  ".LCQ_X14_%=:\n" \
  "s_cmp_eq_u32 s78, 2\n" \
  "s_cbranch_scc0 .LCQ_X15_%=\n" \
  "global_load_lds_dwordx4 v2, s[60:61] offset:2048 sc1\n" \
  "global_load_lds_dwordx4 v2, s[60:61] offset:3072 sc1\n" \
  "s_add_u32 s60, s60, 0x8000\n" \
  "s_addc_u32 s61, s61, 0\n" \
  ".LCQ_X15_%=:\n" \
  "ds_read_b128 v[30:33], v0 offset:22784\n" \
  "s_waitcnt lgkmcnt(5)\n" \
  "v_mfma_f32_16x16x32_bf16 v[48:51], v[80:83], v[18:21], v[48:51]\n" \
  "v_cvt_pk_bf16_f32 v52, v38, v39\n" \
  "v_cvt_pk_bf16_f32 v53, v40, v41\n" \
  "v_mfma_f32_16x16x32_bf16 v[54:57], v[144:147], v[18:21], v[54:57]\n" \
  "s_waitcnt lgkmcnt(4)\n" \
  "v_fma_f32 v42, -v10, v8, v42\n" \
  "v_fma_f32 v43, -v11, v8, v43\n" \
  "v_mfma_f32_16x16x32_bf16 v[60:63], v[18:21], v[208:211], v[60:63]\n" \
  "v_fma_f32 v44, -v12, v8, v44\n" \
  "v_fma_f32 v45, -v13, v8, v45\n" \
  "s_cmp_eq_u32 s78, 2\n" \
  "s_cbranch_scc0 .LCQ_X16_%=\n" \
  "s_add_u32 s87, s71, 0x1000\n" \
  "v_bfe_u32 v5, v2, 2, 5\n" \
  "buffer_load_dword v5, v5, s[52:55], s87 offen sc1\n" \
  ".LCQ_X16_%=:\n" \
  "s_and_b32 s82, s79, s78\n" \
  "s_cmp_eq_u32 s78, 1\n" \
  "s_cbranch_scc0 .LCQ_X17_%=\n" \
  "s_waitcnt vmcnt(0)\n" \
  ".LCQ_X17_%=:\n" \
  "s_cmp_eq_u32 s82, 1\n" \
  "s_cbranch_scc0 .LCQ_X18_%=\n" \
  "v_mov_b32 v9, s85\n" \
  "s_mov_b64 exec, 2\n" \
  "ds_add_rtn_u32 v9, v9, v2\n" \
  "s_mov_b64 exec, -1\n" \
  ".LCQ_X18_%=:\n" \
  "ds_read_b128 v[18:21], v0 offset:33024\n" \
  "s_waitcnt lgkmcnt(4)\n" \
  "v_mfma_f32_16x16x32_bf16 v[48:51], v[84:87], v[22:25], v[48:51]\n" \
  "s_waitcnt lgkmcnt(3)\n" \
  "v_mul_f32 v42, v42, v14\n" \
  "v_mul_f32 v43, v43, v15\n" \
  "v_mfma_f32_16x16x32_bf16 v[54:57], v[148:151], v[22:25], v[54:57]\n" \
  "v_mul_f32 v44, v44, v16\n" \
  "v_mul_f32 v45, v45, v17\n" \
  "v_mfma_f32_16x16x32_bf16 v[60:63], v[22:25], v[212:215], v[60:63]\n" \
  "v_cvt_pk_bf16_f32 v58, v42, v43\n" \
  "v_cvt_pk_bf16_f32 v59, v44, v45\n" \
  "ds_read_b128 v[22:25], v0 offset:35072\n" \
  "s_waitcnt lgkmcnt(3)\n" \
  "v_mfma_f32_16x16x32_bf16 v[48:51], v[88:91], v[26:29], v[48:51]\n" \
  "v_mfma_f32_16x16x32_bf16 v[54:57], v[152:155], v[26:29], v[54:57]\n" \
  "v_mfma_f32_16x16x32_bf16 v[60:63], v[26:29], v[216:219], v[60:63]\n" \
  "s_cmp_eq_u32 s82, 1\n" \
  "s_cbranch_scc0 .LCQ_X20_%=\n" \
  "s_waitcnt lgkmcnt(2)\n" \
  "v_readlane_b32 s87, v9, 1\n" \
  "s_mov_b32 s79, 0\n" \
  "s_and_b32 s87, s87, 0x70\n" \
  "s_cmp_eq_u32 s87, 0x70\n" \
  "s_cbranch_scc0 .LCQ_F19_%=\n" \
  "s_mov_b64 exec, 2\n" \
  "buffer_store_dword v9, off, s[56:59], s84 sc1\n" \
  "s_mov_b64 exec, -1\n" \
  ".LCQ_F19_%=:\n" \
  ".LCQ_X20_%=:\n" \
  "s_cmp_eq_u32 s78, 0\n" \
  "s_cbranch_scc0 .LCQ_X21_%=\n" \
  "v_bfe_u32 v5, v2, 2, 5\n" \
  "buffer_load_dword v5, v5, s[52:55], s71 offen sc1\n" \
  ".LCQ_X21_%=:\n" \
  "ds_read_b128 v[26:29], v0 offset:37120\n" \
  "s_waitcnt lgkmcnt(3)\n" \
  "v_mfma_f32_16x16x32_bf16 v[48:51], v[92:95], v[30:33], v[48:51]\n" \
  "v_mfma_f32_16x16x32_bf16 v[54:57], v[156:159], v[30:33], v[54:57]\n" \
  "v_mfma_f32_16x16x32_bf16 v[60:63], v[30:33], v[220:223], v[60:63]\n" \
  "s_setprio 0\n" \
  "ds_read_b128 v[30:33], v0 offset:39168\n" \
  "s_waitcnt lgkmcnt(3)\n" \
  "v_mfma_f32_16x16x32_bf16 v[48:51], v[96:99], v[18:21], v[48:51]\n" \
  "v_mfma_f32_16x16x32_bf16 v[54:57], v[160:163], v[18:21], v[54:57]\n" \
  "v_mfma_f32_16x16x32_bf16 v[60:63], v[18:21], v[224:227], v[60:63]\n" \
  "ds_read_b128 v[18:21], v0 offset:49408\n" \
  "s_waitcnt lgkmcnt(3)\n" \
  "v_mfma_f32_16x16x32_bf16 v[48:51], v[100:103], v[22:25], v[48:51]\n" \
  "v_mfma_f32_16x16x32_bf16 v[54:57], v[164:167], v[22:25], v[54:57]\n" \
  "v_mfma_f32_16x16x32_bf16 v[60:63], v[22:25], v[228:231], v[60:63]\n" \
  "ds_read_b128 v[22:25], v0 offset:51456\n" \
  "s_waitcnt lgkmcnt(3)\n" \
  "v_mfma_f32_16x16x32_bf16 v[48:51], v[104:107], v[26:29], v[48:51]\n" \
  "v_bfe_u32 v7, v2, 4, 5\n" \
  "v_and_b32 v8, 0x200, v2\n" \
  "v_mfma_f32_16x16x32_bf16 v[54:57], v[168:171], v[26:29], v[54:57]\n" \
  "v_lshl_add_u32 v6, v7, 3, s73\n" \
  "v_lshl_add_u32 v6, v8, 1, v6\n" \
  "v_mfma_f32_16x16x32_bf16 v[60:63], v[26:29], v[232:235], v[60:63]\n" \
  "s_cmp_eq_u32 s78, 2\n" \
  "s_cbranch_scc0 .LCQ_X22_%=\n" \
  "s_waitcnt vmcnt(5)\n" \
  ".LCQ_X22_%=:\n" \
  "ds_read_b128 v[26:29], v0 offset:53504\n" \
  "s_waitcnt lgkmcnt(3)\n" \
  "v_mfma_f32_16x16x32_bf16 v[48:51], v[108:111], v[30:33], v[48:51]\n" \
  "ds_read2_b64 v[10:13], v6 offset0:0 offset1:32\n" \
  "ds_read2_b64 v[14:17], v6 offset0:64 offset1:96\n" \
  "v_lshl_add_u32 v7, v7, 3, s75\n" \
  "v_mfma_f32_16x16x32_bf16 v[54:57], v[172:175], v[30:33], v[54:57]\n" \
  "v_mfma_f32_16x16x32_bf16 v[60:63], v[30:33], v[236:239], v[60:63]\n" \
  "ds_read_b128 v[30:33], v0 offset:55552\n" \
  "s_waitcnt lgkmcnt(5)\n" \
  "v_mfma_f32_16x16x32_bf16 v[48:51], v[112:115], v[18:21], v[48:51]\n" \
  "s_waitcnt lgkmcnt(1)\n" \
  "v_add_f32 v6, v10, v12\n" \
  "v_add_f32 v8, v14, v16\n" \
  "v_mfma_f32_16x16x32_bf16 v[54:57], v[176:179], v[18:21], v[54:57]\n" \
  "v_add_f32 v6, v6, v8\n" \
  "v_mov_b32 v8, v6\n" \
  "v_mfma_f32_16x16x32_bf16 v[60:63], v[18:21], v[240:243], v[60:63]\n" \
  "v_add_f32 v11, v11, v13\n" \
  "v_add_f32 v15, v15, v17\n" \
  "v_mfma_f32_16x16x32_bf16 v[48:51], v[116:119], v[22:25], v[48:51]\n" \
  "v_permlane32_swap_b32 v6, v8\n" \
  "v_mfma_f32_16x16x32_bf16 v[54:57], v[180:183], v[22:25], v[54:57]\n" \
  "v_add_f32 v6, v6, v8\n" \
  "v_fmamk_f32 v10, v6, 0xbe000000, v10\n" \
  "v_mfma_f32_16x16x32_bf16 v[60:63], v[22:25], v[244:247], v[60:63]\n" \
  "v_fmamk_f32 v12, v6, 0xbe000000, v12\n" \
  "v_fmamk_f32 v14, v6, 0xbe000000, v14\n" \
  "v_mfma_f32_16x16x32_bf16 v[48:51], v[120:123], v[26:29], v[48:51]\n" \
  "v_fmamk_f32 v16, v6, 0xbe000000, v16\n" \
  "v_add_f32 v11, v11, v15\n" \
  "v_mfma_f32_16x16x32_bf16 v[54:57], v[184:187], v[26:29], v[54:57]\n" \
  "v_mul_f32 v10, v10, v10\n" \
  "v_fmac_f32 v10, v12, v12\n" \
  "v_mfma_f32_16x16x32_bf16 v[60:63], v[26:29], v[248:251], v[60:63]\n" \
  "v_fmac_f32 v10, v14, v14\n" \
  "v_fmac_f32 v10, v16, v16\n" \
  "s_waitcnt lgkmcnt(0)\n" \
  "v_mfma_f32_16x16x32_bf16 v[48:51], v[124:127], v[30:33], v[48:51]\n" \
  "v_fmamk_f32 v11, v10, 0x3e000000, v11\n" \
  "v_mov_b32 v8, v11\n" \
  "v_mfma_f32_16x16x32_bf16 v[54:57], v[188:191], v[30:33], v[54:57]\n" \
  "v_mov_b32 v10, v6\n" \
  "s_nop 0\n" \
  "v_permlane32_swap_b32 v11, v8\n" \
  "v_mfma_f32_16x16x32_bf16 v[60:63], v[30:33], v[252:255], v[60:63]\n" \
  "v_add_f32 v11, v11, v8\n" \
  "ds_write_b64 v7, v[10:11]\n" \
  "s_cmp_eq_u32 s78, 1\n" \
  "s_cbranch_scc1 .LCQ_NV_%=\n" \
  "s_waitcnt vmcnt(0)\n" \
  ".LCQ_NV_%=:\n" \
  "v_cmp_eq_u32 vcc, 0, v5\n" \
  "s_cmp_eq_u64 vcc, 0\n" \
  "s_cselect_b32 s80, 1, 0\n" \
  "s_sub_u32 s64, s64, 1\n" \
  "s_cmp_lt_u32 s64, 62\n" \
  "s_cselect_b32 s81, s78, 0\n" \
  "s_min_u32 s81, s81, 1\n" \
  "s_cmp_lg_u32 s81, 0\n" \
  "s_cbranch_scc1 .LCQ_TOP_%=\n" \
  "s_cmp_lg_u32 s78, 0\n" \
  "s_cbranch_scc1 .LCQ_NA_%=\n" \
  "s_cmp_eq_u32 s80, 1\n" \
  "s_cbranch_scc0 .LCQ_X23_%=\n" \
  "s_mov_b32 m0, s73\n" \
  "s_nop 0\n" \
  "global_load_lds_dwordx4 v2, s[62:63] offset:0 sc1\n" \
  "global_load_lds_dwordx4 v2, s[62:63] offset:1024 sc1\n" \
  "s_add_u32 s62, s62, 0x4000\n" \
  "s_addc_u32 s63, s63, 0\n" \
  "s_mov_b32 m0, s68\n" \
  "s_xor_b32 s68, s68, 0x10000\n" \
  "global_load_lds_dwordx4 v2, s[60:61] offset:0 sc1\n" \
  "global_load_lds_dwordx4 v2, s[60:61] offset:1024 sc1\n" \
  "global_load_lds_dwordx4 v2, s[60:61] offset:2048 sc1\n" \
  "global_load_lds_dwordx4 v2, s[60:61] offset:3072 sc1\n" \
  "s_add_u32 s60, s60, 0x8000\n" \
  "s_addc_u32 s61, s61, 0\n" \
  "s_add_u32 s87, s71, 0x1000\n" \
  "v_bfe_u32 v5, v2, 2, 5\n" \
  "buffer_load_dword v5, v5, s[52:55], s87 offen sc1\n" \
  ".LCQ_X23_%=:\n" \
  ".LCQ_NA_%=:\n" \
  "ds_read2_b32 v[6:7], v3 offset0:16 offset1:48\n" \
  "ds_read_b128 v[10:13], v4 offset:256\n" \
  "ds_read_b128 v[14:17], v4 offset:320\n" \
  "ds_read_b32 v8, v3 offset:384\n" \
  "s_waitcnt lgkmcnt(2)\n" \
  "v_fma_f32 v48, -v6, v10, v48\n" \
  "v_fma_f32 v49, -v6, v11, v49\n" \
  "v_fma_f32 v50, -v6, v12, v50\n" \
  "v_fma_f32 v51, -v6, v13, v51\n" \
  "ds_read_b128 v[10:13], v4 offset:64\n" \
  "s_waitcnt lgkmcnt(2)\n" \
  "v_fma_f32 v54, -v6, v14, v54\n" \
  "v_fma_f32 v55, -v6, v15, v55\n" \
  "v_fma_f32 v56, -v6, v16, v56\n" \
  "v_fma_f32 v57, -v6, v17, v57\n" \
  "ds_read_b128 v[14:17], v4 offset:192\n" \
  "v_mul_f32 v48, v48, v7\n" \
  "v_mul_f32 v49, v49, v7\n" \
  "v_mul_f32 v50, v50, v7\n" \
  "v_mul_f32 v51, v51, v7\n" \
  "v_cvt_pk_bf16_f32 v48, v48, v49\n" \
  "v_cvt_pk_bf16_f32 v49, v50, v51\n" \
  "v_mul_f32 v54, v54, v7\n" \
  "v_mul_f32 v55, v55, v7\n" \
  "v_mul_f32 v56, v56, v7\n" \
  "v_mul_f32 v57, v57, v7\n" \
  "v_cvt_pk_bf16_f32 v54, v54, v55\n" \
  "v_cvt_pk_bf16_f32 v55, v56, v57\n" \
  "s_waitcnt lgkmcnt(1)\n" \
  "v_fma_f32 v60, -v10, v8, v60\n" \
  "v_fma_f32 v61, -v11, v8, v61\n" \
  "v_fma_f32 v62, -v12, v8, v62\n" \
  "v_fma_f32 v63, -v13, v8, v63\n" \
  "v_permlane16_swap_b32 v46, v48\n" \
  "v_permlane16_swap_b32 v47, v49\n" \
  "v_permlane16_swap_b32 v52, v54\n" \
  "v_permlane16_swap_b32 v53, v55\n" \
  "s_waitcnt lgkmcnt(0)\n" \
  "v_mul_f32 v60, v60, v14\n" \
  "v_mul_f32 v61, v61, v15\n" \
  "v_mul_f32 v62, v62, v16\n" \
  "v_mul_f32 v63, v63, v17\n" \
  "v_cvt_pk_bf16_f32 v60, v60, v61\n" \
  "v_cvt_pk_bf16_f32 v61, v62, v63\n" \
  "v_mov_b64 v[10:11], 0\n" \
  "v_lshrrev_b32 v12, 1, v2\n" \
  "s_add_u32 s87, s65, 0x2000\n" \
  "buffer_store_dwordx4 v[52:55], v1, s[40:43], s87 offen sc1\n" \
  "buffer_store_dwordx4 v[58:61], v2, s[44:47], s66 offen sc1\n" \
  "buffer_store_dwordx4 v[46:49], v1, s[40:43], s65 offen sc1\n" \
  "buffer_store_dwordx2 v[10:11], v12, s[48:51], s67 offen sc1\n" \
  "s_add_u32 s65, s65, 0x80000\n" \
  "s_add_u32 s66, s66, 0x40000\n" \
  "s_add_u32 s67, s67, 0x10000\n" \
  "s_mov_b32 s84, s72\n" \
  "s_add_u32 s72, s72, 0x1000\n" \
  "s_mov_b32 s85, s70\n" \
  "s_xor_b32 s70, s70, 4\n" \
  "s_mov_b32 s79, 1\n" \
  "s_cmp_lg_u32 s78, 0\n" \
  "s_cbranch_scc1 .LCQ_TOP_%=\n" \
  "s_cmp_eq_u32 s80, 0\n" \
  "s_cbranch_scc1 .LCQ_POLL_%=\n" \
  "s_waitcnt vmcnt(4)\n" \
  "s_branch .LCQ_GOT_%=\n" \
  ".LCQ_POLL_%=:\n" \
  "s_mov_b32 s86, 0x40000\n" \
  "s_branch .LCQ_PW_%=\n" \
  ".LCQ_PV_%=:\n" \
  "s_sleep 12\n" \
  ".LCQ_PW_%=:\n" \
  "v_bfe_u32 v5, v2, 2, 5\n" \
  "buffer_load_dword v5, v5, s[52:55], s71 offen sc1\n" \
  "s_sub_u32 s86, s86, 1\n" \
  "s_cbranch_scc1 .LCQ_END_%=\n" \
  "s_waitcnt vmcnt(0)\n" \
  "s_cmp_eq_u32 s79, 0\n" \
  "s_cbranch_scc1 .LCQ_NPD_%=\n" \
  "v_mov_b32 v9, s85\n" \
  "s_mov_b64 exec, 2\n" \
  "ds_add_rtn_u32 v9, v9, v2\n" \
  "s_mov_b64 exec, -1\n" \
  "s_waitcnt lgkmcnt(0)\n" \
  "v_readlane_b32 s87, v9, 1\n" \
  "s_mov_b32 s79, 0\n" \
  "s_and_b32 s87, s87, 0x70\n" \
  "s_cmp_eq_u32 s87, 0x70\n" \
  "s_cbranch_scc0 .LCQ_F24_%=\n" \
  "s_mov_b64 exec, 2\n" \
  "buffer_store_dword v9, off, s[56:59], s84 sc1\n" \
  "s_mov_b64 exec, -1\n" \
  ".LCQ_F24_%=:\n" \
  "s_cmp_eq_u32 s64, 0\n" \
  "s_cbranch_scc1 .LCQ_END_%=\n" \
  ".LCQ_NPD_%=:\n" \
  "v_cmp_eq_u32 vcc, 0, v5\n" \
  "s_cmp_eq_u64 vcc, 0\n" \
  "s_cbranch_scc0 .LCQ_PV_%=\n" \
  "s_mov_b32 m0, s73\n" \
  "s_nop 0\n" \
  "global_load_lds_dwordx4 v2, s[62:63] offset:0 sc1\n" \
  "global_load_lds_dwordx4 v2, s[62:63] offset:1024 sc1\n" \
  "s_add_u32 s62, s62, 0x4000\n" \
  "s_addc_u32 s63, s63, 0\n" \
  "s_mov_b32 m0, s68\n" \
  "s_xor_b32 s68, s68, 0x10000\n" \
  "global_load_lds_dwordx4 v2, s[60:61] offset:0 sc1\n" \
  "global_load_lds_dwordx4 v2, s[60:61] offset:1024 sc1\n" \
  "global_load_lds_dwordx4 v2, s[60:61] offset:2048 sc1\n" \
  "global_load_lds_dwordx4 v2, s[60:61] offset:3072 sc1\n" \
  "s_add_u32 s60, s60, 0x8000\n" \
  "s_addc_u32 s61, s61, 0\n" \
  "s_add_u32 s87, s71, 0x1000\n" \
  "v_bfe_u32 v5, v2, 2, 5\n" \
  "buffer_load_dword v5, v5, s[52:55], s87 offen sc1\n" \
  "s_waitcnt vmcnt(0)\n" \
  ".LCQ_GOT_%=:\n" \
  "v_bfe_u32 v7, v2, 4, 5\n" \
  "v_and_b32 v8, 0x200, v2\n" \
  "v_lshl_add_u32 v6, v7, 3, s73\n" \
  "v_lshl_add_u32 v6, v8, 1, v6\n" \
  "ds_read2_b64 v[10:13], v6 offset0:0 offset1:32\n" \
  "ds_read2_b64 v[14:17], v6 offset0:64 offset1:96\n" \
  "v_lshl_add_u32 v7, v7, 3, s75\n" \
  "s_waitcnt lgkmcnt(0)\n" \
  "v_add_f32 v6, v10, v12\n" \
  "v_add_f32 v8, v14, v16\n" \
  "v_add_f32 v6, v6, v8\n" \
  "v_mov_b32 v8, v6\n" \
  "v_add_f32 v11, v11, v13\n" \
  "v_add_f32 v15, v15, v17\n" \
  "v_permlane32_swap_b32 v6, v8\n" \
  "v_add_f32 v6, v6, v8\n" \
  "v_fmamk_f32 v10, v6, 0xbe000000, v10\n" \
  "v_fmamk_f32 v12, v6, 0xbe000000, v12\n" \
  "v_fmamk_f32 v14, v6, 0xbe000000, v14\n" \
  "v_fmamk_f32 v16, v6, 0xbe000000, v16\n" \
  "v_add_f32 v11, v11, v15\n" \
  "v_mul_f32 v10, v10, v10\n" \
  "v_fmac_f32 v10, v12, v12\n" \
  "v_fmac_f32 v10, v14, v14\n" \
  "v_fmac_f32 v10, v16, v16\n" \
  "v_fmamk_f32 v11, v10, 0x3e000000, v11\n" \
  "v_mov_b32 v8, v11\n" \
  "v_mov_b32 v10, v6\n" \
  "s_nop 0\n" \
  "v_permlane32_swap_b32 v11, v8\n" \
  "v_add_f32 v11, v11, v8\n" \
  "ds_write_b64 v7, v[10:11]\n" \
  "v_cmp_eq_u32 vcc, 0, v5\n" \
  "s_cmp_eq_u64 vcc, 0\n" \
  "s_cselect_b32 s80, 1, 0\n" \
  "s_branch .LCQ_TOP_%=\n" \
  ".LCQ_END_%=:\n" \
  "s_endpgm\n"
__device__ __forceinline__ void role_qkv(const MegaArgs& A, char* smem, const int L, const int h) {
  const int w = A.wv;
  char* ws = A.ws;
  unsigned* FL = (unsigned*)(ws + OFF_FL);
  const int l = lane_id();
  const int c = l & 15, g = l >> 4;
  const unsigned sb = lds_base(smem);
  v8u wq[8], wk[8], wv[8];
  constexpr int Q_ST = 131072, Q_PR = Q_ST + 8192, Q_WP = Q_PR + 16384, Q_CN = Q_WP + 4096;
  const int rc = blockIdx.x & 3;
  const char* Xb = ws + (L ? OFF_X3 : OFF_XA);
  const char* Sb = ws + OFF_H + L * (SZ_X / 2);
  {
    const int q = (l >> 2) & 3, p = l & 3, f = q | ((g & 1) << 2);
    const unsigned ra = sb + (8 * g + q) * 256 + (w ^ f) * 32 + p * 8;
#define W_READ(W, hf)                                                          \
  {                                                                           \
    _Pragma("unroll") for (int t = 0; t < 4; ++t) {                           \
      const unsigned a_ = ra + (hf) * 8192 + t * 32768;                       \
      const v2u r0 = trd(a_), r1 = trd(a_ + 1024), r2 = trd(a_ + 16384), r3 = trd(a_ + 16384 + 1024); \
      W[4 * (hf) + t] = v8u{r0.x, r0.y, r1.x, r1.y, r2.x, r2.y, r3.x, r3.y};  \
    }                                                                         \
    _Pragma("unroll") for (int t = 0; t < 4; ++t) asm volatile("" : "+v"(W[4 * (hf) + t])); \
  }
#define W_STAGE(W, hf, WAIT, NEXT)                                             \
  {                                                                           \
    WAIT;                                                                     \
    RAW_BAR();                                                                \
    W_READ(W, hf);                                                            \
    RAW_BAR();                                                                \
    NEXT;                                                                     \
  }
    __builtin_amdgcn_s_waitcnt(0x0f70);
    wqkv_issue<false>(A.Wq, 0, h, w, l, sb);
    wqkv_issue<false>(A.Wq, 1, h, w, l, sb);
    const u64 tag = call_tag(A);
    const v4u one = {0x3f803f80u, 0x3f803f80u, 0x3f803f80u, 0x3f803f80u};
    float* cp = reinterpret_cast<float*>(smem + Q_ST + w * 1024 + 256);
    f32x4 cs = {};
#define HALF(x, i) BF8(((i) ? __builtin_shufflevector(x, x, 4, 5, 6, 7) : __builtin_shufflevector(x, x, 0, 1, 2, 3)))
    if (w == 0 && l < 8) reinterpret_cast<unsigned*>(smem + Q_CN)[l] = 0u;
    W_STAGE(wq, 0, WAIT_VM(8), wqkv_issue<false>(A.Wk, 0, h, w, l, sb));
    SST(4);
    W_STAGE(wq, 1, WAIT_VM(8), wqkv_issue<false>(A.Wk, 1, h, w, l, sb));
    SST(5);
    asm volatile("" : "+v"(cs));
#pragma unroll
    for (int ks = 0; ks < 16; ++ks) cs = MFMA16(HALF(wq[ks >> 1], ks & 1), BF8(one), cs);
    *reinterpret_cast<f32x4*>(cp + 4 * g) = cs;
    W_STAGE(wk, 0, WAIT_VM(8), wqkv_issue<false>(A.Wv, 0, h, w, l, sb));
    W_STAGE(wk, 1, WAIT_VM(8), if (w < 4) {
      wqkv_issue<false>(A.Wv, 1, h, w, l, sb);
      wqkv_issue<false>(A.Wv, 1, h, w + 4, l, sb);
    });
    SST(6);
    cs = f32x4{};
#pragma unroll
    for (int ks = 0; ks < 16; ++ks) cs = MFMA16(HALF(wk[ks >> 1], ks & 1), BF8(one), cs);
    *reinterpret_cast<f32x4*>(cp + 16 + 4 * g) = cs;
    W_STAGE(wv, 0, if (w < 4) WAIT_VM(16); else { ready_wait(A, tag, ready_look(A)); __builtin_amdgcn_s_waitcnt(0x0f70); }, (void)0);
    if (w >= 4 && __all(__hip_atomic_load((gu32*)FL + FL_FC + ((L * 64 * 4 + rc) * 8 + 2 * w - 8 + ((l >> 3) & 1)) * 32 + (l & 7), RLX_AGENT) != 0u)) {
      reinterpret_cast<unsigned*>(smem + Q_CN)[w] = 1u;
#pragma unroll
      for (int i = 0; i < 4; ++i) glds16c(Sb + (4 * w - 16 + i) * 1024, l * 16, sb + Q_PR + (4 * w - 16 + i) * 1024);
#pragma unroll
      for (int i = 0; i < 8; ++i) glds16c(Xb + (8 * w - 32 + i) * 1024, l * 16, sb + (w - 4) * 16384 + i * 1024);
    }
    cs = f32x4{};
#pragma unroll
    for (int ks = 0; ks < 8; ++ks) cs = MFMA16(BF8(one), HALF(wv[ks >> 1], ks & 1), cs);
    asm volatile("" : "+v"(cs));
    SST(7);
    WAIT_VM(0);
    RAW_BAR();
    SST(8);
    W_READ(wv, 1);
#pragma unroll
    for (int ks = 8; ks < 16; ++ks) cs = MFMA16(BF8(one), HALF(wv[ks >> 1], ks & 1), cs);
    cp[32 + c] = cs[0];
#undef HALF
#undef W_STAGE
#undef W_READ
  }
  SST(9);
  const int got = __builtin_amdgcn_readfirstlane(reinterpret_cast<unsigned*>(smem + Q_CN)[4 + (w >> 1)]);
  const v4u rsQ = mk_srd(ws + OFF_QK + L * SZ_QK, SZ_QK);
  const v4u rsV = mk_srd(ws + OFF_VF + L * SZ_O, SZ_O);
  const v4u rsM = mk_srd(ws + (w >= 4 ? OFF_MEMV : OFF_MEMK) + L * SZ_MEM, SZ_MEM);
  const v4u rsF = mk_srd(FL + FL_FC + (L * 64 * 4 + rc) * 8 * 32 + w * 32, (64 * 32 - rc * 8 - w) * 128);
  const v4u rsFQ = mk_srd(FL + FL_FQ + (L * 64 * 32 + h) * 32, 64 * 4096 - h * 128);
  v8u pv;
  pv[0] = sb + 65536 + g * 512 + c * 16;
  pv[1] = (c + 16 * (2 * (w & 1) + (g >> 1))) * 16 + 4096 * (g & 1);
  pv[2] = l * 16;
  pv[3] = sb + Q_ST + w * 1024 + c * 4;
  pv[4] = sb + Q_ST + w * 1024 + g * 16;
  pv[5] = pv[7] = 0u;
  pv[6] = got;
  const char* dbg = nullptr;
  asm volatile("s_mov_b64 exec, -1\ns_waitcnt vmcnt(0) lgkmcnt(0)\ns_nop 4\n" Q_ASM
               ::"{v[64:71]}"(wq[0]), "{v[72:79]}"(wq[1]), "{v[80:87]}"(wq[2]), "{v[88:95]}"(wq[3]), "{v[96:103]}"(wq[4]),
               "{v[104:111]}"(wq[5]), "{v[112:119]}"(wq[6]), "{v[120:127]}"(wq[7]), "{v[128:135]}"(wk[0]), "{v[136:143]}"(wk[1]),
               "{v[144:151]}"(wk[2]), "{v[152:159]}"(wk[3]), "{v[160:167]}"(wk[4]), "{v[168:175]}"(wk[5]), "{v[176:183]}"(wk[6]),
               "{v[184:191]}"(wk[7]), "{v[192:199]}"(wv[0]), "{v[200:207]}"(wv[1]), "{v[208:215]}"(wv[2]), "{v[216:223]}"(wv[3]),
               "{v[224:231]}"(wv[4]), "{v[232:239]}"(wv[5]), "{v[240:247]}"(wv[6]), "{v[248:255]}"(wv[7]), "{v[0:7]}"(pv),
               "{s[40:43]}"(rsQ), "{s[44:47]}"(rsV), "{s[48:51]}"(rsM), "{s[52:55]}"(rsF), "{s[56:59]}"(rsFQ), "{s[60:61]}"(Xb + got * 32768 + w * 4096),
               "{s[62:63]}"(Sb + got * 16384 + w * 2048), "{s64}"(NSEG), "{s65}"((h * 16 + (w >> 1)) * 1024), "{s66}"((h * 512 + w * 64) * 16),
               "{s67}"(h * 2048 + (w & 3) * 512), "{s68}"(sb + got * 65536 + (w >> 1) * 16384 + (w & 1) * 4096), "{s69}"(sb + Q_ST + w * 1024), "{s70}"(sb + Q_CN),
               "{s71}"(0), "{s72}"(0), "{s73}"(sb + Q_PR + w * 2048), "{s74}"(sb + Q_WP + 2048), "{s75}"(sb + Q_WP + w * 256), "{s[36:37]}"(dbg)
               : "memory", "vcc", "scc", "s88", "v63");
}

constexpr int MEGA_WGS = 256;
constexpr size_t MEGA_LDS = (size_t)(32 * 4 * 64 * 8 + 8 * SMS + 15 * QS + 128 + 16 * 64 * 8 + 8 * SOS + 512) * 2;
static_assert(MEGA_LDS <= 163840, "lds");
static_assert(MEGA_LDS >= (size_t)AW_CNT + 128, "lds");
static_assert(MEGA_LDS >= (size_t)CV_LDS && 8 * 1568 >= 768 * 16, "lds");
static_assert(MEGA_LDS >= (size_t)2 * 32 * SMS * 2 && MEGA_LDS >= 131072, "lds");
static_assert(MEGA_LDS >= (size_t)8 * 16640, "lds");
__device__ __forceinline__ MegaArgs role_args(const int b, const int wv) {
  auto p = (const __attribute__((address_space(4))) u64*)__builtin_amdgcn_kernarg_segment_ptr();
  asm volatile("" : "+s"(p));
#define KA(i) ((u16*)(__attribute__((address_space(1))) u16*)p[i])
  return MegaArgs{KA(0), KA(1), (char*)KA(2), KA(3), KA(4), KA(5), KA(6), KA(7), KA(8), KA(9), KA(10), KA(11), KA(12), KA(13), b, wv};
#undef KA
}
__global__ __launch_bounds__(512, 2) void mega_kernel(const u16* mem0, const u16* Dskip, char* ws, u16* out, const u16* x0,
                                                      const u16* logAre, const u16* Aim, const u16* Cre, const u16* Cim,
                                                      const u16* logdt, const u16* Wq, const u16* Wk, const u16* Wv,
                                                      const u16* Wo) {
  extern __shared__ __attribute__((aligned(16))) char smem[];
  const int b = vbid(), wv = __builtin_amdgcn_readfirstlane(threadIdx.x >> 6);
#define A role_args(b, wv)
  u64 tag = 0;
  {
    const unsigned t0 = now10();
    if ((b >= 32 && b < 64) || (b >= 200 && b < 232)) start_kgen(A, smem, b < 64 ? b - 32 : b - 168);
    else if ((b >= 64 && b < 104) || b >= 232) tag = start_stats(A, b >= 232 ? b - 192 : b - 64);
    const unsigned dl = (b >= 32 && b < 64) ? DL_C : (b >= 64 && b < 96) ? DL_A : ((b >= 96 && b < 104) || b >= 232) ? DL_R : (b >= 200 && b < 232) ? DL_Q : 0u;
    while (now10() - t0 < dl) __builtin_amdgcn_s_sleep(16);
  }
  if ((EXP_OFF & 1) && b >= 64 && b < 80) return;
  if ((EXP_OFF & 8) && b >= 200 && b < 232) return;
  if ((EXP_OFF & 16) && b >= 32 && b < 64) return;
  if ((EXP_OFF & 32) && b >= 80 && b < 96) return;
  if (b < 64) role_chain(A, smem, b >> 5, b & 31, 0);
  else if (b < 96) role_aw(A, smem, (b >> 4) & 1, b & 15, tag);
  else if (b >= 104 && b < 168) role_conv(A, smem, b - 104);
  else if (b >= 168 && b < 232) role_qkv(A, smem, (b - 168) >> 5, (b - 168) & 31);
  else if (b < 240) role_red(A, smem, 0, b & 15, tag);
  else role_red(A, smem, 1, b - 240, tag);
#undef A
}

extern "C" void kernel_launch(void* const* d_in, const int* in_sizes, int n_in, void* d_out, int out_size, void* d_ws,
                              size_t ws_size, hipStream_t stream) {
  MegaArgs A;
  A.x0 = (const u16*)d_in[0];
  A.logAre = (const u16*)d_in[1];
  A.Aim = (const u16*)d_in[2];
  A.Cre = (const u16*)d_in[3];
  A.Cim = (const u16*)d_in[4];
  A.logdt = (const u16*)d_in[5];
  A.Dskip = (const u16*)d_in[6];
  A.mem0 = (const u16*)d_in[8];
  A.Wq = (const u16*)d_in[9];
  A.Wk = (const u16*)d_in[10];
  A.Wv = (const u16*)d_in[11];
  A.Wo = (const u16*)d_in[12];
  A.ws = (char*)d_ws;
  A.out = (u16*)d_out;

  (void)hipFuncSetAttribute((const void*)mega_kernel, hipFuncAttributeMaxDynamicSharedMemorySize, (int)MEGA_LDS);
  mega_kernel<<<MEGA_WGS, 512, MEGA_LDS, stream>>>(A.mem0, A.Dskip, A.ws, A.out, A.x0, A.logAre, A.Aim, A.Cre, A.Cim, A.logdt,
                                                   A.Wq, A.Wk, A.Wv, A.Wo);
}
```

```cpp
#include <hip/hip_runtime.h>
#include <hip/hip_bf16.h>
#include <stdint.h>

constexpr int L_SEQ = 2048;
constexpr int DM    = 512;
constexpr int NST   = 128;
constexpr int NH    = 32;
constexpr int DH    = 128;
constexpr int HD    = NH * DH;
constexpr int MM    = 8;
constexpr int SEG   = 32;
constexpr int NSEG  = L_SEQ / SEG;
constexpr int LK    = MM + SEG;

typedef unsigned short u16;
using bf16x8 = __attribute__((ext_vector_type(8))) short;
using f32x4  = __attribute__((ext_vector_type(4))) float;
using f32x16 = __attribute__((ext_vector_type(16))) float;

static __device__ __forceinline__ u16 f2bf(float f) {
  unsigned u = __float_as_uint(f);
  u += 0x7fffu + ((u >> 16) & 1u);
  return (u16)(u >> 16);
}
static __device__ __forceinline__ float bf2f(u16 h) { return __uint_as_float(((unsigned)h) << 16); }
static __device__ __forceinline__ float bfr(float f) { return bf2f(f2bf(f)); }

constexpr size_t SZ_X      = (size_t)L_SEQ * DM * 2;
constexpr size_t OFF_KF    = 0;
constexpr size_t OFF_UT    = OFF_KF + (size_t)DM * L_SEQ * 4;
constexpr size_t OFF_YT    = OFF_UT + SZ_X;
constexpr size_t OFF_XA    = OFF_YT + SZ_X;
constexpr size_t OFF_H     = OFF_XA + SZ_X;
constexpr size_t OFF_X2    = OFF_H + SZ_X;
constexpr size_t OFF_U2    = OFF_X2 + SZ_X;
constexpr size_t OFF_X3    = OFF_U2 + SZ_X;
constexpr size_t SZ_QK     = (size_t)L_SEQ * 2 * HD * 2;
constexpr size_t OFF_QK    = OFF_X3 + SZ_X;
constexpr size_t SZ_O      = (size_t)L_SEQ * HD * 2;
constexpr size_t OFF_VF    = OFF_QK + 2 * SZ_QK;
constexpr size_t SZ_PW     = (size_t)2 * NSEG * 16 * SEG * DM * 2;
constexpr size_t OFF_PW    = OFF_VF + 2 * SZ_O;
constexpr size_t SZ_MEM    = (size_t)NSEG * MM * HD * 2;
constexpr size_t OFF_MEMK  = OFF_PW + SZ_PW;
constexpr size_t OFF_MEMV  = OFF_MEMK + 2 * SZ_MEM;
constexpr size_t OFF_GT    = OFF_MEMV + 2 * SZ_MEM;
constexpr size_t OFF_EP    = OFF_GT + 65536;
constexpr size_t OFF_RD    = OFF_EP + 4096;
constexpr size_t OFF_KG    = OFF_RD + 4096;
constexpr size_t OFF_G     = OFF_KG + 8192;
constexpr size_t SZ_ACC    = (size_t)2 * 32768;
constexpr size_t OFF_Z     = OFF_GT + (size_t)64 * DM * 4;
constexpr size_t OFF_ACC   = OFF_Z;
constexpr size_t OFF_FL    = OFF_ACC + 2 * SZ_ACC;
constexpr int FL_FK  = 0;
constexpr int FL_FW  = FL_FK + 64 * 8 * 32;
constexpr int FL_FR  = FL_FW + 2 * 64 * 16 * 32;
constexpr int FL_FC  = FL_FR + 64 * 16 * 32;
constexpr int FL_FQ  = FL_FC + 2 * 64 * 32 * 32;
constexpr int FL_FZ  = FL_FQ + 2 * 64 * 32 * 32;
constexpr int FL_TMO = FL_FZ + 16 * 32;
constexpr int FL_END = FL_TMO + 64;
constexpr size_t Z_BYTES   = OFF_FL + (size_t)FL_END * 4 - OFF_Z;
constexpr size_t WS_TOTAL  = OFF_Z + Z_BYTES;
static_assert(Z_BYTES % 16 == 0 && OFF_Z % 256 == 0 && Z_BYTES <= 4 * 64 * 8192 && OFF_G + 2048 <= OFF_Z, "zero region");
static_assert(WS_TOTAL + (4 << 20) <= (size_t)256 * 1024 * 1024, "workspace too large");

static_assert(WS_TOTAL - OFF_XA <= (size_t)384 * 8 * 65536, "translation warm-up");
constexpr int CKN = 2208;
static_assert((size_t)DM * 2 * CKN * 2 <= (size_t)DM * L_SEQ * 4 + SZ_X, "K buffer");
__device__ __forceinline__ float pos_of_index(int p) {
  if (p < 256) return (float)p;
  if (p < 384) return (float)(256 + 2 * (p - 256));
  if (p < 512) return (float)(512 + 4 * (p - 384));
  if (p < 640) return (float)(1024 + 8 * (p - 512));
  return 2048.0f;
}
__device__ __forceinline__ int index_of_l(int l) {
  float pf = bfr((float)l);
  int p = (int)pf;
  if (p < 256) return p;
  if (p < 512) return 256 + ((p - 256) >> 1);
  if (p < 1024) return 384 + ((p - 512) >> 2);
  if (p < 2048) return 512 + ((p - 1024) >> 3);
  return 640;
}

template <int CTRL> __device__ __forceinline__ float dppr(float x) {
  return __builtin_bit_cast(float, __builtin_amdgcn_update_dpp(0, __builtin_bit_cast(int, x), CTRL, 0xf, 0xf, false));
}
__device__ __forceinline__ float wave_sum(float v) {
  v += dppr<0x121>(v);
  v += dppr<0x122>(v);
  v += dppr<0x124>(v);
  v += dppr<0x128>(v);
  {
    auto r = __builtin_amdgcn_permlane16_swap(__float_as_uint(v), __float_as_uint(v), false, false);
    v = __uint_as_float(r[0]) + __uint_as_float(r[1]);
  }
  {
    auto r = __builtin_amdgcn_permlane32_swap(__float_as_uint(v), __float_as_uint(v), false, false);
    v = __uint_as_float(r[0]) + __uint_as_float(r[1]);
  }
  return v;
}
__device__ __forceinline__ void ln_row8(float (&v)[8]) {
  float s = 0.f;
#pragma unroll
  for (int j = 0; j < 8; ++j) s += v[j];
  s = wave_sum(s);
  const float mean = s * (1.0f / DM);
  float q = 0.f;
#pragma unroll
  for (int j = 0; j < 8; ++j) { float t = v[j] - mean; q += t * t; }
  q = wave_sum(q);
  const float var = q * (1.0f / DM);
  const float mb = bfr(mean);
  const float sd = bfr(sqrtf(bfr(bfr(var) + 1e-5f)));
#pragma unroll
  for (int j = 0; j < 8; ++j) v[j] = bfr(bfr(v[j] - mb) / sd);
}

#define RLX_AGENT __ATOMIC_RELAXED, __HIP_MEMORY_SCOPE_AGENT
typedef __attribute__((address_space(1))) unsigned gu32;
typedef unsigned v4u __attribute__((ext_vector_type(4)));
constexpr int AUX_SC1 = 16;
constexpr unsigned SPIN_CAP = 1u << 18;

__device__ __forceinline__ bool wait_flags(const unsigned* flags, int lane, int nfl, unsigned want, unsigned* tmo) {
  for (unsigned spins = 0;; ++spins) {
    unsigned v = want;
    if (lane < nfl) v = __hip_atomic_load((gu32*)flags + lane * 32, RLX_AGENT);
    if (__all(v >= want)) break;
    if (spins > SPIN_CAP) {
      if (lane == 0) __hip_atomic_store((gu32*)tmo, 1u, RLX_AGENT);
      return false;
    }
  }
  __builtin_amdgcn_fence(__ATOMIC_ACQUIRE, "wavefront");
  return true;
}

#define STAMP(i)
#define STAMP_DUMP()
#define WSTAMP(i)
template <int CTRL> __device__ __forceinline__ float dppf(float old, float x) {
  return __builtin_bit_cast(float, __builtin_amdgcn_update_dpp(__builtin_bit_cast(int, old), __builtin_bit_cast(int, x), CTRL, 0xf, 0xf, false));
}
__device__ __forceinline__ float wave_max_dpp(float x) {
  const float ninf = -3e38f;
  x = fmaxf(x, dppf<0x111>(ninf, x));
  x = fmaxf(x, dppf<0x112>(ninf, x));
  x = fmaxf(x, dppf<0x114>(ninf, x));
  x = fmaxf(x, dppf<0x118>(ninf, x));
  x = fmaxf(x, dppf<0x142>(ninf, x));
  x = fmaxf(x, dppf<0x143>(ninf, x));
  return __builtin_bit_cast(float, __builtin_amdgcn_readlane(__builtin_bit_cast(int, x), 63));
}
__device__ __forceinline__ float wave_sum_dpp(float x) {
  x += dppf<0x111>(0.f, x);
  x += dppf<0x112>(0.f, x);
  x += dppf<0x114>(0.f, x);
  x += dppf<0x118>(0.f, x);
  x += dppf<0x142>(0.f, x);
  x += dppf<0x143>(0.f, x);
  return __builtin_bit_cast(float, __builtin_amdgcn_readlane(__builtin_bit_cast(int, x), 63));
}
constexpr int SMS = 528;
constexpr int QS = 144;
constexpr int VS = 80;
constexpr int SOS = 144;
#define VAR_NREP 4
constexpr int NREP = VAR_NREP;
__device__ __forceinline__ float xsum16(float x) {
  auto r = __builtin_amdgcn_permlane16_swap(__float_as_uint(x), __float_as_uint(x), false, false);
  return __uint_as_float(r[0]) + __uint_as_float(r[1]);
}
__device__ __forceinline__ float xsum32(float x) {
  auto r = __builtin_amdgcn_permlane32_swap(__float_as_uint(x), __float_as_uint(x), false, false);
  return __uint_as_float(r[0]) + __uint_as_float(r[1]);
}
__device__ __forceinline__ float xmax16(float x) {
  auto r = __builtin_amdgcn_permlane16_swap(__float_as_uint(x), __float_as_uint(x), false, false);
  return fmaxf(__uint_as_float(r[0]), __uint_as_float(r[1]));
}
__device__ __forceinline__ float xmax32(float x) {
  auto r = __builtin_amdgcn_permlane32_swap(__float_as_uint(x), __float_as_uint(x), false, false);
  return fmaxf(__uint_as_float(r[0]), __uint_as_float(r[1]));
}
__device__ __forceinline__ float red_mod4(float x) {
  x += dppf<0x124>(0.f, x);
  x += dppf<0x128>(0.f, x);
  return xsum32(xsum16(x));
}
typedef __bf16 bf16x2_t __attribute__((ext_vector_type(2)));
typedef float f32x2_t __attribute__((ext_vector_type(2)));
__device__ __forceinline__ unsigned pk2(float a, float b) {
  f32x2_t v = {a, b};
  bf16x2_t r = __builtin_convertvector(v, bf16x2_t);
  return __builtin_bit_cast(unsigned, r);
}
__device__ __forceinline__ float one(float x) {
  asm("" : "+v"(x));
  return x;
}
__device__ __forceinline__ unsigned pk2s(float a, float b) { return pk2(one(a), one(b)); }
__device__ __forceinline__ float dot2sq(const unsigned x, const float acc) {
  return __builtin_amdgcn_fdot2_f32_bf16(__builtin_bit_cast(bf16x2_t, x), __builtin_bit_cast(bf16x2_t, x), acc, false);
}
__device__ __forceinline__ float lo16(unsigned u) { return __uint_as_float(u << 16); }
__device__ __forceinline__ float hi16(unsigned u) { return __uint_as_float(u & 0xffff0000u); }
__device__ __forceinline__ void scale_pair(float a, float b, float scale, float& oa, float& ob) {
  unsigned u = pk2(a, b);
  unsigned v = pk2s(lo16(u) * scale, hi16(u) * scale);
  oa = lo16(v);
  ob = hi16(v);
}

#define XMAP 2
__device__ __forceinline__ int vbid() {
  const int b = blockIdx.x, x = b & 7, t = b >> 3;
  if (XMAP == 0) {
    if (b < 64) return (b & 32) + 4 * x + (t & 3);
    if (b < 96) return (b & ~15) + 2 * x + (t & 1);
    if (b >= 104 && b < 168) return 104 + 8 * x + t - 13;
    if (b >= 168 && b < 232) return 168 + ((b - 168) & 32) + 4 * x + ((t - 21) & 3);
    return b;
  }
  if (XMAP == 2) {
    if (x < 4) return t < 16 ? 4 * (t & 8) + 8 * x + (t & 7) : 104 + 16 * x + t - 16;
    const int y = x - 4;
    if (t >= 24) return (t >= 28 ? 240 : y < 2 ? 96 : 224) + 4 * y + (t & 3);
    if (t < 16) return 168 + 4 * (t & 8) + 8 * y + (t & 7);
    return 64 + 4 * (t & 4) + 4 * y + (t & 3);
  }
  if (x < 2) return 2 * (t & 16) + 16 * x + (t & 15);
  const int i = 16 * (x - 2) + (t & 15);
  if (t < 16) {
    const int k = (i * 171) >> 9, r = i - 3 * k;
    return r == 0 ? 168 + k : r == 1 ? 200 + k : 64 + 16 * (k & 1) + (k >> 1);
  }
  const int g = i >> 3, e = i & 7;
  if (g == 3) return 96 + e;
  if (g == 7) return 232 + e;
  if (g == 9) return 240 + e;
  if (g == 11) return 248 + e;
  return 104 + 8 * (g - (g > 3) - (g > 7) - (g > 9)) + e;
}
#define MSTAMP()
#define WSTAMP2()
#define SST(k)
#define TS(k, ss, t)
#define LS(k, ss)
#define LS_DUMP()
struct MegaArgs {
  const u16* mem0;
  const u16* Dskip;
  char* ws;
  u16* out;
  const u16* x0;
  const u16 *logAre, *Aim, *Cre, *Cim, *logdt;
  const u16 *Wq, *Wk, *Wv, *Wo;
  int vb, wv;
};

__device__ __forceinline__ int lane_id() {
  int l;
  asm volatile("v_mbcnt_lo_u32_b32 %0, -1, 0\n\tv_mbcnt_hi_u32_b32 %0, -1, %0" : "=v"(l));
  __builtin_assume(l >= 0 && l < 64);
  return l;
}
__device__ __forceinline__ bool spin_timeout(unsigned spins, unsigned* tmo) {
  if (spins > SPIN_CAP) {
    if (lane_id() == 0) __hip_atomic_store((gu32*)tmo, 1u, RLX_AGENT);
    return true;
  }
  return false;
}
#define ROT_H (4 * (h & 1))
#define PNL 8
#define GSL 12
#define AWDIR 1
#define AWAT 0
#define AWRP 0
#define PEPS 4
#define FGATE 0
#define DL_C 0
#define DL_A 800
#define DL_R 900
#define DL_Q 0
#define KC0 0
#define CPRIO 1
#define P1PRIO 3
#define QLEAD 2
#define POLL_SLEEP 12
#define SLP_C POLL_SLEEP
#define SLP_A POLL_SLEEP
#define SLP_W POLL_SLEEP
#define SLP_R POLL_SLEEP
#define SLP_V POLL_SLEEP
#define SLP_Q POLL_SLEEP
#define SLP_M 4
#define AWPRIO 2
#define SLP_S 8
#define SPOS 4
#define CVPRE 4
#define QPD 3
#define QPRI 2
#define TLBW 1
#define QHV 0
#define QCHK 0
#define LEADN 99
#define GATE_V 170
#define GATE_R 380
#define GATE_N 380
#define AWAHEAD 1
#define GATE_M 150
#define PRI_A 3
#define PRI_WA 2
#define PRI_WB 1
#define AWSKIP 3
#define PRI_0 3
#define AWDEF 0
#define SLP_F 2
#define EXP_OFF 0
#define AWSPEC 1
#define CVTHR 48
#define NR_C 8
#define NR_R 8
#define NR_W 8
#define NR_N 2
#define QAHEAD 0
#define NS0 4
#define SLP_N POLL_SLEEP
template <int SLP, int WPL>
__device__ __forceinline__ bool wait_n(const unsigned* f, int n, unsigned* tmo) {
  const int l = lane_id();
  for (unsigned spins = 0;; ++spins) {
    unsigned v = 1u;
    if (l < n) v = __hip_atomic_load((gu32*)f + (l / WPL) * 32 + l % WPL, RLX_AGENT);
    if (__all(v != 0u)) break;
    if (spin_timeout(spins, tmo)) return false;
    __builtin_amdgcn_s_sleep(SLP);
  }
  asm volatile("" ::: "memory");
  return true;
}
__device__ __forceinline__ unsigned now10() { return (unsigned)__builtin_amdgcn_s_memrealtime(); }
__device__ __forceinline__ void gate(const unsigned t) {
  for (int i = 0; i < 64; ++i) {
    const int d = (int)(t - now10());
    if (d <= 0) break;
    if (d > 100) __builtin_amdgcn_s_sleep(32);
    else if (d > 25) __builtin_amdgcn_s_sleep(8);
    else __builtin_amdgcn_s_sleep(2);
  }
}
template <int SLP, int WPL, int GT>
__device__ __forceinline__ void grp_wait(const bool leader, volatile unsigned* sw, const unsigned val, const unsigned* f, int n,
                                         unsigned* tmo, unsigned& tl) {
  if (leader) {
    const int l = lane_id();
    for (unsigned spins = 0;; ++spins) {
      unsigned v = 1u;
      if (l < n) v = __hip_atomic_load((gu32*)f + (l / WPL) * 32 + l % WPL, RLX_AGENT);
      if (__all(v != 0u) || spin_timeout(spins, tmo)) break;
      if (GT && spins == 0) gate(tl);
      else __builtin_amdgcn_s_sleep(SLP);
    }
    if (GT) tl = now10() + GT;
    if (l == 0) *sw = val;
  } else {
    for (unsigned spins = 0; *sw < val; ++spins) {
      if (spins > 64u * SPIN_CAP) break;
      if (FGATE && GT && spins == 0) gate(tl);
      __builtin_amdgcn_s_sleep(SLP_F);
    }
    if (FGATE && GT) tl = now10() + GT;
  }
  asm volatile("" ::: "memory");
}
template <int SLP, int WPL>
__device__ __forceinline__ bool wg_wait(const unsigned* f, int n, unsigned* tmo) {
  if (threadIdx.x < 64) (void)wait_n<SLP, WPL>(f, n, tmo);
  __syncthreads();
  asm volatile("" ::: "memory");
  return true;
}
__device__ __forceinline__ void set_flag(unsigned* f) { __hip_atomic_store((gu32*)f, 1u, RLX_AGENT); }
#define DRAIN() asm volatile("s_waitcnt vmcnt(0)" ::: "memory")
#define MKRS(ptr, bytes) __builtin_amdgcn_make_buffer_rsrc((void*)(ptr), 0, (int)(bytes), 0x00020000)
typedef unsigned v2u __attribute__((ext_vector_type(2)));
#define LD128(rs, voff, soff) __builtin_amdgcn_raw_buffer_load_b128(rs, voff, soff, AUX_SC1)
#define LD64(rs, voff, soff) __builtin_amdgcn_raw_buffer_load_b64(rs, voff, soff, AUX_SC1)
#define ST128(v, rs, voff, soff)                                              \
  do {                                                                        \
    const v4u st_d_ = (v);                                                    \
    __builtin_amdgcn_raw_buffer_store_b128(st_d_, rs, voff, soff, AUX_SC1);   \
    asm volatile("s_nop 1" ::"v"(st_d_) : "memory");                          \
  } while (0)
#define ST64(v, rs, voff, soff) __builtin_amdgcn_raw_buffer_store_b64(v, rs, voff, soff, AUX_SC1)
#define BF8(x) __builtin_bit_cast(bf16x8, x)
#define MFMA16(a, b, c) __builtin_amdgcn_mfma_f32_16x16x32_bf16(a, b, c, 0, 0, 0)
typedef short bf16x4 __attribute__((ext_vector_type(4)));
#define MFMA16K(a, b, c) \
  __builtin_amdgcn_mfma_f32_16x16x16bf16_1k(__builtin_bit_cast(bf16x4, a), __builtin_bit_cast(bf16x4, b), c, 0, 0, 0)
__device__ __forceinline__ float fmax_nn(float a, float b) { return __builtin_elementwise_maximum(a, b); }
typedef __bf16 bfv8 __attribute__((ext_vector_type(8)));
typedef __bf16 bfv16 __attribute__((ext_vector_type(16)));
typedef unsigned v8u __attribute__((ext_vector_type(8)));
#define SMF(a, b, c, idx) \
  __builtin_amdgcn_smfmac_f32_16x16x64_bf16(__builtin_bit_cast(bfv8, a), __builtin_bit_cast(bfv16, b), c, idx, 0, 0)
__device__ __forceinline__ float swap_add32(float a, float b) {
  auto r = __builtin_amdgcn_permlane32_swap(__float_as_uint(a), __float_as_uint(b), false, false);
  return __uint_as_float(r[0]) + __uint_as_float(r[1]);
}
__device__ __forceinline__ float swap_add16(float a, float b) {
  auto r = __builtin_amdgcn_permlane16_swap(__float_as_uint(a), __float_as_uint(b), false, false);
  return __uint_as_float(r[0]) + __uint_as_float(r[1]);
}
__device__ __forceinline__ v4u smf_fix(const v4u d) {
  auto x = __builtin_amdgcn_permlane16_swap(d.x, d.z, false, false);
  auto y = __builtin_amdgcn_permlane16_swap(d.y, d.w, false, false);
  return v4u{x[0], x[1], y[0], y[1]};
}
__device__ __forceinline__ v8u smf_bfrag(const u16* p) {
  const v2u a = *reinterpret_cast<const v2u*>(p), b = *reinterpret_cast<const v2u*>(p + 8);
  const v2u c = *reinterpret_cast<const v2u*>(p + 32), d = *reinterpret_cast<const v2u*>(p + 40);
  return v8u{a.x, b.x, a.y, b.y, c.x, d.x, c.y, d.y};
}

#define VAR_PPA 0
#define VAR_PPB 0
#define VAR_THR 0
#define VAR_THR2 0
#define CV_LEAD 100
#define VAR_EARLY 1
#define VAR_DRAIN 1
#define VAR_D1 0
#define VAR_D2 0
#define VAR_DR2 1
#define VAR_S1 0
#define VAR_S2 0
typedef __attribute__((address_space(1))) unsigned long long gu64;
typedef __attribute__((address_space(1))) char gch;
__device__ __forceinline__ int cvt_rpi(float x) {
  int r;
  asm("v_cvt_rpi_i32_f32 %0, %1" : "=v"(r) : "v"(x));
  return r;
}
typedef unsigned long long u64;
__device__ __forceinline__ u64 call_tag(const MegaArgs& A) {
  u64 t = *(const __attribute__((address_space(4))) u64*)(A.ws + OFF_EP) * 6364136223846793005ull + 1442695040888963407ull;
  asm volatile("" : "+s"(t)::"memory");
  return t;
}
__device__ __forceinline__ u64 tagged(const u64 tag, const unsigned v) { return (tag & 0xffffffffffffull) | (u64)(v >> 16) << 48; }
__device__ __forceinline__ bool has_tag(const u64 tag, const unsigned lo, const unsigned hi) {
  return lo == (unsigned)tag && (hi & 0xffffu) == ((unsigned)(tag >> 32) & 0xffffu);
}
__device__ __forceinline__ u64 ready_look(const MegaArgs& A) {
  return __hip_atomic_load((gu64*)(A.ws + OFF_RD) + (blockIdx.x & 7) * 64 + lane_id(), RLX_AGENT);
}
__device__ __forceinline__ void ready_wait(const MegaArgs& A, const u64 tag, u64 v) {
  for (unsigned spins = 0; !__all(v == tag) && spins < SPIN_CAP; ++spins) {
    __builtin_amdgcn_s_sleep(8);
    v = ready_look(A);
  }
  asm volatile("" ::: "memory");
}
typedef short v4s __attribute__((ext_vector_type(4)));
typedef __attribute__((address_space(3))) v4s* lp4s;
__device__ __forceinline__ void glds16(const void* sbase, const unsigned voff, const unsigned lds) {
  unsigned keep;
  asm volatile("s_nop 4\n\ts_mov_b32 %0, m0\n\ts_mov_b32 m0, %3\n\ts_nop 0\n\tglobal_load_lds_dwordx4 %1, %2\n\ts_mov_b32 m0, %0"
               : "=&s"(keep)
               : "v"(voff), "s"(sbase), "s"(lds)
               : "memory");
}
__device__ __forceinline__ void glds16c(const void* sbase, const unsigned voff, const unsigned lds) {
  unsigned keep;
  asm volatile("s_nop 4\n\ts_mov_b32 %0, m0\n\ts_mov_b32 m0, %3\n\ts_nop 0\n\tglobal_load_lds_dwordx4 %1, %2 sc1\n\ts_mov_b32 m0, %0"
               : "=&s"(keep)
               : "v"(voff), "s"(sbase), "s"(lds)
               : "memory");
}
__device__ __forceinline__ v2u trd(const unsigned a) {
  return __builtin_bit_cast(v2u, __builtin_amdgcn_ds_read_tr16_b64_v4i16((lp4s)(unsigned long)a));
}
__device__ __forceinline__ unsigned lds_base(char* smem) { return (unsigned)(unsigned long)(__attribute__((address_space(3))) char*)smem; }
#define RAW_BAR()                                           \
  do {                                                      \
    asm volatile("s_waitcnt lgkmcnt(0)" ::: "memory");      \
    __builtin_amdgcn_s_barrier();                           \
    asm volatile("" ::: "memory");                          \
  } while (0)
#define WAIT_VM(n) asm volatile("s_waitcnt vmcnt(" #n ")" ::: "memory")
template <bool SP>
__device__ __forceinline__ void wqkv_issue(const u16* Wm, const int hf, const int h, const int w, const int l, const unsigned sb) {
  const char* base = (const char*)Wm + (size_t)(256 * hf + 32 * w) * (HD * 2) + h * (DH * 2);
#pragma unroll
  for (int i = 0; i < 8; ++i) {
    const int r = l >> 4;
    const int f = SP ? ((r & 1) | ((i & 1) << 1) | (((i >> 1) & 1) << 2)) : (r | (((i >> 1) & 1) << 2));
    glds16(base, (unsigned)((4 * i + r) * (HD * 2) + (((l & 15) ^ (f << 1)) << 4)), sb + w * 16384 + hf * 8192 + i * 1024);
  }
}

__device__ __forceinline__ unsigned row_stats(const v4u raw) {
  float v[8];
#pragma unroll
  for (int e = 0; e < 4; ++e) { v[2 * e] = lo16(raw[e]); v[2 * e + 1] = hi16(raw[e]); }
  float s = 0.f;
#pragma unroll
  for (int e = 0; e < 8; ++e) s += v[e];
  const float mean = wave_sum(s) * (1.0f / DM);
  float q = 0.f;
#pragma unroll
  for (int e = 0; e < 8; ++e) { const float t = v[e] - mean; q += t * t; }
  const float var = wave_sum(q) * (1.0f / DM);
  const float sd = bfr(sqrtf(bfr(bfr(var) + 1e-5f)));
  return (unsigned)f2bf(mean) | ((unsigned)f2bf(sd) << 16);
}
__device__ __forceinline__ void ask_slow(const MegaArgs& A, const int k, unsigned (&t)[3]) {
  const int l = lane_id(), x = blockIdx.x & 7;
  const char *wq = (const char*)A.Wq, *wv = (const char*)A.Wv;
  asm("" : "+s"(wq), "+s"(wv));
#pragma unroll
  for (int m = 0; m < 3; ++m) {
    const char* W = m == 1 ? (const char*)A.Wk : ((m == 0) == (x < 4)) ? wv : wq;
    const int a = (int)((unsigned long)W >> 8) + 8 * (x & 3), h = k < 32 ? ((1 - a) & 3) + 4 * (k & 1) : (31 - a) & 31;
    const int r = k < 32 ? k >> 1 : k - 32;
    t[m] = __builtin_amdgcn_raw_buffer_load_b32(MKRS(W, (unsigned)k < 48u && h < 8 ? DM * HD * 2 : 0), (l >> 1) * (HD * 2) + (l & 1) * 128,
                                                (32 * (r & 15)) * (HD * 2) + (8 * (x & 3) + (h & 7)) * 256, 0);
  }
}
#define ASKED(t) asm volatile("" ::"v"(t[0]), "v"(t[1]), "v"(t[2]))
__device__ __forceinline__ u64 start_stats(const MegaArgs& A, const int idx) {
  const int w = A.wv, l = lane_id();
  const int r0 = 32 * idx + 4 * w;
  unsigned ta[3];
  v4u raw[4];
#pragma unroll
  for (int i = 0; i < 4; ++i) raw[i] = *reinterpret_cast<const v4u*>(A.x0 + (size_t)(r0 + i) * DM + l * 8);
  asm volatile("" ::: "memory");
  ask_slow(A, 16 * w + (idx & 3) + (idx >> 4) * 4, ta);
  const u64 tag = call_tag(A);
  unsigned stw = 0u;
#pragma unroll
  for (int i = 0; i < 4; ++i) {
    const unsigned t = row_stats(raw[i]);
    if ((l >> 1) == i) stw = t;
  }
  if (l < 8) __hip_atomic_store((gu64*)(A.ws + OFF_GT) + 2 * r0 + l, tagged(tag, (l & 1) ? stw : stw << 16), RLX_AGENT);
  SST(0);
  ASKED(ta);
  return tag;
}
struct Cx {
  f32x2_t r, i;
};
__device__ __forceinline__ Cx cmul(const Cx a, const Cx b) { return Cx{a.r * b.r - a.i * b.i, a.r * b.i + a.i * b.r}; }
struct KGen {
  unsigned pa, pb, pc, pd;
  u16 pe;
  f32x2_t ar, ai;
  Cx C;
  char* st;
  v4u bh[8], bl[8];
  __device__ __forceinline__ void load(const MegaArgs& A, const int d, const int l) {
    pa = *reinterpret_cast<const unsigned*>(A.logAre + d * NST + 2 * l);
    pb = *reinterpret_cast<const unsigned*>(A.Aim + d * NST + 2 * l);
    pc = *reinterpret_cast<const unsigned*>(A.Cre + d * NST + 2 * l);
    pd = *reinterpret_cast<const unsigned*>(A.Cim + d * NST + 2 * l);
    pe = A.logdt[d];
  }
  __device__ __forceinline__ void setup() {
    const float dt = bfr(expf(bf2f(pe)));
    ar = f32x2_t{-bfr(expf(lo16(pa))) * dt, -bfr(expf(hi16(pa))) * dt};
    ai = f32x2_t{lo16(pb) * dt, hi16(pb) * dt};
    C = Cx{f32x2_t{lo16(pc) * dt, hi16(pc) * dt}, f32x2_t{lo16(pd) * dt, hi16(pd) * dt}};
  }
  __device__ __forceinline__ Cx power(const float p) const {
    Cx z;
#pragma unroll
    for (int e = 0; e < 2; ++e) {
      const float x = ai[e] * p;
      const float f = __builtin_fmaf(x, 6.4206382e-9f, __builtin_fmaf(x, 0.15915494f, -__builtin_rintf(x * 0.15915494f)));
      const float m = __expf(ar[e] * p);
      z.r[e] = m * __builtin_amdgcn_cosf(f);
      z.i[e] = m * __builtin_amdgcn_sinf(f);
    }
    return z;
  }
  __device__ __forceinline__ char* rows(char* p, Cx y, const Cx s, const int n) const {
#pragma unroll 1
    for (int i = 0; i < n; i += 2) {
#pragma unroll
      for (int k = 0; k < 2; ++k) {
        const unsigned h0 = pk2(y.r.x, y.r.y), h1 = pk2(y.i.x, y.i.y);
        const f32x2_t a = y.r - f32x2_t{lo16(h0), hi16(h0)}, b = y.i - f32x2_t{lo16(h1), hi16(h1)};
        *reinterpret_cast<v2u*>(p + 1040 * k) = v2u{h0, h1};
        *reinterpret_cast<v2u*>(p + 1040 * k + 512) = v2u{pk2(a.x, a.y), pk2(b.x, b.y)};
        y = cmul(y, s);
      }
      p += 2080;
    }
    return p;
  }
  __device__ __forceinline__ void get(v4u (&h)[8], v4u (&o)[8], const int l) const {
    const char* p = st + (l & 15) * 1040 + (l >> 4) * 16;
    asm volatile("" ::: "memory");
#pragma unroll
    for (int k = 0; k < 8; ++k) {
      h[k] = *reinterpret_cast<const v4u*>(p + 64 * k);
      o[k] = *reinterpret_cast<const v4u*>(p + 512 + 64 * k);
    }
    asm volatile("" ::: "memory");
  }
  __device__ __forceinline__ void columns(const int l) {
    Cx s = power(16.f);
    s.i = -s.i;
    rows(st + l * 8, Cx{f32x2_t{1.f, 1.f}, f32x2_t{0.f, 0.f}}, s, 16);
    get(bh, bl, l);
  }
  __device__ __forceinline__ v4u tile(const int l) const {
    v4u h[8], o[8];
    get(h, o, l);
    f32x4 d = {}, e = {}, f = {};
#pragma unroll
    for (int k = 0; k < 8; ++k) {
      d = MFMA16(BF8(h[k]), BF8(bh[k]), d);
      e = MFMA16(BF8(o[k]), BF8(bh[k]), e);
      f = MFMA16(BF8(h[k]), BF8(bl[k]), f);
    }
    d = (d + (e + f)) * 2.0f;
    v4u r;
#pragma unroll
    for (int i = 0; i < 4; ++i) r[i] = pk2(d[i], d[i] - lo16(pk2(d[i], d[i])));
    return r;
  }
};
__device__ __forceinline__ void start_kgen(const MegaArgs& A, char* smem, const int kg) {
  const int w = A.wv, l = lane_id(), tid = 64 * w + l, c = l & 15, g = l >> 4;
  KGen G;
  G.load(A, 8 * kg + w, l);
  {
    const __amdgpu_buffer_rsrc_t rz = MKRS(A.ws + OFF_Z, Z_BYTES);
#pragma unroll
    for (int i = 0; i < 4; ++i) ST128((v4u{0u, 0u, 0u, 0u}), rz, tid * 16, (i * 64 + kg) * 8192);
  }
  const u64 tag = call_tag(A);
  DRAIN();
  __syncthreads();
  if (tid < 8) __hip_atomic_store((gu64*)(A.ws + OFF_RD) + tid * 64 + kg, tag, RLX_AGENT);
  asm volatile("" : "+v"(G.pa), "+v"(G.pb), "+v"(G.pe));
  G.setup();
  G.st = smem + w * 16640;
  const __amdgpu_buffer_rsrc_t rs = MKRS(A.ws + OFF_KF + (8 * kg + w) * 2048, 386 * 4);
  SST(1);
  unsigned tw = 0u;
  if (TLBW && tid < 384) {
    const unsigned o = (unsigned)OFF_XA + (unsigned)(tid * 8 + ((blockIdx.x >> 3) & 7)) * 65536u;
    if (o < WS_TOTAL) asm volatile("global_load_dword %0, %1, %2" : "=v"(tw) : "v"(o), "s"(A.ws) : "memory");
  }
  G.columns(l);
  const Cx zb = G.power(256.f);
  Cx b = G.C, z = G.power(2.f);
#pragma unroll 1
  for (int f = 1; f < 9;) {
    char* p = G.st + l * 8;
    const int fe = f == 1 ? 4 : 9;
#pragma unroll 1
    for (; f < fe; ++f) {
      b = cmul(b, zb);
      if (f == 2 || f == 4) z = cmul(z, z);
      p = G.rows(p, b, z, f == 1 ? 8 : f < 4 ? 4 : 2);
    }
    const v4u r = G.tile(l);
    if (f == 4) {
      ST128(r, rs, (g < 2 ? 8 * c + 4 * g : 64 * g + 4 * c) * 4, 0);
    } else {
      ST64((v2u{r.x, r.y}), rs, (256 + 64 * g + 2 * c) * 4, 0);
      ST64((v2u{r.z, r.w}), rs, (288 + 64 * g + 2 * c) * 4, 0);
    }
  }
  SST(2);
  DRAIN();
  asm volatile("" ::"v"(tw));
  if (l == 0) __hip_atomic_store((gu64*)(A.ws + OFF_KG) + kg * 16 + w, tag, RLX_AGENT);
  __syncthreads();
  SST(3);
}

constexpr int C_SM = 131072, C_SO = C_SM + 8192, C_KT = C_SO + 2048, C_X = C_KT + 16384, C_SQ = C_X + 1024;
constexpr int C_SK = C_SQ + (7 * QS + 128) * 2;
static_assert(C_SK + 8 * QS * 2 <= 163840, "lds");
#define TDSLOT 0
#define CHAIN_ASM_ATT \
  "s_mov_b32 s92, 1152\n" \
  "s_mov_b32 s93, 576\n" \
  "s_mov_b32 s94, 2272\n" \
  "s_mov_b32 s95, 272\n" \
  "s_mov_b32 s96, 288\n" \
  "s_mov_b32 s91, 0x40000001\n" \
  "s_mov_b32 s84, 0x4b400000\n" \
  "s_mov_b32 s85, 0x4b400000\n" \
  "s_mov_b32 s78, 0x3e0293ee\n" \
  "s_mov_b32 s76, 0xff00ff00\n" \
  "s_mov_b32 s77, 0xff00ff00\n" \
  "s_add_u32 s58, s56, s60\n" \
  "s_addc_u32 s59, s57, 0\n" \
  "s_lshl_b32 s98, s73, 10\n" \
  "s_add_u32 s98, s98, s74\n" \
  "s_add_u32 s98, s98, 131072\n" \
  "s_lshl_b32 s97, s73, 5\n" \
  "s_add_u32 s97, s97, s74\n" \
  "s_add_u32 s97, s97, 158720\n" \
  "s_lshl_b32 s99, s73, 10\n" \
  "v_mbcnt_lo_u32_b32 v48, -1, 0\n" \
  "v_mbcnt_hi_u32_b32 v48, -1, v48\n" \
  "s_add_u32 s88, s74, 157760\n" \
  "v_mov_b32 v59, s88\n" \
  "v_and_b32 v49, 15, v48\n" \
  "v_lshl_add_u32 v8, v49, 1, s97\n" \
  "v_bfe_u32 v49, v48, 4, 1\n" \
  "v_mad_u32_u24 v8, v49, s92, v8\n" \
  "v_lshrrev_b32 v49, 5, v48\n" \
  "v_mad_u32_u24 v9, v49, s93, v8\n" \
  "v_mad_u32_u24 v8, v49, s94, v8\n" \
  "v_add_u32 v10, s99, v1\n" \
  "s_lshr_b32 s88, s99, 2\n" \
  "s_add_u32 s88, s88, s74\n" \
  "s_add_u32 s88, s88, 139264\n" \
  "v_lshl_add_u32 v7, v49, 10, s88\n" \
  "v_and_b32 v49, 31, v48\n" \
  "v_lshl_add_u32 v7, v49, 3, v7\n" \
  "v_mov_b64 v[12:13], 0\n" \
  "v_mov_b64 v[14:15], 0\n" \
  "v_mov_b64 v[16:17], 0\n" \
  "v_mov_b64 v[18:19], 0\n" \
  "v_mov_b64 v[20:21], 0\n" \
  "v_mov_b64 v[22:23], 0\n" \
  "v_mov_b32 v24, 0x44870000\n" \
  "v_mul_f32 v24, s72, v24\n" \
  "s_nop 0\n" \
  "v_readfirstlane_b32 s86, v24\n" \
  "s_lshr_b32 s86, s86, 23\n" \
  "s_addk_i32 s86, 0xff83\n" \
  "s_ashr_i32 s86, s86, 1\n" \
  "s_max_i32 s86, s86, -20\n" \
  "s_min_i32 s86, s86, 100\n" \
  "s_mov_b32 s82, s86\n" \
  "s_lshr_b32 s83, s72, 23\n" \
  "s_sub_i32 s83, s83, 242\n" \
  "s_lshl_b32 s88, s73, 10\n" \
  "v_add_u32 v24, s88, v1\n" \
  "ds_read_b64 v[32:33], v24 offset:0\n" \
  "ds_read_b64 v[36:37], v24 offset:8\n" \
  "s_waitcnt lgkmcnt(0)\n" \
  "v_mov_b32 v11, 0\n" \
  "v_dot2c_f32_bf16 v11, v32, v32\n" \
  "v_dot2c_f32_bf16 v11, v33, v33\n" \
  "v_dot2c_f32_bf16 v11, v36, v36\n" \
  "v_dot2c_f32_bf16 v11, v37, v37\n" \
  "s_nop 1\n" \
  "s_nop 0\n" \
  "v_cmpx_lt_f32 vcc, 2.0, v11\n" \
  "ds_max_u32 v59, v11\n" \
  "s_mov_b64 exec, -1\n" \
  ".LCA_TOP_%=:\n" \
  "s_waitcnt lgkmcnt(0)\n" \
  "s_barrier\n" \
  "ds_read_b128 v[40:43], v1 offset:0\n" \
  "ds_read_b128 v[44:47], v1 offset:1024\n" \
  "ds_read_b128 v[48:51], v1 offset:2048\n" \
  "ds_read_b128 v[52:55], v1 offset:3072\n" \
  "ds_read_b128 v[56:59], v1 offset:4096\n" \
  "ds_read_b128 v[60:63], v1 offset:5120\n" \
  "s_cmp_lg_u32 s73, 0\n" \
  "s_cbranch_scc1 .LCA_NOZ_%=\n" \
  "s_xor_b32 s88, s60, 0x8000\n" \
  "s_add_u32 s88, s88, s61\n" \
  "buffer_store_dwordx2 v[12:13], v5, s[40:43], s88 offen sc1\n" \
  ".LCA_NOZ_%=:\n" \
  "s_waitcnt lgkmcnt(5)\n" \
  "v_smfmac_f32_16x16x64_bf16 v[20:23], v[40:43], v[192:199], v0\n" \
  "v_smfmac_f32_16x16x64_bf16 v[12:15], v[40:43], v[64:71], v0\n" \
  "v_smfmac_f32_16x16x64_bf16 v[16:19], v[40:43], v[128:135], v0\n" \
  "ds_read_b128 v[40:43], v1 offset:6144\n" \
  "s_waitcnt lgkmcnt(5)\n" \
  "v_smfmac_f32_16x16x64_bf16 v[20:23], v[44:47], v[200:207], v0\n" \
  "v_smfmac_f32_16x16x64_bf16 v[12:15], v[44:47], v[72:79], v0\n" \
  "v_smfmac_f32_16x16x64_bf16 v[16:19], v[44:47], v[136:143], v0\n" \
  "ds_read_b128 v[44:47], v1 offset:7168\n" \
  "s_waitcnt lgkmcnt(5)\n" \
  "v_smfmac_f32_16x16x64_bf16 v[20:23], v[48:51], v[208:215], v0\n" \
  "v_smfmac_f32_16x16x64_bf16 v[12:15], v[48:51], v[80:87], v0\n" \
  "v_smfmac_f32_16x16x64_bf16 v[16:19], v[48:51], v[144:151], v0\n" \
  "ds_read_b128 v[24:27], v1 offset:10240\n" \
  "ds_read_b128 v[28:31], v1 offset:14336\n" \
  "s_waitcnt lgkmcnt(6)\n" \
  "v_smfmac_f32_16x16x64_bf16 v[20:23], v[52:55], v[216:223], v0\n" \
  "v_smfmac_f32_16x16x64_bf16 v[12:15], v[52:55], v[88:95], v0\n" \
  "v_smfmac_f32_16x16x64_bf16 v[16:19], v[52:55], v[152:159], v0\n" \
  "ds_read_b128 v[32:35], v1 offset:11264\n" \
  "ds_read_b128 v[36:39], v1 offset:15360\n" \
  "s_waitcnt lgkmcnt(7)\n" \
  "v_smfmac_f32_16x16x64_bf16 v[20:23], v[56:59], v[224:231], v0\n" \
  "v_smfmac_f32_16x16x64_bf16 v[12:15], v[56:59], v[96:103], v0\n" \
  "v_smfmac_f32_16x16x64_bf16 v[16:19], v[56:59], v[160:167], v0\n" \
  "s_waitcnt lgkmcnt(6)\n" \
  "v_smfmac_f32_16x16x64_bf16 v[20:23], v[60:63], v[232:239], v0\n" \
  "v_smfmac_f32_16x16x64_bf16 v[12:15], v[60:63], v[104:111], v0\n" \
  "v_smfmac_f32_16x16x64_bf16 v[16:19], v[60:63], v[168:175], v0\n" \
  "s_waitcnt lgkmcnt(5)\n" \
  "v_smfmac_f32_16x16x64_bf16 v[20:23], v[40:43], v[240:247], v0\n" \
  "v_smfmac_f32_16x16x64_bf16 v[12:15], v[40:43], v[112:119], v0\n" \
  "v_smfmac_f32_16x16x64_bf16 v[16:19], v[40:43], v[176:183], v0\n" \
  "s_waitcnt lgkmcnt(4)\n" \
  "v_smfmac_f32_16x16x64_bf16 v[20:23], v[44:47], v[248:255], v0\n" \
  "v_smfmac_f32_16x16x64_bf16 v[12:15], v[44:47], v[120:127], v0\n" \
  "v_smfmac_f32_16x16x64_bf16 v[16:19], v[44:47], v[184:191], v0\n" \
  "s_cmp_eq_u32 s71, 0\n" \
  "s_nop 4\n" \
  "v_permlane32_swap_b32 v20, v22\n" \
  "v_permlane32_swap_b32 v21, v23\n" \
  "v_pk_add_f32 v[20:21], v[20:21], v[22:23]\n" \
  "v_permlane32_swap_b32 v12, v16\n" \
  "v_permlane32_swap_b32 v13, v17\n" \
  "v_cvt_pk_bf16_f32 v20, v20, v21\n" \
  "v_pk_add_f32 v[12:13], v[12:13], v[16:17]\n" \
  "v_permlane32_swap_b32 v14, v18\n" \
  "v_permlane32_swap_b32 v15, v19\n" \
  "ds_write_b16 v9, v20 offset:128\n" \
  "v_cvt_pk_bf16_f32 v12, v12, v13\n" \
  "v_pk_add_f32 v[14:15], v[14:15], v[18:19]\n" \
  "ds_write_b16_d16_hi v9, v20 offset:416\n" \
  "ds_write_b16 v8, v12\n" \
  "v_cvt_pk_bf16_f32 v14, v14, v15\n" \
  "ds_write_b16_d16_hi v8, v12 offset:288\n" \
  "ds_write_b16 v8, v14 offset:576\n" \
  "ds_write_b16_d16_hi v8, v14 offset:864\n" \
  "s_setprio 3\n" \
  "s_waitcnt lgkmcnt(0)\n" \
  "s_barrier\n" \
  "s_cbranch_scc1 .LCA_LAST_%=\n" \
  "ds_read_b32 v11, v1 offset:26688\n" \
  "ds_read_b128 v[40:43], v2 offset:0\n" \
  "ds_read_b128 v[44:47], v3 offset:0\n" \
  "ds_read_b128 v[48:51], v2 offset:64\n" \
  "ds_read_b128 v[52:55], v3 offset:64\n" \
  "ds_read_b128 v[56:59], v2 offset:128\n" \
  "ds_read_b128 v[60:63], v3 offset:128\n" \
  "s_waitcnt lgkmcnt(5)\n" \
  "v_mfma_f32_16x16x32_bf16 v[12:15], v[24:27], v[40:43], 0\n" \
  "v_mfma_f32_16x16x32_bf16 v[16:19], v[28:31], v[40:43], 0\n" \
  "v_readfirstlane_b32 s86, v11\n" \
  "ds_read_b128 v[24:27], v1 offset:12288\n" \
  "ds_read_b128 v[28:31], v1 offset:16384\n" \
  "s_waitcnt lgkmcnt(6)\n" \
  "v_mfma_f32_16x16x32_bf16 v[20:23], v[44:47], v[40:43], 0\n" \
  "s_lshr_b32 s86, s86, 23\n" \
  "s_add_i32 s86, s86, s83\n" \
  "ds_read_b128 v[40:43], v2 offset:192\n" \
  "ds_read_b128 v[44:47], v3 offset:192\n" \
  "s_waitcnt lgkmcnt(7)\n" \
  "v_mfma_f32_16x16x32_bf16 v[12:15], v[32:35], v[48:51], v[12:15]\n" \
  "v_mfma_f32_16x16x32_bf16 v[16:19], v[36:39], v[48:51], v[16:19]\n" \
  "s_ashr_i32 s86, s86, 1\n" \
  "s_max_i32 s86, s86, s82\n" \
  "ds_read_b128 v[32:35], v1 offset:13312\n" \
  "ds_read_b128 v[36:39], v1 offset:17408\n" \
  "s_waitcnt lgkmcnt(8)\n" \
  "v_mfma_f32_16x16x32_bf16 v[20:23], v[52:55], v[48:51], v[20:23]\n" \
  "s_min_i32 s86, s86, 100\n" \
  "s_lshl_b32 s86, s86, 23\n" \
  "s_sub_i32 s84, 0x4a800000, s86\n" \
  "ds_read_b128 v[48:51], v10 offset:18432\n" \
  "ds_read_b128 v[52:55], v10 offset:22528\n" \
  "s_waitcnt lgkmcnt(8)\n" \
  "v_mfma_f32_16x16x32_bf16 v[20:23], v[60:63], v[56:59], v[20:23]\n" \
  "s_waitcnt lgkmcnt(6)\n" \
  "v_mfma_f32_16x16x32_bf16 v[12:15], v[24:27], v[56:59], v[12:15]\n" \
  "v_mfma_f32_16x16x32_bf16 v[16:19], v[28:31], v[56:59], v[16:19]\n" \
  "ds_read_b128 v[56:59], v6 offset:0\n" \
  "ds_read_b128 v[60:63], v6 offset:1024\n" \
  "s_waitcnt lgkmcnt(6)\n" \
  "v_mfma_f32_16x16x32_bf16 v[20:23], v[44:47], v[40:43], v[20:23]\n" \
  "ds_read_b128 v[24:27], v6 offset:2048\n" \
  "ds_read_b128 v[28:31], v6 offset:3072\n" \
  "s_waitcnt lgkmcnt(6)\n" \
  "v_mfma_f32_16x16x32_bf16 v[12:15], v[32:35], v[40:43], v[12:15]\n" \
  "v_mfma_f32_16x16x32_bf16 v[16:19], v[36:39], v[40:43], v[16:19]\n" \
  "ds_read_b128 v[32:35], v6 offset:8192\n" \
  "ds_read_b128 v[36:39], v6 offset:9216\n" \
  "s_waitcnt lgkmcnt(6)\n" \
  "v_cndmask_b32 v20, v20, v21, s[76:77]\n" \
  "v_mov_b32 v41, 0\n" \
  "v_mov_b32 v42, 0\n" \
  "s_nop 1\n" \
  "v_cndmask_b32 v12, v12, v16, s[76:77]\n" \
  "v_cndmask_b32 v13, v13, v17, s[76:77]\n" \
  "v_cndmask_b32 v14, v14, v18, s[76:77]\n" \
  "v_cndmask_b32 v15, v15, v19, s[76:77]\n" \
  "v_maximum3_f32 v16, v20, v12, v13\n" \
  "v_maximum3_f32 v16, v16, v14, v15\n" \
  "s_nop 1\n" \
  "v_max_f32_dpp v16, v16, v16 row_ror:8 row_mask:0xf bank_mask:0xf\n" \
  "v_mov_b32 v17, v16\n" \
  "s_nop 1\n" \
  "v_permlane16_swap_b32 v16, v17\n" \
  "v_max_f32 v16, v16, v17\n" \
  "v_mov_b32 v17, v16\n" \
  "s_nop 1\n" \
  "v_permlane32_swap_b32 v16, v17\n" \
  "v_max_f32 v16, v16, v17\n" \
  "v_mul_f32 v16, s78, v16\n" \
  "v_pk_fma_f32 v[12:13], v[12:13], s[78:79], v[16:17] op_sel_hi:[1,0,0] neg_lo:[0,0,1] neg_hi:[0,0,1]\n" \
  "v_pk_fma_f32 v[14:15], v[14:15], s[78:79], v[16:17] op_sel_hi:[1,0,0] neg_lo:[0,0,1] neg_hi:[0,0,1]\n" \
  "v_fma_f32 v20, v20, s78, -v16\n" \
  "v_exp_f32 v12, v12\n" \
  "v_exp_f32 v13, v13\n" \
  "v_exp_f32 v14, v14\n" \
  "v_exp_f32 v15, v15\n" \
  "v_exp_f32 v20, v20\n" \
  "s_barrier\n" \
  "ds_read_b64 v[22:23], v7 offset:0\n" \
  "v_cvt_pk_bf16_f32 v44, v12, v13\n" \
  "v_cvt_pk_bf16_f32 v45, v14, v15\n" \
  "v_pk_add_f32 v[8:9], v[12:13], v[14:15]\n" \
  "v_mov_b32_dpp v46, v44 row_ror:8 row_mask:0xf bank_mask:0xf\n" \
  "v_mov_b32_dpp v17, v20 row_ror:8 row_mask:0xf bank_mask:0xf\n" \
  "v_mov_b32_dpp v47, v45 row_ror:8 row_mask:0xf bank_mask:0xf\n" \
  "v_add_f32 v10, v8, v20\n" \
  "v_cvt_pk_bf16_f32 v40, v20, v17\n" \
  "v_add_f32 v10, v10, v9\n" \
  "v_mfma_f32_16x16x32_bf16 v[12:15], v[48:51], v[44:47], 0\n" \
  "v_cvt_pk_bf16_f32 v43, v20, v17\n" \
  "v_add_f32_dpp v10, v10, v10 row_ror:8 row_mask:0xf bank_mask:0xf\n" \
  "v_mfma_f32_16x16x32_bf16 v[16:19], v[52:55], v[44:47], 0\n" \
  "v_mov_b32 v11, v10\n" \
  "s_waitcnt lgkmcnt(0)\n" \
  "v_mfma_f32_16x16x16_bf16 v[12:15], v[22:23], v[40:41], v[12:15]\n" \
  "v_mfma_f32_16x16x16_bf16 v[16:19], v[22:23], v[42:43], v[16:19]\n" \
  "v_permlane16_swap_b32 v10, v11\n" \
  "v_add_f32 v10, v10, v11\n" \
  "ds_read_b128 v[48:51], v6 offset:10240\n" \
  "v_mov_b32 v11, v10\n" \
  "ds_read_b128 v[52:55], v6 offset:11264\n" \
  "v_mov_b64 v[20:21], 0\n" \
  "v_mov_b64 v[22:23], 0\n" \
  "v_permlane32_swap_b32 v10, v11\n" \
  "v_add_f32 v10, v10, v11\n" \
  "v_rcp_f32 v10, v10\n" \
  "s_nop 0\n" \
  "v_pk_mul_f32 v[12:13], v[12:13], v[10:11] op_sel_hi:[1,0]\n" \
  "v_pk_mul_f32 v[14:15], v[14:15], v[10:11] op_sel_hi:[1,0]\n" \
  "v_pk_mul_f32 v[16:17], v[16:17], v[10:11] op_sel_hi:[1,0]\n" \
  "v_pk_mul_f32 v[18:19], v[18:19], v[10:11] op_sel_hi:[1,0]\n" \
  "v_cvt_pk_bf16_f32 v40, v12, v13\n" \
  "v_cvt_pk_bf16_f32 v41, v14, v15\n" \
  "v_cvt_pk_bf16_f32 v42, v16, v17\n" \
  "ds_write_b64 v4, v[40:41]\n" \
  "v_cvt_pk_bf16_f32 v43, v18, v19\n" \
  "ds_write_b64 v4, v[42:43] offset:1024\n" \
  "v_mov_b64 v[12:13], 0\n" \
  "v_mov_b64 v[14:15], 0\n" \
  "v_mov_b64 v[16:17], 0\n" \
  "v_mov_b64 v[18:19], 0\n" \
  "v_mov_b64 v[8:9], 0\n" \
  "v_mov_b64 v[10:11], 0\n" \
  "s_setprio 0\n" \
  "s_waitcnt lgkmcnt(0)\n" \
  "s_barrier\n" \
  "ds_read_b128 v[40:43], v1 offset:8192\n" \
  "ds_read_b128 v[44:47], v1 offset:9216\n" \
  "s_waitcnt lgkmcnt(1)\n" \
  "v_smfmac_f32_16x16x64_bf16 v[12:15], v[40:43], v[56:63], v0\n" \
  "ds_read_b128 v[56:59], v6 offset:4096\n" \
  "ds_read_b128 v[60:63], v6 offset:5120\n" \
  "v_smfmac_f32_16x16x64_bf16 v[16:19], v[40:43], v[24:31], v0\n" \
  "ds_read_b128 v[24:27], v6 offset:6144\n" \
  "ds_read_b128 v[28:31], v6 offset:7168\n" \
  "s_waitcnt lgkmcnt(4)\n" \
  "v_smfmac_f32_16x16x64_bf16 v[12:15], v[44:47], v[32:39], v0\n" \
  "ds_read_b128 v[32:35], v6 offset:12288\n" \
  "ds_read_b128 v[36:39], v6 offset:13312\n" \
  "v_smfmac_f32_16x16x64_bf16 v[16:19], v[44:47], v[48:55], v0\n" \
  "ds_read_b128 v[48:51], v6 offset:14336\n" \
  "ds_read_b128 v[52:55], v6 offset:15360\n" \
  "s_nop 5\n" \
  "v_permlane32_swap_b32 v12, v17\n" \
  "v_permlane32_swap_b32 v13, v16\n" \
  "v_permlane32_swap_b32 v14, v19\n" \
  "v_permlane32_swap_b32 v15, v18\n" \
  "v_pk_add_f32 v[12:13], v[12:13], v[16:17] op_sel:[0,1] op_sel_hi:[1,0]\n" \
  "v_pk_add_f32 v[14:15], v[14:15], v[18:19] op_sel:[0,1] op_sel_hi:[1,0]\n" \
  "v_pk_fma_f32 v[12:13], v[12:13], s[84:85], s[84:85] op_sel:[0,0,1] op_sel_hi:[1,0,1]\n" \
  "v_pk_fma_f32 v[14:15], v[14:15], s[84:85], s[84:85] op_sel:[0,0,1] op_sel_hi:[1,0,1]\n" \
  "v_lshl_add_u32 v12, v12, 6, s91\n" \
  "v_lshl_add_u32 v13, v13, 2, s75\n" \
  "global_atomic_add_x2 v5, v[12:13], s[58:59] offset:0\n" \
  "v_lshl_add_u32 v14, v14, 6, s91\n" \
  "v_lshl_add_u32 v15, v15, 2, s75\n" \
  "global_atomic_add_x2 v5, v[14:15], s[58:59] offset:2048\n" \
  "s_waitcnt lgkmcnt(6)\n" \
  "v_smfmac_f32_16x16x64_bf16 v[20:23], v[40:43], v[56:63], v0\n" \
  "s_waitcnt lgkmcnt(4)\n" \
  "v_smfmac_f32_16x16x64_bf16 v[8:11], v[40:43], v[24:31], v0\n" \
  "s_waitcnt lgkmcnt(2)\n" \
  "v_smfmac_f32_16x16x64_bf16 v[20:23], v[44:47], v[32:39], v0\n" \
  "s_waitcnt lgkmcnt(0)\n" \
  "v_smfmac_f32_16x16x64_bf16 v[8:11], v[44:47], v[48:55], v0\n" \
  "s_nop 7\n" \
  "v_permlane32_swap_b32 v20, v9\n" \
  "v_permlane32_swap_b32 v21, v8\n" \
  "v_permlane32_swap_b32 v22, v11\n" \
  "v_permlane32_swap_b32 v23, v10\n" \
  "v_pk_add_f32 v[20:21], v[20:21], v[8:9] op_sel:[0,1] op_sel_hi:[1,0]\n" \
  "v_pk_add_f32 v[22:23], v[22:23], v[10:11] op_sel:[0,1] op_sel_hi:[1,0]\n" \
  "v_pk_fma_f32 v[20:21], v[20:21], s[84:85], s[84:85] op_sel:[0,0,1] op_sel_hi:[1,0,1]\n" \
  "v_pk_fma_f32 v[22:23], v[22:23], s[84:85], s[84:85] op_sel:[0,0,1] op_sel_hi:[1,0,1]\n" \
  "v_lshl_add_u32 v20, v20, 6, s91\n" \
  "v_lshl_add_u32 v21, v21, 2, s75\n" \
  "global_atomic_add_x2 v5, v[20:21], s[58:59] offset:3072\n" \
  "v_lshl_add_u32 v22, v22, 6, s91\n" \
  "v_lshl_add_u32 v23, v23, 2, s75\n" \
  "global_atomic_add_x2 v5, v[22:23], s[58:59] offset:1024\n" \
  "s_memrealtime s[38:39]\n" \
  "v_mbcnt_lo_u32_b32 v48, -1, 0\n" \
  "v_mbcnt_hi_u32_b32 v48, -1, v48\n" \
  "s_add_u32 s88, s74, 157760\n" \
  "v_mov_b32 v59, s88\n" \
  "v_lshl_add_u32 v52, v48, 2, v59\n" \
  "ds_read_b32 v52, v52 offset:192\n" \
  "v_lshrrev_b32 v49, 3, v48\n" \
  "v_and_b32 v50, 7, v48\n" \
  "v_lshlrev_b32 v50, 4, v50\n" \
  "v_lshl_or_b32 v44, v49, 8, v50\n" \
  "s_mov_b32 s87, 0x40000\n" \
  "v_and_b32 v49, 15, v48\n" \
  "v_lshl_add_u32 v8, v49, 1, s97\n" \
  "v_bfe_u32 v49, v48, 4, 1\n" \
  "v_mad_u32_u24 v8, v49, s92, v8\n" \
  "v_lshrrev_b32 v49, 5, v48\n" \
  "v_mad_u32_u24 v9, v49, s93, v8\n" \
  "v_mad_u32_u24 v8, v49, s94, v8\n" \
  "v_add_u32 v10, s99, v1\n" \
  "s_lshr_b32 s88, s99, 2\n" \
  "s_add_u32 s88, s88, s74\n" \
  "s_add_u32 s88, s88, 139264\n" \
  "v_lshl_add_u32 v7, v49, 10, s88\n" \
  "v_and_b32 v49, 31, v48\n" \
  "v_lshl_add_u32 v7, v49, 3, v7\n" \
  "v_mov_b64 v[12:13], 0\n" \
  "v_mov_b64 v[14:15], 0\n" \
  "v_mov_b64 v[16:17], 0\n" \
  "v_mov_b64 v[18:19], 0\n" \
  "v_mov_b64 v[20:21], 0\n" \
  "v_mov_b64 v[22:23], 0\n" \
  "v_and_b32 v50, 3, v48\n" \
  "v_lshl_add_u32 v40, v50, 2, s98\n" \
  "v_lshrrev_b32 v50, 4, v48\n" \
  "v_mad_u32_u24 v40, v50, s95, v40\n" \
  "v_bfe_u32 v49, v48, 3, 1\n" \
  "v_lshl_add_u32 v40, v49, 6, v40\n" \
  "v_bfe_u32 v49, v48, 2, 1\n" \
  "v_lshl_add_u32 v40, v49, 7, v40\n" \
  "v_xor_b32 v41, 16, v40\n" \
  "v_xor_b32 v42, 32, v40\n" \
  "v_xor_b32 v43, 48, v40\n" \
  "s_add_i32 s80, s86, 0x32800000\n" \
  "s_add_i32 s81, s86, 0x33800000\n" \
  "s_add_i32 s88, s86, 0xb4000000\n" \
  "v_mov_b32 v46, s88\n" \
  "v_mov_b32 v47, 0\n" \
  "v_mov_b32 v11, 0\n" \
  "s_mov_b32 s89, s60\n" \
  "s_xor_b32 s60, s60, 0x80\n" \
  "s_bitcmp0_b32 s60, 7\n" \
  "s_cselect_b32 s88, 0x8000, 0\n" \
  "s_add_u32 s60, s60, s88\n" \
  "s_and_b32 s60, s60, 0xffff\n" \
  "s_add_u32 s58, s56, s60\n" \
  "s_addc_u32 s59, s57, 0\n" \
  "s_sub_u32 s71, s71, 1\n" \
  "s_waitcnt lgkmcnt(0)\n" \
  "s_sub_u32 s39, s38, 150\n" \
  "v_subrev_u32 v52, s39, v52\n" \
  "v_cmp_lt_u32 vcc, 0x1000, v52\n" \
  "s_cmp_eq_u64 vcc, 0\n" \
  "s_cbranch_scc1 .LCA_OKM_%=\n" \
  ".LCA_AGN_%=:\n" \
  "buffer_load_dword v52, v5, s[0:3], s66 offen sc1\n" \
  "s_sub_u32 s87, s87, 1\n" \
  "s_cbranch_scc1 .LCA_END_%=\n" \
  "s_waitcnt vmcnt(0)\n" \
  "v_cmp_eq_u32 vcc, 0, v52\n" \
  "v_subrev_u32 v52, s39, v52\n" \
  "s_cmp_eq_u64 vcc, 0\n" \
  "s_cbranch_scc1 .LCA_OKM_%=\n" \
  "s_sleep 4\n" \
  "s_branch .LCA_AGN_%=\n" \
  ".LCA_OKM_%=:\n" \
  "s_nop 1\n" \
  "v_max_i32_dpp v52, v52, v52 row_ror:1 row_mask:0xf bank_mask:0xf\n" \
  "s_nop 1\n" \
  "v_max_i32_dpp v52, v52, v52 row_ror:2 row_mask:0xf bank_mask:0xf\n" \
  "s_nop 1\n" \
  "v_max_i32_dpp v52, v52, v52 row_ror:4 row_mask:0xf bank_mask:0xf\n" \
  "s_movk_i32 vcc_lo, 150\n" \
  "v_readlane_b32 s88, v52, 0\n" \
  "s_max_i32 vcc_lo, vcc_lo, s88\n" \
  "v_readlane_b32 s88, v52, 16\n" \
  "s_max_i32 vcc_lo, vcc_lo, s88\n" \
  "v_readlane_b32 s88, v52, 32\n" \
  "s_max_i32 vcc_lo, vcc_lo, s88\n" \
  "v_readlane_b32 s88, v52, 48\n" \
  "s_max_i32 vcc_lo, vcc_lo, s88\n" \
  "s_add_u32 s66, s66, 1024\n" \
  "s_add_u32 vcc_lo, vcc_lo, s39\n" \
  "s_add_u32 vcc_lo, vcc_lo, 30\n" \
  ".LCA_TW_%=:\n" \
  "s_memrealtime s[38:39]\n" \
  "s_waitcnt lgkmcnt(0)\n" \
  "s_sub_u32 s38, s38, vcc_lo\n" \
  "s_cmp_lt_i32 s38, 0\n" \
  "s_cbranch_scc1 .LCA_TW_%=\n" \
  "buffer_load_dwordx4 v[24:27], v44, s[40:43], s89 offen sc1\n" \
  "buffer_load_dwordx4 v[28:31], v44, s[40:43], s89 offen offset:2048 sc1\n" \
  ".LCA_SEE_%=:\n" \
  "s_waitcnt vmcnt(1)\n" \
  "v_bitop3_b32 v45, v24, v26, 32 bitop3:0x80\n" \
  "v_alignbit_b32 v32, v25, v24, 2\n" \
  "v_alignbit_b32 v34, v27, v26, 2\n" \
  "s_waitcnt vmcnt(0)\n" \
  "v_bitop3_b32 v45, v45, v28, v30 bitop3:0x80\n" \
  "v_cmp_eq_u32 vcc, 0, v45\n" \
  "v_cvt_f32_i32 v32, v32\n" \
  "v_cvt_f32_i32 v33, v25\n" \
  "s_cmp_eq_u64 vcc, 0\n" \
  "s_cbranch_scc0 .LCA_POLL_%=\n" \
  "v_cvt_f32_i32 v34, v34\n" \
  "v_cvt_f32_i32 v35, v27\n" \
  "v_pk_fma_f32 v[32:33], v[32:33], s[80:81], v[46:47]\n" \
  "v_alignbit_b32 v36, v29, v28, 2\n" \
  "v_pk_fma_f32 v[34:35], v[34:35], s[80:81], v[46:47]\n" \
  "v_alignbit_b32 v38, v31, v30, 2\n" \
  "v_cvt_pk_bf16_f32 v32, v32, v34\n" \
  "v_cvt_pk_bf16_f32 v33, v33, v35\n" \
  "v_cvt_f32_i32 v36, v36\n" \
  "ds_write_b32 v40, v32\n" \
  "v_cvt_f32_i32 v37, v29\n" \
  "ds_write_b32 v41, v33\n" \
  "v_cvt_f32_i32 v38, v38\n" \
  "v_cvt_f32_i32 v39, v31\n" \
  "v_pk_fma_f32 v[36:37], v[36:37], s[80:81], v[46:47]\n" \
  "v_pk_fma_f32 v[38:39], v[38:39], s[80:81], v[46:47]\n" \
  "v_dot2c_f32_bf16 v11, v32, v32\n" \
  "v_cvt_pk_bf16_f32 v36, v36, v38\n" \
  "v_cvt_pk_bf16_f32 v37, v37, v39\n" \
  "ds_write_b32 v42, v36\n" \
  "ds_write_b32 v43, v37\n" \
  "v_dot2c_f32_bf16 v11, v33, v33\n" \
  "v_dot2c_f32_bf16 v11, v36, v36\n" \
  "v_dot2c_f32_bf16 v11, v37, v37\n" \
  "s_nop 1\n" \
  "s_nop 0\n" \
  "v_cmpx_lt_f32 vcc, 2.0, v11\n" \
  "ds_max_u32 v59, v11\n" \
  "s_mov_b64 exec, -1\n" \
  "s_branch .LCA_TOP_%=\n" \
  ".LCA_POLL_%=:\n" \
  "buffer_load_dwordx4 v[24:27], v44, s[40:43], s89 offen sc1\n" \
  "buffer_load_dwordx4 v[28:31], v44, s[40:43], s89 offen offset:2048 sc1\n" \
  "s_sub_u32 s87, s87, 1\n" \
  "s_cbranch_scc1 .LCA_END_%=\n" \
  "s_branch .LCA_SEE_%=\n" \
  ".LCA_LAST_%=:\n" \
  ".LCA_END_%=:\n" \
  "s_endpgm\n"
#define CHAIN_ASM_VO \
  "s_mov_b32 s92, 1152\n" \
  "s_mov_b32 s93, 576\n" \
  "s_mov_b32 s94, 2272\n" \
  "s_mov_b32 s95, 272\n" \
  "s_mov_b32 s96, 288\n" \
  "s_mov_b32 s91, 0x40000001\n" \
  "s_mov_b32 s84, 0x4b400000\n" \
  "s_mov_b32 s85, 0x4b400000\n" \
  "s_mov_b32 s78, 0x3e0293ee\n" \
  "s_mov_b32 s76, 0xff00ff00\n" \
  "s_mov_b32 s77, 0xff00ff00\n" \
  "s_add_u32 s58, s56, s60\n" \
  "s_addc_u32 s59, s57, 0\n" \
  "s_lshl_b32 s98, s73, 10\n" \
  "s_add_u32 s98, s98, s74\n" \
  "s_add_u32 s98, s98, 131072\n" \
  "s_lshl_b32 s97, s73, 5\n" \
  "s_add_u32 s97, s97, s74\n" \
  "s_add_u32 s97, s97, 160992\n" \
  "s_lshl_b32 s99, s73, 11\n" \
  "s_add_u32 s99, s99, s74\n" \
  "s_add_u32 s99, s99, 133120\n" \
  "s_lshl_b32 s94, s73, 2\n" \
  "s_add_u32 s94, s94, s74\n" \
  "s_add_u32 s94, s94, 157828\n" \
  "v_mbcnt_lo_u32_b32 v48, -1, 0\n" \
  "v_mbcnt_hi_u32_b32 v48, -1, v48\n" \
  "s_add_u32 s88, s74, 157760\n" \
  "v_mov_b32 v59, s88\n" \
  "s_sub_u32 s88, s73, 4\n" \
  "v_and_b32 v51, 15, v48\n" \
  "v_lshl_add_u32 v4, v51, 1, s97\n" \
  "v_bfe_u32 v49, v48, 4, 1\n" \
  "v_mad_u32_u24 v4, v49, s92, v4\n" \
  "v_lshrrev_b32 v50, 5, v48\n" \
  "v_mad_u32_u24 v4, v50, s93, v4\n" \
  "v_lshlrev_b32 v8, 4, v49\n" \
  "v_lshl_add_u32 v8, v51, 5, v8\n" \
  "v_lshl_add_u32 v8, v50, 3, v8\n" \
  "s_lshl_b32 s89, s88, 8\n" \
  "s_add_u32 s89, s89, s74\n" \
  "s_add_u32 s89, s89, 139264\n" \
  "v_and_b32 v49, 31, v48\n" \
  "v_lshl_add_u32 v5, v49, 3, s89\n" \
  "v_lshl_add_u32 v5, v50, 10, v5\n" \
  "v_lshrrev_b32 v49, 2, v51\n" \
  "v_and_b32 v50, 5, v51\n" \
  "v_and_or_b32 v49, v49, 2, v50\n" \
  "s_lshl_b32 s89, s88, 6\n" \
  "s_add_u32 s89, s89, s74\n" \
  "s_add_u32 s89, s89, 160992\n" \
  "v_and_b32 v50, 48, v48\n" \
  "v_add_u32 v6, s89, v50\n" \
  "v_mad_u32_u24 v6, v49, s96, v6\n" \
  "v_lshlrev_b32 v7, 3, v50\n" \
  "v_lshl_add_u32 v7, v49, 4, v7\n" \
  "v_and_b32 v49, 2, v51\n" \
  "v_lshl_add_u32 v7, v49, 29, v7\n" \
  "v_mov_b64 v[12:13], 0\n" \
  "v_mov_b64 v[14:15], 0\n" \
  "v_mov_b64 v[16:17], 0\n" \
  "v_mov_b64 v[18:19], 0\n" \
  "v_mov_b64 v[20:21], 0\n" \
  "v_mov_b64 v[22:23], 0\n" \
  "v_lshlrev_b32 v10, 4, v48\n" \
  "s_add_u32 s78, s74, 157952\n" \
  "v_lshl_add_u32 v52, v48, 2, v59\n" \
  "ds_write_b32 v52, v12 offset:192\n" \
  "s_movk_i32 s79, 128\n" \
  "v_mov_b32 v24, 0x44870000\n" \
  "v_mul_f32 v24, s72, v24\n" \
  "s_nop 0\n" \
  "v_readfirstlane_b32 s86, v24\n" \
  "s_lshr_b32 s86, s86, 23\n" \
  "s_addk_i32 s86, 0xff83\n" \
  "s_ashr_i32 s86, s86, 1\n" \
  "s_max_i32 s86, s86, -20\n" \
  "s_min_i32 s86, s86, 100\n" \
  "s_mov_b32 s82, s86\n" \
  "s_lshr_b32 s83, s72, 23\n" \
  "s_sub_i32 s83, s83, 242\n" \
  "s_lshl_b32 s88, s73, 10\n" \
  "v_add_u32 v24, s88, v1\n" \
  "ds_read_b64 v[32:33], v24 offset:0\n" \
  "ds_read_b64 v[36:37], v24 offset:8\n" \
  "s_waitcnt lgkmcnt(0)\n" \
  "v_mov_b32 v11, 0\n" \
  "v_dot2c_f32_bf16 v11, v32, v32\n" \
  "v_dot2c_f32_bf16 v11, v33, v33\n" \
  "v_dot2c_f32_bf16 v11, v36, v36\n" \
  "v_dot2c_f32_bf16 v11, v37, v37\n" \
  "s_nop 1\n" \
  "s_nop 0\n" \
  "v_cmpx_lt_f32 vcc, 2.0, v11\n" \
  "ds_max_u32 v59, v11\n" \
  "s_mov_b64 exec, -1\n" \
  "s_cmp_lg_u32 s61, 0\n" \
  "s_movk_i32 s76, -8000\n" \
  "s_cmovk_i32 s76, 9152\n" \
  "s_movk_i32 s77, 90\n" \
  "s_cmovk_i32 s77, 90\n" \
  "s_cselect_b32 s72, -1, 55\n" \
  ".LCV_TOP_%=:\n" \
  "s_waitcnt lgkmcnt(0)\n" \
  "s_barrier\n" \
  "s_setprio 3\n" \
  "ds_read_b128 v[24:27], v1 offset:0\n" \
  "ds_read_b128 v[28:31], v1 offset:1024\n" \
  "ds_read_b128 v[32:35], v1 offset:2048\n" \
  "ds_read_b128 v[36:39], v1 offset:3072\n" \
  "ds_read_b128 v[40:43], v1 offset:4096\n" \
  "ds_read_b128 v[44:47], v1 offset:5120\n" \
  "ds_read_b128 v[48:51], v1 offset:6144\n" \
  "ds_read_b128 v[52:55], v1 offset:7168\n" \
  "s_memrealtime s[38:39]\n" \
  "buffer_load_dword v58, off, s[44:47], s68 sc1\n" \
  "s_waitcnt lgkmcnt(7)\n" \
  "v_smfmac_f32_16x16x64_bf16 v[12:15], v[24:27], v[64:71], v0\n" \
  "s_waitcnt lgkmcnt(6)\n" \
  "v_smfmac_f32_16x16x64_bf16 v[12:15], v[28:31], v[72:79], v0\n" \
  "s_waitcnt lgkmcnt(5)\n" \
  "v_smfmac_f32_16x16x64_bf16 v[12:15], v[32:35], v[80:87], v0\n" \
  "s_waitcnt lgkmcnt(4)\n" \
  "v_smfmac_f32_16x16x64_bf16 v[12:15], v[36:39], v[88:95], v0\n" \
  "s_waitcnt lgkmcnt(3)\n" \
  "v_smfmac_f32_16x16x64_bf16 v[12:15], v[40:43], v[96:103], v0\n" \
  "s_waitcnt lgkmcnt(2)\n" \
  "v_smfmac_f32_16x16x64_bf16 v[12:15], v[44:47], v[104:111], v0\n" \
  "s_waitcnt lgkmcnt(1)\n" \
  "v_smfmac_f32_16x16x64_bf16 v[12:15], v[48:51], v[112:119], v0\n" \
  "s_waitcnt lgkmcnt(0)\n" \
  "v_smfmac_f32_16x16x64_bf16 v[12:15], v[52:55], v[120:127], v0\n" \
  "ds_read_b32 v11, v1 offset:26688\n" \
  "s_mov_b32 s61, s38\n" \
  "s_add_u32 s38, s38, s79\n" \
  "v_or_b32 v57, 1, s38\n" \
  "v_add_u32 v56, s67, v10\n" \
  "s_add_u32 s39, s67, s76\n" \
  "s_add_u32 s38, s38, s77\n" \
  "s_bitset1_b32 s38, 0\n" \
  "v_writelane_b32 v56, s39, 1\n" \
  "v_writelane_b32 v57, s38, 1\n" \
  "s_sub_u32 s39, s71, s72\n" \
  "s_cmp_lt_u32 s39, 9\n" \
  "s_cselect_b64 exec, 19, 3\n" \
  "buffer_store_dword v57, v56, s[0:3], 0 offen sc1\n" \
  "s_mov_b64 exec, -1\n" \
  "v_permlane32_swap_b32 v12, v14\n" \
  "v_permlane32_swap_b32 v13, v15\n" \
  "v_pk_add_f32 v[12:13], v[12:13], v[14:15]\n" \
  "s_nop 0\n" \
  "v_cvt_pk_bf16_f32 v12, v12, v13\n" \
  "ds_write_b16 v4, v12\n" \
  "ds_write_b16_d16_hi v4, v12 offset:288\n" \
  "s_setprio 0\n" \
  "v_mov_b64 v[12:13], 0\n" \
  "v_mov_b64 v[14:15], 0\n" \
  "s_waitcnt lgkmcnt(2)\n" \
  "v_readfirstlane_b32 s86, v11\n" \
  "s_lshr_b32 s86, s86, 23\n" \
  "s_add_i32 s86, s86, s83\n" \
  "s_ashr_i32 s86, s86, 1\n" \
  "s_max_i32 s86, s86, s82\n" \
  "s_min_i32 s86, s86, 100\n" \
  "s_lshl_b32 s86, s86, 23\n" \
  "s_sub_i32 s84, 0x4a800000, s86\n" \
  "s_cmp_eq_u32 s73, 5\n" \
  "s_waitcnt lgkmcnt(0)\n" \
  "s_barrier\n" \
  "ds_read_b128 v[60:63], v6 offset:0\n" \
  "v_smfmac_f32_16x16x64_bf16 v[16:19], v[24:27], v[128:135], v0\n" \
  "v_smfmac_f32_16x16x64_bf16 v[20:23], v[24:27], v[192:199], v0\n" \
  "v_smfmac_f32_16x16x64_bf16 v[16:19], v[28:31], v[136:143], v0\n" \
  "v_smfmac_f32_16x16x64_bf16 v[20:23], v[28:31], v[200:207], v0\n" \
  "v_smfmac_f32_16x16x64_bf16 v[16:19], v[32:35], v[144:151], v0\n" \
  "s_waitcnt lgkmcnt(0)\n" \
  "v_max_u32 v60, 1, v60\n" \
  "v_smfmac_f32_16x16x64_bf16 v[20:23], v[32:35], v[208:215], v0\n" \
  "v_max_u32 v61, 1, v61\n" \
  "v_smfmac_f32_16x16x64_bf16 v[16:19], v[36:39], v[152:159], v0\n" \
  "v_max_u32 v62, 1, v62\n" \
  "v_smfmac_f32_16x16x64_bf16 v[20:23], v[36:39], v[216:223], v0\n" \
  "v_max_u32 v63, 1, v63\n" \
  "v_smfmac_f32_16x16x64_bf16 v[16:19], v[40:43], v[160:167], v0\n" \
  "buffer_store_dwordx4 v[60:63], v7, s[48:51], s69 offen sc1\n" \
  "v_smfmac_f32_16x16x64_bf16 v[20:23], v[40:43], v[224:231], v0\n" \
  "v_smfmac_f32_16x16x64_bf16 v[16:19], v[44:47], v[168:175], v0\n" \
  "s_cbranch_scc0 .LCV_NMK_%=\n" \
  "s_mov_b32 m0, s78\n" \
  "s_nop 0\n" \
  "buffer_load_dword v2, s[0:3], s66 offen sc1 lds\n" \
  ".LCV_NMK_%=:\n" \
  "s_cmp_eq_u32 s71, 0\n" \
  "v_smfmac_f32_16x16x64_bf16 v[20:23], v[44:47], v[232:239], v0\n" \
  "v_smfmac_f32_16x16x64_bf16 v[16:19], v[48:51], v[176:183], v0\n" \
  "v_smfmac_f32_16x16x64_bf16 v[20:23], v[48:51], v[240:247], v0\n" \
  "v_smfmac_f32_16x16x64_bf16 v[16:19], v[52:55], v[184:191], v0\n" \
  "v_smfmac_f32_16x16x64_bf16 v[20:23], v[52:55], v[248:255], v0\n" \
  "ds_read_b128 v[24:27], v3 offset:0\n" \
  "ds_read_b128 v[28:31], v3 offset:1024\n" \
  "ds_read_b128 v[32:35], v3 offset:2048\n" \
  "ds_read_b128 v[36:39], v3 offset:3072\n" \
  "ds_read_b128 v[40:43], v3 offset:8192\n" \
  "ds_read_b128 v[44:47], v3 offset:9216\n" \
  "s_nop 0\n" \
  "v_permlane32_swap_b32 v16, v18\n" \
  "v_permlane32_swap_b32 v17, v19\n" \
  "v_permlane32_swap_b32 v20, v22\n" \
  "v_permlane32_swap_b32 v21, v23\n" \
  "v_pk_add_f32 v[16:17], v[16:17], v[18:19]\n" \
  "v_pk_add_f32 v[20:21], v[20:21], v[22:23]\n" \
  "v_cvt_pk_bf16_f32 v16, v16, v17\n" \
  "v_cvt_pk_bf16_f32 v17, v20, v21\n" \
  "ds_write_b64 v5, v[16:17]\n" \
  "v_max_u32 v20, 1, v16\n" \
  "v_max_u32 v21, 1, v17\n" \
  "s_nop 1\n" \
  "v_permlane32_swap_b32 v20, v21\n" \
  "buffer_store_dwordx2 v[20:21], v8, s[52:55], s70 offen sc1\n" \
  "s_cbranch_scc1 .LCV_LAST_%=\n" \
  "s_waitcnt vmcnt(3)\n" \
  "v_cmp_eq_u32 vcc, 0, v58\n" \
  "s_cmp_eq_u64 vcc, 0\n" \
  "s_cselect_b32 s90, 1, 0\n" \
  "s_waitcnt lgkmcnt(0)\n" \
  "s_barrier\n" \
  "s_cbranch_scc0 .LCV_NOTOK_%=\n" \
  "s_mov_b32 m0, s99\n" \
  "s_add_u32 s88, s99, 8192\n" \
  "global_load_lds_dwordx4 v10, s[62:63] sc1\n" \
  "global_load_lds_dwordx4 v10, s[62:63] offset:1024 sc1\n" \
  "s_mov_b32 m0, s88\n" \
  "s_nop 0\n" \
  "global_load_lds_dwordx4 v10, s[64:65] sc1\n" \
  "global_load_lds_dwordx4 v10, s[64:65] offset:1024 sc1\n" \
  ".LCV_NOTOK_%=:\n" \
  "s_add_u32 s67, s67, 1024\n" \
  "ds_write_b32 v59, v12\n" \
  "ds_read_b128 v[48:51], v3 offset:10240\n" \
  "ds_read_b128 v[52:55], v3 offset:11264\n" \
  "v_mov_b64 v[18:19], 0\n" \
  "v_mov_b64 v[20:21], 0\n" \
  "v_mov_b64 v[22:23], 0\n" \
  "v_mov_b64 v[16:17], 0\n" \
  "ds_read_b128 v[56:59], v3 offset:4096\n" \
  "ds_read_b128 v[60:63], v3 offset:5120\n" \
  "s_cmp_eq_u32 s90, 1\n" \
  "s_cbranch_scc1 .LCV_NM0_%=\n" \
  "s_mov_b32 m0, s94\n" \
  "s_mov_b64 exec, 1\n" \
  "buffer_load_dword off, s[44:47], s68 sc1 lds\n" \
  "s_mov_b64 exec, -1\n" \
  ".LCV_NM0_%=:\n" \
  "s_setprio 2\n" \
  "s_waitcnt lgkmcnt(0)\n" \
  "s_barrier\n" \
  "ds_read_b128 v[4:7], v1 offset:8192\n" \
  "ds_read_b128 v[8:11], v1 offset:9216\n" \
  "s_waitcnt lgkmcnt(1)\n" \
  "v_smfmac_f32_16x16x64_bf16 v[12:15], v[4:7], v[24:31], v0\n" \
  "ds_read_b128 v[24:27], v3 offset:6144\n" \
  "ds_read_b128 v[28:31], v3 offset:7168\n" \
  "v_smfmac_f32_16x16x64_bf16 v[16:19], v[4:7], v[32:39], v0\n" \
  "ds_read_b128 v[32:35], v3 offset:12288\n" \
  "ds_read_b128 v[36:39], v3 offset:13312\n" \
  "s_waitcnt lgkmcnt(4)\n" \
  "v_smfmac_f32_16x16x64_bf16 v[12:15], v[8:11], v[40:47], v0\n" \
  "v_smfmac_f32_16x16x64_bf16 v[16:19], v[8:11], v[48:55], v0\n" \
  "v_smfmac_f32_16x16x64_bf16 v[20:23], v[4:7], v[56:63], v0\n" \
  "ds_read_b128 v[56:59], v3 offset:14336\n" \
  "ds_read_b128 v[60:63], v3 offset:15360\n" \
  "v_mov_b64 v[40:41], 0\n" \
  "v_mov_b64 v[42:43], 0\n" \
  "s_nop 2\n" \
  "v_permlane32_swap_b32 v12, v17\n" \
  "v_permlane32_swap_b32 v13, v16\n" \
  "v_permlane32_swap_b32 v14, v19\n" \
  "v_permlane32_swap_b32 v15, v18\n" \
  "v_pk_add_f32 v[12:13], v[12:13], v[16:17] op_sel:[0,1] op_sel_hi:[1,0]\n" \
  "v_pk_add_f32 v[14:15], v[14:15], v[18:19] op_sel:[0,1] op_sel_hi:[1,0]\n" \
  "v_pk_fma_f32 v[12:13], v[12:13], s[84:85], s[84:85] op_sel:[0,0,1] op_sel_hi:[1,0,1]\n" \
  "v_pk_fma_f32 v[14:15], v[14:15], s[84:85], s[84:85] op_sel:[0,0,1] op_sel_hi:[1,0,1]\n" \
  "v_lshl_add_u32 v12, v12, 6, s91\n" \
  "v_lshl_add_u32 v13, v13, 2, s75\n" \
  "global_atomic_add_x2 v2, v[12:13], s[58:59] offset:0\n" \
  "v_lshl_add_u32 v14, v14, 6, s91\n" \
  "v_lshl_add_u32 v15, v15, 2, s75\n" \
  "global_atomic_add_x2 v2, v[14:15], s[58:59] offset:2048\n" \
  "s_waitcnt lgkmcnt(2)\n" \
  "v_smfmac_f32_16x16x64_bf16 v[20:23], v[8:11], v[32:39], v0\n" \
  "v_smfmac_f32_16x16x64_bf16 v[40:43], v[4:7], v[24:31], v0\n" \
  "s_waitcnt lgkmcnt(0)\n" \
  "v_smfmac_f32_16x16x64_bf16 v[40:43], v[8:11], v[56:63], v0\n" \
  "s_nop 7\n" \
  "v_permlane32_swap_b32 v20, v41\n" \
  "v_permlane32_swap_b32 v21, v40\n" \
  "v_permlane32_swap_b32 v22, v43\n" \
  "v_permlane32_swap_b32 v23, v42\n" \
  "v_pk_add_f32 v[20:21], v[20:21], v[40:41] op_sel:[0,1] op_sel_hi:[1,0]\n" \
  "v_pk_add_f32 v[22:23], v[22:23], v[42:43] op_sel:[0,1] op_sel_hi:[1,0]\n" \
  "v_pk_fma_f32 v[20:21], v[20:21], s[84:85], s[84:85] op_sel:[0,0,1] op_sel_hi:[1,0,1]\n" \
  "v_pk_fma_f32 v[22:23], v[22:23], s[84:85], s[84:85] op_sel:[0,0,1] op_sel_hi:[1,0,1]\n" \
  "v_lshl_add_u32 v20, v20, 6, s91\n" \
  "v_lshl_add_u32 v21, v21, 2, s75\n" \
  "global_atomic_add_x2 v2, v[20:21], s[58:59] offset:3072\n" \
  "v_lshl_add_u32 v22, v22, 6, s91\n" \
  "v_lshl_add_u32 v23, v23, 2, s75\n" \
  "global_atomic_add_x2 v2, v[22:23], s[58:59] offset:1024\n" \
  "s_setprio 0\n" \
  "s_memrealtime s[38:39]\n" \
  "v_mbcnt_lo_u32_b32 v48, -1, 0\n" \
  "v_mbcnt_hi_u32_b32 v48, -1, v48\n" \
  "s_add_u32 s88, s74, 157760\n" \
  "v_mov_b32 v59, s88\n" \
  "v_lshl_add_u32 v52, v48, 2, v59\n" \
  "ds_read_b32 v52, v52 offset:192\n" \
  "v_lshrrev_b32 v49, 3, v48\n" \
  "v_and_b32 v50, 7, v48\n" \
  "v_lshlrev_b32 v50, 4, v50\n" \
  "v_lshl_or_b32 v44, v49, 8, v50\n" \
  "s_mov_b32 s87, 0x40000\n" \
  "v_lshlrev_b32 v10, 4, v48\n" \
  "s_cmp_eq_u32 s90, 1\n" \
  "s_cbranch_scc1 .LCV_HAVE_%=\n" \
  "v_mov_b32 v58, s94\n" \
  "s_waitcnt vmcnt(4)\n" \
  "ds_read_b32 v58, v58\n" \
  "s_waitcnt lgkmcnt(0)\n" \
  "v_cmp_eq_u32 vcc, 0, v58\n" \
  "s_cmp_eq_u64 vcc, 0\n" \
  "s_cbranch_scc1 .LCV_GOT_%=\n" \
  "s_branch .LCV_ASK_%=\n" \
  ".LCV_LATE_%=:\n" \
  "v_cmp_eq_u32 vcc, 0, v58\n" \
  "s_cmp_eq_u64 vcc, 0\n" \
  "s_cbranch_scc1 .LCV_GOT_%=\n" \
  "s_sleep 12\n" \
  ".LCV_ASK_%=:\n" \
  "buffer_load_dword v58, off, s[44:47], s68 sc1\n" \
  "s_sub_u32 s87, s87, 1\n" \
  "s_cbranch_scc1 .LCV_END_%=\n" \
  "s_waitcnt vmcnt(0)\n" \
  "s_branch .LCV_LATE_%=\n" \
  ".LCV_GOT_%=:\n" \
  "s_mov_b32 m0, s99\n" \
  "s_add_u32 s88, s99, 8192\n" \
  "global_load_lds_dwordx4 v10, s[62:63] sc1\n" \
  "global_load_lds_dwordx4 v10, s[62:63] offset:1024 sc1\n" \
  "s_mov_b32 m0, s88\n" \
  "s_nop 0\n" \
  "global_load_lds_dwordx4 v10, s[64:65] sc1\n" \
  "global_load_lds_dwordx4 v10, s[64:65] offset:1024 sc1\n" \
  ".LCV_HAVE_%=:\n" \
  "s_waitcnt lgkmcnt(0)\n" \
  "s_sub_u32 s61, s38, s61\n" \
  "s_add_u32 s79, s79, 2\n" \
  "s_min_u32 s79, s79, s61\n" \
  "s_sub_u32 s39, s38, 150\n" \
  "v_subrev_u32 v52, s39, v52\n" \
  "v_cmp_lt_u32 vcc, 0x1000, v52\n" \
  "s_cmp_eq_u64 vcc, 0\n" \
  "s_cbranch_scc1 .LCV_OKM_%=\n" \
  ".LCV_AGN_%=:\n" \
  "buffer_load_dword v52, v2, s[0:3], s66 offen sc1\n" \
  "s_sub_u32 s87, s87, 1\n" \
  "s_cbranch_scc1 .LCV_END_%=\n" \
  "s_waitcnt vmcnt(0)\n" \
  "v_cmp_eq_u32 vcc, 0, v52\n" \
  "v_subrev_u32 v52, s39, v52\n" \
  "s_cmp_eq_u64 vcc, 0\n" \
  "s_cbranch_scc1 .LCV_OKM_%=\n" \
  "s_sleep 4\n" \
  "s_branch .LCV_AGN_%=\n" \
  ".LCV_OKM_%=:\n" \
  "s_nop 1\n" \
  "v_max_i32_dpp v52, v52, v52 row_ror:1 row_mask:0xf bank_mask:0xf\n" \
  "s_nop 1\n" \
  "v_max_i32_dpp v52, v52, v52 row_ror:2 row_mask:0xf bank_mask:0xf\n" \
  "s_nop 1\n" \
  "v_max_i32_dpp v52, v52, v52 row_ror:4 row_mask:0xf bank_mask:0xf\n" \
  "s_movk_i32 vcc_lo, 150\n" \
  "v_readlane_b32 s88, v52, 0\n" \
  "s_max_i32 vcc_lo, vcc_lo, s88\n" \
  "v_readlane_b32 s88, v52, 16\n" \
  "s_max_i32 vcc_lo, vcc_lo, s88\n" \
  "v_readlane_b32 s88, v52, 32\n" \
  "s_max_i32 vcc_lo, vcc_lo, s88\n" \
  "v_readlane_b32 s88, v52, 48\n" \
  "s_max_i32 vcc_lo, vcc_lo, s88\n" \
  "s_add_u32 s66, s66, 1024\n" \
  "s_add_u32 vcc_lo, vcc_lo, s39\n" \
  "s_add_u32 vcc_lo, vcc_lo, 30\n" \
  ".LCV_TW_%=:\n" \
  "s_memrealtime s[38:39]\n" \
  "s_waitcnt lgkmcnt(0)\n" \
  "s_sub_u32 s38, s38, vcc_lo\n" \
  "s_cmp_lt_i32 s38, 0\n" \
  "s_cbranch_scc1 .LCV_TW_%=\n" \
  "buffer_load_dwordx4 v[24:27], v44, s[40:43], s60 offen sc1\n" \
  "buffer_load_dwordx4 v[28:31], v44, s[40:43], s60 offen offset:2048 sc1\n" \
  "s_add_u32 s62, s62, 0x80000\n" \
  "s_addc_u32 s63, s63, 0\n" \
  "s_add_u32 s64, s64, 0x40000\n" \
  "s_addc_u32 s65, s65, 0\n" \
  "s_add_u32 s68, s68, 0x1000\n" \
  "s_add_u32 s69, s69, 0x10000\n" \
  "s_add_u32 s70, s70, 0x10000\n" \
  "s_sub_u32 s88, s73, 4\n" \
  "v_and_b32 v51, 15, v48\n" \
  "v_lshl_add_u32 v4, v51, 1, s97\n" \
  "v_bfe_u32 v49, v48, 4, 1\n" \
  "v_mad_u32_u24 v4, v49, s92, v4\n" \
  "v_lshrrev_b32 v50, 5, v48\n" \
  "v_mad_u32_u24 v4, v50, s93, v4\n" \
  "v_lshlrev_b32 v8, 4, v49\n" \
  "v_lshl_add_u32 v8, v51, 5, v8\n" \
  "v_lshl_add_u32 v8, v50, 3, v8\n" \
  "s_lshl_b32 s89, s88, 8\n" \
  "s_add_u32 s89, s89, s74\n" \
  "s_add_u32 s89, s89, 139264\n" \
  "v_and_b32 v49, 31, v48\n" \
  "v_lshl_add_u32 v5, v49, 3, s89\n" \
  "v_lshl_add_u32 v5, v50, 10, v5\n" \
  "v_lshrrev_b32 v49, 2, v51\n" \
  "v_and_b32 v50, 5, v51\n" \
  "v_and_or_b32 v49, v49, 2, v50\n" \
  "s_lshl_b32 s89, s88, 6\n" \
  "s_add_u32 s89, s89, s74\n" \
  "s_add_u32 s89, s89, 160992\n" \
  "v_and_b32 v50, 48, v48\n" \
  "v_add_u32 v6, s89, v50\n" \
  "v_mad_u32_u24 v6, v49, s96, v6\n" \
  "v_lshlrev_b32 v7, 3, v50\n" \
  "v_lshl_add_u32 v7, v49, 4, v7\n" \
  "v_and_b32 v49, 2, v51\n" \
  "v_lshl_add_u32 v7, v49, 29, v7\n" \
  "v_mov_b64 v[12:13], 0\n" \
  "v_mov_b64 v[14:15], 0\n" \
  "v_mov_b64 v[16:17], 0\n" \
  "v_mov_b64 v[18:19], 0\n" \
  "v_mov_b64 v[20:21], 0\n" \
  "v_mov_b64 v[22:23], 0\n" \
  "v_and_b32 v50, 3, v48\n" \
  "v_lshl_add_u32 v40, v50, 2, s98\n" \
  "v_lshrrev_b32 v50, 4, v48\n" \
  "v_mad_u32_u24 v40, v50, s95, v40\n" \
  "v_bfe_u32 v49, v48, 3, 1\n" \
  "v_lshl_add_u32 v40, v49, 6, v40\n" \
  "v_bfe_u32 v49, v48, 2, 1\n" \
  "v_lshl_add_u32 v40, v49, 7, v40\n" \
  "v_xor_b32 v41, 16, v40\n" \
  "v_xor_b32 v42, 32, v40\n" \
  "v_xor_b32 v43, 48, v40\n" \
  "s_add_i32 s80, s86, 0x32800000\n" \
  "s_add_i32 s81, s86, 0x33800000\n" \
  "s_add_i32 s88, s86, 0xb4000000\n" \
  "v_mov_b32 v46, s88\n" \
  "v_mov_b32 v47, 0\n" \
  "v_mov_b32 v11, 0\n" \
  "s_mov_b32 s89, s60\n" \
  "s_xor_b32 s60, s60, 0x80\n" \
  "s_bitcmp0_b32 s60, 7\n" \
  "s_cselect_b32 s88, 0x8000, 0\n" \
  "s_add_u32 s60, s60, s88\n" \
  "s_and_b32 s60, s60, 0xffff\n" \
  "s_add_u32 s58, s56, s60\n" \
  "s_addc_u32 s59, s57, 0\n" \
  "s_sub_u32 s71, s71, 1\n" \
  ".LCV_SEE_%=:\n" \
  "s_waitcnt vmcnt(1)\n" \
  "v_bitop3_b32 v45, v24, v26, 32 bitop3:0x80\n" \
  "v_alignbit_b32 v32, v25, v24, 2\n" \
  "v_alignbit_b32 v34, v27, v26, 2\n" \
  "s_waitcnt vmcnt(0)\n" \
  "v_bitop3_b32 v45, v45, v28, v30 bitop3:0x80\n" \
  "v_cmp_eq_u32 vcc, 0, v45\n" \
  "v_cvt_f32_i32 v32, v32\n" \
  "v_cvt_f32_i32 v33, v25\n" \
  "s_cmp_eq_u64 vcc, 0\n" \
  "s_cbranch_scc0 .LCV_POLL_%=\n" \
  "v_cvt_f32_i32 v34, v34\n" \
  "v_cvt_f32_i32 v35, v27\n" \
  "v_pk_fma_f32 v[32:33], v[32:33], s[80:81], v[46:47]\n" \
  "v_alignbit_b32 v36, v29, v28, 2\n" \
  "v_pk_fma_f32 v[34:35], v[34:35], s[80:81], v[46:47]\n" \
  "v_alignbit_b32 v38, v31, v30, 2\n" \
  "v_cvt_pk_bf16_f32 v32, v32, v34\n" \
  "v_cvt_pk_bf16_f32 v33, v33, v35\n" \
  "v_cvt_f32_i32 v36, v36\n" \
  "ds_write_b32 v40, v32\n" \
  "v_cvt_f32_i32 v37, v29\n" \
  "ds_write_b32 v41, v33\n" \
  "v_cvt_f32_i32 v38, v38\n" \
  "v_cvt_f32_i32 v39, v31\n" \
  "v_pk_fma_f32 v[36:37], v[36:37], s[80:81], v[46:47]\n" \
  "v_pk_fma_f32 v[38:39], v[38:39], s[80:81], v[46:47]\n" \
  "v_dot2c_f32_bf16 v11, v32, v32\n" \
  "v_cvt_pk_bf16_f32 v36, v36, v38\n" \
  "v_cvt_pk_bf16_f32 v37, v37, v39\n" \
  "ds_write_b32 v42, v36\n" \
  "ds_write_b32 v43, v37\n" \
  "v_dot2c_f32_bf16 v11, v33, v33\n" \
  "v_dot2c_f32_bf16 v11, v36, v36\n" \
  "v_dot2c_f32_bf16 v11, v37, v37\n" \
  "s_nop 1\n" \
  "s_nop 0\n" \
  "v_cmpx_lt_f32 vcc, 2.0, v11\n" \
  "ds_max_u32 v59, v11\n" \
  "s_mov_b64 exec, -1\n" \
  "s_branch .LCV_TOP_%=\n" \
  ".LCV_POLL_%=:\n" \
  "buffer_load_dwordx4 v[24:27], v44, s[40:43], s89 offen sc1\n" \
  "buffer_load_dwordx4 v[28:31], v44, s[40:43], s89 offen offset:2048 sc1\n" \
  "s_sub_u32 s87, s87, 1\n" \
  "s_cbranch_scc1 .LCV_END_%=\n" \
  "s_branch .LCV_SEE_%=\n" \
  ".LCV_LAST_%=:\n" \
  "s_waitcnt vmcnt(0)\n" \
  ".LCV_END_%=:\n" \
  "s_endpgm\n"
__device__ __forceinline__ v4u mk_srd(const void* p, const unsigned bytes) {
  const unsigned long long a = (unsigned long long)p;
  return v4u{(unsigned)a, (unsigned)(a >> 32) & 0xffffu, bytes, 0x00020000u};
}
template <int R>
__device__ __forceinline__ void pin8(v8u& x) {
#define PIN_(a, b) if constexpr (R == a) asm volatile("" : "+{v[" #a ":" #b "]}"(x));
  PIN_(64, 71)  PIN_(72, 79)  PIN_(80, 87)  PIN_(88, 95)  PIN_(96, 103)  PIN_(104, 111)  PIN_(112, 119)  PIN_(120, 127)
  PIN_(128, 135)  PIN_(136, 143)  PIN_(144, 151)  PIN_(152, 159)  PIN_(160, 167)  PIN_(168, 175)  PIN_(176, 183)  PIN_(184, 191)
  PIN_(192, 199)  PIN_(200, 207)  PIN_(208, 215)  PIN_(216, 223)  PIN_(224, 231)  PIN_(232, 239)  PIN_(240, 247)  PIN_(248, 255)
#undef PIN_
}
template <int R>
__device__ __forceinline__ void pin4(v4u& x) {
#define PIN_(a, b) if constexpr (R == a) asm volatile("" : "+{v[" #a ":" #b "]}"(x));
  PIN_(32, 35)  PIN_(36, 39)  PIN_(40, 43)  PIN_(44, 47)  PIN_(48, 51)  PIN_(52, 55)  PIN_(56, 59)  PIN_(60, 63)
  PIN_(96, 99)  PIN_(100, 103)  PIN_(104, 107)  PIN_(108, 111)  PIN_(112, 115)  PIN_(116, 119)  PIN_(120, 123)  PIN_(124, 127)
  PIN_(160, 163)  PIN_(164, 167)  PIN_(168, 171)  PIN_(172, 175)  PIN_(176, 179)  PIN_(180, 183)  PIN_(184, 187)  PIN_(188, 191)
#undef PIN_
}
template <bool ATT>
__device__ __forceinline__ void role_chain_t(const MegaArgs& A, char* smem, const int L, const int h, const int rep) {
  u16* swo = reinterpret_cast<u16*>(smem);
  const int w = A.wv;
  char* ws = A.ws;
  unsigned* FL = (unsigned*)(ws + OFF_FL);
  unsigned* TMO = FL + FL_TMO;
#define LANE_IDS()                         \
  const int l = lane_id();                 \
  const int c = l & 15, g = l >> 4;        \
  (void)c; (void)g;
  float* sgf = reinterpret_cast<float*>(smem + C_X);
  unsigned* FQ = FL + FL_FQ + L * 64 * 32 * 32;
  const int wb = (w + ROT_H) & 7;
  v8u w0[8], w1[8], w2[8];
  u64 tag;
  {
    LANE_IDS();
    const unsigned sb = lds_base(smem);
    const int q = (l >> 2) & 3, p = l & 3;
    const int f = (q & 1) | ((g & 1) << 1) | ((q >> 1) << 2);
    const unsigned ra = sb + (16 * (g >> 1) + 4 * (g & 1) + (q & 1) + 8 * (q >> 1)) * 256 + (w ^ f) * 32 + p * 8;
#define W_READ(N, hf, X)                                                       \
  {                                                                           \
    _Pragma("unroll") for (int t = 0; t < 4; ++t) {                           \
      const unsigned a_ = (ra ^ (X)) + (hf) * 8192 + t * 32768;               \
      const v2u r0 = trd(a_), r1 = trd(a_ + 512), r2 = trd(a_ + 16384), r3 = trd(a_ + 16384 + 512); \
      w##N[4 * (hf) + t] = v8u{r0.x, r0.y, r1.x, r1.y, r2.x, r2.y, r3.x, r3.y}; \
    }                                                                         \
    pin8<64 * N + 64 + 32 * hf>(w##N[4 * hf]);                                \
    pin8<64 * N + 72 + 32 * hf>(w##N[4 * hf + 1]);                            \
    pin8<64 * N + 80 + 32 * hf>(w##N[4 * hf + 2]);                            \
    pin8<64 * N + 88 + 32 * hf>(w##N[4 * hf + 3]);                            \
  }
#define W_STAGE(M, hf, WAIT, NEXT)                                             \
  {                                                                           \
    WAIT;                                                                     \
    RAW_BAR();                                                                \
    if ((M) == 0 && ATT) { W_READ(0, hf, 0); W_READ(2, hf, 128); }            \
    if ((M) == 1 && ATT) { W_READ(1, hf, 0); }                                \
    if ((M) == 1 && !ATT) { W_READ(0, hf, 0); }                               \
    if ((M) == 2 && !ATT) { W_READ(1, hf, 128); W_READ(2, hf, 0); }           \
    if ((M) == 3) {                                                           \
      _Pragma("unroll") for (int i = 0; i < 8; ++i) {                         \
        const unsigned a_ = rb + (hf) * 8192 + (i & 1) * 65536 + ((4 * wb + (i >> 1)) ^ f) * 32; \
        const v2u r0 = trd(a_), r1 = trd(a_ + 2048);                          \
        fr[hf][i] = v4u{r0.x, r0.y, r1.x, r1.y};                              \
      }                                                                       \
      constexpr int R_ = hf ? 32 : ATT ? 160 : 96;                            \
      pin4<R_>(fr[hf][0]); pin4<R_ + 4>(fr[hf][1]); pin4<R_ + 8>(fr[hf][2]); pin4<R_ + 12>(fr[hf][3]); \
      pin4<R_ + 16>(fr[hf][4]); pin4<R_ + 20>(fr[hf][5]); pin4<R_ + 24>(fr[hf][6]); pin4<R_ + 28>(fr[hf][7]); \
    }                                                                         \
    RAW_BAR();                                                                \
    NEXT;                                                                     \
  }
#define WO_ISSUE(hf)                                                          \
  {                                                                           \
    const char* base_ = (const char*)A.Wo + (size_t)(h * DH + 64 * (hf) + 8 * w) * (DM * 2); \
    _Pragma("unroll") for (int i = 0; i < 8; ++i) {                           \
      const int fd = (i & 1) | (((i >> 2) & 1) << 1) | ((w & 1) << 2);        \
      glds16(base_, (unsigned)(i * 1024 + ((l ^ (fd << 1)) << 4)), sb + w * 16384 + (hf) * 8192 + i * 1024); \
    }                                                                         \
  }
    const unsigned rb = sb + (2 * (g >> 1) + (q >> 1)) * 16384 + (4 * (g & 1) + (q & 1)) * 1024 + p * 8;
    v4u fr[2][8];
#define WO_PUT(hf) \
  _Pragma("unroll") for (int i = 0; i < 8; ++i) *reinterpret_cast<v4u*>(swo + w * 8192 + (hf) * 4096 + i * 512 + l * 8) = fr[hf][i];
    __builtin_amdgcn_s_waitcnt(0x0f70);
    glds16(A.mem0, (unsigned)((c & 7) * DM + 64 * w + 16 * g + 8 * (c >> 3)) * 2, sb + C_SM + w * 1024);
    wqkv_issue<true>(A.Wv, 0, h, w, l, sb);
    wqkv_issue<true>(A.Wv, 1, h, w, l, sb);
    tag = call_tag(A);
    W_STAGE(2, 0, WAIT_VM(8), WO_ISSUE(0));
    W_STAGE(2, 1, WAIT_VM(8), WO_ISSUE(1));
    SST(4);
    if (!ATT) {
      float f2 = 0.f;
      asm volatile("" : "+v"(f2));
#pragma unroll
      for (int t = 0; t < 8; ++t)
#pragma unroll
        for (int e = 0; e < 8; ++e) f2 = dot2sq(w2[t][e], dot2sq(w1[t][e], f2));
      f2 = wave_sum_dpp(f2);
      if (l == 0) { sgf[w] = f2; sgf[16] = 0.f; }
    }
    W_STAGE(3, 0, WAIT_VM(8), wqkv_issue<true>(A.Wq, 0, h, w, l, sb));
    W_STAGE(3, 1, WAIT_VM(8), wqkv_issue<true>(A.Wq, 1, h, w, l, sb));
    SST(5);
    {
      float cn[4] = {0.f, 0.f, 0.f, 0.f};
      asm volatile("" : "+v"(cn[0]), "+v"(cn[1]), "+v"(cn[2]), "+v"(cn[3]));
#pragma unroll
      for (int e = 0; e < 64; ++e) cn[e & 3] = dot2sq(fr[e >> 5][2 * (e & 3) + ((e >> 4) & 1)][(e >> 2) & 3], cn[e & 3]);
      const float m2 = wave_max_dpp(swap_add32(swap_add16(cn[0], cn[1]), swap_add16(cn[2], cn[3])));
      if (l == 0) sgf[8 + w] = m2;
    }
#define G_VALUE()                                                                                                   \
  if (w == 4 && L == 0) {                                                                                           \
    const f32x4 a = *reinterpret_cast<const f32x4*>(sgf + 4), b = *reinterpret_cast<const f32x4*>(sgf + 8);         \
    const f32x4 d = *reinterpret_cast<const f32x4*>(sgf + 12);                                                      \
    const float m2 = fmaxf(fmaxf(fmaxf(b.x, b.y), fmaxf(b.z, b.w)), fmaxf(fmaxf(d.x, d.y), fmaxf(d.z, d.w)));       \
    if (l < 8) __hip_atomic_store((gu64*)(ws + OFF_G) + l * 32 + h, tagged(tag, __float_as_uint(((a.x + a.y) + (a.z + a.w)) * m2 * 1.01f)), RLX_AGENT); \
  }
    W_STAGE(0, 0, WAIT_VM(8), G_VALUE(); wqkv_issue<true>(A.Wk, 0, h, w, l, sb));
    W_STAGE(0, 1, WAIT_VM(8), if (ATT) {
      wqkv_issue<true>(A.Wk, 1, h, w, l, sb);
      wqkv_issue<true>(A.Wk, 1, h, w + 4, l, sb);
    });
    SST(6);
    W_STAGE(1, 0, if (ATT) WAIT_VM(16); else { ready_wait(A, tag, ready_look(A)); __builtin_amdgcn_s_waitcnt(0x0f70); }, WO_PUT(0));
    if (w == 4) {
      u64 gv;
      for (unsigned spins = 0;; ++spins) {
        gv = __hip_atomic_load((gu64*)(ws + OFF_G) + (blockIdx.x & 7) * 32 + (l & 31), RLX_AGENT);
        if (__all(has_tag(tag, (unsigned)gv, (unsigned)(gv >> 32))) || spins > SPIN_CAP) break;
        __builtin_amdgcn_s_sleep(GSL);
      }
      const float gm = wave_max_dpp(hi16((unsigned)(gv >> 32)));
      if (l == 0) sgf[0] = gm * 1.063f;
    }
    W_STAGE(1, 1, WAIT_VM(0), WO_PUT(1));
    SST(7);
#undef G_VALUE
#undef WO_PUT
#undef W_STAGE
#undef W_READ
#undef WO_ISSUE
  }
  WSTAMP2();
  const unsigned sb = lds_base(smem);
  const char* gK = ws + OFF_QK + L * SZ_QK + ((size_t)h * 16 + 2 * w) * 1024;
  const char* gV = ws + OFF_VF + L * SZ_O + ((size_t)h * 8 + 2 * w - 8) * 1024;
  v8u pv;
  {
    LANE_IDS();
    const unsigned voff = g * 256 + c * 8, wp = sb + w * 16384 + l * 16;
    pv[0] = (c & 8) ? 0xEEEE : 0x4444;
    pv[1] = sb + C_SM + l * 16;
    pv[2] = ATT ? sb + C_SQ + ((c & 7) * QS + 8 * g) * 2 : voff;
    pv[3] = ATT ? sb + C_SK + ((4 * ((c >> 2) & 1) + 2 * (c >> 3) + (c & 1)) * QS + 8 * g) * 2 : wp;
    pv[4] = sb + (c < 8 ? C_SO : C_SQ) + w * 256 + ((c & 7) + 8 * (g >> 1)) * 16 + 8 * (g & 1);
    pv[5] = voff;
    pv[6] = wp;
    pv[7] = sb + C_SO + (l >> 5) * 1024 + w * 256 + (l & 31) * 8;
  }
  if (!ATT) {
    if (!wait_n<4, 1>(FQ + h * 32, 1, TMO)) return;
    const unsigned lo = lane_id() * 16, d = sb + C_KT + (2 * w - 8) * 1024;
    glds16c(gK, lo, d);
    glds16c(gK + 1024, lo, d + 1024);
    glds16c(gV, lo, d + 8192);
    glds16c(gV + 1024, lo, d + 9216);
  }
  const unsigned gw2 = __builtin_amdgcn_readfirstlane(__float_as_uint(sgf[0]));
  DRAIN();
  SST(8);
  const v4u rsA = mk_srd(ws + OFF_ACC + L * SZ_ACC, SZ_ACC);
  const v4u rsF = mk_srd(FQ, 64 * 32 * 32 * 4);
  const v4u rsMK = mk_srd(ws + OFF_MEMK + L * SZ_MEM, SZ_MEM);
  const v4u rsMV = mk_srd(ws + OFF_MEMV + L * SZ_MEM, SZ_MEM);
  const char* pAB = ws + OFF_ACC + L * SZ_ACC + (wb - w) * 4096;
  const char* pGK = gK + NH * 16 * 1024;
  const char* pGV = gV + NH * 8 * 1024;
  const v4u rsT = mk_srd(FL + FL_FK + L * 32, 64 * 1024 - 128);
  unsigned sc[8];
  sc[0] = (32 + h) * 128;
  sc[1] = (h * 4 + w - 4) * 512;
  sc[2] = (h * 4 + w - 4) * 512;
  sc[3] = NSEG - 1;
  sc[4] = gw2;
  sc[5] = w;
  sc[6] = sb;
  sc[7] = h == 0 ? 0xd4000000u - 2u - (1u << 29) : 0xd4000000u;
  const char* dbg = ws;
#define CHAIN_OPS                                                                                                          \
  ::"{v[64:71]}"(w0[0]), "{v[72:79]}"(w0[1]), "{v[80:87]}"(w0[2]), "{v[88:95]}"(w0[3]), "{v[96:103]}"(w0[4]),              \
      "{v[104:111]}"(w0[5]), "{v[112:119]}"(w0[6]), "{v[120:127]}"(w0[7]), "{v[128:135]}"(w1[0]), "{v[136:143]}"(w1[1]),    \
      "{v[144:151]}"(w1[2]), "{v[152:159]}"(w1[3]), "{v[160:167]}"(w1[4]), "{v[168:175]}"(w1[5]), "{v[176:183]}"(w1[6]),    \
      "{v[184:191]}"(w1[7]), "{v[192:199]}"(w2[0]), "{v[200:207]}"(w2[1]), "{v[208:215]}"(w2[2]), "{v[216:223]}"(w2[3]),    \
      "{v[224:231]}"(w2[4]), "{v[232:239]}"(w2[5]), "{v[240:247]}"(w2[6]), "{v[248:255]}"(w2[7]), "{v[0:7]}"(pv),          \
      "{s[40:43]}"(rsA), "{s[44:47]}"(rsF), "{s[48:51]}"(rsMK), "{s[52:55]}"(rsMV), "{s[56:57]}"(pAB), "{s60}"(w * 4096), "{s61}"(ATT ? h * 1024 : L), \
      "{s[62:63]}"(pGK), "{s[64:65]}"(pGV), "{s66}"(0), "{s67}"(w == 4 ? (h >> 3) * 256 + (h & 7) * 8 : 0x40000000), "{s68}"(sc[0]), "{s69}"(sc[1]), "{s70}"(sc[2]),    \
      "{s71}"(sc[3]), "{s72}"(sc[4]), "{s73}"(sc[5]), "{s74}"(sc[6]), "{s75}"(sc[7]), "{s[36:37]}"(dbg),                   \
      "{s[0:3]}"(rsT)                                                                                                      \
      : "memory", "vcc", "scc", "s99", "v63"
  if (ATT) asm volatile("s_mov_b64 exec, -1\ns_nop 4\n" CHAIN_ASM_ATT CHAIN_OPS);
  else asm volatile("s_mov_b64 exec, -1\ns_nop 4\n" CHAIN_ASM_VO CHAIN_OPS);
#undef CHAIN_OPS
#undef LANE_IDS
}

__device__ __forceinline__ void role_chain(const MegaArgs& A, char* smem, const int L, const int h, const int rep) {
  if (A.wv < 4) role_chain_t<true>(A, smem, L, h, rep);
  else role_chain_t<false>(A, smem, L, h, rep);
}

constexpr int AW_SO = 0, AW_TK = 32768, AW_CNT = AW_TK + 98304;
__device__ __forceinline__ unsigned umin3(unsigned a, unsigned b, unsigned c) {
  return __builtin_elementwise_min(__builtin_elementwise_min(a, b), c);
}
#define AW_ASM_X \
  "s_mov_b32 s61, 0\n" \
  "s_mov_b32 s67, 1\n" \
  "s_mov_b32 s68, 0\n" \
  "s_mov_b32 s70, 0\n" \
  "s_mov_b32 s73, 0\n" \
  "s_mov_b32 s71, 0x3e0293ee\n" \
  "s_mov_b32 s72, 0xbe0293ee\n" \
  "s_mov_b32 s65, 49152\n" \
  "s_mov_b32 s66, 16384\n" \
  "v_mov_b32 v38, s58\n" \
  "s_memrealtime s[38:39]\n" \
  "s_waitcnt lgkmcnt(0)\n" \
  "s_mov_b32 s69, s38\n" \
  "s_setprio 3\n" \
  ".LCX_TOP_%=:\n" \
  "s_mov_b32 s62, 0x200000\n" \
  "s_cmp_ge_u32 s73, s67\n" \
  "s_cbranch_scc1 .LCX_TOK_%=\n" \
  "s_cmp_eq_u32 s61, 0\n" \
  "s_cbranch_scc1 .LCX_SPIN_%=\n" \
  "s_waitcnt vmcnt(0)\n" \
  "v_mov_b32 v46, s59\n" \
  "v_mov_b32 v47, 1\n" \
  "s_mov_b64 exec, 1\n" \
  "ds_add_rtn_u32 v46, v46, v47\n" \
  "s_mov_b64 exec, -1\n" \
  "s_xor_b32 s59, s59, 4\n" \
  "s_mov_b32 s61, 0\n" \
  "s_waitcnt lgkmcnt(0)\n" \
  "v_readfirstlane_b32 s63, v46\n" \
  "v_lshlrev_b32 v46, 5, v5\n" \
  "s_and_b32 s63, s63, 7\n" \
  "s_cmp_eq_u32 s63, 7\n" \
  "s_cbranch_scc0 .LCX_W1_%=\n" \
  "s_mov_b64 exec, 15\n" \
  "global_store_dword v46, v47, s[52:53] sc1\n" \
  "s_mov_b64 exec, -1\n" \
  ".LCX_W1_%=:\n" \
  "s_add_u32 s52, s52, 2048\n" \
  "s_addc_u32 s53, s53, 0\n" \
  ".LCX_SPIN_%=:\n" \
  "s_sleep 2\n" \
  "s_memrealtime s[38:39]\n" \
  "ds_read_b32 v37, v38\n" \
  "s_sub_u32 s62, s62, 1\n" \
  "s_cbranch_scc1 .LCX_END_%=\n" \
  "s_waitcnt lgkmcnt(0)\n" \
  "v_readfirstlane_b32 s73, v37\n" \
  "s_add_u32 s38, s38, 80\n" \
  "s_bfe_u32 s64, s73, 0x80008\n" \
  "s_and_b32 s73, s73, 0xff\n" \
  "s_min_u32 s73, s73, s64\n" \
  "s_cmp_ge_u32 s73, s67\n" \
  "s_cbranch_scc0 .LCX_SPIN_%=\n" \
  "s_cmp_eq_u32 s70, 0\n" \
  "s_cbranch_scc1 .LCX_TOK_%=\n" \
  "s_sub_u32 s63, s38, s69\n" \
  "s_max_i32 s63, s63, 0\n" \
  "s_add_u32 s69, s69, s63\n" \
  ".LCX_TOK_%=:\n" \
  "ds_read_b128 v[48:51], v6 offset:0\n" \
  "ds_read_b128 v[64:67], v0 offset:8192\n" \
  "ds_read_b128 v[80:83], v0 offset:12288\n" \
  "ds_read_b128 v[52:55], v6 offset:1024\n" \
  "ds_read_b128 v[68:71], v0 offset:9216\n" \
  "ds_read_b128 v[84:87], v0 offset:13312\n" \
  "ds_read_b128 v[56:59], v6 offset:2048\n" \
  "ds_read_b128 v[72:75], v0 offset:10240\n" \
  "ds_read_b128 v[88:91], v0 offset:14336\n" \
  "ds_read_b128 v[60:63], v6 offset:3072\n" \
  "ds_read_b128 v[76:79], v0 offset:11264\n" \
  "ds_read_b128 v[92:95], v0 offset:15360\n" \
  "s_waitcnt lgkmcnt(9)\n" \
  "v_mfma_f32_16x16x32_bf16 v[20:23], v[64:67], v[48:51], v[8:11]\n" \
  "v_mfma_f32_16x16x32_bf16 v[24:27], v[80:83], v[48:51], v[12:15]\n" \
  "ds_read_b128 v[96:99], v0 offset:16384\n" \
  "ds_read_b128 v[100:103], v0 offset:17408\n" \
  "s_waitcnt lgkmcnt(8)\n" \
  "v_mfma_f32_16x16x32_bf16 v[20:23], v[68:71], v[52:55], v[20:23]\n" \
  "v_mfma_f32_16x16x32_bf16 v[24:27], v[84:87], v[52:55], v[24:27]\n" \
  "ds_read_b128 v[104:107], v0 offset:18432\n" \
  "ds_read_b128 v[108:111], v0 offset:19456\n" \
  "s_waitcnt lgkmcnt(7)\n" \
  "v_mfma_f32_16x16x32_bf16 v[20:23], v[72:75], v[56:59], v[20:23]\n" \
  "v_mfma_f32_16x16x32_bf16 v[24:27], v[88:91], v[56:59], v[24:27]\n" \
  "ds_read_b128 v[112:115], v0 offset:20480\n" \
  "ds_read_b128 v[116:119], v0 offset:21504\n" \
  "s_waitcnt lgkmcnt(6)\n" \
  "v_mfma_f32_16x16x32_bf16 v[20:23], v[76:79], v[60:63], v[20:23]\n" \
  "v_mfma_f32_16x16x32_bf16 v[24:27], v[92:95], v[60:63], v[24:27]\n" \
  "ds_read_b128 v[120:123], v0 offset:22528\n" \
  "ds_read_b128 v[124:127], v0 offset:23552\n" \
  "s_add_u32 s67, s67, 1\n" \
  "v_add_u32 v0, s65, v0\n" \
  "v_add_u32 v6, s65, v6\n" \
  "s_sub_u32 s65, 0, s65\n" \
  "s_nop 1\n" \
  "v_max3_f32 v32, v20, v21, v22\n" \
  "v_max3_f32 v33, v23, v24, v25\n" \
  "v_max3_f32 v32, v32, v26, v27\n" \
  "v_max_f32 v32, v32, v33\n" \
  "v_mov_b32 v33, v32\n" \
  "s_nop 1\n" \
  "v_permlane16_swap_b32 v32, v33\n" \
  "v_max_f32 v32, v32, v33\n" \
  "v_mov_b32 v33, v32\n" \
  "s_nop 1\n" \
  "v_permlane32_swap_b32 v32, v33\n" \
  "v_max_f32 v32, v32, v33\n" \
  "v_mul_f32 v33, s72, v32\n" \
  "v_fma_f32 v20, v20, s71, v33\n" \
  "v_fma_f32 v21, v21, s71, v33\n" \
  "v_fma_f32 v22, v22, s71, v33\n" \
  "v_fma_f32 v23, v23, s71, v33\n" \
  "v_fma_f32 v24, v24, s71, v33\n" \
  "v_fma_f32 v25, v25, s71, v33\n" \
  "v_fma_f32 v26, v26, s71, v33\n" \
  "v_fma_f32 v27, v27, s71, v33\n" \
  "v_exp_f32 v20, v20\n" \
  "v_exp_f32 v21, v21\n" \
  "v_exp_f32 v22, v22\n" \
  "v_exp_f32 v23, v23\n" \
  "v_exp_f32 v24, v24\n" \
  "v_exp_f32 v25, v25\n" \
  "v_exp_f32 v26, v26\n" \
  "v_exp_f32 v27, v27\n" \
  "v_add_f32 v34, v20, v21\n" \
  "v_add_f32 v33, v22, v23\n" \
  "v_add_f32 v36, v24, v25\n" \
  "v_add_f32 v34, v34, v33\n" \
  "v_add_f32 v33, v26, v27\n" \
  "v_add_f32 v34, v34, v36\n" \
  "v_add_f32 v34, v34, v33\n" \
  "s_waitcnt vmcnt(0) lgkmcnt(0)\n" \
  "s_cmp_eq_u32 s61, 0\n" \
  "s_cbranch_scc1 .LCX_NP_%=\n" \
  "v_mov_b32 v46, s59\n" \
  "v_mov_b32 v47, 1\n" \
  "s_mov_b64 exec, 1\n" \
  "ds_add_rtn_u32 v46, v46, v47\n" \
  "s_mov_b64 exec, -1\n" \
  "s_xor_b32 s59, s59, 4\n" \
  "s_mov_b32 s61, 0\n" \
  "s_waitcnt lgkmcnt(0)\n" \
  "v_readfirstlane_b32 s63, v46\n" \
  "v_lshlrev_b32 v46, 5, v5\n" \
  "s_and_b32 s63, s63, 7\n" \
  "s_cmp_eq_u32 s63, 7\n" \
  "s_cbranch_scc0 .LCX_W2_%=\n" \
  "s_mov_b64 exec, 15\n" \
  "global_store_dword v46, v47, s[52:53] sc1\n" \
  "s_mov_b64 exec, -1\n" \
  ".LCX_W2_%=:\n" \
  "s_add_u32 s52, s52, 2048\n" \
  "s_addc_u32 s53, s53, 0\n" \
  ".LCX_NP_%=:\n" \
  "s_cmp_lg_u32 s68, 0\n" \
  "s_cbranch_scc1 .LCX_TEST_%=\n" \
  ".LCX_WF_%=:\n" \
  "s_memrealtime s[38:39]\n" \
  "s_waitcnt lgkmcnt(0)\n" \
  "s_sub_u32 s63, s69, s38\n" \
  "s_cmp_le_i32 s63, 0\n" \
  "s_cbranch_scc1 .LCX_GO_%=\n" \
  "s_cmp_lt_i32 s63, 12\n" \
  "s_cbranch_scc1 .LCX_WF_%=\n" \
  "s_sleep 3\n" \
  "s_branch .LCX_WF_%=\n" \
  ".LCX_GO_%=:\n" \
  "v_mov_b32 v35, 0\n" \
  "global_load_dwordx4 v[128:131], v2, s[48:49] offset:0 sc1\n" \
  "global_load_dwordx4 v[132:135], v2, s[48:49] offset:512 sc1\n" \
  "global_load_dwordx4 v[136:139], v2, s[48:49] offset:1024 sc1\n" \
  "global_load_dwordx4 v[140:143], v2, s[48:49] offset:1536 sc1\n" \
  "global_load_dwordx4 v[144:147], v3, s[48:49] offset:0 sc1\n" \
  "global_load_dwordx4 v[148:151], v3, s[48:49] offset:512 sc1\n" \
  "global_load_dwordx4 v[152:155], v3, s[48:49] offset:1024 sc1\n" \
  "global_load_dwordx4 v[156:159], v3, s[48:49] offset:1536 sc1\n" \
  "global_load_dword v35, v35, s[50:51] sc1\n" \
  "s_sub_u32 s62, s62, 8\n" \
  "s_cbranch_scc1 .LCX_END_%=\n" \
  "s_waitcnt vmcnt(0)\n" \
  ".LCX_TEST_%=:\n" \
  "v_mfma_f32_16x16x32_bf16 v[28:31], v[128:131], v[48:51], v[16:19]\n" \
  "v_min3_u32 v33, v128, v129, v130\n" \
  "v_min3_u32 v36, v131, v132, v133\n" \
  "v_min3_u32 v33, v33, v134, v135\n" \
  "v_min3_u32 v36, v36, v136, v137\n" \
  "v_mfma_f32_16x16x32_bf16 v[28:31], v[132:135], v[52:55], v[28:31]\n" \
  "v_min3_u32 v33, v33, v138, v139\n" \
  "v_min3_u32 v36, v36, v140, v141\n" \
  "v_min3_u32 v33, v33, v142, v143\n" \
  "v_min3_u32 v36, v36, v144, v145\n" \
  "v_mfma_f32_16x16x32_bf16 v[28:31], v[136:139], v[56:59], v[28:31]\n" \
  "v_min3_u32 v33, v33, v146, v147\n" \
  "v_min3_u32 v36, v36, v148, v149\n" \
  "v_min3_u32 v33, v33, v150, v151\n" \
  "v_min3_u32 v36, v36, v152, v153\n" \
  "v_mfma_f32_16x16x32_bf16 v[28:31], v[140:143], v[60:63], v[28:31]\n" \
  "v_min3_u32 v33, v33, v154, v155\n" \
  "v_min3_u32 v36, v36, v156, v157\n" \
  "v_min3_u32 v33, v33, v158, v159\n" \
  "v_min_u32 v33, v33, v36\n" \
  "v_cmp_eq_u32 vcc, 0, v33\n" \
  "v_readfirstlane_b32 s64, v35\n" \
  "s_mov_b32 s68, 0\n" \
  "s_cmp_eq_u64 vcc, 0\n" \
  "s_cbranch_scc0 .LCX_FAIL_%=\n" \
  "v_max3_f32 v36, v28, v29, v30\n" \
  "v_max3_f32 v36, v36, v31, v32\n" \
  "v_mov_b32 v33, v36\n" \
  "s_cmp_eq_u32 s64, 0\n" \
  "s_cselect_b32 s64, s38, s64\n" \
  "v_permlane16_swap_b32 v36, v33\n" \
  "v_max_f32 v36, v36, v33\n" \
  "v_mov_b32 v33, v36\n" \
  "s_sub_u32 s63, s64, s70\n" \
  "s_mov_b32 s70, s64\n" \
  "v_permlane32_swap_b32 v36, v33\n" \
  "v_max_f32 v36, v36, v33\n" \
  "v_mul_f32 v36, s72, v36\n" \
  "v_fma_f32 v32, v32, s71, v36\n" \
  "v_fma_f32 v28, v28, s71, v36\n" \
  "v_fma_f32 v29, v29, s71, v36\n" \
  "v_fma_f32 v30, v30, s71, v36\n" \
  "v_fma_f32 v31, v31, s71, v36\n" \
  "v_exp_f32 v32, v32\n" \
  "v_exp_f32 v28, v28\n" \
  "v_exp_f32 v29, v29\n" \
  "v_exp_f32 v30, v30\n" \
  "v_exp_f32 v31, v31\n" \
  "v_fma_f32 v34, v34, v32, v28\n" \
  "v_add_f32 v33, v29, v30\n" \
  "v_add_f32 v34, v34, v31\n" \
  "v_add_f32 v34, v34, v33\n" \
  "v_mov_b32 v33, v34\n" \
  "s_min_u32 s63, s63, 250\n" \
  "s_add_u32 s69, s64, s63\n" \
  "v_permlane16_swap_b32 v34, v33\n" \
  "v_add_f32 v34, v34, v33\n" \
  "v_mov_b32 v33, v34\n" \
  "s_add_u32 s69, s69, -48\n" \
  "s_add_u32 s48, s48, 0x10000\n" \
  "s_addc_u32 s49, s49, 0\n" \
  "v_permlane32_swap_b32 v34, v33\n" \
  "v_add_f32 v34, v34, v33\n" \
  "v_rcp_f32 v34, v34\n" \
  "s_add_u32 s50, s50, 1024\n" \
  "s_addc_u32 s51, s51, 0\n" \
  "v_mul_f32 v32, v32, v34\n" \
  "v_mul_f32 v20, v20, v32\n" \
  "v_mul_f32 v21, v21, v32\n" \
  "v_mul_f32 v22, v22, v32\n" \
  "v_mul_f32 v23, v23, v32\n" \
  "v_mul_f32 v24, v24, v32\n" \
  "v_mul_f32 v25, v25, v32\n" \
  "v_mul_f32 v26, v26, v32\n" \
  "v_mul_f32 v27, v27, v32\n" \
  "v_cvt_pk_bf16_f32 v40, v20, v21\n" \
  "v_cvt_pk_bf16_f32 v41, v22, v23\n" \
  "v_cvt_pk_bf16_f32 v42, v24, v25\n" \
  "v_cvt_pk_bf16_f32 v43, v26, v27\n" \
  "v_mul_f32 v28, v28, v34\n" \
  "v_mul_f32 v29, v29, v34\n" \
  "v_mfma_f32_16x16x32_bf16 v[64:67], v[96:99], v[40:43], 0\n" \
  "v_mul_f32 v30, v30, v34\n" \
  "v_mfma_f32_16x16x32_bf16 v[68:71], v[100:103], v[40:43], 0\n" \
  "v_mul_f32 v31, v31, v34\n" \
  "v_mfma_f32_16x16x32_bf16 v[72:75], v[104:107], v[40:43], 0\n" \
  "v_cvt_pk_bf16_f32 v44, v28, v29\n" \
  "v_mfma_f32_16x16x32_bf16 v[76:79], v[108:111], v[40:43], 0\n" \
  "v_cvt_pk_bf16_f32 v45, v30, v31\n" \
  "v_mfma_f32_16x16x32_bf16 v[80:83], v[112:115], v[40:43], 0\n" \
  "v_mfma_f32_16x16x32_bf16 v[84:87], v[116:119], v[40:43], 0\n" \
  "v_mfma_f32_16x16x32_bf16 v[88:91], v[120:123], v[40:43], 0\n" \
  "v_mfma_f32_16x16x32_bf16 v[92:95], v[124:127], v[40:43], 0\n" \
  "s_memrealtime s[38:39]\n" \
  "ds_read_b32 v37, v38\n" \
  "v_mfma_f32_16x16x16_bf16 v[64:67], v[144:145], v[44:45], v[64:67]\n" \
  "v_mfma_f32_16x16x16_bf16 v[68:71], v[148:149], v[44:45], v[68:71]\n" \
  "v_mfma_f32_16x16x16_bf16 v[72:75], v[152:153], v[44:45], v[72:75]\n" \
  "v_mfma_f32_16x16x16_bf16 v[76:79], v[156:157], v[44:45], v[76:79]\n" \
  "v_mfma_f32_16x16x16_bf16 v[80:83], v[146:147], v[44:45], v[80:83]\n" \
  "v_mfma_f32_16x16x16_bf16 v[84:87], v[150:151], v[44:45], v[84:87]\n" \
  "v_mfma_f32_16x16x16_bf16 v[88:91], v[154:155], v[44:45], v[88:91]\n" \
  "v_mfma_f32_16x16x16_bf16 v[92:95], v[158:159], v[44:45], v[92:95]\n" \
  "s_nop 0\n" \
  "v_cvt_pk_bf16_f32 v160, v64, v65\n" \
  "v_cvt_pk_bf16_f32 v161, v66, v67\n" \
  "v_cvt_pk_bf16_f32 v162, v68, v69\n" \
  "v_cvt_pk_bf16_f32 v163, v70, v71\n" \
  "ds_write_b128 v7, v[160:163] offset:0\n" \
  "v_cvt_pk_bf16_f32 v164, v72, v73\n" \
  "v_cvt_pk_bf16_f32 v165, v74, v75\n" \
  "v_cvt_pk_bf16_f32 v166, v76, v77\n" \
  "v_cvt_pk_bf16_f32 v167, v78, v79\n" \
  "ds_write_b128 v7, v[164:167] offset:1024\n" \
  "v_cvt_pk_bf16_f32 v168, v80, v81\n" \
  "v_cvt_pk_bf16_f32 v169, v82, v83\n" \
  "v_cvt_pk_bf16_f32 v170, v84, v85\n" \
  "v_cvt_pk_bf16_f32 v171, v86, v87\n" \
  "ds_write_b128 v7, v[168:171] offset:2048\n" \
  "v_cvt_pk_bf16_f32 v172, v88, v89\n" \
  "v_cvt_pk_bf16_f32 v173, v90, v91\n" \
  "v_cvt_pk_bf16_f32 v174, v92, v93\n" \
  "v_cvt_pk_bf16_f32 v175, v94, v95\n" \
  "ds_write_b128 v7, v[172:175] offset:3072\n" \
  "v_add_u32 v7, s66, v7\n" \
  "s_waitcnt lgkmcnt(0)\n" \
  "s_barrier\n" \
  "ds_read_b128 v[48:51], v1 offset:0\n" \
  "ds_read_b128 v[52:55], v1 offset:4096\n" \
  "ds_read_b128 v[56:59], v1 offset:1024\n" \
  "ds_read_b128 v[60:63], v1 offset:5120\n" \
  "ds_read_b128 v[64:67], v1 offset:2048\n" \
  "ds_read_b128 v[68:71], v1 offset:6144\n" \
  "ds_read_b128 v[72:75], v1 offset:3072\n" \
  "ds_read_b128 v[76:79], v1 offset:7168\n" \
  "ds_read_b128 v[80:83], v1 offset:8192\n" \
  "ds_read_b128 v[84:87], v1 offset:12288\n" \
  "ds_read_b128 v[88:91], v1 offset:9216\n" \
  "ds_read_b128 v[92:95], v1 offset:13312\n" \
  "ds_read_b128 v[96:99], v1 offset:10240\n" \
  "ds_read_b128 v[100:103], v1 offset:14336\n" \
  "v_readfirstlane_b32 s73, v37\n" \
  "s_sub_u32 s63, s38, s69\n" \
  "s_bfe_u32 s64, s73, 0x80010\n" \
  "s_sub_u32 s64, s64, s67\n" \
  "s_or_b32 s63, s63, s64\n" \
  "s_bfe_u32 s64, s73, 0x80008\n" \
  "s_and_b32 s73, s73, 0xff\n" \
  "s_min_u32 s73, s73, s64\n" \
  "s_or_b32 s63, s63, s68\n" \
  "s_cmp_lt_i32 s63, 0\n" \
  "s_cbranch_scc1 .LCX_E3_%=\n" \
  "v_mov_b32 v35, 0\n" \
  "global_load_dwordx4 v[128:131], v2, s[48:49] offset:0 sc1\n" \
  "global_load_dwordx4 v[132:135], v2, s[48:49] offset:512 sc1\n" \
  "global_load_dwordx4 v[136:139], v2, s[48:49] offset:1024 sc1\n" \
  "global_load_dwordx4 v[140:143], v2, s[48:49] offset:1536 sc1\n" \
  "global_load_dwordx4 v[144:147], v3, s[48:49] offset:0 sc1\n" \
  "global_load_dwordx4 v[148:151], v3, s[48:49] offset:512 sc1\n" \
  "global_load_dwordx4 v[152:155], v3, s[48:49] offset:1024 sc1\n" \
  "global_load_dwordx4 v[156:159], v3, s[48:49] offset:1536 sc1\n" \
  "global_load_dword v35, v35, s[50:51] sc1\n" \
  "v_mov_b32 v33, s67\n" \
  "ds_write_b32 v38, v33 offset:8\n" \
  "s_mov_b32 s68, -1\n" \
  ".LCX_E3_%=:\n" \
  "s_waitcnt lgkmcnt(12)\n" \
  "v_mfma_f32_16x16x32_bf16 v[112:115], v[192:195], v[48:51], 0\n" \
  "v_mfma_f32_16x16x32_bf16 v[116:119], v[224:227], v[48:51], 0\n" \
  "v_mfma_f32_16x16x32_bf16 v[120:123], v[192:195], v[52:55], 0\n" \
  "v_mfma_f32_16x16x32_bf16 v[124:127], v[224:227], v[52:55], 0\n" \
  "ds_read_b128 v[104:107], v1 offset:11264\n" \
  "ds_read_b128 v[108:111], v1 offset:15360\n" \
  "s_waitcnt lgkmcnt(12)\n" \
  "v_mfma_f32_16x16x32_bf16 v[112:115], v[196:199], v[56:59], v[112:115]\n" \
  "v_mfma_f32_16x16x32_bf16 v[116:119], v[228:231], v[56:59], v[116:119]\n" \
  "v_mfma_f32_16x16x32_bf16 v[120:123], v[196:199], v[60:63], v[120:123]\n" \
  "v_mfma_f32_16x16x32_bf16 v[124:127], v[228:231], v[60:63], v[124:127]\n" \
  "s_waitcnt lgkmcnt(10)\n" \
  "v_mfma_f32_16x16x32_bf16 v[112:115], v[200:203], v[64:67], v[112:115]\n" \
  "v_mfma_f32_16x16x32_bf16 v[116:119], v[232:235], v[64:67], v[116:119]\n" \
  "v_mfma_f32_16x16x32_bf16 v[120:123], v[200:203], v[68:71], v[120:123]\n" \
  "v_mfma_f32_16x16x32_bf16 v[124:127], v[232:235], v[68:71], v[124:127]\n" \
  "s_waitcnt lgkmcnt(8)\n" \
  "v_mfma_f32_16x16x32_bf16 v[112:115], v[204:207], v[72:75], v[112:115]\n" \
  "v_mfma_f32_16x16x32_bf16 v[116:119], v[236:239], v[72:75], v[116:119]\n" \
  "v_mfma_f32_16x16x32_bf16 v[120:123], v[204:207], v[76:79], v[120:123]\n" \
  "v_mfma_f32_16x16x32_bf16 v[124:127], v[236:239], v[76:79], v[124:127]\n" \
  "s_waitcnt lgkmcnt(6)\n" \
  "v_mfma_f32_16x16x32_bf16 v[112:115], v[208:211], v[80:83], v[112:115]\n" \
  "v_mfma_f32_16x16x32_bf16 v[116:119], v[240:243], v[80:83], v[116:119]\n" \
  "v_mfma_f32_16x16x32_bf16 v[120:123], v[208:211], v[84:87], v[120:123]\n" \
  "v_mfma_f32_16x16x32_bf16 v[124:127], v[240:243], v[84:87], v[124:127]\n" \
  "s_waitcnt lgkmcnt(4)\n" \
  "v_mfma_f32_16x16x32_bf16 v[112:115], v[212:215], v[88:91], v[112:115]\n" \
  "v_mfma_f32_16x16x32_bf16 v[116:119], v[244:247], v[88:91], v[116:119]\n" \
  "v_mfma_f32_16x16x32_bf16 v[120:123], v[212:215], v[92:95], v[120:123]\n" \
  "v_mfma_f32_16x16x32_bf16 v[124:127], v[244:247], v[92:95], v[124:127]\n" \
  "s_memrealtime s[38:39]\n" \
  "ds_read_b32 v37, v38\n" \
  "s_waitcnt lgkmcnt(3)\n" \
  "v_mfma_f32_16x16x32_bf16 v[112:115], v[216:219], v[96:99], v[112:115]\n" \
  "v_mfma_f32_16x16x32_bf16 v[116:119], v[248:251], v[96:99], v[116:119]\n" \
  "v_mfma_f32_16x16x32_bf16 v[120:123], v[216:219], v[100:103], v[120:123]\n" \
  "v_mfma_f32_16x16x32_bf16 v[124:127], v[248:251], v[100:103], v[124:127]\n" \
  "s_waitcnt lgkmcnt(0)\n" \
  "v_mfma_f32_16x16x32_bf16 v[112:115], v[220:223], v[104:107], v[112:115]\n" \
  "v_mfma_f32_16x16x32_bf16 v[116:119], v[252:255], v[104:107], v[116:119]\n" \
  "v_mfma_f32_16x16x32_bf16 v[120:123], v[220:223], v[108:111], v[120:123]\n" \
  "v_mfma_f32_16x16x32_bf16 v[124:127], v[252:255], v[108:111], v[124:127]\n" \
  "v_add_u32 v1, s66, v1\n" \
  "s_sub_u32 s66, 0, s66\n" \
  "s_add_u32 s64, s54, 16384\n" \
  "s_mov_b32 s61, 1\n" \
  "s_nop 1\n" \
  "v_cvt_pk_bf16_f32 v20, v112, v113\n" \
  "v_cvt_pk_bf16_f32 v21, v114, v115\n" \
  "v_cvt_pk_bf16_f32 v22, v116, v117\n" \
  "v_cvt_pk_bf16_f32 v23, v118, v119\n" \
  "buffer_store_dwordx4 v[20:23], v4, s[40:43], s54 offen sc1\n" \
  "v_cvt_pk_bf16_f32 v24, v120, v121\n" \
  "v_cvt_pk_bf16_f32 v25, v122, v123\n" \
  "v_cvt_pk_bf16_f32 v26, v124, v125\n" \
  "v_cvt_pk_bf16_f32 v27, v126, v127\n" \
  "buffer_store_dwordx4 v[24:27], v4, s[40:43], s64 offen sc1\n" \
  "v_readfirstlane_b32 s73, v37\n" \
  "s_sub_u32 s63, s38, s69\n" \
  "s_bfe_u32 s64, s73, 0x80010\n" \
  "s_sub_u32 s64, s64, s67\n" \
  "s_or_b32 s63, s63, s64\n" \
  "s_bfe_u32 s64, s73, 0x80008\n" \
  "s_and_b32 s73, s73, 0xff\n" \
  "s_min_u32 s73, s73, s64\n" \
  "s_or_b32 s63, s63, s68\n" \
  "s_cmp_lt_i32 s63, 0\n" \
  "s_cbranch_scc1 .LCX_E4_%=\n" \
  "v_mov_b32 v35, 0\n" \
  "global_load_dwordx4 v[128:131], v2, s[48:49] offset:0 sc1\n" \
  "global_load_dwordx4 v[132:135], v2, s[48:49] offset:512 sc1\n" \
  "global_load_dwordx4 v[136:139], v2, s[48:49] offset:1024 sc1\n" \
  "global_load_dwordx4 v[140:143], v2, s[48:49] offset:1536 sc1\n" \
  "global_load_dwordx4 v[144:147], v3, s[48:49] offset:0 sc1\n" \
  "global_load_dwordx4 v[148:151], v3, s[48:49] offset:512 sc1\n" \
  "global_load_dwordx4 v[152:155], v3, s[48:49] offset:1024 sc1\n" \
  "global_load_dwordx4 v[156:159], v3, s[48:49] offset:1536 sc1\n" \
  "global_load_dword v35, v35, s[50:51] sc1\n" \
  "v_mov_b32 v33, s67\n" \
  "ds_write_b32 v38, v33 offset:8\n" \
  "s_mov_b32 s68, -1\n" \
  ".LCX_E4_%=:\n" \
  "s_add_u32 s54, s54, 0x80000\n" \
  "s_sub_u32 s60, s60, 1\n" \
  "s_cmp_lg_u32 s60, 0\n" \
  "s_cbranch_scc1 .LCX_TOP_%=\n" \
  "s_waitcnt vmcnt(0)\n" \
  "v_mov_b32 v46, s59\n" \
  "v_mov_b32 v47, 1\n" \
  "s_mov_b64 exec, 1\n" \
  "ds_add_rtn_u32 v46, v46, v47\n" \
  "s_mov_b64 exec, -1\n" \
  "s_xor_b32 s59, s59, 4\n" \
  "s_mov_b32 s61, 0\n" \
  "s_waitcnt lgkmcnt(0)\n" \
  "v_readfirstlane_b32 s63, v46\n" \
  "v_lshlrev_b32 v46, 5, v5\n" \
  "s_and_b32 s63, s63, 7\n" \
  "s_cmp_eq_u32 s63, 7\n" \
  "s_cbranch_scc0 .LCX_W5_%=\n" \
  "s_mov_b64 exec, 15\n" \
  "global_store_dword v46, v47, s[52:53] sc1\n" \
  "s_mov_b64 exec, -1\n" \
  ".LCX_W5_%=:\n" \
  "s_add_u32 s52, s52, 2048\n" \
  "s_addc_u32 s53, s53, 0\n" \
  ".LCX_END_%=:\n" \
  "s_endpgm\n" \
  ".LCX_FAIL_%=:\n" \
  "s_add_u32 s69, s64, -48\n" \
  "s_cmp_lg_u32 s64, 0\n" \
  "s_cbranch_scc1 .LCX_WF_%=\n" \
  "s_sleep 4\n" \
  "global_load_dword v35, v35, s[50:51] sc1\n" \
  "s_sub_u32 s62, s62, 8\n" \
  "s_cbranch_scc1 .LCX_END_%=\n" \
  "s_waitcnt vmcnt(0)\n" \
  "v_readfirstlane_b32 s64, v35\n" \
  "s_branch .LCX_FAIL_%=\n"
#define AW_ASM_Y \
  "s_mov_b32 s72, 0\n" \
  "s_mov_b32 s67, 0\n" \
  "s_mov_b32 s68, 2\n" \
  "s_mov_b32 s69, 0\n" \
  "s_mov_b32 s65, 49152\n" \
  "s_mov_b32 s66, 16384\n" \
  "v_mov_b32 v5, s58\n" \
  "v_add_u32 v3, 4096, v2\n" \
  "s_mov_b32 s62, 0x40000\n" \
  "s_branch .LCY_ASK_%=\n" \
  ".LCY_BAR_%=:\n" \
  "s_barrier\n" \
  "ds_read_b128 v[8:11], v0 offset:0\n" \
  "ds_read_b128 v[12:15], v0 offset:1024\n" \
  "ds_read_b128 v[16:19], v0 offset:2048\n" \
  "s_add_u32 s68, s68, 1\n" \
  "s_add_u32 s64, s54, 16384\n" \
  "s_cmp_eq_u32 s69, 0\n" \
  "s_cbranch_scc1 .LCY_NI_%=\n" \
  "s_waitcnt vmcnt(1)\n" \
  "s_cmp_lg_u32 s61, 0\n" \
  "s_cbranch_scc1 .LCY_NJ_%=\n" \
  "s_waitcnt vmcnt(0)\n" \
  ".LCY_NJ_%=:\n" \
  "v_mov_b32 v6, s55\n" \
  "s_mov_b64 exec, 1\n" \
  "ds_add_u32 v5, v6\n" \
  "s_mov_b64 exec, -1\n" \
  "s_mov_b32 s69, 0\n" \
  "s_cmp_eq_u32 s72, 0\n" \
  "s_cbranch_scc1 .LCY_NI_%=\n" \
  "s_mov_b32 s72, 0\n" \
  "v_mov_b32 v6, s59\n" \
  "v_mov_b32 v7, 1\n" \
  "s_mov_b64 exec, 1\n" \
  "ds_add_rtn_u32 v6, v6, v7\n" \
  "s_mov_b64 exec, -1\n" \
  "s_xor_b32 s59, s59, 4\n" \
  "s_mov_b32 s61, 0\n" \
  "s_waitcnt lgkmcnt(0)\n" \
  "v_readfirstlane_b32 s63, v6\n" \
  "v_lshlrev_b32 v6, 5, v2\n" \
  "s_and_b32 s63, s63, 7\n" \
  "s_cmp_eq_u32 s63, 7\n" \
  "s_cbranch_scc0 .LCY_W1_%=\n" \
  "s_mov_b64 exec, 15\n" \
  "global_store_dword v6, v7, s[52:53] sc1\n" \
  "s_mov_b64 exec, -1\n" \
  ".LCY_W1_%=:\n" \
  "s_add_u32 s52, s52, 2048\n" \
  "s_addc_u32 s53, s53, 0\n" \
  ".LCY_NI_%=:\n" \
  "v_cmp_eq_u32 vcc, 0, v4\n" \
  "s_cmp_eq_u64 vcc, 0\n" \
  "s_cselect_b32 s70, 1, 0\n" \
  "s_cbranch_scc0 .LCY_NS_%=\n" \
  "v_lshlrev_b32 v6, 16, s55\n" \
  "s_mov_b64 exec, 1\n" \
  "ds_add_u32 v5, v6\n" \
  "s_mov_b64 exec, -1\n" \
  "s_mov_b32 m0, s56\n" \
  "s_add_u32 s71, s56, 4096\n" \
  "global_load_lds_dwordx4 v2, s[48:49] offset:0 sc1\n" \
  "global_load_lds_dwordx4 v2, s[48:49] offset:1024 sc1\n" \
  "global_load_lds_dwordx4 v2, s[48:49] offset:2048 sc1\n" \
  "global_load_lds_dwordx4 v2, s[48:49] offset:3072 sc1\n" \
  "s_mov_b32 m0, s71\n" \
  "s_add_u32 s56, s56, s65\n" \
  "global_load_lds_dwordx4 v3, s[48:49] offset:0 sc1\n" \
  "global_load_lds_dwordx4 v3, s[48:49] offset:1024 sc1\n" \
  ".LCY_NS_%=:\n" \
  "ds_read_b128 v[20:23], v0 offset:3072\n" \
  "s_waitcnt lgkmcnt(3)\n" \
  "v_mfma_f32_16x16x32_bf16 v[32:35], v[64:67], v[8:11], 0\n" \
  "v_mfma_f32_16x16x32_bf16 v[36:39], v[96:99], v[8:11], 0\n" \
  "v_mfma_f32_16x16x32_bf16 v[40:43], v[128:131], v[8:11], 0\n" \
  "v_mfma_f32_16x16x32_bf16 v[44:47], v[160:163], v[8:11], 0\n" \
  "v_mfma_f32_16x16x32_bf16 v[48:51], v[192:195], v[8:11], 0\n" \
  "v_mfma_f32_16x16x32_bf16 v[52:55], v[224:227], v[8:11], 0\n" \
  "ds_read_b128 v[8:11], v0 offset:8192\n" \
  "s_waitcnt lgkmcnt(3)\n" \
  "v_mfma_f32_16x16x32_bf16 v[32:35], v[68:71], v[12:15], v[32:35]\n" \
  "v_mfma_f32_16x16x32_bf16 v[36:39], v[100:103], v[12:15], v[36:39]\n" \
  "v_mfma_f32_16x16x32_bf16 v[40:43], v[132:135], v[12:15], v[40:43]\n" \
  "v_mfma_f32_16x16x32_bf16 v[44:47], v[164:167], v[12:15], v[44:47]\n" \
  "v_mfma_f32_16x16x32_bf16 v[48:51], v[196:199], v[12:15], v[48:51]\n" \
  "v_mfma_f32_16x16x32_bf16 v[52:55], v[228:231], v[12:15], v[52:55]\n" \
  "ds_read_b128 v[12:15], v0 offset:9216\n" \
  "s_waitcnt lgkmcnt(3)\n" \
  "v_mfma_f32_16x16x32_bf16 v[32:35], v[72:75], v[16:19], v[32:35]\n" \
  "v_mfma_f32_16x16x32_bf16 v[36:39], v[104:107], v[16:19], v[36:39]\n" \
  "v_mfma_f32_16x16x32_bf16 v[40:43], v[136:139], v[16:19], v[40:43]\n" \
  "v_mfma_f32_16x16x32_bf16 v[44:47], v[168:171], v[16:19], v[44:47]\n" \
  "v_mfma_f32_16x16x32_bf16 v[48:51], v[200:203], v[16:19], v[48:51]\n" \
  "v_mfma_f32_16x16x32_bf16 v[52:55], v[232:235], v[16:19], v[52:55]\n" \
  "ds_read_b128 v[16:19], v0 offset:10240\n" \
  "s_waitcnt lgkmcnt(3)\n" \
  "v_mfma_f32_16x16x32_bf16 v[32:35], v[76:79], v[20:23], v[32:35]\n" \
  "v_mfma_f32_16x16x32_bf16 v[36:39], v[108:111], v[20:23], v[36:39]\n" \
  "v_mfma_f32_16x16x32_bf16 v[40:43], v[140:143], v[20:23], v[40:43]\n" \
  "v_mfma_f32_16x16x32_bf16 v[44:47], v[172:175], v[20:23], v[44:47]\n" \
  "v_mfma_f32_16x16x32_bf16 v[48:51], v[204:207], v[20:23], v[48:51]\n" \
  "v_mfma_f32_16x16x32_bf16 v[52:55], v[236:239], v[20:23], v[52:55]\n" \
  "ds_read_b128 v[20:23], v0 offset:11264\n" \
  "s_waitcnt lgkmcnt(3)\n" \
  "v_mfma_f32_16x16x32_bf16 v[32:35], v[80:83], v[8:11], v[32:35]\n" \
  "v_mfma_f32_16x16x32_bf16 v[36:39], v[112:115], v[8:11], v[36:39]\n" \
  "v_mfma_f32_16x16x32_bf16 v[40:43], v[144:147], v[8:11], v[40:43]\n" \
  "v_mfma_f32_16x16x32_bf16 v[44:47], v[176:179], v[8:11], v[44:47]\n" \
  "v_mfma_f32_16x16x32_bf16 v[48:51], v[208:211], v[8:11], v[48:51]\n" \
  "v_mfma_f32_16x16x32_bf16 v[52:55], v[240:243], v[8:11], v[52:55]\n" \
  "ds_read_b128 v[8:11], v0 offset:4096\n" \
  "s_waitcnt lgkmcnt(3)\n" \
  "v_mfma_f32_16x16x32_bf16 v[32:35], v[84:87], v[12:15], v[32:35]\n" \
  "v_mfma_f32_16x16x32_bf16 v[36:39], v[116:119], v[12:15], v[36:39]\n" \
  "v_mfma_f32_16x16x32_bf16 v[40:43], v[148:151], v[12:15], v[40:43]\n" \
  "v_mfma_f32_16x16x32_bf16 v[44:47], v[180:183], v[12:15], v[44:47]\n" \
  "v_mfma_f32_16x16x32_bf16 v[48:51], v[212:215], v[12:15], v[48:51]\n" \
  "v_mfma_f32_16x16x32_bf16 v[52:55], v[244:247], v[12:15], v[52:55]\n" \
  "ds_read_b128 v[12:15], v0 offset:5120\n" \
  "s_waitcnt lgkmcnt(3)\n" \
  "v_mfma_f32_16x16x32_bf16 v[32:35], v[88:91], v[16:19], v[32:35]\n" \
  "v_mfma_f32_16x16x32_bf16 v[36:39], v[120:123], v[16:19], v[36:39]\n" \
  "v_mfma_f32_16x16x32_bf16 v[40:43], v[152:155], v[16:19], v[40:43]\n" \
  "v_mfma_f32_16x16x32_bf16 v[44:47], v[184:187], v[16:19], v[44:47]\n" \
  "v_mfma_f32_16x16x32_bf16 v[48:51], v[216:219], v[16:19], v[48:51]\n" \
  "v_mfma_f32_16x16x32_bf16 v[52:55], v[248:251], v[16:19], v[52:55]\n" \
  "ds_read_b128 v[16:19], v0 offset:6144\n" \
  "s_waitcnt lgkmcnt(3)\n" \
  "v_mfma_f32_16x16x32_bf16 v[32:35], v[92:95], v[20:23], v[32:35]\n" \
  "v_mfma_f32_16x16x32_bf16 v[36:39], v[124:127], v[20:23], v[36:39]\n" \
  "v_mfma_f32_16x16x32_bf16 v[40:43], v[156:159], v[20:23], v[40:43]\n" \
  "v_mfma_f32_16x16x32_bf16 v[44:47], v[188:191], v[20:23], v[44:47]\n" \
  "v_mfma_f32_16x16x32_bf16 v[48:51], v[220:223], v[20:23], v[48:51]\n" \
  "v_mfma_f32_16x16x32_bf16 v[52:55], v[252:255], v[20:23], v[52:55]\n" \
  "ds_read_b128 v[20:23], v0 offset:7168\n" \
  "s_waitcnt lgkmcnt(3)\n" \
  "s_nop 1\n" \
  "v_cvt_pk_bf16_f32 v24, v32, v33\n" \
  "v_cvt_pk_bf16_f32 v25, v34, v35\n" \
  "v_cvt_pk_bf16_f32 v26, v36, v37\n" \
  "v_cvt_pk_bf16_f32 v27, v38, v39\n" \
  "s_nop 0\n" \
  "v_mfma_f32_16x16x32_bf16 v[32:35], v[64:67], v[8:11], 0\n" \
  "v_mfma_f32_16x16x32_bf16 v[36:39], v[96:99], v[8:11], 0\n" \
  "buffer_store_dwordx4 v[24:27], v1, s[40:43], s54 offen offset:0 sc1\n" \
  "v_cvt_pk_bf16_f32 v28, v40, v41\n" \
  "v_cvt_pk_bf16_f32 v29, v42, v43\n" \
  "v_cvt_pk_bf16_f32 v30, v44, v45\n" \
  "v_cvt_pk_bf16_f32 v31, v46, v47\n" \
  "s_nop 0\n" \
  "v_mfma_f32_16x16x32_bf16 v[40:43], v[128:131], v[8:11], 0\n" \
  "v_mfma_f32_16x16x32_bf16 v[44:47], v[160:163], v[8:11], 0\n" \
  "buffer_store_dwordx4 v[28:31], v1, s[40:43], s54 offen offset:128 sc1\n" \
  "v_cvt_pk_bf16_f32 v56, v48, v49\n" \
  "v_cvt_pk_bf16_f32 v57, v50, v51\n" \
  "v_cvt_pk_bf16_f32 v58, v52, v53\n" \
  "v_cvt_pk_bf16_f32 v59, v54, v55\n" \
  "s_nop 0\n" \
  "v_mfma_f32_16x16x32_bf16 v[48:51], v[192:195], v[8:11], 0\n" \
  "v_mfma_f32_16x16x32_bf16 v[52:55], v[224:227], v[8:11], 0\n" \
  "buffer_store_dwordx4 v[56:59], v1, s[40:43], s54 offen offset:256 sc1\n" \
  "ds_read_b128 v[8:11], v0 offset:12288\n" \
  "s_waitcnt lgkmcnt(3)\n" \
  "v_mfma_f32_16x16x32_bf16 v[32:35], v[68:71], v[12:15], v[32:35]\n" \
  "v_mfma_f32_16x16x32_bf16 v[36:39], v[100:103], v[12:15], v[36:39]\n" \
  "v_mfma_f32_16x16x32_bf16 v[40:43], v[132:135], v[12:15], v[40:43]\n" \
  "v_mfma_f32_16x16x32_bf16 v[44:47], v[164:167], v[12:15], v[44:47]\n" \
  "v_mfma_f32_16x16x32_bf16 v[48:51], v[196:199], v[12:15], v[48:51]\n" \
  "v_mfma_f32_16x16x32_bf16 v[52:55], v[228:231], v[12:15], v[52:55]\n" \
  "ds_read_b128 v[12:15], v0 offset:13312\n" \
  "s_waitcnt lgkmcnt(3)\n" \
  "v_mfma_f32_16x16x32_bf16 v[32:35], v[72:75], v[16:19], v[32:35]\n" \
  "v_mfma_f32_16x16x32_bf16 v[36:39], v[104:107], v[16:19], v[36:39]\n" \
  "v_mfma_f32_16x16x32_bf16 v[40:43], v[136:139], v[16:19], v[40:43]\n" \
  "v_mfma_f32_16x16x32_bf16 v[44:47], v[168:171], v[16:19], v[44:47]\n" \
  "v_mfma_f32_16x16x32_bf16 v[48:51], v[200:203], v[16:19], v[48:51]\n" \
  "v_mfma_f32_16x16x32_bf16 v[52:55], v[232:235], v[16:19], v[52:55]\n" \
  "ds_read_b128 v[16:19], v0 offset:14336\n" \
  "s_waitcnt lgkmcnt(3)\n" \
  "v_mfma_f32_16x16x32_bf16 v[32:35], v[76:79], v[20:23], v[32:35]\n" \
  "v_mfma_f32_16x16x32_bf16 v[36:39], v[108:111], v[20:23], v[36:39]\n" \
  "v_mfma_f32_16x16x32_bf16 v[40:43], v[140:143], v[20:23], v[40:43]\n" \
  "v_mfma_f32_16x16x32_bf16 v[44:47], v[172:175], v[20:23], v[44:47]\n" \
  "v_mfma_f32_16x16x32_bf16 v[48:51], v[204:207], v[20:23], v[48:51]\n" \
  "v_mfma_f32_16x16x32_bf16 v[52:55], v[236:239], v[20:23], v[52:55]\n" \
  "ds_read_b32 v7, v5 offset:8\n" \
  "ds_read_b128 v[20:23], v0 offset:15360\n" \
  "s_waitcnt lgkmcnt(4)\n" \
  "v_mfma_f32_16x16x32_bf16 v[32:35], v[80:83], v[8:11], v[32:35]\n" \
  "v_mfma_f32_16x16x32_bf16 v[36:39], v[112:115], v[8:11], v[36:39]\n" \
  "v_mfma_f32_16x16x32_bf16 v[40:43], v[144:147], v[8:11], v[40:43]\n" \
  "v_mfma_f32_16x16x32_bf16 v[44:47], v[176:179], v[8:11], v[44:47]\n" \
  "v_mfma_f32_16x16x32_bf16 v[48:51], v[208:211], v[8:11], v[48:51]\n" \
  "v_mfma_f32_16x16x32_bf16 v[52:55], v[240:243], v[8:11], v[52:55]\n" \
  "s_waitcnt lgkmcnt(3)\n" \
  "v_mfma_f32_16x16x32_bf16 v[32:35], v[84:87], v[12:15], v[32:35]\n" \
  "v_mfma_f32_16x16x32_bf16 v[36:39], v[116:119], v[12:15], v[36:39]\n" \
  "v_mfma_f32_16x16x32_bf16 v[40:43], v[148:151], v[12:15], v[40:43]\n" \
  "v_mfma_f32_16x16x32_bf16 v[44:47], v[180:183], v[12:15], v[44:47]\n" \
  "v_mfma_f32_16x16x32_bf16 v[48:51], v[212:215], v[12:15], v[48:51]\n" \
  "v_mfma_f32_16x16x32_bf16 v[52:55], v[244:247], v[12:15], v[52:55]\n" \
  "s_waitcnt lgkmcnt(2)\n" \
  "v_mfma_f32_16x16x32_bf16 v[32:35], v[88:91], v[16:19], v[32:35]\n" \
  "v_mfma_f32_16x16x32_bf16 v[36:39], v[120:123], v[16:19], v[36:39]\n" \
  "v_mfma_f32_16x16x32_bf16 v[40:43], v[152:155], v[16:19], v[40:43]\n" \
  "v_mfma_f32_16x16x32_bf16 v[44:47], v[184:187], v[16:19], v[44:47]\n" \
  "v_mfma_f32_16x16x32_bf16 v[48:51], v[216:219], v[16:19], v[48:51]\n" \
  "v_mfma_f32_16x16x32_bf16 v[52:55], v[248:251], v[16:19], v[52:55]\n" \
  "s_waitcnt lgkmcnt(0)\n" \
  "v_mfma_f32_16x16x32_bf16 v[32:35], v[92:95], v[20:23], v[32:35]\n" \
  "v_mfma_f32_16x16x32_bf16 v[36:39], v[124:127], v[20:23], v[36:39]\n" \
  "v_mfma_f32_16x16x32_bf16 v[40:43], v[156:159], v[20:23], v[40:43]\n" \
  "v_mfma_f32_16x16x32_bf16 v[44:47], v[188:191], v[20:23], v[44:47]\n" \
  "v_mfma_f32_16x16x32_bf16 v[48:51], v[220:223], v[20:23], v[48:51]\n" \
  "v_mfma_f32_16x16x32_bf16 v[52:55], v[252:255], v[20:23], v[52:55]\n" \
  "v_add_u32 v0, s66, v0\n" \
  "s_sub_u32 s66, 0, s66\n" \
  "s_add_u32 s54, s54, 0x80000\n" \
  "s_mov_b32 s62, 0x40000\n" \
  "v_readfirstlane_b32 s63, v7\n" \
  "v_cvt_pk_bf16_f32 v24, v32, v33\n" \
  "v_cvt_pk_bf16_f32 v25, v34, v35\n" \
  "v_cvt_pk_bf16_f32 v26, v36, v37\n" \
  "v_cvt_pk_bf16_f32 v27, v38, v39\n" \
  "buffer_store_dwordx4 v[24:27], v1, s[40:43], s64 offen offset:0 sc1\n" \
  "v_cvt_pk_bf16_f32 v28, v40, v41\n" \
  "v_cvt_pk_bf16_f32 v29, v42, v43\n" \
  "v_cvt_pk_bf16_f32 v30, v44, v45\n" \
  "v_cvt_pk_bf16_f32 v31, v46, v47\n" \
  "buffer_store_dwordx4 v[28:31], v1, s[40:43], s64 offen offset:128 sc1\n" \
  "v_cvt_pk_bf16_f32 v56, v48, v49\n" \
  "v_cvt_pk_bf16_f32 v57, v50, v51\n" \
  "v_cvt_pk_bf16_f32 v58, v52, v53\n" \
  "v_cvt_pk_bf16_f32 v59, v54, v55\n" \
  "buffer_store_dwordx4 v[56:59], v1, s[40:43], s64 offen offset:256 sc1\n" \
  "s_cmp_eq_u32 s70, 0\n" \
  "s_cbranch_scc1 .LCY_ND_%=\n" \
  "global_load_lds_dwordx4 v3, s[48:49] offset:2048 sc1\n" \
  "global_load_lds_dwordx4 v3, s[48:49] offset:3072 sc1\n" \
  "s_add_u32 s48, s48, 0x80000\n" \
  "s_addc_u32 s49, s49, 0\n" \
  "s_mov_b32 m0, s57\n" \
  "s_add_u32 s57, s57, s65\n" \
  "global_load_lds_dwordx4 v2, s[50:51] offset:0 sc1\n" \
  "global_load_lds_dwordx4 v2, s[50:51] offset:1024 sc1\n" \
  "s_sub_u32 s65, 0, s65\n" \
  "s_add_u32 s67, s67, 4096\n" \
  "global_load_lds_dwordx4 v2, s[50:51] offset:2048 sc1\n" \
  "s_sub_u32 s68, s68, 1\n" \
  "s_mov_b32 s69, 1\n" \
  "global_load_lds_dwordx4 v2, s[50:51] offset:3072 sc1\n" \
  "s_add_u32 s50, s50, 0x40000\n" \
  "s_addc_u32 s51, s51, 0\n" \
  "v_mov_b32 v4, 0\n" \
  "buffer_load_dword v4, v4, s[44:47], s67 offen sc1\n" \
  "s_add_u32 s63, s63, s60\n" \
  "s_mov_b32 s61, 0\n" \
  "s_cmp_eq_u32 s63, 66\n" \
  "s_cselect_b32 s72, 1, 0\n" \
  "s_cbranch_scc1 .LCY_DEF_%=\n" \
  "s_waitcnt vmcnt(7)\n" \
  "s_branch .LCY_WC_%=\n" \
  ".LCY_ND_%=:\n" \
  "v_mov_b32 v4, 0\n" \
  "buffer_load_dword v4, v4, s[44:47], s67 offen sc1\n" \
  "s_waitcnt vmcnt(0)\n" \
  ".LCY_WC_%=:\n" \
  "v_mov_b32 v6, s59\n" \
  "v_mov_b32 v7, 1\n" \
  "s_mov_b64 exec, 1\n" \
  "ds_add_rtn_u32 v6, v6, v7\n" \
  "s_mov_b64 exec, -1\n" \
  "s_xor_b32 s59, s59, 4\n" \
  "s_mov_b32 s61, 0\n" \
  "s_waitcnt lgkmcnt(0)\n" \
  "v_readfirstlane_b32 s63, v6\n" \
  "v_lshlrev_b32 v6, 5, v2\n" \
  "s_and_b32 s63, s63, 7\n" \
  "s_cmp_eq_u32 s63, 7\n" \
  "s_cbranch_scc0 .LCY_W2_%=\n" \
  "s_mov_b64 exec, 15\n" \
  "global_store_dword v6, v7, s[52:53] sc1\n" \
  "s_mov_b64 exec, -1\n" \
  "s_mov_b32 s61, 1\n" \
  ".LCY_W2_%=:\n" \
  "s_add_u32 s52, s52, 2048\n" \
  "s_addc_u32 s53, s53, 0\n" \
  ".LCY_DEF_%=:\n" \
  "s_sub_u32 s60, s60, 1\n" \
  "s_cmp_eq_u32 s60, 0\n" \
  "s_cbranch_scc1 .LCY_END_%=\n" \
  ".LCY_NEED_%=:\n" \
  "s_cmp_eq_u32 s68, 0\n" \
  "s_cbranch_scc1 .LCY_BAR_%=\n" \
  "v_cmp_eq_u32 vcc, 0, v4\n" \
  "s_cmp_eq_u64 vcc, 0\n" \
  "s_cbranch_scc1 .LCY_GET_%=\n" \
  "s_cmp_eq_u32 s68, 1\n" \
  "s_cbranch_scc1 .LCY_BAR_%=\n" \
  "s_cmp_ge_u32 s67, 0x40000\n" \
  "s_cbranch_scc1 .LCY_BAR_%=\n" \
  "s_sleep 12\n" \
  ".LCY_ASK_%=:\n" \
  "v_mov_b32 v4, 0\n" \
  "buffer_load_dword v4, v4, s[44:47], s67 offen sc1\n" \
  "s_sub_u32 s62, s62, 1\n" \
  "s_cbranch_scc1 .LCY_END_%=\n" \
  "s_waitcnt vmcnt(0)\n" \
  "s_branch .LCY_NEED_%=\n" \
  ".LCY_GET_%=:\n" \
  "v_lshlrev_b32 v6, 16, s55\n" \
  "s_mov_b64 exec, 1\n" \
  "ds_add_u32 v5, v6\n" \
  "s_mov_b64 exec, -1\n" \
  "s_mov_b32 m0, s56\n" \
  "s_add_u32 s71, s56, 4096\n" \
  "global_load_lds_dwordx4 v2, s[48:49] offset:0 sc1\n" \
  "global_load_lds_dwordx4 v2, s[48:49] offset:1024 sc1\n" \
  "global_load_lds_dwordx4 v2, s[48:49] offset:2048 sc1\n" \
  "global_load_lds_dwordx4 v2, s[48:49] offset:3072 sc1\n" \
  "s_mov_b32 m0, s71\n" \
  "s_add_u32 s56, s56, s65\n" \
  "global_load_lds_dwordx4 v3, s[48:49] offset:0 sc1\n" \
  "global_load_lds_dwordx4 v3, s[48:49] offset:1024 sc1\n" \
  "global_load_lds_dwordx4 v3, s[48:49] offset:2048 sc1\n" \
  "global_load_lds_dwordx4 v3, s[48:49] offset:3072 sc1\n" \
  "s_add_u32 s48, s48, 0x80000\n" \
  "s_addc_u32 s49, s49, 0\n" \
  "s_mov_b32 m0, s57\n" \
  "s_add_u32 s57, s57, s65\n" \
  "global_load_lds_dwordx4 v2, s[50:51] offset:0 sc1\n" \
  "global_load_lds_dwordx4 v2, s[50:51] offset:1024 sc1\n" \
  "s_sub_u32 s65, 0, s65\n" \
  "s_add_u32 s67, s67, 4096\n" \
  "global_load_lds_dwordx4 v2, s[50:51] offset:2048 sc1\n" \
  "s_sub_u32 s68, s68, 1\n" \
  "s_mov_b32 s69, 1\n" \
  "global_load_lds_dwordx4 v2, s[50:51] offset:3072 sc1\n" \
  "s_add_u32 s50, s50, 0x40000\n" \
  "s_addc_u32 s51, s51, 0\n" \
  "v_mov_b32 v4, 0\n" \
  "buffer_load_dword v4, v4, s[44:47], s67 offen sc1\n" \
  "s_waitcnt vmcnt(0)\n" \
  "v_mov_b32 v6, s55\n" \
  "s_mov_b64 exec, 1\n" \
  "ds_add_u32 v5, v6\n" \
  "s_mov_b64 exec, -1\n" \
  "s_mov_b32 s69, 0\n" \
  "s_branch .LCY_NEED_%=\n" \
  ".LCY_END_%=:\n" \
  "s_endpgm\n"
template <bool AT>
__device__ __forceinline__ void role_aw_t(const MegaArgs& A, char* smem, const int L, const int a, const u64 tag) {
  const int w = A.wv;
  const int l = lane_id(), c = l & 15, g = l >> 4;
  const int qt = w & 1, hh = (w >> 1) & 1, h = 2 * a + hh;
  char* ws = A.ws;
  unsigned* FL = (unsigned*)(ws + OFF_FL);
  const unsigned sb = lds_base(smem);
  if (w == 0 && l < 8) reinterpret_cast<unsigned*>(smem + AW_CNT)[l] = 0u;
  constexpr int NP = AT ? 1 : 3;
  const int cp0 = AT ? w : 3 * w - 8;
  v8u wo[2 * NP][4];
  {
    const int q = (l >> 2) & 3, p = l & 3, f = q | ((g & 1) << 2);
    const unsigned ra = sb + (4 * g + q) * 256 + (p & 1) * 16;
#pragma unroll
    for (int r = 0; r < 2; ++r) {
#pragma unroll
      for (int k = 0; k < 2; ++k) {
        const char* base_ = (const char*)A.Wo + ((size_t)((2 * a + r) * DH + 64 * (w & 1) + 32 * k) * DM + 128 * (w >> 1)) * 2;
#pragma unroll
        for (int i = 0; i < 8; ++i) {
          const int fd = g | ((i & 1) << 2);
          glds16(base_, (unsigned)((4 * i + g) * (DM * 2) + ((c ^ (fd << 1)) << 4)), sb + w * 16384 + k * 8192 + i * 1024);
        }
      }
      SST(4 + 3 * r);
      if (r == 1) ready_wait(A, tag, ready_look(A));
      WAIT_VM(0);
      RAW_BAR();
      SST(5 + 3 * r);
#pragma unroll
      for (int k2 = 0; k2 < 2; ++k2) {
#pragma unroll
        for (int ct = 0; ct < 2 * NP; ++ct) {
          const int cp = cp0 + (ct >> 1);
          const unsigned a_ = ra + (cp >> 2) * 32768 + k2 * 16384 + ((2 * (cp & 3) + (p >> 1)) ^ f) * 32 + (ct & 1) * 8;
          const v2u r0 = trd(a_), r1 = trd(a_ + 4096), r2 = trd(a_ + 8192), r3 = trd(a_ + 12288);
          wo[ct][2 * r + k2] = v8u{r0.x, r0.y, r1.x, r1.y, r2.x, r2.y, r3.x, r3.y};
          asm volatile("" : "+v"(wo[ct][2 * r + k2]));
        }
      }
      RAW_BAR();
      SST(6 + 3 * r);
    }
  }
  WSTAMP2();
  const v4u rsW = mk_srd(ws + OFF_PW, SZ_PW);
  const v4u rsF = mk_srd(FL + FL_FQ + (L * 64 * 32 + h) * 32, (64 * 32 - h) * 128);
  const char* p0 = AT ? ws + OFF_MEMK + L * SZ_MEM + h * 2048 : ws + OFF_QK + L * SZ_QK + ((size_t)h * 16 + 8 * qt) * 1024;
  const char* p1 = AT ? (const char*)(FL + FL_FK + L * 32 + (h >> 3) * 64 + (h & 7) * 2) : ws + OFF_VF + L * SZ_O + ((size_t)h * 8 + 4 * qt) * 1024;
  const unsigned* pFW = FL + FL_FW + (L * 64 * 16 + (a >> 2)) * 32 + (a & 3);
  const unsigned tk = sb + AW_TK + hh * 24 * 1024, voff = (c >> 1) * 2048 + (c & 1) * 64 + g * 16;
  const char* dbg = ws + WS_TOTAL + A.vb * 64 * 8;
#define AW_SOPS                                                                                                                 \
  "{s[40:43]}"(rsW), "{s[44:47]}"(rsF), "{s[48:49]}"(p0), "{s[50:51]}"(p1), "{s[52:53]}"(pFW),                                   \
      "{s54}"((L * 64 * 16 + a) * 32768 + cp0 * 128), "{s55}"(1 << (8 * qt)), "{s56}"(tk + qt * 8192), "{s57}"(tk + 16384 + qt * 4096),                  \
      "{s58}"(sb + AW_CNT + 8 + 4 * hh), "{s59}"(sb + AW_CNT), "{s60}"(NSEG), "{s[36:37]}"(dbg)                                  \
      : "memory", "vcc", "scc", "s80"
  if constexpr (AT) {
    const v8u pv = {tk + l * 16, sb + AW_SO + l * 16, (unsigned)(g * 8 + (c & 7)) * 16, (unsigned)(OFF_MEMV - OFF_MEMK) + c * 32 + (g & 1) * 16,
                    voff, (unsigned)l * 16, tk + qt * 4096 + l * 16, sb + AW_SO + w * 4096 + l * 16};
    const unsigned neg = 0xff61b1e6u;
    v8u bt;
#pragma unroll
    for (int i = 0; i < 4; ++i) {
      const unsigned d = 4 * g + i > c ? neg : 0u;
      bt[i] = qt ? 0u : d;
      bt[4 + i] = qt ? d : neg;
    }
    const unsigned e = g >= 2 ? neg : 0u;
    const v4u bm = {e, e, e, e};
    asm volatile("s_mov_b64 exec, -1\ns_waitcnt vmcnt(0) lgkmcnt(0)\ns_nop 4\n" AW_ASM_X
                 ::"{v[192:199]}"(wo[0][0]), "{v[200:207]}"(wo[0][1]), "{v[208:215]}"(wo[0][2]), "{v[216:223]}"(wo[0][3]),
                 "{v[224:231]}"(wo[1][0]), "{v[232:239]}"(wo[1][1]), "{v[240:247]}"(wo[1][2]), "{v[248:255]}"(wo[1][3]),
                 "{v[0:7]}"(pv), "{v[8:15]}"(bt), "{v[16:19]}"(bm), AW_SOPS);
  } else {
    const v4u pv = {sb + AW_SO + l * 16, voff, (unsigned)l * 16, 0u};
    asm volatile("s_mov_b64 exec, -1\ns_waitcnt vmcnt(0) lgkmcnt(0)\ns_nop 4\n" AW_ASM_Y
                 ::"{v[64:71]}"(wo[0][0]), "{v[72:79]}"(wo[0][1]), "{v[80:87]}"(wo[0][2]), "{v[88:95]}"(wo[0][3]),
                 "{v[96:103]}"(wo[1][0]), "{v[104:111]}"(wo[1][1]), "{v[112:119]}"(wo[1][2]), "{v[120:127]}"(wo[1][3]),
                 "{v[128:135]}"(wo[2][0]), "{v[136:143]}"(wo[2][1]), "{v[144:151]}"(wo[2][2]), "{v[152:159]}"(wo[2][3]),
                 "{v[160:167]}"(wo[3][0]), "{v[168:175]}"(wo[3][1]), "{v[176:183]}"(wo[3][2]), "{v[184:191]}"(wo[3][3]),
                 "{v[192:199]}"(wo[4][0]), "{v[200:207]}"(wo[4][1]), "{v[208:215]}"(wo[4][2]), "{v[216:223]}"(wo[4][3]),
                 "{v[224:231]}"(wo[5][0]), "{v[232:239]}"(wo[5][1]), "{v[240:247]}"(wo[5][2]), "{v[248:255]}"(wo[5][3]),
                 "{v[0:3]}"(pv), AW_SOPS);
  }
#undef AW_SOPS
}
__device__ __forceinline__ void role_aw(const MegaArgs& A, char* smem, const int L, const int a, const u64 tag) {
  if (A.wv < 4) role_aw_t<true>(A, smem, L, a, tag);
  else role_aw_t<false>(A, smem, L, a, tag);
}

constexpr int RG = 2, RW = 2;
__device__ __forceinline__ void role_red(const MegaArgs& A, char* smem, const int L, const int idx, const u64 tag) {
  const int w = A.wv;
  const int l = lane_id();
  const int gr = (w >> 1) & 1, r = idx * 2 + (w & 1);
  char* ws = A.ws;
  unsigned* FL = (unsigned*)(ws + OFF_FL);
  unsigned* TMO = FL + FL_TMO;
  const __amdgpu_buffer_rsrc_t rsW = MKRS(ws + OFF_PW, SZ_PW);
  const __amdgpu_buffer_rsrc_t rsX = MKRS(ws + (L ? OFF_X3 : OFF_XA), SZ_X);
  const __amdgpu_buffer_rsrc_t rsY = L ? MKRS(A.out, SZ_X) : MKRS(ws + OFF_X2, SZ_X);
  volatile unsigned* sw = reinterpret_cast<volatile unsigned*>(smem) + (w >> 1) * 32;
  if (w < 2) reinterpret_cast<volatile unsigned*>(smem)[64 * w + l] = 0u;
  __syncthreads();
  if ((EXP_OFF & 2) && L == 0 && w < 4) return;
  if ((EXP_OFF & 64) && L == 1 && w < 4) return;
  if ((EXP_OFF & 8) && L == 1 && w >= 4) return;
  unsigned tl = now10();
  if (w >= 4) return;
  ready_wait(A, tag, ready_look(A));
  if (L == 0) {
    for (int s = gr; s < NSEG; s += RG) ST128((v4u{0u, 0u, 0u, 0u}), rsY, l * 16, (s * SEG + r) * DM * 2);
    DRAIN();
    if (l < 8) set_flag(FL + FL_FZ + (2 * l + (idx >> 3)) * 32 + (idx & 7) * 4 + w);
  }
  for (int s = gr; s < NSEG; s += RG) {
    grp_wait<SLP_R, 4, GATE_R>((w & 1) == 0, sw, s + 1, FL + FL_FW + ((L * 64 + s) * 4 + (blockIdx.x & 3)) * 4 * 32, 16, TMO, tl);
    if (s >= NSEG - 6) tl = now10();
    TS(0, s, gr * RW * 64);
    TS(4, s, gr * RW * 64 + (RW - 1) * 64);
    v4u p[16];
    const v4u xr = LD128(rsX, l * 512, s * 32768 + r * 16);
#pragma unroll
    for (int k = 0; k < 16; ++k)
      p[k] = LD128(rsW, (l >> 2) * 128 + (l & 3) * 16, ((L * 64 + s) * 16 + k) * 32768 + (r >> 1) * 2048 + (r & 1) * 64);
    v4u xo;
#pragma unroll
    for (int e = 0; e < 4; ++e) {
      f32x2_t t[4];
#pragma unroll
      for (int k = 0; k < 4; ++k)
        t[k] = (f32x2_t{lo16(p[4 * k][e]), hi16(p[4 * k][e])} + f32x2_t{lo16(p[4 * k + 1][e]), hi16(p[4 * k + 1][e])}) +
               (f32x2_t{lo16(p[4 * k + 2][e]), hi16(p[4 * k + 2][e])} + f32x2_t{lo16(p[4 * k + 3][e]), hi16(p[4 * k + 3][e])});
      const f32x2_t u = (t[0] + t[1]) + (t[2] + t[3]);
      const unsigned a0 = pk2(u.x, u.y);
      xo[e] = pk2(lo16(xr[e]) + lo16(a0), hi16(xr[e]) + hi16(a0));
      if (L == 0) xo[e] = xo[e] ? xo[e] : 0x8000u;
    }
    ST128(xo, rsY, l * 16, (s * SEG + r) * DM * 2);
    TS(1, s, gr * RW * 64);
    TS(5, s, gr * RW * 64 + (RW - 1) * 64);
    if (L == 0) {
      const unsigned st = row_stats(xo);
      if (l < 4) __hip_atomic_store((gu32*)(FL + FL_FR + ((s * 4 + l) * 4 + (r >> 3)) * 32 + (r & 7)), st, RLX_AGENT);
    }
    TS(6, s, gr * RW * 64 + (RW - 1) * 64);
    TS(3, s, gr * RW * 64);
  }
  if (L == 1 && idx == 0 && w == 0 && l == 0) __hip_atomic_store((gu64*)(ws + OFF_EP), tag, RLX_AGENT);
}

constexpr int CUN = 2144;
__device__ __forceinline__ v2u part_stats(const v4u o) {
  float v[8];
#pragma unroll
  for (int e = 0; e < 4; ++e) { v[2 * e] = lo16(o[e]); v[2 * e + 1] = hi16(o[e]); }
  const float s = ((v[0] + v[1]) + (v[2] + v[3])) + ((v[4] + v[5]) + (v[6] + v[7])), m = s * 0.125f;
  float q0 = 0.f, q1 = 0.f;
#pragma unroll
  for (int e = 0; e < 4; ++e) {
    const float a = v[e] - m, b = v[4 + e] - m;
    q0 += a * a;
    q1 += b * b;
  }
  return v2u{__float_as_uint(s), __float_as_uint(q0 + q1)};
}
constexpr int CRN = 176;
constexpr int CV_LDS = (8 * 2 * CKN + 8 * 2 * CUN + 64 + 2 * 32 * 8 + 16 * CRN + 64 + 256 * 8) * 2 + 8 * 1568;
__device__ __forceinline__ void role_conv(const MegaArgs& A, char* smem, const int cv) {
  u16* Kr = reinterpret_cast<u16*>(smem);
  u16* Uc = Kr + 8 * 2 * CKN;
  u16* Us = Uc + 8 * CUN + 63;
  u16* To = Uc + 8 * 2 * CUN + 64;
  u16* Sx = To + 32 * 8;
  u16* Rc = Sx + 32 * 8;
  u16* Rs = Rc + 8 * CRN + 63;
  u16* Ta = Rc + 16 * CRN + 64;
  const int w = A.wv;
  const int l = lane_id(), tid = 64 * w + l, c = l & 15, g = l >> 4;
  const int d = 8 * cv + w;
  char* ws = A.ws;
  unsigned* FL = (unsigned*)(ws + OFF_FL);
  unsigned* TMO = FL + FL_TMO;
  u16* kr = Kr + w * 2 * CKN;
  const u16* ka = kr + (c >> 3) * CKN + 2080 - 16 * (c & 7) + 8 * g;
#define KFRAG(D0) (*reinterpret_cast<const bf16x8*>(ka - (D0)))
  typedef __attribute__((address_space(3))) const unsigned* lpu;
#define BFRAG(a) BF8((v4u{((lpu)(unsigned long)(a))[0], ((lpu)(unsigned long)(a))[1], ((lpu)(unsigned long)(a))[2], ((lpu)(unsigned long)(a))[3]}))
  f32x4 acc[16], far[8];
  v4u tx[4];
  const __amdgpu_buffer_rsrc_t rz1 = MKRS(ws + OFF_XA, SZ_X), rps = MKRS(ws + OFF_H, SZ_X);
  u16* uc = Uc + w * CUN;
  const unsigned ub = lds_base(reinterpret_cast<char*>(((c & 1) ? Us : Uc) + w * CUN + c + 8 * g + 32));
  const int t0 = 16 * (4 * (g & 1) + (g >> 1)) + c;
  float dsk;
#define SKIP(T) pk2(bf2f(uc[32 + 128 * (T) + t0]) * dsk, bf2f(uc[64 + 128 * (T) + t0]) * dsk)
#define TILE_OUT(T, a, uu)                                                                     \
  {                                                                                            \
    const unsigned yy = pk2(swap_add32(a[0], a[1]), swap_add32(a[2], a[3]));                   \
    const unsigned v_ = pk2(lo16(yy) + lo16(uu), hi16(yy) + hi16(uu));                         \
    Ta[(128 * ((T) & 7) + t0) * 8 + w] = (u16)v_;                                              \
    Ta[(128 * ((T) & 7) + t0 + 32) * 8 + w] = (u16)(v_ >> 16);                                 \
  }
#define CHUNK_ST(i)                                                                            \
  {                                                                                            \
    const v4u a_ = *reinterpret_cast<const v4u*>(Ta + (64 * w + l + 512 * ((i) & 1)) * 8);     \
    v4u o;                                                                                     \
    _Pragma("unroll") for (int e = 0; e < 4; ++e) o[e] = pk2(lo16(tx[i][e]) + lo16(a_[e]), hi16(tx[i][e]) + hi16(a_[e])); \
    ST128(o, rz1, (l >> 5) * 32768 + (l & 31) * 16, (2 * (w + 8 * (i)) * 64 + cv) * 512);      \
    ST64(part_stats(o), rps, (l >> 5) * 16384 + (l & 31) * 8, (2 * (w + 8 * (i)) * 64 + cv) * 256); \
  }
#define CHUNK_FLAG(i) set_flag(FL + FL_FC + (((2 * w + 16 * ((i) + (l >> 3)) + ((l >> 2) & 1)) * 4 + (l & 3)) * 8 + (cv >> 3)) * 32 + (cv & 7))
  {
    unsigned ta[3], k0p;
    v4u kn;
    u64 tag;
    {
      KGen G;
      G.load(A, d, l);
      const u16 pk = A.Dskip[d];
      asm volatile("" ::: "memory");
      tx[0] = *reinterpret_cast<const v4u*>(A.x0 + (size_t)tid * DM + 8 * cv);
      ask_slow(A, 16 * (w - 4) + (cv & 15), ta);
      tag = call_tag(A);
      G.setup();
      dsk = bf2f(pk);
      G.st = smem + w * 16640;
      SST(0);
      G.columns(l);
      const Cx z = G.power(1.f);
      G.rows(G.st + l * 8, cmul(G.C, z), z, 16);
      kn = G.tile(l);
      const float k0 = 2.0f * wave_sum(G.C.r.x + G.C.r.y);
      k0p = l == 0 ? pk2(k0, k0 - lo16(pk2(k0, k0))) : 0u;
    }
    SST(1);
    const __amdgpu_buffer_rsrc_t rst = MKRS(ws + OFF_GT, L_SEQ * 16);
#define U_VAL(i)                                                                               \
  {                                                                                            \
    const float mb_ = lo16(st[i]), ri_ = 1.0f / hi16(st[i]);                                   \
    _Pragma("unroll") for (int e = 0; e < 4; ++e) {                                            \
      const unsigned t_ = pk2(lo16(tx[i][e]) - mb_, hi16(tx[i][e]) - mb_);                     \
      uv[e] = pk2(lo16(t_) * ri_, hi16(t_) * ri_);                                             \
    }                                                                                          \
  }
#define U_PUT(i)                                                                               \
  _Pragma("unroll") for (int e = 0; e < 4; ++e) {                                              \
    ((lph)(unsigned long)up0)[(2 * e) * CUN + 512 * (i)] = (u16)uv[e];                         \
    ((lph)(unsigned long)up0)[(2 * e + 1) * CUN + 512 * (i)] = (u16)(uv[e] >> 16);             \
    ((lph)(unsigned long)up1)[(2 * e) * CUN + 512 * (i)] = (u16)uv[e];                         \
    ((lph)(unsigned long)up1)[(2 * e + 1) * CUN + 512 * (i)] = (u16)(uv[e] >> 16);             \
  }
#define U_ROW(i) { U_VAL(i) U_PUT(i) }
    typedef __attribute__((address_space(3))) u16* lph;
    unsigned st[4], up0 = lds_base((char*)(Uc + 32 + tid)), up1 = lds_base((char*)(Us + 32 + tid));
    asm volatile("" : "+v"(up0), "+v"(up1), "+v"(kn), "+v"(k0p));
    v4u uv;
#define ST_GET(I0, I1)                                                                                                        \
  for (unsigned spins = 0; spins < SPIN_CAP; ++spins) {                                                                       \
    bool ok = true;                                                                                                           \
    v4u t_[4];                                                                                                                \
    _Pragma("unroll") for (int i = I0; i < I1; ++i) t_[i] = LD128(rst, (tid + 512 * i) * 16, 0);                              \
    _Pragma("unroll") for (int i = I0; i < I1; ++i) {                                                                         \
      ok = ok && has_tag(tag, t_[i].x, t_[i].y) && has_tag(tag, t_[i].z, t_[i].w);                                            \
      st[i] = __builtin_amdgcn_perm(t_[i].w, t_[i].y, 0x07060302);                                                            \
    }                                                                                                                         \
    if (__all(ok)) break;                                                                                                     \
    __builtin_amdgcn_s_sleep(4);                                                                                              \
    asm volatile("" ::: "memory");                                                                                            \
  }
    if (w < 4) {
      ST_GET(0, 4);
      U_VAL(0);
      asm volatile("" : "+v"(uv));
    }
#pragma unroll
    for (int i = 1; i < 4; ++i) tx[i] = *reinterpret_cast<const v4u*>(A.x0 + (size_t)(tid + 512 * i) * DM + 8 * cv);
    RAW_BAR();
    SST(2);
    if (w < 4) { U_PUT(0) }
    *reinterpret_cast<v2u*>(kr + 2076 - 16 * c - 4 * g) = v2u{__builtin_amdgcn_perm(kn.z, kn.w, 0x05040100), __builtin_amdgcn_perm(kn.x, kn.y, 0x05040100)};
    *reinterpret_cast<v2u*>(kr + CKN + 2076 - 16 * c - 4 * g) = v2u{__builtin_amdgcn_perm(kn.z, kn.w, 0x07060302), __builtin_amdgcn_perm(kn.x, kn.y, 0x07060302)};
    if (l < 18) {
      reinterpret_cast<v4u*>(kr)[l < 16 ? 260 + l : 210 + l] = v4u{k0p & 0xffffu, 0u, 0u, 0u};
      reinterpret_cast<v4u*>(kr + CKN)[l < 16 ? 260 + l : 210 + l] = v4u{k0p >> 16, 0u, 0u, 0u};
    }
    (l < 32 ? uc : Us + w * CUN - 32)[l] = 0;
    uc[32 + L_SEQ + l] = Us[w * CUN + 32 + L_SEQ + l] = 0;
    uc[32 + 256 + l] = Us[w * CUN + 32 + 256 + l] = 0;
    RAW_BAR();
    SST(11);
    {
      f32x4 a0 = {}, a1 = {}, a2 = {};
      bf16x8 bf[9], af[9];
#pragma unroll
      for (int b = 0; b < 9; ++b) {
        af[(b + 4) % 9] = KFRAG(160 - 32 * ((b + 4) % 9));
        bf[b] = BFRAG(ub + 64 * (b - 1));
      }
      const unsigned k0 = SKIP(0), k1 = SKIP(1);
      __builtin_amdgcn_sched_barrier(0);
#pragma unroll
      for (int b = 0; b < 5; ++b) a0 = MFMA16(af[b + 4], bf[b], a0);
#pragma unroll
      for (int b = 0; b < 4; ++b) {
        a2 = MFMA16(af[b + 4], bf[b + 4], a2);
        a1 = MFMA16(af[b], bf[b], a1);
      }
      a2 = MFMA16(af[8], bf[8], a2);
      TILE_OUT(0, a0, k0);
      a1 += a2;
      TILE_OUT(1, a1, k1);
    }
    const u64 rv = ready_look(A);
    RAW_BAR();
    SST(3);
    if (w < 4) {
      CHUNK_ST(0);
      asm volatile("" : "+v"(tx[1]));
      U_ROW(1);
      DRAIN();
      ready_wait(A, tag, rv);
      if (l < 8) CHUNK_FLAG(0);
    } else {
      ST_GET(0, 4);
      ready_wait(A, tag, rv);
      asm volatile("" : "+v"(tx[0]), "+v"(tx[1]));
      U_ROW(0);
      U_ROW(1);
    }
    ASKED(ta);
    if (tid < (16 * CRN + 64) / 8) reinterpret_cast<v4u*>(Rc)[tid] = v4u{0u, 0u, 0u, 0u};
    SST(4);
    const gu64* fk = (gu64*)(ws + OFF_KG) + cv * 16 + w;
    u64 kg = __hip_atomic_load(fk, RLX_AGENT);
    U_ROW(2);
    for (unsigned spins = 0; !__all(kg == tag) && spins < SPIN_CAP; ++spins) {
      __builtin_amdgcn_s_sleep(8);
      kg = __hip_atomic_load(fk, RLX_AGENT);
    }
    asm volatile("" : "+v"(tx[3]), "+v"(st[3])::"memory");
    SST(5);
    const unsigned* kd = reinterpret_cast<const unsigned*>(Ta + 256 * 8) + w * 392;
    glds16c(ws + OFF_KF + d * 2048, l * 16, lds_base((char*)kd));
    if (l < 34) glds16c(ws + OFF_KF + d * 2048 + 1024, l * 16, lds_base((char*)kd) + 1024);
    U_ROW(3);
    DRAIN();
    SST(6);
#pragma unroll 1
    for (int un = l; un < 228; un += 64) {
      const int kc = un < 132 ? 2 : un < 196 ? 1 : 0;
      v4u oh, ol;
#pragma unroll
      for (int e = 0; e < 4; ++e) {
        unsigned kv[2];
#pragma unroll
        for (int k = 0; k < 2; ++k) {
          const int lag = 2080 - (8 * un + 2 * e + k);
          const int q = (lag + (1 << kc) - 1 + ((lag >> (kc + 1)) & 1)) >> (kc + 1);
          kv[k] = kd[128 * kc - 128 + q];
          if (lag >= L_SEQ) kv[k] = 0u;
        }
        oh[e] = __builtin_amdgcn_perm(kv[1], kv[0], 0x05040100);
        ol[e] = __builtin_amdgcn_perm(kv[1], kv[0], 0x07060302);
      }
      reinterpret_cast<v4u*>(kr)[un] = oh;
      reinterpret_cast<v4u*>(kr + CKN)[un] = ol;
    }
    RAW_BAR();
    SST(7);
    {
      unsigned uq[2] = {ub, ub + 1024};
      bf16x8 bf[8], a[4];
      const bf16x8 bm = BFRAG(ub - 64);
      asm volatile("" : "+v"(uq[0]), "+v"(uq[1]));
#pragma unroll
      for (int j = 1; j < 8; ++j) bf[j] = BFRAG(uq[j >> 2] + 256 * (j & 3));
#pragma unroll
      for (int T = 2; T < 16; ++T) (T < 8 ? acc[T] : far[T & 7]) = MFMA16(KFRAG(128 * T + 32), bm, (f32x4{}));
#pragma unroll
      for (int e = 0; e < 3; ++e) a[e] = KFRAG(128 * e);
      const u16* kq = ka - 128 * 7;
#pragma unroll 1
      for (int r = 0; r < 4; ++r) {
#pragma unroll
        for (int e = 0; e < 8; ++e) {
          a[(e + 3) & 3] = *reinterpret_cast<const bf16x8*>(kq + 128 * (7 - ((e + 3) & 7)) + (e >= 5 ? 32 : 0));
          const int j = (8 - e) & 7;
          bf[j] = BFRAG(uq[j >> 2] + 256 * (j & 3) + (e ? 64 : 0));
          __builtin_amdgcn_sched_barrier(0);
#pragma unroll
          for (int T = 7; T >= (e > 2 ? e : 2); --T) acc[T] = MFMA16(a[e & 3], bf[T - e], acc[T]);
          __builtin_amdgcn_sched_barrier(0);
        }
        kq += 32;
        uq[0] += 64;
        uq[1] += 64;
        asm volatile("" : "+v"(uq[0]), "+v"(uq[1]));
      }
    }
    SST(8);
    {
      unsigned k[8];
#pragma unroll
      for (int T = 2; T < 8; ++T) k[T] = SKIP(T);
#pragma unroll
      for (int T = 2; T < 8; ++T) TILE_OUT(T, acc[T], k[T]);
    }
    RAW_BAR();
    SST(10);
    if (w >= 4) CHUNK_ST(0);
    CHUNK_ST(1);
    if (w == 1) (void)wait_n<SLP_V, 32>(FL + FL_FZ + (blockIdx.x & 7) * 64, 64, TMO);
    DRAIN();
    if (l < 16) CHUNK_FLAG(0);
    SST(9);
#undef U_ROW
#undef U_VAL
#undef U_PUT
#undef ST_GET
  }
#define FAR_HALF(r, J0, J1)                                                                     \
  {                                                                                            \
    bf16x8 bf[16], af[16];                                                                     \
    unsigned u_ = ub + 64 * (r) + 256 * (J0), v_ = u_ + 1024;                                  \
    asm volatile("" : "+v"(u_), "+v"(v_));                                                     \
    _Pragma("unroll") for (int j = (J0); j < (J1); ++j) bf[j] = BFRAG((j - (J0) < 4 ? u_ : v_) + 256 * ((j - (J0)) & 3)); \
    _Pragma("unroll") for (int e = (9 - (J1) > 0 ? 9 - (J1) : 0); e < 16 - (J0); ++e) af[e] = KFRAG(128 * e - 32 * (r)); \
    __builtin_amdgcn_sched_barrier(0);                                                         \
    _Pragma("unroll") for (int j = (J1) - 1; j >= (J0); --j)                                   \
      _Pragma("unroll") for (int T = (j > 8 ? j : 8); T < 16; ++T) far[T - 8] = MFMA16(af[T - j], bf[j], far[T - 8]); \
  }
#define FAR_PIECE(p)                                                                            \
  {                                                                                            \
    int l = tid & 63;                                                                          \
    asm volatile("" : "+v"(l), "+v"(tx[2]), "+v"(tx[3]));                                      \
    if ((p) < 16) {                                                                            \
      const int r_ = (p) >> 2;                                                                 \
      if (((p) & 3) == 0) FAR_HALF(r_, 0, 3) else if (((p) & 3) == 1) FAR_HALF(r_, 3, 6)       \
      else if (((p) & 3) == 2) FAR_HALF(r_, 6, 9) else FAR_HALF(r_, 9, 16)                     \
    } else if ((p) == 16) {                                                                    \
      unsigned k_[8];                                                                          \
      _Pragma("unroll") for (int T = 0; T < 8; ++T) k_[T] = SKIP(8 + T);                       \
      _Pragma("unroll") for (int T = 0; T < 8; ++T) TILE_OUT(T, far[T], k_[T]);                \
    } else if ((p) == 17) {                                                                    \
      CHUNK_ST(2);                                                                             \
    } else if ((p) == 18) {                                                                    \
      CHUNK_ST(3);                                                                             \
    } else {                                                                                   \
      DRAIN();                                                                                 \
      if (l < 16) CHUNK_FLAG(2);                                                               \
    }                                                                                          \
  }
  static_assert(CVPRE <= 16, "the later pieces need the barriers of the segments");
  const unsigned rb = lds_base(reinterpret_cast<char*>(((c & 1) ? Rs : Rc) + w * CRN + c + 8 * g));
#pragma unroll
  for (int j = 0; j < 16; ++j) acc[j] = f32x4{};
  const __amdgpu_buffer_rsrc_t rx = MKRS(ws + OFF_X2, SZ_X), rz = MKRS(ws + OFF_X3, SZ_X);
  const __amdgpu_buffer_rsrc_t rf = MKRS(FL + FL_FR, 64 * 16 * 128);
  unsigned st = 0u;
  v2u xx = {0u, 0u};
#define X2_LOAD(ss, far)                                                                                                      \
  {                                                                                                                      \
    xx = LD64(rx, (l >> 1) * (DM * 2) + (l & 1) * 8, (far) ? (int)SZ_X : ((ss) * SEG * DM + 8 * cv) * 2);                 \
    st = __builtin_amdgcn_raw_buffer_load_b32(rf, (l >> 4) * 128 + ((l >> 1) & 7) * 4, ((ss) * 4 + (blockIdx.x & 3)) * 512, AUX_SC1); \
  }
  unsigned tl = now10();
  for (int s = -CVPRE; s < NSEG; ++s) {
   if (s >= 0) {
    const int q = s & 3;
    if (w == 1) {
      for (unsigned spins = 0; !__all(umin3(xx.x, xx.y, st) != 0u); ++spins) {
        if (spin_timeout(spins, TMO)) break;
        if (GATE_V && spins == 0 && !__any(st != 0u)) gate(tl);
        if (spins && !__all(st != 0u)) __builtin_amdgcn_s_sleep(SLP_V);
        X2_LOAD(s, __builtin_popcountll(__ballot(st != 0u)) < CVTHR);
      }
      tl = now10() + GATE_V;
      TS(0, s, 64);
      const float mb = lo16(st), ri = 1.0f / hi16(st);
      const unsigned t0 = pk2(lo16(xx.x) - mb, hi16(xx.x) - mb), t1 = pk2(lo16(xx.y) - mb, hi16(xx.y) - mb);
      const unsigned u0 = pk2(lo16(t0) * ri, hi16(t0) * ri), u1 = pk2(lo16(t1) * ri, hi16(t1) * ri);
      u16* rp = Rc + (l & 1) * 4 * CRN + 32 * q + (l >> 1);
#define R_PUT(o) { rp[o] = (u16)u0; rp[(o) + CRN] = (u16)(u0 >> 16); rp[(o) + 2 * CRN] = (u16)u1; rp[(o) + 3 * CRN] = (u16)(u1 >> 16); }
      R_PUT(0) R_PUT(8 * CRN + 63)
      if (q == 0) { R_PUT(128) R_PUT(8 * CRN + 63 + 128) }
#undef R_PUT
      *reinterpret_cast<v2u*>(Sx + l * 4) = xx;
    }
    const u16* kq = ka + 32 * q;
    const bf16x8 a0 = *reinterpret_cast<const bf16x8*>(kq - 32), a1 = *reinterpret_cast<const bf16x8*>(kq);
    __syncthreads();
    TS(1, s, 0);
    const bf16x8 bp = BFRAG(rb + 64 * ((q + 3) & 3)), bc = BFRAG(rb + 64 * q);
    const float u0 = bf2f(Rc[w * CRN + 32 * q + c]), u1 = bf2f(Rc[w * CRN + 32 * q + 16 + c]);
    const float x0 = bf2f(Sx[c * 8 + w]), x1 = bf2f(Sx[(16 + c) * 8 + w]);
    acc[0] = MFMA16(a0, bp, (q == 0 ? acc[1] : acc[0]));
    const f32x4 tmp = MFMA16(a1, bc, acc[0]);
    const unsigned uu = pk2(u0 * dsk, u1 * dsk);
    const unsigned yy = pk2(xsum32((q & 1) ? tmp[2] : tmp[0]), xsum32((q & 1) ? tmp[3] : tmp[1]));
    const unsigned aa = pk2(lo16(yy) + lo16(uu), hi16(yy) + hi16(uu)), oo = pk2(x0 + lo16(aa), x1 + hi16(aa));
    if (g == (q >> 1)) {
      To[c * 8 + w] = (u16)oo;
      To[(16 + c) * 8 + w] = (u16)(oo >> 16);
    }
    __syncthreads();
    if (w == 0 && l < 32) {
      const v4u o = *reinterpret_cast<const v4u*>(To + l * 8);
      ST128(o, rz, l * 16, (s * 64 + cv) * 512);
      ST64(part_stats(o), rps, l * 8, ((64 + s) * 64 + cv) * 256);
    }
    if (w == 1) X2_LOAD(s + 1, CVTHR > 0);
    const int left = 15 - (s >> 2);
#define PUSH(J0, SH)                                                                                              \
  if (left >= (J0)) {                                                                                             \
    _Pragma("unroll") for (int j = (J0); j < (J0) + 4 && j < 16; ++j)                                             \
      acc[j] = MFMA16(*reinterpret_cast<const bf16x8*>(kq - 32 - 128 * j), bp, (j + (SH) < 16 ? acc[(j + (SH)) & 15] : f32x4{})); \
  }
    TS(2, s, 0);
    if (w == 0) {
      DRAIN();
      if (l < 4) set_flag(FL + FL_FC + (((64 + s) * 4 + l) * 8 + (cv >> 3)) * 32 + (cv & 7));
    }
    TS(3, s, 0);
    if (q == 0) { PUSH(1, 1) PUSH(5, 1) PUSH(9, 1) PUSH(13, 1) }
    else { PUSH(1, 0) PUSH(5, 0) PUSH(9, 0) PUSH(13, 0) }
#undef PUSH
    MSTAMP();
   }
    if (s < 20 - CVPRE) FAR_PIECE(s + CVPRE);
  }
#undef KFRAG
#undef BFRAG
#undef X2_LOAD
#undef FAR_PIECE
#undef FAR_HALF
#undef TILE_OUT
#undef SKIP
#undef CHUNK_ST
#undef CHUNK_FLAG
}

__device__ __forceinline__ v4u ln_row_packed(const v4u xr) {
  float v[8];
#pragma unroll
  for (int e = 0; e < 4; ++e) { v[2 * e] = lo16(xr[e]); v[2 * e + 1] = hi16(xr[e]); }
  float s = ((v[0] + v[1]) + (v[2] + v[3])) + ((v[4] + v[5]) + (v[6] + v[7]));
  s = wave_sum(s);
  const float mean = s * (1.0f / DM);
  float q = 0.f;
#pragma unroll
  for (int j = 0; j < 8; ++j) { float t = v[j] - mean; q += t * t; }
  q = wave_sum(q);
  const float var = q * (1.0f / DM);
  const float mb = bfr(mean);
  const float sd = bfr(sqrtf(bfr(bfr(var) + 1e-5f)));
  const float ri = 1.0f / sd;
  v4u o;
#pragma unroll
  for (int e = 0; e < 4; ++e) {
    const unsigned t = pk2(v[2 * e] - mb, v[2 * e + 1] - mb);
    const float x0 = lo16(t), x1 = hi16(t);
    float q0 = x0 * ri, q1 = x1 * ri;
    q0 = __builtin_fmaf(__builtin_fmaf(-q0, sd, x0), ri, q0);
    q1 = __builtin_fmaf(__builtin_fmaf(-q1, sd, x1), ri, q1);
    o[e] = pk2(q0, q1);
  }
  return o;
}

__device__ __forceinline__ float bfr1(float x) { return lo16(pk2(x, x)); }
__device__ __forceinline__ void ln4_packed(v4u (&x)[4]) {
  f32x2_t v[16];
#pragma unroll
  for (int e = 0; e < 16; ++e) v[e] = f32x2_t{lo16(x[e >> 2][e & 3]), hi16(x[e >> 2][e & 3])};
  const f32x2_t t = (((v[0] + v[1]) + (v[2] + v[3])) + ((v[4] + v[5]) + (v[6] + v[7]))) +
                    (((v[8] + v[9]) + (v[10] + v[11])) + ((v[12] + v[13]) + (v[14] + v[15])));
  const float mean = red_mod4(t.x + t.y) * (1.0f / DM);
  const float mb = bfr1(mean);
  f32x2_t q0 = {0.f, 0.f}, q1 = {0.f, 0.f};
#pragma unroll
  for (int e = 0; e < 16; e += 2) {
    v[e] -= mb;
    v[e + 1] -= mb;
    q0 += v[e] * v[e];
    q1 += v[e + 1] * v[e + 1];
  }
  q0 += q1;
  const float dm = mean - mb;
  const float var = fmaxf(red_mod4(q0.x + q0.y) * (1.0f / DM) - dm * dm, 0.f);
  const float sd = bfr1(sqrtf(bfr1(bfr1(var) + 1e-5f)));
  const float ri = 1.0f / sd;
#pragma unroll
  for (int e = 0; e < 16; ++e) {
    const unsigned t2 = pk2(v[e].x, v[e].y);
    const f32x2_t q = f32x2_t{lo16(t2), hi16(t2)} * ri;
    x[e >> 2][e & 3] = pk2(q.x, q.y);
  }
}

#define Q_ASM \
  "v_readfirstlane_b32 s78, v6\n" \
  "s_mov_b32 s79, 0\n" \
  "s_mov_b32 s81, 0\n" \
  "s_mov_b32 s83, 0x10000\n" \
  "s_mov_b32 s76, 0\n" \
  "s_mov_b32 s77, -1\n" \
  "s_cmp_lg_u32 s78, 0\n" \
  "s_cbranch_scc1 .LCQ_GOT_%=\n" \
  "s_branch .LCQ_POLL_%=\n" \
  ".LCQ_TOP_%=:\n" \
  "s_waitcnt lgkmcnt(0)\n" \
  "s_barrier\n" \
  "s_sub_i32 s83, 0, s83\n" \
  "v_add_u32 v0, s83, v0\n" \
  "ds_read_b128 v[18:21], v0 offset:0\n" \
  "ds_read_b128 v[22:25], v0 offset:2048\n" \
  "ds_read_b128 v[26:29], v0 offset:4096\n" \
  "ds_read2_b32 v[6:7], v3 offset0:16 offset1:48\n" \
  "ds_read_b128 v[10:13], v4 offset:256\n" \
  "ds_read_b128 v[14:17], v4 offset:320\n" \
  "ds_read_b32 v8, v3 offset:384\n" \
  "s_add_u32 s71, s71, 0x1000\n" \
  "s_mov_b32 s78, s80\n" \
  "s_xor_b32 s74, s74, 2048\n" \
  "s_xor_b32 s75, s75, 2048\n" \
  "s_setprio 3\n" \
  "ds_read_b128 v[30:33], v0 offset:6144\n" \
  "s_waitcnt lgkmcnt(7)\n" \
  "v_mfma_f32_16x16x32_bf16 v[34:37], v[64:67], v[18:21], 0\n" \
  "v_mfma_f32_16x16x32_bf16 v[38:41], v[128:131], v[18:21], 0\n" \
  "v_mfma_f32_16x16x32_bf16 v[42:45], v[18:21], v[192:195], 0\n" \
  "s_cmp_eq_u32 s78, 1\n" \
  "s_cbranch_scc0 .LCQ_X1_%=\n" \
  "s_mov_b32 m0, s73\n" \
  "s_nop 0\n" \
  "global_load_lds_dwordx4 v2, s[62:63] offset:0 sc1\n" \
  "global_load_lds_dwordx4 v2, s[62:63] offset:1024 sc1\n" \
  "s_add_u32 s62, s62, 0x4000\n" \
  "s_addc_u32 s63, s63, 0\n" \
  ".LCQ_X1_%=:\n" \
  "ds_read_b128 v[18:21], v0 offset:16384\n" \
  "s_waitcnt lgkmcnt(7)\n" \
  "v_mfma_f32_16x16x32_bf16 v[34:37], v[68:71], v[22:25], v[34:37]\n" \
  "s_waitcnt lgkmcnt(4)\n" \
  "v_fma_f32 v48, -v6, v10, v48\n" \
  "v_fma_f32 v49, -v6, v11, v49\n" \
  "v_mfma_f32_16x16x32_bf16 v[38:41], v[132:135], v[22:25], v[38:41]\n" \
  "v_fma_f32 v50, -v6, v12, v50\n" \
  "v_fma_f32 v51, -v6, v13, v51\n" \
  "v_mfma_f32_16x16x32_bf16 v[42:45], v[22:25], v[196:199], v[42:45]\n" \
  "ds_read_b128 v[10:13], v4 offset:64\n" \
  "s_waitcnt lgkmcnt(4)\n" \
  "v_fma_f32 v54, -v6, v14, v54\n" \
  "v_fma_f32 v55, -v6, v15, v55\n" \
  "s_cmp_eq_u32 s78, 1\n" \
  "s_cbranch_scc0 .LCQ_X2_%=\n" \
  "s_mov_b32 m0, s68\n" \
  "s_xor_b32 s68, s68, 0x10000\n" \
  "global_load_lds_dwordx4 v2, s[60:61] offset:0 sc1\n" \
  "global_load_lds_dwordx4 v2, s[60:61] offset:1024 sc1\n" \
  ".LCQ_X2_%=:\n" \
  "ds_read_b128 v[22:25], v0 offset:18432\n" \
  "v_mfma_f32_16x16x32_bf16 v[34:37], v[72:75], v[26:29], v[34:37]\n" \
  "v_fma_f32 v56, -v6, v16, v56\n" \
  "v_fma_f32 v57, -v6, v17, v57\n" \
  "v_mfma_f32_16x16x32_bf16 v[38:41], v[136:139], v[26:29], v[38:41]\n" \
  "ds_read_b128 v[14:17], v4 offset:192\n" \
  "v_mul_f32 v48, v48, v7\n" \
  "v_mul_f32 v49, v49, v7\n" \
  "v_mfma_f32_16x16x32_bf16 v[42:45], v[26:29], v[200:203], v[42:45]\n" \
  "v_mul_f32 v50, v50, v7\n" \
  "v_mul_f32 v51, v51, v7\n" \
  "s_cmp_eq_u32 s78, 1\n" \
  "s_cbranch_scc0 .LCQ_X3_%=\n" \
  "global_load_lds_dwordx4 v2, s[60:61] offset:2048 sc1\n" \
  "global_load_lds_dwordx4 v2, s[60:61] offset:3072 sc1\n" \
  "s_add_u32 s60, s60, 0x8000\n" \
  "s_addc_u32 s61, s61, 0\n" \
  ".LCQ_X3_%=:\n" \
  "ds_read_b128 v[26:29], v0 offset:20480\n" \
  "s_waitcnt lgkmcnt(5)\n" \
  "v_mfma_f32_16x16x32_bf16 v[34:37], v[76:79], v[30:33], v[34:37]\n" \
  "v_cvt_pk_bf16_f32 v48, v48, v49\n" \
  "v_cvt_pk_bf16_f32 v49, v50, v51\n" \
  "v_mfma_f32_16x16x32_bf16 v[38:41], v[140:143], v[30:33], v[38:41]\n" \
  "v_mul_f32 v54, v54, v7\n" \
  "v_mul_f32 v55, v55, v7\n" \
  "v_mfma_f32_16x16x32_bf16 v[42:45], v[30:33], v[204:207], v[42:45]\n" \
  "v_mul_f32 v56, v56, v7\n" \
  "v_mul_f32 v57, v57, v7\n" \
  "s_cmp_eq_u32 s78, 1\n" \
  "s_cbranch_scc0 .LCQ_X4_%=\n" \
  "s_add_u32 s87, s71, 0x1000\n" \
  "v_bfe_u32 v5, v2, 2, 5\n" \
  "buffer_load_dword v5, v5, s[52:55], s87 offen sc1\n" \
  ".LCQ_X4_%=:\n" \
  "ds_read_b128 v[30:33], v0 offset:22528\n" \
  "s_waitcnt lgkmcnt(5)\n" \
  "v_mfma_f32_16x16x32_bf16 v[34:37], v[80:83], v[18:21], v[34:37]\n" \
  "v_cvt_pk_bf16_f32 v54, v54, v55\n" \
  "v_cvt_pk_bf16_f32 v55, v56, v57\n" \
  "v_mfma_f32_16x16x32_bf16 v[38:41], v[144:147], v[18:21], v[38:41]\n" \
  "s_waitcnt lgkmcnt(4)\n" \
  "v_fma_f32 v60, -v10, v8, v60\n" \
  "v_fma_f32 v61, -v11, v8, v61\n" \
  "v_mfma_f32_16x16x32_bf16 v[42:45], v[18:21], v[208:211], v[42:45]\n" \
  "v_fma_f32 v62, -v12, v8, v62\n" \
  "v_fma_f32 v63, -v13, v8, v63\n" \
  "ds_read_b128 v[18:21], v0 offset:32768\n" \
  "s_waitcnt lgkmcnt(4)\n" \
  "v_mfma_f32_16x16x32_bf16 v[34:37], v[84:87], v[22:25], v[34:37]\n" \
  "v_permlane16_swap_b32 v46, v48\n" \
  "v_mfma_f32_16x16x32_bf16 v[38:41], v[148:151], v[22:25], v[38:41]\n" \
  "v_permlane16_swap_b32 v47, v49\n" \
  "v_mfma_f32_16x16x32_bf16 v[42:45], v[22:25], v[212:215], v[42:45]\n" \
  "v_permlane16_swap_b32 v52, v54\n" \
  "ds_read_b128 v[22:25], v0 offset:34816\n" \
  "s_waitcnt lgkmcnt(3)\n" \
  "v_mfma_f32_16x16x32_bf16 v[34:37], v[88:91], v[26:29], v[34:37]\n" \
  "v_permlane16_swap_b32 v53, v55\n" \
  "v_mfma_f32_16x16x32_bf16 v[38:41], v[152:155], v[26:29], v[38:41]\n" \
  "v_mul_f32 v60, v60, v14\n" \
  "v_mul_f32 v61, v61, v15\n" \
  "v_mfma_f32_16x16x32_bf16 v[42:45], v[26:29], v[216:219], v[42:45]\n" \
  "v_mul_f32 v62, v62, v16\n" \
  "v_mul_f32 v63, v63, v17\n" \
  "ds_read_b128 v[26:29], v0 offset:36864\n" \
  "s_waitcnt lgkmcnt(3)\n" \
  "v_mfma_f32_16x16x32_bf16 v[34:37], v[92:95], v[30:33], v[34:37]\n" \
  "v_cvt_pk_bf16_f32 v60, v60, v61\n" \
  "v_cvt_pk_bf16_f32 v61, v62, v63\n" \
  "v_mfma_f32_16x16x32_bf16 v[38:41], v[156:159], v[30:33], v[38:41]\n" \
  "v_mov_b64 v[10:11], 0\n" \
  "v_lshrrev_b32 v12, 1, v2\n" \
  "v_mfma_f32_16x16x32_bf16 v[42:45], v[30:33], v[220:223], v[42:45]\n" \
  "s_cmp_eq_u32 s81, 0\n" \
  "s_cbranch_scc1 .LCQ_S5_%=\n" \
  "s_add_u32 s87, s65, 0x2000\n" \
  "buffer_store_dwordx4 v[52:55], v1, s[40:43], s87 offen sc1\n" \
  "buffer_store_dwordx4 v[58:61], v2, s[44:47], s66 offen sc1\n" \
  "buffer_store_dwordx4 v[46:49], v1, s[40:43], s65 offen sc1\n" \
  "buffer_store_dwordx2 v[10:11], v12, s[48:51], s67 offen sc1\n" \
  "s_add_u32 s65, s65, 0x80000\n" \
  "s_add_u32 s66, s66, 0x40000\n" \
  "s_add_u32 s67, s67, 0x10000\n" \
  "s_mov_b32 s84, s72\n" \
  "s_add_u32 s72, s72, 0x1000\n" \
  "s_mov_b32 s85, s70\n" \
  "s_xor_b32 s70, s70, 4\n" \
  "s_mov_b32 s79, 1\n" \
  ".LCQ_S5_%=:\n" \
  "s_setprio 2\n" \
  "ds_read_b128 v[30:33], v0 offset:38912\n" \
  "s_waitcnt lgkmcnt(3)\n" \
  "v_mfma_f32_16x16x32_bf16 v[34:37], v[96:99], v[18:21], v[34:37]\n" \
  "v_bfe_u32 v7, v2, 4, 5\n" \
  "v_and_b32 v8, 0x200, v2\n" \
  "v_mfma_f32_16x16x32_bf16 v[38:41], v[160:163], v[18:21], v[38:41]\n" \
  "v_lshl_add_u32 v6, v7, 3, s74\n" \
  "v_lshl_add_u32 v6, v8, 1, v6\n" \
  "v_mfma_f32_16x16x32_bf16 v[42:45], v[18:21], v[224:227], v[42:45]\n" \
  "ds_read2_b64 v[10:13], v6 offset0:0 offset1:32\n" \
  "ds_read2_b64 v[14:17], v6 offset0:64 offset1:96\n" \
  "v_lshrrev_b32 v7, 2, v2\n" \
  "s_andn2_b32 s82, s79, s81\n" \
  "s_cbranch_scc0 .LCQ_NPA_%=\n" \
  "s_waitcnt vmcnt(7)\n" \
  "s_cmp_lg_u32 s78, 0\n" \
  "s_cbranch_scc1 .LCQ_PA_%=\n" \
  "s_waitcnt vmcnt(0)\n" \
  ".LCQ_PA_%=:\n" \
  "v_mov_b32 v9, s85\n" \
  "s_mov_b64 exec, 2\n" \
  "ds_add_rtn_u32 v9, v9, v2\n" \
  "s_mov_b64 exec, -1\n" \
  ".LCQ_NPA_%=:\n" \
  "s_cmp_eq_u32 s78, 0\n" \
  "s_cbranch_scc0 .LCQ_X6_%=\n" \
  "v_bfe_u32 v5, v2, 2, 5\n" \
  "buffer_load_dword v5, v5, s[52:55], s71 offen sc1\n" \
  ".LCQ_X6_%=:\n" \
  "ds_read_b128 v[18:21], v0 offset:49152\n" \
  "s_waitcnt lgkmcnt(5)\n" \
  "v_mfma_f32_16x16x32_bf16 v[34:37], v[100:103], v[22:25], v[34:37]\n" \
  "s_waitcnt lgkmcnt(1)\n" \
  "v_add_f32 v6, v10, v12\n" \
  "v_add_f32 v8, v14, v16\n" \
  "v_mfma_f32_16x16x32_bf16 v[38:41], v[164:167], v[22:25], v[38:41]\n" \
  "v_add_f32 v6, v6, v8\n" \
  "v_mov_b32 v8, v6\n" \
  "v_mfma_f32_16x16x32_bf16 v[42:45], v[22:25], v[228:231], v[42:45]\n" \
  "v_add_f32 v11, v11, v13\n" \
  "v_add_f32 v15, v15, v17\n" \
  "ds_read_b128 v[22:25], v0 offset:51200\n" \
  "v_mfma_f32_16x16x32_bf16 v[34:37], v[104:107], v[26:29], v[34:37]\n" \
  "v_permlane32_swap_b32 v6, v8\n" \
  "v_mfma_f32_16x16x32_bf16 v[38:41], v[168:171], v[26:29], v[38:41]\n" \
  "v_add_f32 v6, v6, v8\n" \
  "v_fmamk_f32 v10, v6, 0xbe000000, v10\n" \
  "v_mfma_f32_16x16x32_bf16 v[42:45], v[26:29], v[232:235], v[42:45]\n" \
  "v_fmamk_f32 v12, v6, 0xbe000000, v12\n" \
  "v_fmamk_f32 v14, v6, 0xbe000000, v14\n" \
  "s_cmp_eq_u32 s82, 1\n" \
  "s_cbranch_scc0 .LCQ_X8_%=\n" \
  "s_waitcnt lgkmcnt(2)\n" \
  "v_readlane_b32 s87, v9, 1\n" \
  "s_mov_b32 s79, 0\n" \
  "s_and_b32 s87, s87, 0x70\n" \
  "s_cmp_eq_u32 s87, 0x70\n" \
  "s_cbranch_scc0 .LCQ_F7_%=\n" \
  "s_mov_b64 exec, 2\n" \
  "buffer_store_dword v9, off, s[56:59], s84 sc1\n" \
  "s_mov_b64 exec, -1\n" \
  ".LCQ_F7_%=:\n" \
  ".LCQ_X8_%=:\n" \
  "ds_read_b128 v[26:29], v0 offset:53248\n" \
  "v_mfma_f32_16x16x32_bf16 v[34:37], v[108:111], v[30:33], v[34:37]\n" \
  "v_fmamk_f32 v16, v6, 0xbe000000, v16\n" \
  "v_add_f32 v11, v11, v15\n" \
  "v_mfma_f32_16x16x32_bf16 v[38:41], v[172:175], v[30:33], v[38:41]\n" \
  "v_mul_f32 v10, v10, v10\n" \
  "v_fmac_f32 v10, v12, v12\n" \
  "v_mfma_f32_16x16x32_bf16 v[42:45], v[30:33], v[236:239], v[42:45]\n" \
  "v_fmac_f32 v10, v14, v14\n" \
  "v_fmac_f32 v10, v16, v16\n" \
  "ds_read_b128 v[30:33], v0 offset:55296\n" \
  "s_waitcnt lgkmcnt(3)\n" \
  "v_mfma_f32_16x16x32_bf16 v[34:37], v[112:115], v[18:21], v[34:37]\n" \
  "v_fmamk_f32 v11, v10, 0x3c800000, v11\n" \
  "v_mov_b32 v8, v11\n" \
  "v_mfma_f32_16x16x32_bf16 v[38:41], v[176:179], v[18:21], v[38:41]\n" \
  "v_mul_f32 v6, 0x3b000000, v6\n" \
  "v_add_u32 v7, s69, v7\n" \
  "v_mfma_f32_16x16x32_bf16 v[42:45], v[18:21], v[240:243], v[42:45]\n" \
  "v_permlane32_swap_b32 v11, v8\n" \
  "ds_read_b128 v[18:21], v0 offset:256\n" \
  "s_waitcnt lgkmcnt(3)\n" \
  "v_mfma_f32_16x16x32_bf16 v[34:37], v[116:119], v[22:25], v[34:37]\n" \
  "v_add_f32 v11, v11, v8\n" \
  "v_mul_f32 v10, 0x3b000000, v11\n" \
  "v_mfma_f32_16x16x32_bf16 v[38:41], v[180:183], v[22:25], v[38:41]\n" \
  "v_cvt_pk_bf16_f32 v6, v6, v10\n" \
  "v_and_b32 v10, 0xffff0000, v6\n" \
  "v_mfma_f32_16x16x32_bf16 v[42:45], v[22:25], v[244:247], v[42:45]\n" \
  "v_lshlrev_b32 v6, 16, v6\n" \
  "v_add_f32 v10, 0x3727c5ac, v10\n" \
  "ds_read_b128 v[22:25], v0 offset:2304\n" \
  "s_waitcnt lgkmcnt(3)\n" \
  "v_mfma_f32_16x16x32_bf16 v[34:37], v[120:123], v[26:29], v[34:37]\n" \
  "v_cvt_pk_bf16_f32 v10, v10, v10\n" \
  "v_lshlrev_b32 v10, 16, v10\n" \
  "v_mfma_f32_16x16x32_bf16 v[38:41], v[184:187], v[26:29], v[38:41]\n" \
  "v_sqrt_f32 v10, v10\n" \
  "s_nop 0\n" \
  "v_cvt_pk_bf16_f32 v10, v10, v10\n" \
  "v_mfma_f32_16x16x32_bf16 v[42:45], v[26:29], v[248:251], v[42:45]\n" \
  "v_lshlrev_b32 v10, 16, v10\n" \
  "v_rcp_f32 v10, v10\n" \
  "ds_read_b128 v[26:29], v0 offset:4352\n" \
  "s_waitcnt lgkmcnt(3)\n" \
  "v_mfma_f32_16x16x32_bf16 v[34:37], v[124:127], v[30:33], v[34:37]\n" \
  "s_nop 0\n" \
  "v_cndmask_b32 v10, v6, v10, s[76:77]\n" \
  "ds_write_b32 v7, v10\n" \
  "v_mfma_f32_16x16x32_bf16 v[38:41], v[188:191], v[30:33], v[38:41]\n" \
  "v_mfma_f32_16x16x32_bf16 v[42:45], v[30:33], v[252:255], v[42:45]\n" \
  "s_setprio 1\n" \
  "ds_read_b128 v[30:33], v0 offset:6400\n" \
  "s_waitcnt lgkmcnt(4)\n" \
  "v_mfma_f32_16x16x32_bf16 v[48:51], v[64:67], v[18:21], 0\n" \
  "ds_read2_b32 v[6:7], v3 offset0:0 offset1:32\n" \
  "ds_read_b128 v[10:13], v4 offset:256\n" \
  "ds_read_b128 v[14:17], v4 offset:320\n" \
  "ds_read_b32 v8, v3 offset:384\n" \
  "v_mfma_f32_16x16x32_bf16 v[54:57], v[128:131], v[18:21], 0\n" \
  "v_mfma_f32_16x16x32_bf16 v[60:63], v[18:21], v[192:195], 0\n" \
  "ds_read_b128 v[18:21], v0 offset:16640\n" \
  "s_waitcnt lgkmcnt(8)\n" \
  "v_mfma_f32_16x16x32_bf16 v[48:51], v[68:71], v[22:25], v[48:51]\n" \
  "s_waitcnt lgkmcnt(3)\n" \
  "v_fma_f32 v34, -v6, v10, v34\n" \
  "v_fma_f32 v35, -v6, v11, v35\n" \
  "v_mfma_f32_16x16x32_bf16 v[54:57], v[132:135], v[22:25], v[54:57]\n" \
  "v_fma_f32 v36, -v6, v12, v36\n" \
  "v_fma_f32 v37, -v6, v13, v37\n" \
  "v_mfma_f32_16x16x32_bf16 v[60:63], v[22:25], v[196:199], v[60:63]\n" \
  "ds_read_b128 v[10:13], v4 offset:0\n" \
  "s_waitcnt lgkmcnt(3)\n" \
  "v_fma_f32 v38, -v6, v14, v38\n" \
  "v_fma_f32 v39, -v6, v15, v39\n" \
  "s_mov_b32 s82, 0\n" \
  "s_cmp_eq_u32 s78, 0\n" \
  "s_cbranch_scc0 .LCQ_X9_%=\n" \
  "s_waitcnt vmcnt(0)\n" \
  "v_cmp_eq_u32 vcc, 0, v5\n" \
  "s_cmp_eq_u64 vcc, 0\n" \
  "s_cselect_b32 s78, 2, 0\n" \
  "s_mov_b32 s82, s79\n" \
  ".LCQ_X9_%=:\n" \
  "s_cmp_eq_u32 s82, 1\n" \
  "s_cbranch_scc0 .LCQ_X10_%=\n" \
  "v_mov_b32 v9, s85\n" \
  "s_mov_b64 exec, 2\n" \
  "ds_add_rtn_u32 v9, v9, v2\n" \
  "s_mov_b64 exec, -1\n" \
  ".LCQ_X10_%=:\n" \
  "s_cmp_eq_u32 s78, 2\n" \
  "s_cbranch_scc0 .LCQ_X11_%=\n" \
  "s_mov_b32 m0, s73\n" \
  "s_nop 0\n" \
  "global_load_lds_dwordx4 v2, s[62:63] offset:0 sc1\n" \
  "global_load_lds_dwordx4 v2, s[62:63] offset:1024 sc1\n" \
  "s_add_u32 s62, s62, 0x4000\n" \
  "s_addc_u32 s63, s63, 0\n" \
  ".LCQ_X11_%=:\n" \
  "ds_read_b128 v[22:25], v0 offset:18688\n" \
  "v_mfma_f32_16x16x32_bf16 v[48:51], v[72:75], v[26:29], v[48:51]\n" \
  "v_fma_f32 v40, -v6, v16, v40\n" \
  "v_fma_f32 v41, -v6, v17, v41\n" \
  "v_mfma_f32_16x16x32_bf16 v[54:57], v[136:139], v[26:29], v[54:57]\n" \
  "ds_read_b128 v[14:17], v4 offset:128\n" \
  "v_mul_f32 v34, v34, v7\n" \
  "v_mul_f32 v35, v35, v7\n" \
  "v_mfma_f32_16x16x32_bf16 v[60:63], v[26:29], v[200:203], v[60:63]\n" \
  "v_mul_f32 v36, v36, v7\n" \
  "v_mul_f32 v37, v37, v7\n" \
  "s_cmp_eq_u32 s78, 2\n" \
  "s_cbranch_scc0 .LCQ_X12_%=\n" \
  "s_mov_b32 m0, s68\n" \
  "s_xor_b32 s68, s68, 0x10000\n" \
  "global_load_lds_dwordx4 v2, s[60:61] offset:0 sc1\n" \
  "global_load_lds_dwordx4 v2, s[60:61] offset:1024 sc1\n" \
  ".LCQ_X12_%=:\n" \
  "ds_read_b128 v[26:29], v0 offset:20736\n" \
  "v_mfma_f32_16x16x32_bf16 v[48:51], v[76:79], v[30:33], v[48:51]\n" \
  "v_cvt_pk_bf16_f32 v46, v34, v35\n" \
  "v_cvt_pk_bf16_f32 v47, v36, v37\n" \
  "v_mfma_f32_16x16x32_bf16 v[54:57], v[140:143], v[30:33], v[54:57]\n" \
  "v_mul_f32 v38, v38, v7\n" \
  "v_mul_f32 v39, v39, v7\n" \
  "v_mfma_f32_16x16x32_bf16 v[60:63], v[30:33], v[204:207], v[60:63]\n" \
  "v_mul_f32 v40, v40, v7\n" \
  "v_mul_f32 v41, v41, v7\n" \
  "s_cmp_eq_u32 s82, 1\n" \
  "s_cbranch_scc0 .LCQ_X14_%=\n" \
  "s_waitcnt lgkmcnt(2)\n" \
  "v_readlane_b32 s87, v9, 1\n" \
  "s_mov_b32 s79, 0\n" \
  "s_and_b32 s87, s87, 0x70\n" \
  "s_cmp_eq_u32 s87, 0x70\n" \
  "s_cbranch_scc0 .LCQ_F13_%=\n" \
  "s_mov_b64 exec, 2\n" \
  "buffer_store_dword v9, off, s[56:59], s84 sc1\n" \
  "s_mov_b64 exec, -1\n" \
  ".LCQ_F13_%=:\n" \
  ".LCQ_X14_%=:\n" \
  "s_cmp_eq_u32 s78, 2\n" \
  "s_cbranch_scc0 .LCQ_X15_%=\n" \
  "global_load_lds_dwordx4 v2, s[60:61] offset:2048 sc1\n" \
  "global_load_lds_dwordx4 v2, s[60:61] offset:3072 sc1\n" \
  "s_add_u32 s60, s60, 0x8000\n" \
  "s_addc_u32 s61, s61, 0\n" \
  ".LCQ_X15_%=:\n" \
  "ds_read_b128 v[30:33], v0 offset:22784\n" \
  "s_waitcnt lgkmcnt(5)\n" \
  "v_mfma_f32_16x16x32_bf16 v[48:51], v[80:83], v[18:21], v[48:51]\n" \
  "v_cvt_pk_bf16_f32 v52, v38, v39\n" \
  "v_cvt_pk_bf16_f32 v53, v40, v41\n" \
  "v_mfma_f32_16x16x32_bf16 v[54:57], v[144:147], v[18:21], v[54:57]\n" \
  "s_waitcnt lgkmcnt(4)\n" \
  "v_fma_f32 v42, -v10, v8, v42\n" \
  "v_fma_f32 v43, -v11, v8, v43\n" \
  "v_mfma_f32_16x16x32_bf16 v[60:63], v[18:21], v[208:211], v[60:63]\n" \
  "v_fma_f32 v44, -v12, v8, v44\n" \
  "v_fma_f32 v45, -v13, v8, v45\n" \
  "s_cmp_eq_u32 s78, 2\n" \
  "s_cbranch_scc0 .LCQ_X16_%=\n" \
  "s_add_u32 s87, s71, 0x1000\n" \
  "v_bfe_u32 v5, v2, 2, 5\n" \
  "buffer_load_dword v5, v5, s[52:55], s87 offen sc1\n" \
  ".LCQ_X16_%=:\n" \
  "s_and_b32 s82, s79, s78\n" \
  "s_cmp_eq_u32 s78, 1\n" \
  "s_cbranch_scc0 .LCQ_X17_%=\n" \
  "s_waitcnt vmcnt(0)\n" \
  ".LCQ_X17_%=:\n" \
  "s_cmp_eq_u32 s82, 1\n" \
  "s_cbranch_scc0 .LCQ_X18_%=\n" \
  "v_mov_b32 v9, s85\n" \
  "s_mov_b64 exec, 2\n" \
  "ds_add_rtn_u32 v9, v9, v2\n" \
  "s_mov_b64 exec, -1\n" \
  ".LCQ_X18_%=:\n" \
  "ds_read_b128 v[18:21], v0 offset:33024\n" \
  "s_waitcnt lgkmcnt(4)\n" \
  "v_mfma_f32_16x16x32_bf16 v[48:51], v[84:87], v[22:25], v[48:51]\n" \
  "s_waitcnt lgkmcnt(3)\n" \
  "v_mul_f32 v42, v42, v14\n" \
  "v_mul_f32 v43, v43, v15\n" \
  "v_mfma_f32_16x16x32_bf16 v[54:57], v[148:151], v[22:25], v[54:57]\n" \
  "v_mul_f32 v44, v44, v16\n" \
  "v_mul_f32 v45, v45, v17\n" \
  "v_mfma_f32_16x16x32_bf16 v[60:63], v[22:25], v[212:215], v[60:63]\n" \
  "v_cvt_pk_bf16_f32 v58, v42, v43\n" \
  "v_cvt_pk_bf16_f32 v59, v44, v45\n" \
  "ds_read_b128 v[22:25], v0 offset:35072\n" \
  "s_waitcnt lgkmcnt(3)\n" \
  "v_mfma_f32_16x16x32_bf16 v[48:51], v[88:91], v[26:29], v[48:51]\n" \
  "v_mfma_f32_16x16x32_bf16 v[54:57], v[152:155], v[26:29], v[54:57]\n" \
  "v_mfma_f32_16x16x32_bf16 v[60:63], v[26:29], v[216:219], v[60:63]\n" \
  "s_cmp_eq_u32 s82, 1\n" \
  "s_cbranch_scc0 .LCQ_X20_%=\n" \
  "s_waitcnt lgkmcnt(2)\n" \
  "v_readlane_b32 s87, v9, 1\n" \
  "s_mov_b32 s79, 0\n" \
  "s_and_b32 s87, s87, 0x70\n" \
  "s_cmp_eq_u32 s87, 0x70\n" \
  "s_cbranch_scc0 .LCQ_F19_%=\n" \
  "s_mov_b64 exec, 2\n" \
  "buffer_store_dword v9, off, s[56:59], s84 sc1\n" \
  "s_mov_b64 exec, -1\n" \
  ".LCQ_F19_%=:\n" \
  ".LCQ_X20_%=:\n" \
  "s_cmp_eq_u32 s78, 0\n" \
  "s_cbranch_scc0 .LCQ_X21_%=\n" \
  "v_bfe_u32 v5, v2, 2, 5\n" \
  "buffer_load_dword v5, v5, s[52:55], s71 offen sc1\n" \
  ".LCQ_X21_%=:\n" \
  "ds_read_b128 v[26:29], v0 offset:37120\n" \
  "s_waitcnt lgkmcnt(3)\n" \
  "v_mfma_f32_16x16x32_bf16 v[48:51], v[92:95], v[30:33], v[48:51]\n" \
  "v_mfma_f32_16x16x32_bf16 v[54:57], v[156:159], v[30:33], v[54:57]\n" \
  "v_mfma_f32_16x16x32_bf16 v[60:63], v[30:33], v[220:223], v[60:63]\n" \
  "s_setprio 0\n" \
  "ds_read_b128 v[30:33], v0 offset:39168\n" \
  "s_waitcnt lgkmcnt(3)\n" \
  "v_mfma_f32_16x16x32_bf16 v[48:51], v[96:99], v[18:21], v[48:51]\n" \
  "v_mfma_f32_16x16x32_bf16 v[54:57], v[160:163], v[18:21], v[54:57]\n" \
  "v_mfma_f32_16x16x32_bf16 v[60:63], v[18:21], v[224:227], v[60:63]\n" \
  "ds_read_b128 v[18:21], v0 offset:49408\n" \
  "s_waitcnt lgkmcnt(3)\n" \
  "v_mfma_f32_16x16x32_bf16 v[48:51], v[100:103], v[22:25], v[48:51]\n" \
  "v_mfma_f32_16x16x32_bf16 v[54:57], v[164:167], v[22:25], v[54:57]\n" \
  "v_mfma_f32_16x16x32_bf16 v[60:63], v[22:25], v[228:231], v[60:63]\n" \
  "ds_read_b128 v[22:25], v0 offset:51456\n" \
  "s_waitcnt lgkmcnt(3)\n" \
  "v_mfma_f32_16x16x32_bf16 v[48:51], v[104:107], v[26:29], v[48:51]\n" \
  "v_bfe_u32 v7, v2, 4, 5\n" \
  "v_and_b32 v8, 0x200, v2\n" \
  "v_mfma_f32_16x16x32_bf16 v[54:57], v[168:171], v[26:29], v[54:57]\n" \
  "v_lshl_add_u32 v6, v7, 3, s73\n" \
  "v_lshl_add_u32 v6, v8, 1, v6\n" \
  "v_mfma_f32_16x16x32_bf16 v[60:63], v[26:29], v[232:235], v[60:63]\n" \
  "s_cmp_eq_u32 s78, 2\n" \
  "s_cbranch_scc0 .LCQ_X22_%=\n" \
  "s_waitcnt vmcnt(5)\n" \
  ".LCQ_X22_%=:\n" \
  "ds_read_b128 v[26:29], v0 offset:53504\n" \
  "s_waitcnt lgkmcnt(3)\n" \
  "v_mfma_f32_16x16x32_bf16 v[48:51], v[108:111], v[30:33], v[48:51]\n" \
  "ds_read2_b64 v[10:13], v6 offset0:0 offset1:32\n" \
  "ds_read2_b64 v[14:17], v6 offset0:64 offset1:96\n" \
  "v_lshl_add_u32 v7, v7, 3, s75\n" \
  "v_mfma_f32_16x16x32_bf16 v[54:57], v[172:175], v[30:33], v[54:57]\n" \
  "v_mfma_f32_16x16x32_bf16 v[60:63], v[30:33], v[236:239], v[60:63]\n" \
  "ds_read_b128 v[30:33], v0 offset:55552\n" \
  "s_waitcnt lgkmcnt(5)\n" \
  "v_mfma_f32_16x16x32_bf16 v[48:51], v[112:115], v[18:21], v[48:51]\n" \
  "s_waitcnt lgkmcnt(1)\n" \
  "v_add_f32 v6, v10, v12\n" \
  "v_add_f32 v8, v14, v16\n" \
  "v_mfma_f32_16x16x32_bf16 v[54:57], v[176:179], v[18:21], v[54:57]\n" \
  "v_add_f32 v6, v6, v8\n" \
  "v_mov_b32 v8, v6\n" \
  "v_mfma_f32_16x16x32_bf16 v[60:63], v[18:21], v[240:243], v[60:63]\n" \
  "v_add_f32 v11, v11, v13\n" \
  "v_add_f32 v15, v15, v17\n" \
  "v_mfma_f32_16x16x32_bf16 v[48:51], v[116:119], v[22:25], v[48:51]\n" \
  "v_permlane32_swap_b32 v6, v8\n" \
  "v_mfma_f32_16x16x32_bf16 v[54:57], v[180:183], v[22:25], v[54:57]\n" \
  "v_add_f32 v6, v6, v8\n" \
  "v_fmamk_f32 v10, v6, 0xbe000000, v10\n" \
  "v_mfma_f32_16x16x32_bf16 v[60:63], v[22:25], v[244:247], v[60:63]\n" \
  "v_fmamk_f32 v12, v6, 0xbe000000, v12\n" \
  "v_fmamk_f32 v14, v6, 0xbe000000, v14\n" \
  "v_mfma_f32_16x16x32_bf16 v[48:51], v[120:123], v[26:29], v[48:51]\n" \
  "v_fmamk_f32 v16, v6, 0xbe000000, v16\n" \
  "v_add_f32 v11, v11, v15\n" \
  "v_mfma_f32_16x16x32_bf16 v[54:57], v[184:187], v[26:29], v[54:57]\n" \
  "v_mul_f32 v10, v10, v10\n" \
  "v_fmac_f32 v10, v12, v12\n" \
  "v_mfma_f32_16x16x32_bf16 v[60:63], v[26:29], v[248:251], v[60:63]\n" \
  "v_fmac_f32 v10, v14, v14\n" \
  "v_fmac_f32 v10, v16, v16\n" \
  "s_waitcnt lgkmcnt(0)\n" \
  "v_mfma_f32_16x16x32_bf16 v[48:51], v[124:127], v[30:33], v[48:51]\n" \
  "v_fmamk_f32 v11, v10, 0x3e000000, v11\n" \
  "v_mov_b32 v8, v11\n" \
  "v_mfma_f32_16x16x32_bf16 v[54:57], v[188:191], v[30:33], v[54:57]\n" \
  "v_mov_b32 v10, v6\n" \
  "s_nop 0\n" \
  "v_permlane32_swap_b32 v11, v8\n" \
  "v_mfma_f32_16x16x32_bf16 v[60:63], v[30:33], v[252:255], v[60:63]\n" \
  "v_add_f32 v11, v11, v8\n" \
  "ds_write_b64 v7, v[10:11]\n" \
  "s_cmp_eq_u32 s78, 1\n" \
  "s_cbranch_scc1 .LCQ_NV_%=\n" \
  "s_waitcnt vmcnt(0)\n" \
  ".LCQ_NV_%=:\n" \
  "v_cmp_eq_u32 vcc, 0, v5\n" \
  "s_cmp_eq_u64 vcc, 0\n" \
  "s_cselect_b32 s80, 1, 0\n" \
  "s_sub_u32 s64, s64, 1\n" \
  "s_cmp_lt_u32 s64, 62\n" \
  "s_cselect_b32 s81, s78, 0\n" \
  "s_min_u32 s81, s81, 1\n" \
  "s_cmp_lg_u32 s81, 0\n" \
  "s_cbranch_scc1 .LCQ_TOP_%=\n" \
  "s_cmp_lg_u32 s78, 0\n" \
  "s_cbranch_scc1 .LCQ_NA_%=\n" \
  "s_cmp_eq_u32 s80, 1\n" \
  "s_cbranch_scc0 .LCQ_X23_%=\n" \
  "s_mov_b32 m0, s73\n" \
  "s_nop 0\n" \
  "global_load_lds_dwordx4 v2, s[62:63] offset:0 sc1\n" \
  "global_load_lds_dwordx4 v2, s[62:63] offset:1024 sc1\n" \
  "s_add_u32 s62, s62, 0x4000\n" \
  "s_addc_u32 s63, s63, 0\n" \
  "s_mov_b32 m0, s68\n" \
  "s_xor_b32 s68, s68, 0x10000\n" \
  "global_load_lds_dwordx4 v2, s[60:61] offset:0 sc1\n" \
  "global_load_lds_dwordx4 v2, s[60:61] offset:1024 sc1\n" \
  "global_load_lds_dwordx4 v2, s[60:61] offset:2048 sc1\n" \
  "global_load_lds_dwordx4 v2, s[60:61] offset:3072 sc1\n" \
  "s_add_u32 s60, s60, 0x8000\n" \
  "s_addc_u32 s61, s61, 0\n" \
  "s_add_u32 s87, s71, 0x1000\n" \
  "v_bfe_u32 v5, v2, 2, 5\n" \
  "buffer_load_dword v5, v5, s[52:55], s87 offen sc1\n" \
  ".LCQ_X23_%=:\n" \
  ".LCQ_NA_%=:\n" \
  "ds_read2_b32 v[6:7], v3 offset0:16 offset1:48\n" \
  "ds_read_b128 v[10:13], v4 offset:256\n" \
  "ds_read_b128 v[14:17], v4 offset:320\n" \
  "ds_read_b32 v8, v3 offset:384\n" \
  "s_waitcnt lgkmcnt(2)\n" \
  "v_fma_f32 v48, -v6, v10, v48\n" \
  "v_fma_f32 v49, -v6, v11, v49\n" \
  "v_fma_f32 v50, -v6, v12, v50\n" \
  "v_fma_f32 v51, -v6, v13, v51\n" \
  "ds_read_b128 v[10:13], v4 offset:64\n" \
  "s_waitcnt lgkmcnt(2)\n" \
  "v_fma_f32 v54, -v6, v14, v54\n" \
  "v_fma_f32 v55, -v6, v15, v55\n" \
  "v_fma_f32 v56, -v6, v16, v56\n" \
  "v_fma_f32 v57, -v6, v17, v57\n" \
  "ds_read_b128 v[14:17], v4 offset:192\n" \
  "v_mul_f32 v48, v48, v7\n" \
  "v_mul_f32 v49, v49, v7\n" \
  "v_mul_f32 v50, v50, v7\n" \
  "v_mul_f32 v51, v51, v7\n" \
  "v_cvt_pk_bf16_f32 v48, v48, v49\n" \
  "v_cvt_pk_bf16_f32 v49, v50, v51\n" \
  "v_mul_f32 v54, v54, v7\n" \
  "v_mul_f32 v55, v55, v7\n" \
  "v_mul_f32 v56, v56, v7\n" \
  "v_mul_f32 v57, v57, v7\n" \
  "v_cvt_pk_bf16_f32 v54, v54, v55\n" \
  "v_cvt_pk_bf16_f32 v55, v56, v57\n" \
  "s_waitcnt lgkmcnt(1)\n" \
  "v_fma_f32 v60, -v10, v8, v60\n" \
  "v_fma_f32 v61, -v11, v8, v61\n" \
  "v_fma_f32 v62, -v12, v8, v62\n" \
  "v_fma_f32 v63, -v13, v8, v63\n" \
  "v_permlane16_swap_b32 v46, v48\n" \
  "v_permlane16_swap_b32 v47, v49\n" \
  "v_permlane16_swap_b32 v52, v54\n" \
  "v_permlane16_swap_b32 v53, v55\n" \
  "s_waitcnt lgkmcnt(0)\n" \
  "v_mul_f32 v60, v60, v14\n" \
  "v_mul_f32 v61, v61, v15\n" \
  "v_mul_f32 v62, v62, v16\n" \
  "v_mul_f32 v63, v63, v17\n" \
  "v_cvt_pk_bf16_f32 v60, v60, v61\n" \
  "v_cvt_pk_bf16_f32 v61, v62, v63\n" \
  "v_mov_b64 v[10:11], 0\n" \
  "v_lshrrev_b32 v12, 1, v2\n" \
  "s_add_u32 s87, s65, 0x2000\n" \
  "buffer_store_dwordx4 v[52:55], v1, s[40:43], s87 offen sc1\n" \
  "buffer_store_dwordx4 v[58:61], v2, s[44:47], s66 offen sc1\n" \
  "buffer_store_dwordx4 v[46:49], v1, s[40:43], s65 offen sc1\n" \
  "buffer_store_dwordx2 v[10:11], v12, s[48:51], s67 offen sc1\n" \
  "s_add_u32 s65, s65, 0x80000\n" \
  "s_add_u32 s66, s66, 0x40000\n" \
  "s_add_u32 s67, s67, 0x10000\n" \
  "s_mov_b32 s84, s72\n" \
  "s_add_u32 s72, s72, 0x1000\n" \
  "s_mov_b32 s85, s70\n" \
  "s_xor_b32 s70, s70, 4\n" \
  "s_mov_b32 s79, 1\n" \
  "s_cmp_lg_u32 s78, 0\n" \
  "s_cbranch_scc1 .LCQ_TOP_%=\n" \
  "s_cmp_eq_u32 s80, 0\n" \
  "s_cbranch_scc1 .LCQ_POLL_%=\n" \
  "s_waitcnt vmcnt(4)\n" \
  "s_branch .LCQ_GOT_%=\n" \
  ".LCQ_POLL_%=:\n" \
  "s_mov_b32 s86, 0x40000\n" \
  "s_branch .LCQ_PW_%=\n" \
  ".LCQ_PV_%=:\n" \
  "s_sleep 12\n" \
  ".LCQ_PW_%=:\n" \
  "v_bfe_u32 v5, v2, 2, 5\n" \
  "buffer_load_dword v5, v5, s[52:55], s71 offen sc1\n" \
  "s_sub_u32 s86, s86, 1\n" \
  "s_cbranch_scc1 .LCQ_END_%=\n" \
  "s_waitcnt vmcnt(0)\n" \
  "s_cmp_eq_u32 s79, 0\n" \
  "s_cbranch_scc1 .LCQ_NPD_%=\n" \
  "v_mov_b32 v9, s85\n" \
  "s_mov_b64 exec, 2\n" \
  "ds_add_rtn_u32 v9, v9, v2\n" \
  "s_mov_b64 exec, -1\n" \
  "s_waitcnt lgkmcnt(0)\n" \
  "v_readlane_b32 s87, v9, 1\n" \
  "s_mov_b32 s79, 0\n" \
  "s_and_b32 s87, s87, 0x70\n" \
  "s_cmp_eq_u32 s87, 0x70\n" \
  "s_cbranch_scc0 .LCQ_F24_%=\n" \
  "s_mov_b64 exec, 2\n" \
  "buffer_store_dword v9, off, s[56:59], s84 sc1\n" \
  "s_mov_b64 exec, -1\n" \
  ".LCQ_F24_%=:\n" \
  "s_cmp_eq_u32 s64, 0\n" \
  "s_cbranch_scc1 .LCQ_END_%=\n" \
  ".LCQ_NPD_%=:\n" \
  "v_cmp_eq_u32 vcc, 0, v5\n" \
  "s_cmp_eq_u64 vcc, 0\n" \
  "s_cbranch_scc0 .LCQ_PV_%=\n" \
  "s_mov_b32 m0, s73\n" \
  "s_nop 0\n" \
  "global_load_lds_dwordx4 v2, s[62:63] offset:0 sc1\n" \
  "global_load_lds_dwordx4 v2, s[62:63] offset:1024 sc1\n" \
  "s_add_u32 s62, s62, 0x4000\n" \
  "s_addc_u32 s63, s63, 0\n" \
  "s_mov_b32 m0, s68\n" \
  "s_xor_b32 s68, s68, 0x10000\n" \
  "global_load_lds_dwordx4 v2, s[60:61] offset:0 sc1\n" \
  "global_load_lds_dwordx4 v2, s[60:61] offset:1024 sc1\n" \
  "global_load_lds_dwordx4 v2, s[60:61] offset:2048 sc1\n" \
  "global_load_lds_dwordx4 v2, s[60:61] offset:3072 sc1\n" \
  "s_add_u32 s60, s60, 0x8000\n" \
  "s_addc_u32 s61, s61, 0\n" \
  "s_add_u32 s87, s71, 0x1000\n" \
  "v_bfe_u32 v5, v2, 2, 5\n" \
  "buffer_load_dword v5, v5, s[52:55], s87 offen sc1\n" \
  "s_waitcnt vmcnt(0)\n" \
  ".LCQ_GOT_%=:\n" \
  "v_bfe_u32 v7, v2, 4, 5\n" \
  "v_and_b32 v8, 0x200, v2\n" \
  "v_lshl_add_u32 v6, v7, 3, s73\n" \
  "v_lshl_add_u32 v6, v8, 1, v6\n" \
  "ds_read2_b64 v[10:13], v6 offset0:0 offset1:32\n" \
  "ds_read2_b64 v[14:17], v6 offset0:64 offset1:96\n" \
  "v_lshl_add_u32 v7, v7, 3, s75\n" \
  "s_waitcnt lgkmcnt(0)\n" \
  "v_add_f32 v6, v10, v12\n" \
  "v_add_f32 v8, v14, v16\n" \
  "v_add_f32 v6, v6, v8\n" \
  "v_mov_b32 v8, v6\n" \
  "v_add_f32 v11, v11, v13\n" \
  "v_add_f32 v15, v15, v17\n" \
  "v_permlane32_swap_b32 v6, v8\n" \
  "v_add_f32 v6, v6, v8\n" \
  "v_fmamk_f32 v10, v6, 0xbe000000, v10\n" \
  "v_fmamk_f32 v12, v6, 0xbe000000, v12\n" \
  "v_fmamk_f32 v14, v6, 0xbe000000, v14\n" \
  "v_fmamk_f32 v16, v6, 0xbe000000, v16\n" \
  "v_add_f32 v11, v11, v15\n" \
  "v_mul_f32 v10, v10, v10\n" \
  "v_fmac_f32 v10, v12, v12\n" \
  "v_fmac_f32 v10, v14, v14\n" \
  "v_fmac_f32 v10, v16, v16\n" \
  "v_fmamk_f32 v11, v10, 0x3e000000, v11\n" \
  "v_mov_b32 v8, v11\n" \
  "v_mov_b32 v10, v6\n" \
  "s_nop 0\n" \
  "v_permlane32_swap_b32 v11, v8\n" \
  "v_add_f32 v11, v11, v8\n" \
  "ds_write_b64 v7, v[10:11]\n" \
  "v_cmp_eq_u32 vcc, 0, v5\n" \
  "s_cmp_eq_u64 vcc, 0\n" \
  "s_cselect_b32 s80, 1, 0\n" \
  "s_branch .LCQ_TOP_%=\n" \
  ".LCQ_END_%=:\n" \
  "s_endpgm\n"
__device__ __forceinline__ void role_qkv(const MegaArgs& A, char* smem, const int L, const int h) {
  const int w = A.wv;
  char* ws = A.ws;
  unsigned* FL = (unsigned*)(ws + OFF_FL);
  const int l = lane_id();
  const int c = l & 15, g = l >> 4;
  const unsigned sb = lds_base(smem);
  v8u wq[8], wk[8], wv[8];
  constexpr int Q_ST = 131072, Q_PR = Q_ST + 8192, Q_WP = Q_PR + 16384, Q_CN = Q_WP + 4096;
  const int rc = blockIdx.x & 3;
  const char* Xb = ws + (L ? OFF_X3 : OFF_XA);
  const char* Sb = ws + OFF_H + L * (SZ_X / 2);
  {
    const int q = (l >> 2) & 3, p = l & 3, f = q | ((g & 1) << 2);
    const unsigned ra = sb + (8 * g + q) * 256 + (w ^ f) * 32 + p * 8;
#define W_READ(W, hf)                                                          \
  {                                                                           \
    _Pragma("unroll") for (int t = 0; t < 4; ++t) {                           \
      const unsigned a_ = ra + (hf) * 8192 + t * 32768;                       \
      const v2u r0 = trd(a_), r1 = trd(a_ + 1024), r2 = trd(a_ + 16384), r3 = trd(a_ + 16384 + 1024); \
      W[4 * (hf) + t] = v8u{r0.x, r0.y, r1.x, r1.y, r2.x, r2.y, r3.x, r3.y};  \
    }                                                                         \
    _Pragma("unroll") for (int t = 0; t < 4; ++t) asm volatile("" : "+v"(W[4 * (hf) + t])); \
  }
#define W_STAGE(W, hf, WAIT, NEXT)                                             \
  {                                                                           \
    WAIT;                                                                     \
    RAW_BAR();                                                                \
    W_READ(W, hf);                                                            \
    RAW_BAR();                                                                \
    NEXT;                                                                     \
  }
    __builtin_amdgcn_s_waitcnt(0x0f70);
    wqkv_issue<false>(A.Wq, 0, h, w, l, sb);
    wqkv_issue<false>(A.Wq, 1, h, w, l, sb);
    const u64 tag = call_tag(A);
    const v4u one = {0x3f803f80u, 0x3f803f80u, 0x3f803f80u, 0x3f803f80u};
    float* cp = reinterpret_cast<float*>(smem + Q_ST + w * 1024 + 256);
    f32x4 cs = {};
#define HALF(x, i) BF8(((i) ? __builtin_shufflevector(x, x, 4, 5, 6, 7) : __builtin_shufflevector(x, x, 0, 1, 2, 3)))
    if (w == 0 && l < 8) reinterpret_cast<unsigned*>(smem + Q_CN)[l] = 0u;
    W_STAGE(wq, 0, WAIT_VM(8), wqkv_issue<false>(A.Wk, 0, h, w, l, sb));
    SST(4);
    W_STAGE(wq, 1, WAIT_VM(8), wqkv_issue<false>(A.Wk, 1, h, w, l, sb));
    SST(5);
    asm volatile("" : "+v"(cs));
#pragma unroll
    for (int ks = 0; ks < 16; ++ks) cs = MFMA16(HALF(wq[ks >> 1], ks & 1), BF8(one), cs);
    *reinterpret_cast<f32x4*>(cp + 4 * g) = cs;
    W_STAGE(wk, 0, WAIT_VM(8), wqkv_issue<false>(A.Wv, 0, h, w, l, sb));
    W_STAGE(wk, 1, WAIT_VM(8), if (w < 4) {
      wqkv_issue<false>(A.Wv, 1, h, w, l, sb);
      wqkv_issue<false>(A.Wv, 1, h, w + 4, l, sb);
    });
    SST(6);
    cs = f32x4{};
#pragma unroll
    for (int ks = 0; ks < 16; ++ks) cs = MFMA16(HALF(wk[ks >> 1], ks & 1), BF8(one), cs);
    *reinterpret_cast<f32x4*>(cp + 16 + 4 * g) = cs;
    W_STAGE(wv, 0, if (w < 4) WAIT_VM(16); else { ready_wait(A, tag, ready_look(A)); __builtin_amdgcn_s_waitcnt(0x0f70); }, (void)0);
    if (w >= 4 && __all(__hip_atomic_load((gu32*)FL + FL_FC + ((L * 64 * 4 + rc) * 8 + 2 * w - 8 + ((l >> 3) & 1)) * 32 + (l & 7), RLX_AGENT) != 0u)) {
      reinterpret_cast<unsigned*>(smem + Q_CN)[w] = 1u;
#pragma unroll
      for (int i = 0; i < 4; ++i) glds16c(Sb + (4 * w - 16 + i) * 1024, l * 16, sb + Q_PR + (4 * w - 16 + i) * 1024);
#pragma unroll
      for (int i = 0; i < 8; ++i) glds16c(Xb + (8 * w - 32 + i) * 1024, l * 16, sb + (w - 4) * 16384 + i * 1024);
    }
    cs = f32x4{};
#pragma unroll
    for (int ks = 0; ks < 8; ++ks) cs = MFMA16(BF8(one), HALF(wv[ks >> 1], ks & 1), cs);
    asm volatile("" : "+v"(cs));
    SST(7);
    WAIT_VM(0);
    RAW_BAR();
    SST(8);
    W_READ(wv, 1);
#pragma unroll
    for (int ks = 8; ks < 16; ++ks) cs = MFMA16(BF8(one), HALF(wv[ks >> 1], ks & 1), cs);
    cp[32 + c] = cs[0];
#undef HALF
#undef W_STAGE
#undef W_READ
  }
  SST(9);
  const int got = __builtin_amdgcn_readfirstlane(reinterpret_cast<unsigned*>(smem + Q_CN)[4 + (w >> 1)]);
  const v4u rsQ = mk_srd(ws + OFF_QK + L * SZ_QK, SZ_QK);
  const v4u rsV = mk_srd(ws + OFF_VF + L * SZ_O, SZ_O);
  const v4u rsM = mk_srd(ws + (w >= 4 ? OFF_MEMV : OFF_MEMK) + L * SZ_MEM, SZ_MEM);
  const v4u rsF = mk_srd(FL + FL_FC + (L * 64 * 4 + rc) * 8 * 32 + w * 32, (64 * 32 - rc * 8 - w) * 128);
  const v4u rsFQ = mk_srd(FL + FL_FQ + (L * 64 * 32 + h) * 32, 64 * 4096 - h * 128);
  v8u pv;
  pv[0] = sb + 65536 + g * 512 + c * 16;
  pv[1] = (c + 16 * (2 * (w & 1) + (g >> 1))) * 16 + 4096 * (g & 1);
  pv[2] = l * 16;
  pv[3] = sb + Q_ST + w * 1024 + c * 4;
  pv[4] = sb + Q_ST + w * 1024 + g * 16;
  pv[5] = pv[7] = 0u;
  pv[6] = got;
  const char* dbg = nullptr;
  asm volatile("s_mov_b64 exec, -1\ns_waitcnt vmcnt(0) lgkmcnt(0)\ns_nop 4\n" Q_ASM
               ::"{v[64:71]}"(wq[0]), "{v[72:79]}"(wq[1]), "{v[80:87]}"(wq[2]), "{v[88:95]}"(wq[3]), "{v[96:103]}"(wq[4]),
               "{v[104:111]}"(wq[5]), "{v[112:119]}"(wq[6]), "{v[120:127]}"(wq[7]), "{v[128:135]}"(wk[0]), "{v[136:143]}"(wk[1]),
               "{v[144:151]}"(wk[2]), "{v[152:159]}"(wk[3]), "{v[160:167]}"(wk[4]), "{v[168:175]}"(wk[5]), "{v[176:183]}"(wk[6]),
               "{v[184:191]}"(wk[7]), "{v[192:199]}"(wv[0]), "{v[200:207]}"(wv[1]), "{v[208:215]}"(wv[2]), "{v[216:223]}"(wv[3]),
               "{v[224:231]}"(wv[4]), "{v[232:239]}"(wv[5]), "{v[240:247]}"(wv[6]), "{v[248:255]}"(wv[7]), "{v[0:7]}"(pv),
               "{s[40:43]}"(rsQ), "{s[44:47]}"(rsV), "{s[48:51]}"(rsM), "{s[52:55]}"(rsF), "{s[56:59]}"(rsFQ), "{s[60:61]}"(Xb + got * 32768 + w * 4096),
               "{s[62:63]}"(Sb + got * 16384 + w * 2048), "{s64}"(NSEG), "{s65}"((h * 16 + (w >> 1)) * 1024), "{s66}"((h * 512 + w * 64) * 16),
               "{s67}"(h * 2048 + (w & 3) * 512), "{s68}"(sb + got * 65536 + (w >> 1) * 16384 + (w & 1) * 4096), "{s69}"(sb + Q_ST + w * 1024), "{s70}"(sb + Q_CN),
               "{s71}"(0), "{s72}"(0), "{s73}"(sb + Q_PR + w * 2048), "{s74}"(sb + Q_WP + 2048), "{s75}"(sb + Q_WP + w * 256), "{s[36:37]}"(dbg)
               : "memory", "vcc", "scc", "s88", "v63");
}

constexpr int MEGA_WGS = 256;
constexpr size_t MEGA_LDS = (size_t)(32 * 4 * 64 * 8 + 8 * SMS + 15 * QS + 128 + 16 * 64 * 8 + 8 * SOS + 512) * 2;
static_assert(MEGA_LDS <= 163840, "lds");
static_assert(MEGA_LDS >= (size_t)AW_CNT + 128, "lds");
static_assert(MEGA_LDS >= (size_t)CV_LDS && 8 * 1568 >= 768 * 16, "lds");
static_assert(MEGA_LDS >= (size_t)2 * 32 * SMS * 2 && MEGA_LDS >= 131072, "lds");
static_assert(MEGA_LDS >= (size_t)8 * 16640, "lds");
__device__ __forceinline__ MegaArgs role_args(const int b, const int wv) {
  auto p = (const __attribute__((address_space(4))) u64*)__builtin_amdgcn_kernarg_segment_ptr();
  asm volatile("" : "+s"(p));
#define KA(i) ((u16*)(__attribute__((address_space(1))) u16*)p[i])
  return MegaArgs{KA(0), KA(1), (char*)KA(2), KA(3), KA(4), KA(5), KA(6), KA(7), KA(8), KA(9), KA(10), KA(11), KA(12), KA(13), b, wv};
#undef KA
}
__global__ __launch_bounds__(512, 2) void mega_kernel(const u16* mem0, const u16* Dskip, char* ws, u16* out, const u16* x0,
                                                      const u16* logAre, const u16* Aim, const u16* Cre, const u16* Cim,
                                                      const u16* logdt, const u16* Wq, const u16* Wk, const u16* Wv,
                                                      const u16* Wo) {
  extern __shared__ __attribute__((aligned(16))) char smem[];
  const int b = vbid(), wv = __builtin_amdgcn_readfirstlane(threadIdx.x >> 6);
#define A role_args(b, wv)
  u64 tag = 0;
  {
    const unsigned t0 = now10();
    if ((b >= 32 && b < 64) || (b >= 200 && b < 232)) start_kgen(A, smem, b < 64 ? b - 32 : b - 168);
    else if ((b >= 64 && b < 104) || b >= 232) tag = start_stats(A, b >= 232 ? b - 192 : b - 64);
    const unsigned dl = (b >= 32 && b < 64) ? DL_C : (b >= 64 && b < 96) ? DL_A : ((b >= 96 && b < 104) || b >= 232) ? DL_R : (b >= 200 && b < 232) ? DL_Q : 0u;
    while (now10() - t0 < dl) __builtin_amdgcn_s_sleep(16);
  }
  if ((EXP_OFF & 1) && b >= 64 && b < 80) return;
  if ((EXP_OFF & 8) && b >= 200 && b < 232) return;
  if ((EXP_OFF & 16) && b >= 32 && b < 64) return;
  if ((EXP_OFF & 32) && b >= 80 && b < 96) return;
  if (b < 64) role_chain(A, smem, b >> 5, b & 31, 0);
  else if (b < 96) role_aw(A, smem, (b >> 4) & 1, b & 15, tag);
  else if (b >= 104 && b < 168) role_conv(A, smem, b - 104);
  else if (b >= 168 && b < 232) role_qkv(A, smem, (b - 168) >> 5, (b - 168) & 31);
  else if (b < 240) role_red(A, smem, 0, b & 15, tag);
  else role_red(A, smem, 1, b - 240, tag);
#undef A
}

extern "C" void kernel_launch(void* const* d_in, const int* in_sizes, int n_in, void* d_out, int out_size, void* d_ws,
                              size_t ws_size, hipStream_t stream) {
  MegaArgs A;
  A.x0 = (const u16*)d_in[0];
  A.logAre = (const u16*)d_in[1];
  A.Aim = (const u16*)d_in[2];
  A.Cre = (const u16*)d_in[3];
  A.Cim = (const u16*)d_in[4];
  A.logdt = (const u16*)d_in[5];
  A.Dskip = (const u16*)d_in[6];
  A.mem0 = (const u16*)d_in[8];
  A.Wq = (const u16*)d_in[9];
  A.Wk = (const u16*)d_in[10];
  A.Wv = (const u16*)d_in[11];
  A.Wo = (const u16*)d_in[12];
  A.ws = (char*)d_ws;
  A.out = (u16*)d_out;

  (void)hipFuncSetAttribute((const void*)mega_kernel, hipFuncAttributeMaxDynamicSharedMemorySize, (int)MEGA_LDS);
  mega_kernel<<<MEGA_WGS, 512, MEGA_LDS, stream>>>(A.mem0, A.Dskip, A.ws, A.out, A.x0, A.logAre, A.Aim, A.Cre, A.Cim, A.logdt,
                                                   A.Wq, A.Wk, A.Wv, A.Wo);
}
```

```cpp
#include <hip/hip_runtime.h>
#include <hip/hip_bf16.h>
#include <stdint.h>

constexpr int L_SEQ = 2048;
constexpr int DM    = 512;
constexpr int NST   = 128;
constexpr int NH    = 32;
constexpr int DH    = 128;
constexpr int HD    = NH * DH;
constexpr int MM    = 8;
constexpr int SEG   = 32;
constexpr int NSEG  = L_SEQ / SEG;
constexpr int LK    = MM + SEG;

typedef unsigned short u16;
using bf16x8 = __attribute__((ext_vector_type(8))) short;
using f32x4  = __attribute__((ext_vector_type(4))) float;
using f32x16 = __attribute__((ext_vector_type(16))) float;

static __device__ __forceinline__ u16 f2bf(float f) {
  unsigned u = __float_as_uint(f);
  u += 0x7fffu + ((u >> 16) & 1u);
  return (u16)(u >> 16);
}
static __device__ __forceinline__ float bf2f(u16 h) { return __uint_as_float(((unsigned)h) << 16); }
static __device__ __forceinline__ float bfr(float f) { return bf2f(f2bf(f)); }

constexpr size_t SZ_X      = (size_t)L_SEQ * DM * 2;
constexpr size_t OFF_KF    = 0;
constexpr size_t OFF_UT    = OFF_KF + (size_t)DM * L_SEQ * 4;
constexpr size_t OFF_YT    = OFF_UT + SZ_X;
constexpr size_t OFF_XA    = OFF_YT + SZ_X;
constexpr size_t OFF_H     = OFF_XA + SZ_X;
constexpr size_t OFF_X2    = OFF_H + SZ_X;
constexpr size_t OFF_U2    = OFF_X2 + SZ_X;
constexpr size_t OFF_X3    = OFF_U2 + SZ_X;
constexpr size_t SZ_QK     = (size_t)L_SEQ * 2 * HD * 2;
constexpr size_t OFF_QK    = OFF_X3 + SZ_X;
constexpr size_t SZ_O      = (size_t)L_SEQ * HD * 2;
constexpr size_t OFF_VF    = OFF_QK + 2 * SZ_QK;
constexpr size_t SZ_PW     = (size_t)2 * NSEG * 16 * SEG * DM * 2;
constexpr size_t OFF_PW    = OFF_VF + 2 * SZ_O;
constexpr size_t SZ_MEM    = (size_t)NSEG * MM * HD * 2;
constexpr size_t OFF_MEMK  = OFF_PW + SZ_PW;
constexpr size_t OFF_MEMV  = OFF_MEMK + 2 * SZ_MEM;
constexpr size_t OFF_GT    = OFF_MEMV + 2 * SZ_MEM;
constexpr size_t OFF_EP    = OFF_GT + 65536;
constexpr size_t OFF_RD    = OFF_EP + 4096;
constexpr size_t OFF_KG    = OFF_RD + 4096;
constexpr size_t OFF_G     = OFF_KG + 8192;
constexpr size_t SZ_ACC    = (size_t)2 * 32768;
constexpr size_t OFF_Z     = OFF_GT + (size_t)64 * DM * 4;
constexpr size_t OFF_ACC   = OFF_Z;
constexpr size_t OFF_FL    = OFF_ACC + 2 * SZ_ACC;
constexpr int FL_FK  = 0;
constexpr int FL_FW  = FL_FK + 64 * 8 * 32;
constexpr int FL_FR  = FL_FW + 2 * 64 * 16 * 32;
constexpr int FL_FC  = FL_FR + 64 * 16 * 32;
constexpr int FL_FQ  = FL_FC + 2 * 64 * 32 * 32;
constexpr int FL_FP  = FL_FQ + 2 * 64 * 32 * 32;
constexpr int FL_FZ  = FL_FP + 64 * 32 * 32;
constexpr int FL_TMO = FL_FZ + 16 * 32;
constexpr int FL_END = FL_TMO + 64;
constexpr size_t Z_BYTES   = OFF_FL + (size_t)FL_END * 4 - OFF_Z;
constexpr size_t WS_TOTAL  = OFF_Z + Z_BYTES;
static_assert(Z_BYTES % 16 == 0 && OFF_Z % 256 == 0 && Z_BYTES <= 4 * 64 * 8192 && OFF_G + 2048 <= OFF_Z, "zero region");
static_assert(WS_TOTAL + (4 << 20) <= (size_t)256 * 1024 * 1024, "workspace too large");

static_assert(WS_TOTAL - OFF_XA <= (size_t)384 * 8 * 65536, "translation warm-up");
constexpr int CKN = 2208;
static_assert((size_t)DM * 2 * CKN * 2 <= (size_t)DM * L_SEQ * 4 + SZ_X, "K buffer");
__device__ __forceinline__ float pos_of_index(int p) {
  if (p < 256) return (float)p;
  if (p < 384) return (float)(256 + 2 * (p - 256));
  if (p < 512) return (float)(512 + 4 * (p - 384));
  if (p < 640) return (float)(1024 + 8 * (p - 512));
  return 2048.0f;
}
__device__ __forceinline__ int index_of_l(int l) {
  float pf = bfr((float)l);
  int p = (int)pf;
  if (p < 256) return p;
  if (p < 512) return 256 + ((p - 256) >> 1);
  if (p < 1024) return 384 + ((p - 512) >> 2);
  if (p < 2048) return 512 + ((p - 1024) >> 3);
  return 640;
}

template <int CTRL> __device__ __forceinline__ float dppr(float x) {
  return __builtin_bit_cast(float, __builtin_amdgcn_update_dpp(0, __builtin_bit_cast(int, x), CTRL, 0xf, 0xf, false));
}
__device__ __forceinline__ float wave_sum(float v) {
  v += dppr<0x121>(v);
  v += dppr<0x122>(v);
  v += dppr<0x124>(v);
  v += dppr<0x128>(v);
  {
    auto r = __builtin_amdgcn_permlane16_swap(__float_as_uint(v), __float_as_uint(v), false, false);
    v = __uint_as_float(r[0]) + __uint_as_float(r[1]);
  }
  {
    auto r = __builtin_amdgcn_permlane32_swap(__float_as_uint(v), __float_as_uint(v), false, false);
    v = __uint_as_float(r[0]) + __uint_as_float(r[1]);
  }
  return v;
}
__device__ __forceinline__ void ln_row8(float (&v)[8]) {
  float s = 0.f;
#pragma unroll
  for (int j = 0; j < 8; ++j) s += v[j];
  s = wave_sum(s);
  const float mean = s * (1.0f / DM);
  float q = 0.f;
#pragma unroll
  for (int j = 0; j < 8; ++j) { float t = v[j] - mean; q += t * t; }
  q = wave_sum(q);
  const float var = q * (1.0f / DM);
  const float mb = bfr(mean);
  const float sd = bfr(sqrtf(bfr(bfr(var) + 1e-5f)));
#pragma unroll
  for (int j = 0; j < 8; ++j) v[j] = bfr(bfr(v[j] - mb) / sd);
}

#define RLX_AGENT __ATOMIC_RELAXED, __HIP_MEMORY_SCOPE_AGENT
typedef __attribute__((address_space(1))) unsigned gu32;
typedef unsigned v4u __attribute__((ext_vector_type(4)));
constexpr int AUX_SC1 = 16;
constexpr unsigned SPIN_CAP = 1u << 18;

__device__ __forceinline__ bool wait_flags(const unsigned* flags, int lane, int nfl, unsigned want, unsigned* tmo) {
  for (unsigned spins = 0;; ++spins) {
    unsigned v = want;
    if (lane < nfl) v = __hip_atomic_load((gu32*)flags + lane * 32, RLX_AGENT);
    if (__all(v >= want)) break;
    if (spins > SPIN_CAP) {
      if (lane == 0) __hip_atomic_store((gu32*)tmo, 1u, RLX_AGENT);
      return false;
    }
  }
  __builtin_amdgcn_fence(__ATOMIC_ACQUIRE, "wavefront");
  return true;
}

#define STAMP(i)
#define STAMP_DUMP()
#define WSTAMP(i)
template <int CTRL> __device__ __forceinline__ float dppf(float old, float x) {
  return __builtin_bit_cast(float, __builtin_amdgcn_update_dpp(__builtin_bit_cast(int, old), __builtin_bit_cast(int, x), CTRL, 0xf, 0xf, false));
}
__device__ __forceinline__ float wave_max_dpp(float x) {
  const float ninf = -3e38f;
  x = fmaxf(x, dppf<0x111>(ninf, x));
  x = fmaxf(x, dppf<0x112>(ninf, x));
  x = fmaxf(x, dppf<0x114>(ninf, x));
  x = fmaxf(x, dppf<0x118>(ninf, x));
  x = fmaxf(x, dppf<0x142>(ninf, x));
  x = fmaxf(x, dppf<0x143>(ninf, x));
  return __builtin_bit_cast(float, __builtin_amdgcn_readlane(__builtin_bit_cast(int, x), 63));
}
__device__ __forceinline__ float wave_sum_dpp(float x) {
  x += dppf<0x111>(0.f, x);
  x += dppf<0x112>(0.f, x);
  x += dppf<0x114>(0.f, x);
  x += dppf<0x118>(0.f, x);
  x += dppf<0x142>(0.f, x);
  x += dppf<0x143>(0.f, x);
  return __builtin_bit_cast(float, __builtin_amdgcn_readlane(__builtin_bit_cast(int, x), 63));
}
constexpr int SMS = 528;
constexpr int QS = 144;
constexpr int VS = 80;
constexpr int SOS = 144;
#define VAR_NREP 4
constexpr int NREP = VAR_NREP;
__device__ __forceinline__ float xsum16(float x) {
  auto r = __builtin_amdgcn_permlane16_swap(__float_as_uint(x), __float_as_uint(x), false, false);
  return __uint_as_float(r[0]) + __uint_as_float(r[1]);
}
__device__ __forceinline__ float xsum32(float x) {
  auto r = __builtin_amdgcn_permlane32_swap(__float_as_uint(x), __float_as_uint(x), false, false);
  return __uint_as_float(r[0]) + __uint_as_float(r[1]);
}
__device__ __forceinline__ float xmax16(float x) {
  auto r = __builtin_amdgcn_permlane16_swap(__float_as_uint(x), __float_as_uint(x), false, false);
  return fmaxf(__uint_as_float(r[0]), __uint_as_float(r[1]));
}
__device__ __forceinline__ float xmax32(float x) {
  auto r = __builtin_amdgcn_permlane32_swap(__float_as_uint(x), __float_as_uint(x), false, false);
  return fmaxf(__uint_as_float(r[0]), __uint_as_float(r[1]));
}
__device__ __forceinline__ float red_mod4(float x) {
  x += dppf<0x124>(0.f, x);
  x += dppf<0x128>(0.f, x);
  return xsum32(xsum16(x));
}
typedef __bf16 bf16x2_t __attribute__((ext_vector_type(2)));
typedef float f32x2_t __attribute__((ext_vector_type(2)));
__device__ __forceinline__ unsigned pk2(float a, float b) {
  f32x2_t v = {a, b};
  bf16x2_t r = __builtin_convertvector(v, bf16x2_t);
  return __builtin_bit_cast(unsigned, r);
}
__device__ __forceinline__ float one(float x) {
  asm("" : "+v"(x));
  return x;
}
__device__ __forceinline__ unsigned pk2s(float a, float b) { return pk2(one(a), one(b)); }
__device__ __forceinline__ float dot2sq(const unsigned x, const float acc) {
  return __builtin_amdgcn_fdot2_f32_bf16(__builtin_bit_cast(bf16x2_t, x), __builtin_bit_cast(bf16x2_t, x), acc, false);
}
__device__ __forceinline__ float lo16(unsigned u) { return __uint_as_float(u << 16); }
__device__ __forceinline__ float hi16(unsigned u) { return __uint_as_float(u & 0xffff0000u); }
__device__ __forceinline__ float bfr1(float x) { return lo16(pk2(x, x)); }
__device__ __forceinline__ float half_sum(float v) {
  v += dppr<0x121>(v);
  v += dppr<0x122>(v);
  v += dppr<0x124>(v);
  v += dppr<0x128>(v);
  return xsum16(v);
}
__device__ __forceinline__ void scale_pair(float a, float b, float scale, float& oa, float& ob) {
  unsigned u = pk2(a, b);
  unsigned v = pk2s(lo16(u) * scale, hi16(u) * scale);
  oa = lo16(v);
  ob = hi16(v);
}

#define XMAP 2
__device__ __forceinline__ int vbid() {
  const int b = blockIdx.x, x = b & 7, t = b >> 3;
  if (XMAP == 0) {
    if (b < 64) return (b & 32) + 4 * x + (t & 3);
    if (b < 96) return (b & ~15) + 2 * x + (t & 1);
    if (b >= 104 && b < 168) return 104 + 8 * x + t - 13;
    if (b >= 168 && b < 232) return 168 + ((b - 168) & 32) + 4 * x + ((t - 21) & 3);
    return b;
  }
  if (XMAP == 2) {
    if (x < 4) return t < 16 ? 4 * (t & 8) + 8 * x + (t & 7) : 104 + 16 * x + t - 16;
    const int y = x - 4;
    if (t >= 24) return (t >= 28 ? 240 : y < 2 ? 96 : 224) + 4 * y + (t & 3);
    if (t < 16) return 168 + 4 * (t & 8) + 8 * y + (t & 7);
    return 64 + 4 * (t & 4) + 4 * y + (t & 3);
  }
  if (x < 2) return 2 * (t & 16) + 16 * x + (t & 15);
  const int i = 16 * (x - 2) + (t & 15);
  if (t < 16) {
    const int k = (i * 171) >> 9, r = i - 3 * k;
    return r == 0 ? 168 + k : r == 1 ? 200 + k : 64 + 16 * (k & 1) + (k >> 1);
  }
  const int g = i >> 3, e = i & 7;
  if (g == 3) return 96 + e;
  if (g == 7) return 232 + e;
  if (g == 9) return 240 + e;
  if (g == 11) return 248 + e;
  return 104 + 8 * (g - (g > 3) - (g > 7) - (g > 9)) + e;
}
#define MSTAMP()
#define WSTAMP2()
#define SST(k)
#define TS(k, ss, t)
#define LS(k, ss)
#define LS_DUMP()
struct MegaArgs {
  const u16* mem0;
  const u16* Dskip;
  char* ws;
  u16* out;
  const u16* x0;
  const u16 *logAre, *Aim, *Cre, *Cim, *logdt;
  const u16 *Wq, *Wk, *Wv, *Wo;
  int vb, wv;
};

__device__ __forceinline__ int lane_id() {
  int l;
  asm volatile("v_mbcnt_lo_u32_b32 %0, -1, 0\n\tv_mbcnt_hi_u32_b32 %0, -1, %0" : "=v"(l));
  __builtin_assume(l >= 0 && l < 64);
  return l;
}
__device__ __forceinline__ bool spin_timeout(unsigned spins, unsigned* tmo) {
  if (spins > SPIN_CAP) {
    if (lane_id() == 0) __hip_atomic_store((gu32*)tmo, 1u, RLX_AGENT);
    return true;
  }
  return false;
}
#define ROT_H (4 * (h & 1))
#define SLP_H 12
#define HX0 6
#define CVSTEP 2
#define CVLATE 64
#define PACE 218
#define PACE0 6
#define RPRIO 3
#define DL_R2 1400
#define PNL 8
#define GSL 12
#define AWDIR 1
#define AWAT 0
#define AWRP 0
#define PEPS 4
#define FGATE 0
#define DL_C 0
#define DL_A 800
#define DL_R 0
#define DL_Q 0
#define KC0 0
#define CPRIO 1
#define P1PRIO 3
#define QLEAD 2
#define POLL_SLEEP 12
#define SLP_C POLL_SLEEP
#define SLP_A POLL_SLEEP
#define SLP_W POLL_SLEEP
#define SLP_R POLL_SLEEP
#define SLP_V POLL_SLEEP
#define SLP_Q POLL_SLEEP
#define SLP_M 4
#define AWPRIO 2
#define SLP_S 8
#define SPOS 4
#define CVPRE 12
#define QPD 3
#define QPRI 2
#define TLBW 1
#define QHV 0
#define QCHK 0
#define LEADN 99
#define GATE_V 170
#define GATE_R 380
#define GATE_N 380
#define AWAHEAD 1
#define GATE_M 150
#define PRI_A 3
#define PRI_WA 2
#define PRI_WB 1
#define AWSKIP 3
#define PRI_0 3
#define AWDEF 0
#define SLP_F 2
#define EXP_OFF 0
#define AWSPEC 1
#define CVTHR 48
#define NR_C 8
#define NR_R 8
#define NR_W 8
#define NR_N 2
#define QAHEAD 0
#define NS0 4
#define SLP_N POLL_SLEEP
template <int SLP, int WPL>
__device__ __forceinline__ bool wait_n(const unsigned* f, int n, unsigned* tmo) {
  const int l = lane_id();
  for (unsigned spins = 0;; ++spins) {
    unsigned v = 1u;
    if (l < n) v = __hip_atomic_load((gu32*)f + (l / WPL) * 32 + l % WPL, RLX_AGENT);
    if (__all(v != 0u)) break;
    if (spin_timeout(spins, tmo)) return false;
    __builtin_amdgcn_s_sleep(SLP);
  }
  asm volatile("" ::: "memory");
  return true;
}
__device__ __forceinline__ unsigned now10() { return (unsigned)__builtin_amdgcn_s_memrealtime(); }
__device__ __forceinline__ void gate(const unsigned t) {
  for (int i = 0; i < 64; ++i) {
    const int d = (int)(t - now10());
    if (d <= 0) break;
    if (d > 100) __builtin_amdgcn_s_sleep(32);
    else if (d > 25) __builtin_amdgcn_s_sleep(8);
    else __builtin_amdgcn_s_sleep(2);
  }
}
template <int SLP, int WPL, int GT>
__device__ __forceinline__ void grp_wait(const bool leader, volatile unsigned* sw, const unsigned val, const unsigned* f, int n,
                                         unsigned* tmo, unsigned& tl) {
  if (leader) {
    const int l = lane_id();
    for (unsigned spins = 0;; ++spins) {
      unsigned v = 1u;
      if (l < n) v = __hip_atomic_load((gu32*)f + (l / WPL) * 32 + l % WPL, RLX_AGENT);
      if (__all(v != 0u) || spin_timeout(spins, tmo)) break;
      if (GT && spins == 0) gate(tl);
      else __builtin_amdgcn_s_sleep(SLP);
    }
    if (GT) tl = now10() + GT;
    if (sw && l == 0) *sw = val;
  } else {
    for (unsigned spins = 0; *sw < val; ++spins) {
      if (spins > 64u * SPIN_CAP) break;
      if (FGATE && GT && spins == 0) gate(tl);
      __builtin_amdgcn_s_sleep(SLP_F);
    }
    if (FGATE && GT) tl = now10() + GT;
  }
  asm volatile("" ::: "memory");
}
template <int SLP, int WPL>
__device__ __forceinline__ bool wg_wait(const unsigned* f, int n, unsigned* tmo) {
  if (threadIdx.x < 64) (void)wait_n<SLP, WPL>(f, n, tmo);
  __syncthreads();
  asm volatile("" ::: "memory");
  return true;
}
__device__ __forceinline__ void set_flag(unsigned* f) { __hip_atomic_store((gu32*)f, 1u, RLX_AGENT); }
#define DRAIN() asm volatile("s_waitcnt vmcnt(0)" ::: "memory")
#define MKRS(ptr, bytes) __builtin_amdgcn_make_buffer_rsrc((void*)(ptr), 0, (int)(bytes), 0x00020000)
typedef unsigned v2u __attribute__((ext_vector_type(2)));
#define LD128(rs, voff, soff) __builtin_amdgcn_raw_buffer_load_b128(rs, voff, soff, AUX_SC1)
#define LD64(rs, voff, soff) __builtin_amdgcn_raw_buffer_load_b64(rs, voff, soff, AUX_SC1)
#define ST128(v, rs, voff, soff)                                              \
  do {                                                                        \
    const v4u st_d_ = (v);                                                    \
    __builtin_amdgcn_raw_buffer_store_b128(st_d_, rs, voff, soff, AUX_SC1);   \
    asm volatile("s_nop 1" ::"v"(st_d_) : "memory");                          \
  } while (0)
#define ST64(v, rs, voff, soff) __builtin_amdgcn_raw_buffer_store_b64(v, rs, voff, soff, AUX_SC1)
#define BF8(x) __builtin_bit_cast(bf16x8, x)
#define MFMA16(a, b, c) __builtin_amdgcn_mfma_f32_16x16x32_bf16(a, b, c, 0, 0, 0)
typedef short bf16x4 __attribute__((ext_vector_type(4)));
#define MFMA16K(a, b, c) \
  __builtin_amdgcn_mfma_f32_16x16x16bf16_1k(__builtin_bit_cast(bf16x4, a), __builtin_bit_cast(bf16x4, b), c, 0, 0, 0)
__device__ __forceinline__ float fmax_nn(float a, float b) { return __builtin_elementwise_maximum(a, b); }
typedef __bf16 bfv8 __attribute__((ext_vector_type(8)));
typedef __bf16 bfv16 __attribute__((ext_vector_type(16)));
typedef unsigned v8u __attribute__((ext_vector_type(8)));
#define SMF(a, b, c, idx) \
  __builtin_amdgcn_smfmac_f32_16x16x64_bf16(__builtin_bit_cast(bfv8, a), __builtin_bit_cast(bfv16, b), c, idx, 0, 0)
__device__ __forceinline__ float swap_add32(float a, float b) {
  auto r = __builtin_amdgcn_permlane32_swap(__float_as_uint(a), __float_as_uint(b), false, false);
  return __uint_as_float(r[0]) + __uint_as_float(r[1]);
}
__device__ __forceinline__ float swap_add16(float a, float b) {
  auto r = __builtin_amdgcn_permlane16_swap(__float_as_uint(a), __float_as_uint(b), false, false);
  return __uint_as_float(r[0]) + __uint_as_float(r[1]);
}
__device__ __forceinline__ v4u smf_fix(const v4u d) {
  auto x = __builtin_amdgcn_permlane16_swap(d.x, d.z, false, false);
  auto y = __builtin_amdgcn_permlane16_swap(d.y, d.w, false, false);
  return v4u{x[0], x[1], y[0], y[1]};
}
__device__ __forceinline__ v8u smf_bfrag(const u16* p) {
  const v2u a = *reinterpret_cast<const v2u*>(p), b = *reinterpret_cast<const v2u*>(p + 8);
  const v2u c = *reinterpret_cast<const v2u*>(p + 32), d = *reinterpret_cast<const v2u*>(p + 40);
  return v8u{a.x, b.x, a.y, b.y, c.x, d.x, c.y, d.y};
}

#define VAR_PPA 0
#define VAR_PPB 0
#define VAR_THR 0
#define VAR_THR2 0
#define CV_LEAD 100
#define VAR_EARLY 1
#define VAR_DRAIN 1
#define VAR_D1 0
#define VAR_D2 0
#define VAR_DR2 1
#define VAR_S1 0
#define VAR_S2 0
typedef __attribute__((address_space(1))) unsigned long long gu64;
typedef __attribute__((address_space(1))) char gch;
__device__ __forceinline__ int cvt_rpi(float x) {
  int r;
  asm("v_cvt_rpi_i32_f32 %0, %1" : "=v"(r) : "v"(x));
  return r;
}
typedef unsigned long long u64;
__device__ __forceinline__ u64 call_tag(const MegaArgs& A) {
  u64 t = *(const __attribute__((address_space(4))) u64*)(A.ws + OFF_EP) * 6364136223846793005ull + 1442695040888963407ull;
  asm volatile("" : "+s"(t)::"memory");
  return t;
}
__device__ __forceinline__ u64 tagged(const u64 tag, const unsigned v) { return (tag & 0xffffffffffffull) | (u64)(v >> 16) << 48; }
__device__ __forceinline__ bool has_tag(const u64 tag, const unsigned lo, const unsigned hi) {
  return lo == (unsigned)tag && (hi & 0xffffu) == ((unsigned)(tag >> 32) & 0xffffu);
}
__device__ __forceinline__ u64 ready_look(const MegaArgs& A) {
  return __hip_atomic_load((gu64*)(A.ws + OFF_RD) + (blockIdx.x & 7) * 64 + lane_id(), RLX_AGENT);
}
__device__ __forceinline__ void ready_wait(const MegaArgs& A, const u64 tag, u64 v) {
  for (unsigned spins = 0; !__all(v == tag) && spins < SPIN_CAP; ++spins) {
    __builtin_amdgcn_s_sleep(8);
    v = ready_look(A);
  }
  asm volatile("" ::: "memory");
}
typedef short v4s __attribute__((ext_vector_type(4)));
typedef __attribute__((address_space(3))) v4s* lp4s;
__device__ __forceinline__ void glds16(const void* sbase, const unsigned voff, const unsigned lds) {
  unsigned keep;
  asm volatile("s_nop 4\n\ts_mov_b32 %0, m0\n\ts_mov_b32 m0, %3\n\ts_nop 0\n\tglobal_load_lds_dwordx4 %1, %2\n\ts_mov_b32 m0, %0"
               : "=&s"(keep)
               : "v"(voff), "s"(sbase), "s"(lds)
               : "memory");
}
__device__ __forceinline__ void glds16c(const void* sbase, const unsigned voff, const unsigned lds) {
  unsigned keep;
  asm volatile("s_nop 4\n\ts_mov_b32 %0, m0\n\ts_mov_b32 m0, %3\n\ts_nop 0\n\tglobal_load_lds_dwordx4 %1, %2 sc1\n\ts_mov_b32 m0, %0"
               : "=&s"(keep)
               : "v"(voff), "s"(sbase), "s"(lds)
               : "memory");
}
__device__ __forceinline__ void glds64c(const void* sbase, const unsigned voff, const unsigned lds) {
  unsigned keep;
  asm volatile("s_nop 4\n\ts_mov_b32 %0, m0\n\ts_mov_b32 m0, %3\n\ts_nop 0\n\tglobal_load_lds_dwordx4 %1, %2 sc1\n\t"
               "global_load_lds_dwordx4 %1, %2 offset:1024 sc1\n\tglobal_load_lds_dwordx4 %1, %2 offset:2048 sc1\n\t"
               "global_load_lds_dwordx4 %1, %2 offset:3072 sc1\n\ts_mov_b32 m0, %0"
               : "=&s"(keep)
               : "v"(voff), "s"(sbase), "s"(lds)
               : "memory");
}
__device__ __forceinline__ v2u trd(const unsigned a) {
  return __builtin_bit_cast(v2u, __builtin_amdgcn_ds_read_tr16_b64_v4i16((lp4s)(unsigned long)a));
}
__device__ __forceinline__ unsigned lds_base(char* smem) { return (unsigned)(unsigned long)(__attribute__((address_space(3))) char*)smem; }
#define RAW_BAR()                                           \
  do {                                                      \
    asm volatile("s_waitcnt lgkmcnt(0)" ::: "memory");      \
    __builtin_amdgcn_s_barrier();                           \
    asm volatile("" ::: "memory");                          \
  } while (0)
#define WAIT_VM(n) asm volatile("s_waitcnt vmcnt(" #n ")" ::: "memory")
template <bool SP>
__device__ __forceinline__ void wqkv_issue(const u16* Wm, const int hf, const int h, const int w, const int l, const unsigned sb) {
  const char* base = (const char*)Wm + (size_t)(256 * hf + 32 * w) * (HD * 2) + h * (DH * 2);
#pragma unroll
  for (int i = 0; i < 8; ++i) {
    const int r = l >> 4;
    const int f = SP ? ((r & 1) | ((i & 1) << 1) | (((i >> 1) & 1) << 2)) : (r | (((i >> 1) & 1) << 2));
    glds16(base, (unsigned)((4 * i + r) * (HD * 2) + (((l & 15) ^ (f << 1)) << 4)), sb + w * 16384 + hf * 8192 + i * 1024);
  }
}

__device__ __forceinline__ unsigned row_stats(const v4u raw) {
  float v[8];
#pragma unroll
  for (int e = 0; e < 4; ++e) { v[2 * e] = lo16(raw[e]); v[2 * e + 1] = hi16(raw[e]); }
  float s = 0.f;
#pragma unroll
  for (int e = 0; e < 8; ++e) s += v[e];
  const float mean = wave_sum(s) * (1.0f / DM);
  float q = 0.f;
#pragma unroll
  for (int e = 0; e < 8; ++e) { const float t = v[e] - mean; q += t * t; }
  const float var = wave_sum(q) * (1.0f / DM);
  const float sd = bfr(sqrtf(bfr(bfr(var) + 1e-5f)));
  return (unsigned)f2bf(mean) | ((unsigned)f2bf(sd) << 16);
}
__device__ __forceinline__ void ask_slow(const MegaArgs& A, const int k, unsigned (&t)[3]) {
  const int l = lane_id(), x = blockIdx.x & 7;
  const char *wq = (const char*)A.Wq, *wv = (const char*)A.Wv;
  asm("" : "+s"(wq), "+s"(wv));
#pragma unroll
  for (int m = 0; m < 3; ++m) {
    const char* W = m == 1 ? (const char*)A.Wk : ((m == 0) == (x < 4)) ? wv : wq;
    const int a = (int)((unsigned long)W >> 8) + 8 * (x & 3), h = k < 32 ? ((1 - a) & 3) + 4 * (k & 1) : (31 - a) & 31;
    const int r = k < 32 ? k >> 1 : k - 32;
    t[m] = __builtin_amdgcn_raw_buffer_load_b32(MKRS(W, (unsigned)k < 48u && h < 8 ? DM * HD * 2 : 0), (l >> 1) * (HD * 2) + (l & 1) * 128,
                                                (32 * (r & 15)) * (HD * 2) + (8 * (x & 3) + (h & 7)) * 256, 0);
  }
}
#define ASKED(t) asm volatile("" ::"v"(t[0]), "v"(t[1]), "v"(t[2]))
template <int N>
__device__ __forceinline__ u64 start_stats(const MegaArgs& A, const int idx) {
  const int w = A.wv, l = lane_id();
  const int r0 = N * (8 * idx + w);
  unsigned ta[3], tb[3];
  v4u raw[N];
#pragma unroll
  for (int i = 0; i < N; ++i) raw[i] = *reinterpret_cast<const v4u*>(A.x0 + (size_t)(r0 + i) * DM + l * 8);
  asm volatile("" ::: "memory");
  ask_slow(A, 16 * w + (idx & 3) + (idx >> 4) * N, ta);
  if (N > 4) ask_slow(A, 16 * w + (idx & 3) + 12, tb);
  const u64 tag = call_tag(A);
  unsigned stw = 0u;
#pragma unroll
  for (int i = 0; i < N; ++i) {
    const unsigned t = row_stats(raw[i]);
    if ((l >> 1) == i) stw = t;
  }
  if (l < 2 * N) __hip_atomic_store((gu64*)(A.ws + OFF_GT) + 2 * r0 + l, tagged(tag, (l & 1) ? stw : stw << 16), RLX_AGENT);
  SST(0);
  ASKED(ta);
  if (N > 4) ASKED(tb);
  return tag;
}
struct Cx {
  f32x2_t r, i;
};
__device__ __forceinline__ Cx cmul(const Cx a, const Cx b) { return Cx{a.r * b.r - a.i * b.i, a.r * b.i + a.i * b.r}; }
struct KGen {
  unsigned pa, pb, pc, pd;
  u16 pe;
  f32x2_t ar, ai;
  Cx C;
  char* st;
  v4u bh[8], bl[8];
  __device__ __forceinline__ void load(const MegaArgs& A, const int d, const int l) {
    pa = *reinterpret_cast<const unsigned*>(A.logAre + d * NST + 2 * l);
    pb = *reinterpret_cast<const unsigned*>(A.Aim + d * NST + 2 * l);
    pc = *reinterpret_cast<const unsigned*>(A.Cre + d * NST + 2 * l);
    pd = *reinterpret_cast<const unsigned*>(A.Cim + d * NST + 2 * l);
    pe = A.logdt[d];
  }
  __device__ __forceinline__ void setup() {
    const float dt = bfr(expf(bf2f(pe)));
    ar = f32x2_t{-bfr(expf(lo16(pa))) * dt, -bfr(expf(hi16(pa))) * dt};
    ai = f32x2_t{lo16(pb) * dt, hi16(pb) * dt};
    C = Cx{f32x2_t{lo16(pc) * dt, hi16(pc) * dt}, f32x2_t{lo16(pd) * dt, hi16(pd) * dt}};
  }
  __device__ __forceinline__ Cx power(const float p) const {
    Cx z;
#pragma unroll
    for (int e = 0; e < 2; ++e) {
      const float x = ai[e] * p;
      const float f = __builtin_fmaf(x, 6.4206382e-9f, __builtin_fmaf(x, 0.15915494f, -__builtin_rintf(x * 0.15915494f)));
      const float m = __expf(ar[e] * p);
      z.r[e] = m * __builtin_amdgcn_cosf(f);
      z.i[e] = m * __builtin_amdgcn_sinf(f);
    }
    return z;
  }
  __device__ __forceinline__ char* rows(char* p, Cx y, const Cx s, const int n) const {
#pragma unroll 1
    for (int i = 0; i < n; i += 2) {
#pragma unroll
      for (int k = 0; k < 2; ++k) {
        const unsigned h0 = pk2(y.r.x, y.r.y), h1 = pk2(y.i.x, y.i.y);
        const f32x2_t a = y.r - f32x2_t{lo16(h0), hi16(h0)}, b = y.i - f32x2_t{lo16(h1), hi16(h1)};
        *reinterpret_cast<v2u*>(p + 1040 * k) = v2u{h0, h1};
        *reinterpret_cast<v2u*>(p + 1040 * k + 512) = v2u{pk2(a.x, a.y), pk2(b.x, b.y)};
        y = cmul(y, s);
      }
      p += 2080;
    }
    return p;
  }
  __device__ __forceinline__ void get(v4u (&h)[8], v4u (&o)[8], const int l) const {
    const char* p = st + (l & 15) * 1040 + (l >> 4) * 16;
    asm volatile("" ::: "memory");
#pragma unroll
    for (int k = 0; k < 8; ++k) {
      h[k] = *reinterpret_cast<const v4u*>(p + 64 * k);
      o[k] = *reinterpret_cast<const v4u*>(p + 512 + 64 * k);
    }
    asm volatile("" ::: "memory");
  }
  __device__ __forceinline__ void columns(const int l) {
    Cx s = power(16.f);
    s.i = -s.i;
    rows(st + l * 8, Cx{f32x2_t{1.f, 1.f}, f32x2_t{0.f, 0.f}}, s, 16);
    get(bh, bl, l);
  }
  __device__ __forceinline__ v4u tile(const int l) const {
    v4u h[8], o[8];
    get(h, o, l);
    f32x4 d = {}, e = {}, f = {};
#pragma unroll
    for (int k = 0; k < 8; ++k) {
      d = MFMA16(BF8(h[k]), BF8(bh[k]), d);
      e = MFMA16(BF8(o[k]), BF8(bh[k]), e);
      f = MFMA16(BF8(h[k]), BF8(bl[k]), f);
    }
    d = (d + (e + f)) * 2.0f;
    v4u r;
#pragma unroll
    for (int i = 0; i < 4; ++i) r[i] = pk2(d[i], d[i] - lo16(pk2(d[i], d[i])));
    return r;
  }
};
__device__ __forceinline__ void start_kgen(const MegaArgs& A, char* smem, const int kg) {
  const int w = A.wv, l = lane_id(), tid = 64 * w + l, c = l & 15, g = l >> 4;
  KGen G;
  G.load(A, 8 * kg + w, l);
  {
    const __amdgpu_buffer_rsrc_t rz = MKRS(A.ws + OFF_Z, Z_BYTES);
#pragma unroll
    for (int i = 0; i < 4; ++i) ST128((v4u{0u, 0u, 0u, 0u}), rz, tid * 16, (i * 64 + kg) * 8192);
  }
  const u64 tag = call_tag(A);
  DRAIN();
  __syncthreads();
  if (tid < 8) __hip_atomic_store((gu64*)(A.ws + OFF_RD) + tid * 64 + kg, tag, RLX_AGENT);
  asm volatile("" : "+v"(G.pa), "+v"(G.pb), "+v"(G.pe));
  G.setup();
  G.st = smem + w * 16640;
  const __amdgpu_buffer_rsrc_t rs = MKRS(A.ws + OFF_KF + (8 * kg + w) * 2048, 386 * 4);
  SST(1);
  unsigned tw = 0u;
  if (TLBW && tid < 384) {
    const unsigned o = (unsigned)OFF_XA + (unsigned)(tid * 8 + ((blockIdx.x >> 3) & 7)) * 65536u;
    if (o < WS_TOTAL) asm volatile("global_load_dword %0, %1, %2" : "=v"(tw) : "v"(o), "s"(A.ws) : "memory");
  }
  G.columns(l);
  const Cx zb = G.power(256.f);
  Cx b = G.C, z = G.power(2.f);
#pragma unroll 1
  for (int f = 1; f < 9;) {
    char* p = G.st + l * 8;
    const int fe = f == 1 ? 4 : 9;
#pragma unroll 1
    for (; f < fe; ++f) {
      b = cmul(b, zb);
      if (f == 2 || f == 4) z = cmul(z, z);
      p = G.rows(p, b, z, f == 1 ? 8 : f < 4 ? 4 : 2);
    }
    const v4u r = G.tile(l);
    if (f == 4) {
      ST128(r, rs, (g < 2 ? 8 * c + 4 * g : 64 * g + 4 * c) * 4, 0);
    } else {
      ST64((v2u{r.x, r.y}), rs, (256 + 64 * g + 2 * c) * 4, 0);
      ST64((v2u{r.z, r.w}), rs, (288 + 64 * g + 2 * c) * 4, 0);
    }
  }
  SST(2);
  DRAIN();
  asm volatile("" ::"v"(tw));
  if (l == 0) __hip_atomic_store((gu64*)(A.ws + OFF_KG) + kg * 16 + w, tag, RLX_AGENT);
  __syncthreads();
  SST(3);
}

constexpr int C_SM = 131072, C_SO = C_SM + 8192, C_KT = C_SO + 2048, C_X = C_KT + 16384, C_SQ = C_X + 1024;
constexpr int C_SK = C_SQ + (7 * QS + 128) * 2;
static_assert(C_SK + 8 * QS * 2 <= 163840, "lds");
#define TDSLOT 0
#define CHAIN_ASM_ATT \
  "s_mov_b32 s92, 1152\n" \
  "s_mov_b32 s93, 576\n" \
  "s_mov_b32 s94, 2272\n" \
  "s_mov_b32 s95, 272\n" \
  "s_mov_b32 s96, 288\n" \
  "s_mov_b32 s91, 0x40000001\n" \
  "s_mov_b32 s84, 0x4b400000\n" \
  "s_mov_b32 s85, 0x4b400000\n" \
  "s_mov_b32 s78, 0x3e0293ee\n" \
  "s_mov_b32 s76, 0xff00ff00\n" \
  "s_mov_b32 s77, 0xff00ff00\n" \
  "s_add_u32 s58, s56, s60\n" \
  "s_addc_u32 s59, s57, 0\n" \
  "s_lshl_b32 s98, s73, 10\n" \
  "s_add_u32 s98, s98, s74\n" \
  "s_add_u32 s98, s98, 131072\n" \
  "s_lshl_b32 s97, s73, 5\n" \
  "s_add_u32 s97, s97, s74\n" \
  "s_add_u32 s97, s97, 158720\n" \
  "s_lshl_b32 s99, s73, 10\n" \
  "v_mbcnt_lo_u32_b32 v48, -1, 0\n" \
  "v_mbcnt_hi_u32_b32 v48, -1, v48\n" \
  "s_add_u32 s88, s74, 157760\n" \
  "v_mov_b32 v59, s88\n" \
  "v_and_b32 v49, 15, v48\n" \
  "v_lshl_add_u32 v8, v49, 1, s97\n" \
  "v_bfe_u32 v49, v48, 4, 1\n" \
  "v_mad_u32_u24 v8, v49, s92, v8\n" \
  "v_lshrrev_b32 v49, 5, v48\n" \
  "v_mad_u32_u24 v9, v49, s93, v8\n" \
  "v_mad_u32_u24 v8, v49, s94, v8\n" \
  "v_add_u32 v10, s99, v1\n" \
  "s_lshr_b32 s88, s99, 2\n" \
  "s_add_u32 s88, s88, s74\n" \
  "s_add_u32 s88, s88, 139264\n" \
  "v_lshl_add_u32 v7, v49, 10, s88\n" \
  "v_and_b32 v49, 31, v48\n" \
  "v_lshl_add_u32 v7, v49, 3, v7\n" \
  "v_mov_b64 v[12:13], 0\n" \
  "v_mov_b64 v[14:15], 0\n" \
  "v_mov_b64 v[16:17], 0\n" \
  "v_mov_b64 v[18:19], 0\n" \
  "v_mov_b64 v[20:21], 0\n" \
  "v_mov_b64 v[22:23], 0\n" \
  "v_mov_b32 v24, 0x44870000\n" \
  "v_mul_f32 v24, s72, v24\n" \
  "s_nop 0\n" \
  "v_readfirstlane_b32 s86, v24\n" \
  "s_lshr_b32 s86, s86, 23\n" \
  "s_addk_i32 s86, 0xff83\n" \
  "s_ashr_i32 s86, s86, 1\n" \
  "s_max_i32 s86, s86, -20\n" \
  "s_min_i32 s86, s86, 100\n" \
  "s_mov_b32 s82, s86\n" \
  "s_lshr_b32 s83, s72, 23\n" \
  "s_sub_i32 s83, s83, 242\n" \
  "s_lshl_b32 s88, s73, 10\n" \
  "v_add_u32 v24, s88, v1\n" \
  "ds_read_b64 v[32:33], v24 offset:0\n" \
  "ds_read_b64 v[36:37], v24 offset:8\n" \
  "s_waitcnt lgkmcnt(0)\n" \
  "v_mov_b32 v11, 0\n" \
  "v_dot2c_f32_bf16 v11, v32, v32\n" \
  "v_dot2c_f32_bf16 v11, v33, v33\n" \
  "v_dot2c_f32_bf16 v11, v36, v36\n" \
  "v_dot2c_f32_bf16 v11, v37, v37\n" \
  "s_nop 1\n" \
  "s_nop 0\n" \
  "v_cmpx_lt_f32 vcc, 2.0, v11\n" \
  "ds_max_u32 v59, v11\n" \
  "s_mov_b64 exec, -1\n" \
  ".LCA_TOP_%=:\n" \
  "s_waitcnt lgkmcnt(0)\n" \
  "s_barrier\n" \
  "ds_read_b128 v[40:43], v1 offset:0\n" \
  "ds_read_b128 v[44:47], v1 offset:1024\n" \
  "ds_read_b128 v[48:51], v1 offset:2048\n" \
  "ds_read_b128 v[52:55], v1 offset:3072\n" \
  "ds_read_b128 v[56:59], v1 offset:4096\n" \
  "ds_read_b128 v[60:63], v1 offset:5120\n" \
  "s_cmp_lg_u32 s73, 0\n" \
  "s_cbranch_scc1 .LCA_NOZ_%=\n" \
  "s_xor_b32 s88, s60, 0x8000\n" \
  "s_add_u32 s88, s88, s61\n" \
  "buffer_store_dwordx2 v[12:13], v5, s[40:43], s88 offen sc1\n" \
  ".LCA_NOZ_%=:\n" \
  "s_waitcnt lgkmcnt(5)\n" \
  "v_smfmac_f32_16x16x64_bf16 v[20:23], v[40:43], v[192:199], v0\n" \
  "v_smfmac_f32_16x16x64_bf16 v[12:15], v[40:43], v[64:71], v0\n" \
  "v_smfmac_f32_16x16x64_bf16 v[16:19], v[40:43], v[128:135], v0\n" \
  "ds_read_b128 v[40:43], v1 offset:6144\n" \
  "s_waitcnt lgkmcnt(5)\n" \
  "v_smfmac_f32_16x16x64_bf16 v[20:23], v[44:47], v[200:207], v0\n" \
  "v_smfmac_f32_16x16x64_bf16 v[12:15], v[44:47], v[72:79], v0\n" \
  "v_smfmac_f32_16x16x64_bf16 v[16:19], v[44:47], v[136:143], v0\n" \
  "ds_read_b128 v[44:47], v1 offset:7168\n" \
  "s_waitcnt lgkmcnt(5)\n" \
  "v_smfmac_f32_16x16x64_bf16 v[20:23], v[48:51], v[208:215], v0\n" \
  "v_smfmac_f32_16x16x64_bf16 v[12:15], v[48:51], v[80:87], v0\n" \
  "v_smfmac_f32_16x16x64_bf16 v[16:19], v[48:51], v[144:151], v0\n" \
  "ds_read_b128 v[24:27], v1 offset:10240\n" \
  "ds_read_b128 v[28:31], v1 offset:14336\n" \
  "s_waitcnt lgkmcnt(6)\n" \
  "v_smfmac_f32_16x16x64_bf16 v[20:23], v[52:55], v[216:223], v0\n" \
  "v_smfmac_f32_16x16x64_bf16 v[12:15], v[52:55], v[88:95], v0\n" \
  "v_smfmac_f32_16x16x64_bf16 v[16:19], v[52:55], v[152:159], v0\n" \
  "ds_read_b128 v[32:35], v1 offset:11264\n" \
  "ds_read_b128 v[36:39], v1 offset:15360\n" \
  "s_waitcnt lgkmcnt(7)\n" \
  "v_smfmac_f32_16x16x64_bf16 v[20:23], v[56:59], v[224:231], v0\n" \
  "v_smfmac_f32_16x16x64_bf16 v[12:15], v[56:59], v[96:103], v0\n" \
  "v_smfmac_f32_16x16x64_bf16 v[16:19], v[56:59], v[160:167], v0\n" \
  "s_waitcnt lgkmcnt(6)\n" \
  "v_smfmac_f32_16x16x64_bf16 v[20:23], v[60:63], v[232:239], v0\n" \
  "v_smfmac_f32_16x16x64_bf16 v[12:15], v[60:63], v[104:111], v0\n" \
  "v_smfmac_f32_16x16x64_bf16 v[16:19], v[60:63], v[168:175], v0\n" \
  "s_waitcnt lgkmcnt(5)\n" \
  "v_smfmac_f32_16x16x64_bf16 v[20:23], v[40:43], v[240:247], v0\n" \
  "v_smfmac_f32_16x16x64_bf16 v[12:15], v[40:43], v[112:119], v0\n" \
  "v_smfmac_f32_16x16x64_bf16 v[16:19], v[40:43], v[176:183], v0\n" \
  "s_waitcnt lgkmcnt(4)\n" \
  "v_smfmac_f32_16x16x64_bf16 v[20:23], v[44:47], v[248:255], v0\n" \
  "v_smfmac_f32_16x16x64_bf16 v[12:15], v[44:47], v[120:127], v0\n" \
  "v_smfmac_f32_16x16x64_bf16 v[16:19], v[44:47], v[184:191], v0\n" \
  "s_cmp_eq_u32 s71, 0\n" \
  "s_nop 4\n" \
  "v_permlane32_swap_b32 v20, v22\n" \
  "v_permlane32_swap_b32 v21, v23\n" \
  "v_pk_add_f32 v[20:21], v[20:21], v[22:23]\n" \
  "v_permlane32_swap_b32 v12, v16\n" \
  "v_permlane32_swap_b32 v13, v17\n" \
  "v_cvt_pk_bf16_f32 v20, v20, v21\n" \
  "v_pk_add_f32 v[12:13], v[12:13], v[16:17]\n" \
  "v_permlane32_swap_b32 v14, v18\n" \
  "v_permlane32_swap_b32 v15, v19\n" \
  "ds_write_b16 v9, v20 offset:128\n" \
  "v_cvt_pk_bf16_f32 v12, v12, v13\n" \
  "v_pk_add_f32 v[14:15], v[14:15], v[18:19]\n" \
  "ds_write_b16_d16_hi v9, v20 offset:416\n" \
  "ds_write_b16 v8, v12\n" \
  "v_cvt_pk_bf16_f32 v14, v14, v15\n" \
  "ds_write_b16_d16_hi v8, v12 offset:288\n" \
  "ds_write_b16 v8, v14 offset:576\n" \
  "ds_write_b16_d16_hi v8, v14 offset:864\n" \
  "s_setprio 3\n" \
  "s_waitcnt lgkmcnt(0)\n" \
  "s_barrier\n" \
  "s_cbranch_scc1 .LCA_LAST_%=\n" \
  "ds_read_b32 v11, v1 offset:26688\n" \
  "ds_read_b128 v[40:43], v2 offset:0\n" \
  "ds_read_b128 v[44:47], v3 offset:0\n" \
  "ds_read_b128 v[48:51], v2 offset:64\n" \
  "ds_read_b128 v[52:55], v3 offset:64\n" \
  "ds_read_b128 v[56:59], v2 offset:128\n" \
  "ds_read_b128 v[60:63], v3 offset:128\n" \
  "s_waitcnt lgkmcnt(5)\n" \
  "v_mfma_f32_16x16x32_bf16 v[12:15], v[24:27], v[40:43], 0\n" \
  "v_mfma_f32_16x16x32_bf16 v[16:19], v[28:31], v[40:43], 0\n" \
  "v_readfirstlane_b32 s86, v11\n" \
  "ds_read_b128 v[24:27], v1 offset:12288\n" \
  "ds_read_b128 v[28:31], v1 offset:16384\n" \
  "s_waitcnt lgkmcnt(6)\n" \
  "v_mfma_f32_16x16x32_bf16 v[20:23], v[44:47], v[40:43], 0\n" \
  "s_lshr_b32 s86, s86, 23\n" \
  "s_add_i32 s86, s86, s83\n" \
  "ds_read_b128 v[40:43], v2 offset:192\n" \
  "ds_read_b128 v[44:47], v3 offset:192\n" \
  "s_waitcnt lgkmcnt(7)\n" \
  "v_mfma_f32_16x16x32_bf16 v[12:15], v[32:35], v[48:51], v[12:15]\n" \
  "v_mfma_f32_16x16x32_bf16 v[16:19], v[36:39], v[48:51], v[16:19]\n" \
  "s_ashr_i32 s86, s86, 1\n" \
  "s_max_i32 s86, s86, s82\n" \
  "ds_read_b128 v[32:35], v1 offset:13312\n" \
  "ds_read_b128 v[36:39], v1 offset:17408\n" \
  "s_waitcnt lgkmcnt(8)\n" \
  "v_mfma_f32_16x16x32_bf16 v[20:23], v[52:55], v[48:51], v[20:23]\n" \
  "s_min_i32 s86, s86, 100\n" \
  "s_lshl_b32 s86, s86, 23\n" \
  "s_sub_i32 s84, 0x4a800000, s86\n" \
  "ds_read_b128 v[48:51], v10 offset:18432\n" \
  "ds_read_b128 v[52:55], v10 offset:22528\n" \
  "s_waitcnt lgkmcnt(8)\n" \
  "v_mfma_f32_16x16x32_bf16 v[20:23], v[60:63], v[56:59], v[20:23]\n" \
  "s_waitcnt lgkmcnt(6)\n" \
  "v_mfma_f32_16x16x32_bf16 v[12:15], v[24:27], v[56:59], v[12:15]\n" \
  "v_mfma_f32_16x16x32_bf16 v[16:19], v[28:31], v[56:59], v[16:19]\n" \
  "ds_read_b128 v[56:59], v6 offset:0\n" \
  "ds_read_b128 v[60:63], v6 offset:1024\n" \
  "s_waitcnt lgkmcnt(6)\n" \
  "v_mfma_f32_16x16x32_bf16 v[20:23], v[44:47], v[40:43], v[20:23]\n" \
  "ds_read_b128 v[24:27], v6 offset:2048\n" \
  "ds_read_b128 v[28:31], v6 offset:3072\n" \
  "s_waitcnt lgkmcnt(6)\n" \
  "v_mfma_f32_16x16x32_bf16 v[12:15], v[32:35], v[40:43], v[12:15]\n" \
  "v_mfma_f32_16x16x32_bf16 v[16:19], v[36:39], v[40:43], v[16:19]\n" \
  "ds_read_b128 v[32:35], v6 offset:8192\n" \
  "ds_read_b128 v[36:39], v6 offset:9216\n" \
  "s_waitcnt lgkmcnt(6)\n" \
  "v_cndmask_b32 v20, v20, v21, s[76:77]\n" \
  "v_mov_b32 v41, 0\n" \
  "v_mov_b32 v42, 0\n" \
  "s_nop 1\n" \
  "v_cndmask_b32 v12, v12, v16, s[76:77]\n" \
  "v_cndmask_b32 v13, v13, v17, s[76:77]\n" \
  "v_cndmask_b32 v14, v14, v18, s[76:77]\n" \
  "v_cndmask_b32 v15, v15, v19, s[76:77]\n" \
  "v_maximum3_f32 v16, v20, v12, v13\n" \
  "v_maximum3_f32 v16, v16, v14, v15\n" \
  "s_nop 1\n" \
  "v_max_f32_dpp v16, v16, v16 row_ror:8 row_mask:0xf bank_mask:0xf\n" \
  "v_mov_b32 v17, v16\n" \
  "s_nop 1\n" \
  "v_permlane16_swap_b32 v16, v17\n" \
  "v_max_f32 v16, v16, v17\n" \
  "v_mov_b32 v17, v16\n" \
  "s_nop 1\n" \
  "v_permlane32_swap_b32 v16, v17\n" \
  "v_max_f32 v16, v16, v17\n" \
  "v_mul_f32 v16, s78, v16\n" \
  "v_pk_fma_f32 v[12:13], v[12:13], s[78:79], v[16:17] op_sel_hi:[1,0,0] neg_lo:[0,0,1] neg_hi:[0,0,1]\n" \
  "v_pk_fma_f32 v[14:15], v[14:15], s[78:79], v[16:17] op_sel_hi:[1,0,0] neg_lo:[0,0,1] neg_hi:[0,0,1]\n" \
  "v_fma_f32 v20, v20, s78, -v16\n" \
  "v_exp_f32 v12, v12\n" \
  "v_exp_f32 v13, v13\n" \
  "v_exp_f32 v14, v14\n" \
  "v_exp_f32 v15, v15\n" \
  "v_exp_f32 v20, v20\n" \
  "s_barrier\n" \
  "ds_read_b64 v[22:23], v7 offset:0\n" \
  "v_cvt_pk_bf16_f32 v44, v12, v13\n" \
  "v_cvt_pk_bf16_f32 v45, v14, v15\n" \
  "v_pk_add_f32 v[8:9], v[12:13], v[14:15]\n" \
  "v_mov_b32_dpp v46, v44 row_ror:8 row_mask:0xf bank_mask:0xf\n" \
  "v_mov_b32_dpp v17, v20 row_ror:8 row_mask:0xf bank_mask:0xf\n" \
  "v_mov_b32_dpp v47, v45 row_ror:8 row_mask:0xf bank_mask:0xf\n" \
  "v_add_f32 v10, v8, v20\n" \
  "v_cvt_pk_bf16_f32 v40, v20, v17\n" \
  "v_add_f32 v10, v10, v9\n" \
  "v_mfma_f32_16x16x32_bf16 v[12:15], v[48:51], v[44:47], 0\n" \
  "v_cvt_pk_bf16_f32 v43, v20, v17\n" \
  "v_add_f32_dpp v10, v10, v10 row_ror:8 row_mask:0xf bank_mask:0xf\n" \
  "v_mfma_f32_16x16x32_bf16 v[16:19], v[52:55], v[44:47], 0\n" \
  "v_mov_b32 v11, v10\n" \
  "s_waitcnt lgkmcnt(0)\n" \
  "v_mfma_f32_16x16x16_bf16 v[12:15], v[22:23], v[40:41], v[12:15]\n" \
  "v_mfma_f32_16x16x16_bf16 v[16:19], v[22:23], v[42:43], v[16:19]\n" \
  "v_permlane16_swap_b32 v10, v11\n" \
  "v_add_f32 v10, v10, v11\n" \
  "ds_read_b128 v[48:51], v6 offset:10240\n" \
  "v_mov_b32 v11, v10\n" \
  "ds_read_b128 v[52:55], v6 offset:11264\n" \
  "v_mov_b64 v[20:21], 0\n" \
  "v_mov_b64 v[22:23], 0\n" \
  "v_permlane32_swap_b32 v10, v11\n" \
  "v_add_f32 v10, v10, v11\n" \
  "v_rcp_f32 v10, v10\n" \
  "s_nop 0\n" \
  "v_pk_mul_f32 v[12:13], v[12:13], v[10:11] op_sel_hi:[1,0]\n" \
  "v_pk_mul_f32 v[14:15], v[14:15], v[10:11] op_sel_hi:[1,0]\n" \
  "v_pk_mul_f32 v[16:17], v[16:17], v[10:11] op_sel_hi:[1,0]\n" \
  "v_pk_mul_f32 v[18:19], v[18:19], v[10:11] op_sel_hi:[1,0]\n" \
  "v_cvt_pk_bf16_f32 v40, v12, v13\n" \
  "v_cvt_pk_bf16_f32 v41, v14, v15\n" \
  "v_cvt_pk_bf16_f32 v42, v16, v17\n" \
  "ds_write_b64 v4, v[40:41]\n" \
  "v_cvt_pk_bf16_f32 v43, v18, v19\n" \
  "ds_write_b64 v4, v[42:43] offset:1024\n" \
  "v_mov_b64 v[12:13], 0\n" \
  "v_mov_b64 v[14:15], 0\n" \
  "v_mov_b64 v[16:17], 0\n" \
  "v_mov_b64 v[18:19], 0\n" \
  "v_mov_b64 v[8:9], 0\n" \
  "v_mov_b64 v[10:11], 0\n" \
  "s_setprio 0\n" \
  "s_waitcnt lgkmcnt(0)\n" \
  "s_barrier\n" \
  "ds_read_b128 v[40:43], v1 offset:8192\n" \
  "ds_read_b128 v[44:47], v1 offset:9216\n" \
  "s_waitcnt lgkmcnt(1)\n" \
  "v_smfmac_f32_16x16x64_bf16 v[12:15], v[40:43], v[56:63], v0\n" \
  "ds_read_b128 v[56:59], v6 offset:4096\n" \
  "ds_read_b128 v[60:63], v6 offset:5120\n" \
  "v_smfmac_f32_16x16x64_bf16 v[16:19], v[40:43], v[24:31], v0\n" \
  "ds_read_b128 v[24:27], v6 offset:6144\n" \
  "ds_read_b128 v[28:31], v6 offset:7168\n" \
  "s_waitcnt lgkmcnt(4)\n" \
  "v_smfmac_f32_16x16x64_bf16 v[12:15], v[44:47], v[32:39], v0\n" \
  "ds_read_b128 v[32:35], v6 offset:12288\n" \
  "ds_read_b128 v[36:39], v6 offset:13312\n" \
  "v_smfmac_f32_16x16x64_bf16 v[16:19], v[44:47], v[48:55], v0\n" \
  "ds_read_b128 v[48:51], v6 offset:14336\n" \
  "ds_read_b128 v[52:55], v6 offset:15360\n" \
  "s_nop 5\n" \
  "v_permlane32_swap_b32 v12, v17\n" \
  "v_permlane32_swap_b32 v13, v16\n" \
  "v_permlane32_swap_b32 v14, v19\n" \
  "v_permlane32_swap_b32 v15, v18\n" \
  "v_pk_add_f32 v[12:13], v[12:13], v[16:17] op_sel:[0,1] op_sel_hi:[1,0]\n" \
  "v_pk_add_f32 v[14:15], v[14:15], v[18:19] op_sel:[0,1] op_sel_hi:[1,0]\n" \
  "v_pk_fma_f32 v[12:13], v[12:13], s[84:85], s[84:85] op_sel:[0,0,1] op_sel_hi:[1,0,1]\n" \
  "v_pk_fma_f32 v[14:15], v[14:15], s[84:85], s[84:85] op_sel:[0,0,1] op_sel_hi:[1,0,1]\n" \
  "v_lshl_add_u32 v12, v12, 6, s91\n" \
  "v_lshl_add_u32 v13, v13, 2, s75\n" \
  "global_atomic_add_x2 v5, v[12:13], s[58:59] offset:0\n" \
  "v_lshl_add_u32 v14, v14, 6, s91\n" \
  "v_lshl_add_u32 v15, v15, 2, s75\n" \
  "global_atomic_add_x2 v5, v[14:15], s[58:59] offset:2048\n" \
  "s_waitcnt lgkmcnt(6)\n" \
  "v_smfmac_f32_16x16x64_bf16 v[20:23], v[40:43], v[56:63], v0\n" \
  "s_waitcnt lgkmcnt(4)\n" \
  "v_smfmac_f32_16x16x64_bf16 v[8:11], v[40:43], v[24:31], v0\n" \
  "s_waitcnt lgkmcnt(2)\n" \
  "v_smfmac_f32_16x16x64_bf16 v[20:23], v[44:47], v[32:39], v0\n" \
  "s_waitcnt lgkmcnt(0)\n" \
  "v_smfmac_f32_16x16x64_bf16 v[8:11], v[44:47], v[48:55], v0\n" \
  "s_nop 7\n" \
  "v_permlane32_swap_b32 v20, v9\n" \
  "v_permlane32_swap_b32 v21, v8\n" \
  "v_permlane32_swap_b32 v22, v11\n" \
  "v_permlane32_swap_b32 v23, v10\n" \
  "v_pk_add_f32 v[20:21], v[20:21], v[8:9] op_sel:[0,1] op_sel_hi:[1,0]\n" \
  "v_pk_add_f32 v[22:23], v[22:23], v[10:11] op_sel:[0,1] op_sel_hi:[1,0]\n" \
  "v_pk_fma_f32 v[20:21], v[20:21], s[84:85], s[84:85] op_sel:[0,0,1] op_sel_hi:[1,0,1]\n" \
  "v_pk_fma_f32 v[22:23], v[22:23], s[84:85], s[84:85] op_sel:[0,0,1] op_sel_hi:[1,0,1]\n" \
  "v_lshl_add_u32 v20, v20, 6, s91\n" \
  "v_lshl_add_u32 v21, v21, 2, s75\n" \
  "global_atomic_add_x2 v5, v[20:21], s[58:59] offset:3072\n" \
  "v_lshl_add_u32 v22, v22, 6, s91\n" \
  "v_lshl_add_u32 v23, v23, 2, s75\n" \
  "global_atomic_add_x2 v5, v[22:23], s[58:59] offset:1024\n" \
  "s_memrealtime s[38:39]\n" \
  "v_mbcnt_lo_u32_b32 v48, -1, 0\n" \
  "v_mbcnt_hi_u32_b32 v48, -1, v48\n" \
  "s_add_u32 s88, s74, 157760\n" \
  "v_mov_b32 v59, s88\n" \
  "v_lshl_add_u32 v52, v48, 2, v59\n" \
  "ds_read_b32 v52, v52 offset:192\n" \
  "v_lshrrev_b32 v49, 3, v48\n" \
  "v_and_b32 v50, 7, v48\n" \
  "v_lshlrev_b32 v50, 4, v50\n" \
  "v_lshl_or_b32 v44, v49, 8, v50\n" \
  "s_mov_b32 s87, 0x40000\n" \
  "v_and_b32 v49, 15, v48\n" \
  "v_lshl_add_u32 v8, v49, 1, s97\n" \
  "v_bfe_u32 v49, v48, 4, 1\n" \
  "v_mad_u32_u24 v8, v49, s92, v8\n" \
  "v_lshrrev_b32 v49, 5, v48\n" \
  "v_mad_u32_u24 v9, v49, s93, v8\n" \
  "v_mad_u32_u24 v8, v49, s94, v8\n" \
  "v_add_u32 v10, s99, v1\n" \
  "s_lshr_b32 s88, s99, 2\n" \
  "s_add_u32 s88, s88, s74\n" \
  "s_add_u32 s88, s88, 139264\n" \
  "v_lshl_add_u32 v7, v49, 10, s88\n" \
  "v_and_b32 v49, 31, v48\n" \
  "v_lshl_add_u32 v7, v49, 3, v7\n" \
  "v_mov_b64 v[12:13], 0\n" \
  "v_mov_b64 v[14:15], 0\n" \
  "v_mov_b64 v[16:17], 0\n" \
  "v_mov_b64 v[18:19], 0\n" \
  "v_mov_b64 v[20:21], 0\n" \
  "v_mov_b64 v[22:23], 0\n" \
  "v_and_b32 v50, 3, v48\n" \
  "v_lshl_add_u32 v40, v50, 2, s98\n" \
  "v_lshrrev_b32 v50, 4, v48\n" \
  "v_mad_u32_u24 v40, v50, s95, v40\n" \
  "v_bfe_u32 v49, v48, 3, 1\n" \
  "v_lshl_add_u32 v40, v49, 6, v40\n" \
  "v_bfe_u32 v49, v48, 2, 1\n" \
  "v_lshl_add_u32 v40, v49, 7, v40\n" \
  "v_xor_b32 v41, 16, v40\n" \
  "v_xor_b32 v42, 32, v40\n" \
  "v_xor_b32 v43, 48, v40\n" \
  "s_add_i32 s80, s86, 0x32800000\n" \
  "s_add_i32 s81, s86, 0x33800000\n" \
  "s_add_i32 s88, s86, 0xb4000000\n" \
  "v_mov_b32 v46, s88\n" \
  "v_mov_b32 v47, 0\n" \
  "v_mov_b32 v11, 0\n" \
  "s_mov_b32 s89, s60\n" \
  "s_xor_b32 s60, s60, 0x80\n" \
  "s_bitcmp0_b32 s60, 7\n" \
  "s_cselect_b32 s88, 0x8000, 0\n" \
  "s_add_u32 s60, s60, s88\n" \
  "s_and_b32 s60, s60, 0xffff\n" \
  "s_add_u32 s58, s56, s60\n" \
  "s_addc_u32 s59, s57, 0\n" \
  "s_sub_u32 s71, s71, 1\n" \
  "s_waitcnt lgkmcnt(0)\n" \
  "s_sub_u32 s39, s38, 150\n" \
  "v_subrev_u32 v52, s39, v52\n" \
  "v_cmp_lt_u32 vcc, 0x1000, v52\n" \
  "s_cmp_eq_u64 vcc, 0\n" \
  "s_cbranch_scc1 .LCA_OKM_%=\n" \
  ".LCA_AGN_%=:\n" \
  "buffer_load_dword v52, v5, s[0:3], s66 offen sc1\n" \
  "s_sub_u32 s87, s87, 1\n" \
  "s_cbranch_scc1 .LCA_END_%=\n" \
  "s_waitcnt vmcnt(0)\n" \
  "v_cmp_eq_u32 vcc, 0, v52\n" \
  "v_subrev_u32 v52, s39, v52\n" \
  "s_cmp_eq_u64 vcc, 0\n" \
  "s_cbranch_scc1 .LCA_OKM_%=\n" \
  "s_sleep 4\n" \
  "s_branch .LCA_AGN_%=\n" \
  ".LCA_OKM_%=:\n" \
  "s_nop 1\n" \
  "v_max_i32_dpp v52, v52, v52 row_ror:1 row_mask:0xf bank_mask:0xf\n" \
  "s_nop 1\n" \
  "v_max_i32_dpp v52, v52, v52 row_ror:2 row_mask:0xf bank_mask:0xf\n" \
  "s_nop 1\n" \
  "v_max_i32_dpp v52, v52, v52 row_ror:4 row_mask:0xf bank_mask:0xf\n" \
  "s_movk_i32 vcc_lo, 150\n" \
  "v_readlane_b32 s88, v52, 0\n" \
  "s_max_i32 vcc_lo, vcc_lo, s88\n" \
  "v_readlane_b32 s88, v52, 16\n" \
  "s_max_i32 vcc_lo, vcc_lo, s88\n" \
  "v_readlane_b32 s88, v52, 32\n" \
  "s_max_i32 vcc_lo, vcc_lo, s88\n" \
  "v_readlane_b32 s88, v52, 48\n" \
  "s_max_i32 vcc_lo, vcc_lo, s88\n" \
  "s_add_u32 s66, s66, 1024\n" \
  "s_add_u32 vcc_lo, vcc_lo, s39\n" \
  "s_add_u32 vcc_lo, vcc_lo, 30\n" \
  ".LCA_TW_%=:\n" \
  "s_memrealtime s[38:39]\n" \
  "s_waitcnt lgkmcnt(0)\n" \
  "s_sub_u32 s38, s38, vcc_lo\n" \
  "s_cmp_lt_i32 s38, 0\n" \
  "s_cbranch_scc1 .LCA_TW_%=\n" \
  "buffer_load_dwordx4 v[24:27], v44, s[40:43], s89 offen sc1\n" \
  "buffer_load_dwordx4 v[28:31], v44, s[40:43], s89 offen offset:2048 sc1\n" \
  ".LCA_SEE_%=:\n" \
  "s_waitcnt vmcnt(1)\n" \
  "v_bitop3_b32 v45, v24, v26, 32 bitop3:0x80\n" \
  "v_alignbit_b32 v32, v25, v24, 2\n" \
  "v_alignbit_b32 v34, v27, v26, 2\n" \
  "s_waitcnt vmcnt(0)\n" \
  "v_bitop3_b32 v45, v45, v28, v30 bitop3:0x80\n" \
  "v_cmp_eq_u32 vcc, 0, v45\n" \
  "v_cvt_f32_i32 v32, v32\n" \
  "v_cvt_f32_i32 v33, v25\n" \
  "s_cmp_eq_u64 vcc, 0\n" \
  "s_cbranch_scc0 .LCA_POLL_%=\n" \
  "v_cvt_f32_i32 v34, v34\n" \
  "v_cvt_f32_i32 v35, v27\n" \
  "v_pk_fma_f32 v[32:33], v[32:33], s[80:81], v[46:47]\n" \
  "v_alignbit_b32 v36, v29, v28, 2\n" \
  "v_pk_fma_f32 v[34:35], v[34:35], s[80:81], v[46:47]\n" \
  "v_alignbit_b32 v38, v31, v30, 2\n" \
  "v_cvt_pk_bf16_f32 v32, v32, v34\n" \
  "v_cvt_pk_bf16_f32 v33, v33, v35\n" \
  "v_cvt_f32_i32 v36, v36\n" \
  "ds_write_b32 v40, v32\n" \
  "v_cvt_f32_i32 v37, v29\n" \
  "ds_write_b32 v41, v33\n" \
  "v_cvt_f32_i32 v38, v38\n" \
  "v_cvt_f32_i32 v39, v31\n" \
  "v_pk_fma_f32 v[36:37], v[36:37], s[80:81], v[46:47]\n" \
  "v_pk_fma_f32 v[38:39], v[38:39], s[80:81], v[46:47]\n" \
  "v_dot2c_f32_bf16 v11, v32, v32\n" \
  "v_cvt_pk_bf16_f32 v36, v36, v38\n" \
  "v_cvt_pk_bf16_f32 v37, v37, v39\n" \
  "ds_write_b32 v42, v36\n" \
  "ds_write_b32 v43, v37\n" \
  "v_dot2c_f32_bf16 v11, v33, v33\n" \
  "v_dot2c_f32_bf16 v11, v36, v36\n" \
  "v_dot2c_f32_bf16 v11, v37, v37\n" \
  "s_nop 1\n" \
  "s_nop 0\n" \
  "v_cmpx_lt_f32 vcc, 2.0, v11\n" \
  "ds_max_u32 v59, v11\n" \
  "s_mov_b64 exec, -1\n" \
  "s_branch .LCA_TOP_%=\n" \
  ".LCA_POLL_%=:\n" \
  "buffer_load_dwordx4 v[24:27], v44, s[40:43], s89 offen sc1\n" \
  "buffer_load_dwordx4 v[28:31], v44, s[40:43], s89 offen offset:2048 sc1\n" \
  "s_sub_u32 s87, s87, 1\n" \
  "s_cbranch_scc1 .LCA_END_%=\n" \
  "s_branch .LCA_SEE_%=\n" \
  ".LCA_LAST_%=:\n" \
  ".LCA_END_%=:\n" \
  "s_endpgm\n"
#define CHAIN_ASM_VO \
  "s_mov_b32 s92, 1152\n" \
  "s_mov_b32 s93, 576\n" \
  "s_mov_b32 s94, 2272\n" \
  "s_mov_b32 s95, 272\n" \
  "s_mov_b32 s96, 288\n" \
  "s_mov_b32 s91, 0x40000001\n" \
  "s_mov_b32 s84, 0x4b400000\n" \
  "s_mov_b32 s85, 0x4b400000\n" \
  "s_mov_b32 s78, 0x3e0293ee\n" \
  "s_mov_b32 s76, 0xff00ff00\n" \
  "s_mov_b32 s77, 0xff00ff00\n" \
  "s_add_u32 s58, s56, s60\n" \
  "s_addc_u32 s59, s57, 0\n" \
  "s_lshl_b32 s98, s73, 10\n" \
  "s_add_u32 s98, s98, s74\n" \
  "s_add_u32 s98, s98, 131072\n" \
  "s_lshl_b32 s97, s73, 5\n" \
  "s_add_u32 s97, s97, s74\n" \
  "s_add_u32 s97, s97, 160992\n" \
  "s_lshl_b32 s99, s73, 11\n" \
  "s_add_u32 s99, s99, s74\n" \
  "s_add_u32 s99, s99, 133120\n" \
  "s_lshl_b32 s94, s73, 4\n" \
  "s_add_u32 s94, s94, s74\n" \
  "s_add_u32 s94, s94, 157760\n" \
  "v_mbcnt_lo_u32_b32 v48, -1, 0\n" \
  "v_mbcnt_hi_u32_b32 v48, -1, v48\n" \
  "s_add_u32 s88, s74, 157760\n" \
  "v_mov_b32 v59, s88\n" \
  "s_sub_u32 s88, s73, 4\n" \
  "v_and_b32 v51, 15, v48\n" \
  "v_lshl_add_u32 v4, v51, 1, s97\n" \
  "v_bfe_u32 v49, v48, 4, 1\n" \
  "v_mad_u32_u24 v4, v49, s92, v4\n" \
  "v_lshrrev_b32 v50, 5, v48\n" \
  "v_mad_u32_u24 v4, v50, s93, v4\n" \
  "v_lshlrev_b32 v8, 4, v49\n" \
  "v_lshl_add_u32 v8, v51, 5, v8\n" \
  "v_lshl_add_u32 v8, v50, 3, v8\n" \
  "s_lshl_b32 s89, s88, 8\n" \
  "s_add_u32 s89, s89, s74\n" \
  "s_add_u32 s89, s89, 139264\n" \
  "v_and_b32 v49, 31, v48\n" \
  "v_lshl_add_u32 v5, v49, 3, s89\n" \
  "v_lshl_add_u32 v5, v50, 10, v5\n" \
  "v_lshrrev_b32 v49, 2, v51\n" \
  "v_and_b32 v50, 5, v51\n" \
  "v_and_or_b32 v49, v49, 2, v50\n" \
  "s_lshl_b32 s89, s88, 6\n" \
  "s_add_u32 s89, s89, s74\n" \
  "s_add_u32 s89, s89, 160992\n" \
  "v_and_b32 v50, 48, v48\n" \
  "v_add_u32 v6, s89, v50\n" \
  "v_mad_u32_u24 v6, v49, s96, v6\n" \
  "v_lshlrev_b32 v7, 3, v50\n" \
  "v_lshl_add_u32 v7, v49, 4, v7\n" \
  "v_and_b32 v49, 2, v51\n" \
  "v_lshl_add_u32 v7, v49, 29, v7\n" \
  "v_mov_b64 v[12:13], 0\n" \
  "v_mov_b64 v[14:15], 0\n" \
  "v_mov_b64 v[16:17], 0\n" \
  "v_mov_b64 v[18:19], 0\n" \
  "v_mov_b64 v[20:21], 0\n" \
  "v_mov_b64 v[22:23], 0\n" \
  "v_lshlrev_b32 v10, 4, v48\n" \
  "s_add_u32 s78, s74, 157952\n" \
  "v_lshl_add_u32 v52, v48, 2, v59\n" \
  "ds_write_b32 v52, v12 offset:192\n" \
  "s_movk_i32 s79, 128\n" \
  "v_mov_b32 v24, 0x44870000\n" \
  "v_mul_f32 v24, s72, v24\n" \
  "s_nop 0\n" \
  "v_readfirstlane_b32 s86, v24\n" \
  "s_lshr_b32 s86, s86, 23\n" \
  "s_addk_i32 s86, 0xff83\n" \
  "s_ashr_i32 s86, s86, 1\n" \
  "s_max_i32 s86, s86, -20\n" \
  "s_min_i32 s86, s86, 100\n" \
  "s_mov_b32 s82, s86\n" \
  "s_lshr_b32 s83, s72, 23\n" \
  "s_sub_i32 s83, s83, 242\n" \
  "s_lshl_b32 s88, s73, 10\n" \
  "v_add_u32 v24, s88, v1\n" \
  "ds_read_b64 v[32:33], v24 offset:0\n" \
  "ds_read_b64 v[36:37], v24 offset:8\n" \
  "s_waitcnt lgkmcnt(0)\n" \
  "v_mov_b32 v11, 0\n" \
  "v_dot2c_f32_bf16 v11, v32, v32\n" \
  "v_dot2c_f32_bf16 v11, v33, v33\n" \
  "v_dot2c_f32_bf16 v11, v36, v36\n" \
  "v_dot2c_f32_bf16 v11, v37, v37\n" \
  "s_nop 1\n" \
  "s_nop 0\n" \
  "v_cmpx_lt_f32 vcc, 2.0, v11\n" \
  "ds_max_u32 v59, v11\n" \
  "s_mov_b64 exec, -1\n" \
  "s_cmp_lg_u32 s61, 0\n" \
  "s_movk_i32 s76, -6976\n" \
  "s_cmovk_i32 s76, 8128\n" \
  "s_movk_i32 s77, 90\n" \
  "s_cmovk_i32 s77, 90\n" \
  "s_cselect_b32 s72, -1, 56\n" \
  ".LCV_TOP_%=:\n" \
  "s_waitcnt lgkmcnt(0)\n" \
  "s_barrier\n" \
  "s_setprio 3\n" \
  "ds_read_b128 v[24:27], v1 offset:0\n" \
  "ds_read_b128 v[28:31], v1 offset:1024\n" \
  "ds_read_b128 v[32:35], v1 offset:2048\n" \
  "ds_read_b128 v[36:39], v1 offset:3072\n" \
  "ds_read_b128 v[40:43], v1 offset:4096\n" \
  "ds_read_b128 v[44:47], v1 offset:5120\n" \
  "ds_read_b128 v[48:51], v1 offset:6144\n" \
  "ds_read_b128 v[52:55], v1 offset:7168\n" \
  "s_memrealtime s[38:39]\n" \
  "s_mov_b64 exec, 15\n" \
  "buffer_load_dword v58, v2, s[44:47], s68 offen sc1\n" \
  "s_mov_b64 exec, -1\n" \
  "s_waitcnt lgkmcnt(7)\n" \
  "v_smfmac_f32_16x16x64_bf16 v[12:15], v[24:27], v[64:71], v0\n" \
  "s_waitcnt lgkmcnt(6)\n" \
  "v_smfmac_f32_16x16x64_bf16 v[12:15], v[28:31], v[72:79], v0\n" \
  "s_waitcnt lgkmcnt(5)\n" \
  "v_smfmac_f32_16x16x64_bf16 v[12:15], v[32:35], v[80:87], v0\n" \
  "s_waitcnt lgkmcnt(4)\n" \
  "v_smfmac_f32_16x16x64_bf16 v[12:15], v[36:39], v[88:95], v0\n" \
  "s_waitcnt lgkmcnt(3)\n" \
  "v_smfmac_f32_16x16x64_bf16 v[12:15], v[40:43], v[96:103], v0\n" \
  "s_waitcnt lgkmcnt(2)\n" \
  "v_smfmac_f32_16x16x64_bf16 v[12:15], v[44:47], v[104:111], v0\n" \
  "s_waitcnt lgkmcnt(1)\n" \
  "v_smfmac_f32_16x16x64_bf16 v[12:15], v[48:51], v[112:119], v0\n" \
  "s_waitcnt lgkmcnt(0)\n" \
  "v_smfmac_f32_16x16x64_bf16 v[12:15], v[52:55], v[120:127], v0\n" \
  "ds_read_b32 v11, v1 offset:26688\n" \
  "s_mov_b32 s61, s38\n" \
  "s_add_u32 s38, s38, s79\n" \
  "v_or_b32 v57, 1, s38\n" \
  "v_add_u32 v56, s67, v10\n" \
  "s_add_u32 s39, s67, s76\n" \
  "s_add_u32 s38, s38, s77\n" \
  "s_bitset1_b32 s38, 0\n" \
  "v_writelane_b32 v56, s39, 1\n" \
  "v_writelane_b32 v57, s38, 1\n" \
  "s_sub_u32 s39, s71, s72\n" \
  "s_cmp_lt_u32 s39, 8\n" \
  "s_cselect_b64 exec, 19, 3\n" \
  "buffer_store_dword v57, v56, s[0:3], 0 offen sc1\n" \
  "s_mov_b64 exec, -1\n" \
  "v_permlane32_swap_b32 v12, v14\n" \
  "v_permlane32_swap_b32 v13, v15\n" \
  "v_pk_add_f32 v[12:13], v[12:13], v[14:15]\n" \
  "s_nop 0\n" \
  "v_cvt_pk_bf16_f32 v12, v12, v13\n" \
  "ds_write_b16 v4, v12\n" \
  "ds_write_b16_d16_hi v4, v12 offset:288\n" \
  "s_setprio 0\n" \
  "v_mov_b64 v[12:13], 0\n" \
  "v_mov_b64 v[14:15], 0\n" \
  "s_waitcnt lgkmcnt(2)\n" \
  "v_readfirstlane_b32 s86, v11\n" \
  "s_lshr_b32 s86, s86, 23\n" \
  "s_add_i32 s86, s86, s83\n" \
  "s_ashr_i32 s86, s86, 1\n" \
  "s_max_i32 s86, s86, s82\n" \
  "s_min_i32 s86, s86, 100\n" \
  "s_lshl_b32 s86, s86, 23\n" \
  "s_sub_i32 s84, 0x4a800000, s86\n" \
  "s_cmp_eq_u32 s73, 5\n" \
  "s_waitcnt lgkmcnt(0)\n" \
  "s_barrier\n" \
  "ds_read_b128 v[60:63], v6 offset:0\n" \
  "v_smfmac_f32_16x16x64_bf16 v[16:19], v[24:27], v[128:135], v0\n" \
  "v_smfmac_f32_16x16x64_bf16 v[20:23], v[24:27], v[192:199], v0\n" \
  "v_smfmac_f32_16x16x64_bf16 v[16:19], v[28:31], v[136:143], v0\n" \
  "v_smfmac_f32_16x16x64_bf16 v[20:23], v[28:31], v[200:207], v0\n" \
  "v_smfmac_f32_16x16x64_bf16 v[16:19], v[32:35], v[144:151], v0\n" \
  "s_waitcnt lgkmcnt(0)\n" \
  "v_max_u32 v60, 1, v60\n" \
  "v_smfmac_f32_16x16x64_bf16 v[20:23], v[32:35], v[208:215], v0\n" \
  "v_max_u32 v61, 1, v61\n" \
  "v_smfmac_f32_16x16x64_bf16 v[16:19], v[36:39], v[152:159], v0\n" \
  "v_max_u32 v62, 1, v62\n" \
  "v_smfmac_f32_16x16x64_bf16 v[20:23], v[36:39], v[216:223], v0\n" \
  "v_max_u32 v63, 1, v63\n" \
  "v_smfmac_f32_16x16x64_bf16 v[16:19], v[40:43], v[160:167], v0\n" \
  "buffer_store_dwordx4 v[60:63], v7, s[48:51], s69 offen sc1\n" \
  "v_smfmac_f32_16x16x64_bf16 v[20:23], v[40:43], v[224:231], v0\n" \
  "v_smfmac_f32_16x16x64_bf16 v[16:19], v[44:47], v[168:175], v0\n" \
  "s_cbranch_scc0 .LCV_NMK_%=\n" \
  "s_mov_b32 m0, s78\n" \
  "s_nop 0\n" \
  "buffer_load_dword v2, s[0:3], s66 offen sc1 lds\n" \
  ".LCV_NMK_%=:\n" \
  "s_cmp_eq_u32 s71, 0\n" \
  "v_smfmac_f32_16x16x64_bf16 v[20:23], v[44:47], v[232:239], v0\n" \
  "v_smfmac_f32_16x16x64_bf16 v[16:19], v[48:51], v[176:183], v0\n" \
  "v_smfmac_f32_16x16x64_bf16 v[20:23], v[48:51], v[240:247], v0\n" \
  "v_smfmac_f32_16x16x64_bf16 v[16:19], v[52:55], v[184:191], v0\n" \
  "v_smfmac_f32_16x16x64_bf16 v[20:23], v[52:55], v[248:255], v0\n" \
  "ds_read_b128 v[24:27], v3 offset:0\n" \
  "ds_read_b128 v[28:31], v3 offset:1024\n" \
  "ds_read_b128 v[32:35], v3 offset:2048\n" \
  "ds_read_b128 v[36:39], v3 offset:3072\n" \
  "ds_read_b128 v[40:43], v3 offset:8192\n" \
  "ds_read_b128 v[44:47], v3 offset:9216\n" \
  "s_nop 0\n" \
  "v_permlane32_swap_b32 v16, v18\n" \
  "v_permlane32_swap_b32 v17, v19\n" \
  "v_permlane32_swap_b32 v20, v22\n" \
  "v_permlane32_swap_b32 v21, v23\n" \
  "v_pk_add_f32 v[16:17], v[16:17], v[18:19]\n" \
  "v_pk_add_f32 v[20:21], v[20:21], v[22:23]\n" \
  "v_cvt_pk_bf16_f32 v16, v16, v17\n" \
  "v_cvt_pk_bf16_f32 v17, v20, v21\n" \
  "ds_write_b64 v5, v[16:17]\n" \
  "v_max_u32 v20, 1, v16\n" \
  "v_max_u32 v21, 1, v17\n" \
  "s_nop 1\n" \
  "v_permlane32_swap_b32 v20, v21\n" \
  "buffer_store_dwordx2 v[20:21], v8, s[52:55], s70 offen sc1\n" \
  "s_cbranch_scc1 .LCV_LAST_%=\n" \
  "s_waitcnt vmcnt(3)\n" \
  "v_cmp_eq_u32 vcc, 0, v58\n" \
  "s_and_b32 vcc_lo, vcc_lo, 15\n" \
  "s_cselect_b32 s90, 0, 1\n" \
  "s_waitcnt lgkmcnt(0)\n" \
  "s_barrier\n" \
  "s_cbranch_scc1 .LCV_NOTOK_%=\n" \
  "s_mov_b32 m0, s99\n" \
  "s_add_u32 s88, s99, 8192\n" \
  "global_load_lds_dwordx4 v10, s[62:63] sc1\n" \
  "global_load_lds_dwordx4 v10, s[62:63] offset:1024 sc1\n" \
  "s_mov_b32 m0, s88\n" \
  "s_nop 0\n" \
  "global_load_lds_dwordx4 v10, s[64:65] sc1\n" \
  "global_load_lds_dwordx4 v10, s[64:65] offset:1024 sc1\n" \
  ".LCV_NOTOK_%=:\n" \
  "s_add_u32 s67, s67, 1024\n" \
  "ds_write_b32 v59, v12\n" \
  "ds_read_b128 v[48:51], v3 offset:10240\n" \
  "ds_read_b128 v[52:55], v3 offset:11264\n" \
  "v_mov_b64 v[18:19], 0\n" \
  "v_mov_b64 v[20:21], 0\n" \
  "v_mov_b64 v[22:23], 0\n" \
  "v_mov_b64 v[16:17], 0\n" \
  "ds_read_b128 v[56:59], v3 offset:4096\n" \
  "ds_read_b128 v[60:63], v3 offset:5120\n" \
  "s_cmp_eq_u32 s90, 1\n" \
  "s_cbranch_scc1 .LCV_NM0_%=\n" \
  "s_mov_b32 m0, s94\n" \
  "s_mov_b64 exec, 15\n" \
  "buffer_load_dword v2, s[44:47], s68 offen sc1 lds\n" \
  "s_mov_b64 exec, -1\n" \
  ".LCV_NM0_%=:\n" \
  "s_setprio 2\n" \
  "s_waitcnt lgkmcnt(0)\n" \
  "s_barrier\n" \
  "ds_read_b128 v[4:7], v1 offset:8192\n" \
  "ds_read_b128 v[8:11], v1 offset:9216\n" \
  "s_waitcnt lgkmcnt(1)\n" \
  "v_smfmac_f32_16x16x64_bf16 v[12:15], v[4:7], v[24:31], v0\n" \
  "ds_read_b128 v[24:27], v3 offset:6144\n" \
  "ds_read_b128 v[28:31], v3 offset:7168\n" \
  "v_smfmac_f32_16x16x64_bf16 v[16:19], v[4:7], v[32:39], v0\n" \
  "ds_read_b128 v[32:35], v3 offset:12288\n" \
  "ds_read_b128 v[36:39], v3 offset:13312\n" \
  "s_waitcnt lgkmcnt(4)\n" \
  "v_smfmac_f32_16x16x64_bf16 v[12:15], v[8:11], v[40:47], v0\n" \
  "v_smfmac_f32_16x16x64_bf16 v[16:19], v[8:11], v[48:55], v0\n" \
  "v_smfmac_f32_16x16x64_bf16 v[20:23], v[4:7], v[56:63], v0\n" \
  "ds_read_b128 v[56:59], v3 offset:14336\n" \
  "ds_read_b128 v[60:63], v3 offset:15360\n" \
  "v_mov_b64 v[40:41], 0\n" \
  "v_mov_b64 v[42:43], 0\n" \
  "s_nop 2\n" \
  "v_permlane32_swap_b32 v12, v17\n" \
  "v_permlane32_swap_b32 v13, v16\n" \
  "v_permlane32_swap_b32 v14, v19\n" \
  "v_permlane32_swap_b32 v15, v18\n" \
  "v_pk_add_f32 v[12:13], v[12:13], v[16:17] op_sel:[0,1] op_sel_hi:[1,0]\n" \
  "v_pk_add_f32 v[14:15], v[14:15], v[18:19] op_sel:[0,1] op_sel_hi:[1,0]\n" \
  "v_pk_fma_f32 v[12:13], v[12:13], s[84:85], s[84:85] op_sel:[0,0,1] op_sel_hi:[1,0,1]\n" \
  "v_pk_fma_f32 v[14:15], v[14:15], s[84:85], s[84:85] op_sel:[0,0,1] op_sel_hi:[1,0,1]\n" \
  "v_lshl_add_u32 v12, v12, 6, s91\n" \
  "v_lshl_add_u32 v13, v13, 2, s75\n" \
  "global_atomic_add_x2 v2, v[12:13], s[58:59] offset:0\n" \
  "v_lshl_add_u32 v14, v14, 6, s91\n" \
  "v_lshl_add_u32 v15, v15, 2, s75\n" \
  "global_atomic_add_x2 v2, v[14:15], s[58:59] offset:2048\n" \
  "s_waitcnt lgkmcnt(2)\n" \
  "v_smfmac_f32_16x16x64_bf16 v[20:23], v[8:11], v[32:39], v0\n" \
  "v_smfmac_f32_16x16x64_bf16 v[40:43], v[4:7], v[24:31], v0\n" \
  "s_waitcnt lgkmcnt(0)\n" \
  "v_smfmac_f32_16x16x64_bf16 v[40:43], v[8:11], v[56:63], v0\n" \
  "s_nop 7\n" \
  "v_permlane32_swap_b32 v20, v41\n" \
  "v_permlane32_swap_b32 v21, v40\n" \
  "v_permlane32_swap_b32 v22, v43\n" \
  "v_permlane32_swap_b32 v23, v42\n" \
  "v_pk_add_f32 v[20:21], v[20:21], v[40:41] op_sel:[0,1] op_sel_hi:[1,0]\n" \
  "v_pk_add_f32 v[22:23], v[22:23], v[42:43] op_sel:[0,1] op_sel_hi:[1,0]\n" \
  "v_pk_fma_f32 v[20:21], v[20:21], s[84:85], s[84:85] op_sel:[0,0,1] op_sel_hi:[1,0,1]\n" \
  "v_pk_fma_f32 v[22:23], v[22:23], s[84:85], s[84:85] op_sel:[0,0,1] op_sel_hi:[1,0,1]\n" \
  "v_lshl_add_u32 v20, v20, 6, s91\n" \
  "v_lshl_add_u32 v21, v21, 2, s75\n" \
  "global_atomic_add_x2 v2, v[20:21], s[58:59] offset:3072\n" \
  "v_lshl_add_u32 v22, v22, 6, s91\n" \
  "v_lshl_add_u32 v23, v23, 2, s75\n" \
  "global_atomic_add_x2 v2, v[22:23], s[58:59] offset:1024\n" \
  "s_setprio 0\n" \
  "s_memrealtime s[38:39]\n" \
  "v_mbcnt_lo_u32_b32 v48, -1, 0\n" \
  "v_mbcnt_hi_u32_b32 v48, -1, v48\n" \
  "s_add_u32 s88, s74, 157760\n" \
  "v_mov_b32 v59, s88\n" \
  "v_lshl_add_u32 v52, v48, 2, v59\n" \
  "ds_read_b32 v52, v52 offset:192\n" \
  "v_lshrrev_b32 v49, 3, v48\n" \
  "v_and_b32 v50, 7, v48\n" \
  "v_lshlrev_b32 v50, 4, v50\n" \
  "v_lshl_or_b32 v44, v49, 8, v50\n" \
  "s_mov_b32 s87, 0x40000\n" \
  "v_lshlrev_b32 v10, 4, v48\n" \
  "s_cmp_eq_u32 s90, 1\n" \
  "s_cbranch_scc1 .LCV_HAVE_%=\n" \
  "v_lshl_add_u32 v58, v48, 2, s94\n" \
  "s_waitcnt vmcnt(4)\n" \
  "ds_read_b32 v58, v58\n" \
  "s_waitcnt lgkmcnt(0)\n" \
  "v_cmp_eq_u32 vcc, 0, v58\n" \
  "s_and_b32 vcc_lo, vcc_lo, 15\n" \
  "s_cbranch_scc0 .LCV_GOT_%=\n" \
  "s_branch .LCV_ASK_%=\n" \
  ".LCV_LATE_%=:\n" \
  "v_cmp_eq_u32 vcc, 0, v58\n" \
  "s_and_b32 vcc_lo, vcc_lo, 15\n" \
  "s_cbranch_scc0 .LCV_GOT_%=\n" \
  "s_sleep 12\n" \
  ".LCV_ASK_%=:\n" \
  "s_mov_b64 exec, 15\n" \
  "buffer_load_dword v58, v2, s[44:47], s68 offen sc1\n" \
  "s_mov_b64 exec, -1\n" \
  "s_sub_u32 s87, s87, 1\n" \
  "s_cbranch_scc1 .LCV_END_%=\n" \
  "s_waitcnt vmcnt(0)\n" \
  "s_branch .LCV_LATE_%=\n" \
  ".LCV_GOT_%=:\n" \
  "s_mov_b32 m0, s99\n" \
  "s_add_u32 s88, s99, 8192\n" \
  "global_load_lds_dwordx4 v10, s[62:63] sc1\n" \
  "global_load_lds_dwordx4 v10, s[62:63] offset:1024 sc1\n" \
  "s_mov_b32 m0, s88\n" \
  "s_nop 0\n" \
  "global_load_lds_dwordx4 v10, s[64:65] sc1\n" \
  "global_load_lds_dwordx4 v10, s[64:65] offset:1024 sc1\n" \
  ".LCV_HAVE_%=:\n" \
  "s_waitcnt lgkmcnt(0)\n" \
  "s_sub_u32 s61, s38, s61\n" \
  "s_add_u32 s79, s79, 2\n" \
  "s_min_u32 s79, s79, s61\n" \
  "s_sub_u32 s39, s38, 150\n" \
  "v_subrev_u32 v52, s39, v52\n" \
  "v_cmp_lt_u32 vcc, 0x1000, v52\n" \
  "s_cmp_eq_u64 vcc, 0\n" \
  "s_cbranch_scc1 .LCV_OKM_%=\n" \
  ".LCV_AGN_%=:\n" \
  "buffer_load_dword v52, v2, s[0:3], s66 offen sc1\n" \
  "s_sub_u32 s87, s87, 1\n" \
  "s_cbranch_scc1 .LCV_END_%=\n" \
  "s_waitcnt vmcnt(0)\n" \
  "v_cmp_eq_u32 vcc, 0, v52\n" \
  "v_subrev_u32 v52, s39, v52\n" \
  "s_cmp_eq_u64 vcc, 0\n" \
  "s_cbranch_scc1 .LCV_OKM_%=\n" \
  "s_sleep 4\n" \
  "s_branch .LCV_AGN_%=\n" \
  ".LCV_OKM_%=:\n" \
  "s_nop 1\n" \
  "v_max_i32_dpp v52, v52, v52 row_ror:1 row_mask:0xf bank_mask:0xf\n" \
  "s_nop 1\n" \
  "v_max_i32_dpp v52, v52, v52 row_ror:2 row_mask:0xf bank_mask:0xf\n" \
  "s_nop 1\n" \
  "v_max_i32_dpp v52, v52, v52 row_ror:4 row_mask:0xf bank_mask:0xf\n" \
  "s_movk_i32 vcc_lo, 150\n" \
  "v_readlane_b32 s88, v52, 0\n" \
  "s_max_i32 vcc_lo, vcc_lo, s88\n" \
  "v_readlane_b32 s88, v52, 16\n" \
  "s_max_i32 vcc_lo, vcc_lo, s88\n" \
  "v_readlane_b32 s88, v52, 32\n" \
  "s_max_i32 vcc_lo, vcc_lo, s88\n" \
  "v_readlane_b32 s88, v52, 48\n" \
  "s_max_i32 vcc_lo, vcc_lo, s88\n" \
  "s_add_u32 s66, s66, 1024\n" \
  "s_add_u32 vcc_lo, vcc_lo, s39\n" \
  "s_add_u32 vcc_lo, vcc_lo, 30\n" \
  ".LCV_TW_%=:\n" \
  "s_memrealtime s[38:39]\n" \
  "s_waitcnt lgkmcnt(0)\n" \
  "s_sub_u32 s38, s38, vcc_lo\n" \
  "s_cmp_lt_i32 s38, 0\n" \
  "s_cbranch_scc1 .LCV_TW_%=\n" \
  "buffer_load_dwordx4 v[24:27], v44, s[40:43], s60 offen sc1\n" \
  "buffer_load_dwordx4 v[28:31], v44, s[40:43], s60 offen offset:2048 sc1\n" \
  "s_add_u32 s62, s62, 0x80000\n" \
  "s_addc_u32 s63, s63, 0\n" \
  "s_add_u32 s64, s64, 0x40000\n" \
  "s_addc_u32 s65, s65, 0\n" \
  "s_add_u32 s68, s68, 0x1000\n" \
  "s_add_u32 s69, s69, 0x10000\n" \
  "s_add_u32 s70, s70, 0x10000\n" \
  "s_sub_u32 s88, s73, 4\n" \
  "v_and_b32 v51, 15, v48\n" \
  "v_lshl_add_u32 v4, v51, 1, s97\n" \
  "v_bfe_u32 v49, v48, 4, 1\n" \
  "v_mad_u32_u24 v4, v49, s92, v4\n" \
  "v_lshrrev_b32 v50, 5, v48\n" \
  "v_mad_u32_u24 v4, v50, s93, v4\n" \
  "v_lshlrev_b32 v8, 4, v49\n" \
  "v_lshl_add_u32 v8, v51, 5, v8\n" \
  "v_lshl_add_u32 v8, v50, 3, v8\n" \
  "s_lshl_b32 s89, s88, 8\n" \
  "s_add_u32 s89, s89, s74\n" \
  "s_add_u32 s89, s89, 139264\n" \
  "v_and_b32 v49, 31, v48\n" \
  "v_lshl_add_u32 v5, v49, 3, s89\n" \
  "v_lshl_add_u32 v5, v50, 10, v5\n" \
  "v_lshrrev_b32 v49, 2, v51\n" \
  "v_and_b32 v50, 5, v51\n" \
  "v_and_or_b32 v49, v49, 2, v50\n" \
  "s_lshl_b32 s89, s88, 6\n" \
  "s_add_u32 s89, s89, s74\n" \
  "s_add_u32 s89, s89, 160992\n" \
  "v_and_b32 v50, 48, v48\n" \
  "v_add_u32 v6, s89, v50\n" \
  "v_mad_u32_u24 v6, v49, s96, v6\n" \
  "v_lshlrev_b32 v7, 3, v50\n" \
  "v_lshl_add_u32 v7, v49, 4, v7\n" \
  "v_and_b32 v49, 2, v51\n" \
  "v_lshl_add_u32 v7, v49, 29, v7\n" \
  "v_mov_b64 v[12:13], 0\n" \
  "v_mov_b64 v[14:15], 0\n" \
  "v_mov_b64 v[16:17], 0\n" \
  "v_mov_b64 v[18:19], 0\n" \
  "v_mov_b64 v[20:21], 0\n" \
  "v_mov_b64 v[22:23], 0\n" \
  "v_and_b32 v50, 3, v48\n" \
  "v_lshl_add_u32 v40, v50, 2, s98\n" \
  "v_lshrrev_b32 v50, 4, v48\n" \
  "v_mad_u32_u24 v40, v50, s95, v40\n" \
  "v_bfe_u32 v49, v48, 3, 1\n" \
  "v_lshl_add_u32 v40, v49, 6, v40\n" \
  "v_bfe_u32 v49, v48, 2, 1\n" \
  "v_lshl_add_u32 v40, v49, 7, v40\n" \
  "v_xor_b32 v41, 16, v40\n" \
  "v_xor_b32 v42, 32, v40\n" \
  "v_xor_b32 v43, 48, v40\n" \
  "s_add_i32 s80, s86, 0x32800000\n" \
  "s_add_i32 s81, s86, 0x33800000\n" \
  "s_add_i32 s88, s86, 0xb4000000\n" \
  "v_mov_b32 v46, s88\n" \
  "v_mov_b32 v47, 0\n" \
  "v_mov_b32 v11, 0\n" \
  "s_mov_b32 s89, s60\n" \
  "s_xor_b32 s60, s60, 0x80\n" \
  "s_bitcmp0_b32 s60, 7\n" \
  "s_cselect_b32 s88, 0x8000, 0\n" \
  "s_add_u32 s60, s60, s88\n" \
  "s_and_b32 s60, s60, 0xffff\n" \
  "s_add_u32 s58, s56, s60\n" \
  "s_addc_u32 s59, s57, 0\n" \
  "s_sub_u32 s71, s71, 1\n" \
  ".LCV_SEE_%=:\n" \
  "s_waitcnt vmcnt(1)\n" \
  "v_bitop3_b32 v45, v24, v26, 32 bitop3:0x80\n" \
  "v_alignbit_b32 v32, v25, v24, 2\n" \
  "v_alignbit_b32 v34, v27, v26, 2\n" \
  "s_waitcnt vmcnt(0)\n" \
  "v_bitop3_b32 v45, v45, v28, v30 bitop3:0x80\n" \
  "v_cmp_eq_u32 vcc, 0, v45\n" \
  "v_cvt_f32_i32 v32, v32\n" \
  "v_cvt_f32_i32 v33, v25\n" \
  "s_cmp_eq_u64 vcc, 0\n" \
  "s_cbranch_scc0 .LCV_POLL_%=\n" \
  "v_cvt_f32_i32 v34, v34\n" \
  "v_cvt_f32_i32 v35, v27\n" \
  "v_pk_fma_f32 v[32:33], v[32:33], s[80:81], v[46:47]\n" \
  "v_alignbit_b32 v36, v29, v28, 2\n" \
  "v_pk_fma_f32 v[34:35], v[34:35], s[80:81], v[46:47]\n" \
  "v_alignbit_b32 v38, v31, v30, 2\n" \
  "v_cvt_pk_bf16_f32 v32, v32, v34\n" \
  "v_cvt_pk_bf16_f32 v33, v33, v35\n" \
  "v_cvt_f32_i32 v36, v36\n" \
  "ds_write_b32 v40, v32\n" \
  "v_cvt_f32_i32 v37, v29\n" \
  "ds_write_b32 v41, v33\n" \
  "v_cvt_f32_i32 v38, v38\n" \
  "v_cvt_f32_i32 v39, v31\n" \
  "v_pk_fma_f32 v[36:37], v[36:37], s[80:81], v[46:47]\n" \
  "v_pk_fma_f32 v[38:39], v[38:39], s[80:81], v[46:47]\n" \
  "v_dot2c_f32_bf16 v11, v32, v32\n" \
  "v_cvt_pk_bf16_f32 v36, v36, v38\n" \
  "v_cvt_pk_bf16_f32 v37, v37, v39\n" \
  "ds_write_b32 v42, v36\n" \
  "ds_write_b32 v43, v37\n" \
  "v_dot2c_f32_bf16 v11, v33, v33\n" \
  "v_dot2c_f32_bf16 v11, v36, v36\n" \
  "v_dot2c_f32_bf16 v11, v37, v37\n" \
  "s_nop 1\n" \
  "s_nop 0\n" \
  "v_cmpx_lt_f32 vcc, 2.0, v11\n" \
  "ds_max_u32 v59, v11\n" \
  "s_mov_b64 exec, -1\n" \
  "s_branch .LCV_TOP_%=\n" \
  ".LCV_POLL_%=:\n" \
  "buffer_load_dwordx4 v[24:27], v44, s[40:43], s89 offen sc1\n" \
  "buffer_load_dwordx4 v[28:31], v44, s[40:43], s89 offen offset:2048 sc1\n" \
  "s_sub_u32 s87, s87, 1\n" \
  "s_cbranch_scc1 .LCV_END_%=\n" \
  "s_branch .LCV_SEE_%=\n" \
  ".LCV_LAST_%=:\n" \
  "s_waitcnt vmcnt(0)\n" \
  ".LCV_END_%=:\n" \
  "s_endpgm\n"
__device__ __forceinline__ v4u mk_srd(const void* p, const unsigned bytes) {
  const unsigned long long a = (unsigned long long)p;
  return v4u{(unsigned)a, (unsigned)(a >> 32) & 0xffffu, bytes, 0x00020000u};
}
template <int R>
__device__ __forceinline__ void pin8(v8u& x) {
#define PIN_(a, b) if constexpr (R == a) asm volatile("" : "+{v[" #a ":" #b "]}"(x));
  PIN_(64, 71)  PIN_(72, 79)  PIN_(80, 87)  PIN_(88, 95)  PIN_(96, 103)  PIN_(104, 111)  PIN_(112, 119)  PIN_(120, 127)
  PIN_(128, 135)  PIN_(136, 143)  PIN_(144, 151)  PIN_(152, 159)  PIN_(160, 167)  PIN_(168, 175)  PIN_(176, 183)  PIN_(184, 191)
  PIN_(192, 199)  PIN_(200, 207)  PIN_(208, 215)  PIN_(216, 223)  PIN_(224, 231)  PIN_(232, 239)  PIN_(240, 247)  PIN_(248, 255)
#undef PIN_
}
template <int R>
__device__ __forceinline__ void pin4(v4u& x) {
#define PIN_(a, b) if constexpr (R == a) asm volatile("" : "+{v[" #a ":" #b "]}"(x));
  PIN_(32, 35)  PIN_(36, 39)  PIN_(40, 43)  PIN_(44, 47)  PIN_(48, 51)  PIN_(52, 55)  PIN_(56, 59)  PIN_(60, 63)
  PIN_(96, 99)  PIN_(100, 103)  PIN_(104, 107)  PIN_(108, 111)  PIN_(112, 115)  PIN_(116, 119)  PIN_(120, 123)  PIN_(124, 127)
  PIN_(160, 163)  PIN_(164, 167)  PIN_(168, 171)  PIN_(172, 175)  PIN_(176, 179)  PIN_(180, 183)  PIN_(184, 187)  PIN_(188, 191)
#undef PIN_
}
template <bool ATT>
__device__ __forceinline__ void role_chain_t(const MegaArgs& A, char* smem, const int L, const int h, const int rep) {
  u16* swo = reinterpret_cast<u16*>(smem);
  const int w = A.wv;
  char* ws = A.ws;
  unsigned* FL = (unsigned*)(ws + OFF_FL);
  unsigned* TMO = FL + FL_TMO;
#define LANE_IDS()                         \
  const int l = lane_id();                 \
  const int c = l & 15, g = l >> 4;        \
  (void)c; (void)g;
  float* sgf = reinterpret_cast<float*>(smem + C_X);
  unsigned* FQ = FL + FL_FQ + L * 64 * 32 * 32;
  const int wb = (w + ROT_H) & 7;
  v8u w0[8], w1[8], w2[8];
  u64 tag;
  {
    LANE_IDS();
    const unsigned sb = lds_base(smem);
    const int q = (l >> 2) & 3, p = l & 3;
    const int f = (q & 1) | ((g & 1) << 1) | ((q >> 1) << 2);
    const unsigned ra = sb + (16 * (g >> 1) + 4 * (g & 1) + (q & 1) + 8 * (q >> 1)) * 256 + (w ^ f) * 32 + p * 8;
#define W_READ(N, hf, X)                                                       \
  {                                                                           \
    _Pragma("unroll") for (int t = 0; t < 4; ++t) {                           \
      const unsigned a_ = (ra ^ (X)) + (hf) * 8192 + t * 32768;               \
      const v2u r0 = trd(a_), r1 = trd(a_ + 512), r2 = trd(a_ + 16384), r3 = trd(a_ + 16384 + 512); \
      w##N[4 * (hf) + t] = v8u{r0.x, r0.y, r1.x, r1.y, r2.x, r2.y, r3.x, r3.y}; \
    }                                                                         \
    pin8<64 * N + 64 + 32 * hf>(w##N[4 * hf]);                                \
    pin8<64 * N + 72 + 32 * hf>(w##N[4 * hf + 1]);                            \
    pin8<64 * N + 80 + 32 * hf>(w##N[4 * hf + 2]);                            \
    pin8<64 * N + 88 + 32 * hf>(w##N[4 * hf + 3]);                            \
  }
#define W_STAGE(M, hf, WAIT, NEXT)                                             \
  {                                                                           \
    WAIT;                                                                     \
    RAW_BAR();                                                                \
    if ((M) == 0 && ATT) { W_READ(0, hf, 0); W_READ(2, hf, 128); }            \
    if ((M) == 1 && ATT) { W_READ(1, hf, 0); }                                \
    if ((M) == 1 && !ATT) { W_READ(0, hf, 0); }                               \
    if ((M) == 2 && !ATT) { W_READ(1, hf, 128); W_READ(2, hf, 0); }           \
    if ((M) == 3) {                                                           \
      _Pragma("unroll") for (int i = 0; i < 8; ++i) {                         \
        const unsigned a_ = rb + (hf) * 8192 + (i & 1) * 65536 + ((4 * wb + (i >> 1)) ^ f) * 32; \
        const v2u r0 = trd(a_), r1 = trd(a_ + 2048);                          \
        fr[hf][i] = v4u{r0.x, r0.y, r1.x, r1.y};                              \
      }                                                                       \
      constexpr int R_ = hf ? 32 : ATT ? 160 : 96;                            \
      pin4<R_>(fr[hf][0]); pin4<R_ + 4>(fr[hf][1]); pin4<R_ + 8>(fr[hf][2]); pin4<R_ + 12>(fr[hf][3]); \
      pin4<R_ + 16>(fr[hf][4]); pin4<R_ + 20>(fr[hf][5]); pin4<R_ + 24>(fr[hf][6]); pin4<R_ + 28>(fr[hf][7]); \
    }                                                                         \
    RAW_BAR();                                                                \
    NEXT;                                                                     \
  }
#define WO_ISSUE(hf)                                                          \
  {                                                                           \
    const char* base_ = (const char*)A.Wo + (size_t)(h * DH + 64 * (hf) + 8 * w) * (DM * 2); \
    _Pragma("unroll") for (int i = 0; i < 8; ++i) {                           \
      const int fd = (i & 1) | (((i >> 2) & 1) << 1) | ((w & 1) << 2);        \
      glds16(base_, (unsigned)(i * 1024 + ((l ^ (fd << 1)) << 4)), sb + w * 16384 + (hf) * 8192 + i * 1024); \
    }                                                                         \
  }
    const unsigned rb = sb + (2 * (g >> 1) + (q >> 1)) * 16384 + (4 * (g & 1) + (q & 1)) * 1024 + p * 8;
    v4u fr[2][8];
#define WO_PUT(hf) \
  _Pragma("unroll") for (int i = 0; i < 8; ++i) *reinterpret_cast<v4u*>(swo + w * 8192 + (hf) * 4096 + i * 512 + l * 8) = fr[hf][i];
    __builtin_amdgcn_s_waitcnt(0x0f70);
    glds16(A.mem0, (unsigned)((c & 7) * DM + 64 * w + 16 * g + 8 * (c >> 3)) * 2, sb + C_SM + w * 1024);
    wqkv_issue<true>(A.Wv, 0, h, w, l, sb);
    wqkv_issue<true>(A.Wv, 1, h, w, l, sb);
    tag = call_tag(A);
    W_STAGE(2, 0, WAIT_VM(8), WO_ISSUE(0));
    W_STAGE(2, 1, WAIT_VM(8), WO_ISSUE(1));
    SST(4);
    if (!ATT) {
      float f2 = 0.f;
      asm volatile("" : "+v"(f2));
#pragma unroll
      for (int t = 0; t < 8; ++t)
#pragma unroll
        for (int e = 0; e < 8; ++e) f2 = dot2sq(w2[t][e], dot2sq(w1[t][e], f2));
      f2 = wave_sum_dpp(f2);
      if (l == 0) { sgf[w] = f2; sgf[16] = 0.f; }
    }
    W_STAGE(3, 0, WAIT_VM(8), wqkv_issue<true>(A.Wq, 0, h, w, l, sb));
    W_STAGE(3, 1, WAIT_VM(8), wqkv_issue<true>(A.Wq, 1, h, w, l, sb));
    SST(5);
    {
      float cn[4] = {0.f, 0.f, 0.f, 0.f};
      asm volatile("" : "+v"(cn[0]), "+v"(cn[1]), "+v"(cn[2]), "+v"(cn[3]));
#pragma unroll
      for (int e = 0; e < 64; ++e) cn[e & 3] = dot2sq(fr[e >> 5][2 * (e & 3) + ((e >> 4) & 1)][(e >> 2) & 3], cn[e & 3]);
      const float m2 = wave_max_dpp(swap_add32(swap_add16(cn[0], cn[1]), swap_add16(cn[2], cn[3])));
      if (l == 0) sgf[8 + w] = m2;
    }
#define G_VALUE()                                                                                                   \
  if (w == 4 && L == 0) {                                                                                           \
    const f32x4 a = *reinterpret_cast<const f32x4*>(sgf + 4), b = *reinterpret_cast<const f32x4*>(sgf + 8);         \
    const f32x4 d = *reinterpret_cast<const f32x4*>(sgf + 12);                                                      \
    const float m2 = fmaxf(fmaxf(fmaxf(b.x, b.y), fmaxf(b.z, b.w)), fmaxf(fmaxf(d.x, d.y), fmaxf(d.z, d.w)));       \
    if (l < 8) __hip_atomic_store((gu64*)(ws + OFF_G) + l * 32 + h, tagged(tag, __float_as_uint(((a.x + a.y) + (a.z + a.w)) * m2 * 1.01f)), RLX_AGENT); \
  }
    W_STAGE(0, 0, WAIT_VM(8), G_VALUE(); wqkv_issue<true>(A.Wk, 0, h, w, l, sb));
    W_STAGE(0, 1, WAIT_VM(8), if (ATT) {
      wqkv_issue<true>(A.Wk, 1, h, w, l, sb);
      wqkv_issue<true>(A.Wk, 1, h, w + 4, l, sb);
    });
    SST(6);
    W_STAGE(1, 0, if (ATT) WAIT_VM(16); else { ready_wait(A, tag, ready_look(A)); __builtin_amdgcn_s_waitcnt(0x0f70); }, WO_PUT(0));
    if (w == 4) {
      u64 gv;
      for (unsigned spins = 0;; ++spins) {
        gv = __hip_atomic_load((gu64*)(ws + OFF_G) + (blockIdx.x & 7) * 32 + (l & 31), RLX_AGENT);
        if (__all(has_tag(tag, (unsigned)gv, (unsigned)(gv >> 32))) || spins > SPIN_CAP) break;
        __builtin_amdgcn_s_sleep(GSL);
      }
      const float gm = wave_max_dpp(hi16((unsigned)(gv >> 32)));
      if (l == 0) sgf[0] = gm * 1.063f;
    }
    W_STAGE(1, 1, WAIT_VM(0), WO_PUT(1));
    SST(7);
#undef G_VALUE
#undef WO_PUT
#undef W_STAGE
#undef W_READ
#undef WO_ISSUE
  }
  WSTAMP2();
  const unsigned sb = lds_base(smem);
  const char* gK = ws + OFF_QK + L * SZ_QK + ((size_t)h * 16 + 2 * w) * 1024;
  const char* gV = ws + OFF_VF + L * SZ_O + ((size_t)h * 8 + 2 * w - 8) * 1024;
  v8u pv;
  {
    LANE_IDS();
    const unsigned voff = g * 256 + c * 8, wp = sb + w * 16384 + l * 16;
    pv[0] = (c & 8) ? 0xEEEE : 0x4444;
    pv[1] = sb + C_SM + l * 16;
    pv[2] = ATT ? sb + C_SQ + ((c & 7) * QS + 8 * g) * 2 : voff;
    pv[3] = ATT ? sb + C_SK + ((4 * ((c >> 2) & 1) + 2 * (c >> 3) + (c & 1)) * QS + 8 * g) * 2 : wp;
    pv[4] = sb + (c < 8 ? C_SO : C_SQ) + w * 256 + ((c & 7) + 8 * (g >> 1)) * 16 + 8 * (g & 1);
    pv[5] = voff;
    pv[6] = wp;
    pv[7] = sb + C_SO + (l >> 5) * 1024 + w * 256 + (l & 31) * 8;
  }
  if (!ATT) {
    for (unsigned spins = 0; !__all(__hip_atomic_load((gu32*)FQ + h * 32 + 2 * (lane_id() & 3), RLX_AGENT) != 0u); ++spins) {
      if (spin_timeout(spins, TMO)) return;
      __builtin_amdgcn_s_sleep(4);
    }
    asm volatile("" ::: "memory");
    const unsigned lo = lane_id() * 16, d = sb + C_KT + (2 * w - 8) * 1024;
    glds16c(gK, lo, d);
    glds16c(gK + 1024, lo, d + 1024);
    glds16c(gV, lo, d + 8192);
    glds16c(gV + 1024, lo, d + 9216);
  }
  const unsigned gw2 = __builtin_amdgcn_readfirstlane(__float_as_uint(sgf[0]));
  DRAIN();
  SST(8);
  const v4u rsA = mk_srd(ws + OFF_ACC + L * SZ_ACC, SZ_ACC);
  const v4u rsF = mk_srd(FQ, 64 * 32 * 32 * 4);
  const v4u rsMK = mk_srd(ws + OFF_MEMK + L * SZ_MEM, SZ_MEM);
  const v4u rsMV = mk_srd(ws + OFF_MEMV + L * SZ_MEM, SZ_MEM);
  const char* pAB = ws + OFF_ACC + L * SZ_ACC + (wb - w) * 4096;
  const char* pGK = gK + NH * 16 * 1024;
  const char* pGV = gV + NH * 8 * 1024;
  const v4u rsT = mk_srd(FL + FL_FK + L * 32, 64 * 1024 - 128);
  unsigned sc[8];
  sc[0] = (32 + h) * 128;
  sc[1] = (h * 4 + w - 4) * 512;
  sc[2] = (h * 4 + w - 4) * 512;
  sc[3] = NSEG - 1;
  sc[4] = gw2;
  sc[5] = w;
  sc[6] = sb;
  sc[7] = h == 0 ? 0xd4000000u - 2u - (1u << 29) : 0xd4000000u;
  const char* dbg = ws;
#define CHAIN_OPS                                                                                                          \
  ::"{v[64:71]}"(w0[0]), "{v[72:79]}"(w0[1]), "{v[80:87]}"(w0[2]), "{v[88:95]}"(w0[3]), "{v[96:103]}"(w0[4]),              \
      "{v[104:111]}"(w0[5]), "{v[112:119]}"(w0[6]), "{v[120:127]}"(w0[7]), "{v[128:135]}"(w1[0]), "{v[136:143]}"(w1[1]),    \
      "{v[144:151]}"(w1[2]), "{v[152:159]}"(w1[3]), "{v[160:167]}"(w1[4]), "{v[168:175]}"(w1[5]), "{v[176:183]}"(w1[6]),    \
      "{v[184:191]}"(w1[7]), "{v[192:199]}"(w2[0]), "{v[200:207]}"(w2[1]), "{v[208:215]}"(w2[2]), "{v[216:223]}"(w2[3]),    \
      "{v[224:231]}"(w2[4]), "{v[232:239]}"(w2[5]), "{v[240:247]}"(w2[6]), "{v[248:255]}"(w2[7]), "{v[0:7]}"(pv),          \
      "{s[40:43]}"(rsA), "{s[44:47]}"(rsF), "{s[48:51]}"(rsMK), "{s[52:55]}"(rsMV), "{s[56:57]}"(pAB), "{s60}"(w * 4096), "{s61}"(ATT ? h * 1024 : L), \
      "{s[62:63]}"(pGK), "{s[64:65]}"(pGV), "{s66}"(0), "{s67}"(w == 4 ? (h >> 3) * 256 + (h & 7) * 8 : 0x40000000), "{s68}"(sc[0]), "{s69}"(sc[1]), "{s70}"(sc[2]),    \
      "{s71}"(sc[3]), "{s72}"(sc[4]), "{s73}"(sc[5]), "{s74}"(sc[6]), "{s75}"(sc[7]), "{s[36:37]}"(dbg),                   \
      "{s[0:3]}"(rsT)                                                                                                      \
      : "memory", "vcc", "scc", "s99", "v63"
  if (ATT) asm volatile("s_mov_b64 exec, -1\ns_nop 4\n" CHAIN_ASM_ATT CHAIN_OPS);
  else asm volatile("s_mov_b64 exec, -1\ns_nop 4\n" CHAIN_ASM_VO CHAIN_OPS);
#undef CHAIN_OPS
#undef LANE_IDS
}

__device__ __forceinline__ void role_chain(const MegaArgs& A, char* smem, const int L, const int h, const int rep) {
  if (A.wv < 4) role_chain_t<true>(A, smem, L, h, rep);
  else role_chain_t<false>(A, smem, L, h, rep);
}

constexpr int AW_SO = 0, AW_TK = 32768, AW_CNT = AW_TK + 98304;
__device__ __forceinline__ unsigned umin3(unsigned a, unsigned b, unsigned c) {
  return __builtin_elementwise_min(__builtin_elementwise_min(a, b), c);
}
#define AW_ASM_X \
  "s_mov_b32 s61, 0\n" \
  "s_mov_b32 s67, 1\n" \
  "s_mov_b32 s68, 0\n" \
  "s_mov_b32 s70, 0\n" \
  "s_mov_b32 s73, 0\n" \
  "s_mov_b32 s71, 0x3e0293ee\n" \
  "s_mov_b32 s72, 0xbe0293ee\n" \
  "s_mov_b32 s65, 49152\n" \
  "s_mov_b32 s66, 16384\n" \
  "v_mov_b32 v38, s58\n" \
  "s_memrealtime s[38:39]\n" \
  "s_waitcnt lgkmcnt(0)\n" \
  "s_mov_b32 s69, s38\n" \
  "s_setprio 3\n" \
  ".LCX_TOP_%=:\n" \
  "s_mov_b32 s62, 0x200000\n" \
  "s_cmp_ge_u32 s73, s67\n" \
  "s_cbranch_scc1 .LCX_TOK_%=\n" \
  "s_cmp_eq_u32 s61, 0\n" \
  "s_cbranch_scc1 .LCX_SPIN_%=\n" \
  "s_waitcnt vmcnt(0)\n" \
  "v_mov_b32 v46, s59\n" \
  "v_mov_b32 v47, 1\n" \
  "s_mov_b64 exec, 1\n" \
  "ds_add_rtn_u32 v46, v46, v47\n" \
  "s_mov_b64 exec, -1\n" \
  "s_xor_b32 s59, s59, 4\n" \
  "s_mov_b32 s61, 0\n" \
  "s_waitcnt lgkmcnt(0)\n" \
  "v_readfirstlane_b32 s63, v46\n" \
  "v_lshlrev_b32 v46, 5, v5\n" \
  "s_and_b32 s63, s63, 7\n" \
  "s_cmp_eq_u32 s63, 7\n" \
  "s_cbranch_scc0 .LCX_W1_%=\n" \
  "s_mov_b64 exec, 15\n" \
  "global_store_dword v46, v47, s[52:53] sc1\n" \
  "s_mov_b64 exec, -1\n" \
  ".LCX_W1_%=:\n" \
  "s_add_u32 s52, s52, 2048\n" \
  "s_addc_u32 s53, s53, 0\n" \
  ".LCX_SPIN_%=:\n" \
  "s_sleep 2\n" \
  "s_memrealtime s[38:39]\n" \
  "ds_read_b32 v37, v38\n" \
  "s_sub_u32 s62, s62, 1\n" \
  "s_cbranch_scc1 .LCX_END_%=\n" \
  "s_waitcnt lgkmcnt(0)\n" \
  "v_readfirstlane_b32 s73, v37\n" \
  "s_add_u32 s38, s38, 80\n" \
  "s_bfe_u32 s64, s73, 0x80008\n" \
  "s_and_b32 s73, s73, 0xff\n" \
  "s_min_u32 s73, s73, s64\n" \
  "s_cmp_ge_u32 s73, s67\n" \
  "s_cbranch_scc0 .LCX_SPIN_%=\n" \
  "s_cmp_eq_u32 s70, 0\n" \
  "s_cbranch_scc1 .LCX_TOK_%=\n" \
  "s_sub_u32 s63, s38, s69\n" \
  "s_max_i32 s63, s63, 0\n" \
  "s_add_u32 s69, s69, s63\n" \
  ".LCX_TOK_%=:\n" \
  "ds_read_b128 v[48:51], v6 offset:0\n" \
  "ds_read_b128 v[64:67], v0 offset:8192\n" \
  "ds_read_b128 v[80:83], v0 offset:12288\n" \
  "ds_read_b128 v[52:55], v6 offset:1024\n" \
  "ds_read_b128 v[68:71], v0 offset:9216\n" \
  "ds_read_b128 v[84:87], v0 offset:13312\n" \
  "ds_read_b128 v[56:59], v6 offset:2048\n" \
  "ds_read_b128 v[72:75], v0 offset:10240\n" \
  "ds_read_b128 v[88:91], v0 offset:14336\n" \
  "ds_read_b128 v[60:63], v6 offset:3072\n" \
  "ds_read_b128 v[76:79], v0 offset:11264\n" \
  "ds_read_b128 v[92:95], v0 offset:15360\n" \
  "s_waitcnt lgkmcnt(9)\n" \
  "v_mfma_f32_16x16x32_bf16 v[20:23], v[64:67], v[48:51], v[8:11]\n" \
  "v_mfma_f32_16x16x32_bf16 v[24:27], v[80:83], v[48:51], v[12:15]\n" \
  "ds_read_b128 v[96:99], v0 offset:16384\n" \
  "ds_read_b128 v[100:103], v0 offset:17408\n" \
  "s_waitcnt lgkmcnt(8)\n" \
  "v_mfma_f32_16x16x32_bf16 v[20:23], v[68:71], v[52:55], v[20:23]\n" \
  "v_mfma_f32_16x16x32_bf16 v[24:27], v[84:87], v[52:55], v[24:27]\n" \
  "ds_read_b128 v[104:107], v0 offset:18432\n" \
  "ds_read_b128 v[108:111], v0 offset:19456\n" \
  "s_waitcnt lgkmcnt(7)\n" \
  "v_mfma_f32_16x16x32_bf16 v[20:23], v[72:75], v[56:59], v[20:23]\n" \
  "v_mfma_f32_16x16x32_bf16 v[24:27], v[88:91], v[56:59], v[24:27]\n" \
  "ds_read_b128 v[112:115], v0 offset:20480\n" \
  "ds_read_b128 v[116:119], v0 offset:21504\n" \
  "s_waitcnt lgkmcnt(6)\n" \
  "v_mfma_f32_16x16x32_bf16 v[20:23], v[76:79], v[60:63], v[20:23]\n" \
  "v_mfma_f32_16x16x32_bf16 v[24:27], v[92:95], v[60:63], v[24:27]\n" \
  "ds_read_b128 v[120:123], v0 offset:22528\n" \
  "ds_read_b128 v[124:127], v0 offset:23552\n" \
  "s_add_u32 s67, s67, 1\n" \
  "v_add_u32 v0, s65, v0\n" \
  "v_add_u32 v6, s65, v6\n" \
  "s_sub_u32 s65, 0, s65\n" \
  "s_nop 1\n" \
  "v_max3_f32 v32, v20, v21, v22\n" \
  "v_max3_f32 v33, v23, v24, v25\n" \
  "v_max3_f32 v32, v32, v26, v27\n" \
  "v_max_f32 v32, v32, v33\n" \
  "v_mov_b32 v33, v32\n" \
  "s_nop 1\n" \
  "v_permlane16_swap_b32 v32, v33\n" \
  "v_max_f32 v32, v32, v33\n" \
  "v_mov_b32 v33, v32\n" \
  "s_nop 1\n" \
  "v_permlane32_swap_b32 v32, v33\n" \
  "v_max_f32 v32, v32, v33\n" \
  "v_mul_f32 v33, s72, v32\n" \
  "v_fma_f32 v20, v20, s71, v33\n" \
  "v_fma_f32 v21, v21, s71, v33\n" \
  "v_fma_f32 v22, v22, s71, v33\n" \
  "v_fma_f32 v23, v23, s71, v33\n" \
  "v_fma_f32 v24, v24, s71, v33\n" \
  "v_fma_f32 v25, v25, s71, v33\n" \
  "v_fma_f32 v26, v26, s71, v33\n" \
  "v_fma_f32 v27, v27, s71, v33\n" \
  "v_exp_f32 v20, v20\n" \
  "v_exp_f32 v21, v21\n" \
  "v_exp_f32 v22, v22\n" \
  "v_exp_f32 v23, v23\n" \
  "v_exp_f32 v24, v24\n" \
  "v_exp_f32 v25, v25\n" \
  "v_exp_f32 v26, v26\n" \
  "v_exp_f32 v27, v27\n" \
  "v_add_f32 v34, v20, v21\n" \
  "v_add_f32 v33, v22, v23\n" \
  "v_add_f32 v36, v24, v25\n" \
  "v_add_f32 v34, v34, v33\n" \
  "v_add_f32 v33, v26, v27\n" \
  "v_add_f32 v34, v34, v36\n" \
  "v_add_f32 v34, v34, v33\n" \
  "s_waitcnt vmcnt(0) lgkmcnt(0)\n" \
  "s_cmp_eq_u32 s61, 0\n" \
  "s_cbranch_scc1 .LCX_NP_%=\n" \
  "v_mov_b32 v46, s59\n" \
  "v_mov_b32 v47, 1\n" \
  "s_mov_b64 exec, 1\n" \
  "ds_add_rtn_u32 v46, v46, v47\n" \
  "s_mov_b64 exec, -1\n" \
  "s_xor_b32 s59, s59, 4\n" \
  "s_mov_b32 s61, 0\n" \
  "s_waitcnt lgkmcnt(0)\n" \
  "v_readfirstlane_b32 s63, v46\n" \
  "v_lshlrev_b32 v46, 5, v5\n" \
  "s_and_b32 s63, s63, 7\n" \
  "s_cmp_eq_u32 s63, 7\n" \
  "s_cbranch_scc0 .LCX_W2_%=\n" \
  "s_mov_b64 exec, 15\n" \
  "global_store_dword v46, v47, s[52:53] sc1\n" \
  "s_mov_b64 exec, -1\n" \
  ".LCX_W2_%=:\n" \
  "s_add_u32 s52, s52, 2048\n" \
  "s_addc_u32 s53, s53, 0\n" \
  ".LCX_NP_%=:\n" \
  "s_cmp_lg_u32 s68, 0\n" \
  "s_cbranch_scc1 .LCX_TEST_%=\n" \
  ".LCX_WF_%=:\n" \
  "s_memrealtime s[38:39]\n" \
  "s_waitcnt lgkmcnt(0)\n" \
  "s_sub_u32 s63, s69, s38\n" \
  "s_cmp_le_i32 s63, 0\n" \
  "s_cbranch_scc1 .LCX_GO_%=\n" \
  "s_cmp_lt_i32 s63, 12\n" \
  "s_cbranch_scc1 .LCX_WF_%=\n" \
  "s_sleep 3\n" \
  "s_branch .LCX_WF_%=\n" \
  ".LCX_GO_%=:\n" \
  "v_mov_b32 v35, 0\n" \
  "global_load_dwordx4 v[128:131], v2, s[48:49] offset:0 sc1\n" \
  "global_load_dwordx4 v[132:135], v2, s[48:49] offset:512 sc1\n" \
  "global_load_dwordx4 v[136:139], v2, s[48:49] offset:1024 sc1\n" \
  "global_load_dwordx4 v[140:143], v2, s[48:49] offset:1536 sc1\n" \
  "global_load_dwordx4 v[144:147], v3, s[48:49] offset:0 sc1\n" \
  "global_load_dwordx4 v[148:151], v3, s[48:49] offset:512 sc1\n" \
  "global_load_dwordx4 v[152:155], v3, s[48:49] offset:1024 sc1\n" \
  "global_load_dwordx4 v[156:159], v3, s[48:49] offset:1536 sc1\n" \
  "global_load_dword v35, v35, s[50:51] sc1\n" \
  "s_sub_u32 s62, s62, 8\n" \
  "s_cbranch_scc1 .LCX_END_%=\n" \
  "s_waitcnt vmcnt(0)\n" \
  ".LCX_TEST_%=:\n" \
  "v_mfma_f32_16x16x32_bf16 v[28:31], v[128:131], v[48:51], v[16:19]\n" \
  "v_min3_u32 v33, v128, v129, v130\n" \
  "v_min3_u32 v36, v131, v132, v133\n" \
  "v_min3_u32 v33, v33, v134, v135\n" \
  "v_min3_u32 v36, v36, v136, v137\n" \
  "v_mfma_f32_16x16x32_bf16 v[28:31], v[132:135], v[52:55], v[28:31]\n" \
  "v_min3_u32 v33, v33, v138, v139\n" \
  "v_min3_u32 v36, v36, v140, v141\n" \
  "v_min3_u32 v33, v33, v142, v143\n" \
  "v_min3_u32 v36, v36, v144, v145\n" \
  "v_mfma_f32_16x16x32_bf16 v[28:31], v[136:139], v[56:59], v[28:31]\n" \
  "v_min3_u32 v33, v33, v146, v147\n" \
  "v_min3_u32 v36, v36, v148, v149\n" \
  "v_min3_u32 v33, v33, v150, v151\n" \
  "v_min3_u32 v36, v36, v152, v153\n" \
  "v_mfma_f32_16x16x32_bf16 v[28:31], v[140:143], v[60:63], v[28:31]\n" \
  "v_min3_u32 v33, v33, v154, v155\n" \
  "v_min3_u32 v36, v36, v156, v157\n" \
  "v_min3_u32 v33, v33, v158, v159\n" \
  "v_min_u32 v33, v33, v36\n" \
  "v_cmp_eq_u32 vcc, 0, v33\n" \
  "v_readfirstlane_b32 s64, v35\n" \
  "s_mov_b32 s68, 0\n" \
  "s_cmp_eq_u64 vcc, 0\n" \
  "s_cbranch_scc0 .LCX_FAIL_%=\n" \
  "v_mov_b32 v33, s67\n" \
  "ds_write_b32 v38, v33 offset:16\n" \
  "v_max3_f32 v36, v28, v29, v30\n" \
  "v_max3_f32 v36, v36, v31, v32\n" \
  "v_mov_b32 v33, v36\n" \
  "s_cmp_eq_u32 s64, 0\n" \
  "s_cselect_b32 s64, s38, s64\n" \
  "v_permlane16_swap_b32 v36, v33\n" \
  "v_max_f32 v36, v36, v33\n" \
  "v_mov_b32 v33, v36\n" \
  "s_sub_u32 s63, s64, s70\n" \
  "s_mov_b32 s70, s64\n" \
  "v_permlane32_swap_b32 v36, v33\n" \
  "v_max_f32 v36, v36, v33\n" \
  "v_mul_f32 v36, s72, v36\n" \
  "v_fma_f32 v32, v32, s71, v36\n" \
  "v_fma_f32 v28, v28, s71, v36\n" \
  "v_fma_f32 v29, v29, s71, v36\n" \
  "v_fma_f32 v30, v30, s71, v36\n" \
  "v_fma_f32 v31, v31, s71, v36\n" \
  "v_exp_f32 v32, v32\n" \
  "v_exp_f32 v28, v28\n" \
  "v_exp_f32 v29, v29\n" \
  "v_exp_f32 v30, v30\n" \
  "v_exp_f32 v31, v31\n" \
  "v_fma_f32 v34, v34, v32, v28\n" \
  "v_add_f32 v33, v29, v30\n" \
  "v_add_f32 v34, v34, v31\n" \
  "v_add_f32 v34, v34, v33\n" \
  "v_mov_b32 v33, v34\n" \
  "s_min_u32 s63, s63, 250\n" \
  "s_add_u32 s69, s64, s63\n" \
  "v_permlane16_swap_b32 v34, v33\n" \
  "v_add_f32 v34, v34, v33\n" \
  "v_mov_b32 v33, v34\n" \
  "s_add_u32 s69, s69, -48\n" \
  "s_add_u32 s48, s48, 0x10000\n" \
  "s_addc_u32 s49, s49, 0\n" \
  "v_permlane32_swap_b32 v34, v33\n" \
  "v_add_f32 v34, v34, v33\n" \
  "v_rcp_f32 v34, v34\n" \
  "s_add_u32 s50, s50, 1024\n" \
  "s_addc_u32 s51, s51, 0\n" \
  "v_mul_f32 v32, v32, v34\n" \
  "v_mul_f32 v20, v20, v32\n" \
  "v_mul_f32 v21, v21, v32\n" \
  "v_mul_f32 v22, v22, v32\n" \
  "v_mul_f32 v23, v23, v32\n" \
  "v_mul_f32 v24, v24, v32\n" \
  "v_mul_f32 v25, v25, v32\n" \
  "v_mul_f32 v26, v26, v32\n" \
  "v_mul_f32 v27, v27, v32\n" \
  "v_cvt_pk_bf16_f32 v40, v20, v21\n" \
  "v_cvt_pk_bf16_f32 v41, v22, v23\n" \
  "v_cvt_pk_bf16_f32 v42, v24, v25\n" \
  "v_cvt_pk_bf16_f32 v43, v26, v27\n" \
  "v_mul_f32 v28, v28, v34\n" \
  "v_mul_f32 v29, v29, v34\n" \
  "v_mfma_f32_16x16x32_bf16 v[64:67], v[96:99], v[40:43], 0\n" \
  "v_mul_f32 v30, v30, v34\n" \
  "v_mfma_f32_16x16x32_bf16 v[68:71], v[100:103], v[40:43], 0\n" \
  "v_mul_f32 v31, v31, v34\n" \
  "v_mfma_f32_16x16x32_bf16 v[72:75], v[104:107], v[40:43], 0\n" \
  "v_cvt_pk_bf16_f32 v44, v28, v29\n" \
  "v_mfma_f32_16x16x32_bf16 v[76:79], v[108:111], v[40:43], 0\n" \
  "v_cvt_pk_bf16_f32 v45, v30, v31\n" \
  "v_mfma_f32_16x16x32_bf16 v[80:83], v[112:115], v[40:43], 0\n" \
  "v_mfma_f32_16x16x32_bf16 v[84:87], v[116:119], v[40:43], 0\n" \
  "v_mfma_f32_16x16x32_bf16 v[88:91], v[120:123], v[40:43], 0\n" \
  "v_mfma_f32_16x16x32_bf16 v[92:95], v[124:127], v[40:43], 0\n" \
  "s_memrealtime s[38:39]\n" \
  "ds_read_b32 v37, v38\n" \
  "v_mfma_f32_16x16x16_bf16 v[64:67], v[144:145], v[44:45], v[64:67]\n" \
  "v_mfma_f32_16x16x16_bf16 v[68:71], v[148:149], v[44:45], v[68:71]\n" \
  "v_mfma_f32_16x16x16_bf16 v[72:75], v[152:153], v[44:45], v[72:75]\n" \
  "v_mfma_f32_16x16x16_bf16 v[76:79], v[156:157], v[44:45], v[76:79]\n" \
  "v_mfma_f32_16x16x16_bf16 v[80:83], v[146:147], v[44:45], v[80:83]\n" \
  "v_mfma_f32_16x16x16_bf16 v[84:87], v[150:151], v[44:45], v[84:87]\n" \
  "v_mfma_f32_16x16x16_bf16 v[88:91], v[154:155], v[44:45], v[88:91]\n" \
  "v_mfma_f32_16x16x16_bf16 v[92:95], v[158:159], v[44:45], v[92:95]\n" \
  "s_nop 0\n" \
  "v_cvt_pk_bf16_f32 v160, v64, v65\n" \
  "v_cvt_pk_bf16_f32 v161, v66, v67\n" \
  "v_cvt_pk_bf16_f32 v162, v68, v69\n" \
  "v_cvt_pk_bf16_f32 v163, v70, v71\n" \
  "ds_write_b128 v7, v[160:163] offset:0\n" \
  "v_cvt_pk_bf16_f32 v164, v72, v73\n" \
  "v_cvt_pk_bf16_f32 v165, v74, v75\n" \
  "v_cvt_pk_bf16_f32 v166, v76, v77\n" \
  "v_cvt_pk_bf16_f32 v167, v78, v79\n" \
  "ds_write_b128 v7, v[164:167] offset:1024\n" \
  "v_cvt_pk_bf16_f32 v168, v80, v81\n" \
  "v_cvt_pk_bf16_f32 v169, v82, v83\n" \
  "v_cvt_pk_bf16_f32 v170, v84, v85\n" \
  "v_cvt_pk_bf16_f32 v171, v86, v87\n" \
  "ds_write_b128 v7, v[168:171] offset:2048\n" \
  "v_cvt_pk_bf16_f32 v172, v88, v89\n" \
  "v_cvt_pk_bf16_f32 v173, v90, v91\n" \
  "v_cvt_pk_bf16_f32 v174, v92, v93\n" \
  "v_cvt_pk_bf16_f32 v175, v94, v95\n" \
  "ds_write_b128 v7, v[172:175] offset:3072\n" \
  "v_add_u32 v7, s66, v7\n" \
  "s_waitcnt lgkmcnt(0)\n" \
  "s_barrier\n" \
  "ds_read_b128 v[48:51], v1 offset:0\n" \
  "ds_read_b128 v[52:55], v1 offset:4096\n" \
  "ds_read_b128 v[56:59], v1 offset:1024\n" \
  "ds_read_b128 v[60:63], v1 offset:5120\n" \
  "ds_read_b128 v[64:67], v1 offset:2048\n" \
  "ds_read_b128 v[68:71], v1 offset:6144\n" \
  "ds_read_b128 v[72:75], v1 offset:3072\n" \
  "ds_read_b128 v[76:79], v1 offset:7168\n" \
  "ds_read_b128 v[80:83], v1 offset:8192\n" \
  "ds_read_b128 v[84:87], v1 offset:12288\n" \
  "ds_read_b128 v[88:91], v1 offset:9216\n" \
  "ds_read_b128 v[92:95], v1 offset:13312\n" \
  "ds_read_b128 v[96:99], v1 offset:10240\n" \
  "ds_read_b128 v[100:103], v1 offset:14336\n" \
  "v_readfirstlane_b32 s73, v37\n" \
  "s_sub_u32 s63, s38, s69\n" \
  "s_bfe_u32 s64, s73, 0x80010\n" \
  "s_sub_u32 s64, s64, s67\n" \
  "s_or_b32 s63, s63, s64\n" \
  "s_bfe_u32 s64, s73, 0x80008\n" \
  "s_and_b32 s73, s73, 0xff\n" \
  "s_min_u32 s73, s73, s64\n" \
  "s_or_b32 s63, s63, s68\n" \
  "s_cmp_lt_i32 s63, 0\n" \
  "s_cbranch_scc1 .LCX_E3_%=\n" \
  "v_mov_b32 v35, 0\n" \
  "global_load_dwordx4 v[128:131], v2, s[48:49] offset:0 sc1\n" \
  "global_load_dwordx4 v[132:135], v2, s[48:49] offset:512 sc1\n" \
  "global_load_dwordx4 v[136:139], v2, s[48:49] offset:1024 sc1\n" \
  "global_load_dwordx4 v[140:143], v2, s[48:49] offset:1536 sc1\n" \
  "global_load_dwordx4 v[144:147], v3, s[48:49] offset:0 sc1\n" \
  "global_load_dwordx4 v[148:151], v3, s[48:49] offset:512 sc1\n" \
  "global_load_dwordx4 v[152:155], v3, s[48:49] offset:1024 sc1\n" \
  "global_load_dwordx4 v[156:159], v3, s[48:49] offset:1536 sc1\n" \
  "global_load_dword v35, v35, s[50:51] sc1\n" \
  "v_mov_b32 v33, s67\n" \
  "ds_write_b32 v38, v33 offset:8\n" \
  "s_mov_b32 s68, -1\n" \
  ".LCX_E3_%=:\n" \
  "s_waitcnt lgkmcnt(12)\n" \
  "v_mfma_f32_16x16x32_bf16 v[112:115], v[192:195], v[48:51], 0\n" \
  "v_mfma_f32_16x16x32_bf16 v[116:119], v[224:227], v[48:51], 0\n" \
  "v_mfma_f32_16x16x32_bf16 v[120:123], v[192:195], v[52:55], 0\n" \
  "v_mfma_f32_16x16x32_bf16 v[124:127], v[224:227], v[52:55], 0\n" \
  "ds_read_b128 v[104:107], v1 offset:11264\n" \
  "ds_read_b128 v[108:111], v1 offset:15360\n" \
  "s_waitcnt lgkmcnt(12)\n" \
  "v_mfma_f32_16x16x32_bf16 v[112:115], v[196:199], v[56:59], v[112:115]\n" \
  "v_mfma_f32_16x16x32_bf16 v[116:119], v[228:231], v[56:59], v[116:119]\n" \
  "v_mfma_f32_16x16x32_bf16 v[120:123], v[196:199], v[60:63], v[120:123]\n" \
  "v_mfma_f32_16x16x32_bf16 v[124:127], v[228:231], v[60:63], v[124:127]\n" \
  "s_waitcnt lgkmcnt(10)\n" \
  "v_mfma_f32_16x16x32_bf16 v[112:115], v[200:203], v[64:67], v[112:115]\n" \
  "v_mfma_f32_16x16x32_bf16 v[116:119], v[232:235], v[64:67], v[116:119]\n" \
  "v_mfma_f32_16x16x32_bf16 v[120:123], v[200:203], v[68:71], v[120:123]\n" \
  "v_mfma_f32_16x16x32_bf16 v[124:127], v[232:235], v[68:71], v[124:127]\n" \
  "s_waitcnt lgkmcnt(8)\n" \
  "v_mfma_f32_16x16x32_bf16 v[112:115], v[204:207], v[72:75], v[112:115]\n" \
  "v_mfma_f32_16x16x32_bf16 v[116:119], v[236:239], v[72:75], v[116:119]\n" \
  "v_mfma_f32_16x16x32_bf16 v[120:123], v[204:207], v[76:79], v[120:123]\n" \
  "v_mfma_f32_16x16x32_bf16 v[124:127], v[236:239], v[76:79], v[124:127]\n" \
  "s_waitcnt lgkmcnt(6)\n" \
  "v_mfma_f32_16x16x32_bf16 v[112:115], v[208:211], v[80:83], v[112:115]\n" \
  "v_mfma_f32_16x16x32_bf16 v[116:119], v[240:243], v[80:83], v[116:119]\n" \
  "v_mfma_f32_16x16x32_bf16 v[120:123], v[208:211], v[84:87], v[120:123]\n" \
  "v_mfma_f32_16x16x32_bf16 v[124:127], v[240:243], v[84:87], v[124:127]\n" \
  "s_waitcnt lgkmcnt(4)\n" \
  "v_mfma_f32_16x16x32_bf16 v[112:115], v[212:215], v[88:91], v[112:115]\n" \
  "v_mfma_f32_16x16x32_bf16 v[116:119], v[244:247], v[88:91], v[116:119]\n" \
  "v_mfma_f32_16x16x32_bf16 v[120:123], v[212:215], v[92:95], v[120:123]\n" \
  "v_mfma_f32_16x16x32_bf16 v[124:127], v[244:247], v[92:95], v[124:127]\n" \
  "s_memrealtime s[38:39]\n" \
  "ds_read_b32 v37, v38\n" \
  "s_waitcnt lgkmcnt(3)\n" \
  "v_mfma_f32_16x16x32_bf16 v[112:115], v[216:219], v[96:99], v[112:115]\n" \
  "v_mfma_f32_16x16x32_bf16 v[116:119], v[248:251], v[96:99], v[116:119]\n" \
  "v_mfma_f32_16x16x32_bf16 v[120:123], v[216:219], v[100:103], v[120:123]\n" \
  "v_mfma_f32_16x16x32_bf16 v[124:127], v[248:251], v[100:103], v[124:127]\n" \
  "s_waitcnt lgkmcnt(0)\n" \
  "v_mfma_f32_16x16x32_bf16 v[112:115], v[220:223], v[104:107], v[112:115]\n" \
  "v_mfma_f32_16x16x32_bf16 v[116:119], v[252:255], v[104:107], v[116:119]\n" \
  "v_mfma_f32_16x16x32_bf16 v[120:123], v[220:223], v[108:111], v[120:123]\n" \
  "v_mfma_f32_16x16x32_bf16 v[124:127], v[252:255], v[108:111], v[124:127]\n" \
  "v_add_u32 v1, s66, v1\n" \
  "s_sub_u32 s66, 0, s66\n" \
  "s_add_u32 s64, s54, 16384\n" \
  "s_mov_b32 s61, 1\n" \
  "s_nop 1\n" \
  "v_cvt_pk_bf16_f32 v20, v112, v113\n" \
  "v_cvt_pk_bf16_f32 v21, v114, v115\n" \
  "v_cvt_pk_bf16_f32 v22, v116, v117\n" \
  "v_cvt_pk_bf16_f32 v23, v118, v119\n" \
  "buffer_store_dwordx4 v[20:23], v4, s[40:43], s54 offen sc1\n" \
  "v_cvt_pk_bf16_f32 v24, v120, v121\n" \
  "v_cvt_pk_bf16_f32 v25, v122, v123\n" \
  "v_cvt_pk_bf16_f32 v26, v124, v125\n" \
  "v_cvt_pk_bf16_f32 v27, v126, v127\n" \
  "buffer_store_dwordx4 v[24:27], v4, s[40:43], s64 offen sc1\n" \
  "v_readfirstlane_b32 s73, v37\n" \
  "s_sub_u32 s63, s38, s69\n" \
  "s_bfe_u32 s64, s73, 0x80010\n" \
  "s_sub_u32 s64, s64, s67\n" \
  "s_or_b32 s63, s63, s64\n" \
  "s_bfe_u32 s64, s73, 0x80008\n" \
  "s_and_b32 s73, s73, 0xff\n" \
  "s_min_u32 s73, s73, s64\n" \
  "s_or_b32 s63, s63, s68\n" \
  "s_cmp_lt_i32 s63, 0\n" \
  "s_cbranch_scc1 .LCX_E4_%=\n" \
  "v_mov_b32 v35, 0\n" \
  "global_load_dwordx4 v[128:131], v2, s[48:49] offset:0 sc1\n" \
  "global_load_dwordx4 v[132:135], v2, s[48:49] offset:512 sc1\n" \
  "global_load_dwordx4 v[136:139], v2, s[48:49] offset:1024 sc1\n" \
  "global_load_dwordx4 v[140:143], v2, s[48:49] offset:1536 sc1\n" \
  "global_load_dwordx4 v[144:147], v3, s[48:49] offset:0 sc1\n" \
  "global_load_dwordx4 v[148:151], v3, s[48:49] offset:512 sc1\n" \
  "global_load_dwordx4 v[152:155], v3, s[48:49] offset:1024 sc1\n" \
  "global_load_dwordx4 v[156:159], v3, s[48:49] offset:1536 sc1\n" \
  "global_load_dword v35, v35, s[50:51] sc1\n" \
  "v_mov_b32 v33, s67\n" \
  "ds_write_b32 v38, v33 offset:8\n" \
  "s_mov_b32 s68, -1\n" \
  ".LCX_E4_%=:\n" \
  "s_add_u32 s54, s54, 0x80000\n" \
  "s_sub_u32 s60, s60, 1\n" \
  "s_cmp_lg_u32 s60, 0\n" \
  "s_cbranch_scc1 .LCX_TOP_%=\n" \
  "s_waitcnt vmcnt(0)\n" \
  "v_mov_b32 v46, s59\n" \
  "v_mov_b32 v47, 1\n" \
  "s_mov_b64 exec, 1\n" \
  "ds_add_rtn_u32 v46, v46, v47\n" \
  "s_mov_b64 exec, -1\n" \
  "s_xor_b32 s59, s59, 4\n" \
  "s_mov_b32 s61, 0\n" \
  "s_waitcnt lgkmcnt(0)\n" \
  "v_readfirstlane_b32 s63, v46\n" \
  "v_lshlrev_b32 v46, 5, v5\n" \
  "s_and_b32 s63, s63, 7\n" \
  "s_cmp_eq_u32 s63, 7\n" \
  "s_cbranch_scc0 .LCX_W5_%=\n" \
  "s_mov_b64 exec, 15\n" \
  "global_store_dword v46, v47, s[52:53] sc1\n" \
  "s_mov_b64 exec, -1\n" \
  ".LCX_W5_%=:\n" \
  "s_add_u32 s52, s52, 2048\n" \
  "s_addc_u32 s53, s53, 0\n" \
  ".LCX_END_%=:\n" \
  "s_endpgm\n" \
  ".LCX_FAIL_%=:\n" \
  "s_add_u32 s69, s64, -48\n" \
  "s_cmp_lg_u32 s64, 0\n" \
  "s_cbranch_scc1 .LCX_WF_%=\n" \
  "s_sleep 4\n" \
  "global_load_dword v35, v35, s[50:51] sc1\n" \
  "s_sub_u32 s62, s62, 8\n" \
  "s_cbranch_scc1 .LCX_END_%=\n" \
  "s_waitcnt vmcnt(0)\n" \
  "v_readfirstlane_b32 s64, v35\n" \
  "s_branch .LCX_FAIL_%=\n"
#define AW_ASM_Y \
  "s_mov_b32 s72, 0\n" \
  "s_mov_b32 s67, 0\n" \
  "s_mov_b32 s68, 2\n" \
  "s_mov_b32 s69, 0\n" \
  "s_mov_b32 s65, 49152\n" \
  "s_mov_b32 s66, 16384\n" \
  "v_mov_b32 v5, s58\n" \
  "v_mov_b32 v60, v3\n" \
  "v_add_u32 v3, 4096, v2\n" \
  "s_mov_b32 s62, 0x40000\n" \
  "s_branch .LCY_ASK_%=\n" \
  ".LCY_BAR_%=:\n" \
  "s_barrier\n" \
  "ds_read_b128 v[8:11], v0 offset:0\n" \
  "ds_read_b128 v[12:15], v0 offset:1024\n" \
  "ds_read_b128 v[16:19], v0 offset:2048\n" \
  "s_add_u32 s68, s68, 1\n" \
  "s_add_u32 s64, s54, 16384\n" \
  "s_cmp_eq_u32 s69, 0\n" \
  "s_cbranch_scc1 .LCY_NI_%=\n" \
  "s_waitcnt vmcnt(1)\n" \
  "s_cmp_lg_u32 s61, 0\n" \
  "s_cbranch_scc1 .LCY_NJ_%=\n" \
  "s_waitcnt vmcnt(0)\n" \
  ".LCY_NJ_%=:\n" \
  "v_mov_b32 v6, s55\n" \
  "s_mov_b64 exec, 1\n" \
  "ds_add_u32 v5, v6\n" \
  "s_mov_b64 exec, -1\n" \
  "s_mov_b32 s69, 0\n" \
  "s_cmp_eq_u32 s72, 0\n" \
  "s_cbranch_scc1 .LCY_NI_%=\n" \
  "s_mov_b32 s72, 0\n" \
  "v_mov_b32 v6, s59\n" \
  "v_mov_b32 v7, 1\n" \
  "s_mov_b64 exec, 1\n" \
  "ds_add_rtn_u32 v6, v6, v7\n" \
  "s_mov_b64 exec, -1\n" \
  "s_xor_b32 s59, s59, 4\n" \
  "s_mov_b32 s61, 0\n" \
  "s_waitcnt lgkmcnt(0)\n" \
  "v_readfirstlane_b32 s63, v6\n" \
  "v_lshlrev_b32 v6, 5, v2\n" \
  "s_and_b32 s63, s63, 7\n" \
  "s_cmp_eq_u32 s63, 7\n" \
  "s_cbranch_scc0 .LCY_W1_%=\n" \
  "s_mov_b64 exec, 15\n" \
  "global_store_dword v6, v7, s[52:53] sc1\n" \
  "s_mov_b64 exec, -1\n" \
  ".LCY_W1_%=:\n" \
  "s_add_u32 s52, s52, 2048\n" \
  "s_addc_u32 s53, s53, 0\n" \
  ".LCY_NI_%=:\n" \
  "v_cmp_eq_u32 vcc, 0, v4\n" \
  "s_cmp_eq_u64 vcc, 0\n" \
  "s_cselect_b32 s70, 1, 0\n" \
  "s_cbranch_scc0 .LCY_NS_%=\n" \
  "v_lshlrev_b32 v6, 16, s55\n" \
  "s_mov_b64 exec, 1\n" \
  "ds_add_u32 v5, v6\n" \
  "s_mov_b64 exec, -1\n" \
  "s_mov_b32 m0, s56\n" \
  "s_add_u32 s71, s56, 4096\n" \
  "global_load_lds_dwordx4 v2, s[48:49] offset:0 sc1\n" \
  "global_load_lds_dwordx4 v2, s[48:49] offset:1024 sc1\n" \
  "global_load_lds_dwordx4 v2, s[48:49] offset:2048 sc1\n" \
  "global_load_lds_dwordx4 v2, s[48:49] offset:3072 sc1\n" \
  "s_mov_b32 m0, s71\n" \
  "s_add_u32 s56, s56, s65\n" \
  "global_load_lds_dwordx4 v3, s[48:49] offset:0 sc1\n" \
  "global_load_lds_dwordx4 v3, s[48:49] offset:1024 sc1\n" \
  ".LCY_NS_%=:\n" \
  "ds_read_b128 v[20:23], v0 offset:3072\n" \
  "s_waitcnt lgkmcnt(3)\n" \
  "v_mfma_f32_16x16x32_bf16 v[32:35], v[64:67], v[8:11], 0\n" \
  "v_mfma_f32_16x16x32_bf16 v[36:39], v[96:99], v[8:11], 0\n" \
  "v_mfma_f32_16x16x32_bf16 v[40:43], v[128:131], v[8:11], 0\n" \
  "v_mfma_f32_16x16x32_bf16 v[44:47], v[160:163], v[8:11], 0\n" \
  "v_mfma_f32_16x16x32_bf16 v[48:51], v[192:195], v[8:11], 0\n" \
  "v_mfma_f32_16x16x32_bf16 v[52:55], v[224:227], v[8:11], 0\n" \
  "ds_read_b128 v[8:11], v0 offset:8192\n" \
  "s_waitcnt lgkmcnt(3)\n" \
  "v_mfma_f32_16x16x32_bf16 v[32:35], v[68:71], v[12:15], v[32:35]\n" \
  "v_mfma_f32_16x16x32_bf16 v[36:39], v[100:103], v[12:15], v[36:39]\n" \
  "v_mfma_f32_16x16x32_bf16 v[40:43], v[132:135], v[12:15], v[40:43]\n" \
  "v_mfma_f32_16x16x32_bf16 v[44:47], v[164:167], v[12:15], v[44:47]\n" \
  "v_mfma_f32_16x16x32_bf16 v[48:51], v[196:199], v[12:15], v[48:51]\n" \
  "v_mfma_f32_16x16x32_bf16 v[52:55], v[228:231], v[12:15], v[52:55]\n" \
  "ds_read_b128 v[12:15], v0 offset:9216\n" \
  "s_waitcnt lgkmcnt(3)\n" \
  "v_mfma_f32_16x16x32_bf16 v[32:35], v[72:75], v[16:19], v[32:35]\n" \
  "v_mfma_f32_16x16x32_bf16 v[36:39], v[104:107], v[16:19], v[36:39]\n" \
  "v_mfma_f32_16x16x32_bf16 v[40:43], v[136:139], v[16:19], v[40:43]\n" \
  "v_mfma_f32_16x16x32_bf16 v[44:47], v[168:171], v[16:19], v[44:47]\n" \
  "v_mfma_f32_16x16x32_bf16 v[48:51], v[200:203], v[16:19], v[48:51]\n" \
  "v_mfma_f32_16x16x32_bf16 v[52:55], v[232:235], v[16:19], v[52:55]\n" \
  "ds_read_b128 v[16:19], v0 offset:10240\n" \
  "s_waitcnt lgkmcnt(3)\n" \
  "v_mfma_f32_16x16x32_bf16 v[32:35], v[76:79], v[20:23], v[32:35]\n" \
  "v_mfma_f32_16x16x32_bf16 v[36:39], v[108:111], v[20:23], v[36:39]\n" \
  "v_mfma_f32_16x16x32_bf16 v[40:43], v[140:143], v[20:23], v[40:43]\n" \
  "v_mfma_f32_16x16x32_bf16 v[44:47], v[172:175], v[20:23], v[44:47]\n" \
  "v_mfma_f32_16x16x32_bf16 v[48:51], v[204:207], v[20:23], v[48:51]\n" \
  "v_mfma_f32_16x16x32_bf16 v[52:55], v[236:239], v[20:23], v[52:55]\n" \
  "ds_read_b128 v[20:23], v0 offset:11264\n" \
  "s_waitcnt lgkmcnt(3)\n" \
  "v_mfma_f32_16x16x32_bf16 v[32:35], v[80:83], v[8:11], v[32:35]\n" \
  "v_mfma_f32_16x16x32_bf16 v[36:39], v[112:115], v[8:11], v[36:39]\n" \
  "v_mfma_f32_16x16x32_bf16 v[40:43], v[144:147], v[8:11], v[40:43]\n" \
  "v_mfma_f32_16x16x32_bf16 v[44:47], v[176:179], v[8:11], v[44:47]\n" \
  "v_mfma_f32_16x16x32_bf16 v[48:51], v[208:211], v[8:11], v[48:51]\n" \
  "v_mfma_f32_16x16x32_bf16 v[52:55], v[240:243], v[8:11], v[52:55]\n" \
  "ds_read_b128 v[8:11], v0 offset:4096\n" \
  "s_waitcnt lgkmcnt(3)\n" \
  "v_mfma_f32_16x16x32_bf16 v[32:35], v[84:87], v[12:15], v[32:35]\n" \
  "v_mfma_f32_16x16x32_bf16 v[36:39], v[116:119], v[12:15], v[36:39]\n" \
  "v_mfma_f32_16x16x32_bf16 v[40:43], v[148:151], v[12:15], v[40:43]\n" \
  "v_mfma_f32_16x16x32_bf16 v[44:47], v[180:183], v[12:15], v[44:47]\n" \
  "v_mfma_f32_16x16x32_bf16 v[48:51], v[212:215], v[12:15], v[48:51]\n" \
  "v_mfma_f32_16x16x32_bf16 v[52:55], v[244:247], v[12:15], v[52:55]\n" \
  "ds_read_b128 v[12:15], v0 offset:5120\n" \
  "s_waitcnt lgkmcnt(3)\n" \
  "v_mfma_f32_16x16x32_bf16 v[32:35], v[88:91], v[16:19], v[32:35]\n" \
  "v_mfma_f32_16x16x32_bf16 v[36:39], v[120:123], v[16:19], v[36:39]\n" \
  "v_mfma_f32_16x16x32_bf16 v[40:43], v[152:155], v[16:19], v[40:43]\n" \
  "v_mfma_f32_16x16x32_bf16 v[44:47], v[184:187], v[16:19], v[44:47]\n" \
  "v_mfma_f32_16x16x32_bf16 v[48:51], v[216:219], v[16:19], v[48:51]\n" \
  "v_mfma_f32_16x16x32_bf16 v[52:55], v[248:251], v[16:19], v[52:55]\n" \
  "ds_read_b128 v[16:19], v0 offset:6144\n" \
  "s_waitcnt lgkmcnt(3)\n" \
  "v_mfma_f32_16x16x32_bf16 v[32:35], v[92:95], v[20:23], v[32:35]\n" \
  "v_mfma_f32_16x16x32_bf16 v[36:39], v[124:127], v[20:23], v[36:39]\n" \
  "v_mfma_f32_16x16x32_bf16 v[40:43], v[156:159], v[20:23], v[40:43]\n" \
  "v_mfma_f32_16x16x32_bf16 v[44:47], v[188:191], v[20:23], v[44:47]\n" \
  "v_mfma_f32_16x16x32_bf16 v[48:51], v[220:223], v[20:23], v[48:51]\n" \
  "v_mfma_f32_16x16x32_bf16 v[52:55], v[252:255], v[20:23], v[52:55]\n" \
  "ds_read_b128 v[20:23], v0 offset:7168\n" \
  "s_waitcnt lgkmcnt(3)\n" \
  "s_nop 1\n" \
  "v_cvt_pk_bf16_f32 v24, v32, v33\n" \
  "v_cvt_pk_bf16_f32 v25, v34, v35\n" \
  "v_cvt_pk_bf16_f32 v26, v36, v37\n" \
  "v_cvt_pk_bf16_f32 v27, v38, v39\n" \
  "s_nop 0\n" \
  "v_mfma_f32_16x16x32_bf16 v[32:35], v[64:67], v[8:11], 0\n" \
  "v_mfma_f32_16x16x32_bf16 v[36:39], v[96:99], v[8:11], 0\n" \
  "buffer_store_dwordx4 v[24:27], v1, s[40:43], s54 offen offset:0 sc1\n" \
  "v_cvt_pk_bf16_f32 v28, v40, v41\n" \
  "v_cvt_pk_bf16_f32 v29, v42, v43\n" \
  "v_cvt_pk_bf16_f32 v30, v44, v45\n" \
  "v_cvt_pk_bf16_f32 v31, v46, v47\n" \
  "s_nop 0\n" \
  "v_mfma_f32_16x16x32_bf16 v[40:43], v[128:131], v[8:11], 0\n" \
  "v_mfma_f32_16x16x32_bf16 v[44:47], v[160:163], v[8:11], 0\n" \
  "buffer_store_dwordx4 v[28:31], v1, s[40:43], s54 offen offset:128 sc1\n" \
  "v_cvt_pk_bf16_f32 v56, v48, v49\n" \
  "v_cvt_pk_bf16_f32 v57, v50, v51\n" \
  "v_cvt_pk_bf16_f32 v58, v52, v53\n" \
  "v_cvt_pk_bf16_f32 v59, v54, v55\n" \
  "s_nop 0\n" \
  "v_mfma_f32_16x16x32_bf16 v[48:51], v[192:195], v[8:11], 0\n" \
  "v_mfma_f32_16x16x32_bf16 v[52:55], v[224:227], v[8:11], 0\n" \
  "buffer_store_dwordx4 v[56:59], v1, s[40:43], s54 offen offset:256 sc1\n" \
  "ds_read_b128 v[8:11], v0 offset:12288\n" \
  "s_waitcnt lgkmcnt(3)\n" \
  "v_mfma_f32_16x16x32_bf16 v[32:35], v[68:71], v[12:15], v[32:35]\n" \
  "v_mfma_f32_16x16x32_bf16 v[36:39], v[100:103], v[12:15], v[36:39]\n" \
  "v_mfma_f32_16x16x32_bf16 v[40:43], v[132:135], v[12:15], v[40:43]\n" \
  "v_mfma_f32_16x16x32_bf16 v[44:47], v[164:167], v[12:15], v[44:47]\n" \
  "v_mfma_f32_16x16x32_bf16 v[48:51], v[196:199], v[12:15], v[48:51]\n" \
  "v_mfma_f32_16x16x32_bf16 v[52:55], v[228:231], v[12:15], v[52:55]\n" \
  "ds_read_b128 v[12:15], v0 offset:13312\n" \
  "s_waitcnt lgkmcnt(3)\n" \
  "v_mfma_f32_16x16x32_bf16 v[32:35], v[72:75], v[16:19], v[32:35]\n" \
  "v_mfma_f32_16x16x32_bf16 v[36:39], v[104:107], v[16:19], v[36:39]\n" \
  "v_mfma_f32_16x16x32_bf16 v[40:43], v[136:139], v[16:19], v[40:43]\n" \
  "v_mfma_f32_16x16x32_bf16 v[44:47], v[168:171], v[16:19], v[44:47]\n" \
  "v_mfma_f32_16x16x32_bf16 v[48:51], v[200:203], v[16:19], v[48:51]\n" \
  "v_mfma_f32_16x16x32_bf16 v[52:55], v[232:235], v[16:19], v[52:55]\n" \
  "ds_read_b128 v[16:19], v0 offset:14336\n" \
  "s_waitcnt lgkmcnt(3)\n" \
  "v_mfma_f32_16x16x32_bf16 v[32:35], v[76:79], v[20:23], v[32:35]\n" \
  "v_mfma_f32_16x16x32_bf16 v[36:39], v[108:111], v[20:23], v[36:39]\n" \
  "v_mfma_f32_16x16x32_bf16 v[40:43], v[140:143], v[20:23], v[40:43]\n" \
  "v_mfma_f32_16x16x32_bf16 v[44:47], v[172:175], v[20:23], v[44:47]\n" \
  "v_mfma_f32_16x16x32_bf16 v[48:51], v[204:207], v[20:23], v[48:51]\n" \
  "v_mfma_f32_16x16x32_bf16 v[52:55], v[236:239], v[20:23], v[52:55]\n" \
  "ds_read_b32 v7, v5 offset:8\n" \
  "ds_read_b128 v[20:23], v0 offset:15360\n" \
  "s_waitcnt lgkmcnt(4)\n" \
  "v_mfma_f32_16x16x32_bf16 v[32:35], v[80:83], v[8:11], v[32:35]\n" \
  "v_mfma_f32_16x16x32_bf16 v[36:39], v[112:115], v[8:11], v[36:39]\n" \
  "v_mfma_f32_16x16x32_bf16 v[40:43], v[144:147], v[8:11], v[40:43]\n" \
  "v_mfma_f32_16x16x32_bf16 v[44:47], v[176:179], v[8:11], v[44:47]\n" \
  "v_mfma_f32_16x16x32_bf16 v[48:51], v[208:211], v[8:11], v[48:51]\n" \
  "v_mfma_f32_16x16x32_bf16 v[52:55], v[240:243], v[8:11], v[52:55]\n" \
  "s_waitcnt lgkmcnt(3)\n" \
  "v_mfma_f32_16x16x32_bf16 v[32:35], v[84:87], v[12:15], v[32:35]\n" \
  "v_mfma_f32_16x16x32_bf16 v[36:39], v[116:119], v[12:15], v[36:39]\n" \
  "v_mfma_f32_16x16x32_bf16 v[40:43], v[148:151], v[12:15], v[40:43]\n" \
  "v_mfma_f32_16x16x32_bf16 v[44:47], v[180:183], v[12:15], v[44:47]\n" \
  "v_mfma_f32_16x16x32_bf16 v[48:51], v[212:215], v[12:15], v[48:51]\n" \
  "v_mfma_f32_16x16x32_bf16 v[52:55], v[244:247], v[12:15], v[52:55]\n" \
  "s_waitcnt lgkmcnt(2)\n" \
  "v_mfma_f32_16x16x32_bf16 v[32:35], v[88:91], v[16:19], v[32:35]\n" \
  "v_mfma_f32_16x16x32_bf16 v[36:39], v[120:123], v[16:19], v[36:39]\n" \
  "v_mfma_f32_16x16x32_bf16 v[40:43], v[152:155], v[16:19], v[40:43]\n" \
  "v_mfma_f32_16x16x32_bf16 v[44:47], v[184:187], v[16:19], v[44:47]\n" \
  "v_mfma_f32_16x16x32_bf16 v[48:51], v[216:219], v[16:19], v[48:51]\n" \
  "v_mfma_f32_16x16x32_bf16 v[52:55], v[248:251], v[16:19], v[52:55]\n" \
  "s_waitcnt lgkmcnt(0)\n" \
  "v_mfma_f32_16x16x32_bf16 v[32:35], v[92:95], v[20:23], v[32:35]\n" \
  "v_mfma_f32_16x16x32_bf16 v[36:39], v[124:127], v[20:23], v[36:39]\n" \
  "v_mfma_f32_16x16x32_bf16 v[40:43], v[156:159], v[20:23], v[40:43]\n" \
  "v_mfma_f32_16x16x32_bf16 v[44:47], v[188:191], v[20:23], v[44:47]\n" \
  "v_mfma_f32_16x16x32_bf16 v[48:51], v[220:223], v[20:23], v[48:51]\n" \
  "v_mfma_f32_16x16x32_bf16 v[52:55], v[252:255], v[20:23], v[52:55]\n" \
  "v_add_u32 v0, s66, v0\n" \
  "s_sub_u32 s66, 0, s66\n" \
  "s_add_u32 s54, s54, 0x80000\n" \
  "s_mov_b32 s62, 0x40000\n" \
  "v_readfirstlane_b32 s63, v7\n" \
  "v_cvt_pk_bf16_f32 v24, v32, v33\n" \
  "v_cvt_pk_bf16_f32 v25, v34, v35\n" \
  "v_cvt_pk_bf16_f32 v26, v36, v37\n" \
  "v_cvt_pk_bf16_f32 v27, v38, v39\n" \
  "buffer_store_dwordx4 v[24:27], v1, s[40:43], s64 offen offset:0 sc1\n" \
  "v_cvt_pk_bf16_f32 v28, v40, v41\n" \
  "v_cvt_pk_bf16_f32 v29, v42, v43\n" \
  "v_cvt_pk_bf16_f32 v30, v44, v45\n" \
  "v_cvt_pk_bf16_f32 v31, v46, v47\n" \
  "buffer_store_dwordx4 v[28:31], v1, s[40:43], s64 offen offset:128 sc1\n" \
  "v_cvt_pk_bf16_f32 v56, v48, v49\n" \
  "v_cvt_pk_bf16_f32 v57, v50, v51\n" \
  "v_cvt_pk_bf16_f32 v58, v52, v53\n" \
  "v_cvt_pk_bf16_f32 v59, v54, v55\n" \
  "buffer_store_dwordx4 v[56:59], v1, s[40:43], s64 offen offset:256 sc1\n" \
  "s_cmp_eq_u32 s70, 0\n" \
  "s_cbranch_scc1 .LCY_ND_%=\n" \
  "global_load_lds_dwordx4 v3, s[48:49] offset:2048 sc1\n" \
  "global_load_lds_dwordx4 v3, s[48:49] offset:3072 sc1\n" \
  "s_add_u32 s48, s48, 0x80000\n" \
  "s_addc_u32 s49, s49, 0\n" \
  "s_mov_b32 m0, s57\n" \
  "s_add_u32 s57, s57, s65\n" \
  "global_load_lds_dwordx4 v2, s[50:51] offset:0 sc1\n" \
  "global_load_lds_dwordx4 v2, s[50:51] offset:1024 sc1\n" \
  "s_sub_u32 s65, 0, s65\n" \
  "s_add_u32 s67, s67, 4096\n" \
  "global_load_lds_dwordx4 v2, s[50:51] offset:2048 sc1\n" \
  "s_sub_u32 s68, s68, 1\n" \
  "s_mov_b32 s69, 1\n" \
  "global_load_lds_dwordx4 v2, s[50:51] offset:3072 sc1\n" \
  "s_add_u32 s50, s50, 0x40000\n" \
  "s_addc_u32 s51, s51, 0\n" \
  "v_mov_b32 v4, 0\n" \
  "buffer_load_dword v4, v60, s[44:47], s67 offen sc1\n" \
  "s_add_u32 s63, s63, s60\n" \
  "s_mov_b32 s61, 0\n" \
  "s_cmp_eq_u32 s63, 66\n" \
  "s_cselect_b32 s72, 1, 0\n" \
  "s_cbranch_scc1 .LCY_DEF_%=\n" \
  "s_waitcnt vmcnt(7)\n" \
  "s_branch .LCY_WC_%=\n" \
  ".LCY_ND_%=:\n" \
  "v_mov_b32 v4, 0\n" \
  "buffer_load_dword v4, v60, s[44:47], s67 offen sc1\n" \
  "s_waitcnt vmcnt(0)\n" \
  ".LCY_WC_%=:\n" \
  "v_mov_b32 v6, s59\n" \
  "v_mov_b32 v7, 1\n" \
  "s_mov_b64 exec, 1\n" \
  "ds_add_rtn_u32 v6, v6, v7\n" \
  "s_mov_b64 exec, -1\n" \
  "s_xor_b32 s59, s59, 4\n" \
  "s_mov_b32 s61, 0\n" \
  "s_waitcnt lgkmcnt(0)\n" \
  "v_readfirstlane_b32 s63, v6\n" \
  "v_lshlrev_b32 v6, 5, v2\n" \
  "s_and_b32 s63, s63, 7\n" \
  "s_cmp_eq_u32 s63, 7\n" \
  "s_cbranch_scc0 .LCY_W2_%=\n" \
  "s_mov_b64 exec, 15\n" \
  "global_store_dword v6, v7, s[52:53] sc1\n" \
  "s_mov_b64 exec, -1\n" \
  "s_mov_b32 s61, 1\n" \
  ".LCY_W2_%=:\n" \
  "s_add_u32 s52, s52, 2048\n" \
  "s_addc_u32 s53, s53, 0\n" \
  ".LCY_DEF_%=:\n" \
  "s_sub_u32 s60, s60, 1\n" \
  "s_cmp_eq_u32 s60, 0\n" \
  "s_cbranch_scc1 .LCY_END_%=\n" \
  ".LCY_NEED_%=:\n" \
  "s_cmp_eq_u32 s68, 0\n" \
  "s_cbranch_scc1 .LCY_BAR_%=\n" \
  "v_cmp_eq_u32 vcc, 0, v4\n" \
  "s_cmp_eq_u64 vcc, 0\n" \
  "s_cbranch_scc1 .LCY_GET_%=\n" \
  "s_cmp_ge_u32 s67, 0x40000\n" \
  "s_cbranch_scc1 .LCY_BAR_%=\n" \
  "s_sleep 4\n" \
  "ds_read_b32 v6, v5 offset:16\n" \
  "s_waitcnt lgkmcnt(0)\n" \
  "v_readfirstlane_b32 s63, v6\n" \
  "s_add_u32 s63, s63, s60\n" \
  "s_cmp_ge_u32 s63, 66\n" \
  "s_cbranch_scc1 .LCY_BAR_%=\n" \
  ".LCY_ASK_%=:\n" \
  "v_mov_b32 v4, 0\n" \
  "buffer_load_dword v4, v60, s[44:47], s67 offen sc1\n" \
  "s_sub_u32 s62, s62, 1\n" \
  "s_cbranch_scc1 .LCY_END_%=\n" \
  "s_waitcnt vmcnt(0)\n" \
  "s_branch .LCY_NEED_%=\n" \
  ".LCY_GET_%=:\n" \
  "v_lshlrev_b32 v6, 16, s55\n" \
  "s_mov_b64 exec, 1\n" \
  "ds_add_u32 v5, v6\n" \
  "s_mov_b64 exec, -1\n" \
  "s_mov_b32 m0, s56\n" \
  "s_add_u32 s71, s56, 4096\n" \
  "global_load_lds_dwordx4 v2, s[48:49] offset:0 sc1\n" \
  "global_load_lds_dwordx4 v2, s[48:49] offset:1024 sc1\n" \
  "global_load_lds_dwordx4 v2, s[48:49] offset:2048 sc1\n" \
  "global_load_lds_dwordx4 v2, s[48:49] offset:3072 sc1\n" \
  "s_mov_b32 m0, s71\n" \
  "s_add_u32 s56, s56, s65\n" \
  "global_load_lds_dwordx4 v3, s[48:49] offset:0 sc1\n" \
  "global_load_lds_dwordx4 v3, s[48:49] offset:1024 sc1\n" \
  "global_load_lds_dwordx4 v3, s[48:49] offset:2048 sc1\n" \
  "global_load_lds_dwordx4 v3, s[48:49] offset:3072 sc1\n" \
  "s_add_u32 s48, s48, 0x80000\n" \
  "s_addc_u32 s49, s49, 0\n" \
  "s_mov_b32 m0, s57\n" \
  "s_add_u32 s57, s57, s65\n" \
  "global_load_lds_dwordx4 v2, s[50:51] offset:0 sc1\n" \
  "global_load_lds_dwordx4 v2, s[50:51] offset:1024 sc1\n" \
  "s_sub_u32 s65, 0, s65\n" \
  "s_add_u32 s67, s67, 4096\n" \
  "global_load_lds_dwordx4 v2, s[50:51] offset:2048 sc1\n" \
  "s_sub_u32 s68, s68, 1\n" \
  "s_mov_b32 s69, 1\n" \
  "global_load_lds_dwordx4 v2, s[50:51] offset:3072 sc1\n" \
  "s_add_u32 s50, s50, 0x40000\n" \
  "s_addc_u32 s51, s51, 0\n" \
  "v_mov_b32 v4, 0\n" \
  "buffer_load_dword v4, v60, s[44:47], s67 offen sc1\n" \
  "s_mov_b32 s61, 0\n" \
  "s_cmp_eq_u32 s68, 0\n" \
  "s_cbranch_scc1 .LCY_BAR_%=\n" \
  "s_waitcnt vmcnt(0)\n" \
  "v_mov_b32 v6, s55\n" \
  "s_mov_b64 exec, 1\n" \
  "ds_add_u32 v5, v6\n" \
  "s_mov_b64 exec, -1\n" \
  "s_mov_b32 s69, 0\n" \
  "s_branch .LCY_NEED_%=\n" \
  ".LCY_END_%=:\n" \
  "s_endpgm\n"
template <bool AT>
__device__ __forceinline__ void role_aw_t(const MegaArgs& A, char* smem, const int L, const int a, const u64 tag) {
  const int w = A.wv;
  const int l = lane_id(), c = l & 15, g = l >> 4;
  const int qt = w & 1, hh = (w >> 1) & 1, h = 2 * a + hh;
  char* ws = A.ws;
  unsigned* FL = (unsigned*)(ws + OFF_FL);
  const unsigned sb = lds_base(smem);
  if (w == 0 && l < 8) reinterpret_cast<unsigned*>(smem + AW_CNT)[l] = 0u;
  constexpr int NP = AT ? 1 : 3;
  const int cp0 = AT ? w : 3 * w - 8;
  v8u wo[2 * NP][4];
  {
    const int q = (l >> 2) & 3, p = l & 3, f = q | ((g & 1) << 2);
    const unsigned ra = sb + (4 * g + q) * 256 + (p & 1) * 16;
#pragma unroll
    for (int r = 0; r < 2; ++r) {
#pragma unroll
      for (int k = 0; k < 2; ++k) {
        const char* base_ = (const char*)A.Wo + ((size_t)((2 * a + r) * DH + 64 * (w & 1) + 32 * k) * DM + 128 * (w >> 1)) * 2;
#pragma unroll
        for (int i = 0; i < 8; ++i) {
          const int fd = g | ((i & 1) << 2);
          glds16(base_, (unsigned)((4 * i + g) * (DM * 2) + ((c ^ (fd << 1)) << 4)), sb + w * 16384 + k * 8192 + i * 1024);
        }
      }
      SST(4 + 3 * r);
      if (r == 1) ready_wait(A, tag, ready_look(A));
      WAIT_VM(0);
      RAW_BAR();
      SST(5 + 3 * r);
#pragma unroll
      for (int k2 = 0; k2 < 2; ++k2) {
#pragma unroll
        for (int ct = 0; ct < 2 * NP; ++ct) {
          const int cp = cp0 + (ct >> 1);
          const unsigned a_ = ra + (cp >> 2) * 32768 + k2 * 16384 + ((2 * (cp & 3) + (p >> 1)) ^ f) * 32 + (ct & 1) * 8;
          const v2u r0 = trd(a_), r1 = trd(a_ + 4096), r2 = trd(a_ + 8192), r3 = trd(a_ + 12288);
          wo[ct][2 * r + k2] = v8u{r0.x, r0.y, r1.x, r1.y, r2.x, r2.y, r3.x, r3.y};
          asm volatile("" : "+v"(wo[ct][2 * r + k2]));
        }
      }
      RAW_BAR();
      SST(6 + 3 * r);
    }
  }
  WSTAMP2();
  const v4u rsW = mk_srd(ws + OFF_PW, SZ_PW);
  const int dp = (FL_FP - FL_FQ + (L * 16 + a - L * 64 * 32 - h) * 32 + 2 * hh) * 4;
  const v4u rsF = mk_srd(FL + FL_FQ + (L * 64 * 32 + h) * 32, (qt ? 0 : dp) + 64 * 4096 - 128);
  const char* p0 = AT ? ws + OFF_MEMK + L * SZ_MEM + h * 2048 : ws + OFF_QK + L * SZ_QK + ((size_t)h * 16 + 8 * qt) * 1024;
  const char* p1 = AT ? (const char*)(FL + FL_FK + L * 32 + (h >> 3) * 64 + (h & 7) * 2) : ws + OFF_VF + L * SZ_O + ((size_t)h * 8 + 4 * qt) * 1024;
  const unsigned* pFW = FL + FL_FW + (L * 64 * 16 + (a >> 2)) * 32 + (a & 3);
  const unsigned tk = sb + AW_TK + hh * 24 * 1024, voff = (c >> 1) * 2048 + (c & 1) * 64 + g * 16;
  const char* dbg = ws + WS_TOTAL + A.vb * 64 * 8;
#define AW_SOPS                                                                                                                 \
  "{s[40:43]}"(rsW), "{s[44:47]}"(rsF), "{s[48:49]}"(p0), "{s[50:51]}"(p1), "{s[52:53]}"(pFW),                                   \
      "{s54}"((L * 64 * 16 + a) * 32768 + cp0 * 128), "{s55}"(1 << (8 * qt)), "{s56}"(tk + qt * 8192), "{s57}"(tk + 16384 + qt * 4096),                  \
      "{s58}"(sb + AW_CNT + 8 + 4 * hh), "{s59}"(sb + AW_CNT), "{s60}"(NSEG), "{s[36:37]}"(dbg)                                  \
      : "memory", "vcc", "scc", "s80"
  if constexpr (AT) {
    const v8u pv = {tk + l * 16, sb + AW_SO + l * 16, (unsigned)(g * 8 + (c & 7)) * 16, (unsigned)(OFF_MEMV - OFF_MEMK) + c * 32 + (g & 1) * 16,
                    voff, (unsigned)l * 16, tk + qt * 4096 + l * 16, sb + AW_SO + w * 4096 + l * 16};
    const unsigned neg = 0xff61b1e6u;
    v8u bt;
#pragma unroll
    for (int i = 0; i < 4; ++i) {
      const unsigned d = 4 * g + i > c ? neg : 0u;
      bt[i] = qt ? 0u : d;
      bt[4 + i] = qt ? d : neg;
    }
    const unsigned e = g >= 2 ? neg : 0u;
    const v4u bm = {e, e, e, e};
    asm volatile("s_mov_b64 exec, -1\ns_waitcnt vmcnt(0) lgkmcnt(0)\ns_nop 4\n" AW_ASM_X
                 ::"{v[192:199]}"(wo[0][0]), "{v[200:207]}"(wo[0][1]), "{v[208:215]}"(wo[0][2]), "{v[216:223]}"(wo[0][3]),
                 "{v[224:231]}"(wo[1][0]), "{v[232:239]}"(wo[1][1]), "{v[240:247]}"(wo[1][2]), "{v[248:255]}"(wo[1][3]),
                 "{v[0:7]}"(pv), "{v[8:15]}"(bt), "{v[16:19]}"(bm), AW_SOPS);
  } else {
    const v4u pv = {sb + AW_SO + l * 16, voff, (unsigned)l * 16, (l & 2) ? 16u + 8 * qt : qt ? 8u * (l & 1) : dp + 4 * (l & 1)};
    asm volatile("s_mov_b64 exec, -1\ns_waitcnt vmcnt(0) lgkmcnt(0)\ns_nop 4\n" AW_ASM_Y
                 ::"{v[64:71]}"(wo[0][0]), "{v[72:79]}"(wo[0][1]), "{v[80:87]}"(wo[0][2]), "{v[88:95]}"(wo[0][3]),
                 "{v[96:103]}"(wo[1][0]), "{v[104:111]}"(wo[1][1]), "{v[112:119]}"(wo[1][2]), "{v[120:127]}"(wo[1][3]),
                 "{v[128:135]}"(wo[2][0]), "{v[136:143]}"(wo[2][1]), "{v[144:151]}"(wo[2][2]), "{v[152:159]}"(wo[2][3]),
                 "{v[160:167]}"(wo[3][0]), "{v[168:175]}"(wo[3][1]), "{v[176:183]}"(wo[3][2]), "{v[184:191]}"(wo[3][3]),
                 "{v[192:199]}"(wo[4][0]), "{v[200:207]}"(wo[4][1]), "{v[208:215]}"(wo[4][2]), "{v[216:223]}"(wo[4][3]),
                 "{v[224:231]}"(wo[5][0]), "{v[232:239]}"(wo[5][1]), "{v[240:247]}"(wo[5][2]), "{v[248:255]}"(wo[5][3]),
                 "{v[0:3]}"(pv), AW_SOPS);
  }
#undef AW_SOPS
}
__device__ __forceinline__ void role_aw(const MegaArgs& A, char* smem, const int L, const int a, const u64 tag) {
  if (A.wv < 4) role_aw_t<true>(A, smem, L, a, tag);
  else role_aw_t<false>(A, smem, L, a, tag);
}

#define P_ASM \
  "s_mov_b32 s68, 0\n" \
  "s_mov_b32 s66, 3\n" \
  "s_mov_b32 s67, 0\n" \
  "s_mov_b32 s72, 0\n" \
  "v_mov_b32 v7, 1\n" \
  "v_mov_b32 v6, s70\n" \
  "v_mov_b32 v5, 0\n" \
  "v_add_u32 v10, 4096, v2\n" \
  ".LCP_TOP_%=:\n" \
  "ds_read_b128 v[28:31], v1 offset:0\n" \
  "ds_read_b128 v[32:35], v1 offset:1024\n" \
  "s_add_u32 s66, s66, 3\n" \
  "s_add_u32 s67, s67, 1\n" \
  "s_mov_b32 s69, 0x400000\n" \
  "v_xor_b32 v0, 0x8000, v0\n" \
  "v_xor_b32 v3, 0x100, v3\n" \
  "v_readfirstlane_b32 s71, v5\n" \
  "s_cmp_ge_u32 s71, s66\n" \
  "s_cbranch_scc1 .LCP_GO_%=\n" \
  "s_cmp_eq_u32 s68, 1\n" \
  "s_cbranch_scc0 .LCP_X1_%=\n" \
  "s_waitcnt vmcnt(0)\n" \
  "s_mov_b64 exec, 1\n" \
  "buffer_store_dword v7, off, s[56:59], s72 sc1\n" \
  "s_mov_b64 exec, -1\n" \
  "s_add_u32 s72, s72, 0x1000\n" \
  "s_mov_b32 s68, 0\n" \
  ".LCP_X1_%=:\n" \
  ".LCP_S2_%=:\n" \
  "s_sleep 1\n" \
  "ds_read_b32 v5, v6 offset:0\n" \
  "s_sub_u32 s69, s69, 1\n" \
  "s_cbranch_scc1 .LCP_END_%=\n" \
  "s_waitcnt lgkmcnt(0)\n" \
  "v_readfirstlane_b32 s71, v5\n" \
  "s_cmp_ge_u32 s71, s66\n" \
  "s_cbranch_scc1 .LCP_GO_%=\n" \
  "s_branch .LCP_S2_%=\n" \
  ".LCP_GO_%=:\n" \
  "ds_read_b128 v[12:15], v0 offset:0\n" \
  "ds_read_b128 v[16:19], v0 offset:2048\n" \
  "ds_read_b128 v[20:23], v0 offset:4096\n" \
  "ds_read_b128 v[24:27], v0 offset:6144\n" \
  "ds_read_b128 v[36:39], v1 offset:2048\n" \
  "s_waitcnt lgkmcnt(4)\n" \
  "v_mfma_f32_16x16x32_bf16 v[48:51], v[64:67], v[12:15], 0\n" \
  "v_mfma_f32_16x16x32_bf16 v[52:55], v[128:131], v[12:15], 0\n" \
  "v_mfma_f32_16x16x32_bf16 v[56:59], v[192:195], v[12:15], 0\n" \
  "v_mfma_f32_16x16x32_bf16 v[60:63], v[28:31], v[12:15], 0\n" \
  "ds_read_b128 v[12:15], v0 offset:8192\n" \
  "ds_read_b128 v[28:31], v1 offset:3072\n" \
  "s_waitcnt lgkmcnt(5)\n" \
  "v_mfma_f32_16x16x32_bf16 v[48:51], v[68:71], v[16:19], v[48:51]\n" \
  "v_mfma_f32_16x16x32_bf16 v[52:55], v[132:135], v[16:19], v[52:55]\n" \
  "v_mfma_f32_16x16x32_bf16 v[56:59], v[196:199], v[16:19], v[56:59]\n" \
  "v_mfma_f32_16x16x32_bf16 v[60:63], v[32:35], v[16:19], v[60:63]\n" \
  "ds_read_b128 v[16:19], v0 offset:10240\n" \
  "ds_read_b128 v[32:35], v1 offset:4096\n" \
  "s_waitcnt lgkmcnt(6)\n" \
  "v_mfma_f32_16x16x32_bf16 v[48:51], v[72:75], v[20:23], v[48:51]\n" \
  "v_mfma_f32_16x16x32_bf16 v[52:55], v[136:139], v[20:23], v[52:55]\n" \
  "v_mfma_f32_16x16x32_bf16 v[56:59], v[200:203], v[20:23], v[56:59]\n" \
  "s_waitcnt lgkmcnt(4)\n" \
  "v_mfma_f32_16x16x32_bf16 v[60:63], v[36:39], v[20:23], v[60:63]\n" \
  "ds_read_b128 v[20:23], v0 offset:12288\n" \
  "ds_read_b128 v[36:39], v1 offset:5120\n" \
  "v_mfma_f32_16x16x32_bf16 v[48:51], v[76:79], v[24:27], v[48:51]\n" \
  "v_mfma_f32_16x16x32_bf16 v[52:55], v[140:143], v[24:27], v[52:55]\n" \
  "v_mfma_f32_16x16x32_bf16 v[56:59], v[204:207], v[24:27], v[56:59]\n" \
  "s_waitcnt lgkmcnt(4)\n" \
  "v_mfma_f32_16x16x32_bf16 v[60:63], v[28:31], v[24:27], v[60:63]\n" \
  "ds_read_b128 v[24:27], v0 offset:14336\n" \
  "ds_read_b128 v[28:31], v1 offset:6144\n" \
  "v_mfma_f32_16x16x32_bf16 v[48:51], v[80:83], v[12:15], v[48:51]\n" \
  "v_mfma_f32_16x16x32_bf16 v[52:55], v[144:147], v[12:15], v[52:55]\n" \
  "v_mfma_f32_16x16x32_bf16 v[56:59], v[208:211], v[12:15], v[56:59]\n" \
  "s_waitcnt lgkmcnt(4)\n" \
  "v_mfma_f32_16x16x32_bf16 v[60:63], v[32:35], v[12:15], v[60:63]\n" \
  "ds_read_b128 v[12:15], v0 offset:16384\n" \
  "ds_read_b128 v[32:35], v1 offset:7168\n" \
  "v_mfma_f32_16x16x32_bf16 v[48:51], v[84:87], v[16:19], v[48:51]\n" \
  "v_mfma_f32_16x16x32_bf16 v[52:55], v[148:151], v[16:19], v[52:55]\n" \
  "v_mfma_f32_16x16x32_bf16 v[56:59], v[212:215], v[16:19], v[56:59]\n" \
  "s_waitcnt lgkmcnt(4)\n" \
  "v_mfma_f32_16x16x32_bf16 v[60:63], v[36:39], v[16:19], v[60:63]\n" \
  "ds_read_b128 v[16:19], v0 offset:18432\n" \
  "ds_read_b128 v[36:39], v1 offset:8192\n" \
  "v_mfma_f32_16x16x32_bf16 v[48:51], v[88:91], v[20:23], v[48:51]\n" \
  "v_mfma_f32_16x16x32_bf16 v[52:55], v[152:155], v[20:23], v[52:55]\n" \
  "v_mfma_f32_16x16x32_bf16 v[56:59], v[216:219], v[20:23], v[56:59]\n" \
  "s_waitcnt lgkmcnt(4)\n" \
  "v_mfma_f32_16x16x32_bf16 v[60:63], v[28:31], v[20:23], v[60:63]\n" \
  "ds_read_b128 v[20:23], v0 offset:20480\n" \
  "ds_read_b128 v[28:31], v1 offset:9216\n" \
  "v_mfma_f32_16x16x32_bf16 v[48:51], v[92:95], v[24:27], v[48:51]\n" \
  "v_mfma_f32_16x16x32_bf16 v[52:55], v[156:159], v[24:27], v[52:55]\n" \
  "v_mfma_f32_16x16x32_bf16 v[56:59], v[220:223], v[24:27], v[56:59]\n" \
  "s_waitcnt lgkmcnt(4)\n" \
  "v_mfma_f32_16x16x32_bf16 v[60:63], v[32:35], v[24:27], v[60:63]\n" \
  "ds_read_b128 v[24:27], v0 offset:22528\n" \
  "ds_read_b128 v[32:35], v1 offset:10240\n" \
  "v_mfma_f32_16x16x32_bf16 v[48:51], v[96:99], v[12:15], v[48:51]\n" \
  "v_mfma_f32_16x16x32_bf16 v[52:55], v[160:163], v[12:15], v[52:55]\n" \
  "v_mfma_f32_16x16x32_bf16 v[56:59], v[224:227], v[12:15], v[56:59]\n" \
  "s_waitcnt lgkmcnt(4)\n" \
  "v_mfma_f32_16x16x32_bf16 v[60:63], v[36:39], v[12:15], v[60:63]\n" \
  "ds_read_b128 v[12:15], v0 offset:24576\n" \
  "ds_read_b128 v[36:39], v1 offset:11264\n" \
  "v_mfma_f32_16x16x32_bf16 v[48:51], v[100:103], v[16:19], v[48:51]\n" \
  "v_mfma_f32_16x16x32_bf16 v[52:55], v[164:167], v[16:19], v[52:55]\n" \
  "v_mfma_f32_16x16x32_bf16 v[56:59], v[228:231], v[16:19], v[56:59]\n" \
  "s_waitcnt lgkmcnt(4)\n" \
  "v_mfma_f32_16x16x32_bf16 v[60:63], v[28:31], v[16:19], v[60:63]\n" \
  "ds_read_b128 v[16:19], v0 offset:26624\n" \
  "ds_read_b128 v[28:31], v1 offset:12288\n" \
  "v_mfma_f32_16x16x32_bf16 v[48:51], v[104:107], v[20:23], v[48:51]\n" \
  "v_mfma_f32_16x16x32_bf16 v[52:55], v[168:171], v[20:23], v[52:55]\n" \
  "v_mfma_f32_16x16x32_bf16 v[56:59], v[232:235], v[20:23], v[56:59]\n" \
  "s_waitcnt lgkmcnt(4)\n" \
  "v_mfma_f32_16x16x32_bf16 v[60:63], v[32:35], v[20:23], v[60:63]\n" \
  "ds_read_b128 v[20:23], v0 offset:28672\n" \
  "ds_read_b128 v[32:35], v1 offset:13312\n" \
  "v_mfma_f32_16x16x32_bf16 v[48:51], v[108:111], v[24:27], v[48:51]\n" \
  "v_mfma_f32_16x16x32_bf16 v[52:55], v[172:175], v[24:27], v[52:55]\n" \
  "v_mfma_f32_16x16x32_bf16 v[56:59], v[236:239], v[24:27], v[56:59]\n" \
  "s_waitcnt lgkmcnt(4)\n" \
  "v_mfma_f32_16x16x32_bf16 v[60:63], v[36:39], v[24:27], v[60:63]\n" \
  "s_cmp_eq_u32 s68, 1\n" \
  "s_cbranch_scc0 .LCP_X3_%=\n" \
  "s_waitcnt vmcnt(0)\n" \
  "s_mov_b64 exec, 1\n" \
  "buffer_store_dword v7, off, s[56:59], s72 sc1\n" \
  "s_mov_b64 exec, -1\n" \
  "s_add_u32 s72, s72, 0x1000\n" \
  "s_mov_b32 s68, 0\n" \
  ".LCP_X3_%=:\n" \
  "ds_read_b32 v5, v6 offset:16\n" \
  "ds_read_b128 v[24:27], v0 offset:30720\n" \
  "ds_read_b128 v[36:39], v1 offset:14336\n" \
  "v_mfma_f32_16x16x32_bf16 v[48:51], v[112:115], v[12:15], v[48:51]\n" \
  "v_mfma_f32_16x16x32_bf16 v[52:55], v[176:179], v[12:15], v[52:55]\n" \
  "v_mfma_f32_16x16x32_bf16 v[56:59], v[240:243], v[12:15], v[56:59]\n" \
  "s_waitcnt lgkmcnt(5)\n" \
  "v_mfma_f32_16x16x32_bf16 v[60:63], v[28:31], v[12:15], v[60:63]\n" \
  "ds_read_b128 v[12:15], v0 offset:256\n" \
  "ds_read_b128 v[28:31], v1 offset:15360\n" \
  "ds_read_b128 v[40:43], v4 offset:0\n" \
  "ds_read_b128 v[44:47], v4 offset:64\n" \
  "v_mfma_f32_16x16x32_bf16 v[48:51], v[116:119], v[16:19], v[48:51]\n" \
  "v_mfma_f32_16x16x32_bf16 v[52:55], v[180:183], v[16:19], v[52:55]\n" \
  "v_mfma_f32_16x16x32_bf16 v[56:59], v[244:247], v[16:19], v[56:59]\n" \
  "s_waitcnt lgkmcnt(7)\n" \
  "v_mfma_f32_16x16x32_bf16 v[60:63], v[32:35], v[16:19], v[60:63]\n" \
  "s_waitcnt lgkmcnt(6)\n" \
  "v_readfirstlane_b32 s71, v5\n" \
  "s_cmp_ge_u32 s71, s67\n" \
  "s_cbranch_scc1 .LCP_ST_%=\n" \
  ".LCP_S4_%=:\n" \
  "s_sleep 1\n" \
  "ds_read_b32 v5, v6 offset:16\n" \
  "s_sub_u32 s69, s69, 1\n" \
  "s_cbranch_scc1 .LCP_END_%=\n" \
  "s_waitcnt lgkmcnt(0)\n" \
  "v_readfirstlane_b32 s71, v5\n" \
  "s_cmp_ge_u32 s71, s67\n" \
  "s_cbranch_scc1 .LCP_ST_%=\n" \
  "s_branch .LCP_S4_%=\n" \
  ".LCP_ST_%=:\n" \
  "ds_read_b128 v[16:19], v0 offset:2304\n" \
  "ds_read_b128 v[32:35], v4 offset:128\n" \
  "ds_read2_b32 v[8:9], v3 offset0:0 offset1:32\n" \
  "v_mfma_f32_16x16x32_bf16 v[48:51], v[120:123], v[20:23], v[48:51]\n" \
  "v_mfma_f32_16x16x32_bf16 v[52:55], v[184:187], v[20:23], v[52:55]\n" \
  "v_mfma_f32_16x16x32_bf16 v[56:59], v[248:251], v[20:23], v[56:59]\n" \
  "s_waitcnt lgkmcnt(7)\n" \
  "v_mfma_f32_16x16x32_bf16 v[60:63], v[36:39], v[20:23], v[60:63]\n" \
  "ds_read_b128 v[20:23], v0 offset:4352\n" \
  "ds_read_b128 v[36:39], v4 offset:192\n" \
  "v_mfma_f32_16x16x32_bf16 v[48:51], v[124:127], v[24:27], v[48:51]\n" \
  "v_mfma_f32_16x16x32_bf16 v[52:55], v[188:191], v[24:27], v[52:55]\n" \
  "v_mfma_f32_16x16x32_bf16 v[56:59], v[252:255], v[24:27], v[56:59]\n" \
  "s_waitcnt lgkmcnt(7)\n" \
  "v_mfma_f32_16x16x32_bf16 v[60:63], v[28:31], v[24:27], v[60:63]\n" \
  "s_waitcnt lgkmcnt(0)\n" \
  "s_nop 4\n" \
  "v_fma_f32 v48, -v8, v40, v48\n" \
  "v_fma_f32 v49, -v8, v41, v49\n" \
  "v_fma_f32 v50, -v8, v42, v50\n" \
  "v_fma_f32 v51, -v8, v43, v51\n" \
  "v_fma_f32 v52, -v8, v44, v52\n" \
  "v_fma_f32 v53, -v8, v45, v53\n" \
  "v_fma_f32 v54, -v8, v46, v54\n" \
  "v_fma_f32 v55, -v8, v47, v55\n" \
  "v_fma_f32 v56, -v8, v32, v56\n" \
  "v_fma_f32 v57, -v8, v33, v57\n" \
  "v_fma_f32 v58, -v8, v34, v58\n" \
  "v_fma_f32 v59, -v8, v35, v59\n" \
  "v_fma_f32 v60, -v8, v36, v60\n" \
  "v_fma_f32 v61, -v8, v37, v61\n" \
  "v_fma_f32 v62, -v8, v38, v62\n" \
  "v_fma_f32 v63, -v8, v39, v63\n" \
  "ds_read_b128 v[32:35], v1 offset:0\n" \
  "ds_read_b128 v[36:39], v1 offset:1024\n" \
  "v_mul_f32 v48, v48, v9\n" \
  "v_mul_f32 v49, v49, v9\n" \
  "v_mul_f32 v50, v50, v9\n" \
  "v_mul_f32 v51, v51, v9\n" \
  "v_mul_f32 v52, v52, v9\n" \
  "v_mul_f32 v53, v53, v9\n" \
  "v_mul_f32 v54, v54, v9\n" \
  "v_mul_f32 v55, v55, v9\n" \
  "v_cvt_pk_bf16_f32 v40, v48, v49\n" \
  "v_cvt_pk_bf16_f32 v41, v50, v51\n" \
  "v_cvt_pk_bf16_f32 v42, v52, v53\n" \
  "v_cvt_pk_bf16_f32 v43, v54, v55\n" \
  "buffer_store_dwordx4 v[40:43], v2, s[40:43], s65 offen offset:0 sc1\n" \
  "v_mul_f32 v56, v56, v9\n" \
  "v_mul_f32 v57, v57, v9\n" \
  "v_mul_f32 v58, v58, v9\n" \
  "v_mul_f32 v59, v59, v9\n" \
  "v_mul_f32 v60, v60, v9\n" \
  "v_mul_f32 v61, v61, v9\n" \
  "v_mul_f32 v62, v62, v9\n" \
  "v_mul_f32 v63, v63, v9\n" \
  "v_cvt_pk_bf16_f32 v44, v56, v57\n" \
  "v_cvt_pk_bf16_f32 v45, v58, v59\n" \
  "v_cvt_pk_bf16_f32 v46, v60, v61\n" \
  "v_cvt_pk_bf16_f32 v47, v62, v63\n" \
  "buffer_store_dwordx4 v[44:47], v2, s[40:43], s65 offen offset:1024 sc1\n" \
  "ds_read_b128 v[24:27], v0 offset:6400\n" \
  "ds_read_b128 v[28:31], v1 offset:2048\n" \
  "v_mfma_f32_16x16x32_bf16 v[48:51], v[64:67], v[12:15], 0\n" \
  "v_mfma_f32_16x16x32_bf16 v[52:55], v[128:131], v[12:15], 0\n" \
  "v_mfma_f32_16x16x32_bf16 v[56:59], v[192:195], v[12:15], 0\n" \
  "s_waitcnt lgkmcnt(3)\n" \
  "v_mfma_f32_16x16x32_bf16 v[60:63], v[32:35], v[12:15], 0\n" \
  "ds_read_b128 v[12:15], v0 offset:8448\n" \
  "ds_read_b128 v[32:35], v1 offset:3072\n" \
  "v_mfma_f32_16x16x32_bf16 v[48:51], v[68:71], v[16:19], v[48:51]\n" \
  "v_mfma_f32_16x16x32_bf16 v[52:55], v[132:135], v[16:19], v[52:55]\n" \
  "v_mfma_f32_16x16x32_bf16 v[56:59], v[196:199], v[16:19], v[56:59]\n" \
  "s_waitcnt lgkmcnt(4)\n" \
  "v_mfma_f32_16x16x32_bf16 v[60:63], v[36:39], v[16:19], v[60:63]\n" \
  "ds_read_b128 v[16:19], v0 offset:10496\n" \
  "ds_read_b128 v[36:39], v1 offset:4096\n" \
  "v_mfma_f32_16x16x32_bf16 v[48:51], v[72:75], v[20:23], v[48:51]\n" \
  "v_mfma_f32_16x16x32_bf16 v[52:55], v[136:139], v[20:23], v[52:55]\n" \
  "v_mfma_f32_16x16x32_bf16 v[56:59], v[200:203], v[20:23], v[56:59]\n" \
  "s_waitcnt lgkmcnt(4)\n" \
  "v_mfma_f32_16x16x32_bf16 v[60:63], v[28:31], v[20:23], v[60:63]\n" \
  "ds_read_b128 v[20:23], v0 offset:12544\n" \
  "ds_read_b128 v[28:31], v1 offset:5120\n" \
  "v_mfma_f32_16x16x32_bf16 v[48:51], v[76:79], v[24:27], v[48:51]\n" \
  "v_mfma_f32_16x16x32_bf16 v[52:55], v[140:143], v[24:27], v[52:55]\n" \
  "v_mfma_f32_16x16x32_bf16 v[56:59], v[204:207], v[24:27], v[56:59]\n" \
  "s_waitcnt lgkmcnt(4)\n" \
  "v_mfma_f32_16x16x32_bf16 v[60:63], v[32:35], v[24:27], v[60:63]\n" \
  "ds_read_b128 v[24:27], v0 offset:14592\n" \
  "ds_read_b128 v[32:35], v1 offset:6144\n" \
  "v_mfma_f32_16x16x32_bf16 v[48:51], v[80:83], v[12:15], v[48:51]\n" \
  "v_mfma_f32_16x16x32_bf16 v[52:55], v[144:147], v[12:15], v[52:55]\n" \
  "v_mfma_f32_16x16x32_bf16 v[56:59], v[208:211], v[12:15], v[56:59]\n" \
  "s_waitcnt lgkmcnt(4)\n" \
  "v_mfma_f32_16x16x32_bf16 v[60:63], v[36:39], v[12:15], v[60:63]\n" \
  "ds_read_b128 v[12:15], v0 offset:16640\n" \
  "ds_read_b128 v[36:39], v1 offset:7168\n" \
  "v_mfma_f32_16x16x32_bf16 v[48:51], v[84:87], v[16:19], v[48:51]\n" \
  "v_mfma_f32_16x16x32_bf16 v[52:55], v[148:151], v[16:19], v[52:55]\n" \
  "v_mfma_f32_16x16x32_bf16 v[56:59], v[212:215], v[16:19], v[56:59]\n" \
  "s_waitcnt lgkmcnt(4)\n" \
  "v_mfma_f32_16x16x32_bf16 v[60:63], v[28:31], v[16:19], v[60:63]\n" \
  "ds_read_b128 v[16:19], v0 offset:18688\n" \
  "ds_read_b128 v[28:31], v1 offset:8192\n" \
  "v_mfma_f32_16x16x32_bf16 v[48:51], v[88:91], v[20:23], v[48:51]\n" \
  "v_mfma_f32_16x16x32_bf16 v[52:55], v[152:155], v[20:23], v[52:55]\n" \
  "v_mfma_f32_16x16x32_bf16 v[56:59], v[216:219], v[20:23], v[56:59]\n" \
  "s_waitcnt lgkmcnt(4)\n" \
  "v_mfma_f32_16x16x32_bf16 v[60:63], v[32:35], v[20:23], v[60:63]\n" \
  "ds_read_b128 v[20:23], v0 offset:20736\n" \
  "ds_read_b128 v[32:35], v1 offset:9216\n" \
  "v_mfma_f32_16x16x32_bf16 v[48:51], v[92:95], v[24:27], v[48:51]\n" \
  "v_mfma_f32_16x16x32_bf16 v[52:55], v[156:159], v[24:27], v[52:55]\n" \
  "v_mfma_f32_16x16x32_bf16 v[56:59], v[220:223], v[24:27], v[56:59]\n" \
  "s_waitcnt lgkmcnt(4)\n" \
  "v_mfma_f32_16x16x32_bf16 v[60:63], v[36:39], v[24:27], v[60:63]\n" \
  "ds_read_b128 v[24:27], v0 offset:22784\n" \
  "ds_read_b128 v[36:39], v1 offset:10240\n" \
  "v_mfma_f32_16x16x32_bf16 v[48:51], v[96:99], v[12:15], v[48:51]\n" \
  "v_mfma_f32_16x16x32_bf16 v[52:55], v[160:163], v[12:15], v[52:55]\n" \
  "v_mfma_f32_16x16x32_bf16 v[56:59], v[224:227], v[12:15], v[56:59]\n" \
  "s_waitcnt lgkmcnt(4)\n" \
  "v_mfma_f32_16x16x32_bf16 v[60:63], v[28:31], v[12:15], v[60:63]\n" \
  "ds_read_b128 v[12:15], v0 offset:24832\n" \
  "ds_read_b128 v[28:31], v1 offset:11264\n" \
  "v_mfma_f32_16x16x32_bf16 v[48:51], v[100:103], v[16:19], v[48:51]\n" \
  "v_mfma_f32_16x16x32_bf16 v[52:55], v[164:167], v[16:19], v[52:55]\n" \
  "v_mfma_f32_16x16x32_bf16 v[56:59], v[228:231], v[16:19], v[56:59]\n" \
  "s_waitcnt lgkmcnt(4)\n" \
  "v_mfma_f32_16x16x32_bf16 v[60:63], v[32:35], v[16:19], v[60:63]\n" \
  "ds_read_b128 v[16:19], v0 offset:26880\n" \
  "ds_read_b128 v[32:35], v1 offset:12288\n" \
  "v_mfma_f32_16x16x32_bf16 v[48:51], v[104:107], v[20:23], v[48:51]\n" \
  "v_mfma_f32_16x16x32_bf16 v[52:55], v[168:171], v[20:23], v[52:55]\n" \
  "v_mfma_f32_16x16x32_bf16 v[56:59], v[232:235], v[20:23], v[56:59]\n" \
  "s_waitcnt lgkmcnt(4)\n" \
  "v_mfma_f32_16x16x32_bf16 v[60:63], v[36:39], v[20:23], v[60:63]\n" \
  "ds_read_b128 v[20:23], v0 offset:28928\n" \
  "ds_read_b128 v[36:39], v1 offset:13312\n" \
  "v_mfma_f32_16x16x32_bf16 v[48:51], v[108:111], v[24:27], v[48:51]\n" \
  "v_mfma_f32_16x16x32_bf16 v[52:55], v[172:175], v[24:27], v[52:55]\n" \
  "v_mfma_f32_16x16x32_bf16 v[56:59], v[236:239], v[24:27], v[56:59]\n" \
  "s_waitcnt lgkmcnt(4)\n" \
  "v_mfma_f32_16x16x32_bf16 v[60:63], v[28:31], v[24:27], v[60:63]\n" \
  "ds_read_b128 v[24:27], v0 offset:30976\n" \
  "ds_read_b128 v[28:31], v1 offset:14336\n" \
  "v_mfma_f32_16x16x32_bf16 v[48:51], v[112:115], v[12:15], v[48:51]\n" \
  "v_mfma_f32_16x16x32_bf16 v[52:55], v[176:179], v[12:15], v[52:55]\n" \
  "v_mfma_f32_16x16x32_bf16 v[56:59], v[240:243], v[12:15], v[56:59]\n" \
  "s_waitcnt lgkmcnt(4)\n" \
  "v_mfma_f32_16x16x32_bf16 v[60:63], v[32:35], v[12:15], v[60:63]\n" \
  "ds_read_b128 v[32:35], v1 offset:15360\n" \
  "ds_read_b128 v[40:43], v4 offset:0\n" \
  "ds_read_b128 v[44:47], v4 offset:64\n" \
  "v_mfma_f32_16x16x32_bf16 v[48:51], v[116:119], v[16:19], v[48:51]\n" \
  "v_mfma_f32_16x16x32_bf16 v[52:55], v[180:183], v[16:19], v[52:55]\n" \
  "v_mfma_f32_16x16x32_bf16 v[56:59], v[244:247], v[16:19], v[56:59]\n" \
  "s_waitcnt lgkmcnt(5)\n" \
  "v_mfma_f32_16x16x32_bf16 v[60:63], v[36:39], v[16:19], v[60:63]\n" \
  "ds_read_b128 v[36:39], v4 offset:128\n" \
  "ds_read2_b32 v[8:9], v3 offset0:16 offset1:48\n" \
  "v_mfma_f32_16x16x32_bf16 v[48:51], v[120:123], v[20:23], v[48:51]\n" \
  "v_mfma_f32_16x16x32_bf16 v[52:55], v[184:187], v[20:23], v[52:55]\n" \
  "v_mfma_f32_16x16x32_bf16 v[56:59], v[248:251], v[20:23], v[56:59]\n" \
  "s_waitcnt lgkmcnt(5)\n" \
  "v_mfma_f32_16x16x32_bf16 v[60:63], v[28:31], v[20:23], v[60:63]\n" \
  "ds_read_b128 v[28:31], v4 offset:192\n" \
  "s_mov_b64 exec, 1\n" \
  "ds_add_u32 v6, v7 offset:8\n" \
  "s_mov_b64 exec, -1\n" \
  "v_xor_b32 v6, 4, v6\n" \
  "v_mfma_f32_16x16x32_bf16 v[48:51], v[124:127], v[24:27], v[48:51]\n" \
  "v_mfma_f32_16x16x32_bf16 v[52:55], v[188:191], v[24:27], v[52:55]\n" \
  "v_mfma_f32_16x16x32_bf16 v[56:59], v[252:255], v[24:27], v[56:59]\n" \
  "s_waitcnt lgkmcnt(6)\n" \
  "v_mfma_f32_16x16x32_bf16 v[60:63], v[32:35], v[24:27], v[60:63]\n" \
  "s_waitcnt lgkmcnt(1)\n" \
  "s_nop 4\n" \
  "ds_read_b32 v5, v6 offset:0\n" \
  "v_fma_f32 v48, -v8, v40, v48\n" \
  "v_fma_f32 v49, -v8, v41, v49\n" \
  "v_fma_f32 v50, -v8, v42, v50\n" \
  "v_fma_f32 v51, -v8, v43, v51\n" \
  "v_fma_f32 v52, -v8, v44, v52\n" \
  "v_fma_f32 v53, -v8, v45, v53\n" \
  "v_fma_f32 v54, -v8, v46, v54\n" \
  "v_fma_f32 v55, -v8, v47, v55\n" \
  "v_fma_f32 v56, -v8, v36, v56\n" \
  "v_fma_f32 v57, -v8, v37, v57\n" \
  "v_fma_f32 v58, -v8, v38, v58\n" \
  "v_fma_f32 v59, -v8, v39, v59\n" \
  "v_fma_f32 v60, -v8, v28, v60\n" \
  "v_fma_f32 v61, -v8, v29, v61\n" \
  "v_fma_f32 v62, -v8, v30, v62\n" \
  "v_fma_f32 v63, -v8, v31, v63\n" \
  "v_mul_f32 v48, v48, v9\n" \
  "v_mul_f32 v49, v49, v9\n" \
  "v_mul_f32 v50, v50, v9\n" \
  "v_mul_f32 v51, v51, v9\n" \
  "v_mul_f32 v52, v52, v9\n" \
  "v_mul_f32 v53, v53, v9\n" \
  "v_mul_f32 v54, v54, v9\n" \
  "v_mul_f32 v55, v55, v9\n" \
  "v_cvt_pk_bf16_f32 v40, v48, v49\n" \
  "v_cvt_pk_bf16_f32 v41, v50, v51\n" \
  "v_cvt_pk_bf16_f32 v42, v52, v53\n" \
  "v_cvt_pk_bf16_f32 v43, v54, v55\n" \
  "buffer_store_dwordx4 v[40:43], v10, s[40:43], s65 offen offset:0 sc1\n" \
  "v_mul_f32 v56, v56, v9\n" \
  "v_mul_f32 v57, v57, v9\n" \
  "v_mul_f32 v58, v58, v9\n" \
  "v_mul_f32 v59, v59, v9\n" \
  "v_mul_f32 v60, v60, v9\n" \
  "v_mul_f32 v61, v61, v9\n" \
  "v_mul_f32 v62, v62, v9\n" \
  "v_mul_f32 v63, v63, v9\n" \
  "v_cvt_pk_bf16_f32 v44, v56, v57\n" \
  "v_cvt_pk_bf16_f32 v45, v58, v59\n" \
  "v_cvt_pk_bf16_f32 v46, v60, v61\n" \
  "v_cvt_pk_bf16_f32 v47, v62, v63\n" \
  "buffer_store_dwordx4 v[44:47], v10, s[40:43], s65 offen offset:1024 sc1\n" \
  "s_waitcnt lgkmcnt(0)\n" \
  "s_add_u32 s65, s65, 0x80000\n" \
  "s_sub_u32 s64, s64, 1\n" \
  "s_mov_b32 s68, 1\n" \
  "s_cmp_lg_u32 s64, 0\n" \
  "s_cbranch_scc1 .LCP_TOP_%=\n" \
  "s_waitcnt vmcnt(0)\n" \
  "s_mov_b64 exec, 1\n" \
  "buffer_store_dword v7, off, s[56:59], s72 sc1\n" \
  "s_mov_b64 exec, -1\n" \
  "s_add_u32 s72, s72, 0x1000\n" \
  "s_mov_b32 s68, 0\n" \
  ".LCP_END_%=:\n" \
  "s_endpgm\n"
#define V_ASM \
  "s_mov_b32 s68, 0\n" \
  "s_mov_b32 s66, 3\n" \
  "s_mov_b32 s67, 0\n" \
  "s_mov_b32 s72, 0\n" \
  "v_mov_b32 v7, 1\n" \
  "v_mov_b32 v6, s70\n" \
  "v_mov_b32 v5, 0\n" \
  "ds_read_b32 v8, v4 offset:0\n" \
  "ds_read_b32 v9, v4 offset:64\n" \
  "ds_read_b32 v10, v4 offset:128\n" \
  "ds_read_b32 v4, v4 offset:192\n" \
  "s_waitcnt lgkmcnt(0)\n" \
  ".LCW_TOP_%=:\n" \
  "ds_read_b128 v[28:31], v1 offset:0\n" \
  "ds_read_b128 v[32:35], v1 offset:1024\n" \
  "s_add_u32 s66, s66, 3\n" \
  "s_add_u32 s67, s67, 1\n" \
  "s_mov_b32 s69, 0x400000\n" \
  "v_xor_b32 v0, 0x8000, v0\n" \
  "v_xor_b32 v3, 0x100, v3\n" \
  "v_readfirstlane_b32 s71, v5\n" \
  "s_cmp_ge_u32 s71, s66\n" \
  "s_cbranch_scc1 .LCW_GO_%=\n" \
  "s_cmp_eq_u32 s68, 1\n" \
  "s_cbranch_scc0 .LCW_X1_%=\n" \
  "s_waitcnt vmcnt(0)\n" \
  "s_mov_b64 exec, 1\n" \
  "buffer_store_dword v7, off, s[56:59], s72 sc1\n" \
  "s_mov_b64 exec, -1\n" \
  "s_add_u32 s72, s72, 0x1000\n" \
  "s_mov_b32 s68, 0\n" \
  ".LCW_X1_%=:\n" \
  ".LCW_S2_%=:\n" \
  "s_sleep 1\n" \
  "ds_read_b32 v5, v6 offset:0\n" \
  "s_sub_u32 s69, s69, 1\n" \
  "s_cbranch_scc1 .LCW_END_%=\n" \
  "s_waitcnt lgkmcnt(0)\n" \
  "v_readfirstlane_b32 s71, v5\n" \
  "s_cmp_ge_u32 s71, s66\n" \
  "s_cbranch_scc1 .LCW_GO_%=\n" \
  "s_branch .LCW_S2_%=\n" \
  ".LCW_GO_%=:\n" \
  "ds_read_b128 v[12:15], v0 offset:0\n" \
  "ds_read_b128 v[16:19], v0 offset:2048\n" \
  "ds_read_b128 v[20:23], v0 offset:4096\n" \
  "ds_read_b128 v[24:27], v0 offset:6144\n" \
  "ds_read_b128 v[36:39], v1 offset:2048\n" \
  "s_waitcnt lgkmcnt(4)\n" \
  "v_mfma_f32_16x16x32_bf16 v[42:45], v[12:15], v[64:67], 0\n" \
  "v_mfma_f32_16x16x32_bf16 v[48:51], v[12:15], v[128:131], 0\n" \
  "v_mfma_f32_16x16x32_bf16 v[54:57], v[12:15], v[192:195], 0\n" \
  "v_mfma_f32_16x16x32_bf16 v[60:63], v[12:15], v[28:31], 0\n" \
  "ds_read_b128 v[12:15], v0 offset:8192\n" \
  "ds_read_b128 v[28:31], v1 offset:3072\n" \
  "s_waitcnt lgkmcnt(5)\n" \
  "v_mfma_f32_16x16x32_bf16 v[42:45], v[16:19], v[68:71], v[42:45]\n" \
  "v_mfma_f32_16x16x32_bf16 v[48:51], v[16:19], v[132:135], v[48:51]\n" \
  "v_mfma_f32_16x16x32_bf16 v[54:57], v[16:19], v[196:199], v[54:57]\n" \
  "v_mfma_f32_16x16x32_bf16 v[60:63], v[16:19], v[32:35], v[60:63]\n" \
  "ds_read_b128 v[16:19], v0 offset:10240\n" \
  "ds_read_b128 v[32:35], v1 offset:4096\n" \
  "s_waitcnt lgkmcnt(6)\n" \
  "v_mfma_f32_16x16x32_bf16 v[42:45], v[20:23], v[72:75], v[42:45]\n" \
  "v_mfma_f32_16x16x32_bf16 v[48:51], v[20:23], v[136:139], v[48:51]\n" \
  "v_mfma_f32_16x16x32_bf16 v[54:57], v[20:23], v[200:203], v[54:57]\n" \
  "s_waitcnt lgkmcnt(4)\n" \
  "v_mfma_f32_16x16x32_bf16 v[60:63], v[20:23], v[36:39], v[60:63]\n" \
  "ds_read_b128 v[20:23], v0 offset:12288\n" \
  "ds_read_b128 v[36:39], v1 offset:5120\n" \
  "v_mfma_f32_16x16x32_bf16 v[42:45], v[24:27], v[76:79], v[42:45]\n" \
  "v_mfma_f32_16x16x32_bf16 v[48:51], v[24:27], v[140:143], v[48:51]\n" \
  "v_mfma_f32_16x16x32_bf16 v[54:57], v[24:27], v[204:207], v[54:57]\n" \
  "s_waitcnt lgkmcnt(4)\n" \
  "v_mfma_f32_16x16x32_bf16 v[60:63], v[24:27], v[28:31], v[60:63]\n" \
  "ds_read_b128 v[24:27], v0 offset:14336\n" \
  "ds_read_b128 v[28:31], v1 offset:6144\n" \
  "v_mfma_f32_16x16x32_bf16 v[42:45], v[12:15], v[80:83], v[42:45]\n" \
  "v_mfma_f32_16x16x32_bf16 v[48:51], v[12:15], v[144:147], v[48:51]\n" \
  "v_mfma_f32_16x16x32_bf16 v[54:57], v[12:15], v[208:211], v[54:57]\n" \
  "s_waitcnt lgkmcnt(4)\n" \
  "v_mfma_f32_16x16x32_bf16 v[60:63], v[12:15], v[32:35], v[60:63]\n" \
  "ds_read_b128 v[12:15], v0 offset:16384\n" \
  "ds_read_b128 v[32:35], v1 offset:7168\n" \
  "v_mfma_f32_16x16x32_bf16 v[42:45], v[16:19], v[84:87], v[42:45]\n" \
  "v_mfma_f32_16x16x32_bf16 v[48:51], v[16:19], v[148:151], v[48:51]\n" \
  "v_mfma_f32_16x16x32_bf16 v[54:57], v[16:19], v[212:215], v[54:57]\n" \
  "s_waitcnt lgkmcnt(4)\n" \
  "v_mfma_f32_16x16x32_bf16 v[60:63], v[16:19], v[36:39], v[60:63]\n" \
  "ds_read_b128 v[16:19], v0 offset:18432\n" \
  "ds_read_b128 v[36:39], v1 offset:8192\n" \
  "v_mfma_f32_16x16x32_bf16 v[42:45], v[20:23], v[88:91], v[42:45]\n" \
  "v_mfma_f32_16x16x32_bf16 v[48:51], v[20:23], v[152:155], v[48:51]\n" \
  "v_mfma_f32_16x16x32_bf16 v[54:57], v[20:23], v[216:219], v[54:57]\n" \
  "s_waitcnt lgkmcnt(4)\n" \
  "v_mfma_f32_16x16x32_bf16 v[60:63], v[20:23], v[28:31], v[60:63]\n" \
  "ds_read_b128 v[20:23], v0 offset:20480\n" \
  "ds_read_b128 v[28:31], v1 offset:9216\n" \
  "v_mfma_f32_16x16x32_bf16 v[42:45], v[24:27], v[92:95], v[42:45]\n" \
  "v_mfma_f32_16x16x32_bf16 v[48:51], v[24:27], v[156:159], v[48:51]\n" \
  "v_mfma_f32_16x16x32_bf16 v[54:57], v[24:27], v[220:223], v[54:57]\n" \
  "s_waitcnt lgkmcnt(4)\n" \
  "v_mfma_f32_16x16x32_bf16 v[60:63], v[24:27], v[32:35], v[60:63]\n" \
  "ds_read_b128 v[24:27], v0 offset:22528\n" \
  "ds_read_b128 v[32:35], v1 offset:10240\n" \
  "v_mfma_f32_16x16x32_bf16 v[42:45], v[12:15], v[96:99], v[42:45]\n" \
  "v_mfma_f32_16x16x32_bf16 v[48:51], v[12:15], v[160:163], v[48:51]\n" \
  "v_mfma_f32_16x16x32_bf16 v[54:57], v[12:15], v[224:227], v[54:57]\n" \
  "s_waitcnt lgkmcnt(4)\n" \
  "v_mfma_f32_16x16x32_bf16 v[60:63], v[12:15], v[36:39], v[60:63]\n" \
  "ds_read_b128 v[12:15], v0 offset:24576\n" \
  "ds_read_b128 v[36:39], v1 offset:11264\n" \
  "v_mfma_f32_16x16x32_bf16 v[42:45], v[16:19], v[100:103], v[42:45]\n" \
  "v_mfma_f32_16x16x32_bf16 v[48:51], v[16:19], v[164:167], v[48:51]\n" \
  "v_mfma_f32_16x16x32_bf16 v[54:57], v[16:19], v[228:231], v[54:57]\n" \
  "s_waitcnt lgkmcnt(4)\n" \
  "v_mfma_f32_16x16x32_bf16 v[60:63], v[16:19], v[28:31], v[60:63]\n" \
  "ds_read_b128 v[16:19], v0 offset:26624\n" \
  "ds_read_b128 v[28:31], v1 offset:12288\n" \
  "v_mfma_f32_16x16x32_bf16 v[42:45], v[20:23], v[104:107], v[42:45]\n" \
  "v_mfma_f32_16x16x32_bf16 v[48:51], v[20:23], v[168:171], v[48:51]\n" \
  "v_mfma_f32_16x16x32_bf16 v[54:57], v[20:23], v[232:235], v[54:57]\n" \
  "s_waitcnt lgkmcnt(4)\n" \
  "v_mfma_f32_16x16x32_bf16 v[60:63], v[20:23], v[32:35], v[60:63]\n" \
  "ds_read_b128 v[20:23], v0 offset:28672\n" \
  "ds_read_b128 v[32:35], v1 offset:13312\n" \
  "v_mfma_f32_16x16x32_bf16 v[42:45], v[24:27], v[108:111], v[42:45]\n" \
  "v_mfma_f32_16x16x32_bf16 v[48:51], v[24:27], v[172:175], v[48:51]\n" \
  "v_mfma_f32_16x16x32_bf16 v[54:57], v[24:27], v[236:239], v[54:57]\n" \
  "s_waitcnt lgkmcnt(4)\n" \
  "v_mfma_f32_16x16x32_bf16 v[60:63], v[24:27], v[36:39], v[60:63]\n" \
  "s_cmp_eq_u32 s68, 1\n" \
  "s_cbranch_scc0 .LCW_X3_%=\n" \
  "s_waitcnt vmcnt(0)\n" \
  "s_mov_b64 exec, 1\n" \
  "buffer_store_dword v7, off, s[56:59], s72 sc1\n" \
  "s_mov_b64 exec, -1\n" \
  "s_add_u32 s72, s72, 0x1000\n" \
  "s_mov_b32 s68, 0\n" \
  ".LCW_X3_%=:\n" \
  "ds_read_b32 v5, v6 offset:16\n" \
  "ds_read_b128 v[24:27], v0 offset:30720\n" \
  "ds_read_b128 v[36:39], v1 offset:14336\n" \
  "v_mfma_f32_16x16x32_bf16 v[42:45], v[12:15], v[112:115], v[42:45]\n" \
  "v_mfma_f32_16x16x32_bf16 v[48:51], v[12:15], v[176:179], v[48:51]\n" \
  "v_mfma_f32_16x16x32_bf16 v[54:57], v[12:15], v[240:243], v[54:57]\n" \
  "s_waitcnt lgkmcnt(5)\n" \
  "v_mfma_f32_16x16x32_bf16 v[60:63], v[12:15], v[28:31], v[60:63]\n" \
  "ds_read_b128 v[12:15], v0 offset:256\n" \
  "ds_read_b128 v[28:31], v1 offset:15360\n" \
  "v_mfma_f32_16x16x32_bf16 v[42:45], v[16:19], v[116:119], v[42:45]\n" \
  "v_mfma_f32_16x16x32_bf16 v[48:51], v[16:19], v[180:183], v[48:51]\n" \
  "v_mfma_f32_16x16x32_bf16 v[54:57], v[16:19], v[244:247], v[54:57]\n" \
  "s_waitcnt lgkmcnt(5)\n" \
  "v_mfma_f32_16x16x32_bf16 v[60:63], v[16:19], v[32:35], v[60:63]\n" \
  "s_waitcnt lgkmcnt(4)\n" \
  "v_readfirstlane_b32 s71, v5\n" \
  "s_cmp_ge_u32 s71, s67\n" \
  "s_cbranch_scc1 .LCW_ST_%=\n" \
  ".LCW_S4_%=:\n" \
  "s_sleep 1\n" \
  "ds_read_b32 v5, v6 offset:16\n" \
  "s_sub_u32 s69, s69, 1\n" \
  "s_cbranch_scc1 .LCW_END_%=\n" \
  "s_waitcnt lgkmcnt(0)\n" \
  "v_readfirstlane_b32 s71, v5\n" \
  "s_cmp_ge_u32 s71, s67\n" \
  "s_cbranch_scc1 .LCW_ST_%=\n" \
  "s_branch .LCW_S4_%=\n" \
  ".LCW_ST_%=:\n" \
  "ds_read_b128 v[16:19], v0 offset:2304\n" \
  "ds_read_b128 v[32:35], v3 offset:0\n" \
  "v_mfma_f32_16x16x32_bf16 v[42:45], v[20:23], v[120:123], v[42:45]\n" \
  "v_mfma_f32_16x16x32_bf16 v[48:51], v[20:23], v[184:187], v[48:51]\n" \
  "v_mfma_f32_16x16x32_bf16 v[54:57], v[20:23], v[248:251], v[54:57]\n" \
  "s_waitcnt lgkmcnt(4)\n" \
  "v_mfma_f32_16x16x32_bf16 v[60:63], v[20:23], v[36:39], v[60:63]\n" \
  "ds_read_b128 v[20:23], v0 offset:4352\n" \
  "ds_read_b128 v[36:39], v3 offset:128\n" \
  "v_mfma_f32_16x16x32_bf16 v[42:45], v[24:27], v[124:127], v[42:45]\n" \
  "v_mfma_f32_16x16x32_bf16 v[48:51], v[24:27], v[188:191], v[48:51]\n" \
  "v_mfma_f32_16x16x32_bf16 v[54:57], v[24:27], v[252:255], v[54:57]\n" \
  "s_waitcnt lgkmcnt(4)\n" \
  "v_mfma_f32_16x16x32_bf16 v[60:63], v[24:27], v[28:31], v[60:63]\n" \
  "s_waitcnt lgkmcnt(0)\n" \
  "s_nop 4\n" \
  "v_fma_f32 v42, -v32, v8, v42\n" \
  "v_fma_f32 v43, -v33, v8, v43\n" \
  "v_fma_f32 v44, -v34, v8, v44\n" \
  "v_fma_f32 v45, -v35, v8, v45\n" \
  "v_fma_f32 v48, -v32, v9, v48\n" \
  "v_fma_f32 v49, -v33, v9, v49\n" \
  "v_fma_f32 v50, -v34, v9, v50\n" \
  "v_fma_f32 v51, -v35, v9, v51\n" \
  "v_fma_f32 v54, -v32, v10, v54\n" \
  "v_fma_f32 v55, -v33, v10, v55\n" \
  "v_fma_f32 v56, -v34, v10, v56\n" \
  "v_fma_f32 v57, -v35, v10, v57\n" \
  "v_fma_f32 v60, -v32, v4, v60\n" \
  "v_fma_f32 v61, -v33, v4, v61\n" \
  "v_fma_f32 v62, -v34, v4, v62\n" \
  "v_fma_f32 v63, -v35, v4, v63\n" \
  "ds_read_b128 v[32:35], v1 offset:0\n" \
  "v_mul_f32 v42, v42, v36\n" \
  "v_mul_f32 v43, v43, v37\n" \
  "v_mul_f32 v44, v44, v38\n" \
  "v_mul_f32 v45, v45, v39\n" \
  "v_mul_f32 v48, v48, v36\n" \
  "v_mul_f32 v49, v49, v37\n" \
  "v_mul_f32 v50, v50, v38\n" \
  "v_mul_f32 v51, v51, v39\n" \
  "v_mul_f32 v54, v54, v36\n" \
  "v_mul_f32 v55, v55, v37\n" \
  "v_mul_f32 v56, v56, v38\n" \
  "v_mul_f32 v57, v57, v39\n" \
  "v_mul_f32 v60, v60, v36\n" \
  "v_mul_f32 v61, v61, v37\n" \
  "v_mul_f32 v62, v62, v38\n" \
  "v_mul_f32 v63, v63, v39\n" \
  "ds_read_b128 v[36:39], v1 offset:1024\n" \
  "v_cvt_pk_bf16_f32 v40, v42, v43\n" \
  "v_cvt_pk_bf16_f32 v41, v44, v45\n" \
  "v_cvt_pk_bf16_f32 v46, v48, v49\n" \
  "v_cvt_pk_bf16_f32 v47, v50, v51\n" \
  "v_cvt_pk_bf16_f32 v52, v54, v55\n" \
  "v_cvt_pk_bf16_f32 v53, v56, v57\n" \
  "v_cvt_pk_bf16_f32 v58, v60, v61\n" \
  "v_cvt_pk_bf16_f32 v59, v62, v63\n" \
  "ds_read_b128 v[24:27], v0 offset:6400\n" \
  "ds_read_b128 v[28:31], v1 offset:2048\n" \
  "v_mfma_f32_16x16x32_bf16 v[42:45], v[12:15], v[64:67], 0\n" \
  "v_mfma_f32_16x16x32_bf16 v[48:51], v[12:15], v[128:131], 0\n" \
  "v_mfma_f32_16x16x32_bf16 v[54:57], v[12:15], v[192:195], 0\n" \
  "s_waitcnt lgkmcnt(3)\n" \
  "v_mfma_f32_16x16x32_bf16 v[60:63], v[12:15], v[32:35], 0\n" \
  "ds_read_b128 v[12:15], v0 offset:8448\n" \
  "ds_read_b128 v[32:35], v1 offset:3072\n" \
  "v_mfma_f32_16x16x32_bf16 v[42:45], v[16:19], v[68:71], v[42:45]\n" \
  "v_mfma_f32_16x16x32_bf16 v[48:51], v[16:19], v[132:135], v[48:51]\n" \
  "v_mfma_f32_16x16x32_bf16 v[54:57], v[16:19], v[196:199], v[54:57]\n" \
  "s_waitcnt lgkmcnt(4)\n" \
  "v_mfma_f32_16x16x32_bf16 v[60:63], v[16:19], v[36:39], v[60:63]\n" \
  "ds_read_b128 v[16:19], v0 offset:10496\n" \
  "ds_read_b128 v[36:39], v1 offset:4096\n" \
  "v_mfma_f32_16x16x32_bf16 v[42:45], v[20:23], v[72:75], v[42:45]\n" \
  "v_mfma_f32_16x16x32_bf16 v[48:51], v[20:23], v[136:139], v[48:51]\n" \
  "v_mfma_f32_16x16x32_bf16 v[54:57], v[20:23], v[200:203], v[54:57]\n" \
  "s_waitcnt lgkmcnt(4)\n" \
  "v_mfma_f32_16x16x32_bf16 v[60:63], v[20:23], v[28:31], v[60:63]\n" \
  "ds_read_b128 v[20:23], v0 offset:12544\n" \
  "ds_read_b128 v[28:31], v1 offset:5120\n" \
  "v_mfma_f32_16x16x32_bf16 v[42:45], v[24:27], v[76:79], v[42:45]\n" \
  "v_mfma_f32_16x16x32_bf16 v[48:51], v[24:27], v[140:143], v[48:51]\n" \
  "v_mfma_f32_16x16x32_bf16 v[54:57], v[24:27], v[204:207], v[54:57]\n" \
  "s_waitcnt lgkmcnt(4)\n" \
  "v_mfma_f32_16x16x32_bf16 v[60:63], v[24:27], v[32:35], v[60:63]\n" \
  "ds_read_b128 v[24:27], v0 offset:14592\n" \
  "ds_read_b128 v[32:35], v1 offset:6144\n" \
  "v_mfma_f32_16x16x32_bf16 v[42:45], v[12:15], v[80:83], v[42:45]\n" \
  "v_mfma_f32_16x16x32_bf16 v[48:51], v[12:15], v[144:147], v[48:51]\n" \
  "v_mfma_f32_16x16x32_bf16 v[54:57], v[12:15], v[208:211], v[54:57]\n" \
  "s_waitcnt lgkmcnt(4)\n" \
  "v_mfma_f32_16x16x32_bf16 v[60:63], v[12:15], v[36:39], v[60:63]\n" \
  "ds_read_b128 v[12:15], v0 offset:16640\n" \
  "ds_read_b128 v[36:39], v1 offset:7168\n" \
  "v_mfma_f32_16x16x32_bf16 v[42:45], v[16:19], v[84:87], v[42:45]\n" \
  "v_mfma_f32_16x16x32_bf16 v[48:51], v[16:19], v[148:151], v[48:51]\n" \
  "v_mfma_f32_16x16x32_bf16 v[54:57], v[16:19], v[212:215], v[54:57]\n" \
  "s_waitcnt lgkmcnt(4)\n" \
  "v_mfma_f32_16x16x32_bf16 v[60:63], v[16:19], v[28:31], v[60:63]\n" \
  "ds_read_b128 v[16:19], v0 offset:18688\n" \
  "ds_read_b128 v[28:31], v1 offset:8192\n" \
  "v_mfma_f32_16x16x32_bf16 v[42:45], v[20:23], v[88:91], v[42:45]\n" \
  "v_mfma_f32_16x16x32_bf16 v[48:51], v[20:23], v[152:155], v[48:51]\n" \
  "v_mfma_f32_16x16x32_bf16 v[54:57], v[20:23], v[216:219], v[54:57]\n" \
  "s_waitcnt lgkmcnt(4)\n" \
  "v_mfma_f32_16x16x32_bf16 v[60:63], v[20:23], v[32:35], v[60:63]\n" \
  "ds_read_b128 v[20:23], v0 offset:20736\n" \
  "ds_read_b128 v[32:35], v1 offset:9216\n" \
  "v_mfma_f32_16x16x32_bf16 v[42:45], v[24:27], v[92:95], v[42:45]\n" \
  "v_mfma_f32_16x16x32_bf16 v[48:51], v[24:27], v[156:159], v[48:51]\n" \
  "v_mfma_f32_16x16x32_bf16 v[54:57], v[24:27], v[220:223], v[54:57]\n" \
  "s_waitcnt lgkmcnt(4)\n" \
  "v_mfma_f32_16x16x32_bf16 v[60:63], v[24:27], v[36:39], v[60:63]\n" \
  "ds_read_b128 v[24:27], v0 offset:22784\n" \
  "ds_read_b128 v[36:39], v1 offset:10240\n" \
  "v_mfma_f32_16x16x32_bf16 v[42:45], v[12:15], v[96:99], v[42:45]\n" \
  "v_mfma_f32_16x16x32_bf16 v[48:51], v[12:15], v[160:163], v[48:51]\n" \
  "v_mfma_f32_16x16x32_bf16 v[54:57], v[12:15], v[224:227], v[54:57]\n" \
  "s_waitcnt lgkmcnt(4)\n" \
  "v_mfma_f32_16x16x32_bf16 v[60:63], v[12:15], v[28:31], v[60:63]\n" \
  "ds_read_b128 v[12:15], v0 offset:24832\n" \
  "ds_read_b128 v[28:31], v1 offset:11264\n" \
  "v_mfma_f32_16x16x32_bf16 v[42:45], v[16:19], v[100:103], v[42:45]\n" \
  "v_mfma_f32_16x16x32_bf16 v[48:51], v[16:19], v[164:167], v[48:51]\n" \
  "v_mfma_f32_16x16x32_bf16 v[54:57], v[16:19], v[228:231], v[54:57]\n" \
  "s_waitcnt lgkmcnt(4)\n" \
  "v_mfma_f32_16x16x32_bf16 v[60:63], v[16:19], v[32:35], v[60:63]\n" \
  "ds_read_b128 v[16:19], v0 offset:26880\n" \
  "ds_read_b128 v[32:35], v1 offset:12288\n" \
  "v_mfma_f32_16x16x32_bf16 v[42:45], v[20:23], v[104:107], v[42:45]\n" \
  "v_mfma_f32_16x16x32_bf16 v[48:51], v[20:23], v[168:171], v[48:51]\n" \
  "v_mfma_f32_16x16x32_bf16 v[54:57], v[20:23], v[232:235], v[54:57]\n" \
  "s_waitcnt lgkmcnt(4)\n" \
  "v_mfma_f32_16x16x32_bf16 v[60:63], v[20:23], v[36:39], v[60:63]\n" \
  "ds_read_b128 v[20:23], v0 offset:28928\n" \
  "ds_read_b128 v[36:39], v1 offset:13312\n" \
  "v_mfma_f32_16x16x32_bf16 v[42:45], v[24:27], v[108:111], v[42:45]\n" \
  "v_mfma_f32_16x16x32_bf16 v[48:51], v[24:27], v[172:175], v[48:51]\n" \
  "v_mfma_f32_16x16x32_bf16 v[54:57], v[24:27], v[236:239], v[54:57]\n" \
  "s_waitcnt lgkmcnt(4)\n" \
  "v_mfma_f32_16x16x32_bf16 v[60:63], v[24:27], v[28:31], v[60:63]\n" \
  "ds_read_b128 v[24:27], v0 offset:30976\n" \
  "ds_read_b128 v[28:31], v1 offset:14336\n" \
  "v_mfma_f32_16x16x32_bf16 v[42:45], v[12:15], v[112:115], v[42:45]\n" \
  "v_mfma_f32_16x16x32_bf16 v[48:51], v[12:15], v[176:179], v[48:51]\n" \
  "v_mfma_f32_16x16x32_bf16 v[54:57], v[12:15], v[240:243], v[54:57]\n" \
  "s_waitcnt lgkmcnt(4)\n" \
  "v_mfma_f32_16x16x32_bf16 v[60:63], v[12:15], v[32:35], v[60:63]\n" \
  "ds_read_b128 v[32:35], v1 offset:15360\n" \
  "v_mfma_f32_16x16x32_bf16 v[42:45], v[16:19], v[116:119], v[42:45]\n" \
  "v_mfma_f32_16x16x32_bf16 v[48:51], v[16:19], v[180:183], v[48:51]\n" \
  "v_mfma_f32_16x16x32_bf16 v[54:57], v[16:19], v[244:247], v[54:57]\n" \
  "s_waitcnt lgkmcnt(3)\n" \
  "v_mfma_f32_16x16x32_bf16 v[60:63], v[16:19], v[36:39], v[60:63]\n" \
  "ds_read_b128 v[36:39], v3 offset:64\n" \
  "v_mfma_f32_16x16x32_bf16 v[42:45], v[20:23], v[120:123], v[42:45]\n" \
  "v_mfma_f32_16x16x32_bf16 v[48:51], v[20:23], v[184:187], v[48:51]\n" \
  "v_mfma_f32_16x16x32_bf16 v[54:57], v[20:23], v[248:251], v[54:57]\n" \
  "s_waitcnt lgkmcnt(2)\n" \
  "v_mfma_f32_16x16x32_bf16 v[60:63], v[20:23], v[28:31], v[60:63]\n" \
  "ds_read_b128 v[28:31], v3 offset:192\n" \
  "s_mov_b64 exec, 1\n" \
  "ds_add_u32 v6, v7 offset:8\n" \
  "s_mov_b64 exec, -1\n" \
  "v_xor_b32 v6, 4, v6\n" \
  "v_mfma_f32_16x16x32_bf16 v[42:45], v[24:27], v[124:127], v[42:45]\n" \
  "v_mfma_f32_16x16x32_bf16 v[48:51], v[24:27], v[188:191], v[48:51]\n" \
  "v_mfma_f32_16x16x32_bf16 v[54:57], v[24:27], v[252:255], v[54:57]\n" \
  "s_waitcnt lgkmcnt(3)\n" \
  "v_mfma_f32_16x16x32_bf16 v[60:63], v[24:27], v[32:35], v[60:63]\n" \
  "s_waitcnt lgkmcnt(1)\n" \
  "s_nop 4\n" \
  "ds_read_b32 v5, v6 offset:0\n" \
  "v_fma_f32 v42, -v36, v8, v42\n" \
  "v_fma_f32 v43, -v37, v8, v43\n" \
  "v_fma_f32 v44, -v38, v8, v44\n" \
  "v_fma_f32 v45, -v39, v8, v45\n" \
  "v_mul_f32 v42, v42, v28\n" \
  "v_mul_f32 v43, v43, v29\n" \
  "v_mul_f32 v44, v44, v30\n" \
  "v_mul_f32 v45, v45, v31\n" \
  "v_cvt_pk_bf16_f32 v42, v42, v43\n" \
  "v_cvt_pk_bf16_f32 v43, v44, v45\n" \
  "buffer_store_dwordx4 v[40:43], v2, s[40:43], s65 offen offset:0 sc1\n" \
  "v_fma_f32 v48, -v36, v9, v48\n" \
  "v_fma_f32 v49, -v37, v9, v49\n" \
  "v_fma_f32 v50, -v38, v9, v50\n" \
  "v_fma_f32 v51, -v39, v9, v51\n" \
  "v_mul_f32 v48, v48, v28\n" \
  "v_mul_f32 v49, v49, v29\n" \
  "v_mul_f32 v50, v50, v30\n" \
  "v_mul_f32 v51, v51, v31\n" \
  "v_cvt_pk_bf16_f32 v48, v48, v49\n" \
  "v_cvt_pk_bf16_f32 v49, v50, v51\n" \
  "buffer_store_dwordx4 v[46:49], v2, s[40:43], s65 offen offset:1024 sc1\n" \
  "v_fma_f32 v54, -v36, v10, v54\n" \
  "v_fma_f32 v55, -v37, v10, v55\n" \
  "v_fma_f32 v56, -v38, v10, v56\n" \
  "v_fma_f32 v57, -v39, v10, v57\n" \
  "v_mul_f32 v54, v54, v28\n" \
  "v_mul_f32 v55, v55, v29\n" \
  "v_mul_f32 v56, v56, v30\n" \
  "v_mul_f32 v57, v57, v31\n" \
  "v_cvt_pk_bf16_f32 v54, v54, v55\n" \
  "v_cvt_pk_bf16_f32 v55, v56, v57\n" \
  "buffer_store_dwordx4 v[52:55], v2, s[40:43], s65 offen offset:2048 sc1\n" \
  "v_fma_f32 v60, -v36, v4, v60\n" \
  "v_fma_f32 v61, -v37, v4, v61\n" \
  "v_fma_f32 v62, -v38, v4, v62\n" \
  "v_fma_f32 v63, -v39, v4, v63\n" \
  "v_mul_f32 v60, v60, v28\n" \
  "v_mul_f32 v61, v61, v29\n" \
  "v_mul_f32 v62, v62, v30\n" \
  "v_mul_f32 v63, v63, v31\n" \
  "v_cvt_pk_bf16_f32 v60, v60, v61\n" \
  "v_cvt_pk_bf16_f32 v61, v62, v63\n" \
  "buffer_store_dwordx4 v[58:61], v2, s[40:43], s65 offen offset:3072 sc1\n" \
  "s_waitcnt lgkmcnt(0)\n" \
  "s_add_u32 s65, s65, 0x40000\n" \
  "s_sub_u32 s64, s64, 1\n" \
  "s_mov_b32 s68, 1\n" \
  "s_cmp_lg_u32 s64, 0\n" \
  "s_cbranch_scc1 .LCW_TOP_%=\n" \
  "s_waitcnt vmcnt(0)\n" \
  "s_mov_b64 exec, 1\n" \
  "buffer_store_dword v7, off, s[56:59], s72 sc1\n" \
  "s_mov_b64 exec, -1\n" \
  "s_add_u32 s72, s72, 0x1000\n" \
  "s_mov_b32 s68, 0\n" \
  ".LCW_END_%=:\n" \
  "s_endpgm\n"
constexpr int RG = 2;
#define RFL(x) __builtin_amdgcn_readfirstlane(x)
#define HALF(x, i) BF8(((i) ? __builtin_shufflevector(x, x, 4, 5, 6, 7) : __builtin_shufflevector(x, x, 0, 1, 2, 3)))
constexpr int P_W = 65536, P_ST = 131072, P_SS = P_ST + 1024, P_CN = P_SS + 512;
template <bool PR>
__device__ __forceinline__ void role_proj_t(const MegaArgs& A, char* smem, const int L, const int idx, const bool kv) {
  const int w = A.wv;
  const int l = lane_id();
  const int c = l & 15, g = l >> 4;
  char* ws = A.ws;
  unsigned* FL = (unsigned*)(ws + OFF_FL);
  unsigned* TMO = FL + FL_TMO;
  const unsigned sb = lds_base(smem);
  auto cn = (volatile __attribute__((address_space(3))) unsigned*)(unsigned long)(sb + P_CN);
  u64 tag;
  {
    const int j = w & 3;
    const bool vt = kv && j >= 2;
    constexpr int NT = PR ? 3 : 1;
    v8u wq[NT][8];
    const u16* Wm = (kv ? (PR ? A.Wv : A.Wk) : A.Wq) - (PR ? 128 : 0) * HD;
    const int hm = kv ? idx : 2 * idx + PR;
    const int q = (l >> 2) & 3, p = l & 3, f = q | ((g & 1) << 2);
    unsigned ra[NT];
#pragma unroll
    for (int m = 0; m < NT; ++m) {
      const int ct = PR ? m : 3;
      ra[m] = sb + (j >> 1) * 65536 + (8 * g + q) * 256 + ((4 * (j & 1) + (vt ? ct : (ct & 2) + (p >> 1))) ^ f) * 32 + (vt ? p * 8 : (p & 1) * 16 + (ct & 1) * 8);
    }
#define W_ISSUE(n) wqkv_issue<false>(Wm + (128 * (n) - 256 * ((n) & 1)) * HD, (n) & 1, hm, w, l, sb)
#define W_STAGE(n, WAIT, NEXT)                                                                             \
  {                                                                                                        \
    WAIT;                                                                                                  \
    RAW_BAR();                                                                                             \
    _Pragma("unroll") for (int m = 0; m < NT; ++m) {                                                       \
      _Pragma("unroll") for (int t = 0; t < 2; ++t) {                                                      \
        const unsigned a_ = ra[m] + ((n) & 1) * 8192 + t * 32768;                                          \
        const v2u r0 = trd(a_), r1 = trd(a_ + 1024), r2 = trd(a_ + 16384), r3 = trd(a_ + 16384 + 1024);    \
        wq[m][2 * (n) + t] = v8u{r0.x, r0.y, r1.x, r1.y, r2.x, r2.y, r3.x, r3.y};                          \
      }                                                                                                    \
    }                                                                                                      \
    _Pragma("unroll") for (int m = 0; m < 2 * NT; ++m) asm volatile("" : "+v"(wq[m >> 1][2 * (n) + (m & 1)])); \
    RAW_BAR();                                                                                             \
    NEXT;                                                                                                  \
    _Pragma("unroll") for (int m = 0; m < 4 * NT; ++m)                                                     \
      cs[m % NT] = MFMA16(HALF(wq[m % NT][2 * (n) + m / NT / 2], (m / NT) & 1), BF8(one), cs[m % NT]);     \
    _Pragma("unroll") for (int m = 0; m < NT; ++m) asm volatile("" : "+v"(cs[m]));                         \
  }
    __builtin_amdgcn_s_waitcnt(0x0f70);
    W_ISSUE(0);
    W_ISSUE(1);
    tag = call_tag(A);
    if (w == 0 && l < 8) cn[l] = l == 1 ? 3u : 0u;
    const v4u one = {0x3f803f80u, 0x3f803f80u, 0x3f803f80u, 0x3f803f80u};
    f32x4 cs[NT] = {};
    W_STAGE(0, WAIT_VM(8), W_ISSUE(2));
    SST(4);
    W_STAGE(1, WAIT_VM(8), W_ISSUE(3));
    SST(5);
    W_STAGE(2, WAIT_VM(8), (void)0);
    SST(6);
    ready_wait(A, tag, ready_look(A));
    W_STAGE(3, WAIT_VM(0), if (!PR) {
      _Pragma("unroll") for (int ks = 0; ks < 16; ++ks)
        *reinterpret_cast<bf16x8*>(smem + P_W + j * 16384 + ks * 1024 + l * 16) = HALF(wq[0][ks >> 1], ks & 1);
    });
    SST(7);
#undef W_STAGE
#undef W_ISSUE
#pragma unroll
    for (int m = 0; m < NT; ++m) *reinterpret_cast<f32x4*>(smem + P_ST + j * 256 + (PR ? m : 3) * 64 + g * 16) = cs[m];
    RAW_BAR();
    SST(8);
    if constexpr (PR) {
      const v4u rsO = mk_srd(ws + RFL((unsigned)(vt ? OFF_VF + L * SZ_O : OFF_QK + L * SZ_QK)), RFL((unsigned)(vt ? SZ_O : SZ_QK)));
      const v4u rsP = mk_srd(FL + RFL(kv ? FL_FQ + (L * 64 * 32 + idx) * 32 + 2 * j : FL_FP + (L * 16 + idx) * 32 + j), 63 * 4096 + 4);
      const int so = RFL(vt ? idx * 8 + 4 * (j & 1) : (kv ? idx * 16 + 8 : (2 * idx + (j >> 1)) * 16) + 2 * (j & 1));
      const v8u pv = {sb + 32768 + g * 512 + c * 16, sb + P_W + j * 16384 + l * 16, (unsigned)l * 16, sb + P_SS + 256 + (vt ? g * 16 : c * 4),
                      sb + P_ST + j * 256 + (vt ? c * 4 : g * 16), 0u, 0u, 0u};
      const char* dbg = nullptr;
#define P_OPS                                                                                                                               \
  ::"{v[64:71]}"(wq[0][0]), "{v[72:79]}"(wq[0][1]), "{v[80:87]}"(wq[0][2]), "{v[88:95]}"(wq[0][3]), "{v[96:103]}"(wq[0][4]),                \
      "{v[104:111]}"(wq[0][5]), "{v[112:119]}"(wq[0][6]), "{v[120:127]}"(wq[0][7]), "{v[128:135]}"(wq[1][0]), "{v[136:143]}"(wq[1][1]),     \
      "{v[144:151]}"(wq[1][2]), "{v[152:159]}"(wq[1][3]), "{v[160:167]}"(wq[1][4]), "{v[168:175]}"(wq[1][5]), "{v[176:183]}"(wq[1][6]),     \
      "{v[184:191]}"(wq[1][7]), "{v[192:199]}"(wq[2][0]), "{v[200:207]}"(wq[2][1]), "{v[208:215]}"(wq[2][2]), "{v[216:223]}"(wq[2][3]),     \
      "{v[224:231]}"(wq[2][4]), "{v[232:239]}"(wq[2][5]), "{v[240:247]}"(wq[2][6]), "{v[248:255]}"(wq[2][7]), "{v[0:7]}"(pv),               \
      "{s[40:43]}"(rsO), "{s[56:59]}"(rsP), "{s64}"(NSEG), "{s65}"(so * 1024), "{s70}"(sb + P_CN), "{s[36:37]}"(dbg)                        \
      : "memory", "vcc", "scc", "s80", "v63"
      if (vt) asm volatile("s_mov_b64 exec, -1\ns_waitcnt vmcnt(0) lgkmcnt(0)\ns_nop 4\n" V_ASM P_OPS);
      else asm volatile("s_mov_b64 exec, -1\ns_waitcnt vmcnt(0) lgkmcnt(0)\ns_nop 4\n" P_ASM P_OPS);
#undef P_OPS
      return;
    }
  }
  __builtin_amdgcn_s_setprio(RPRIO);
  if (kv || w >= 2) {
    const char* Xb = ws + (L ? OFF_X3 : OFF_XA);
    const __amdgpu_buffer_rsrc_t rsS = MKRS(ws + OFF_H + L * (SZ_X / 2), SZ_X / 2);
    const __amdgpu_buffer_rsrc_t rsM = MKRS(ws + OFF_MEMK + L * SZ_MEM, kv && w == 2 ? 3 * SZ_MEM : 0);
    const int b0 = RFL(kv ? 3 * w : w == 2 ? 0 : HX0), b1 = RFL(kv ? (w == 2 ? 8 : 3 * w + 3 * (w < 2)) : w == 2 ? HX0 : 8);
    const __amdgpu_buffer_rsrc_t rsF = MKRS(FL + FL_FC + L * 64 * 32 * 32, 64 * 4096);
    const int fo = ((blockIdx.x & 3) * 8 + (w == 3 ? l >> 3 : min(b0 + (l >> 3), b1 - 1))) * 128 + (l & 7) * 4;
#define FC_LOOK(s) __builtin_amdgcn_raw_buffer_load_b32(rsF, fo, (s) * 4096, AUX_SC1)
    unsigned tn = now10(), nf = 0u;
    for (int s = 0; s < NSEG; ++s) {
      if (kv && w == 2) {
#pragma unroll
        for (int i = 0; i < 4; ++i) ST128((v4u{0u, 0u, 0u, 0u}), rsM, l * 16 + (i & 1) * 1024, s * 65536 + idx * 2048 + (i >> 1) * (int)(OFF_MEMV - OFF_MEMK));
      }
      for (unsigned spins = 0; !__all(nf != 0u); ++spins) {
        if (spin_timeout(spins, TMO)) return;
        if (spins) __builtin_amdgcn_s_sleep(SLP_H);
        nf = FC_LOOK(s);
        asm volatile("" ::: "memory");
      }
      for (unsigned spins = 0; cn[2 + (s & 1)] < 4u * (s >> 1); ++spins) {
        if (spins > 64u * SPIN_CAP) return;
        __builtin_amdgcn_s_sleep(2);
      }
      if (L == 0 && s >= PACE0) gate(tn);
      else tn = now10();
      tn += PACE;
      for (int i = b0; i < b1; ++i) glds64c(Xb + RFL(s * 32768 + i * 4096), l * 16, RFL(sb + (s & 1) * 32768 + i * 4096));
      if (w != 3) {
        nf = L ? 0u : FC_LOOK(s + 1);
        WAIT_VM(0);
        if (l == 0) __hip_atomic_fetch_add((__attribute__((address_space(3))) unsigned*)cn + (s & 1), kv ? 2u : 3u, __ATOMIC_RELAXED, __HIP_MEMORY_SCOPE_WORKGROUP);
      } else {
        v4u t[16];
#pragma unroll
        for (int i = 0; i < 16; ++i) t[i] = LD128(rsS, l * 16, s * 16384 + i * 1024);
        WAIT_VM(16);
        nf = L ? 0u : FC_LOOK(s + 1);
        if (l == 0 && !kv) __hip_atomic_fetch_add((__attribute__((address_space(3))) unsigned*)cn + (s & 1), 3u, __ATOMIC_RELAXED, __HIP_MEMORY_SCOPE_WORKGROUP);
        float a = 0.f, b = 0.f, qa = 0.f, qb = 0.f;
#pragma unroll
        for (int i = 0; i < 16; ++i) {
          a = one(a + __uint_as_float(t[i].x));
          b = one(b + __uint_as_float(t[i].z));
          qa = one(qa + __uint_as_float(t[i].y));
          qb = one(qb + __uint_as_float(t[i].w));
        }
        a = xsum32(xsum16(a));
        b = xsum32(xsum16(b));
        float da = 0.f, db = 0.f;
#pragma unroll
        for (int i = 0; i < 16; ++i) {
          const float u = one(__uint_as_float(t[i].x) - a * (1.0f / 64)), v = one(__uint_as_float(t[i].z) - b * (1.0f / 64));
          da = one(da + u * u);
          db = one(db + v * v);
        }
        const float var = xsum32(swap_add16(qa + da * 0.125f, qb + db * 0.125f)) * (1.0f / DM);
        const float ri = 1.0f / bfr1(sqrtf(bfr1(bfr1(var) + 1e-5f)));
        reinterpret_cast<float*>(smem + P_SS)[(s & 1) * 64 + (g >> 1) * 32 + 2 * c + (g & 1)] = g < 2 ? bfr1(((g & 1) ? b : a) * (1.0f / DM)) : ri;
        asm volatile("" ::: "memory");
        if (l == 0) cn[4 + (s & 1)] = s + 1;
      }
    }
    return;
  }
  const __amdgpu_buffer_rsrc_t rsW = MKRS(ws + OFF_PW, SZ_PW);
  const __amdgpu_buffer_rsrc_t rsX = MKRS(ws + (L ? OFF_X3 : OFF_XA), SZ_X);
  const __amdgpu_buffer_rsrc_t rsY = L ? MKRS(A.out, SZ_X) : MKRS(ws + OFF_X2, SZ_X);
  const int r = 2 * idx + (g >> 1);
  if (L == 0) {
    for (int s = 0; s < NSEG; ++s) ST128((v4u{0u, 0u, 0u, 0u}), rsY, l * 16, (s * SEG + 2 * idx + w) * DM * 2);
    DRAIN();
    if (l < 16) set_flag(FL + FL_FZ + (2 * (l & 7) + (idx >> 3)) * 32 + (idx & 7) * 4 + 2 * w + (l >> 3));
  }
  v4u sel;
#pragma unroll
  for (int e = 0; e < 4; ++e) sel[e] = (g >> 1) == (c >> 3) && ((c & 7) >> 1) == e ? 0x3f80u << (16 * (c & 1)) : 0u;
  unsigned tl = now10();
  for (int s = w; s < NSEG; s += RG) {
    grp_wait<SLP_R, 4, GATE_R>(true, nullptr, 0, FL + FL_FW + ((L * 64 + s) * 4 + (blockIdx.x & 3)) * 4 * 32, 16, TMO, tl);
    if (s >= NSEG - 6) tl = now10();
    TS(0, s, w * 64);
    v4u p[8][4], xr[2];
#pragma unroll
    for (int u = 0; u < 2; ++u) {
      xr[u] = LD128(rsX, (l & 31) * 512 + r * 16, s * 32768 + u * 16384);
#pragma unroll
      for (int k = 0; k < 8; ++k)
#pragma unroll
        for (int b = 2 * u; b < 2 * u + 2; ++b)
          p[k][b] = LD128(rsW, (g & 1) * 32768 + (c >> 2) * 128 + (g >> 1) * 64 + (c & 3) * 16 + b * 512, ((L * 64 + s) * 16 + 2 * k) * 32768 + idx * 2048);
      __builtin_amdgcn_sched_barrier(0);
    }
    __builtin_amdgcn_sched_barrier(0);
    f32x4 ac[4] = {};
#pragma unroll
    for (int k = 0; k < 8; ++k)
#pragma unroll
      for (int b = 0; b < 4; ++b) ac[b] = MFMA16(BF8(sel), BF8(p[k][b]), ac[b]);
    v4u xo[2];
    float sm = 0.f;
#pragma unroll
    for (int u = 0; u < 2; ++u) {
      const auto x = __builtin_amdgcn_permlane16_swap(pk2s(ac[2 * u][0], ac[2 * u][1]), pk2s(ac[2 * u + 1][0], ac[2 * u + 1][1]), false, false);
      const auto y = __builtin_amdgcn_permlane16_swap(pk2s(ac[2 * u][2], ac[2 * u][3]), pk2s(ac[2 * u + 1][2], ac[2 * u + 1][3]), false, false);
      const v4u a = {x[0], y[0], x[1], y[1]};
#pragma unroll
      for (int e = 0; e < 4; ++e) {
        xo[u][e] = pk2s(lo16(xr[u][e]) + lo16(a[e]), hi16(xr[u][e]) + hi16(a[e]));
        if (L == 0) xo[u][e] = xo[u][e] ? xo[u][e] : 0x8000u;
      }
      ST128(xo[u], rsY, r * (DM * 2) + (l & 31) * 16 + u * 512, s * SEG * DM * 2);
    }
    TS(1, s, w * 64);
    if (L == 0) {
#pragma unroll
      for (int e = 0; e < 8; ++e) sm = one(sm + one(lo16(xo[e >> 2][e & 3]) + hi16(xo[e >> 2][e & 3])));
      const float mean = half_sum(sm) * (1.0f / DM);
      float q0 = 0.f, q1 = 0.f;
#pragma unroll
      for (int e = 0; e < 8; ++e) {
        const float u = one(lo16(xo[e >> 2][e & 3]) - mean), v = one(hi16(xo[e >> 2][e & 3]) - mean);
        q0 = one(q0 + u * u);
        q1 = one(q1 + v * v);
      }
      const float var = half_sum(q0 + q1) * (1.0f / DM);
      const unsigned st = pk2(mean, sqrtf(bfr1(bfr1(var) + 1e-5f)));
      if ((l & 31) < 4) __hip_atomic_store((gu32*)(FL + FL_FR + ((s * 4 + (l & 3)) * 4 + (r >> 3)) * 32 + (r & 7)), st, RLX_AGENT);
    }
    TS(3, s, w * 64);
  }
  if (L == 1 && idx == 0 && w == 0 && l == 0) __hip_atomic_store((gu64*)(ws + OFF_EP), tag, RLX_AGENT);
}
__device__ __forceinline__ void role_proj(const MegaArgs& A, char* smem, const int L, const int idx, const bool kv) {
  if (A.wv < 4) role_proj_t<false>(A, smem, L, idx, kv);
  else role_proj_t<true>(A, smem, L, idx, kv);
}

constexpr int CUN = 2144;
__device__ __forceinline__ v2u part_stats(const v4u o) {
  float v[8];
#pragma unroll
  for (int e = 0; e < 4; ++e) { v[2 * e] = lo16(o[e]); v[2 * e + 1] = hi16(o[e]); }
  const float s = ((v[0] + v[1]) + (v[2] + v[3])) + ((v[4] + v[5]) + (v[6] + v[7])), m = s * 0.125f;
  float q0 = 0.f, q1 = 0.f;
#pragma unroll
  for (int e = 0; e < 4; ++e) {
    const float a = v[e] - m, b = v[4 + e] - m;
    q0 += a * a;
    q1 += b * b;
  }
  return v2u{__float_as_uint(s), __float_as_uint(q0 + q1)};
}
constexpr int CRN = 176;
constexpr int CV_LDS = (8 * 2 * CKN + 8 * 2 * CUN + 64 + 2 * 32 * 8 + 16 * CRN + 64 + 256 * 8) * 2 + 8 * 1568;
__device__ __forceinline__ void role_conv(const MegaArgs& A, char* smem, const int cv) {
  u16* Kr = reinterpret_cast<u16*>(smem);
  u16* Uc = Kr + 8 * 2 * CKN;
  u16* Us = Uc + 8 * CUN + 63;
  u16* To = Uc + 8 * 2 * CUN + 64;
  u16* Sx = To + 32 * 8;
  u16* Rc = Sx + 32 * 8;
  u16* Rs = Rc + 8 * CRN + 63;
  u16* Ta = Rc + 16 * CRN + 64;
  const int w = A.wv;
  const int l = lane_id(), tid = 64 * w + l, c = l & 15, g = l >> 4;
  const int d = 8 * cv + w;
  char* ws = A.ws;
  unsigned* FL = (unsigned*)(ws + OFF_FL);
  unsigned* TMO = FL + FL_TMO;
  u16* kr = Kr + w * 2 * CKN;
  const u16* ka = kr + (c >> 3) * CKN + 2080 - 16 * (c & 7) + 8 * g;
#define KFRAG(D0) (*reinterpret_cast<const bf16x8*>(ka - (D0)))
  typedef __attribute__((address_space(3))) const unsigned* lpu;
#define BFRAG(a) BF8((v4u{((lpu)(unsigned long)(a))[0], ((lpu)(unsigned long)(a))[1], ((lpu)(unsigned long)(a))[2], ((lpu)(unsigned long)(a))[3]}))
  f32x4 acc[16], far[8];
  v4u tx[4];
  const __amdgpu_buffer_rsrc_t rz1 = MKRS(ws + OFF_XA, SZ_X), rps = MKRS(ws + OFF_H, SZ_X);
  u16* uc = Uc + w * CUN;
  const unsigned ub = lds_base(reinterpret_cast<char*>(((c & 1) ? Us : Uc) + w * CUN + c + 8 * g + 32));
  const int t0 = 16 * (4 * (g & 1) + (g >> 1)) + c;
  float dsk;
#define SKIP(T) pk2(bf2f(uc[32 + 128 * (T) + t0]) * dsk, bf2f(uc[64 + 128 * (T) + t0]) * dsk)
#define TILE_OUT(T, a, uu)                                                                     \
  {                                                                                            \
    const unsigned yy = pk2(swap_add32(a[0], a[1]), swap_add32(a[2], a[3]));                   \
    const unsigned v_ = pk2(lo16(yy) + lo16(uu), hi16(yy) + hi16(uu));                         \
    Ta[(128 * ((T) & 7) + t0) * 8 + w] = (u16)v_;                                              \
    Ta[(128 * ((T) & 7) + t0 + 32) * 8 + w] = (u16)(v_ >> 16);                                 \
  }
#define CHUNK_ST(i)                                                                            \
  {                                                                                            \
    const v4u a_ = *reinterpret_cast<const v4u*>(Ta + (64 * w + l + 512 * ((i) & 1)) * 8);     \
    v4u o;                                                                                     \
    _Pragma("unroll") for (int e = 0; e < 4; ++e) o[e] = pk2(lo16(tx[i][e]) + lo16(a_[e]), hi16(tx[i][e]) + hi16(a_[e])); \
    ST128(o, rz1, (l >> 5) * 32768 + (l & 31) * 16, (2 * (w + 8 * (i)) * 64 + cv) * 512);      \
    ST64(part_stats(o), rps, (l >> 5) * 16384 + (l & 31) * 8, (2 * (w + 8 * (i)) * 64 + cv) * 256); \
  }
#define CHUNK_FLAG(i) set_flag(FL + FL_FC + (((2 * w + 16 * ((i) + (l >> 3)) + ((l >> 2) & 1)) * 4 + (l & 3)) * 8 + (cv >> 3)) * 32 + (cv & 7))
  {
    unsigned ta[3], k0p;
    v4u kn;
    u64 tag;
    {
      KGen G;
      G.load(A, d, l);
      const u16 pk = A.Dskip[d];
      asm volatile("" ::: "memory");
      tx[0] = *reinterpret_cast<const v4u*>(A.x0 + (size_t)tid * DM + 8 * cv);
      ask_slow(A, 16 * (w - 4) + (cv & 15), ta);
      tag = call_tag(A);
      G.setup();
      dsk = bf2f(pk);
      G.st = smem + w * 16640;
      SST(0);
      G.columns(l);
      const Cx z = G.power(1.f);
      G.rows(G.st + l * 8, cmul(G.C, z), z, 16);
      kn = G.tile(l);
      const float k0 = 2.0f * wave_sum(G.C.r.x + G.C.r.y);
      k0p = l == 0 ? pk2(k0, k0 - lo16(pk2(k0, k0))) : 0u;
    }
    SST(1);
    const __amdgpu_buffer_rsrc_t rst = MKRS(ws + OFF_GT, L_SEQ * 16);
#define U_VAL(i)                                                                               \
  {                                                                                            \
    const float mb_ = lo16(st[i]), ri_ = 1.0f / hi16(st[i]);                                   \
    _Pragma("unroll") for (int e = 0; e < 4; ++e) {                                            \
      const unsigned t_ = pk2(lo16(tx[i][e]) - mb_, hi16(tx[i][e]) - mb_);                     \
      uv[e] = pk2(lo16(t_) * ri_, hi16(t_) * ri_);                                             \
    }                                                                                          \
  }
#define U_PUT(i)                                                                               \
  _Pragma("unroll") for (int e = 0; e < 4; ++e) {                                              \
    ((lph)(unsigned long)up0)[(2 * e) * CUN + 512 * (i)] = (u16)uv[e];                         \
    ((lph)(unsigned long)up0)[(2 * e + 1) * CUN + 512 * (i)] = (u16)(uv[e] >> 16);             \
    ((lph)(unsigned long)up1)[(2 * e) * CUN + 512 * (i)] = (u16)uv[e];                         \
    ((lph)(unsigned long)up1)[(2 * e + 1) * CUN + 512 * (i)] = (u16)(uv[e] >> 16);             \
  }
#define U_ROW(i) { U_VAL(i) U_PUT(i) }
    typedef __attribute__((address_space(3))) u16* lph;
    unsigned st[4], up0 = lds_base((char*)(Uc + 32 + tid)), up1 = lds_base((char*)(Us + 32 + tid));
    asm volatile("" : "+v"(up0), "+v"(up1), "+v"(kn), "+v"(k0p));
    v4u uv;
#define ST_GET(I0, I1)                                                                                                        \
  for (unsigned spins = 0; spins < SPIN_CAP; ++spins) {                                                                       \
    bool ok = true;                                                                                                           \
    v4u t_[4];                                                                                                                \
    _Pragma("unroll") for (int i = I0; i < I1; ++i) t_[i] = LD128(rst, (tid + 512 * i) * 16, 0);                              \
    _Pragma("unroll") for (int i = I0; i < I1; ++i) {                                                                         \
      ok = ok && has_tag(tag, t_[i].x, t_[i].y) && has_tag(tag, t_[i].z, t_[i].w);                                            \
      st[i] = __builtin_amdgcn_perm(t_[i].w, t_[i].y, 0x07060302);                                                            \
    }                                                                                                                         \
    if (__all(ok)) break;                                                                                                     \
    __builtin_amdgcn_s_sleep(4);                                                                                              \
    asm volatile("" ::: "memory");                                                                                            \
  }
    if (w < 4) {
      ST_GET(0, 2);
      U_VAL(0);
      asm volatile("" : "+v"(uv));
    }
#pragma unroll
    for (int i = 1; i < 4; ++i) tx[i] = *reinterpret_cast<const v4u*>(A.x0 + (size_t)(tid + 512 * i) * DM + 8 * cv);
    RAW_BAR();
    SST(2);
    if (w < 4) { U_PUT(0) }
    *reinterpret_cast<v2u*>(kr + 2076 - 16 * c - 4 * g) = v2u{__builtin_amdgcn_perm(kn.z, kn.w, 0x05040100), __builtin_amdgcn_perm(kn.x, kn.y, 0x05040100)};
    *reinterpret_cast<v2u*>(kr + CKN + 2076 - 16 * c - 4 * g) = v2u{__builtin_amdgcn_perm(kn.z, kn.w, 0x07060302), __builtin_amdgcn_perm(kn.x, kn.y, 0x07060302)};
    if (l < 18) {
      reinterpret_cast<v4u*>(kr)[l < 16 ? 260 + l : 210 + l] = v4u{k0p & 0xffffu, 0u, 0u, 0u};
      reinterpret_cast<v4u*>(kr + CKN)[l < 16 ? 260 + l : 210 + l] = v4u{k0p >> 16, 0u, 0u, 0u};
    }
    (l < 32 ? uc : Us + w * CUN - 32)[l] = 0;
    uc[32 + L_SEQ + l] = Us[w * CUN + 32 + L_SEQ + l] = 0;
    uc[32 + 256 + l] = Us[w * CUN + 32 + 256 + l] = 0;
    RAW_BAR();
    SST(11);
    {
      f32x4 a0 = {}, a1 = {}, a2 = {};
      bf16x8 bf[9], af[9];
#pragma unroll
      for (int b = 0; b < 9; ++b) {
        af[(b + 4) % 9] = KFRAG(160 - 32 * ((b + 4) % 9));
        bf[b] = BFRAG(ub + 64 * (b - 1));
      }
      const unsigned k0 = SKIP(0), k1 = SKIP(1);
      __builtin_amdgcn_sched_barrier(0);
#pragma unroll
      for (int b = 0; b < 5; ++b) a0 = MFMA16(af[b + 4], bf[b], a0);
#pragma unroll
      for (int b = 0; b < 4; ++b) {
        a2 = MFMA16(af[b + 4], bf[b + 4], a2);
        a1 = MFMA16(af[b], bf[b], a1);
      }
      a2 = MFMA16(af[8], bf[8], a2);
      TILE_OUT(0, a0, k0);
      a1 += a2;
      TILE_OUT(1, a1, k1);
    }
    const u64 rv = ready_look(A);
    RAW_BAR();
    SST(3);
    if (w < 4) {
      CHUNK_ST(0);
      asm volatile("" : "+v"(tx[1]));
      U_ROW(1);
      DRAIN();
      ready_wait(A, tag, rv);
      if (l < 8) CHUNK_FLAG(0);
      ST_GET(2, 4);
    } else {
      ST_GET(0, 4);
      ready_wait(A, tag, rv);
      asm volatile("" : "+v"(tx[0]), "+v"(tx[1]));
      U_ROW(0);
      U_ROW(1);
    }
    ASKED(ta);
    if (tid < (16 * CRN + 64) / 8) reinterpret_cast<v4u*>(Rc)[tid] = v4u{0u, 0u, 0u, 0u};
    SST(4);
    const gu64* fk = (gu64*)(ws + OFF_KG) + cv * 16 + w;
    u64 kg = __hip_atomic_load(fk, RLX_AGENT);
    U_ROW(2);
    for (unsigned spins = 0; !__all(kg == tag) && spins < SPIN_CAP; ++spins) {
      __builtin_amdgcn_s_sleep(8);
      kg = __hip_atomic_load(fk, RLX_AGENT);
    }
    asm volatile("" : "+v"(tx[3]), "+v"(st[3])::"memory");
    SST(5);
    const unsigned* kd = reinterpret_cast<const unsigned*>(Ta + 256 * 8) + w * 392;
    glds16c(ws + OFF_KF + d * 2048, l * 16, lds_base((char*)kd));
    if (l < 34) glds16c(ws + OFF_KF + d * 2048 + 1024, l * 16, lds_base((char*)kd) + 1024);
    U_ROW(3);
    DRAIN();
    SST(6);
#pragma unroll 1
    for (int un = l; un < 228; un += 64) {
      const int kc = un < 132 ? 2 : un < 196 ? 1 : 0;
      v4u oh, ol;
#pragma unroll
      for (int e = 0; e < 4; ++e) {
        unsigned kv[2];
#pragma unroll
        for (int k = 0; k < 2; ++k) {
          const int lag = 2080 - (8 * un + 2 * e + k);
          const int q = (lag + (1 << kc) - 1 + ((lag >> (kc + 1)) & 1)) >> (kc + 1);
          kv[k] = kd[128 * kc - 128 + q];
          if (lag >= L_SEQ) kv[k] = 0u;
        }
        oh[e] = __builtin_amdgcn_perm(kv[1], kv[0], 0x05040100);
        ol[e] = __builtin_amdgcn_perm(kv[1], kv[0], 0x07060302);
      }
      reinterpret_cast<v4u*>(kr)[un] = oh;
      reinterpret_cast<v4u*>(kr + CKN)[un] = ol;
    }
    RAW_BAR();
    SST(7);
    {
      unsigned uq[2] = {ub, ub + 1024};
      bf16x8 bf[8], a[4];
      const bf16x8 bm = BFRAG(ub - 64);
      asm volatile("" : "+v"(uq[0]), "+v"(uq[1]));
#pragma unroll
      for (int j = 1; j < 8; ++j) bf[j] = BFRAG(uq[j >> 2] + 256 * (j & 3));
#pragma unroll
      for (int T = 2; T < 16; ++T) (T < 8 ? acc[T] : far[T & 7]) = MFMA16(KFRAG(128 * T + 32), bm, (f32x4{}));
#pragma unroll
      for (int e = 0; e < 3; ++e) a[e] = KFRAG(128 * e);
      const u16* kq = ka - 128 * 7;
#pragma unroll 1
      for (int r = 0; r < 4; ++r) {
#pragma unroll
        for (int e = 0; e < 8; ++e) {
          a[(e + 3) & 3] = *reinterpret_cast<const bf16x8*>(kq + 128 * (7 - ((e + 3) & 7)) + (e >= 5 ? 32 : 0));
          const int j = (8 - e) & 7;
          bf[j] = BFRAG(uq[j >> 2] + 256 * (j & 3) + (e ? 64 : 0));
          __builtin_amdgcn_sched_barrier(0);
#pragma unroll
          for (int T = 7; T >= (e > 2 ? e : 2); --T) acc[T] = MFMA16(a[e & 3], bf[T - e], acc[T]);
          __builtin_amdgcn_sched_barrier(0);
        }
        kq += 32;
        uq[0] += 64;
        uq[1] += 64;
        asm volatile("" : "+v"(uq[0]), "+v"(uq[1]));
      }
    }
    SST(8);
    {
      unsigned k[8];
#pragma unroll
      for (int T = 2; T < 8; ++T) k[T] = SKIP(T);
#pragma unroll
      for (int T = 2; T < 8; ++T) TILE_OUT(T, acc[T], k[T]);
    }
    RAW_BAR();
    SST(10);
    if (w >= 4) CHUNK_ST(0);
    CHUNK_ST(1);
    if (w == 1) (void)wait_n<SLP_V, 32>(FL + FL_FZ + (blockIdx.x & 7) * 64, 64, TMO);
    DRAIN();
    if (l < 16) CHUNK_FLAG(0);
    SST(9);
#undef U_ROW
#undef U_VAL
#undef U_PUT
#undef ST_GET
  }
#define FAR_HALF(r, J0, J1)                                                                     \
  {                                                                                            \
    bf16x8 bf[16], af[16];                                                                     \
    unsigned u_ = ub + 64 * (r) + 256 * (J0), v_ = u_ + 1024;                                  \
    asm volatile("" : "+v"(u_), "+v"(v_));                                                     \
    _Pragma("unroll") for (int j = (J0); j < (J1); ++j) bf[j] = BFRAG((j - (J0) < 4 ? u_ : v_) + 256 * ((j - (J0)) & 3)); \
    _Pragma("unroll") for (int e = (9 - (J1) > 0 ? 9 - (J1) : 0); e < 16 - (J0); ++e) af[e] = KFRAG(128 * e - 32 * (r)); \
    __builtin_amdgcn_sched_barrier(0);                                                         \
    _Pragma("unroll") for (int j = (J1) - 1; j >= (J0); --j)                                   \
      _Pragma("unroll") for (int T = (j > 8 ? j : 8); T < 16; ++T) far[T - 8] = MFMA16(af[T - j], bf[j], far[T - 8]); \
  }
#define FAR_PIECE(p)                                                                            \
  {                                                                                            \
    int l = tid & 63;                                                                          \
    asm volatile("" : "+v"(l), "+v"(tx[2]), "+v"(tx[3]));                                      \
    if ((p) < 16) {                                                                            \
      const int r_ = (p) >> 2;                                                                 \
      if (((p) & 3) == 0) FAR_HALF(r_, 0, 3) else if (((p) & 3) == 1) FAR_HALF(r_, 3, 6)       \
      else if (((p) & 3) == 2) FAR_HALF(r_, 6, 9) else FAR_HALF(r_, 9, 16)                     \
    } else if ((p) == 16) {                                                                    \
      unsigned k_[8];                                                                          \
      _Pragma("unroll") for (int T = 0; T < 8; ++T) k_[T] = SKIP(8 + T);                       \
      _Pragma("unroll") for (int T = 0; T < 8; ++T) TILE_OUT(T, far[T], k_[T]);                \
    } else if ((p) == 17) {                                                                    \
      CHUNK_ST(2);                                                                             \
    } else if ((p) == 18) {                                                                    \
      CHUNK_ST(3);                                                                             \
    } else {                                                                                   \
      DRAIN();                                                                                 \
      if (l < 16) CHUNK_FLAG(2);                                                               \
    }                                                                                          \
  }
  static_assert(CVPRE <= 16, "the later pieces need the barriers of the segments");
  const unsigned rb = lds_base(reinterpret_cast<char*>(((c & 1) ? Rs : Rc) + w * CRN + c + 8 * g));
#pragma unroll
  for (int j = 0; j < 16; ++j) acc[j] = f32x4{};
  const __amdgpu_buffer_rsrc_t rx = MKRS(ws + OFF_X2, SZ_X), rz = MKRS(ws + OFF_X3, SZ_X);
  const __amdgpu_buffer_rsrc_t rf = MKRS(FL + FL_FR, 64 * 16 * 128);
  unsigned st = 0u;
  v2u xx = {0u, 0u};
#define X2_LOAD(ss, far)                                                                                                      \
  {                                                                                                                      \
    xx = LD64(rx, (l >> 1) * (DM * 2) + (l & 1) * 8, (far) ? (int)SZ_X : ((ss) * SEG * DM + 8 * cv) * 2);                 \
    st = __builtin_amdgcn_raw_buffer_load_b32(rf, (l >> 4) * 128 + ((l >> 1) & 7) * 4, ((ss) * 4 + (blockIdx.x & 3)) * 512, AUX_SC1); \
  }
  unsigned tl = now10();
  bool late = false;
  for (int s = -CVPRE; s < NSEG; ++s) {
   if (s >= 0) {
    const int q = s & 3;
    if (w == 1) {
      late = __builtin_popcountll(__ballot(st != 0u)) >= CVLATE;
      for (unsigned spins = 0; !__all(umin3(xx.x, xx.y, st) != 0u); ++spins) {
        if (spin_timeout(spins, TMO)) break;
        if (GATE_V && spins == 0 && !__any(st != 0u)) gate(tl);
        if (spins && !__all(st != 0u)) __builtin_amdgcn_s_sleep(SLP_V);
        X2_LOAD(s, __builtin_popcountll(__ballot(st != 0u)) < CVTHR);
      }
      tl = now10() + GATE_V;
      TS(0, s, 64);
      const float mb = lo16(st), ri = 1.0f / hi16(st);
      const unsigned t0 = pk2(lo16(xx.x) - mb, hi16(xx.x) - mb), t1 = pk2(lo16(xx.y) - mb, hi16(xx.y) - mb);
      const unsigned u0 = pk2(lo16(t0) * ri, hi16(t0) * ri), u1 = pk2(lo16(t1) * ri, hi16(t1) * ri);
      u16* rp = Rc + (l & 1) * 4 * CRN + 32 * q + (l >> 1);
#define R_PUT(o) { rp[o] = (u16)u0; rp[(o) + CRN] = (u16)(u0 >> 16); rp[(o) + 2 * CRN] = (u16)u1; rp[(o) + 3 * CRN] = (u16)(u1 >> 16); }
      R_PUT(0) R_PUT(8 * CRN + 63)
      if (q == 0) { R_PUT(128) R_PUT(8 * CRN + 63 + 128) }
#undef R_PUT
      *reinterpret_cast<v2u*>(Sx + l * 4) = xx;
    }
    const u16* kq = ka + 32 * q;
    const bf16x8 a0 = *reinterpret_cast<const bf16x8*>(kq - 32), a1 = *reinterpret_cast<const bf16x8*>(kq);
    __syncthreads();
    TS(1, s, 0);
    const bf16x8 bp = BFRAG(rb + 64 * ((q + 3) & 3)), bc = BFRAG(rb + 64 * q);
    const float u0 = bf2f(Rc[w * CRN + 32 * q + c]), u1 = bf2f(Rc[w * CRN + 32 * q + 16 + c]);
    const float x0 = bf2f(Sx[c * 8 + w]), x1 = bf2f(Sx[(16 + c) * 8 + w]);
    acc[0] = MFMA16(a0, bp, (q == 0 ? acc[1] : acc[0]));
    const f32x4 tmp = MFMA16(a1, bc, acc[0]);
    const unsigned uu = pk2(u0 * dsk, u1 * dsk);
    const unsigned yy = pk2(xsum32((q & 1) ? tmp[2] : tmp[0]), xsum32((q & 1) ? tmp[3] : tmp[1]));
    const unsigned aa = pk2(lo16(yy) + lo16(uu), hi16(yy) + hi16(uu)), oo = pk2(x0 + lo16(aa), x1 + hi16(aa));
    if (g == (q >> 1)) {
      To[c * 8 + w] = (u16)oo;
      To[(16 + c) * 8 + w] = (u16)(oo >> 16);
    }
    __syncthreads();
    if (w == 0 && l < 32) {
      const v4u o = *reinterpret_cast<const v4u*>(To + l * 8);
      ST128(o, rz, l * 16, (s * 64 + cv) * 512);
      ST64(part_stats(o), rps, l * 8, ((64 + s) * 64 + cv) * 256);
    }
    if (w == 1) X2_LOAD(s + 1, !late);
    const int left = 15 - (s >> 2);
#define PUSH(J0, SH)                                                                                              \
  if (left >= (J0)) {                                                                                             \
    _Pragma("unroll") for (int j = (J0); j < (J0) + 4 && j < 16; ++j)                                             \
      acc[j] = MFMA16(*reinterpret_cast<const bf16x8*>(kq - 32 - 128 * j), bp, (j + (SH) < 16 ? acc[(j + (SH)) & 15] : f32x4{})); \
  }
    TS(2, s, 0);
    if (w == 0) {
      DRAIN();
      if (l < 4) set_flag(FL + FL_FC + (((64 + s) * 4 + l) * 8 + (cv >> 3)) * 32 + (cv & 7));
    }
    TS(3, s, 0);
    if (q == 0) { PUSH(1, 1) PUSH(5, 1) PUSH(9, 1) PUSH(13, 1) }
    else { PUSH(1, 0) PUSH(5, 0) PUSH(9, 0) PUSH(13, 0) }
#undef PUSH
    MSTAMP();
   }
    const int pc = s < 0 ? s + CVPRE : (s % CVSTEP) ? 20 : CVPRE + s / CVSTEP;
    if (pc < 20) FAR_PIECE(pc);
  }
#undef KFRAG
#undef BFRAG
#undef X2_LOAD
#undef FAR_PIECE
#undef FAR_HALF
#undef TILE_OUT
#undef SKIP
#undef CHUNK_ST
#undef CHUNK_FLAG
}

__device__ __forceinline__ v4u ln_row_packed(const v4u xr) {
  float v[8];
#pragma unroll
  for (int e = 0; e < 4; ++e) { v[2 * e] = lo16(xr[e]); v[2 * e + 1] = hi16(xr[e]); }
  float s = ((v[0] + v[1]) + (v[2] + v[3])) + ((v[4] + v[5]) + (v[6] + v[7]));
  s = wave_sum(s);
  const float mean = s * (1.0f / DM);
  float q = 0.f;
#pragma unroll
  for (int j = 0; j < 8; ++j) { float t = v[j] - mean; q += t * t; }
  q = wave_sum(q);
  const float var = q * (1.0f / DM);
  const float mb = bfr(mean);
  const float sd = bfr(sqrtf(bfr(bfr(var) + 1e-5f)));
  const float ri = 1.0f / sd;
  v4u o;
#pragma unroll
  for (int e = 0; e < 4; ++e) {
    const unsigned t = pk2(v[2 * e] - mb, v[2 * e + 1] - mb);
    const float x0 = lo16(t), x1 = hi16(t);
    float q0 = x0 * ri, q1 = x1 * ri;
    q0 = __builtin_fmaf(__builtin_fmaf(-q0, sd, x0), ri, q0);
    q1 = __builtin_fmaf(__builtin_fmaf(-q1, sd, x1), ri, q1);
    o[e] = pk2(q0, q1);
  }
  return o;
}

__device__ __forceinline__ void ln4_packed(v4u (&x)[4]) {
  f32x2_t v[16];
#pragma unroll
  for (int e = 0; e < 16; ++e) v[e] = f32x2_t{lo16(x[e >> 2][e & 3]), hi16(x[e >> 2][e & 3])};
  const f32x2_t t = (((v[0] + v[1]) + (v[2] + v[3])) + ((v[4] + v[5]) + (v[6] + v[7]))) +
                    (((v[8] + v[9]) + (v[10] + v[11])) + ((v[12] + v[13]) + (v[14] + v[15])));
  const float mean = red_mod4(t.x + t.y) * (1.0f / DM);
  const float mb = bfr1(mean);
  f32x2_t q0 = {0.f, 0.f}, q1 = {0.f, 0.f};
#pragma unroll
  for (int e = 0; e < 16; e += 2) {
    v[e] -= mb;
    v[e + 1] -= mb;
    q0 += v[e] * v[e];
    q1 += v[e + 1] * v[e + 1];
  }
  q0 += q1;
  const float dm = mean - mb;
  const float var = fmaxf(red_mod4(q0.x + q0.y) * (1.0f / DM) - dm * dm, 0.f);
  const float sd = bfr1(sqrtf(bfr1(bfr1(var) + 1e-5f)));
  const float ri = 1.0f / sd;
#pragma unroll
  for (int e = 0; e < 16; ++e) {
    const unsigned t2 = pk2(v[e].x, v[e].y);
    const f32x2_t q = f32x2_t{lo16(t2), hi16(t2)} * ri;
    x[e >> 2][e & 3] = pk2(q.x, q.y);
  }
}

constexpr int MEGA_WGS = 256;
constexpr size_t MEGA_LDS = (size_t)(32 * 4 * 64 * 8 + 8 * SMS + 15 * QS + 128 + 16 * 64 * 8 + 8 * SOS + 512) * 2;
static_assert(MEGA_LDS <= 163840, "lds");
static_assert(MEGA_LDS >= (size_t)AW_CNT + 128 && MEGA_LDS >= (size_t)P_CN + 512, "lds");
static_assert(MEGA_LDS >= (size_t)CV_LDS && 8 * 1568 >= 768 * 16, "lds");
static_assert(MEGA_LDS >= (size_t)2 * 32 * SMS * 2 && MEGA_LDS >= 131072, "lds");
static_assert(MEGA_LDS >= (size_t)8 * 16640, "lds");
__device__ __forceinline__ MegaArgs role_args(const int b, const int wv) {
  auto p = (const __attribute__((address_space(4))) u64*)__builtin_amdgcn_kernarg_segment_ptr();
  asm volatile("" : "+s"(p));
#define KA(i) ((u16*)(__attribute__((address_space(1))) u16*)p[i])
  return MegaArgs{KA(0), KA(1), (char*)KA(2), KA(3), KA(4), KA(5), KA(6), KA(7), KA(8), KA(9), KA(10), KA(11), KA(12), KA(13), b, wv};
#undef KA
}
__global__ __launch_bounds__(512, 2) void mega_kernel(const u16* mem0, const u16* Dskip, char* ws, u16* out, const u16* x0,
                                                      const u16* logAre, const u16* Aim, const u16* Cre, const u16* Cim,
                                                      const u16* logdt, const u16* Wq, const u16* Wk, const u16* Wv,
                                                      const u16* Wo) {
  extern __shared__ __attribute__((aligned(16))) char smem[];
  const int b = vbid(), wv = __builtin_amdgcn_readfirstlane(threadIdx.x >> 6);
#define A role_args(b, wv)
  u64 tag = 0;
  {
    const unsigned t0 = now10();
    if ((b >= 32 && b < 64) || (b >= 200 && b < 232)) start_kgen(A, smem, b < 64 ? b - 32 : b - 168);
    else if (b >= 64 && b < 96) tag = start_stats<4>(A, b - 64);
    else if (b >= 240) (void)start_stats<8>(A, b - 224);
    const unsigned dl = (b >= 32 && b < 64) ? DL_C : (b >= 64 && b < 96) ? DL_A : b >= 240 ? DL_R2 : ((b >= 96 && b < 104) || b >= 232) ? DL_R : (b >= 200 && b < 232) ? DL_Q : 0u;
    while (now10() - t0 < dl) __builtin_amdgcn_s_sleep(16);
  }
  if ((EXP_OFF & 1) && b >= 64 && b < 80) return;
  if ((EXP_OFF & 8) && b >= 200 && b < 232) return;
  if ((EXP_OFF & 16) && b >= 32 && b < 64) return;
  if ((EXP_OFF & 32) && b >= 80 && b < 96) return;
  if (b < 64) role_chain(A, smem, b >> 5, b & 31, 0);
  else if (b < 96) role_aw(A, smem, (b >> 4) & 1, b & 15, tag);
  else if (b >= 104 && b < 168) role_conv(A, smem, b - 104);
  else {
    const bool kv = b >= 168 && b < 232;
    role_proj(A, smem, b >= (kv ? 200 : 240), kv ? (b - 168) & 31 : b & 15, kv);
  }
#undef A
}

extern "C" void kernel_launch(void* const* d_in, const int* in_sizes, int n_in, void* d_out, int out_size, void* d_ws,
                              size_t ws_size, hipStream_t stream) {
  MegaArgs A;
  A.x0 = (const u16*)d_in[0];
  A.logAre = (const u16*)d_in[1];
  A.Aim = (const u16*)d_in[2];
  A.Cre = (const u16*)d_in[3];
  A.Cim = (const u16*)d_in[4];
  A.logdt = (const u16*)d_in[5];
  A.Dskip = (const u16*)d_in[6];
  A.mem0 = (const u16*)d_in[8];
  A.Wq = (const u16*)d_in[9];
  A.Wk = (const u16*)d_in[10];
  A.Wv = (const u16*)d_in[11];
  A.Wo = (const u16*)d_in[12];
  A.ws = (char*)d_ws;
  A.out = (u16*)d_out;

  (void)hipFuncSetAttribute((const void*)mega_kernel, hipFuncAttributeMaxDynamicSharedMemorySize, (int)MEGA_LDS);
  mega_kernel<<<MEGA_WGS, 512, MEGA_LDS, stream>>>(A.mem0, A.Dskip, A.ws, A.out, A.x0, A.logAre, A.Aim, A.Cre, A.Cim, A.logdt,
                                                   A.Wq, A.Wk, A.Wv, A.Wo);
}
```

```cpp
#include <hip/hip_runtime.h>
#include <hip/hip_bf16.h>
#include <stdint.h>

constexpr int L_SEQ = 2048;
constexpr int DM    = 512;
constexpr int NST   = 128;
constexpr int NH    = 32;
constexpr int DH    = 128;
constexpr int HD    = NH * DH;
constexpr int MM    = 8;
constexpr int SEG   = 32;
constexpr int NSEG  = L_SEQ / SEG;
constexpr int LK    = MM + SEG;

typedef unsigned short u16;
using bf16x8 = __attribute__((ext_vector_type(8))) short;
using f32x4  = __attribute__((ext_vector_type(4))) float;
using f32x16 = __attribute__((ext_vector_type(16))) float;

static __device__ __forceinline__ u16 f2bf(float f) {
  unsigned u = __float_as_uint(f);
  u += 0x7fffu + ((u >> 16) & 1u);
  return (u16)(u >> 16);
}
static __device__ __forceinline__ float bf2f(u16 h) { return __uint_as_float(((unsigned)h) << 16); }
static __device__ __forceinline__ float bfr(float f) { return bf2f(f2bf(f)); }

constexpr size_t SZ_X      = (size_t)L_SEQ * DM * 2;
constexpr size_t OFF_KF    = 0;
constexpr size_t OFF_UT    = OFF_KF + (size_t)DM * L_SEQ * 4;
constexpr size_t OFF_YT    = OFF_UT + SZ_X;
constexpr size_t OFF_XA    = OFF_YT + SZ_X;
constexpr size_t OFF_H     = OFF_XA + SZ_X;
constexpr size_t OFF_X2    = OFF_H + SZ_X;
constexpr size_t OFF_U2    = OFF_X2 + SZ_X;
constexpr size_t OFF_X3    = OFF_U2 + SZ_X;
constexpr size_t SZ_QK     = (size_t)L_SEQ * 2 * HD * 2;
constexpr size_t OFF_QK    = OFF_X3 + SZ_X;
constexpr size_t SZ_O      = (size_t)L_SEQ * HD * 2;
constexpr size_t OFF_VF    = OFF_QK + 2 * SZ_QK;
constexpr size_t SZ_PW     = (size_t)2 * NSEG * 16 * SEG * DM * 2;
constexpr size_t OFF_PW    = OFF_VF + 2 * SZ_O;
constexpr size_t SZ_MEM    = (size_t)NSEG * MM * HD * 2;
constexpr size_t OFF_MEMK  = OFF_PW + SZ_PW;
constexpr size_t OFF_MEMV  = OFF_MEMK + 2 * SZ_MEM;
constexpr size_t OFF_GT    = OFF_MEMV + 2 * SZ_MEM;
constexpr size_t OFF_EP    = OFF_GT + 65536;
constexpr size_t OFF_RD    = OFF_EP + 4096;
constexpr size_t OFF_KG    = OFF_RD + 4096;
constexpr size_t OFF_G     = OFF_KG + 8192;
constexpr size_t SZ_ACC    = (size_t)2 * 32768;
constexpr size_t OFF_Z     = OFF_GT + (size_t)64 * DM * 4;
constexpr size_t OFF_ACC   = OFF_Z;
constexpr size_t OFF_FL    = OFF_ACC + 2 * SZ_ACC;
constexpr int FL_FK  = 0;
constexpr int FL_FW  = FL_FK + 64 * 8 * 32;
constexpr int FL_FR  = FL_FW + 2 * 64 * 16 * 32;
constexpr int FL_FC  = FL_FR + 64 * 16 * 32;
constexpr int FL_FQ  = FL_FC + 2 * 64 * 32 * 32;
constexpr int FL_FP  = FL_FQ + 2 * 64 * 32 * 32;
constexpr int FL_FZ  = FL_FP + 64 * 32 * 32;
constexpr int FL_TMO = FL_FZ + 16 * 32;
constexpr int FL_END = FL_TMO + 64;
constexpr size_t Z_BYTES   = OFF_FL + (size_t)FL_END * 4 - OFF_Z;
constexpr size_t WS_TOTAL  = OFF_Z + Z_BYTES;
static_assert(Z_BYTES % 16 == 0 && OFF_Z % 256 == 0 && Z_BYTES <= 4 * 64 * 8192 && OFF_G + 2048 <= OFF_Z, "zero region");
static_assert(WS_TOTAL + (4 << 20) <= (size_t)256 * 1024 * 1024, "workspace too large");

static_assert(WS_TOTAL - OFF_XA <= (size_t)384 * 8 * 65536, "translation warm-up");
constexpr int CKN = 2208;
static_assert((size_t)DM * 2 * CKN * 2 <= (size_t)DM * L_SEQ * 4 + SZ_X, "K buffer");
__device__ __forceinline__ float pos_of_index(int p) {
  if (p < 256) return (float)p;
  if (p < 384) return (float)(256 + 2 * (p - 256));
  if (p < 512) return (float)(512 + 4 * (p - 384));
  if (p < 640) return (float)(1024 + 8 * (p - 512));
  return 2048.0f;
}
__device__ __forceinline__ int index_of_l(int l) {
  float pf = bfr((float)l);
  int p = (int)pf;
  if (p < 256) return p;
  if (p < 512) return 256 + ((p - 256) >> 1);
  if (p < 1024) return 384 + ((p - 512) >> 2);
  if (p < 2048) return 512 + ((p - 1024) >> 3);
  return 640;
}

template <int CTRL> __device__ __forceinline__ float dppr(float x) {
  return __builtin_bit_cast(float, __builtin_amdgcn_update_dpp(0, __builtin_bit_cast(int, x), CTRL, 0xf, 0xf, false));
}
__device__ __forceinline__ float wave_sum(float v) {
  v += dppr<0x121>(v);
  v += dppr<0x122>(v);
  v += dppr<0x124>(v);
  v += dppr<0x128>(v);
  {
    auto r = __builtin_amdgcn_permlane16_swap(__float_as_uint(v), __float_as_uint(v), false, false);
    v = __uint_as_float(r[0]) + __uint_as_float(r[1]);
  }
  {
    auto r = __builtin_amdgcn_permlane32_swap(__float_as_uint(v), __float_as_uint(v), false, false);
    v = __uint_as_float(r[0]) + __uint_as_float(r[1]);
  }
  return v;
}
__device__ __forceinline__ void ln_row8(float (&v)[8]) {
  float s = 0.f;
#pragma unroll
  for (int j = 0; j < 8; ++j) s += v[j];
  s = wave_sum(s);
  const float mean = s * (1.0f / DM);
  float q = 0.f;
#pragma unroll
  for (int j = 0; j < 8; ++j) { float t = v[j] - mean; q += t * t; }
  q = wave_sum(q);
  const float var = q * (1.0f / DM);
  const float mb = bfr(mean);
  const float sd = bfr(sqrtf(bfr(bfr(var) + 1e-5f)));
#pragma unroll
  for (int j = 0; j < 8; ++j) v[j] = bfr(bfr(v[j] - mb) / sd);
}

#define RLX_AGENT __ATOMIC_RELAXED, __HIP_MEMORY_SCOPE_AGENT
typedef __attribute__((address_space(1))) unsigned gu32;
typedef unsigned v4u __attribute__((ext_vector_type(4)));
constexpr int AUX_SC1 = 16;
constexpr unsigned SPIN_CAP = 1u << 18;

__device__ __forceinline__ bool wait_flags(const unsigned* flags, int lane, int nfl, unsigned want, unsigned* tmo) {
  for (unsigned spins = 0;; ++spins) {
    unsigned v = want;
    if (lane < nfl) v = __hip_atomic_load((gu32*)flags + lane * 32, RLX_AGENT);
    if (__all(v >= want)) break;
    if (spins > SPIN_CAP) {
      if (lane == 0) __hip_atomic_store((gu32*)tmo, 1u, RLX_AGENT);
      return false;
    }
  }
  __builtin_amdgcn_fence(__ATOMIC_ACQUIRE, "wavefront");
  return true;
}

#define STAMP(i)
#define STAMP_DUMP()
#define WSTAMP(i)
template <int CTRL> __device__ __forceinline__ float dppf(float old, float x) {
  return __builtin_bit_cast(float, __builtin_amdgcn_update_dpp(__builtin_bit_cast(int, old), __builtin_bit_cast(int, x), CTRL, 0xf, 0xf, false));
}
__device__ __forceinline__ float wave_max_dpp(float x) {
  const float ninf = -3e38f;
  x = fmaxf(x, dppf<0x111>(ninf, x));
  x = fmaxf(x, dppf<0x112>(ninf, x));
  x = fmaxf(x, dppf<0x114>(ninf, x));
  x = fmaxf(x, dppf<0x118>(ninf, x));
  x = fmaxf(x, dppf<0x142>(ninf, x));
  x = fmaxf(x, dppf<0x143>(ninf, x));
  return __builtin_bit_cast(float, __builtin_amdgcn_readlane(__builtin_bit_cast(int, x), 63));
}
__device__ __forceinline__ float wave_sum_dpp(float x) {
  x += dppf<0x111>(0.f, x);
  x += dppf<0x112>(0.f, x);
  x += dppf<0x114>(0.f, x);
  x += dppf<0x118>(0.f, x);
  x += dppf<0x142>(0.f, x);
  x += dppf<0x143>(0.f, x);
  return __builtin_bit_cast(float, __builtin_amdgcn_readlane(__builtin_bit_cast(int, x), 63));
}
constexpr int SMS = 528;
constexpr int QS = 144;
constexpr int VS = 80;
constexpr int SOS = 144;
#define VAR_NREP 4
constexpr int NREP = VAR_NREP;
__device__ __forceinline__ float xsum16(float x) {
  auto r = __builtin_amdgcn_permlane16_swap(__float_as_uint(x), __float_as_uint(x), false, false);
  return __uint_as_float(r[0]) + __uint_as_float(r[1]);
}
__device__ __forceinline__ float xsum32(float x) {
  auto r = __builtin_amdgcn_permlane32_swap(__float_as_uint(x), __float_as_uint(x), false, false);
  return __uint_as_float(r[0]) + __uint_as_float(r[1]);
}
__device__ __forceinline__ float xmax16(float x) {
  auto r = __builtin_amdgcn_permlane16_swap(__float_as_uint(x), __float_as_uint(x), false, false);
  return fmaxf(__uint_as_float(r[0]), __uint_as_float(r[1]));
}
__device__ __forceinline__ float xmax32(float x) {
  auto r = __builtin_amdgcn_permlane32_swap(__float_as_uint(x), __float_as_uint(x), false, false);
  return fmaxf(__uint_as_float(r[0]), __uint_as_float(r[1]));
}
__device__ __forceinline__ float red_mod4(float x) {
  x += dppf<0x124>(0.f, x);
  x += dppf<0x128>(0.f, x);
  return xsum32(xsum16(x));
}
typedef __bf16 bf16x2_t __attribute__((ext_vector_type(2)));
typedef float f32x2_t __attribute__((ext_vector_type(2)));
__device__ __forceinline__ unsigned pk2(float a, float b) {
  f32x2_t v = {a, b};
  bf16x2_t r = __builtin_convertvector(v, bf16x2_t);
  return __builtin_bit_cast(unsigned, r);
}
__device__ __forceinline__ float one(float x) {
  asm("" : "+v"(x));
  return x;
}
__device__ __forceinline__ unsigned pk2s(float a, float b) { return pk2(one(a), one(b)); }
__device__ __forceinline__ float dot2sq(const unsigned x, const float acc) {
  return __builtin_amdgcn_fdot2_f32_bf16(__builtin_bit_cast(bf16x2_t, x), __builtin_bit_cast(bf16x2_t, x), acc, false);
}
__device__ __forceinline__ float lo16(unsigned u) { return __uint_as_float(u << 16); }
__device__ __forceinline__ float hi16(unsigned u) { return __uint_as_float(u & 0xffff0000u); }
__device__ __forceinline__ float bfr1(float x) { return lo16(pk2(x, x)); }
__device__ __forceinline__ float half_sum(float v) {
  v += dppr<0x121>(v);
  v += dppr<0x122>(v);
  v += dppr<0x124>(v);
  v += dppr<0x128>(v);
  return xsum16(v);
}
__device__ __forceinline__ void scale_pair(float a, float b, float scale, float& oa, float& ob) {
  unsigned u = pk2(a, b);
  unsigned v = pk2s(lo16(u) * scale, hi16(u) * scale);
  oa = lo16(v);
  ob = hi16(v);
}

#define XMAP 2
__device__ __forceinline__ int vbid() {
  const int b = blockIdx.x, x = b & 7, t = b >> 3;
  if (XMAP == 0) {
    if (b < 64) return (b & 32) + 4 * x + (t & 3);
    if (b < 96) return (b & ~15) + 2 * x + (t & 1);
    if (b >= 104 && b < 168) return 104 + 8 * x + t - 13;
    if (b >= 168 && b < 232) return 168 + ((b - 168) & 32) + 4 * x + ((t - 21) & 3);
    return b;
  }
  if (XMAP == 2) {
    if (x < 4) return t < 16 ? 4 * (t & 8) + 8 * x + (t & 7) : 104 + 16 * x + t - 16;
    const int y = x - 4;
    if (t >= 24) return (t >= 28 ? 240 : y < 2 ? 96 : 224) + 4 * y + (t & 3);
    if (t < 16) return 168 + 4 * (t & 8) + 8 * y + (t & 7);
    return 64 + 4 * (t & 4) + 4 * y + (t & 3);
  }
  if (x < 2) return 2 * (t & 16) + 16 * x + (t & 15);
  const int i = 16 * (x - 2) + (t & 15);
  if (t < 16) {
    const int k = (i * 171) >> 9, r = i - 3 * k;
    return r == 0 ? 168 + k : r == 1 ? 200 + k : 64 + 16 * (k & 1) + (k >> 1);
  }
  const int g = i >> 3, e = i & 7;
  if (g == 3) return 96 + e;
  if (g == 7) return 232 + e;
  if (g == 9) return 240 + e;
  if (g == 11) return 248 + e;
  return 104 + 8 * (g - (g > 3) - (g > 7) - (g > 9)) + e;
}
#define MSTAMP()
#define WSTAMP2()
#define SST(k)
#define TS(k, ss, t)
#define LS(k, ss)
#define LS_DUMP()
struct MegaArgs {
  const u16* mem0;
  const u16* Dskip;
  char* ws;
  u16* out;
  const u16* x0;
  const u16 *logAre, *Aim, *Cre, *Cim, *logdt;
  const u16 *Wq, *Wk, *Wv, *Wo;
  int vb, wv;
};

__device__ __forceinline__ int lane_id() {
  int l;
  asm volatile("v_mbcnt_lo_u32_b32 %0, -1, 0\n\tv_mbcnt_hi_u32_b32 %0, -1, %0" : "=v"(l));
  __builtin_assume(l >= 0 && l < 64);
  return l;
}
__device__ __forceinline__ bool spin_timeout(unsigned spins, unsigned* tmo) {
  if (spins > SPIN_CAP) {
    if (lane_id() == 0) __hip_atomic_store((gu32*)tmo, 1u, RLX_AGENT);
    return true;
  }
  return false;
}
#define ROT_H (4 * (h & 1))
#define SLP_H 12
#define HX0 6
#define CVSTEP 2
#define CVLATE 64
#define PACE 204
#define PACE0 6
#define RPRIO 3
#define DL_R2 1400
#define PNL 8
#define GSL 12
#define AWDIR 1
#define AWAT 0
#define AWRP 0
#define PEPS 4
#define FGATE 0
#define DL_C 0
#define DL_A 800
#define DL_R 0
#define DL_Q 0
#define KC0 0
#define CPRIO 1
#define P1PRIO 3
#define QLEAD 2
#define POLL_SLEEP 12
#define SLP_C POLL_SLEEP
#define SLP_A POLL_SLEEP
#define SLP_W POLL_SLEEP
#define SLP_R POLL_SLEEP
#define SLP_V POLL_SLEEP
#define SLP_Q POLL_SLEEP
#define SLP_M 4
#define AWPRIO 2
#define SLP_S 8
#define SPOS 4
#define CVPRE 12
#define QPD 3
#define QPRI 2
#define TLBW 1
#define QHV 0
#define QCHK 0
#define LEADN 99
#define GATE_V 170
#define GATE_R 380
#define GATE_N 380
#define AWAHEAD 1
#define GATE_M 150
#define PRI_A 3
#define PRI_WA 2
#define PRI_WB 1
#define AWSKIP 3
#define PRI_0 3
#define AWDEF 0
#define SLP_F 2
#define EXP_OFF 0
#define AWSPEC 1
#define CVTHR 48
#define NR_C 8
#define NR_R 8
#define NR_W 8
#define NR_N 2
#define QAHEAD 0
#define NS0 4
#define SLP_N POLL_SLEEP
template <int SLP, int WPL>
__device__ __forceinline__ bool wait_n(const unsigned* f, int n, unsigned* tmo) {
  const int l = lane_id();
  for (unsigned spins = 0;; ++spins) {
    unsigned v = 1u;
    if (l < n) v = __hip_atomic_load((gu32*)f + (l / WPL) * 32 + l % WPL, RLX_AGENT);
    if (__all(v != 0u)) break;
    if (spin_timeout(spins, tmo)) return false;
    __builtin_amdgcn_s_sleep(SLP);
  }
  asm volatile("" ::: "memory");
  return true;
}
__device__ __forceinline__ unsigned now10() { return (unsigned)__builtin_amdgcn_s_memrealtime(); }
__device__ __forceinline__ void gate(const unsigned t) {
  for (int i = 0; i < 64; ++i) {
    const int d = (int)(t - now10());
    if (d <= 0) break;
    if (d > 100) __builtin_amdgcn_s_sleep(32);
    else if (d > 25) __builtin_amdgcn_s_sleep(8);
    else __builtin_amdgcn_s_sleep(2);
  }
}
template <int SLP, int WPL, int GT>
__device__ __forceinline__ void grp_wait(const bool leader, volatile unsigned* sw, const unsigned val, const unsigned* f, int n,
                                         unsigned* tmo, unsigned& tl) {
  if (leader) {
    const int l = lane_id();
    for (unsigned spins = 0;; ++spins) {
      unsigned v = 1u;
      if (l < n) v = __hip_atomic_load((gu32*)f + (l / WPL) * 32 + l % WPL, RLX_AGENT);
      if (__all(v != 0u) || spin_timeout(spins, tmo)) break;
      if (GT && spins == 0) gate(tl);
      else __builtin_amdgcn_s_sleep(SLP);
    }
    if (GT) tl = now10() + GT;
    if (sw && l == 0) *sw = val;
  } else {
    for (unsigned spins = 0; *sw < val; ++spins) {
      if (spins > 64u * SPIN_CAP) break;
      if (FGATE && GT && spins == 0) gate(tl);
      __builtin_amdgcn_s_sleep(SLP_F);
    }
    if (FGATE && GT) tl = now10() + GT;
  }
  asm volatile("" ::: "memory");
}
template <int SLP, int WPL>
__device__ __forceinline__ bool wg_wait(const unsigned* f, int n, unsigned* tmo) {
  if (threadIdx.x < 64) (void)wait_n<SLP, WPL>(f, n, tmo);
  __syncthreads();
  asm volatile("" ::: "memory");
  return true;
}
__device__ __forceinline__ void set_flag(unsigned* f) { __hip_atomic_store((gu32*)f, 1u, RLX_AGENT); }
#define DRAIN() asm volatile("s_waitcnt vmcnt(0)" ::: "memory")
#define MKRS(ptr, bytes) __builtin_amdgcn_make_buffer_rsrc((void*)(ptr), 0, (int)(bytes), 0x00020000)
typedef unsigned v2u __attribute__((ext_vector_type(2)));
#define LD128(rs, voff, soff) __builtin_amdgcn_raw_buffer_load_b128(rs, voff, soff, AUX_SC1)
#define LD64(rs, voff, soff) __builtin_amdgcn_raw_buffer_load_b64(rs, voff, soff, AUX_SC1)
#define ST128(v, rs, voff, soff)                                              \
  do {                                                                        \
    const v4u st_d_ = (v);                                                    \
    __builtin_amdgcn_raw_buffer_store_b128(st_d_, rs, voff, soff, AUX_SC1);   \
    asm volatile("s_nop 1" ::"v"(st_d_) : "memory");                          \
  } while (0)
#define ST64(v, rs, voff, soff) __builtin_amdgcn_raw_buffer_store_b64(v, rs, voff, soff, AUX_SC1)
#define BF8(x) __builtin_bit_cast(bf16x8, x)
#define MFMA16(a, b, c) __builtin_amdgcn_mfma_f32_16x16x32_bf16(a, b, c, 0, 0, 0)
typedef short bf16x4 __attribute__((ext_vector_type(4)));
#define MFMA16K(a, b, c) \
  __builtin_amdgcn_mfma_f32_16x16x16bf16_1k(__builtin_bit_cast(bf16x4, a), __builtin_bit_cast(bf16x4, b), c, 0, 0, 0)
__device__ __forceinline__ float fmax_nn(float a, float b) { return __builtin_elementwise_maximum(a, b); }
typedef __bf16 bfv8 __attribute__((ext_vector_type(8)));
typedef __bf16 bfv16 __attribute__((ext_vector_type(16)));
typedef unsigned v8u __attribute__((ext_vector_type(8)));
#define SMF(a, b, c, idx) \
  __builtin_amdgcn_smfmac_f32_16x16x64_bf16(__builtin_bit_cast(bfv8, a), __builtin_bit_cast(bfv16, b), c, idx, 0, 0)
__device__ __forceinline__ float swap_add32(float a, float b) {
  auto r = __builtin_amdgcn_permlane32_swap(__float_as_uint(a), __float_as_uint(b), false, false);
  return __uint_as_float(r[0]) + __uint_as_float(r[1]);
}
__device__ __forceinline__ float swap_add16(float a, float b) {
  auto r = __builtin_amdgcn_permlane16_swap(__float_as_uint(a), __float_as_uint(b), false, false);
  return __uint_as_float(r[0]) + __uint_as_float(r[1]);
}
__device__ __forceinline__ v4u smf_fix(const v4u d) {
  auto x = __builtin_amdgcn_permlane16_swap(d.x, d.z, false, false);
  auto y = __builtin_amdgcn_permlane16_swap(d.y, d.w, false, false);
  return v4u{x[0], x[1], y[0], y[1]};
}
__device__ __forceinline__ v8u smf_bfrag(const u16* p) {
  const v2u a = *reinterpret_cast<const v2u*>(p), b = *reinterpret_cast<const v2u*>(p + 8);
  const v2u c = *reinterpret_cast<const v2u*>(p + 32), d = *reinterpret_cast<const v2u*>(p + 40);
  return v8u{a.x, b.x, a.y, b.y, c.x, d.x, c.y, d.y};
}

#define VAR_PPA 0
#define VAR_PPB 0
#define VAR_THR 0
#define VAR_THR2 0
#define CV_LEAD 100
#define VAR_EARLY 1
#define VAR_DRAIN 1
#define VAR_D1 0
#define VAR_D2 0
#define VAR_DR2 1
#define VAR_S1 0
#define VAR_S2 0
typedef __attribute__((address_space(1))) unsigned long long gu64;
typedef __attribute__((address_space(1))) char gch;
__device__ __forceinline__ int cvt_rpi(float x) {
  int r;
  asm("v_cvt_rpi_i32_f32 %0, %1" : "=v"(r) : "v"(x));
  return r;
}
typedef unsigned long long u64;
__device__ __forceinline__ u64 call_tag(const MegaArgs& A) {
  u64 t = *(const __attribute__((address_space(4))) u64*)(A.ws + OFF_EP) * 6364136223846793005ull + 1442695040888963407ull;
  asm volatile("" : "+s"(t)::"memory");
  return t;
}
__device__ __forceinline__ u64 tagged(const u64 tag, const unsigned v) { return (tag & 0xffffffffffffull) | (u64)(v >> 16) << 48; }
__device__ __forceinline__ bool has_tag(const u64 tag, const unsigned lo, const unsigned hi) {
  return lo == (unsigned)tag && (hi & 0xffffu) == ((unsigned)(tag >> 32) & 0xffffu);
}
__device__ __forceinline__ u64 ready_look(const MegaArgs& A) {
  return __hip_atomic_load((gu64*)(A.ws + OFF_RD) + (blockIdx.x & 7) * 64 + lane_id(), RLX_AGENT);
}
__device__ __forceinline__ void ready_wait(const MegaArgs& A, const u64 tag, u64 v) {
  for (unsigned spins = 0; !__all(v == tag) && spins < SPIN_CAP; ++spins) {
    __builtin_amdgcn_s_sleep(8);
    v = ready_look(A);
  }
  asm volatile("" ::: "memory");
}
typedef short v4s __attribute__((ext_vector_type(4)));
typedef __attribute__((address_space(3))) v4s* lp4s;
__device__ __forceinline__ void glds16(const void* sbase, const unsigned voff, const unsigned lds) {
  unsigned keep;
  asm volatile("s_nop 4\n\ts_mov_b32 %0, m0\n\ts_mov_b32 m0, %3\n\ts_nop 0\n\tglobal_load_lds_dwordx4 %1, %2\n\ts_mov_b32 m0, %0"
               : "=&s"(keep)
               : "v"(voff), "s"(sbase), "s"(lds)
               : "memory");
}
__device__ __forceinline__ void glds16c(const void* sbase, const unsigned voff, const unsigned lds) {
  unsigned keep;
  asm volatile("s_nop 4\n\ts_mov_b32 %0, m0\n\ts_mov_b32 m0, %3\n\ts_nop 0\n\tglobal_load_lds_dwordx4 %1, %2 sc1\n\ts_mov_b32 m0, %0"
               : "=&s"(keep)
               : "v"(voff), "s"(sbase), "s"(lds)
               : "memory");
}
__device__ __forceinline__ void glds64c(const void* sbase, const unsigned voff, const unsigned lds) {
  unsigned keep;
  asm volatile("s_nop 4\n\ts_mov_b32 %0, m0\n\ts_mov_b32 m0, %3\n\ts_nop 0\n\tglobal_load_lds_dwordx4 %1, %2 sc1\n\t"
               "global_load_lds_dwordx4 %1, %2 offset:1024 sc1\n\tglobal_load_lds_dwordx4 %1, %2 offset:2048 sc1\n\t"
               "global_load_lds_dwordx4 %1, %2 offset:3072 sc1\n\ts_mov_b32 m0, %0"
               : "=&s"(keep)
               : "v"(voff), "s"(sbase), "s"(lds)
               : "memory");
}
__device__ __forceinline__ v2u trd(const unsigned a) {
  return __builtin_bit_cast(v2u, __builtin_amdgcn_ds_read_tr16_b64_v4i16((lp4s)(unsigned long)a));
}
__device__ __forceinline__ unsigned lds_base(char* smem) { return (unsigned)(unsigned long)(__attribute__((address_space(3))) char*)smem; }
#define RAW_BAR()                                           \
  do {                                                      \
    asm volatile("s_waitcnt lgkmcnt(0)" ::: "memory");      \
    __builtin_amdgcn_s_barrier();                           \
    asm volatile("" ::: "memory");                          \
  } while (0)
#define WAIT_VM(n) asm volatile("s_waitcnt vmcnt(" #n ")" ::: "memory")
template <bool SP>
__device__ __forceinline__ void wqkv_issue(const u16* Wm, const int hf, const int h, const int w, const int l, const unsigned sb) {
  const char* base = (const char*)Wm + (size_t)(256 * hf + 32 * w) * (HD * 2) + h * (DH * 2);
#pragma unroll
  for (int i = 0; i < 8; ++i) {
    const int r = l >> 4;
    const int f = SP ? ((r & 1) | ((i & 1) << 1) | (((i >> 1) & 1) << 2)) : (r | (((i >> 1) & 1) << 2));
    glds16(base, (unsigned)((4 * i + r) * (HD * 2) + (((l & 15) ^ (f << 1)) << 4)), sb + w * 16384 + hf * 8192 + i * 1024);
  }
}

__device__ __forceinline__ unsigned row_stats(const v4u raw) {
  float v[8];
#pragma unroll
  for (int e = 0; e < 4; ++e) { v[2 * e] = lo16(raw[e]); v[2 * e + 1] = hi16(raw[e]); }
  float s = 0.f;
#pragma unroll
  for (int e = 0; e < 8; ++e) s += v[e];
  const float mean = wave_sum(s) * (1.0f / DM);
  float q = 0.f;
#pragma unroll
  for (int e = 0; e < 8; ++e) { const float t = v[e] - mean; q += t * t; }
  const float var = wave_sum(q) * (1.0f / DM);
  const float sd = bfr(sqrtf(bfr(bfr(var) + 1e-5f)));
  return (unsigned)f2bf(mean) | ((unsigned)f2bf(sd) << 16);
}
__device__ __forceinline__ void ask_slow(const MegaArgs& A, const int k, unsigned (&t)[3]) {
  const int l = lane_id(), x = blockIdx.x & 7;
  const char *wq = (const char*)A.Wq, *wv = (const char*)A.Wv;
  asm("" : "+s"(wq), "+s"(wv));
#pragma unroll
  for (int m = 0; m < 3; ++m) {
    const char* W = m == 1 ? (const char*)A.Wk : ((m == 0) == (x < 4)) ? wv : wq;
    const int a = (int)((unsigned long)W >> 8) + 8 * (x & 3), h = k < 32 ? ((1 - a) & 3) + 4 * (k & 1) : (31 - a) & 31;
    const int r = k < 32 ? k >> 1 : k - 32;
    t[m] = __builtin_amdgcn_raw_buffer_load_b32(MKRS(W, (unsigned)k < 48u && h < 8 ? DM * HD * 2 : 0), (l >> 1) * (HD * 2) + (l & 1) * 128,
                                                (32 * (r & 15)) * (HD * 2) + (8 * (x & 3) + (h & 7)) * 256, 0);
  }
}
#define ASKED(t) asm volatile("" ::"v"(t[0]), "v"(t[1]), "v"(t[2]))
template <int N>
__device__ __forceinline__ u64 start_stats(const MegaArgs& A, const int idx) {
  const int w = A.wv, l = lane_id();
  const int r0 = N * (8 * idx + w);
  unsigned ta[3], tb[3];
  v4u raw[N];
#pragma unroll
  for (int i = 0; i < N; ++i) raw[i] = *reinterpret_cast<const v4u*>(A.x0 + (size_t)(r0 + i) * DM + l * 8);
  asm volatile("" ::: "memory");
  ask_slow(A, 16 * w + (idx & 3) + (idx >> 4) * N, ta);
  if (N > 4) ask_slow(A, 16 * w + (idx & 3) + 12, tb);
  const u64 tag = call_tag(A);
  unsigned stw = 0u;
#pragma unroll
  for (int i = 0; i < N; ++i) {
    const unsigned t = row_stats(raw[i]);
    if ((l >> 1) == i) stw = t;
  }
  if (l < 2 * N) __hip_atomic_store((gu64*)(A.ws + OFF_GT) + 2 * r0 + l, tagged(tag, (l & 1) ? stw : stw << 16), RLX_AGENT);
  SST(0);
  ASKED(ta);
  if (N > 4) ASKED(tb);
  return tag;
}
struct Cx {
  f32x2_t r, i;
};
__device__ __forceinline__ Cx cmul(const Cx a, const Cx b) { return Cx{a.r * b.r - a.i * b.i, a.r * b.i + a.i * b.r}; }
struct KGen {
  unsigned pa, pb, pc, pd;
  u16 pe;
  f32x2_t ar, ai;
  Cx C;
  char* st;
  v4u bh[8], bl[8];
  __device__ __forceinline__ void load(const MegaArgs& A, const int d, const int l) {
    pa = *reinterpret_cast<const unsigned*>(A.logAre + d * NST + 2 * l);
    pb = *reinterpret_cast<const unsigned*>(A.Aim + d * NST + 2 * l);
    pc = *reinterpret_cast<const unsigned*>(A.Cre + d * NST + 2 * l);
    pd = *reinterpret_cast<const unsigned*>(A.Cim + d * NST + 2 * l);
    pe = A.logdt[d];
  }
  __device__ __forceinline__ void setup() {
    const float dt = bfr(expf(bf2f(pe)));
    ar = f32x2_t{-bfr(expf(lo16(pa))) * dt, -bfr(expf(hi16(pa))) * dt};
    ai = f32x2_t{lo16(pb) * dt, hi16(pb) * dt};
    C = Cx{f32x2_t{lo16(pc) * dt, hi16(pc) * dt}, f32x2_t{lo16(pd) * dt, hi16(pd) * dt}};
  }
  __device__ __forceinline__ Cx power(const float p) const {
    Cx z;
#pragma unroll
    for (int e = 0; e < 2; ++e) {
      const float x = ai[e] * p;
      const float f = __builtin_fmaf(x, 6.4206382e-9f, __builtin_fmaf(x, 0.15915494f, -__builtin_rintf(x * 0.15915494f)));
      const float m = __expf(ar[e] * p);
      z.r[e] = m * __builtin_amdgcn_cosf(f);
      z.i[e] = m * __builtin_amdgcn_sinf(f);
    }
    return z;
  }
  __device__ __forceinline__ char* rows(char* p, Cx y, const Cx s, const int n) const {
#pragma unroll 1
    for (int i = 0; i < n; i += 2) {
#pragma unroll
      for (int k = 0; k < 2; ++k) {
        const unsigned h0 = pk2(y.r.x, y.r.y), h1 = pk2(y.i.x, y.i.y);
        const f32x2_t a = y.r - f32x2_t{lo16(h0), hi16(h0)}, b = y.i - f32x2_t{lo16(h1), hi16(h1)};
        *reinterpret_cast<v2u*>(p + 1040 * k) = v2u{h0, h1};
        *reinterpret_cast<v2u*>(p + 1040 * k + 512) = v2u{pk2(a.x, a.y), pk2(b.x, b.y)};
        y = cmul(y, s);
      }
      p += 2080;
    }
    return p;
  }
  __device__ __forceinline__ void get(v4u (&h)[8], v4u (&o)[8], const int l) const {
    const char* p = st + (l & 15) * 1040 + (l >> 4) * 16;
    asm volatile("" ::: "memory");
#pragma unroll
    for (int k = 0; k < 8; ++k) {
      h[k] = *reinterpret_cast<const v4u*>(p + 64 * k);
      o[k] = *reinterpret_cast<const v4u*>(p + 512 + 64 * k);
    }
    asm volatile("" ::: "memory");
  }
  __device__ __forceinline__ void columns(const int l) {
    Cx s = power(16.f);
    s.i = -s.i;
    rows(st + l * 8, Cx{f32x2_t{1.f, 1.f}, f32x2_t{0.f, 0.f}}, s, 16);
    get(bh, bl, l);
  }
  __device__ __forceinline__ v4u tile(const int l) const {
    v4u h[8], o[8];
    get(h, o, l);
    f32x4 d = {}, e = {}, f = {};
#pragma unroll
    for (int k = 0; k < 8; ++k) {
      d = MFMA16(BF8(h[k]), BF8(bh[k]), d);
      e = MFMA16(BF8(o[k]), BF8(bh[k]), e);
      f = MFMA16(BF8(h[k]), BF8(bl[k]), f);
    }
    d = (d + (e + f)) * 2.0f;
    v4u r;
#pragma unroll
    for (int i = 0; i < 4; ++i) r[i] = pk2(d[i], d[i] - lo16(pk2(d[i], d[i])));
    return r;
  }
};
__device__ __forceinline__ void start_kgen(const MegaArgs& A, char* smem, const int kg) {
  const int w = A.wv, l = lane_id(), tid = 64 * w + l, c = l & 15, g = l >> 4;
  KGen G;
  G.load(A, 8 * kg + w, l);
  {
    const __amdgpu_buffer_rsrc_t rz = MKRS(A.ws + OFF_Z, Z_BYTES);
#pragma unroll
    for (int i = 0; i < 4; ++i) ST128((v4u{0u, 0u, 0u, 0u}), rz, tid * 16, (i * 64 + kg) * 8192);
  }
  const u64 tag = call_tag(A);
  DRAIN();
  __syncthreads();
  if (tid < 8) __hip_atomic_store((gu64*)(A.ws + OFF_RD) + tid * 64 + kg, tag, RLX_AGENT);
  asm volatile("" : "+v"(G.pa), "+v"(G.pb), "+v"(G.pe));
  G.setup();
  G.st = smem + w * 16640;
  const __amdgpu_buffer_rsrc_t rs = MKRS(A.ws + OFF_KF + (8 * kg + w) * 2048, 386 * 4);
  SST(1);
  unsigned tw = 0u;
  if (TLBW && tid < 384) {
    const unsigned o = (unsigned)OFF_XA + (unsigned)(tid * 8 + ((blockIdx.x >> 3) & 7)) * 65536u;
    if (o < WS_TOTAL) asm volatile("global_load_dword %0, %1, %2" : "=v"(tw) : "v"(o), "s"(A.ws) : "memory");
  }
  G.columns(l);
  const Cx zb = G.power(256.f);
  Cx b = G.C, z = G.power(2.f);
#pragma unroll 1
  for (int f = 1; f < 9;) {
    char* p = G.st + l * 8;
    const int fe = f == 1 ? 4 : 9;
#pragma unroll 1
    for (; f < fe; ++f) {
      b = cmul(b, zb);
      if (f == 2 || f == 4) z = cmul(z, z);
      p = G.rows(p, b, z, f == 1 ? 8 : f < 4 ? 4 : 2);
    }
    const v4u r = G.tile(l);
    if (f == 4) {
      ST128(r, rs, (g < 2 ? 8 * c + 4 * g : 64 * g + 4 * c) * 4, 0);
    } else {
      ST64((v2u{r.x, r.y}), rs, (256 + 64 * g + 2 * c) * 4, 0);
      ST64((v2u{r.z, r.w}), rs, (288 + 64 * g + 2 * c) * 4, 0);
    }
  }
  SST(2);
  DRAIN();
  asm volatile("" ::"v"(tw));
  if (l == 0) __hip_atomic_store((gu64*)(A.ws + OFF_KG) + kg * 16 + w, tag, RLX_AGENT);
  __syncthreads();
  SST(3);
}

constexpr int C_SM = 131072, C_SO = C_SM + 8192, C_KT = C_SO + 2048, C_X = C_KT + 16384, C_SQ = C_X + 1024;
static_assert(C_SQ + 4096 <= 163840 && C_SQ % 512 == 0, "lds");
#define TDSLOT 0
#define CHAIN_ASM_ATT \
  "s_mov_b32 s95, 272\n" \
  "s_mov_b32 s91, 0x40000001\n" \
  "s_mov_b32 s84, 0x4b400000\n" \
  "s_mov_b32 s85, 0x4b400000\n" \
  "s_mov_b32 s78, 0x3e0293ee\n" \
  "s_mov_b32 s76, 0x33333333\n" \
  "s_mov_b32 s77, 0x33333333\n" \
  "s_add_u32 s58, s56, s60\n" \
  "s_addc_u32 s59, s57, 0\n" \
  "s_lshl_b32 s98, s73, 10\n" \
  "s_add_u32 s98, s98, s74\n" \
  "s_add_u32 s98, s98, 131072\n" \
  "s_lshl_b32 s97, s73, 9\n" \
  "s_add_u32 s97, s97, s74\n" \
  "s_add_u32 s97, s97, 158720\n" \
  "s_lshl_b32 s99, s73, 10\n" \
  "v_mbcnt_lo_u32_b32 v48, -1, 0\n" \
  "v_mbcnt_hi_u32_b32 v48, -1, v48\n" \
  "s_add_u32 s88, s74, 157760\n" \
  "v_mov_b32 v59, s88\n" \
  "v_lshrrev_b32 v49, 2, v48\n" \
  "v_lshrrev_b32 v50, 4, v48\n" \
  "v_bitop3_b32 v50, v49, v50, 3 bitop3:0x28\n" \
  "v_lshl_add_u32 v8, v50, 3, s97\n" \
  "v_and_b32 v50, 3, v48\n" \
  "v_lshl_add_u32 v8, v50, 5, v8\n" \
  "v_and_b32 v49, 1, v49\n" \
  "v_lshl_add_u32 v8, v49, 8, v8\n" \
  "v_and_b32 v49, 8, v48\n" \
  "v_lshl_add_u32 v8, v49, 4, v8\n" \
  "v_lshrrev_b32 v49, 5, v48\n" \
  "v_add_u32 v10, s99, v1\n" \
  "s_lshr_b32 s88, s99, 2\n" \
  "s_add_u32 s88, s88, s74\n" \
  "s_add_u32 s88, s88, 139264\n" \
  "v_lshl_add_u32 v7, v49, 10, s88\n" \
  "v_and_b32 v49, 31, v48\n" \
  "v_lshl_add_u32 v7, v49, 3, v7\n" \
  "v_mov_b64 v[12:13], 0\n" \
  "v_mov_b64 v[14:15], 0\n" \
  "v_mov_b64 v[16:17], 0\n" \
  "v_mov_b64 v[18:19], 0\n" \
  "v_mov_b64 v[20:21], 0\n" \
  "v_mov_b64 v[22:23], 0\n" \
  "v_mov_b32 v24, 0x44870000\n" \
  "v_mul_f32 v24, s72, v24\n" \
  "s_nop 0\n" \
  "v_readfirstlane_b32 s86, v24\n" \
  "s_lshr_b32 s86, s86, 23\n" \
  "s_addk_i32 s86, 0xff83\n" \
  "s_ashr_i32 s86, s86, 1\n" \
  "s_max_i32 s86, s86, -20\n" \
  "s_min_i32 s86, s86, 100\n" \
  "s_mov_b32 s82, s86\n" \
  "s_lshr_b32 s83, s72, 23\n" \
  "s_sub_i32 s83, s83, 242\n" \
  "s_lshl_b32 s88, s73, 10\n" \
  "v_add_u32 v24, s88, v1\n" \
  "ds_read_b64 v[32:33], v24 offset:0\n" \
  "ds_read_b64 v[36:37], v24 offset:8\n" \
  "s_waitcnt lgkmcnt(0)\n" \
  "v_mov_b32 v11, 0\n" \
  "v_dot2c_f32_bf16 v11, v32, v32\n" \
  "v_dot2c_f32_bf16 v11, v33, v33\n" \
  "v_dot2c_f32_bf16 v11, v36, v36\n" \
  "v_dot2c_f32_bf16 v11, v37, v37\n" \
  "s_nop 1\n" \
  "s_nop 0\n" \
  "v_cmpx_lt_f32 vcc, 2.0, v11\n" \
  "ds_max_u32 v59, v11\n" \
  "s_mov_b64 exec, -1\n" \
  ".LCA_TOP_%=:\n" \
  "s_waitcnt lgkmcnt(0)\n" \
  "s_barrier\n" \
  "ds_read_b128 v[40:43], v1 offset:0\n" \
  "ds_read_b128 v[44:47], v1 offset:1024\n" \
  "ds_read_b128 v[48:51], v1 offset:2048\n" \
  "ds_read_b128 v[52:55], v1 offset:3072\n" \
  "ds_read_b128 v[56:59], v1 offset:4096\n" \
  "ds_read_b128 v[60:63], v1 offset:5120\n" \
  "s_cmp_lg_u32 s73, 0\n" \
  "s_cbranch_scc1 .LCA_NOZ_%=\n" \
  "s_xor_b32 s88, s60, 0x8000\n" \
  "s_add_u32 s88, s88, s61\n" \
  "buffer_store_dwordx2 v[12:13], v5, s[40:43], s88 offen sc1\n" \
  ".LCA_NOZ_%=:\n" \
  "s_waitcnt lgkmcnt(5)\n" \
  "v_smfmac_f32_16x16x64_bf16 v[20:23], v[40:43], v[192:199], v0\n" \
  "v_smfmac_f32_16x16x64_bf16 v[12:15], v[40:43], v[64:71], v0\n" \
  "v_smfmac_f32_16x16x64_bf16 v[16:19], v[40:43], v[128:135], v0\n" \
  "ds_read_b128 v[40:43], v1 offset:6144\n" \
  "s_waitcnt lgkmcnt(5)\n" \
  "v_smfmac_f32_16x16x64_bf16 v[20:23], v[44:47], v[200:207], v0\n" \
  "v_smfmac_f32_16x16x64_bf16 v[12:15], v[44:47], v[72:79], v0\n" \
  "v_smfmac_f32_16x16x64_bf16 v[16:19], v[44:47], v[136:143], v0\n" \
  "ds_read_b128 v[44:47], v1 offset:7168\n" \
  "s_waitcnt lgkmcnt(5)\n" \
  "v_smfmac_f32_16x16x64_bf16 v[20:23], v[48:51], v[208:215], v0\n" \
  "v_smfmac_f32_16x16x64_bf16 v[12:15], v[48:51], v[80:87], v0\n" \
  "v_smfmac_f32_16x16x64_bf16 v[16:19], v[48:51], v[144:151], v0\n" \
  "ds_read_b128 v[24:27], v1 offset:10240\n" \
  "ds_read_b128 v[28:31], v1 offset:14336\n" \
  "s_waitcnt lgkmcnt(6)\n" \
  "v_smfmac_f32_16x16x64_bf16 v[20:23], v[52:55], v[216:223], v0\n" \
  "v_smfmac_f32_16x16x64_bf16 v[12:15], v[52:55], v[88:95], v0\n" \
  "v_smfmac_f32_16x16x64_bf16 v[16:19], v[52:55], v[152:159], v0\n" \
  "ds_read_b128 v[32:35], v1 offset:11264\n" \
  "ds_read_b128 v[36:39], v1 offset:15360\n" \
  "s_waitcnt lgkmcnt(7)\n" \
  "v_smfmac_f32_16x16x64_bf16 v[20:23], v[56:59], v[224:231], v0\n" \
  "v_smfmac_f32_16x16x64_bf16 v[12:15], v[56:59], v[96:103], v0\n" \
  "v_smfmac_f32_16x16x64_bf16 v[16:19], v[56:59], v[160:167], v0\n" \
  "ds_read_b128 v[56:59], v1 offset:12288\n" \
  "s_waitcnt lgkmcnt(7)\n" \
  "v_smfmac_f32_16x16x64_bf16 v[20:23], v[60:63], v[232:239], v0\n" \
  "v_smfmac_f32_16x16x64_bf16 v[12:15], v[60:63], v[104:111], v0\n" \
  "v_smfmac_f32_16x16x64_bf16 v[16:19], v[60:63], v[168:175], v0\n" \
  "ds_read_b128 v[60:63], v1 offset:16384\n" \
  "s_waitcnt lgkmcnt(7)\n" \
  "v_smfmac_f32_16x16x64_bf16 v[20:23], v[40:43], v[240:247], v0\n" \
  "v_smfmac_f32_16x16x64_bf16 v[12:15], v[40:43], v[112:119], v0\n" \
  "v_smfmac_f32_16x16x64_bf16 v[16:19], v[40:43], v[176:183], v0\n" \
  "s_waitcnt lgkmcnt(6)\n" \
  "v_smfmac_f32_16x16x64_bf16 v[20:23], v[44:47], v[248:255], v0\n" \
  "v_smfmac_f32_16x16x64_bf16 v[12:15], v[44:47], v[120:127], v0\n" \
  "v_smfmac_f32_16x16x64_bf16 v[16:19], v[44:47], v[184:191], v0\n" \
  "s_cmp_eq_u32 s71, 0\n" \
  "v_permlane32_swap_b32 v20, v22\n" \
  "v_permlane32_swap_b32 v21, v23\n" \
  "v_pk_add_f32 v[20:21], v[20:21], v[22:23]\n" \
  "s_mov_b64 vcc, s[76:77]\n" \
  "v_permlane32_swap_b32 v12, v14\n" \
  "v_permlane32_swap_b32 v13, v15\n" \
  "v_cvt_pk_bf16_f32 v20, v20, v21\n" \
  "v_pk_add_f32 v[12:13], v[12:13], v[14:15]\n" \
  "v_permlane32_swap_b32 v16, v18\n" \
  "v_permlane32_swap_b32 v17, v19\n" \
  "ds_write_b32 v8, v20 offset:2048\n" \
  "v_cvt_pk_bf16_f32 v12, v12, v13\n" \
  "v_pk_add_f32 v[16:17], v[16:17], v[18:19]\n" \
  "s_nop 0\n" \
  "v_cvt_pk_bf16_f32 v13, v16, v17\n" \
  "ds_write_b64 v8, v[12:13]\n" \
  "s_setprio 1\n" \
  "s_waitcnt lgkmcnt(0)\n" \
  "s_barrier\n" \
  "s_cbranch_scc1 .LCA_LAST_%=\n" \
  "ds_read_b64_tr_b16 v[40:41], v2 offset:0\n" \
  "ds_read_b64_tr_b16 v[42:43], v3 offset:0\n" \
  "ds_read_b64_tr_b16 v[44:45], v2 offset:1024\n" \
  "ds_read_b64_tr_b16 v[46:47], v3 offset:1024\n" \
  "ds_read_b64_tr_b16 v[48:49], v2 offset:2048\n" \
  "ds_read_b64_tr_b16 v[50:51], v3 offset:2048\n" \
  "ds_read_b64_tr_b16 v[52:53], v2 offset:3072\n" \
  "ds_read_b64_tr_b16 v[54:55], v3 offset:3072\n" \
  "ds_read_b32 v11, v1 offset:26688\n" \
  "s_waitcnt lgkmcnt(7)\n" \
  "v_mfma_f32_16x16x32_bf16 v[12:15], v[24:27], v[40:43], 0\n" \
  "v_mfma_f32_16x16x32_bf16 v[16:19], v[28:31], v[40:43], 0\n" \
  "ds_read_b128 v[24:27], v1 offset:13312\n" \
  "ds_read_b128 v[28:31], v1 offset:17408\n" \
  "v_mfma_f32_16x16x32_bf16 v[20:23], v[40:43], v[40:43], 0\n" \
  "s_waitcnt lgkmcnt(7)\n" \
  "v_mfma_f32_16x16x32_bf16 v[12:15], v[32:35], v[44:47], v[12:15]\n" \
  "v_mfma_f32_16x16x32_bf16 v[16:19], v[36:39], v[44:47], v[16:19]\n" \
  "v_mfma_f32_16x16x32_bf16 v[20:23], v[44:47], v[44:47], v[20:23]\n" \
  "s_waitcnt lgkmcnt(2)\n" \
  "v_mfma_f32_16x16x32_bf16 v[12:15], v[56:59], v[48:51], v[12:15]\n" \
  "v_readfirstlane_b32 s86, v11\n" \
  "v_mfma_f32_16x16x32_bf16 v[16:19], v[60:63], v[48:51], v[16:19]\n" \
  "ds_read_b128 v[32:35], v6 offset:8192\n" \
  "ds_read_b128 v[36:39], v6 offset:9216\n" \
  "v_mfma_f32_16x16x32_bf16 v[20:23], v[48:51], v[48:51], v[20:23]\n" \
  "s_lshr_b32 s86, s86, 23\n" \
  "s_add_i32 s86, s86, s83\n" \
  "s_ashr_i32 s86, s86, 1\n" \
  "ds_read_b128 v[48:51], v10 offset:18432\n" \
  "v_mfma_f32_16x16x32_bf16 v[20:23], v[52:55], v[52:55], v[20:23]\n" \
  "s_max_i32 s86, s86, s82\n" \
  "s_min_i32 s86, s86, 100\n" \
  "s_lshl_b32 s86, s86, 23\n" \
  "s_sub_i32 s84, 0x4a800000, s86\n" \
  "s_waitcnt lgkmcnt(3)\n" \
  "v_mfma_f32_16x16x32_bf16 v[12:15], v[24:27], v[52:55], v[12:15]\n" \
  "v_mfma_f32_16x16x32_bf16 v[16:19], v[28:31], v[52:55], v[16:19]\n" \
  "ds_read_b128 v[52:55], v10 offset:22528\n" \
  "ds_read_b128 v[56:59], v6 offset:0\n" \
  "ds_read_b128 v[60:63], v6 offset:1024\n" \
  "ds_read_b128 v[24:27], v6 offset:2048\n" \
  "ds_read_b128 v[28:31], v6 offset:3072\n" \
  "v_cndmask_b32_dpp v20, v23, v22, vcc quad_perm:[2,3,0,1] row_mask:0xf bank_mask:0xf\n" \
  "v_mov_b32 v41, 0\n" \
  "v_mov_b32 v42, 0\n" \
  "v_cndmask_b32_dpp v12, v16, v12, vcc quad_perm:[2,3,0,1] row_mask:0xf bank_mask:0xf\n" \
  "v_cndmask_b32_dpp v13, v17, v13, vcc quad_perm:[2,3,0,1] row_mask:0xf bank_mask:0xf\n" \
  "v_cndmask_b32_dpp v14, v18, v14, vcc quad_perm:[2,3,0,1] row_mask:0xf bank_mask:0xf\n" \
  "v_cndmask_b32_dpp v15, v19, v15, vcc quad_perm:[2,3,0,1] row_mask:0xf bank_mask:0xf\n" \
  "v_maximum3_f32 v16, v20, v12, v13\n" \
  "v_maximum3_f32 v16, v16, v14, v15\n" \
  "s_nop 1\n" \
  "v_max_f32_dpp v16, v16, v16 quad_perm:[2,3,0,1] row_mask:0xf bank_mask:0xf\n" \
  "v_mov_b32 v17, v16\n" \
  "s_nop 1\n" \
  "v_permlane16_swap_b32 v16, v17\n" \
  "v_max_f32 v16, v16, v17\n" \
  "v_mov_b32 v17, v16\n" \
  "s_nop 1\n" \
  "v_permlane32_swap_b32 v16, v17\n" \
  "v_max_f32 v16, v16, v17\n" \
  "v_mul_f32 v16, s78, v16\n" \
  "v_pk_fma_f32 v[12:13], v[12:13], s[78:79], v[16:17] op_sel_hi:[1,0,0] neg_lo:[0,0,1] neg_hi:[0,0,1]\n" \
  "v_pk_fma_f32 v[14:15], v[14:15], s[78:79], v[16:17] op_sel_hi:[1,0,0] neg_lo:[0,0,1] neg_hi:[0,0,1]\n" \
  "v_fma_f32 v20, v20, s78, -v16\n" \
  "v_exp_f32 v12, v12\n" \
  "v_exp_f32 v13, v13\n" \
  "v_exp_f32 v14, v14\n" \
  "v_exp_f32 v15, v15\n" \
  "v_exp_f32 v20, v20\n" \
  "s_waitcnt lgkmcnt(4)\n" \
  "v_cvt_pk_bf16_f32 v44, v12, v13\n" \
  "v_cvt_pk_bf16_f32 v45, v14, v15\n" \
  "v_pk_add_f32 v[8:9], v[12:13], v[14:15]\n" \
  "v_mov_b32_dpp v46, v44 quad_perm:[2,3,0,1] row_mask:0xf bank_mask:0xf\n" \
  "v_mov_b32_dpp v47, v45 quad_perm:[2,3,0,1] row_mask:0xf bank_mask:0xf\n" \
  "v_add_f32 v10, v8, v20\n" \
  "v_add_f32 v10, v10, v9\n" \
  "v_mfma_f32_16x16x32_bf16 v[12:15], v[48:51], v[44:47], 0\n" \
  "v_mov_b32_dpp v21, v20 quad_perm:[2,3,0,1] row_mask:0xf bank_mask:0xf\n" \
  "v_add_f32_dpp v10, v10, v10 quad_perm:[2,3,0,1] row_mask:0xf bank_mask:0xf\n" \
  "v_mov_b32 v11, v10\n" \
  "v_mfma_f32_16x16x32_bf16 v[16:19], v[52:55], v[44:47], 0\n" \
  "s_setprio 3\n" \
  "s_barrier\n" \
  "ds_read_b64 v[22:23], v7 offset:0\n" \
  "s_nop 0\n" \
  "v_permlane16_swap_b32 v10, v11\n" \
  "v_add_f32 v10, v10, v11\n" \
  "v_mov_b32 v11, v10\n" \
  "v_mov_b64 v[8:9], 0\n" \
  "s_nop 0\n" \
  "v_permlane32_swap_b32 v10, v11\n" \
  "v_add_f32 v10, v10, v11\n" \
  "v_rcp_f32 v10, v10\n" \
  "s_nop 0\n" \
  "v_pk_mul_f32 v[20:21], v[20:21], v[10:11] op_sel_hi:[1,0]\n" \
  "v_pk_mul_f32 v[12:13], v[12:13], v[10:11] op_sel_hi:[1,0]\n" \
  "v_pk_mul_f32 v[14:15], v[14:15], v[10:11] op_sel_hi:[1,0]\n" \
  "v_cvt_pk_bf16_f32 v40, v20, v21\n" \
  "v_pk_mul_f32 v[16:17], v[16:17], v[10:11] op_sel_hi:[1,0]\n" \
  "v_cvt_pk_bf16_f32 v43, v20, v21\n" \
  "v_pk_mul_f32 v[18:19], v[18:19], v[10:11] op_sel_hi:[1,0]\n" \
  "s_waitcnt lgkmcnt(0)\n" \
  "v_mfma_f32_16x16x16_bf16 v[12:15], v[22:23], v[40:41], v[12:15]\n" \
  "v_mfma_f32_16x16x16_bf16 v[16:19], v[22:23], v[42:43], v[16:19]\n" \
  "v_mov_b64 v[10:11], 0\n" \
  "ds_read_b128 v[48:51], v6 offset:10240\n" \
  "ds_read_b128 v[52:55], v6 offset:11264\n" \
  "v_mov_b64 v[20:21], 0\n" \
  "v_mov_b64 v[22:23], 0\n" \
  "s_nop 1\n" \
  "v_cvt_pk_bf16_f32 v40, v12, v13\n" \
  "v_cvt_pk_bf16_f32 v41, v14, v15\n" \
  "v_cvt_pk_bf16_f32 v42, v16, v17\n" \
  "ds_write_b64 v4, v[40:41]\n" \
  "v_cvt_pk_bf16_f32 v43, v18, v19\n" \
  "ds_write_b64 v4, v[42:43] offset:1024\n" \
  "v_mov_b64 v[12:13], 0\n" \
  "v_mov_b64 v[14:15], 0\n" \
  "v_mov_b64 v[16:17], 0\n" \
  "v_mov_b64 v[18:19], 0\n" \
  "s_setprio 0\n" \
  "s_waitcnt lgkmcnt(0)\n" \
  "s_barrier\n" \
  "ds_read_b128 v[40:43], v1 offset:8192\n" \
  "ds_read_b128 v[44:47], v1 offset:9216\n" \
  "s_waitcnt lgkmcnt(1)\n" \
  "v_smfmac_f32_16x16x64_bf16 v[12:15], v[40:43], v[56:63], v0\n" \
  "ds_read_b128 v[56:59], v6 offset:4096\n" \
  "ds_read_b128 v[60:63], v6 offset:5120\n" \
  "v_smfmac_f32_16x16x64_bf16 v[16:19], v[40:43], v[24:31], v0\n" \
  "ds_read_b128 v[24:27], v6 offset:6144\n" \
  "ds_read_b128 v[28:31], v6 offset:7168\n" \
  "s_waitcnt lgkmcnt(4)\n" \
  "v_smfmac_f32_16x16x64_bf16 v[12:15], v[44:47], v[32:39], v0\n" \
  "ds_read_b128 v[32:35], v6 offset:12288\n" \
  "ds_read_b128 v[36:39], v6 offset:13312\n" \
  "v_smfmac_f32_16x16x64_bf16 v[16:19], v[44:47], v[48:55], v0\n" \
  "ds_read_b128 v[48:51], v6 offset:14336\n" \
  "ds_read_b128 v[52:55], v6 offset:15360\n" \
  "s_nop 5\n" \
  "v_permlane32_swap_b32 v12, v17\n" \
  "v_permlane32_swap_b32 v13, v16\n" \
  "v_permlane32_swap_b32 v14, v19\n" \
  "v_permlane32_swap_b32 v15, v18\n" \
  "v_pk_add_f32 v[12:13], v[12:13], v[16:17] op_sel:[0,1] op_sel_hi:[1,0]\n" \
  "v_pk_add_f32 v[14:15], v[14:15], v[18:19] op_sel:[0,1] op_sel_hi:[1,0]\n" \
  "v_pk_fma_f32 v[12:13], v[12:13], s[84:85], s[84:85] op_sel:[0,0,1] op_sel_hi:[1,0,1]\n" \
  "v_pk_fma_f32 v[14:15], v[14:15], s[84:85], s[84:85] op_sel:[0,0,1] op_sel_hi:[1,0,1]\n" \
  "v_lshl_add_u32 v12, v12, 6, s91\n" \
  "v_lshl_add_u32 v13, v13, 2, s75\n" \
  "global_atomic_add_x2 v5, v[12:13], s[58:59] offset:0\n" \
  "v_lshl_add_u32 v14, v14, 6, s91\n" \
  "v_lshl_add_u32 v15, v15, 2, s75\n" \
  "global_atomic_add_x2 v5, v[14:15], s[58:59] offset:2048\n" \
  "s_waitcnt lgkmcnt(6)\n" \
  "v_smfmac_f32_16x16x64_bf16 v[20:23], v[40:43], v[56:63], v0\n" \
  "s_waitcnt lgkmcnt(4)\n" \
  "v_smfmac_f32_16x16x64_bf16 v[8:11], v[40:43], v[24:31], v0\n" \
  "s_waitcnt lgkmcnt(2)\n" \
  "v_smfmac_f32_16x16x64_bf16 v[20:23], v[44:47], v[32:39], v0\n" \
  "s_waitcnt lgkmcnt(0)\n" \
  "v_smfmac_f32_16x16x64_bf16 v[8:11], v[44:47], v[48:55], v0\n" \
  "s_nop 7\n" \
  "v_permlane32_swap_b32 v20, v9\n" \
  "v_permlane32_swap_b32 v21, v8\n" \
  "v_permlane32_swap_b32 v22, v11\n" \
  "v_permlane32_swap_b32 v23, v10\n" \
  "v_pk_add_f32 v[20:21], v[20:21], v[8:9] op_sel:[0,1] op_sel_hi:[1,0]\n" \
  "v_pk_add_f32 v[22:23], v[22:23], v[10:11] op_sel:[0,1] op_sel_hi:[1,0]\n" \
  "v_pk_fma_f32 v[20:21], v[20:21], s[84:85], s[84:85] op_sel:[0,0,1] op_sel_hi:[1,0,1]\n" \
  "v_pk_fma_f32 v[22:23], v[22:23], s[84:85], s[84:85] op_sel:[0,0,1] op_sel_hi:[1,0,1]\n" \
  "v_lshl_add_u32 v20, v20, 6, s91\n" \
  "v_lshl_add_u32 v21, v21, 2, s75\n" \
  "global_atomic_add_x2 v5, v[20:21], s[58:59] offset:3072\n" \
  "v_lshl_add_u32 v22, v22, 6, s91\n" \
  "v_lshl_add_u32 v23, v23, 2, s75\n" \
  "global_atomic_add_x2 v5, v[22:23], s[58:59] offset:1024\n" \
  "s_memrealtime s[38:39]\n" \
  "v_mbcnt_lo_u32_b32 v48, -1, 0\n" \
  "v_mbcnt_hi_u32_b32 v48, -1, v48\n" \
  "s_add_u32 s88, s74, 157760\n" \
  "v_mov_b32 v59, s88\n" \
  "v_lshl_add_u32 v52, v48, 2, v59\n" \
  "ds_read_b32 v52, v52 offset:192\n" \
  "v_lshrrev_b32 v49, 3, v48\n" \
  "v_and_b32 v50, 7, v48\n" \
  "v_lshlrev_b32 v50, 4, v50\n" \
  "v_lshl_or_b32 v44, v49, 8, v50\n" \
  "s_mov_b32 s87, 0x40000\n" \
  "v_lshrrev_b32 v49, 2, v48\n" \
  "v_lshrrev_b32 v50, 4, v48\n" \
  "v_bitop3_b32 v50, v49, v50, 3 bitop3:0x28\n" \
  "v_lshl_add_u32 v8, v50, 3, s97\n" \
  "v_and_b32 v50, 3, v48\n" \
  "v_lshl_add_u32 v8, v50, 5, v8\n" \
  "v_and_b32 v49, 1, v49\n" \
  "v_lshl_add_u32 v8, v49, 8, v8\n" \
  "v_and_b32 v49, 8, v48\n" \
  "v_lshl_add_u32 v8, v49, 4, v8\n" \
  "v_lshrrev_b32 v49, 5, v48\n" \
  "v_add_u32 v10, s99, v1\n" \
  "s_lshr_b32 s88, s99, 2\n" \
  "s_add_u32 s88, s88, s74\n" \
  "s_add_u32 s88, s88, 139264\n" \
  "v_lshl_add_u32 v7, v49, 10, s88\n" \
  "v_and_b32 v49, 31, v48\n" \
  "v_lshl_add_u32 v7, v49, 3, v7\n" \
  "v_mov_b64 v[12:13], 0\n" \
  "v_mov_b64 v[14:15], 0\n" \
  "v_mov_b64 v[16:17], 0\n" \
  "v_mov_b64 v[18:19], 0\n" \
  "v_mov_b64 v[20:21], 0\n" \
  "v_mov_b64 v[22:23], 0\n" \
  "v_and_b32 v50, 3, v48\n" \
  "v_lshl_add_u32 v40, v50, 2, s98\n" \
  "v_lshrrev_b32 v50, 4, v48\n" \
  "v_mad_u32_u24 v40, v50, s95, v40\n" \
  "v_bfe_u32 v49, v48, 3, 1\n" \
  "v_lshl_add_u32 v40, v49, 6, v40\n" \
  "v_bfe_u32 v49, v48, 2, 1\n" \
  "v_lshl_add_u32 v40, v49, 7, v40\n" \
  "v_xor_b32 v41, 16, v40\n" \
  "v_xor_b32 v42, 32, v40\n" \
  "v_xor_b32 v43, 48, v40\n" \
  "s_add_i32 s80, s86, 0x32800000\n" \
  "s_add_i32 s81, s86, 0x33800000\n" \
  "s_add_i32 s88, s86, 0xb4000000\n" \
  "v_mov_b32 v46, s88\n" \
  "v_mov_b32 v47, 0\n" \
  "v_mov_b32 v11, 0\n" \
  "s_mov_b32 s89, s60\n" \
  "s_xor_b32 s60, s60, 0x80\n" \
  "s_bitcmp0_b32 s60, 7\n" \
  "s_cselect_b32 s88, 0x8000, 0\n" \
  "s_add_u32 s60, s60, s88\n" \
  "s_and_b32 s60, s60, 0xffff\n" \
  "s_add_u32 s58, s56, s60\n" \
  "s_addc_u32 s59, s57, 0\n" \
  "s_sub_u32 s71, s71, 1\n" \
  "s_waitcnt lgkmcnt(0)\n" \
  "s_sub_u32 s39, s38, 150\n" \
  "v_subrev_u32 v52, s39, v52\n" \
  "v_cmp_lt_u32 vcc, 0x1000, v52\n" \
  "s_cmp_eq_u64 vcc, 0\n" \
  "s_cbranch_scc1 .LCA_OKM_%=\n" \
  ".LCA_AGN_%=:\n" \
  "buffer_load_dword v52, v5, s[0:3], s66 offen sc1\n" \
  "s_sub_u32 s87, s87, 1\n" \
  "s_cbranch_scc1 .LCA_END_%=\n" \
  "s_waitcnt vmcnt(0)\n" \
  "v_cmp_eq_u32 vcc, 0, v52\n" \
  "v_subrev_u32 v52, s39, v52\n" \
  "s_cmp_eq_u64 vcc, 0\n" \
  "s_cbranch_scc1 .LCA_OKM_%=\n" \
  "s_sleep 4\n" \
  "s_branch .LCA_AGN_%=\n" \
  ".LCA_OKM_%=:\n" \
  "s_nop 1\n" \
  "v_max_i32_dpp v52, v52, v52 row_ror:1 row_mask:0xf bank_mask:0xf\n" \
  "s_nop 1\n" \
  "v_max_i32_dpp v52, v52, v52 row_ror:2 row_mask:0xf bank_mask:0xf\n" \
  "s_nop 1\n" \
  "v_max_i32_dpp v52, v52, v52 row_ror:4 row_mask:0xf bank_mask:0xf\n" \
  "s_movk_i32 vcc_lo, 150\n" \
  "v_readlane_b32 s88, v52, 0\n" \
  "s_max_i32 vcc_lo, vcc_lo, s88\n" \
  "v_readlane_b32 s88, v52, 16\n" \
  "s_max_i32 vcc_lo, vcc_lo, s88\n" \
  "v_readlane_b32 s88, v52, 32\n" \
  "s_max_i32 vcc_lo, vcc_lo, s88\n" \
  "v_readlane_b32 s88, v52, 48\n" \
  "s_max_i32 vcc_lo, vcc_lo, s88\n" \
  "s_add_u32 s66, s66, 1024\n" \
  "s_add_u32 vcc_lo, vcc_lo, s39\n" \
  "s_add_u32 vcc_lo, vcc_lo, 30\n" \
  ".LCA_TW_%=:\n" \
  "s_memrealtime s[38:39]\n" \
  "s_waitcnt lgkmcnt(0)\n" \
  "s_sub_u32 s38, s38, vcc_lo\n" \
  "s_cmp_lt_i32 s38, 0\n" \
  "s_cbranch_scc1 .LCA_TW_%=\n" \
  "buffer_load_dwordx4 v[24:27], v44, s[40:43], s89 offen sc1\n" \
  "buffer_load_dwordx4 v[28:31], v44, s[40:43], s89 offen offset:2048 sc1\n" \
  ".LCA_SEE_%=:\n" \
  "s_waitcnt vmcnt(1)\n" \
  "v_bitop3_b32 v45, v24, v26, 32 bitop3:0x80\n" \
  "v_alignbit_b32 v32, v25, v24, 2\n" \
  "v_alignbit_b32 v34, v27, v26, 2\n" \
  "s_waitcnt vmcnt(0)\n" \
  "v_bitop3_b32 v45, v45, v28, v30 bitop3:0x80\n" \
  "v_cmp_eq_u32 vcc, 0, v45\n" \
  "v_cvt_f32_i32 v32, v32\n" \
  "v_cvt_f32_i32 v33, v25\n" \
  "s_cmp_eq_u64 vcc, 0\n" \
  "s_cbranch_scc0 .LCA_POLL_%=\n" \
  "v_cvt_f32_i32 v34, v34\n" \
  "v_cvt_f32_i32 v35, v27\n" \
  "v_pk_fma_f32 v[32:33], v[32:33], s[80:81], v[46:47]\n" \
  "v_alignbit_b32 v36, v29, v28, 2\n" \
  "v_pk_fma_f32 v[34:35], v[34:35], s[80:81], v[46:47]\n" \
  "v_alignbit_b32 v38, v31, v30, 2\n" \
  "v_cvt_pk_bf16_f32 v32, v32, v34\n" \
  "v_cvt_pk_bf16_f32 v33, v33, v35\n" \
  "v_cvt_f32_i32 v36, v36\n" \
  "ds_write_b32 v40, v32\n" \
  "v_cvt_f32_i32 v37, v29\n" \
  "ds_write_b32 v41, v33\n" \
  "v_cvt_f32_i32 v38, v38\n" \
  "v_cvt_f32_i32 v39, v31\n" \
  "v_pk_fma_f32 v[36:37], v[36:37], s[80:81], v[46:47]\n" \
  "v_pk_fma_f32 v[38:39], v[38:39], s[80:81], v[46:47]\n" \
  "v_dot2c_f32_bf16 v11, v32, v32\n" \
  "v_cvt_pk_bf16_f32 v36, v36, v38\n" \
  "v_cvt_pk_bf16_f32 v37, v37, v39\n" \
  "ds_write_b32 v42, v36\n" \
  "ds_write_b32 v43, v37\n" \
  "v_dot2c_f32_bf16 v11, v33, v33\n" \
  "v_dot2c_f32_bf16 v11, v36, v36\n" \
  "v_dot2c_f32_bf16 v11, v37, v37\n" \
  "s_nop 1\n" \
  "s_nop 0\n" \
  "v_cmpx_lt_f32 vcc, 2.0, v11\n" \
  "ds_max_u32 v59, v11\n" \
  "s_mov_b64 exec, -1\n" \
  "s_branch .LCA_TOP_%=\n" \
  ".LCA_POLL_%=:\n" \
  "buffer_load_dwordx4 v[24:27], v44, s[40:43], s89 offen sc1\n" \
  "buffer_load_dwordx4 v[28:31], v44, s[40:43], s89 offen offset:2048 sc1\n" \
  "s_sub_u32 s87, s87, 1\n" \
  "s_cbranch_scc1 .LCA_END_%=\n" \
  "s_branch .LCA_SEE_%=\n" \
  ".LCA_LAST_%=:\n" \
  ".LCA_END_%=:\n" \
  "s_endpgm\n"
#define CHAIN_ASM_VO \
  "s_mov_b32 s95, 272\n" \
  "s_mov_b32 s91, 0x40000001\n" \
  "s_mov_b32 s84, 0x4b400000\n" \
  "s_mov_b32 s85, 0x4b400000\n" \
  "s_mov_b32 s78, 0x3e0293ee\n" \
  "s_mov_b32 s76, 0x33333333\n" \
  "s_mov_b32 s77, 0x33333333\n" \
  "s_add_u32 s58, s56, s60\n" \
  "s_addc_u32 s59, s57, 0\n" \
  "s_lshl_b32 s98, s73, 10\n" \
  "s_add_u32 s98, s98, s74\n" \
  "s_add_u32 s98, s98, 131072\n" \
  "s_lshl_b32 s97, s73, 9\n" \
  "s_add_u32 s97, s97, s74\n" \
  "s_add_u32 s97, s97, 158724\n" \
  "s_lshl_b32 s99, s73, 11\n" \
  "s_add_u32 s99, s99, s74\n" \
  "s_add_u32 s99, s99, 133120\n" \
  "s_lshl_b32 s94, s73, 4\n" \
  "s_add_u32 s94, s94, s74\n" \
  "s_add_u32 s94, s94, 157760\n" \
  "v_mbcnt_lo_u32_b32 v48, -1, 0\n" \
  "v_mbcnt_hi_u32_b32 v48, -1, v48\n" \
  "s_add_u32 s88, s74, 157760\n" \
  "v_mov_b32 v59, s88\n" \
  "s_sub_u32 s88, s73, 4\n" \
  "v_lshrrev_b32 v49, 2, v48\n" \
  "v_lshrrev_b32 v50, 4, v48\n" \
  "v_bitop3_b32 v50, v49, v50, 3 bitop3:0x28\n" \
  "v_lshl_add_u32 v4, v50, 3, s97\n" \
  "v_and_b32 v50, 3, v48\n" \
  "v_lshl_add_u32 v4, v50, 5, v4\n" \
  "v_and_b32 v49, 1, v49\n" \
  "v_lshl_add_u32 v4, v49, 8, v4\n" \
  "v_and_b32 v49, 8, v48\n" \
  "v_lshl_add_u32 v4, v49, 4, v4\n" \
  "v_and_b32 v51, 15, v48\n" \
  "v_bfe_u32 v49, v48, 4, 1\n" \
  "v_lshrrev_b32 v50, 5, v48\n" \
  "s_lshl_b32 s89, s88, 10\n" \
  "s_add_u32 s89, s89, s74\n" \
  "s_add_u32 s89, s89, 158720\n" \
  "v_mul_u32_u24 v6, 18, v49\n" \
  "v_xor_b32 v6, v6, v51\n" \
  "v_lshl_add_u32 v6, v6, 3, s89\n" \
  "v_lshl_add_u32 v6, v50, 9, v6\n" \
  "v_xor_b32 v9, 0x108, v6\n" \
  "v_lshlrev_b32 v8, 4, v49\n" \
  "v_lshl_add_u32 v8, v51, 5, v8\n" \
  "v_lshl_add_u32 v8, v50, 3, v8\n" \
  "s_lshl_b32 s89, s88, 8\n" \
  "s_add_u32 s89, s89, s74\n" \
  "s_add_u32 s89, s89, 139264\n" \
  "v_and_b32 v49, 31, v48\n" \
  "v_lshl_add_u32 v5, v49, 3, s89\n" \
  "v_lshl_add_u32 v5, v50, 10, v5\n" \
  "v_lshrrev_b32 v49, 2, v51\n" \
  "v_and_b32 v50, 5, v51\n" \
  "v_and_or_b32 v49, v49, 2, v50\n" \
  "v_and_b32 v50, 48, v48\n" \
  "v_lshlrev_b32 v7, 3, v50\n" \
  "v_lshl_add_u32 v7, v49, 4, v7\n" \
  "v_bitop3_b32 v49, v51, 2, 2 bitop3:0x0c\n" \
  "v_lshl_add_u32 v7, v49, 29, v7\n" \
  "v_mov_b64 v[12:13], 0\n" \
  "v_mov_b64 v[14:15], 0\n" \
  "v_mov_b64 v[16:17], 0\n" \
  "v_mov_b64 v[18:19], 0\n" \
  "v_mov_b64 v[20:21], 0\n" \
  "v_mov_b64 v[22:23], 0\n" \
  "v_lshlrev_b32 v10, 4, v48\n" \
  "s_add_u32 s78, s74, 157952\n" \
  "v_lshl_add_u32 v52, v48, 2, v59\n" \
  "ds_write_b32 v52, v12 offset:192\n" \
  "s_movk_i32 s79, 105\n" \
  "v_mov_b32 v24, 0x44870000\n" \
  "v_mul_f32 v24, s72, v24\n" \
  "s_nop 0\n" \
  "v_readfirstlane_b32 s86, v24\n" \
  "s_lshr_b32 s86, s86, 23\n" \
  "s_addk_i32 s86, 0xff83\n" \
  "s_ashr_i32 s86, s86, 1\n" \
  "s_max_i32 s86, s86, -20\n" \
  "s_min_i32 s86, s86, 100\n" \
  "s_mov_b32 s82, s86\n" \
  "s_lshr_b32 s83, s72, 23\n" \
  "s_sub_i32 s83, s83, 242\n" \
  "s_lshl_b32 s88, s73, 10\n" \
  "v_add_u32 v24, s88, v1\n" \
  "ds_read_b64 v[32:33], v24 offset:0\n" \
  "ds_read_b64 v[36:37], v24 offset:8\n" \
  "s_waitcnt lgkmcnt(0)\n" \
  "v_mov_b32 v11, 0\n" \
  "v_dot2c_f32_bf16 v11, v32, v32\n" \
  "v_dot2c_f32_bf16 v11, v33, v33\n" \
  "v_dot2c_f32_bf16 v11, v36, v36\n" \
  "v_dot2c_f32_bf16 v11, v37, v37\n" \
  "s_nop 1\n" \
  "s_nop 0\n" \
  "v_cmpx_lt_f32 vcc, 2.0, v11\n" \
  "ds_max_u32 v59, v11\n" \
  "s_mov_b64 exec, -1\n" \
  "s_cmp_lg_u32 s61, 0\n" \
  "s_movk_i32 s76, -8000\n" \
  "s_cmovk_i32 s76, 9152\n" \
  "s_movk_i32 s77, 90\n" \
  "s_cmovk_i32 s77, 90\n" \
  "s_cselect_b32 s72, -1, 55\n" \
  ".LCV_TOP_%=:\n" \
  "s_waitcnt lgkmcnt(0)\n" \
  "s_barrier\n" \
  "s_setprio 3\n" \
  "ds_read_b128 v[24:27], v1 offset:0\n" \
  "ds_read_b128 v[28:31], v1 offset:1024\n" \
  "ds_read_b128 v[32:35], v1 offset:2048\n" \
  "ds_read_b128 v[36:39], v1 offset:3072\n" \
  "ds_read_b128 v[40:43], v1 offset:4096\n" \
  "ds_read_b128 v[44:47], v1 offset:5120\n" \
  "ds_read_b128 v[48:51], v1 offset:6144\n" \
  "ds_read_b128 v[52:55], v1 offset:7168\n" \
  "s_memrealtime s[38:39]\n" \
  "s_waitcnt lgkmcnt(7)\n" \
  "v_smfmac_f32_16x16x64_bf16 v[12:15], v[24:27], v[64:71], v0\n" \
  "s_waitcnt lgkmcnt(6)\n" \
  "v_smfmac_f32_16x16x64_bf16 v[12:15], v[28:31], v[72:79], v0\n" \
  "s_waitcnt lgkmcnt(5)\n" \
  "v_smfmac_f32_16x16x64_bf16 v[12:15], v[32:35], v[80:87], v0\n" \
  "s_waitcnt lgkmcnt(4)\n" \
  "v_smfmac_f32_16x16x64_bf16 v[12:15], v[36:39], v[88:95], v0\n" \
  "s_waitcnt lgkmcnt(3)\n" \
  "v_smfmac_f32_16x16x64_bf16 v[12:15], v[40:43], v[96:103], v0\n" \
  "s_waitcnt lgkmcnt(2)\n" \
  "v_smfmac_f32_16x16x64_bf16 v[12:15], v[44:47], v[104:111], v0\n" \
  "s_waitcnt lgkmcnt(1)\n" \
  "v_smfmac_f32_16x16x64_bf16 v[12:15], v[48:51], v[112:119], v0\n" \
  "s_waitcnt lgkmcnt(0)\n" \
  "v_smfmac_f32_16x16x64_bf16 v[12:15], v[52:55], v[120:127], v0\n" \
  "ds_read_b32 v11, v1 offset:26688\n" \
  "s_mov_b32 s61, s38\n" \
  "s_add_u32 s38, s38, s79\n" \
  "v_or_b32 v57, 1, s38\n" \
  "v_add_u32 v56, s67, v10\n" \
  "s_add_u32 s39, s67, s76\n" \
  "s_add_u32 s38, s38, s77\n" \
  "s_bitset1_b32 s38, 0\n" \
  "v_writelane_b32 v56, s39, 1\n" \
  "v_writelane_b32 v57, s38, 1\n" \
  "s_sub_u32 s39, s71, s72\n" \
  "s_cmp_lt_u32 s39, 9\n" \
  "s_cselect_b64 exec, 19, 3\n" \
  "buffer_store_dword v57, v56, s[0:3], 0 offen sc1\n" \
  "s_mov_b64 exec, -1\n" \
  "v_permlane32_swap_b32 v12, v14\n" \
  "v_permlane32_swap_b32 v13, v15\n" \
  "v_pk_add_f32 v[12:13], v[12:13], v[14:15]\n" \
  "s_nop 0\n" \
  "v_cvt_pk_bf16_f32 v12, v12, v13\n" \
  "ds_write_b32 v4, v12\n" \
  "s_setprio 2\n" \
  "v_mov_b64 v[12:13], 0\n" \
  "v_mov_b64 v[14:15], 0\n" \
  "s_waitcnt lgkmcnt(1)\n" \
  "v_readfirstlane_b32 s86, v11\n" \
  "s_lshr_b32 s86, s86, 23\n" \
  "s_add_i32 s86, s86, s83\n" \
  "s_ashr_i32 s86, s86, 1\n" \
  "s_max_i32 s86, s86, s82\n" \
  "s_min_i32 s86, s86, 100\n" \
  "s_lshl_b32 s86, s86, 23\n" \
  "s_sub_i32 s84, 0x4a800000, s86\n" \
  "s_cmp_eq_u32 s73, 5\n" \
  "v_smfmac_f32_16x16x64_bf16 v[16:19], v[24:27], v[128:135], v0\n" \
  "v_smfmac_f32_16x16x64_bf16 v[16:19], v[28:31], v[136:143], v0\n" \
  "v_smfmac_f32_16x16x64_bf16 v[16:19], v[32:35], v[144:151], v0\n" \
  "v_smfmac_f32_16x16x64_bf16 v[16:19], v[36:39], v[152:159], v0\n" \
  "v_smfmac_f32_16x16x64_bf16 v[16:19], v[40:43], v[160:167], v0\n" \
  "v_smfmac_f32_16x16x64_bf16 v[16:19], v[44:47], v[168:175], v0\n" \
  "s_waitcnt lgkmcnt(0)\n" \
  "s_barrier\n" \
  "ds_read_b64_tr_b16 v[60:61], v6 offset:0\n" \
  "ds_read_b64_tr_b16 v[62:63], v9 offset:0\n" \
  "v_smfmac_f32_16x16x64_bf16 v[16:19], v[48:51], v[176:183], v0\n" \
  "v_smfmac_f32_16x16x64_bf16 v[16:19], v[52:55], v[184:191], v0\n" \
  "v_smfmac_f32_16x16x64_bf16 v[20:23], v[24:27], v[192:199], v0\n" \
  "v_smfmac_f32_16x16x64_bf16 v[20:23], v[28:31], v[200:207], v0\n" \
  "v_smfmac_f32_16x16x64_bf16 v[20:23], v[32:35], v[208:215], v0\n" \
  "v_smfmac_f32_16x16x64_bf16 v[20:23], v[36:39], v[216:223], v0\n" \
  "v_smfmac_f32_16x16x64_bf16 v[20:23], v[40:43], v[224:231], v0\n" \
  "v_smfmac_f32_16x16x64_bf16 v[20:23], v[44:47], v[232:239], v0\n" \
  "v_smfmac_f32_16x16x64_bf16 v[20:23], v[48:51], v[240:247], v0\n" \
  "v_smfmac_f32_16x16x64_bf16 v[20:23], v[52:55], v[248:255], v0\n" \
  "s_cmp_eq_u32 s71, 0\n" \
  "v_permlane32_swap_b32 v16, v18\n" \
  "v_permlane32_swap_b32 v17, v19\n" \
  "v_add_f32 v16, v16, v18\n" \
  "v_add_f32 v17, v17, v19\n" \
  "v_cvt_pk_bf16_f32 v16, v16, v17\n" \
  "s_waitcnt lgkmcnt(0)\n" \
  "v_max_u32 v60, 1, v60\n" \
  "v_max_u32 v61, 1, v61\n" \
  "v_max_u32 v62, 1, v62\n" \
  "v_max_u32 v63, 1, v63\n" \
  "buffer_store_dwordx4 v[60:63], v7, s[48:51], s69 offen sc1\n" \
  "v_permlane32_swap_b32 v20, v22\n" \
  "v_permlane32_swap_b32 v21, v23\n" \
  "v_pk_add_f32 v[20:21], v[20:21], v[22:23]\n" \
  "v_max_u32 v18, 1, v16\n" \
  "v_cvt_pk_bf16_f32 v17, v20, v21\n" \
  "ds_write_b64 v5, v[16:17]\n" \
  "v_max_u32 v19, 1, v17\n" \
  "s_nop 1\n" \
  "v_permlane32_swap_b32 v18, v19\n" \
  "buffer_store_dwordx2 v[18:19], v8, s[52:55], s70 offen sc1\n" \
  "ds_read_b128 v[24:27], v3 offset:0\n" \
  "ds_read_b128 v[28:31], v3 offset:1024\n" \
  "ds_read_b128 v[32:35], v3 offset:2048\n" \
  "ds_read_b128 v[36:39], v3 offset:3072\n" \
  "ds_read_b128 v[40:43], v3 offset:8192\n" \
  "ds_read_b128 v[44:47], v3 offset:9216\n" \
  "ds_read_b128 v[48:51], v3 offset:10240\n" \
  "ds_read_b128 v[52:55], v3 offset:11264\n" \
  "s_cbranch_scc1 .LCV_LAST_%=\n" \
  "s_cmp_eq_u32 s73, 5\n" \
  "s_cbranch_scc0 .LCV_NMK_%=\n" \
  "s_mov_b32 m0, s78\n" \
  "s_nop 0\n" \
  "buffer_load_dword v2, s[0:3], s66 offen sc1 lds\n" \
  ".LCV_NMK_%=:\n" \
  "s_setprio 0\n" \
  "s_waitcnt lgkmcnt(8)\n" \
  "s_barrier\n" \
  "s_add_u32 s67, s67, 1024\n" \
  "ds_write_b32 v59, v12\n" \
  "ds_read_b128 v[56:59], v3 offset:4096\n" \
  "ds_read_b128 v[60:63], v3 offset:5120\n" \
  "v_mov_b64 v[20:21], 0\n" \
  "v_mov_b64 v[22:23], 0\n" \
  "v_mov_b64 v[16:17], 0\n" \
  "v_mov_b64 v[18:19], 0\n" \
  "s_mov_b32 m0, s94\n" \
  "s_mov_b64 exec, 15\n" \
  "buffer_load_dword v2, s[44:47], s68 offen sc1 lds\n" \
  "s_mov_b64 exec, -1\n" \
  "s_setprio 2\n" \
  "s_waitcnt lgkmcnt(0)\n" \
  "s_barrier\n" \
  "ds_read_b128 v[4:7], v1 offset:8192\n" \
  "ds_read_b128 v[8:11], v1 offset:9216\n" \
  "s_waitcnt lgkmcnt(1)\n" \
  "v_smfmac_f32_16x16x64_bf16 v[12:15], v[4:7], v[24:31], v0\n" \
  "ds_read_b128 v[24:27], v3 offset:6144\n" \
  "ds_read_b128 v[28:31], v3 offset:7168\n" \
  "v_smfmac_f32_16x16x64_bf16 v[16:19], v[4:7], v[32:39], v0\n" \
  "ds_read_b128 v[32:35], v3 offset:12288\n" \
  "ds_read_b128 v[36:39], v3 offset:13312\n" \
  "s_waitcnt lgkmcnt(4)\n" \
  "v_smfmac_f32_16x16x64_bf16 v[12:15], v[8:11], v[40:47], v0\n" \
  "v_smfmac_f32_16x16x64_bf16 v[16:19], v[8:11], v[48:55], v0\n" \
  "v_smfmac_f32_16x16x64_bf16 v[20:23], v[4:7], v[56:63], v0\n" \
  "ds_read_b128 v[56:59], v3 offset:14336\n" \
  "ds_read_b128 v[60:63], v3 offset:15360\n" \
  "v_mov_b64 v[40:41], 0\n" \
  "v_mov_b64 v[42:43], 0\n" \
  "s_nop 2\n" \
  "v_permlane32_swap_b32 v12, v17\n" \
  "v_permlane32_swap_b32 v13, v16\n" \
  "v_permlane32_swap_b32 v14, v19\n" \
  "v_permlane32_swap_b32 v15, v18\n" \
  "v_pk_add_f32 v[12:13], v[12:13], v[16:17] op_sel:[0,1] op_sel_hi:[1,0]\n" \
  "v_pk_add_f32 v[14:15], v[14:15], v[18:19] op_sel:[0,1] op_sel_hi:[1,0]\n" \
  "v_pk_fma_f32 v[12:13], v[12:13], s[84:85], s[84:85] op_sel:[0,0,1] op_sel_hi:[1,0,1]\n" \
  "v_pk_fma_f32 v[14:15], v[14:15], s[84:85], s[84:85] op_sel:[0,0,1] op_sel_hi:[1,0,1]\n" \
  "v_lshl_add_u32 v12, v12, 6, s91\n" \
  "v_lshl_add_u32 v13, v13, 2, s75\n" \
  "global_atomic_add_x2 v2, v[12:13], s[58:59] offset:0\n" \
  "v_lshl_add_u32 v14, v14, 6, s91\n" \
  "v_lshl_add_u32 v15, v15, 2, s75\n" \
  "global_atomic_add_x2 v2, v[14:15], s[58:59] offset:2048\n" \
  "s_waitcnt lgkmcnt(2)\n" \
  "v_smfmac_f32_16x16x64_bf16 v[20:23], v[8:11], v[32:39], v0\n" \
  "v_smfmac_f32_16x16x64_bf16 v[40:43], v[4:7], v[24:31], v0\n" \
  "s_waitcnt lgkmcnt(0)\n" \
  "v_smfmac_f32_16x16x64_bf16 v[40:43], v[8:11], v[56:63], v0\n" \
  "s_nop 7\n" \
  "v_permlane32_swap_b32 v20, v41\n" \
  "v_permlane32_swap_b32 v21, v40\n" \
  "v_permlane32_swap_b32 v22, v43\n" \
  "v_permlane32_swap_b32 v23, v42\n" \
  "v_pk_add_f32 v[20:21], v[20:21], v[40:41] op_sel:[0,1] op_sel_hi:[1,0]\n" \
  "v_pk_add_f32 v[22:23], v[22:23], v[42:43] op_sel:[0,1] op_sel_hi:[1,0]\n" \
  "v_pk_fma_f32 v[20:21], v[20:21], s[84:85], s[84:85] op_sel:[0,0,1] op_sel_hi:[1,0,1]\n" \
  "v_pk_fma_f32 v[22:23], v[22:23], s[84:85], s[84:85] op_sel:[0,0,1] op_sel_hi:[1,0,1]\n" \
  "v_lshl_add_u32 v20, v20, 6, s91\n" \
  "v_lshl_add_u32 v21, v21, 2, s75\n" \
  "global_atomic_add_x2 v2, v[20:21], s[58:59] offset:3072\n" \
  "v_lshl_add_u32 v22, v22, 6, s91\n" \
  "v_lshl_add_u32 v23, v23, 2, s75\n" \
  "global_atomic_add_x2 v2, v[22:23], s[58:59] offset:1024\n" \
  "s_setprio 0\n" \
  "s_memrealtime s[38:39]\n" \
  "v_mbcnt_lo_u32_b32 v48, -1, 0\n" \
  "v_mbcnt_hi_u32_b32 v48, -1, v48\n" \
  "s_add_u32 s88, s74, 157760\n" \
  "v_mov_b32 v59, s88\n" \
  "v_lshl_add_u32 v52, v48, 2, v59\n" \
  "ds_read_b32 v52, v52 offset:192\n" \
  "v_lshrrev_b32 v49, 3, v48\n" \
  "v_and_b32 v50, 7, v48\n" \
  "v_lshlrev_b32 v50, 4, v50\n" \
  "v_lshl_or_b32 v44, v49, 8, v50\n" \
  "s_mov_b32 s87, 0x40000\n" \
  "v_lshlrev_b32 v10, 4, v48\n" \
  "v_lshl_add_u32 v58, v48, 2, s94\n" \
  "s_waitcnt vmcnt(4)\n" \
  "ds_read_b32 v58, v58\n" \
  "s_waitcnt lgkmcnt(0)\n" \
  "v_cmp_eq_u32 vcc, 0, v58\n" \
  "s_and_b32 vcc_lo, vcc_lo, 15\n" \
  "s_cbranch_scc0 .LCV_GOT_%=\n" \
  "s_branch .LCV_ASK_%=\n" \
  ".LCV_LATE_%=:\n" \
  "v_cmp_eq_u32 vcc, 0, v58\n" \
  "s_and_b32 vcc_lo, vcc_lo, 15\n" \
  "s_cbranch_scc0 .LCV_GOT_%=\n" \
  "s_sleep 12\n" \
  ".LCV_ASK_%=:\n" \
  "s_mov_b64 exec, 15\n" \
  "buffer_load_dword v58, v2, s[44:47], s68 offen sc1\n" \
  "s_mov_b64 exec, -1\n" \
  "s_sub_u32 s87, s87, 1\n" \
  "s_cbranch_scc1 .LCV_END_%=\n" \
  "s_waitcnt vmcnt(0)\n" \
  "s_branch .LCV_LATE_%=\n" \
  ".LCV_GOT_%=:\n" \
  "s_mov_b32 m0, s99\n" \
  "s_add_u32 s88, s99, 8192\n" \
  "global_load_lds_dwordx4 v10, s[62:63] sc1\n" \
  "global_load_lds_dwordx4 v10, s[62:63] offset:1024 sc1\n" \
  "s_mov_b32 m0, s88\n" \
  "s_nop 0\n" \
  "global_load_lds_dwordx4 v10, s[64:65] sc1\n" \
  "global_load_lds_dwordx4 v10, s[64:65] offset:1024 sc1\n" \
  ".LCV_HAVE_%=:\n" \
  "s_waitcnt lgkmcnt(0)\n" \
  "s_sub_u32 s61, s38, s61\n" \
  "s_add_u32 s79, s79, 2\n" \
  "s_min_u32 s79, s79, s61\n" \
  "s_sub_u32 s39, s38, 150\n" \
  "v_subrev_u32 v52, s39, v52\n" \
  "v_cmp_lt_u32 vcc, 0x1000, v52\n" \
  "s_cmp_eq_u64 vcc, 0\n" \
  "s_cbranch_scc1 .LCV_OKM_%=\n" \
  ".LCV_AGN_%=:\n" \
  "buffer_load_dword v52, v2, s[0:3], s66 offen sc1\n" \
  "s_sub_u32 s87, s87, 1\n" \
  "s_cbranch_scc1 .LCV_END_%=\n" \
  "s_waitcnt vmcnt(0)\n" \
  "v_cmp_eq_u32 vcc, 0, v52\n" \
  "v_subrev_u32 v52, s39, v52\n" \
  "s_cmp_eq_u64 vcc, 0\n" \
  "s_cbranch_scc1 .LCV_OKM_%=\n" \
  "s_sleep 4\n" \
  "s_branch .LCV_AGN_%=\n" \
  ".LCV_OKM_%=:\n" \
  "s_nop 1\n" \
  "v_max_i32_dpp v52, v52, v52 row_ror:1 row_mask:0xf bank_mask:0xf\n" \
  "s_nop 1\n" \
  "v_max_i32_dpp v52, v52, v52 row_ror:2 row_mask:0xf bank_mask:0xf\n" \
  "s_nop 1\n" \
  "v_max_i32_dpp v52, v52, v52 row_ror:4 row_mask:0xf bank_mask:0xf\n" \
  "s_movk_i32 vcc_lo, 150\n" \
  "v_readlane_b32 s88, v52, 0\n" \
  "s_max_i32 vcc_lo, vcc_lo, s88\n" \
  "v_readlane_b32 s88, v52, 16\n" \
  "s_max_i32 vcc_lo, vcc_lo, s88\n" \
  "v_readlane_b32 s88, v52, 32\n" \
  "s_max_i32 vcc_lo, vcc_lo, s88\n" \
  "v_readlane_b32 s88, v52, 48\n" \
  "s_max_i32 vcc_lo, vcc_lo, s88\n" \
  "s_add_u32 s66, s66, 1024\n" \
  "s_add_u32 vcc_lo, vcc_lo, s39\n" \
  "s_add_u32 vcc_lo, vcc_lo, 30\n" \
  ".LCV_TW_%=:\n" \
  "s_memrealtime s[38:39]\n" \
  "s_waitcnt lgkmcnt(0)\n" \
  "s_sub_u32 s38, s38, vcc_lo\n" \
  "s_cmp_lt_i32 s38, 0\n" \
  "s_cbranch_scc1 .LCV_TW_%=\n" \
  "buffer_load_dwordx4 v[24:27], v44, s[40:43], s60 offen sc1\n" \
  "buffer_load_dwordx4 v[28:31], v44, s[40:43], s60 offen offset:2048 sc1\n" \
  "s_add_u32 s62, s62, 0x80000\n" \
  "s_addc_u32 s63, s63, 0\n" \
  "s_add_u32 s64, s64, 0x40000\n" \
  "s_addc_u32 s65, s65, 0\n" \
  "s_add_u32 s68, s68, 0x1000\n" \
  "s_add_u32 s69, s69, 0x10000\n" \
  "s_add_u32 s70, s70, 0x10000\n" \
  "s_sub_u32 s88, s73, 4\n" \
  "v_lshrrev_b32 v49, 2, v48\n" \
  "v_lshrrev_b32 v50, 4, v48\n" \
  "v_bitop3_b32 v50, v49, v50, 3 bitop3:0x28\n" \
  "v_lshl_add_u32 v4, v50, 3, s97\n" \
  "v_and_b32 v50, 3, v48\n" \
  "v_lshl_add_u32 v4, v50, 5, v4\n" \
  "v_and_b32 v49, 1, v49\n" \
  "v_lshl_add_u32 v4, v49, 8, v4\n" \
  "v_and_b32 v49, 8, v48\n" \
  "v_lshl_add_u32 v4, v49, 4, v4\n" \
  "v_and_b32 v51, 15, v48\n" \
  "v_bfe_u32 v49, v48, 4, 1\n" \
  "v_lshrrev_b32 v50, 5, v48\n" \
  "s_lshl_b32 s89, s88, 10\n" \
  "s_add_u32 s89, s89, s74\n" \
  "s_add_u32 s89, s89, 158720\n" \
  "v_mul_u32_u24 v6, 18, v49\n" \
  "v_xor_b32 v6, v6, v51\n" \
  "v_lshl_add_u32 v6, v6, 3, s89\n" \
  "v_lshl_add_u32 v6, v50, 9, v6\n" \
  "v_xor_b32 v9, 0x108, v6\n" \
  "v_lshlrev_b32 v8, 4, v49\n" \
  "v_lshl_add_u32 v8, v51, 5, v8\n" \
  "v_lshl_add_u32 v8, v50, 3, v8\n" \
  "s_lshl_b32 s89, s88, 8\n" \
  "s_add_u32 s89, s89, s74\n" \
  "s_add_u32 s89, s89, 139264\n" \
  "v_and_b32 v49, 31, v48\n" \
  "v_lshl_add_u32 v5, v49, 3, s89\n" \
  "v_lshl_add_u32 v5, v50, 10, v5\n" \
  "v_lshrrev_b32 v49, 2, v51\n" \
  "v_and_b32 v50, 5, v51\n" \
  "v_and_or_b32 v49, v49, 2, v50\n" \
  "v_and_b32 v50, 48, v48\n" \
  "v_lshlrev_b32 v7, 3, v50\n" \
  "v_lshl_add_u32 v7, v49, 4, v7\n" \
  "v_bitop3_b32 v49, v51, 2, 2 bitop3:0x0c\n" \
  "v_lshl_add_u32 v7, v49, 29, v7\n" \
  "v_mov_b64 v[12:13], 0\n" \
  "v_mov_b64 v[14:15], 0\n" \
  "v_mov_b64 v[16:17], 0\n" \
  "v_mov_b64 v[18:19], 0\n" \
  "v_mov_b64 v[20:21], 0\n" \
  "v_mov_b64 v[22:23], 0\n" \
  "v_and_b32 v50, 3, v48\n" \
  "v_lshl_add_u32 v40, v50, 2, s98\n" \
  "v_lshrrev_b32 v50, 4, v48\n" \
  "v_mad_u32_u24 v40, v50, s95, v40\n" \
  "v_bfe_u32 v49, v48, 3, 1\n" \
  "v_lshl_add_u32 v40, v49, 6, v40\n" \
  "v_bfe_u32 v49, v48, 2, 1\n" \
  "v_lshl_add_u32 v40, v49, 7, v40\n" \
  "v_xor_b32 v41, 16, v40\n" \
  "v_xor_b32 v42, 32, v40\n" \
  "v_xor_b32 v43, 48, v40\n" \
  "s_add_i32 s80, s86, 0x32800000\n" \
  "s_add_i32 s81, s86, 0x33800000\n" \
  "s_add_i32 s88, s86, 0xb4000000\n" \
  "v_mov_b32 v46, s88\n" \
  "v_mov_b32 v47, 0\n" \
  "v_mov_b32 v11, 0\n" \
  "s_mov_b32 s89, s60\n" \
  "s_xor_b32 s60, s60, 0x80\n" \
  "s_bitcmp0_b32 s60, 7\n" \
  "s_cselect_b32 s88, 0x8000, 0\n" \
  "s_add_u32 s60, s60, s88\n" \
  "s_and_b32 s60, s60, 0xffff\n" \
  "s_add_u32 s58, s56, s60\n" \
  "s_addc_u32 s59, s57, 0\n" \
  "s_sub_u32 s71, s71, 1\n" \
  ".LCV_SEE_%=:\n" \
  "s_waitcnt vmcnt(1)\n" \
  "v_bitop3_b32 v45, v24, v26, 32 bitop3:0x80\n" \
  "v_alignbit_b32 v32, v25, v24, 2\n" \
  "v_alignbit_b32 v34, v27, v26, 2\n" \
  "s_waitcnt vmcnt(0)\n" \
  "v_bitop3_b32 v45, v45, v28, v30 bitop3:0x80\n" \
  "v_cmp_eq_u32 vcc, 0, v45\n" \
  "v_cvt_f32_i32 v32, v32\n" \
  "v_cvt_f32_i32 v33, v25\n" \
  "s_cmp_eq_u64 vcc, 0\n" \
  "s_cbranch_scc0 .LCV_POLL_%=\n" \
  "v_cvt_f32_i32 v34, v34\n" \
  "v_cvt_f32_i32 v35, v27\n" \
  "v_pk_fma_f32 v[32:33], v[32:33], s[80:81], v[46:47]\n" \
  "v_alignbit_b32 v36, v29, v28, 2\n" \
  "v_pk_fma_f32 v[34:35], v[34:35], s[80:81], v[46:47]\n" \
  "v_alignbit_b32 v38, v31, v30, 2\n" \
  "v_cvt_pk_bf16_f32 v32, v32, v34\n" \
  "v_cvt_pk_bf16_f32 v33, v33, v35\n" \
  "v_cvt_f32_i32 v36, v36\n" \
  "ds_write_b32 v40, v32\n" \
  "v_cvt_f32_i32 v37, v29\n" \
  "ds_write_b32 v41, v33\n" \
  "v_cvt_f32_i32 v38, v38\n" \
  "v_cvt_f32_i32 v39, v31\n" \
  "v_pk_fma_f32 v[36:37], v[36:37], s[80:81], v[46:47]\n" \
  "v_pk_fma_f32 v[38:39], v[38:39], s[80:81], v[46:47]\n" \
  "v_dot2c_f32_bf16 v11, v32, v32\n" \
  "v_cvt_pk_bf16_f32 v36, v36, v38\n" \
  "v_cvt_pk_bf16_f32 v37, v37, v39\n" \
  "ds_write_b32 v42, v36\n" \
  "ds_write_b32 v43, v37\n" \
  "v_dot2c_f32_bf16 v11, v33, v33\n" \
  "v_dot2c_f32_bf16 v11, v36, v36\n" \
  "v_dot2c_f32_bf16 v11, v37, v37\n" \
  "s_nop 1\n" \
  "s_nop 0\n" \
  "v_cmpx_lt_f32 vcc, 2.0, v11\n" \
  "ds_max_u32 v59, v11\n" \
  "s_mov_b64 exec, -1\n" \
  "s_branch .LCV_TOP_%=\n" \
  ".LCV_POLL_%=:\n" \
  "buffer_load_dwordx4 v[24:27], v44, s[40:43], s89 offen sc1\n" \
  "buffer_load_dwordx4 v[28:31], v44, s[40:43], s89 offen offset:2048 sc1\n" \
  "s_sub_u32 s87, s87, 1\n" \
  "s_cbranch_scc1 .LCV_END_%=\n" \
  "s_branch .LCV_SEE_%=\n" \
  ".LCV_LAST_%=:\n" \
  "s_waitcnt vmcnt(0)\n" \
  ".LCV_END_%=:\n" \
  "s_endpgm\n"
__device__ __forceinline__ v4u mk_srd(const void* p, const unsigned bytes) {
  const unsigned long long a = (unsigned long long)p;
  return v4u{(unsigned)a, (unsigned)(a >> 32) & 0xffffu, bytes, 0x00020000u};
}
template <int R>
__device__ __forceinline__ void pin8(v8u& x) {
#define PIN_(a, b) if constexpr (R == a) asm volatile("" : "+{v[" #a ":" #b "]}"(x));
  PIN_(64, 71)  PIN_(72, 79)  PIN_(80, 87)  PIN_(88, 95)  PIN_(96, 103)  PIN_(104, 111)  PIN_(112, 119)  PIN_(120, 127)
  PIN_(128, 135)  PIN_(136, 143)  PIN_(144, 151)  PIN_(152, 159)  PIN_(160, 167)  PIN_(168, 175)  PIN_(176, 183)  PIN_(184, 191)
  PIN_(192, 199)  PIN_(200, 207)  PIN_(208, 215)  PIN_(216, 223)  PIN_(224, 231)  PIN_(232, 239)  PIN_(240, 247)  PIN_(248, 255)
#undef PIN_
}
template <int R>
__device__ __forceinline__ void pin4(v4u& x) {
#define PIN_(a, b) if constexpr (R == a) asm volatile("" : "+{v[" #a ":" #b "]}"(x));
  PIN_(32, 35)  PIN_(36, 39)  PIN_(40, 43)  PIN_(44, 47)  PIN_(48, 51)  PIN_(52, 55)  PIN_(56, 59)  PIN_(60, 63)
  PIN_(96, 99)  PIN_(100, 103)  PIN_(104, 107)  PIN_(108, 111)  PIN_(112, 115)  PIN_(116, 119)  PIN_(120, 123)  PIN_(124, 127)
  PIN_(160, 163)  PIN_(164, 167)  PIN_(168, 171)  PIN_(172, 175)  PIN_(176, 179)  PIN_(180, 183)  PIN_(184, 187)  PIN_(188, 191)
#undef PIN_
}
template <bool ATT>
__device__ __forceinline__ void role_chain_t(const MegaArgs& A, char* smem, const int L, const int h, const int rep) {
  u16* swo = reinterpret_cast<u16*>(smem);
  const int w = A.wv;
  char* ws = A.ws;
  unsigned* FL = (unsigned*)(ws + OFF_FL);
  unsigned* TMO = FL + FL_TMO;
#define LANE_IDS()                         \
  const int l = lane_id();                 \
  const int c = l & 15, g = l >> 4;        \
  (void)c; (void)g;
  float* sgf = reinterpret_cast<float*>(smem + C_X);
  unsigned* FQ = FL + FL_FQ + L * 64 * 32 * 32;
  const int wb = (w + ROT_H) & 7;
  v8u w0[8], w1[8], w2[8];
  u64 tag;
  {
    LANE_IDS();
    const unsigned sb = lds_base(smem);
    const int q = (l >> 2) & 3, p = l & 3;
    const int f = (q & 1) | ((g & 1) << 1) | ((q >> 1) << 2);
    const unsigned ra = sb + (16 * (g >> 1) + 4 * (g & 1) + (q & 1) + 8 * (q >> 1)) * 256 + (w ^ f) * 32 + p * 8;
#define W_READ(N, hf, X)                                                       \
  {                                                                           \
    _Pragma("unroll") for (int t = 0; t < 4; ++t) {                           \
      const unsigned a_ = (ra ^ (X)) + (hf) * 8192 + t * 32768;               \
      const v2u r0 = trd(a_), r1 = trd(a_ + 512), r2 = trd(a_ + 16384), r3 = trd(a_ + 16384 + 512); \
      w##N[4 * (hf) + t] = v8u{r0.x, r0.y, r1.x, r1.y, r2.x, r2.y, r3.x, r3.y}; \
    }                                                                         \
    pin8<64 * N + 64 + 32 * hf>(w##N[4 * hf]);                                \
    pin8<64 * N + 72 + 32 * hf>(w##N[4 * hf + 1]);                            \
    pin8<64 * N + 80 + 32 * hf>(w##N[4 * hf + 2]);                            \
    pin8<64 * N + 88 + 32 * hf>(w##N[4 * hf + 3]);                            \
  }
#define W_STAGE(M, hf, WAIT, NEXT)                                             \
  {                                                                           \
    WAIT;                                                                     \
    RAW_BAR();                                                                \
    if ((M) == 0 && ATT) { W_READ(0, hf, 0); W_READ(2, hf, 128); }            \
    if ((M) == 1 && ATT) { W_READ(1, hf, 0); }                                \
    if ((M) == 1 && !ATT) { W_READ(0, hf, 0); }                               \
    if ((M) == 2 && !ATT) { W_READ(1, hf, 128); W_READ(2, hf, 0); }           \
    if ((M) == 3) {                                                           \
      _Pragma("unroll") for (int i = 0; i < 8; ++i) {                         \
        const unsigned a_ = rb + (hf) * 8192 + (i & 1) * 65536 + ((4 * wb + (i >> 1)) ^ f) * 32; \
        const v2u r0 = trd(a_), r1 = trd(a_ + 2048);                          \
        fr[hf][i] = v4u{r0.x, r0.y, r1.x, r1.y};                              \
      }                                                                       \
      constexpr int R_ = hf ? 32 : ATT ? 160 : 96;                            \
      pin4<R_>(fr[hf][0]); pin4<R_ + 4>(fr[hf][1]); pin4<R_ + 8>(fr[hf][2]); pin4<R_ + 12>(fr[hf][3]); \
      pin4<R_ + 16>(fr[hf][4]); pin4<R_ + 20>(fr[hf][5]); pin4<R_ + 24>(fr[hf][6]); pin4<R_ + 28>(fr[hf][7]); \
    }                                                                         \
    RAW_BAR();                                                                \
    NEXT;                                                                     \
  }
#define WO_ISSUE(hf)                                                          \
  {                                                                           \
    const char* base_ = (const char*)A.Wo + (size_t)(h * DH + 64 * (hf) + 8 * w) * (DM * 2); \
    _Pragma("unroll") for (int i = 0; i < 8; ++i) {                           \
      const int fd = (i & 1) | (((i >> 2) & 1) << 1) | ((w & 1) << 2);        \
      glds16(base_, (unsigned)(i * 1024 + ((l ^ (fd << 1)) << 4)), sb + w * 16384 + (hf) * 8192 + i * 1024); \
    }                                                                         \
  }
    const unsigned rb = sb + (2 * (g >> 1) + (q >> 1)) * 16384 + (4 * (g & 1) + (q & 1)) * 1024 + p * 8;
    v4u fr[2][8];
#define WO_PUT(hf) \
  _Pragma("unroll") for (int i = 0; i < 8; ++i) *reinterpret_cast<v4u*>(swo + w * 8192 + (hf) * 4096 + i * 512 + l * 8) = fr[hf][i];
    __builtin_amdgcn_s_waitcnt(0x0f70);
    glds16(A.mem0, (unsigned)((c & 7) * DM + 64 * w + 16 * g + 8 * (c >> 3)) * 2, sb + C_SM + w * 1024);
    wqkv_issue<true>(A.Wv, 0, h, w, l, sb);
    wqkv_issue<true>(A.Wv, 1, h, w, l, sb);
    tag = call_tag(A);
    W_STAGE(2, 0, WAIT_VM(8), WO_ISSUE(0));
    W_STAGE(2, 1, WAIT_VM(8), WO_ISSUE(1));
    SST(4);
    if (!ATT) {
      float f2 = 0.f;
      asm volatile("" : "+v"(f2));
#pragma unroll
      for (int t = 0; t < 8; ++t)
#pragma unroll
        for (int e = 0; e < 8; ++e) f2 = dot2sq(w2[t][e], dot2sq(w1[t][e], f2));
      f2 = wave_sum_dpp(f2);
      if (l == 0) { sgf[w] = f2; sgf[16] = 0.f; }
    }
    W_STAGE(3, 0, WAIT_VM(8), wqkv_issue<true>(A.Wq, 0, h, w, l, sb));
    W_STAGE(3, 1, WAIT_VM(8), wqkv_issue<true>(A.Wq, 1, h, w, l, sb));
    SST(5);
    {
      float cn[4] = {0.f, 0.f, 0.f, 0.f};
      asm volatile("" : "+v"(cn[0]), "+v"(cn[1]), "+v"(cn[2]), "+v"(cn[3]));
#pragma unroll
      for (int e = 0; e < 64; ++e) cn[e & 3] = dot2sq(fr[e >> 5][2 * (e & 3) + ((e >> 4) & 1)][(e >> 2) & 3], cn[e & 3]);
      const float m2 = wave_max_dpp(swap_add32(swap_add16(cn[0], cn[1]), swap_add16(cn[2], cn[3])));
      if (l == 0) sgf[8 + w] = m2;
    }
#define G_VALUE()                                                                                                   \
  if (w == 4 && L == 0) {                                                                                           \
    const f32x4 a = *reinterpret_cast<const f32x4*>(sgf + 4), b = *reinterpret_cast<const f32x4*>(sgf + 8);         \
    const f32x4 d = *reinterpret_cast<const f32x4*>(sgf + 12);                                                      \
    const float m2 = fmaxf(fmaxf(fmaxf(b.x, b.y), fmaxf(b.z, b.w)), fmaxf(fmaxf(d.x, d.y), fmaxf(d.z, d.w)));       \
    if (l < 8) __hip_atomic_store((gu64*)(ws + OFF_G) + l * 32 + h, tagged(tag, __float_as_uint(((a.x + a.y) + (a.z + a.w)) * m2 * 1.01f)), RLX_AGENT); \
  }
    W_STAGE(0, 0, WAIT_VM(8), G_VALUE(); wqkv_issue<true>(A.Wk, 0, h, w, l, sb));
    W_STAGE(0, 1, WAIT_VM(8), if (ATT) {
      wqkv_issue<true>(A.Wk, 1, h, w, l, sb);
      wqkv_issue<true>(A.Wk, 1, h, w + 4, l, sb);
    });
    SST(6);
    W_STAGE(1, 0, if (ATT) WAIT_VM(16); else { ready_wait(A, tag, ready_look(A)); __builtin_amdgcn_s_waitcnt(0x0f70); }, WO_PUT(0));
    if (w == 4) {
      u64 gv;
      for (unsigned spins = 0;; ++spins) {
        gv = __hip_atomic_load((gu64*)(ws + OFF_G) + (blockIdx.x & 7) * 32 + (l & 31), RLX_AGENT);
        if (__all(has_tag(tag, (unsigned)gv, (unsigned)(gv >> 32))) || spins > SPIN_CAP) break;
        __builtin_amdgcn_s_sleep(GSL);
      }
      const float gm = wave_max_dpp(hi16((unsigned)(gv >> 32)));
      if (l == 0) sgf[0] = gm * 1.063f;
    }
    W_STAGE(1, 1, WAIT_VM(0), WO_PUT(1));
    SST(7);
#undef G_VALUE
#undef WO_PUT
#undef W_STAGE
#undef W_READ
#undef WO_ISSUE
  }
  WSTAMP2();
  const unsigned sb = lds_base(smem);
  const char* gK = ws + OFF_QK + L * SZ_QK + ((size_t)h * 16 + 2 * w) * 1024;
  const char* gV = ws + OFF_VF + L * SZ_O + ((size_t)h * 8 + 2 * w - 8) * 1024;
  v8u pv;
  {
    LANE_IDS();
    const unsigned voff = g * 256 + c * 8, wp = sb + w * 16384 + l * 16;
    pv[0] = (c & 8) ? 0xEEEE : 0x4444;
    pv[1] = sb + C_SM + l * 16;
    const unsigned ra = sb + C_SQ + (g >> 1) * 512 + (c ^ ((g & 1) * 18)) * 8;
    pv[2] = ATT ? ra : voff;
    pv[3] = ATT ? ra ^ 0x108 : wp;
    pv[4] = sb + ((c & 2) ? C_SQ : C_SO) + w * 256 + (((c & 5) | ((c >> 2) & 2)) + 8 * (g >> 1)) * 16 + 8 * ((g ^ (c >> 1)) & 1);
    pv[5] = voff;
    pv[6] = wp;
    pv[7] = sb + C_SO + (l >> 5) * 1024 + w * 256 + (l & 31) * 8;
  }
  if (!ATT) {
    for (unsigned spins = 0; !__all(__hip_atomic_load((gu32*)FQ + h * 32 + 2 * (lane_id() & 3), RLX_AGENT) != 0u); ++spins) {
      if (spin_timeout(spins, TMO)) return;
      __builtin_amdgcn_s_sleep(4);
    }
    asm volatile("" ::: "memory");
    const unsigned lo = lane_id() * 16, d = sb + C_KT + (2 * w - 8) * 1024;
    glds16c(gK, lo, d);
    glds16c(gK + 1024, lo, d + 1024);
    glds16c(gV, lo, d + 8192);
    glds16c(gV + 1024, lo, d + 9216);
  }
  const unsigned gw2 = __builtin_amdgcn_readfirstlane(__float_as_uint(sgf[0]));
  DRAIN();
  SST(8);
  const v4u rsA = mk_srd(ws + OFF_ACC + L * SZ_ACC, SZ_ACC);
  const v4u rsF = mk_srd(FQ, 64 * 32 * 32 * 4);
  const v4u rsMK = mk_srd(ws + OFF_MEMK + L * SZ_MEM, SZ_MEM);
  const v4u rsMV = mk_srd(ws + OFF_MEMV + L * SZ_MEM, SZ_MEM);
  const char* pAB = ws + OFF_ACC + L * SZ_ACC + (wb - w) * 4096;
  const char* pGK = gK + NH * 16 * 1024;
  const char* pGV = gV + NH * 8 * 1024;
  const v4u rsT = mk_srd(FL + FL_FK + L * 32, 64 * 1024 - 128);
  unsigned sc[8];
  sc[0] = (32 + h) * 128;
  sc[1] = (h * 4 + w - 4) * 512;
  sc[2] = (h * 4 + w - 4) * 512;
  sc[3] = NSEG - 1;
  sc[4] = gw2;
  sc[5] = w;
  sc[6] = sb;
  sc[7] = h == 0 ? 0xd4000000u - 2u - (1u << 29) : 0xd4000000u;
  const char* dbg = ws;
#define CHAIN_OPS                                                                                                          \
  ::"{v[64:71]}"(w0[0]), "{v[72:79]}"(w0[1]), "{v[80:87]}"(w0[2]), "{v[88:95]}"(w0[3]), "{v[96:103]}"(w0[4]),              \
      "{v[104:111]}"(w0[5]), "{v[112:119]}"(w0[6]), "{v[120:127]}"(w0[7]), "{v[128:135]}"(w1[0]), "{v[136:143]}"(w1[1]),    \
      "{v[144:151]}"(w1[2]), "{v[152:159]}"(w1[3]), "{v[160:167]}"(w1[4]), "{v[168:175]}"(w1[5]), "{v[176:183]}"(w1[6]),    \
      "{v[184:191]}"(w1[7]), "{v[192:199]}"(w2[0]), "{v[200:207]}"(w2[1]), "{v[208:215]}"(w2[2]), "{v[216:223]}"(w2[3]),    \
      "{v[224:231]}"(w2[4]), "{v[232:239]}"(w2[5]), "{v[240:247]}"(w2[6]), "{v[248:255]}"(w2[7]), "{v[0:7]}"(pv),          \
      "{s[40:43]}"(rsA), "{s[44:47]}"(rsF), "{s[48:51]}"(rsMK), "{s[52:55]}"(rsMV), "{s[56:57]}"(pAB), "{s60}"(w * 4096), "{s61}"(ATT ? h * 1024 : L), \
      "{s[62:63]}"(pGK), "{s[64:65]}"(pGV), "{s66}"(0), "{s67}"(w == 4 ? (h >> 3) * 256 + (h & 7) * 8 : 0x40000000), "{s68}"(sc[0]), "{s69}"(sc[1]), "{s70}"(sc[2]),    \
      "{s71}"(sc[3]), "{s72}"(sc[4]), "{s73}"(sc[5]), "{s74}"(sc[6]), "{s75}"(sc[7]), "{s[36:37]}"(dbg),                   \
      "{s[0:3]}"(rsT)                                                                                                      \
      : "memory", "vcc", "scc", "s99", "v63"
  if (ATT) asm volatile("s_mov_b64 exec, -1\ns_nop 4\n" CHAIN_ASM_ATT CHAIN_OPS);
  else asm volatile("s_mov_b64 exec, -1\ns_nop 4\n" CHAIN_ASM_VO CHAIN_OPS);
#undef CHAIN_OPS
#undef LANE_IDS
}

__device__ __forceinline__ void role_chain(const MegaArgs& A, char* smem, const int L, const int h, const int rep) {
  if (A.wv < 4) role_chain_t<true>(A, smem, L, h, rep);
  else role_chain_t<false>(A, smem, L, h, rep);
}

constexpr int AW_SO = 0, AW_TK = 32768, AW_CNT = AW_TK + 98304;
__device__ __forceinline__ unsigned umin3(unsigned a, unsigned b, unsigned c) {
  return __builtin_elementwise_min(__builtin_elementwise_min(a, b), c);
}
#define AW_ASM_X \
  "s_mov_b32 s61, 0\n" \
  "s_mov_b32 s67, 1\n" \
  "s_mov_b32 s68, 0\n" \
  "s_mov_b32 s70, 0\n" \
  "s_mov_b32 s73, 0\n" \
  "s_mov_b32 s71, 0x3e0293ee\n" \
  "s_mov_b32 s72, 0xbe0293ee\n" \
  "s_mov_b32 s65, 49152\n" \
  "s_mov_b32 s66, 16384\n" \
  "v_mov_b32 v38, s58\n" \
  "s_memrealtime s[38:39]\n" \
  "s_waitcnt lgkmcnt(0)\n" \
  "s_mov_b32 s69, s38\n" \
  "s_setprio 3\n" \
  ".LCX_TOP_%=:\n" \
  "s_mov_b32 s62, 0x200000\n" \
  "s_cmp_ge_u32 s73, s67\n" \
  "s_cbranch_scc1 .LCX_TOK_%=\n" \
  "s_cmp_eq_u32 s61, 0\n" \
  "s_cbranch_scc1 .LCX_SPIN_%=\n" \
  "s_waitcnt vmcnt(0)\n" \
  "v_mov_b32 v46, s59\n" \
  "v_mov_b32 v47, 1\n" \
  "s_mov_b64 exec, 1\n" \
  "ds_add_rtn_u32 v46, v46, v47\n" \
  "s_mov_b64 exec, -1\n" \
  "s_xor_b32 s59, s59, 4\n" \
  "s_mov_b32 s61, 0\n" \
  "s_waitcnt lgkmcnt(0)\n" \
  "v_readfirstlane_b32 s63, v46\n" \
  "v_lshlrev_b32 v46, 5, v5\n" \
  "s_and_b32 s63, s63, 7\n" \
  "s_cmp_eq_u32 s63, 7\n" \
  "s_cbranch_scc0 .LCX_W1_%=\n" \
  "s_mov_b64 exec, 15\n" \
  "global_store_dword v46, v47, s[52:53] sc1\n" \
  "s_mov_b64 exec, -1\n" \
  ".LCX_W1_%=:\n" \
  "s_add_u32 s52, s52, 2048\n" \
  "s_addc_u32 s53, s53, 0\n" \
  ".LCX_SPIN_%=:\n" \
  "s_sleep 2\n" \
  "s_memrealtime s[38:39]\n" \
  "ds_read_b32 v37, v38\n" \
  "s_sub_u32 s62, s62, 1\n" \
  "s_cbranch_scc1 .LCX_END_%=\n" \
  "s_waitcnt lgkmcnt(0)\n" \
  "v_readfirstlane_b32 s73, v37\n" \
  "s_add_u32 s38, s38, 80\n" \
  "s_bfe_u32 s64, s73, 0x80008\n" \
  "s_and_b32 s73, s73, 0xff\n" \
  "s_min_u32 s73, s73, s64\n" \
  "s_cmp_ge_u32 s73, s67\n" \
  "s_cbranch_scc0 .LCX_SPIN_%=\n" \
  "s_cmp_eq_u32 s70, 0\n" \
  "s_cbranch_scc1 .LCX_TOK_%=\n" \
  "s_sub_u32 s63, s38, s69\n" \
  "s_max_i32 s63, s63, 0\n" \
  "s_add_u32 s69, s69, s63\n" \
  ".LCX_TOK_%=:\n" \
  "ds_read_b128 v[48:51], v6 offset:0\n" \
  "ds_read_b128 v[64:67], v0 offset:8192\n" \
  "ds_read_b128 v[80:83], v0 offset:12288\n" \
  "ds_read_b128 v[52:55], v6 offset:1024\n" \
  "ds_read_b128 v[68:71], v0 offset:9216\n" \
  "ds_read_b128 v[84:87], v0 offset:13312\n" \
  "ds_read_b128 v[56:59], v6 offset:2048\n" \
  "ds_read_b128 v[72:75], v0 offset:10240\n" \
  "ds_read_b128 v[88:91], v0 offset:14336\n" \
  "ds_read_b128 v[60:63], v6 offset:3072\n" \
  "ds_read_b128 v[76:79], v0 offset:11264\n" \
  "ds_read_b128 v[92:95], v0 offset:15360\n" \
  "s_waitcnt lgkmcnt(9)\n" \
  "v_mfma_f32_16x16x32_bf16 v[20:23], v[64:67], v[48:51], v[8:11]\n" \
  "v_mfma_f32_16x16x32_bf16 v[24:27], v[80:83], v[48:51], v[12:15]\n" \
  "ds_read_b128 v[96:99], v0 offset:16384\n" \
  "ds_read_b128 v[100:103], v0 offset:17408\n" \
  "s_waitcnt lgkmcnt(8)\n" \
  "v_mfma_f32_16x16x32_bf16 v[20:23], v[68:71], v[52:55], v[20:23]\n" \
  "v_mfma_f32_16x16x32_bf16 v[24:27], v[84:87], v[52:55], v[24:27]\n" \
  "ds_read_b128 v[104:107], v0 offset:18432\n" \
  "ds_read_b128 v[108:111], v0 offset:19456\n" \
  "s_waitcnt lgkmcnt(7)\n" \
  "v_mfma_f32_16x16x32_bf16 v[20:23], v[72:75], v[56:59], v[20:23]\n" \
  "v_mfma_f32_16x16x32_bf16 v[24:27], v[88:91], v[56:59], v[24:27]\n" \
  "ds_read_b128 v[112:115], v0 offset:20480\n" \
  "ds_read_b128 v[116:119], v0 offset:21504\n" \
  "s_waitcnt lgkmcnt(6)\n" \
  "v_mfma_f32_16x16x32_bf16 v[20:23], v[76:79], v[60:63], v[20:23]\n" \
  "v_mfma_f32_16x16x32_bf16 v[24:27], v[92:95], v[60:63], v[24:27]\n" \
  "ds_read_b128 v[120:123], v0 offset:22528\n" \
  "ds_read_b128 v[124:127], v0 offset:23552\n" \
  "s_add_u32 s67, s67, 1\n" \
  "v_add_u32 v0, s65, v0\n" \
  "v_add_u32 v6, s65, v6\n" \
  "s_sub_u32 s65, 0, s65\n" \
  "s_nop 1\n" \
  "v_max3_f32 v32, v20, v21, v22\n" \
  "v_max3_f32 v33, v23, v24, v25\n" \
  "v_max3_f32 v32, v32, v26, v27\n" \
  "v_max_f32 v32, v32, v33\n" \
  "v_mov_b32 v33, v32\n" \
  "s_nop 1\n" \
  "v_permlane16_swap_b32 v32, v33\n" \
  "v_max_f32 v32, v32, v33\n" \
  "v_mov_b32 v33, v32\n" \
  "s_nop 1\n" \
  "v_permlane32_swap_b32 v32, v33\n" \
  "v_max_f32 v32, v32, v33\n" \
  "v_mul_f32 v33, s72, v32\n" \
  "v_fma_f32 v20, v20, s71, v33\n" \
  "v_fma_f32 v21, v21, s71, v33\n" \
  "v_fma_f32 v22, v22, s71, v33\n" \
  "v_fma_f32 v23, v23, s71, v33\n" \
  "v_fma_f32 v24, v24, s71, v33\n" \
  "v_fma_f32 v25, v25, s71, v33\n" \
  "v_fma_f32 v26, v26, s71, v33\n" \
  "v_fma_f32 v27, v27, s71, v33\n" \
  "v_exp_f32 v20, v20\n" \
  "v_exp_f32 v21, v21\n" \
  "v_exp_f32 v22, v22\n" \
  "v_exp_f32 v23, v23\n" \
  "v_exp_f32 v24, v24\n" \
  "v_exp_f32 v25, v25\n" \
  "v_exp_f32 v26, v26\n" \
  "v_exp_f32 v27, v27\n" \
  "v_add_f32 v34, v20, v21\n" \
  "v_add_f32 v33, v22, v23\n" \
  "v_add_f32 v36, v24, v25\n" \
  "v_add_f32 v34, v34, v33\n" \
  "v_add_f32 v33, v26, v27\n" \
  "v_add_f32 v34, v34, v36\n" \
  "v_add_f32 v34, v34, v33\n" \
  "s_waitcnt vmcnt(0) lgkmcnt(0)\n" \
  "s_cmp_eq_u32 s61, 0\n" \
  "s_cbranch_scc1 .LCX_NP_%=\n" \
  "v_mov_b32 v46, s59\n" \
  "v_mov_b32 v47, 1\n" \
  "s_mov_b64 exec, 1\n" \
  "ds_add_rtn_u32 v46, v46, v47\n" \
  "s_mov_b64 exec, -1\n" \
  "s_xor_b32 s59, s59, 4\n" \
  "s_mov_b32 s61, 0\n" \
  "s_waitcnt lgkmcnt(0)\n" \
  "v_readfirstlane_b32 s63, v46\n" \
  "v_lshlrev_b32 v46, 5, v5\n" \
  "s_and_b32 s63, s63, 7\n" \
  "s_cmp_eq_u32 s63, 7\n" \
  "s_cbranch_scc0 .LCX_W2_%=\n" \
  "s_mov_b64 exec, 15\n" \
  "global_store_dword v46, v47, s[52:53] sc1\n" \
  "s_mov_b64 exec, -1\n" \
  ".LCX_W2_%=:\n" \
  "s_add_u32 s52, s52, 2048\n" \
  "s_addc_u32 s53, s53, 0\n" \
  ".LCX_NP_%=:\n" \
  "s_cmp_lg_u32 s68, 0\n" \
  "s_cbranch_scc1 .LCX_TEST_%=\n" \
  ".LCX_WF_%=:\n" \
  "s_memrealtime s[38:39]\n" \
  "s_waitcnt lgkmcnt(0)\n" \
  "s_sub_u32 s63, s69, s38\n" \
  "s_cmp_le_i32 s63, 0\n" \
  "s_cbranch_scc1 .LCX_GO_%=\n" \
  "s_cmp_lt_i32 s63, 12\n" \
  "s_cbranch_scc1 .LCX_WF_%=\n" \
  "s_sleep 3\n" \
  "s_branch .LCX_WF_%=\n" \
  ".LCX_GO_%=:\n" \
  "v_mov_b32 v35, 0\n" \
  "global_load_dwordx4 v[128:131], v2, s[48:49] offset:0 sc1\n" \
  "global_load_dwordx4 v[132:135], v2, s[48:49] offset:512 sc1\n" \
  "global_load_dwordx4 v[136:139], v2, s[48:49] offset:1024 sc1\n" \
  "global_load_dwordx4 v[140:143], v2, s[48:49] offset:1536 sc1\n" \
  "global_load_dwordx4 v[144:147], v3, s[48:49] offset:0 sc1\n" \
  "global_load_dwordx4 v[148:151], v3, s[48:49] offset:512 sc1\n" \
  "global_load_dwordx4 v[152:155], v3, s[48:49] offset:1024 sc1\n" \
  "global_load_dwordx4 v[156:159], v3, s[48:49] offset:1536 sc1\n" \
  "global_load_dword v35, v35, s[50:51] sc1\n" \
  "s_sub_u32 s62, s62, 8\n" \
  "s_cbranch_scc1 .LCX_END_%=\n" \
  "s_waitcnt vmcnt(0)\n" \
  ".LCX_TEST_%=:\n" \
  "v_mfma_f32_16x16x32_bf16 v[28:31], v[128:131], v[48:51], v[16:19]\n" \
  "v_min3_u32 v33, v128, v129, v130\n" \
  "v_min3_u32 v36, v131, v132, v133\n" \
  "v_min3_u32 v33, v33, v134, v135\n" \
  "v_min3_u32 v36, v36, v136, v137\n" \
  "v_mfma_f32_16x16x32_bf16 v[28:31], v[132:135], v[52:55], v[28:31]\n" \
  "v_min3_u32 v33, v33, v138, v139\n" \
  "v_min3_u32 v36, v36, v140, v141\n" \
  "v_min3_u32 v33, v33, v142, v143\n" \
  "v_min3_u32 v36, v36, v144, v145\n" \
  "v_mfma_f32_16x16x32_bf16 v[28:31], v[136:139], v[56:59], v[28:31]\n" \
  "v_min3_u32 v33, v33, v146, v147\n" \
  "v_min3_u32 v36, v36, v148, v149\n" \
  "v_min3_u32 v33, v33, v150, v151\n" \
  "v_min3_u32 v36, v36, v152, v153\n" \
  "v_mfma_f32_16x16x32_bf16 v[28:31], v[140:143], v[60:63], v[28:31]\n" \
  "v_min3_u32 v33, v33, v154, v155\n" \
  "v_min3_u32 v36, v36, v156, v157\n" \
  "v_min3_u32 v33, v33, v158, v159\n" \
  "v_min_u32 v33, v33, v36\n" \
  "v_cmp_eq_u32 vcc, 0, v33\n" \
  "v_readfirstlane_b32 s64, v35\n" \
  "s_mov_b32 s68, 0\n" \
  "s_cmp_eq_u64 vcc, 0\n" \
  "s_cbranch_scc0 .LCX_FAIL_%=\n" \
  "v_mov_b32 v33, s67\n" \
  "ds_write_b32 v38, v33 offset:16\n" \
  "v_max3_f32 v36, v28, v29, v30\n" \
  "v_max3_f32 v36, v36, v31, v32\n" \
  "v_mov_b32 v33, v36\n" \
  "s_cmp_eq_u32 s64, 0\n" \
  "s_cselect_b32 s64, s38, s64\n" \
  "v_permlane16_swap_b32 v36, v33\n" \
  "v_max_f32 v36, v36, v33\n" \
  "v_mov_b32 v33, v36\n" \
  "s_sub_u32 s63, s64, s70\n" \
  "s_mov_b32 s70, s64\n" \
  "v_permlane32_swap_b32 v36, v33\n" \
  "v_max_f32 v36, v36, v33\n" \
  "v_mul_f32 v36, s72, v36\n" \
  "v_fma_f32 v32, v32, s71, v36\n" \
  "v_fma_f32 v28, v28, s71, v36\n" \
  "v_fma_f32 v29, v29, s71, v36\n" \
  "v_fma_f32 v30, v30, s71, v36\n" \
  "v_fma_f32 v31, v31, s71, v36\n" \
  "v_exp_f32 v32, v32\n" \
  "v_exp_f32 v28, v28\n" \
  "v_exp_f32 v29, v29\n" \
  "v_exp_f32 v30, v30\n" \
  "v_exp_f32 v31, v31\n" \
  "v_fma_f32 v34, v34, v32, v28\n" \
  "v_add_f32 v33, v29, v30\n" \
  "v_add_f32 v34, v34, v31\n" \
  "v_add_f32 v34, v34, v33\n" \
  "v_mov_b32 v33, v34\n" \
  "s_min_u32 s63, s63, 250\n" \
  "s_add_u32 s69, s64, s63\n" \
  "v_permlane16_swap_b32 v34, v33\n" \
  "v_add_f32 v34, v34, v33\n" \
  "v_mov_b32 v33, v34\n" \
  "s_add_u32 s69, s69, -48\n" \
  "s_add_u32 s48, s48, 0x10000\n" \
  "s_addc_u32 s49, s49, 0\n" \
  "v_permlane32_swap_b32 v34, v33\n" \
  "v_add_f32 v34, v34, v33\n" \
  "v_rcp_f32 v34, v34\n" \
  "s_add_u32 s50, s50, 1024\n" \
  "s_addc_u32 s51, s51, 0\n" \
  "v_mul_f32 v32, v32, v34\n" \
  "v_mul_f32 v20, v20, v32\n" \
  "v_mul_f32 v21, v21, v32\n" \
  "v_mul_f32 v22, v22, v32\n" \
  "v_mul_f32 v23, v23, v32\n" \
  "v_mul_f32 v24, v24, v32\n" \
  "v_mul_f32 v25, v25, v32\n" \
  "v_mul_f32 v26, v26, v32\n" \
  "v_mul_f32 v27, v27, v32\n" \
  "v_cvt_pk_bf16_f32 v40, v20, v21\n" \
  "v_cvt_pk_bf16_f32 v41, v22, v23\n" \
  "v_cvt_pk_bf16_f32 v42, v24, v25\n" \
  "v_cvt_pk_bf16_f32 v43, v26, v27\n" \
  "v_mul_f32 v28, v28, v34\n" \
  "v_mul_f32 v29, v29, v34\n" \
  "v_mfma_f32_16x16x32_bf16 v[64:67], v[96:99], v[40:43], 0\n" \
  "v_mul_f32 v30, v30, v34\n" \
  "v_mfma_f32_16x16x32_bf16 v[68:71], v[100:103], v[40:43], 0\n" \
  "v_mul_f32 v31, v31, v34\n" \
  "v_mfma_f32_16x16x32_bf16 v[72:75], v[104:107], v[40:43], 0\n" \
  "v_cvt_pk_bf16_f32 v44, v28, v29\n" \
  "v_mfma_f32_16x16x32_bf16 v[76:79], v[108:111], v[40:43], 0\n" \
  "v_cvt_pk_bf16_f32 v45, v30, v31\n" \
  "v_mfma_f32_16x16x32_bf16 v[80:83], v[112:115], v[40:43], 0\n" \
  "v_mfma_f32_16x16x32_bf16 v[84:87], v[116:119], v[40:43], 0\n" \
  "v_mfma_f32_16x16x32_bf16 v[88:91], v[120:123], v[40:43], 0\n" \
  "v_mfma_f32_16x16x32_bf16 v[92:95], v[124:127], v[40:43], 0\n" \
  "s_memrealtime s[38:39]\n" \
  "ds_read_b32 v37, v38\n" \
  "v_mfma_f32_16x16x16_bf16 v[64:67], v[144:145], v[44:45], v[64:67]\n" \
  "v_mfma_f32_16x16x16_bf16 v[68:71], v[148:149], v[44:45], v[68:71]\n" \
  "v_mfma_f32_16x16x16_bf16 v[72:75], v[152:153], v[44:45], v[72:75]\n" \
  "v_mfma_f32_16x16x16_bf16 v[76:79], v[156:157], v[44:45], v[76:79]\n" \
  "v_mfma_f32_16x16x16_bf16 v[80:83], v[146:147], v[44:45], v[80:83]\n" \
  "v_mfma_f32_16x16x16_bf16 v[84:87], v[150:151], v[44:45], v[84:87]\n" \
  "v_mfma_f32_16x16x16_bf16 v[88:91], v[154:155], v[44:45], v[88:91]\n" \
  "v_mfma_f32_16x16x16_bf16 v[92:95], v[158:159], v[44:45], v[92:95]\n" \
  "s_nop 0\n" \
  "v_cvt_pk_bf16_f32 v160, v64, v65\n" \
  "v_cvt_pk_bf16_f32 v161, v66, v67\n" \
  "v_cvt_pk_bf16_f32 v162, v68, v69\n" \
  "v_cvt_pk_bf16_f32 v163, v70, v71\n" \
  "ds_write_b128 v7, v[160:163] offset:0\n" \
  "v_cvt_pk_bf16_f32 v164, v72, v73\n" \
  "v_cvt_pk_bf16_f32 v165, v74, v75\n" \
  "v_cvt_pk_bf16_f32 v166, v76, v77\n" \
  "v_cvt_pk_bf16_f32 v167, v78, v79\n" \
  "ds_write_b128 v7, v[164:167] offset:1024\n" \
  "v_cvt_pk_bf16_f32 v168, v80, v81\n" \
  "v_cvt_pk_bf16_f32 v169, v82, v83\n" \
  "v_cvt_pk_bf16_f32 v170, v84, v85\n" \
  "v_cvt_pk_bf16_f32 v171, v86, v87\n" \
  "ds_write_b128 v7, v[168:171] offset:2048\n" \
  "v_cvt_pk_bf16_f32 v172, v88, v89\n" \
  "v_cvt_pk_bf16_f32 v173, v90, v91\n" \
  "v_cvt_pk_bf16_f32 v174, v92, v93\n" \
  "v_cvt_pk_bf16_f32 v175, v94, v95\n" \
  "ds_write_b128 v7, v[172:175] offset:3072\n" \
  "v_add_u32 v7, s66, v7\n" \
  "s_waitcnt lgkmcnt(0)\n" \
  "s_barrier\n" \
  "ds_read_b128 v[48:51], v1 offset:0\n" \
  "ds_read_b128 v[52:55], v1 offset:4096\n" \
  "ds_read_b128 v[56:59], v1 offset:1024\n" \
  "ds_read_b128 v[60:63], v1 offset:5120\n" \
  "ds_read_b128 v[64:67], v1 offset:2048\n" \
  "ds_read_b128 v[68:71], v1 offset:6144\n" \
  "ds_read_b128 v[72:75], v1 offset:3072\n" \
  "ds_read_b128 v[76:79], v1 offset:7168\n" \
  "ds_read_b128 v[80:83], v1 offset:8192\n" \
  "ds_read_b128 v[84:87], v1 offset:12288\n" \
  "ds_read_b128 v[88:91], v1 offset:9216\n" \
  "ds_read_b128 v[92:95], v1 offset:13312\n" \
  "ds_read_b128 v[96:99], v1 offset:10240\n" \
  "ds_read_b128 v[100:103], v1 offset:14336\n" \
  "v_readfirstlane_b32 s73, v37\n" \
  "s_sub_u32 s63, s38, s69\n" \
  "s_bfe_u32 s64, s73, 0x80010\n" \
  "s_sub_u32 s64, s64, s67\n" \
  "s_or_b32 s63, s63, s64\n" \
  "s_bfe_u32 s64, s73, 0x80008\n" \
  "s_and_b32 s73, s73, 0xff\n" \
  "s_min_u32 s73, s73, s64\n" \
  "s_or_b32 s63, s63, s68\n" \
  "s_cmp_lt_i32 s63, 0\n" \
  "s_cbranch_scc1 .LCX_E3_%=\n" \
  "v_mov_b32 v35, 0\n" \
  "global_load_dwordx4 v[128:131], v2, s[48:49] offset:0 sc1\n" \
  "global_load_dwordx4 v[132:135], v2, s[48:49] offset:512 sc1\n" \
  "global_load_dwordx4 v[136:139], v2, s[48:49] offset:1024 sc1\n" \
  "global_load_dwordx4 v[140:143], v2, s[48:49] offset:1536 sc1\n" \
  "global_load_dwordx4 v[144:147], v3, s[48:49] offset:0 sc1\n" \
  "global_load_dwordx4 v[148:151], v3, s[48:49] offset:512 sc1\n" \
  "global_load_dwordx4 v[152:155], v3, s[48:49] offset:1024 sc1\n" \
  "global_load_dwordx4 v[156:159], v3, s[48:49] offset:1536 sc1\n" \
  "global_load_dword v35, v35, s[50:51] sc1\n" \
  "v_mov_b32 v33, s67\n" \
  "ds_write_b32 v38, v33 offset:8\n" \
  "s_mov_b32 s68, -1\n" \
  ".LCX_E3_%=:\n" \
  "s_waitcnt lgkmcnt(12)\n" \
  "v_mfma_f32_16x16x32_bf16 v[112:115], v[192:195], v[48:51], 0\n" \
  "v_mfma_f32_16x16x32_bf16 v[116:119], v[224:227], v[48:51], 0\n" \
  "v_mfma_f32_16x16x32_bf16 v[120:123], v[192:195], v[52:55], 0\n" \
  "v_mfma_f32_16x16x32_bf16 v[124:127], v[224:227], v[52:55], 0\n" \
  "ds_read_b128 v[104:107], v1 offset:11264\n" \
  "ds_read_b128 v[108:111], v1 offset:15360\n" \
  "s_waitcnt lgkmcnt(12)\n" \
  "v_mfma_f32_16x16x32_bf16 v[112:115], v[196:199], v[56:59], v[112:115]\n" \
  "v_mfma_f32_16x16x32_bf16 v[116:119], v[228:231], v[56:59], v[116:119]\n" \
  "v_mfma_f32_16x16x32_bf16 v[120:123], v[196:199], v[60:63], v[120:123]\n" \
  "v_mfma_f32_16x16x32_bf16 v[124:127], v[228:231], v[60:63], v[124:127]\n" \
  "s_waitcnt lgkmcnt(10)\n" \
  "v_mfma_f32_16x16x32_bf16 v[112:115], v[200:203], v[64:67], v[112:115]\n" \
  "v_mfma_f32_16x16x32_bf16 v[116:119], v[232:235], v[64:67], v[116:119]\n" \
  "v_mfma_f32_16x16x32_bf16 v[120:123], v[200:203], v[68:71], v[120:123]\n" \
  "v_mfma_f32_16x16x32_bf16 v[124:127], v[232:235], v[68:71], v[124:127]\n" \
  "s_waitcnt lgkmcnt(8)\n" \
  "v_mfma_f32_16x16x32_bf16 v[112:115], v[204:207], v[72:75], v[112:115]\n" \
  "v_mfma_f32_16x16x32_bf16 v[116:119], v[236:239], v[72:75], v[116:119]\n" \
  "v_mfma_f32_16x16x32_bf16 v[120:123], v[204:207], v[76:79], v[120:123]\n" \
  "v_mfma_f32_16x16x32_bf16 v[124:127], v[236:239], v[76:79], v[124:127]\n" \
  "s_waitcnt lgkmcnt(6)\n" \
  "v_mfma_f32_16x16x32_bf16 v[112:115], v[208:211], v[80:83], v[112:115]\n" \
  "v_mfma_f32_16x16x32_bf16 v[116:119], v[240:243], v[80:83], v[116:119]\n" \
  "v_mfma_f32_16x16x32_bf16 v[120:123], v[208:211], v[84:87], v[120:123]\n" \
  "v_mfma_f32_16x16x32_bf16 v[124:127], v[240:243], v[84:87], v[124:127]\n" \
  "s_waitcnt lgkmcnt(4)\n" \
  "v_mfma_f32_16x16x32_bf16 v[112:115], v[212:215], v[88:91], v[112:115]\n" \
  "v_mfma_f32_16x16x32_bf16 v[116:119], v[244:247], v[88:91], v[116:119]\n" \
  "v_mfma_f32_16x16x32_bf16 v[120:123], v[212:215], v[92:95], v[120:123]\n" \
  "v_mfma_f32_16x16x32_bf16 v[124:127], v[244:247], v[92:95], v[124:127]\n" \
  "s_memrealtime s[38:39]\n" \
  "ds_read_b32 v37, v38\n" \
  "s_waitcnt lgkmcnt(3)\n" \
  "v_mfma_f32_16x16x32_bf16 v[112:115], v[216:219], v[96:99], v[112:115]\n" \
  "v_mfma_f32_16x16x32_bf16 v[116:119], v[248:251], v[96:99], v[116:119]\n" \
  "v_mfma_f32_16x16x32_bf16 v[120:123], v[216:219], v[100:103], v[120:123]\n" \
  "v_mfma_f32_16x16x32_bf16 v[124:127], v[248:251], v[100:103], v[124:127]\n" \
  "s_waitcnt lgkmcnt(0)\n" \
  "v_mfma_f32_16x16x32_bf16 v[112:115], v[220:223], v[104:107], v[112:115]\n" \
  "v_mfma_f32_16x16x32_bf16 v[116:119], v[252:255], v[104:107], v[116:119]\n" \
  "v_mfma_f32_16x16x32_bf16 v[120:123], v[220:223], v[108:111], v[120:123]\n" \
  "v_mfma_f32_16x16x32_bf16 v[124:127], v[252:255], v[108:111], v[124:127]\n" \
  "v_add_u32 v1, s66, v1\n" \
  "s_sub_u32 s66, 0, s66\n" \
  "s_add_u32 s64, s54, 16384\n" \
  "s_mov_b32 s61, 1\n" \
  "s_nop 1\n" \
  "v_cvt_pk_bf16_f32 v20, v112, v113\n" \
  "v_cvt_pk_bf16_f32 v21, v114, v115\n" \
  "v_cvt_pk_bf16_f32 v22, v116, v117\n" \
  "v_cvt_pk_bf16_f32 v23, v118, v119\n" \
  "buffer_store_dwordx4 v[20:23], v4, s[40:43], s54 offen sc1\n" \
  "v_cvt_pk_bf16_f32 v24, v120, v121\n" \
  "v_cvt_pk_bf16_f32 v25, v122, v123\n" \
  "v_cvt_pk_bf16_f32 v26, v124, v125\n" \
  "v_cvt_pk_bf16_f32 v27, v126, v127\n" \
  "buffer_store_dwordx4 v[24:27], v4, s[40:43], s64 offen sc1\n" \
  "v_readfirstlane_b32 s73, v37\n" \
  "s_sub_u32 s63, s38, s69\n" \
  "s_bfe_u32 s64, s73, 0x80010\n" \
  "s_sub_u32 s64, s64, s67\n" \
  "s_or_b32 s63, s63, s64\n" \
  "s_bfe_u32 s64, s73, 0x80008\n" \
  "s_and_b32 s73, s73, 0xff\n" \
  "s_min_u32 s73, s73, s64\n" \
  "s_or_b32 s63, s63, s68\n" \
  "s_cmp_lt_i32 s63, 0\n" \
  "s_cbranch_scc1 .LCX_E4_%=\n" \
  "v_mov_b32 v35, 0\n" \
  "global_load_dwordx4 v[128:131], v2, s[48:49] offset:0 sc1\n" \
  "global_load_dwordx4 v[132:135], v2, s[48:49] offset:512 sc1\n" \
  "global_load_dwordx4 v[136:139], v2, s[48:49] offset:1024 sc1\n" \
  "global_load_dwordx4 v[140:143], v2, s[48:49] offset:1536 sc1\n" \
  "global_load_dwordx4 v[144:147], v3, s[48:49] offset:0 sc1\n" \
  "global_load_dwordx4 v[148:151], v3, s[48:49] offset:512 sc1\n" \
  "global_load_dwordx4 v[152:155], v3, s[48:49] offset:1024 sc1\n" \
  "global_load_dwordx4 v[156:159], v3, s[48:49] offset:1536 sc1\n" \
  "global_load_dword v35, v35, s[50:51] sc1\n" \
  "v_mov_b32 v33, s67\n" \
  "ds_write_b32 v38, v33 offset:8\n" \
  "s_mov_b32 s68, -1\n" \
  ".LCX_E4_%=:\n" \
  "s_add_u32 s54, s54, 0x80000\n" \
  "s_sub_u32 s60, s60, 1\n" \
  "s_cmp_lg_u32 s60, 0\n" \
  "s_cbranch_scc1 .LCX_TOP_%=\n" \
  "s_waitcnt vmcnt(0)\n" \
  "v_mov_b32 v46, s59\n" \
  "v_mov_b32 v47, 1\n" \
  "s_mov_b64 exec, 1\n" \
  "ds_add_rtn_u32 v46, v46, v47\n" \
  "s_mov_b64 exec, -1\n" \
  "s_xor_b32 s59, s59, 4\n" \
  "s_mov_b32 s61, 0\n" \
  "s_waitcnt lgkmcnt(0)\n" \
  "v_readfirstlane_b32 s63, v46\n" \
  "v_lshlrev_b32 v46, 5, v5\n" \
  "s_and_b32 s63, s63, 7\n" \
  "s_cmp_eq_u32 s63, 7\n" \
  "s_cbranch_scc0 .LCX_W5_%=\n" \
  "s_mov_b64 exec, 15\n" \
  "global_store_dword v46, v47, s[52:53] sc1\n" \
  "s_mov_b64 exec, -1\n" \
  ".LCX_W5_%=:\n" \
  "s_add_u32 s52, s52, 2048\n" \
  "s_addc_u32 s53, s53, 0\n" \
  ".LCX_END_%=:\n" \
  "s_endpgm\n" \
  ".LCX_FAIL_%=:\n" \
  "s_add_u32 s69, s64, -48\n" \
  "s_cmp_lg_u32 s64, 0\n" \
  "s_cbranch_scc1 .LCX_WF_%=\n" \
  "s_sleep 4\n" \
  "global_load_dword v35, v35, s[50:51] sc1\n" \
  "s_sub_u32 s62, s62, 8\n" \
  "s_cbranch_scc1 .LCX_END_%=\n" \
  "s_waitcnt vmcnt(0)\n" \
  "v_readfirstlane_b32 s64, v35\n" \
  "s_branch .LCX_FAIL_%=\n"
#define AW_ASM_Y \
  "s_mov_b32 s72, 0\n" \
  "s_mov_b32 s67, 0\n" \
  "s_mov_b32 s68, 2\n" \
  "s_mov_b32 s69, 0\n" \
  "s_mov_b32 s65, 49152\n" \
  "s_mov_b32 s66, 16384\n" \
  "v_mov_b32 v5, s58\n" \
  "v_mov_b32 v60, v3\n" \
  "v_add_u32 v3, 4096, v2\n" \
  "s_mov_b32 s62, 0x40000\n" \
  "s_branch .LCY_ASK_%=\n" \
  ".LCY_BAR_%=:\n" \
  "s_barrier\n" \
  "ds_read_b128 v[8:11], v0 offset:0\n" \
  "ds_read_b128 v[12:15], v0 offset:1024\n" \
  "ds_read_b128 v[16:19], v0 offset:2048\n" \
  "s_add_u32 s68, s68, 1\n" \
  "s_add_u32 s64, s54, 16384\n" \
  "s_cmp_eq_u32 s69, 0\n" \
  "s_cbranch_scc1 .LCY_NI_%=\n" \
  "s_waitcnt vmcnt(1)\n" \
  "s_cmp_lg_u32 s61, 0\n" \
  "s_cbranch_scc1 .LCY_NJ_%=\n" \
  "s_waitcnt vmcnt(0)\n" \
  ".LCY_NJ_%=:\n" \
  "v_mov_b32 v6, s55\n" \
  "s_mov_b64 exec, 1\n" \
  "ds_add_u32 v5, v6\n" \
  "s_mov_b64 exec, -1\n" \
  "s_mov_b32 s69, 0\n" \
  "s_cmp_eq_u32 s72, 0\n" \
  "s_cbranch_scc1 .LCY_NI_%=\n" \
  "s_mov_b32 s72, 0\n" \
  "v_mov_b32 v6, s59\n" \
  "v_mov_b32 v7, 1\n" \
  "s_mov_b64 exec, 1\n" \
  "ds_add_rtn_u32 v6, v6, v7\n" \
  "s_mov_b64 exec, -1\n" \
  "s_xor_b32 s59, s59, 4\n" \
  "s_mov_b32 s61, 0\n" \
  "s_waitcnt lgkmcnt(0)\n" \
  "v_readfirstlane_b32 s63, v6\n" \
  "v_lshlrev_b32 v6, 5, v2\n" \
  "s_and_b32 s63, s63, 7\n" \
  "s_cmp_eq_u32 s63, 7\n" \
  "s_cbranch_scc0 .LCY_W1_%=\n" \
  "s_mov_b64 exec, 15\n" \
  "global_store_dword v6, v7, s[52:53] sc1\n" \
  "s_mov_b64 exec, -1\n" \
  ".LCY_W1_%=:\n" \
  "s_add_u32 s52, s52, 2048\n" \
  "s_addc_u32 s53, s53, 0\n" \
  ".LCY_NI_%=:\n" \
  "v_cmp_eq_u32 vcc, 0, v4\n" \
  "s_cmp_eq_u64 vcc, 0\n" \
  "s_cselect_b32 s70, 1, 0\n" \
  "s_cbranch_scc0 .LCY_NS_%=\n" \
  "v_lshlrev_b32 v6, 16, s55\n" \
  "s_mov_b64 exec, 1\n" \
  "ds_add_u32 v5, v6\n" \
  "s_mov_b64 exec, -1\n" \
  "s_mov_b32 m0, s56\n" \
  "s_add_u32 s71, s56, 4096\n" \
  "global_load_lds_dwordx4 v2, s[48:49] offset:0 sc1\n" \
  "global_load_lds_dwordx4 v2, s[48:49] offset:1024 sc1\n" \
  "global_load_lds_dwordx4 v2, s[48:49] offset:2048 sc1\n" \
  "global_load_lds_dwordx4 v2, s[48:49] offset:3072 sc1\n" \
  "s_mov_b32 m0, s71\n" \
  "s_add_u32 s56, s56, s65\n" \
  "global_load_lds_dwordx4 v3, s[48:49] offset:0 sc1\n" \
  "global_load_lds_dwordx4 v3, s[48:49] offset:1024 sc1\n" \
  ".LCY_NS_%=:\n" \
  "ds_read_b128 v[20:23], v0 offset:3072\n" \
  "s_waitcnt lgkmcnt(3)\n" \
  "v_mfma_f32_16x16x32_bf16 v[32:35], v[64:67], v[8:11], 0\n" \
  "v_mfma_f32_16x16x32_bf16 v[36:39], v[96:99], v[8:11], 0\n" \
  "v_mfma_f32_16x16x32_bf16 v[40:43], v[128:131], v[8:11], 0\n" \
  "v_mfma_f32_16x16x32_bf16 v[44:47], v[160:163], v[8:11], 0\n" \
  "v_mfma_f32_16x16x32_bf16 v[48:51], v[192:195], v[8:11], 0\n" \
  "v_mfma_f32_16x16x32_bf16 v[52:55], v[224:227], v[8:11], 0\n" \
  "ds_read_b128 v[8:11], v0 offset:8192\n" \
  "s_waitcnt lgkmcnt(3)\n" \
  "v_mfma_f32_16x16x32_bf16 v[32:35], v[68:71], v[12:15], v[32:35]\n" \
  "v_mfma_f32_16x16x32_bf16 v[36:39], v[100:103], v[12:15], v[36:39]\n" \
  "v_mfma_f32_16x16x32_bf16 v[40:43], v[132:135], v[12:15], v[40:43]\n" \
  "v_mfma_f32_16x16x32_bf16 v[44:47], v[164:167], v[12:15], v[44:47]\n" \
  "v_mfma_f32_16x16x32_bf16 v[48:51], v[196:199], v[12:15], v[48:51]\n" \
  "v_mfma_f32_16x16x32_bf16 v[52:55], v[228:231], v[12:15], v[52:55]\n" \
  "ds_read_b128 v[12:15], v0 offset:9216\n" \
  "s_waitcnt lgkmcnt(3)\n" \
  "v_mfma_f32_16x16x32_bf16 v[32:35], v[72:75], v[16:19], v[32:35]\n" \
  "v_mfma_f32_16x16x32_bf16 v[36:39], v[104:107], v[16:19], v[36:39]\n" \
  "v_mfma_f32_16x16x32_bf16 v[40:43], v[136:139], v[16:19], v[40:43]\n" \
  "v_mfma_f32_16x16x32_bf16 v[44:47], v[168:171], v[16:19], v[44:47]\n" \
  "v_mfma_f32_16x16x32_bf16 v[48:51], v[200:203], v[16:19], v[48:51]\n" \
  "v_mfma_f32_16x16x32_bf16 v[52:55], v[232:235], v[16:19], v[52:55]\n" \
  "ds_read_b128 v[16:19], v0 offset:10240\n" \
  "s_waitcnt lgkmcnt(3)\n" \
  "v_mfma_f32_16x16x32_bf16 v[32:35], v[76:79], v[20:23], v[32:35]\n" \
  "v_mfma_f32_16x16x32_bf16 v[36:39], v[108:111], v[20:23], v[36:39]\n" \
  "v_mfma_f32_16x16x32_bf16 v[40:43], v[140:143], v[20:23], v[40:43]\n" \
  "v_mfma_f32_16x16x32_bf16 v[44:47], v[172:175], v[20:23], v[44:47]\n" \
  "v_mfma_f32_16x16x32_bf16 v[48:51], v[204:207], v[20:23], v[48:51]\n" \
  "v_mfma_f32_16x16x32_bf16 v[52:55], v[236:239], v[20:23], v[52:55]\n" \
  "ds_read_b128 v[20:23], v0 offset:11264\n" \
  "s_waitcnt lgkmcnt(3)\n" \
  "v_mfma_f32_16x16x32_bf16 v[32:35], v[80:83], v[8:11], v[32:35]\n" \
  "v_mfma_f32_16x16x32_bf16 v[36:39], v[112:115], v[8:11], v[36:39]\n" \
  "v_mfma_f32_16x16x32_bf16 v[40:43], v[144:147], v[8:11], v[40:43]\n" \
  "v_mfma_f32_16x16x32_bf16 v[44:47], v[176:179], v[8:11], v[44:47]\n" \
  "v_mfma_f32_16x16x32_bf16 v[48:51], v[208:211], v[8:11], v[48:51]\n" \
  "v_mfma_f32_16x16x32_bf16 v[52:55], v[240:243], v[8:11], v[52:55]\n" \
  "ds_read_b128 v[8:11], v0 offset:4096\n" \
  "s_waitcnt lgkmcnt(3)\n" \
  "v_mfma_f32_16x16x32_bf16 v[32:35], v[84:87], v[12:15], v[32:35]\n" \
  "v_mfma_f32_16x16x32_bf16 v[36:39], v[116:119], v[12:15], v[36:39]\n" \
  "v_mfma_f32_16x16x32_bf16 v[40:43], v[148:151], v[12:15], v[40:43]\n" \
  "v_mfma_f32_16x16x32_bf16 v[44:47], v[180:183], v[12:15], v[44:47]\n" \
  "v_mfma_f32_16x16x32_bf16 v[48:51], v[212:215], v[12:15], v[48:51]\n" \
  "v_mfma_f32_16x16x32_bf16 v[52:55], v[244:247], v[12:15], v[52:55]\n" \
  "ds_read_b128 v[12:15], v0 offset:5120\n" \
  "s_waitcnt lgkmcnt(3)\n" \
  "v_mfma_f32_16x16x32_bf16 v[32:35], v[88:91], v[16:19], v[32:35]\n" \
  "v_mfma_f32_16x16x32_bf16 v[36:39], v[120:123], v[16:19], v[36:39]\n" \
  "v_mfma_f32_16x16x32_bf16 v[40:43], v[152:155], v[16:19], v[40:43]\n" \
  "v_mfma_f32_16x16x32_bf16 v[44:47], v[184:187], v[16:19], v[44:47]\n" \
  "v_mfma_f32_16x16x32_bf16 v[48:51], v[216:219], v[16:19], v[48:51]\n" \
  "v_mfma_f32_16x16x32_bf16 v[52:55], v[248:251], v[16:19], v[52:55]\n" \
  "ds_read_b128 v[16:19], v0 offset:6144\n" \
  "s_waitcnt lgkmcnt(3)\n" \
  "v_mfma_f32_16x16x32_bf16 v[32:35], v[92:95], v[20:23], v[32:35]\n" \
  "v_mfma_f32_16x16x32_bf16 v[36:39], v[124:127], v[20:23], v[36:39]\n" \
  "v_mfma_f32_16x16x32_bf16 v[40:43], v[156:159], v[20:23], v[40:43]\n" \
  "v_mfma_f32_16x16x32_bf16 v[44:47], v[188:191], v[20:23], v[44:47]\n" \
  "v_mfma_f32_16x16x32_bf16 v[48:51], v[220:223], v[20:23], v[48:51]\n" \
  "v_mfma_f32_16x16x32_bf16 v[52:55], v[252:255], v[20:23], v[52:55]\n" \
  "ds_read_b128 v[20:23], v0 offset:7168\n" \
  "s_waitcnt lgkmcnt(3)\n" \
  "s_nop 1\n" \
  "v_cvt_pk_bf16_f32 v24, v32, v33\n" \
  "v_cvt_pk_bf16_f32 v25, v34, v35\n" \
  "v_cvt_pk_bf16_f32 v26, v36, v37\n" \
  "v_cvt_pk_bf16_f32 v27, v38, v39\n" \
  "s_nop 0\n" \
  "v_mfma_f32_16x16x32_bf16 v[32:35], v[64:67], v[8:11], 0\n" \
  "v_mfma_f32_16x16x32_bf16 v[36:39], v[96:99], v[8:11], 0\n" \
  "buffer_store_dwordx4 v[24:27], v1, s[40:43], s54 offen offset:0 sc1\n" \
  "v_cvt_pk_bf16_f32 v28, v40, v41\n" \
  "v_cvt_pk_bf16_f32 v29, v42, v43\n" \
  "v_cvt_pk_bf16_f32 v30, v44, v45\n" \
  "v_cvt_pk_bf16_f32 v31, v46, v47\n" \
  "s_nop 0\n" \
  "v_mfma_f32_16x16x32_bf16 v[40:43], v[128:131], v[8:11], 0\n" \
  "v_mfma_f32_16x16x32_bf16 v[44:47], v[160:163], v[8:11], 0\n" \
  "buffer_store_dwordx4 v[28:31], v1, s[40:43], s54 offen offset:128 sc1\n" \
  "v_cvt_pk_bf16_f32 v56, v48, v49\n" \
  "v_cvt_pk_bf16_f32 v57, v50, v51\n" \
  "v_cvt_pk_bf16_f32 v58, v52, v53\n" \
  "v_cvt_pk_bf16_f32 v59, v54, v55\n" \
  "s_nop 0\n" \
  "v_mfma_f32_16x16x32_bf16 v[48:51], v[192:195], v[8:11], 0\n" \
  "v_mfma_f32_16x16x32_bf16 v[52:55], v[224:227], v[8:11], 0\n" \
  "buffer_store_dwordx4 v[56:59], v1, s[40:43], s54 offen offset:256 sc1\n" \
  "ds_read_b128 v[8:11], v0 offset:12288\n" \
  "s_waitcnt lgkmcnt(3)\n" \
  "v_mfma_f32_16x16x32_bf16 v[32:35], v[68:71], v[12:15], v[32:35]\n" \
  "v_mfma_f32_16x16x32_bf16 v[36:39], v[100:103], v[12:15], v[36:39]\n" \
  "v_mfma_f32_16x16x32_bf16 v[40:43], v[132:135], v[12:15], v[40:43]\n" \
  "v_mfma_f32_16x16x32_bf16 v[44:47], v[164:167], v[12:15], v[44:47]\n" \
  "v_mfma_f32_16x16x32_bf16 v[48:51], v[196:199], v[12:15], v[48:51]\n" \
  "v_mfma_f32_16x16x32_bf16 v[52:55], v[228:231], v[12:15], v[52:55]\n" \
  "ds_read_b128 v[12:15], v0 offset:13312\n" \
  "s_waitcnt lgkmcnt(3)\n" \
  "v_mfma_f32_16x16x32_bf16 v[32:35], v[72:75], v[16:19], v[32:35]\n" \
  "v_mfma_f32_16x16x32_bf16 v[36:39], v[104:107], v[16:19], v[36:39]\n" \
  "v_mfma_f32_16x16x32_bf16 v[40:43], v[136:139], v[16:19], v[40:43]\n" \
  "v_mfma_f32_16x16x32_bf16 v[44:47], v[168:171], v[16:19], v[44:47]\n" \
  "v_mfma_f32_16x16x32_bf16 v[48:51], v[200:203], v[16:19], v[48:51]\n" \
  "v_mfma_f32_16x16x32_bf16 v[52:55], v[232:235], v[16:19], v[52:55]\n" \
  "ds_read_b128 v[16:19], v0 offset:14336\n" \
  "s_waitcnt lgkmcnt(3)\n" \
  "v_mfma_f32_16x16x32_bf16 v[32:35], v[76:79], v[20:23], v[32:35]\n" \
  "v_mfma_f32_16x16x32_bf16 v[36:39], v[108:111], v[20:23], v[36:39]\n" \
  "v_mfma_f32_16x16x32_bf16 v[40:43], v[140:143], v[20:23], v[40:43]\n" \
  "v_mfma_f32_16x16x32_bf16 v[44:47], v[172:175], v[20:23], v[44:47]\n" \
  "v_mfma_f32_16x16x32_bf16 v[48:51], v[204:207], v[20:23], v[48:51]\n" \
  "v_mfma_f32_16x16x32_bf16 v[52:55], v[236:239], v[20:23], v[52:55]\n" \
  "ds_read_b32 v7, v5 offset:8\n" \
  "ds_read_b128 v[20:23], v0 offset:15360\n" \
  "s_waitcnt lgkmcnt(4)\n" \
  "v_mfma_f32_16x16x32_bf16 v[32:35], v[80:83], v[8:11], v[32:35]\n" \
  "v_mfma_f32_16x16x32_bf16 v[36:39], v[112:115], v[8:11], v[36:39]\n" \
  "v_mfma_f32_16x16x32_bf16 v[40:43], v[144:147], v[8:11], v[40:43]\n" \
  "v_mfma_f32_16x16x32_bf16 v[44:47], v[176:179], v[8:11], v[44:47]\n" \
  "v_mfma_f32_16x16x32_bf16 v[48:51], v[208:211], v[8:11], v[48:51]\n" \
  "v_mfma_f32_16x16x32_bf16 v[52:55], v[240:243], v[8:11], v[52:55]\n" \
  "s_waitcnt lgkmcnt(3)\n" \
  "v_mfma_f32_16x16x32_bf16 v[32:35], v[84:87], v[12:15], v[32:35]\n" \
  "v_mfma_f32_16x16x32_bf16 v[36:39], v[116:119], v[12:15], v[36:39]\n" \
  "v_mfma_f32_16x16x32_bf16 v[40:43], v[148:151], v[12:15], v[40:43]\n" \
  "v_mfma_f32_16x16x32_bf16 v[44:47], v[180:183], v[12:15], v[44:47]\n" \
  "v_mfma_f32_16x16x32_bf16 v[48:51], v[212:215], v[12:15], v[48:51]\n" \
  "v_mfma_f32_16x16x32_bf16 v[52:55], v[244:247], v[12:15], v[52:55]\n" \
  "s_waitcnt lgkmcnt(2)\n" \
  "v_mfma_f32_16x16x32_bf16 v[32:35], v[88:91], v[16:19], v[32:35]\n" \
  "v_mfma_f32_16x16x32_bf16 v[36:39], v[120:123], v[16:19], v[36:39]\n" \
  "v_mfma_f32_16x16x32_bf16 v[40:43], v[152:155], v[16:19], v[40:43]\n" \
  "v_mfma_f32_16x16x32_bf16 v[44:47], v[184:187], v[16:19], v[44:47]\n" \
  "v_mfma_f32_16x16x32_bf16 v[48:51], v[216:219], v[16:19], v[48:51]\n" \
  "v_mfma_f32_16x16x32_bf16 v[52:55], v[248:251], v[16:19], v[52:55]\n" \
  "s_waitcnt lgkmcnt(0)\n" \
  "v_mfma_f32_16x16x32_bf16 v[32:35], v[92:95], v[20:23], v[32:35]\n" \
  "v_mfma_f32_16x16x32_bf16 v[36:39], v[124:127], v[20:23], v[36:39]\n" \
  "v_mfma_f32_16x16x32_bf16 v[40:43], v[156:159], v[20:23], v[40:43]\n" \
  "v_mfma_f32_16x16x32_bf16 v[44:47], v[188:191], v[20:23], v[44:47]\n" \
  "v_mfma_f32_16x16x32_bf16 v[48:51], v[220:223], v[20:23], v[48:51]\n" \
  "v_mfma_f32_16x16x32_bf16 v[52:55], v[252:255], v[20:23], v[52:55]\n" \
  "v_add_u32 v0, s66, v0\n" \
  "s_sub_u32 s66, 0, s66\n" \
  "s_add_u32 s54, s54, 0x80000\n" \
  "s_mov_b32 s62, 0x40000\n" \
  "v_readfirstlane_b32 s63, v7\n" \
  "v_cvt_pk_bf16_f32 v24, v32, v33\n" \
  "v_cvt_pk_bf16_f32 v25, v34, v35\n" \
  "v_cvt_pk_bf16_f32 v26, v36, v37\n" \
  "v_cvt_pk_bf16_f32 v27, v38, v39\n" \
  "buffer_store_dwordx4 v[24:27], v1, s[40:43], s64 offen offset:0 sc1\n" \
  "v_cvt_pk_bf16_f32 v28, v40, v41\n" \
  "v_cvt_pk_bf16_f32 v29, v42, v43\n" \
  "v_cvt_pk_bf16_f32 v30, v44, v45\n" \
  "v_cvt_pk_bf16_f32 v31, v46, v47\n" \
  "buffer_store_dwordx4 v[28:31], v1, s[40:43], s64 offen offset:128 sc1\n" \
  "v_cvt_pk_bf16_f32 v56, v48, v49\n" \
  "v_cvt_pk_bf16_f32 v57, v50, v51\n" \
  "v_cvt_pk_bf16_f32 v58, v52, v53\n" \
  "v_cvt_pk_bf16_f32 v59, v54, v55\n" \
  "buffer_store_dwordx4 v[56:59], v1, s[40:43], s64 offen offset:256 sc1\n" \
  "s_cmp_eq_u32 s70, 0\n" \
  "s_cbranch_scc1 .LCY_ND_%=\n" \
  "global_load_lds_dwordx4 v3, s[48:49] offset:2048 sc1\n" \
  "global_load_lds_dwordx4 v3, s[48:49] offset:3072 sc1\n" \
  "s_add_u32 s48, s48, 0x80000\n" \
  "s_addc_u32 s49, s49, 0\n" \
  "s_mov_b32 m0, s57\n" \
  "s_add_u32 s57, s57, s65\n" \
  "global_load_lds_dwordx4 v2, s[50:51] offset:0 sc1\n" \
  "global_load_lds_dwordx4 v2, s[50:51] offset:1024 sc1\n" \
  "s_sub_u32 s65, 0, s65\n" \
  "s_add_u32 s67, s67, 4096\n" \
  "global_load_lds_dwordx4 v2, s[50:51] offset:2048 sc1\n" \
  "s_sub_u32 s68, s68, 1\n" \
  "s_mov_b32 s69, 1\n" \
  "global_load_lds_dwordx4 v2, s[50:51] offset:3072 sc1\n" \
  "s_add_u32 s50, s50, 0x40000\n" \
  "s_addc_u32 s51, s51, 0\n" \
  "v_mov_b32 v4, 0\n" \
  "buffer_load_dword v4, v60, s[44:47], s67 offen sc1\n" \
  "s_add_u32 s63, s63, s60\n" \
  "s_mov_b32 s61, 0\n" \
  "s_cmp_eq_u32 s63, 66\n" \
  "s_cselect_b32 s72, 1, 0\n" \
  "s_cbranch_scc1 .LCY_DEF_%=\n" \
  "s_waitcnt vmcnt(7)\n" \
  "s_branch .LCY_WC_%=\n" \
  ".LCY_ND_%=:\n" \
  "v_mov_b32 v4, 0\n" \
  "buffer_load_dword v4, v60, s[44:47], s67 offen sc1\n" \
  "s_waitcnt vmcnt(0)\n" \
  ".LCY_WC_%=:\n" \
  "v_mov_b32 v6, s59\n" \
  "v_mov_b32 v7, 1\n" \
  "s_mov_b64 exec, 1\n" \
  "ds_add_rtn_u32 v6, v6, v7\n" \
  "s_mov_b64 exec, -1\n" \
  "s_xor_b32 s59, s59, 4\n" \
  "s_mov_b32 s61, 0\n" \
  "s_waitcnt lgkmcnt(0)\n" \
  "v_readfirstlane_b32 s63, v6\n" \
  "v_lshlrev_b32 v6, 5, v2\n" \
  "s_and_b32 s63, s63, 7\n" \
  "s_cmp_eq_u32 s63, 7\n" \
  "s_cbranch_scc0 .LCY_W2_%=\n" \
  "s_mov_b64 exec, 15\n" \
  "global_store_dword v6, v7, s[52:53] sc1\n" \
  "s_mov_b64 exec, -1\n" \
  "s_mov_b32 s61, 1\n" \
  ".LCY_W2_%=:\n" \
  "s_add_u32 s52, s52, 2048\n" \
  "s_addc_u32 s53, s53, 0\n" \
  ".LCY_DEF_%=:\n" \
  "s_sub_u32 s60, s60, 1\n" \
  "s_cmp_eq_u32 s60, 0\n" \
  "s_cbranch_scc1 .LCY_END_%=\n" \
  ".LCY_NEED_%=:\n" \
  "s_cmp_eq_u32 s68, 0\n" \
  "s_cbranch_scc1 .LCY_BAR_%=\n" \
  "v_cmp_eq_u32 vcc, 0, v4\n" \
  "s_cmp_eq_u64 vcc, 0\n" \
  "s_cbranch_scc1 .LCY_GET_%=\n" \
  "s_cmp_ge_u32 s67, 0x40000\n" \
  "s_cbranch_scc1 .LCY_BAR_%=\n" \
  "s_sleep 4\n" \
  "ds_read_b32 v6, v5 offset:16\n" \
  "s_waitcnt lgkmcnt(0)\n" \
  "v_readfirstlane_b32 s63, v6\n" \
  "s_add_u32 s63, s63, s60\n" \
  "s_cmp_ge_u32 s63, 66\n" \
  "s_cbranch_scc1 .LCY_BAR_%=\n" \
  ".LCY_ASK_%=:\n" \
  "v_mov_b32 v4, 0\n" \
  "buffer_load_dword v4, v60, s[44:47], s67 offen sc1\n" \
  "s_sub_u32 s62, s62, 1\n" \
  "s_cbranch_scc1 .LCY_END_%=\n" \
  "s_waitcnt vmcnt(0)\n" \
  "s_branch .LCY_NEED_%=\n" \
  ".LCY_GET_%=:\n" \
  "v_lshlrev_b32 v6, 16, s55\n" \
  "s_mov_b64 exec, 1\n" \
  "ds_add_u32 v5, v6\n" \
  "s_mov_b64 exec, -1\n" \
  "s_mov_b32 m0, s56\n" \
  "s_add_u32 s71, s56, 4096\n" \
  "global_load_lds_dwordx4 v2, s[48:49] offset:0 sc1\n" \
  "global_load_lds_dwordx4 v2, s[48:49] offset:1024 sc1\n" \
  "global_load_lds_dwordx4 v2, s[48:49] offset:2048 sc1\n" \
  "global_load_lds_dwordx4 v2, s[48:49] offset:3072 sc1\n" \
  "s_mov_b32 m0, s71\n" \
  "s_add_u32 s56, s56, s65\n" \
  "global_load_lds_dwordx4 v3, s[48:49] offset:0 sc1\n" \
  "global_load_lds_dwordx4 v3, s[48:49] offset:1024 sc1\n" \
  "global_load_lds_dwordx4 v3, s[48:49] offset:2048 sc1\n" \
  "global_load_lds_dwordx4 v3, s[48:49] offset:3072 sc1\n" \
  "s_add_u32 s48, s48, 0x80000\n" \
  "s_addc_u32 s49, s49, 0\n" \
  "s_mov_b32 m0, s57\n" \
  "s_add_u32 s57, s57, s65\n" \
  "global_load_lds_dwordx4 v2, s[50:51] offset:0 sc1\n" \
  "global_load_lds_dwordx4 v2, s[50:51] offset:1024 sc1\n" \
  "s_sub_u32 s65, 0, s65\n" \
  "s_add_u32 s67, s67, 4096\n" \
  "global_load_lds_dwordx4 v2, s[50:51] offset:2048 sc1\n" \
  "s_sub_u32 s68, s68, 1\n" \
  "s_mov_b32 s69, 1\n" \
  "global_load_lds_dwordx4 v2, s[50:51] offset:3072 sc1\n" \
  "s_add_u32 s50, s50, 0x40000\n" \
  "s_addc_u32 s51, s51, 0\n" \
  "v_mov_b32 v4, 0\n" \
  "buffer_load_dword v4, v60, s[44:47], s67 offen sc1\n" \
  "s_mov_b32 s61, 0\n" \
  "s_cmp_eq_u32 s68, 0\n" \
  "s_cbranch_scc1 .LCY_BAR_%=\n" \
  "s_waitcnt vmcnt(0)\n" \
  "v_mov_b32 v6, s55\n" \
  "s_mov_b64 exec, 1\n" \
  "ds_add_u32 v5, v6\n" \
  "s_mov_b64 exec, -1\n" \
  "s_mov_b32 s69, 0\n" \
  "s_branch .LCY_NEED_%=\n" \
  ".LCY_END_%=:\n" \
  "s_endpgm\n"
template <bool AT>
__device__ __forceinline__ void role_aw_t(const MegaArgs& A, char* smem, const int L, const int a, const u64 tag) {
  const int w = A.wv;
  const int l = lane_id(), c = l & 15, g = l >> 4;
  const int qt = w & 1, hh = (w >> 1) & 1, h = 2 * a + hh;
  char* ws = A.ws;
  unsigned* FL = (unsigned*)(ws + OFF_FL);
  const unsigned sb = lds_base(smem);
  if (w == 0 && l < 8) reinterpret_cast<unsigned*>(smem + AW_CNT)[l] = 0u;
  constexpr int NP = AT ? 1 : 3;
  const int cp0 = AT ? w : 3 * w - 8;
  v8u wo[2 * NP][4];
  {
    const int q = (l >> 2) & 3, p = l & 3, f = q | ((g & 1) << 2);
    const unsigned ra = sb + (4 * g + q) * 256 + (p & 1) * 16;
#pragma unroll
    for (int r = 0; r < 2; ++r) {
#pragma unroll
      for (int k = 0; k < 2; ++k) {
        const char* base_ = (const char*)A.Wo + ((size_t)((2 * a + r) * DH + 64 * (w & 1) + 32 * k) * DM + 128 * (w >> 1)) * 2;
#pragma unroll
        for (int i = 0; i < 8; ++i) {
          const int fd = g | ((i & 1) << 2);
          glds16(base_, (unsigned)((4 * i + g) * (DM * 2) + ((c ^ (fd << 1)) << 4)), sb + w * 16384 + k * 8192 + i * 1024);
        }
      }
      SST(4 + 3 * r);
      if (r == 1) ready_wait(A, tag, ready_look(A));
      WAIT_VM(0);
      RAW_BAR();
      SST(5 + 3 * r);
#pragma unroll
      for (int k2 = 0; k2 < 2; ++k2) {
#pragma unroll
        for (int ct = 0; ct < 2 * NP; ++ct) {
          const int cp = cp0 + (ct >> 1);
          const unsigned a_ = ra + (cp >> 2) * 32768 + k2 * 16384 + ((2 * (cp & 3) + (p >> 1)) ^ f) * 32 + (ct & 1) * 8;
          const v2u r0 = trd(a_), r1 = trd(a_ + 4096), r2 = trd(a_ + 8192), r3 = trd(a_ + 12288);
          wo[ct][2 * r + k2] = v8u{r0.x, r0.y, r1.x, r1.y, r2.x, r2.y, r3.x, r3.y};
          asm volatile("" : "+v"(wo[ct][2 * r + k2]));
        }
      }
      RAW_BAR();
      SST(6 + 3 * r);
    }
  }
  WSTAMP2();
  const v4u rsW = mk_srd(ws + OFF_PW, SZ_PW);
  const int dp = (FL_FP - FL_FQ + (L * 16 + a - L * 64 * 32 - h) * 32 + 2 * hh) * 4;
  const v4u rsF = mk_srd(FL + FL_FQ + (L * 64 * 32 + h) * 32, (qt ? 0 : dp) + 64 * 4096 - 128);
  const char* p0 = AT ? ws + OFF_MEMK + L * SZ_MEM + h * 2048 : ws + OFF_QK + L * SZ_QK + ((size_t)h * 16 + 8 * qt) * 1024;
  const char* p1 = AT ? (const char*)(FL + FL_FK + L * 32 + (h >> 3) * 64 + (h & 7) * 2) : ws + OFF_VF + L * SZ_O + ((size_t)h * 8 + 4 * qt) * 1024;
  const unsigned* pFW = FL + FL_FW + (L * 64 * 16 + (a >> 2)) * 32 + (a & 3);
  const unsigned tk = sb + AW_TK + hh * 24 * 1024, voff = (c >> 1) * 2048 + (c & 1) * 64 + g * 16;
  const char* dbg = ws + WS_TOTAL + A.vb * 64 * 8;
#define AW_SOPS                                                                                                                 \
  "{s[40:43]}"(rsW), "{s[44:47]}"(rsF), "{s[48:49]}"(p0), "{s[50:51]}"(p1), "{s[52:53]}"(pFW),                                   \
      "{s54}"((L * 64 * 16 + a) * 32768 + cp0 * 128), "{s55}"(1 << (8 * qt)), "{s56}"(tk + qt * 8192), "{s57}"(tk + 16384 + qt * 4096),                  \
      "{s58}"(sb + AW_CNT + 8 + 4 * hh), "{s59}"(sb + AW_CNT), "{s60}"(NSEG), "{s[36:37]}"(dbg)                                  \
      : "memory", "vcc", "scc", "s80"
  if constexpr (AT) {
    const v8u pv = {tk + l * 16, sb + AW_SO + l * 16, (unsigned)(g * 8 + (c & 7)) * 16, (unsigned)(OFF_MEMV - OFF_MEMK) + c * 32 + (g & 1) * 16,
                    voff, (unsigned)l * 16, tk + qt * 4096 + l * 16, sb + AW_SO + w * 4096 + l * 16};
    const unsigned neg = 0xff61b1e6u;
    v8u bt;
#pragma unroll
    for (int i = 0; i < 4; ++i) {
      const unsigned d = 4 * g + i > c ? neg : 0u;
      bt[i] = qt ? 0u : d;
      bt[4 + i] = qt ? d : neg;
    }
    const unsigned e = g >= 2 ? neg : 0u;
    const v4u bm = {e, e, e, e};
    asm volatile("s_mov_b64 exec, -1\ns_waitcnt vmcnt(0) lgkmcnt(0)\ns_nop 4\n" AW_ASM_X
                 ::"{v[192:199]}"(wo[0][0]), "{v[200:207]}"(wo[0][1]), "{v[208:215]}"(wo[0][2]), "{v[216:223]}"(wo[0][3]),
                 "{v[224:231]}"(wo[1][0]), "{v[232:239]}"(wo[1][1]), "{v[240:247]}"(wo[1][2]), "{v[248:255]}"(wo[1][3]),
                 "{v[0:7]}"(pv), "{v[8:15]}"(bt), "{v[16:19]}"(bm), AW_SOPS);
  } else {
    const v4u pv = {sb + AW_SO + l * 16, voff, (unsigned)l * 16, (l & 2) ? 16u + 8 * qt : qt ? 8u * (l & 1) : dp + 4 * (l & 1)};
    asm volatile("s_mov_b64 exec, -1\ns_waitcnt vmcnt(0) lgkmcnt(0)\ns_nop 4\n" AW_ASM_Y
                 ::"{v[64:71]}"(wo[0][0]), "{v[72:79]}"(wo[0][1]), "{v[80:87]}"(wo[0][2]), "{v[88:95]}"(wo[0][3]),
                 "{v[96:103]}"(wo[1][0]), "{v[104:111]}"(wo[1][1]), "{v[112:119]}"(wo[1][2]), "{v[120:127]}"(wo[1][3]),
                 "{v[128:135]}"(wo[2][0]), "{v[136:143]}"(wo[2][1]), "{v[144:151]}"(wo[2][2]), "{v[152:159]}"(wo[2][3]),
                 "{v[160:167]}"(wo[3][0]), "{v[168:175]}"(wo[3][1]), "{v[176:183]}"(wo[3][2]), "{v[184:191]}"(wo[3][3]),
                 "{v[192:199]}"(wo[4][0]), "{v[200:207]}"(wo[4][1]), "{v[208:215]}"(wo[4][2]), "{v[216:223]}"(wo[4][3]),
                 "{v[224:231]}"(wo[5][0]), "{v[232:239]}"(wo[5][1]), "{v[240:247]}"(wo[5][2]), "{v[248:255]}"(wo[5][3]),
                 "{v[0:3]}"(pv), AW_SOPS);
  }
#undef AW_SOPS
}
__device__ __forceinline__ void role_aw(const MegaArgs& A, char* smem, const int L, const int a, const u64 tag) {
  if (A.wv < 4) role_aw_t<true>(A, smem, L, a, tag);
  else role_aw_t<false>(A, smem, L, a, tag);
}

#define P_ASM \
  "s_mov_b32 s68, 0\n" \
  "s_mov_b32 s66, 3\n" \
  "s_mov_b32 s67, 0\n" \
  "s_mov_b32 s72, 0\n" \
  "v_mov_b32 v7, 1\n" \
  "v_mov_b32 v6, s70\n" \
  "v_mov_b32 v5, 0\n" \
  "v_add_u32 v10, 4096, v2\n" \
  ".LCP_TOP_%=:\n" \
  "ds_read_b128 v[28:31], v1 offset:0\n" \
  "ds_read_b128 v[32:35], v1 offset:1024\n" \
  "s_add_u32 s66, s66, 3\n" \
  "s_add_u32 s67, s67, 1\n" \
  "s_mov_b32 s69, 0x400000\n" \
  "v_xor_b32 v0, 0x8000, v0\n" \
  "v_xor_b32 v3, 0x100, v3\n" \
  "v_readfirstlane_b32 s71, v5\n" \
  "s_cmp_ge_u32 s71, s66\n" \
  "s_cbranch_scc1 .LCP_GO_%=\n" \
  "s_cmp_eq_u32 s68, 1\n" \
  "s_cbranch_scc0 .LCP_X1_%=\n" \
  "s_waitcnt vmcnt(0)\n" \
  "s_mov_b64 exec, 1\n" \
  "buffer_store_dword v7, off, s[56:59], s72 sc1\n" \
  "s_mov_b64 exec, -1\n" \
  "s_add_u32 s72, s72, 0x1000\n" \
  "s_mov_b32 s68, 0\n" \
  ".LCP_X1_%=:\n" \
  ".LCP_S2_%=:\n" \
  "s_sleep 1\n" \
  "ds_read_b32 v5, v6 offset:0\n" \
  "s_sub_u32 s69, s69, 1\n" \
  "s_cbranch_scc1 .LCP_END_%=\n" \
  "s_waitcnt lgkmcnt(0)\n" \
  "v_readfirstlane_b32 s71, v5\n" \
  "s_cmp_ge_u32 s71, s66\n" \
  "s_cbranch_scc1 .LCP_GO_%=\n" \
  "s_branch .LCP_S2_%=\n" \
  ".LCP_GO_%=:\n" \
  "ds_read_b128 v[12:15], v0 offset:0\n" \
  "ds_read_b128 v[16:19], v0 offset:2048\n" \
  "ds_read_b128 v[20:23], v0 offset:4096\n" \
  "ds_read_b128 v[24:27], v0 offset:6144\n" \
  "ds_read_b128 v[36:39], v1 offset:2048\n" \
  "s_waitcnt lgkmcnt(4)\n" \
  "v_mfma_f32_16x16x32_bf16 v[48:51], v[64:67], v[12:15], 0\n" \
  "v_mfma_f32_16x16x32_bf16 v[52:55], v[128:131], v[12:15], 0\n" \
  "v_mfma_f32_16x16x32_bf16 v[56:59], v[192:195], v[12:15], 0\n" \
  "v_mfma_f32_16x16x32_bf16 v[60:63], v[28:31], v[12:15], 0\n" \
  "ds_read_b128 v[12:15], v0 offset:8192\n" \
  "ds_read_b128 v[28:31], v1 offset:3072\n" \
  "s_waitcnt lgkmcnt(5)\n" \
  "v_mfma_f32_16x16x32_bf16 v[48:51], v[68:71], v[16:19], v[48:51]\n" \
  "v_mfma_f32_16x16x32_bf16 v[52:55], v[132:135], v[16:19], v[52:55]\n" \
  "v_mfma_f32_16x16x32_bf16 v[56:59], v[196:199], v[16:19], v[56:59]\n" \
  "v_mfma_f32_16x16x32_bf16 v[60:63], v[32:35], v[16:19], v[60:63]\n" \
  "ds_read_b128 v[16:19], v0 offset:10240\n" \
  "ds_read_b128 v[32:35], v1 offset:4096\n" \
  "s_waitcnt lgkmcnt(6)\n" \
  "v_mfma_f32_16x16x32_bf16 v[48:51], v[72:75], v[20:23], v[48:51]\n" \
  "v_mfma_f32_16x16x32_bf16 v[52:55], v[136:139], v[20:23], v[52:55]\n" \
  "v_mfma_f32_16x16x32_bf16 v[56:59], v[200:203], v[20:23], v[56:59]\n" \
  "s_waitcnt lgkmcnt(4)\n" \
  "v_mfma_f32_16x16x32_bf16 v[60:63], v[36:39], v[20:23], v[60:63]\n" \
  "ds_read_b128 v[20:23], v0 offset:12288\n" \
  "ds_read_b128 v[36:39], v1 offset:5120\n" \
  "v_mfma_f32_16x16x32_bf16 v[48:51], v[76:79], v[24:27], v[48:51]\n" \
  "v_mfma_f32_16x16x32_bf16 v[52:55], v[140:143], v[24:27], v[52:55]\n" \
  "v_mfma_f32_16x16x32_bf16 v[56:59], v[204:207], v[24:27], v[56:59]\n" \
  "s_waitcnt lgkmcnt(4)\n" \
  "v_mfma_f32_16x16x32_bf16 v[60:63], v[28:31], v[24:27], v[60:63]\n" \
  "ds_read_b128 v[24:27], v0 offset:14336\n" \
  "ds_read_b128 v[28:31], v1 offset:6144\n" \
  "v_mfma_f32_16x16x32_bf16 v[48:51], v[80:83], v[12:15], v[48:51]\n" \
  "v_mfma_f32_16x16x32_bf16 v[52:55], v[144:147], v[12:15], v[52:55]\n" \
  "v_mfma_f32_16x16x32_bf16 v[56:59], v[208:211], v[12:15], v[56:59]\n" \
  "s_waitcnt lgkmcnt(4)\n" \
  "v_mfma_f32_16x16x32_bf16 v[60:63], v[32:35], v[12:15], v[60:63]\n" \
  "ds_read_b128 v[12:15], v0 offset:16384\n" \
  "ds_read_b128 v[32:35], v1 offset:7168\n" \
  "v_mfma_f32_16x16x32_bf16 v[48:51], v[84:87], v[16:19], v[48:51]\n" \
  "v_mfma_f32_16x16x32_bf16 v[52:55], v[148:151], v[16:19], v[52:55]\n" \
  "v_mfma_f32_16x16x32_bf16 v[56:59], v[212:215], v[16:19], v[56:59]\n" \
  "s_waitcnt lgkmcnt(4)\n" \
  "v_mfma_f32_16x16x32_bf16 v[60:63], v[36:39], v[16:19], v[60:63]\n" \
  "ds_read_b128 v[16:19], v0 offset:18432\n" \
  "ds_read_b128 v[36:39], v1 offset:8192\n" \
  "v_mfma_f32_16x16x32_bf16 v[48:51], v[88:91], v[20:23], v[48:51]\n" \
  "v_mfma_f32_16x16x32_bf16 v[52:55], v[152:155], v[20:23], v[52:55]\n" \
  "v_mfma_f32_16x16x32_bf16 v[56:59], v[216:219], v[20:23], v[56:59]\n" \
  "s_waitcnt lgkmcnt(4)\n" \
  "v_mfma_f32_16x16x32_bf16 v[60:63], v[28:31], v[20:23], v[60:63]\n" \
  "ds_read_b128 v[20:23], v0 offset:20480\n" \
  "ds_read_b128 v[28:31], v1 offset:9216\n" \
  "v_mfma_f32_16x16x32_bf16 v[48:51], v[92:95], v[24:27], v[48:51]\n" \
  "v_mfma_f32_16x16x32_bf16 v[52:55], v[156:159], v[24:27], v[52:55]\n" \
  "v_mfma_f32_16x16x32_bf16 v[56:59], v[220:223], v[24:27], v[56:59]\n" \
  "s_waitcnt lgkmcnt(4)\n" \
  "v_mfma_f32_16x16x32_bf16 v[60:63], v[32:35], v[24:27], v[60:63]\n" \
  "ds_read_b128 v[24:27], v0 offset:22528\n" \
  "ds_read_b128 v[32:35], v1 offset:10240\n" \
  "v_mfma_f32_16x16x32_bf16 v[48:51], v[96:99], v[12:15], v[48:51]\n" \
  "v_mfma_f32_16x16x32_bf16 v[52:55], v[160:163], v[12:15], v[52:55]\n" \
  "v_mfma_f32_16x16x32_bf16 v[56:59], v[224:227], v[12:15], v[56:59]\n" \
  "s_waitcnt lgkmcnt(4)\n" \
  "v_mfma_f32_16x16x32_bf16 v[60:63], v[36:39], v[12:15], v[60:63]\n" \
  "ds_read_b128 v[12:15], v0 offset:24576\n" \
  "ds_read_b128 v[36:39], v1 offset:11264\n" \
  "v_mfma_f32_16x16x32_bf16 v[48:51], v[100:103], v[16:19], v[48:51]\n" \
  "v_mfma_f32_16x16x32_bf16 v[52:55], v[164:167], v[16:19], v[52:55]\n" \
  "v_mfma_f32_16x16x32_bf16 v[56:59], v[228:231], v[16:19], v[56:59]\n" \
  "s_waitcnt lgkmcnt(4)\n" \
  "v_mfma_f32_16x16x32_bf16 v[60:63], v[28:31], v[16:19], v[60:63]\n" \
  "ds_read_b128 v[16:19], v0 offset:26624\n" \
  "ds_read_b128 v[28:31], v1 offset:12288\n" \
  "v_mfma_f32_16x16x32_bf16 v[48:51], v[104:107], v[20:23], v[48:51]\n" \
  "v_mfma_f32_16x16x32_bf16 v[52:55], v[168:171], v[20:23], v[52:55]\n" \
  "v_mfma_f32_16x16x32_bf16 v[56:59], v[232:235], v[20:23], v[56:59]\n" \
  "s_waitcnt lgkmcnt(4)\n" \
  "v_mfma_f32_16x16x32_bf16 v[60:63], v[32:35], v[20:23], v[60:63]\n" \
  "ds_read_b128 v[20:23], v0 offset:28672\n" \
  "ds_read_b128 v[32:35], v1 offset:13312\n" \
  "v_mfma_f32_16x16x32_bf16 v[48:51], v[108:111], v[24:27], v[48:51]\n" \
  "v_mfma_f32_16x16x32_bf16 v[52:55], v[172:175], v[24:27], v[52:55]\n" \
  "v_mfma_f32_16x16x32_bf16 v[56:59], v[236:239], v[24:27], v[56:59]\n" \
  "s_waitcnt lgkmcnt(4)\n" \
  "v_mfma_f32_16x16x32_bf16 v[60:63], v[36:39], v[24:27], v[60:63]\n" \
  "s_cmp_eq_u32 s68, 1\n" \
  "s_cbranch_scc0 .LCP_X3_%=\n" \
  "s_waitcnt vmcnt(0)\n" \
  "s_mov_b64 exec, 1\n" \
  "buffer_store_dword v7, off, s[56:59], s72 sc1\n" \
  "s_mov_b64 exec, -1\n" \
  "s_add_u32 s72, s72, 0x1000\n" \
  "s_mov_b32 s68, 0\n" \
  ".LCP_X3_%=:\n" \
  "ds_read_b32 v5, v6 offset:16\n" \
  "ds_read_b128 v[24:27], v0 offset:30720\n" \
  "ds_read_b128 v[36:39], v1 offset:14336\n" \
  "v_mfma_f32_16x16x32_bf16 v[48:51], v[112:115], v[12:15], v[48:51]\n" \
  "v_mfma_f32_16x16x32_bf16 v[52:55], v[176:179], v[12:15], v[52:55]\n" \
  "v_mfma_f32_16x16x32_bf16 v[56:59], v[240:243], v[12:15], v[56:59]\n" \
  "s_waitcnt lgkmcnt(5)\n" \
  "v_mfma_f32_16x16x32_bf16 v[60:63], v[28:31], v[12:15], v[60:63]\n" \
  "ds_read_b128 v[12:15], v0 offset:256\n" \
  "ds_read_b128 v[28:31], v1 offset:15360\n" \
  "ds_read_b128 v[40:43], v4 offset:0\n" \
  "ds_read_b128 v[44:47], v4 offset:64\n" \
  "v_mfma_f32_16x16x32_bf16 v[48:51], v[116:119], v[16:19], v[48:51]\n" \
  "v_mfma_f32_16x16x32_bf16 v[52:55], v[180:183], v[16:19], v[52:55]\n" \
  "v_mfma_f32_16x16x32_bf16 v[56:59], v[244:247], v[16:19], v[56:59]\n" \
  "s_waitcnt lgkmcnt(7)\n" \
  "v_mfma_f32_16x16x32_bf16 v[60:63], v[32:35], v[16:19], v[60:63]\n" \
  "s_waitcnt lgkmcnt(6)\n" \
  "v_readfirstlane_b32 s71, v5\n" \
  "s_cmp_ge_u32 s71, s67\n" \
  "s_cbranch_scc1 .LCP_ST_%=\n" \
  ".LCP_S4_%=:\n" \
  "s_sleep 1\n" \
  "ds_read_b32 v5, v6 offset:16\n" \
  "s_sub_u32 s69, s69, 1\n" \
  "s_cbranch_scc1 .LCP_END_%=\n" \
  "s_waitcnt lgkmcnt(0)\n" \
  "v_readfirstlane_b32 s71, v5\n" \
  "s_cmp_ge_u32 s71, s67\n" \
  "s_cbranch_scc1 .LCP_ST_%=\n" \
  "s_branch .LCP_S4_%=\n" \
  ".LCP_ST_%=:\n" \
  "ds_read_b128 v[16:19], v0 offset:2304\n" \
  "ds_read_b128 v[32:35], v4 offset:128\n" \
  "ds_read2_b32 v[8:9], v3 offset0:0 offset1:32\n" \
  "v_mfma_f32_16x16x32_bf16 v[48:51], v[120:123], v[20:23], v[48:51]\n" \
  "v_mfma_f32_16x16x32_bf16 v[52:55], v[184:187], v[20:23], v[52:55]\n" \
  "v_mfma_f32_16x16x32_bf16 v[56:59], v[248:251], v[20:23], v[56:59]\n" \
  "s_waitcnt lgkmcnt(7)\n" \
  "v_mfma_f32_16x16x32_bf16 v[60:63], v[36:39], v[20:23], v[60:63]\n" \
  "ds_read_b128 v[20:23], v0 offset:4352\n" \
  "ds_read_b128 v[36:39], v4 offset:192\n" \
  "v_mfma_f32_16x16x32_bf16 v[48:51], v[124:127], v[24:27], v[48:51]\n" \
  "v_mfma_f32_16x16x32_bf16 v[52:55], v[188:191], v[24:27], v[52:55]\n" \
  "v_mfma_f32_16x16x32_bf16 v[56:59], v[252:255], v[24:27], v[56:59]\n" \
  "s_waitcnt lgkmcnt(7)\n" \
  "v_mfma_f32_16x16x32_bf16 v[60:63], v[28:31], v[24:27], v[60:63]\n" \
  "s_waitcnt lgkmcnt(0)\n" \
  "s_nop 4\n" \
  "v_fma_f32 v48, -v8, v40, v48\n" \
  "v_fma_f32 v49, -v8, v41, v49\n" \
  "v_fma_f32 v50, -v8, v42, v50\n" \
  "v_fma_f32 v51, -v8, v43, v51\n" \
  "v_fma_f32 v52, -v8, v44, v52\n" \
  "v_fma_f32 v53, -v8, v45, v53\n" \
  "v_fma_f32 v54, -v8, v46, v54\n" \
  "v_fma_f32 v55, -v8, v47, v55\n" \
  "v_fma_f32 v56, -v8, v32, v56\n" \
  "v_fma_f32 v57, -v8, v33, v57\n" \
  "v_fma_f32 v58, -v8, v34, v58\n" \
  "v_fma_f32 v59, -v8, v35, v59\n" \
  "v_fma_f32 v60, -v8, v36, v60\n" \
  "v_fma_f32 v61, -v8, v37, v61\n" \
  "v_fma_f32 v62, -v8, v38, v62\n" \
  "v_fma_f32 v63, -v8, v39, v63\n" \
  "ds_read_b128 v[32:35], v1 offset:0\n" \
  "ds_read_b128 v[36:39], v1 offset:1024\n" \
  "v_mul_f32 v48, v48, v9\n" \
  "v_mul_f32 v49, v49, v9\n" \
  "v_mul_f32 v50, v50, v9\n" \
  "v_mul_f32 v51, v51, v9\n" \
  "v_mul_f32 v52, v52, v9\n" \
  "v_mul_f32 v53, v53, v9\n" \
  "v_mul_f32 v54, v54, v9\n" \
  "v_mul_f32 v55, v55, v9\n" \
  "v_cvt_pk_bf16_f32 v40, v48, v49\n" \
  "v_cvt_pk_bf16_f32 v41, v50, v51\n" \
  "v_cvt_pk_bf16_f32 v42, v52, v53\n" \
  "v_cvt_pk_bf16_f32 v43, v54, v55\n" \
  "buffer_store_dwordx4 v[40:43], v2, s[40:43], s65 offen offset:0 sc1\n" \
  "v_mul_f32 v56, v56, v9\n" \
  "v_mul_f32 v57, v57, v9\n" \
  "v_mul_f32 v58, v58, v9\n" \
  "v_mul_f32 v59, v59, v9\n" \
  "v_mul_f32 v60, v60, v9\n" \
  "v_mul_f32 v61, v61, v9\n" \
  "v_mul_f32 v62, v62, v9\n" \
  "v_mul_f32 v63, v63, v9\n" \
  "v_cvt_pk_bf16_f32 v44, v56, v57\n" \
  "v_cvt_pk_bf16_f32 v45, v58, v59\n" \
  "v_cvt_pk_bf16_f32 v46, v60, v61\n" \
  "v_cvt_pk_bf16_f32 v47, v62, v63\n" \
  "buffer_store_dwordx4 v[44:47], v2, s[40:43], s65 offen offset:1024 sc1\n" \
  "ds_read_b128 v[24:27], v0 offset:6400\n" \
  "ds_read_b128 v[28:31], v1 offset:2048\n" \
  "v_mfma_f32_16x16x32_bf16 v[48:51], v[64:67], v[12:15], 0\n" \
  "v_mfma_f32_16x16x32_bf16 v[52:55], v[128:131], v[12:15], 0\n" \
  "v_mfma_f32_16x16x32_bf16 v[56:59], v[192:195], v[12:15], 0\n" \
  "s_waitcnt lgkmcnt(3)\n" \
  "v_mfma_f32_16x16x32_bf16 v[60:63], v[32:35], v[12:15], 0\n" \
  "ds_read_b128 v[12:15], v0 offset:8448\n" \
  "ds_read_b128 v[32:35], v1 offset:3072\n" \
  "v_mfma_f32_16x16x32_bf16 v[48:51], v[68:71], v[16:19], v[48:51]\n" \
  "v_mfma_f32_16x16x32_bf16 v[52:55], v[132:135], v[16:19], v[52:55]\n" \
  "v_mfma_f32_16x16x32_bf16 v[56:59], v[196:199], v[16:19], v[56:59]\n" \
  "s_waitcnt lgkmcnt(4)\n" \
  "v_mfma_f32_16x16x32_bf16 v[60:63], v[36:39], v[16:19], v[60:63]\n" \
  "ds_read_b128 v[16:19], v0 offset:10496\n" \
  "ds_read_b128 v[36:39], v1 offset:4096\n" \
  "v_mfma_f32_16x16x32_bf16 v[48:51], v[72:75], v[20:23], v[48:51]\n" \
  "v_mfma_f32_16x16x32_bf16 v[52:55], v[136:139], v[20:23], v[52:55]\n" \
  "v_mfma_f32_16x16x32_bf16 v[56:59], v[200:203], v[20:23], v[56:59]\n" \
  "s_waitcnt lgkmcnt(4)\n" \
  "v_mfma_f32_16x16x32_bf16 v[60:63], v[28:31], v[20:23], v[60:63]\n" \
  "ds_read_b128 v[20:23], v0 offset:12544\n" \
  "ds_read_b128 v[28:31], v1 offset:5120\n" \
  "v_mfma_f32_16x16x32_bf16 v[48:51], v[76:79], v[24:27], v[48:51]\n" \
  "v_mfma_f32_16x16x32_bf16 v[52:55], v[140:143], v[24:27], v[52:55]\n" \
  "v_mfma_f32_16x16x32_bf16 v[56:59], v[204:207], v[24:27], v[56:59]\n" \
  "s_waitcnt lgkmcnt(4)\n" \
  "v_mfma_f32_16x16x32_bf16 v[60:63], v[32:35], v[24:27], v[60:63]\n" \
  "ds_read_b128 v[24:27], v0 offset:14592\n" \
  "ds_read_b128 v[32:35], v1 offset:6144\n" \
  "v_mfma_f32_16x16x32_bf16 v[48:51], v[80:83], v[12:15], v[48:51]\n" \
  "v_mfma_f32_16x16x32_bf16 v[52:55], v[144:147], v[12:15], v[52:55]\n" \
  "v_mfma_f32_16x16x32_bf16 v[56:59], v[208:211], v[12:15], v[56:59]\n" \
  "s_waitcnt lgkmcnt(4)\n" \
  "v_mfma_f32_16x16x32_bf16 v[60:63], v[36:39], v[12:15], v[60:63]\n" \
  "ds_read_b128 v[12:15], v0 offset:16640\n" \
  "ds_read_b128 v[36:39], v1 offset:7168\n" \
  "v_mfma_f32_16x16x32_bf16 v[48:51], v[84:87], v[16:19], v[48:51]\n" \
  "v_mfma_f32_16x16x32_bf16 v[52:55], v[148:151], v[16:19], v[52:55]\n" \
  "v_mfma_f32_16x16x32_bf16 v[56:59], v[212:215], v[16:19], v[56:59]\n" \
  "s_waitcnt lgkmcnt(4)\n" \
  "v_mfma_f32_16x16x32_bf16 v[60:63], v[28:31], v[16:19], v[60:63]\n" \
  "ds_read_b128 v[16:19], v0 offset:18688\n" \
  "ds_read_b128 v[28:31], v1 offset:8192\n" \
  "v_mfma_f32_16x16x32_bf16 v[48:51], v[88:91], v[20:23], v[48:51]\n" \
  "v_mfma_f32_16x16x32_bf16 v[52:55], v[152:155], v[20:23], v[52:55]\n" \
  "v_mfma_f32_16x16x32_bf16 v[56:59], v[216:219], v[20:23], v[56:59]\n" \
  "s_waitcnt lgkmcnt(4)\n" \
  "v_mfma_f32_16x16x32_bf16 v[60:63], v[32:35], v[20:23], v[60:63]\n" \
  "ds_read_b128 v[20:23], v0 offset:20736\n" \
  "ds_read_b128 v[32:35], v1 offset:9216\n" \
  "v_mfma_f32_16x16x32_bf16 v[48:51], v[92:95], v[24:27], v[48:51]\n" \
  "v_mfma_f32_16x16x32_bf16 v[52:55], v[156:159], v[24:27], v[52:55]\n" \
  "v_mfma_f32_16x16x32_bf16 v[56:59], v[220:223], v[24:27], v[56:59]\n" \
  "s_waitcnt lgkmcnt(4)\n" \
  "v_mfma_f32_16x16x32_bf16 v[60:63], v[36:39], v[24:27], v[60:63]\n" \
  "ds_read_b128 v[24:27], v0 offset:22784\n" \
  "ds_read_b128 v[36:39], v1 offset:10240\n" \
  "v_mfma_f32_16x16x32_bf16 v[48:51], v[96:99], v[12:15], v[48:51]\n" \
  "v_mfma_f32_16x16x32_bf16 v[52:55], v[160:163], v[12:15], v[52:55]\n" \
  "v_mfma_f32_16x16x32_bf16 v[56:59], v[224:227], v[12:15], v[56:59]\n" \
  "s_waitcnt lgkmcnt(4)\n" \
  "v_mfma_f32_16x16x32_bf16 v[60:63], v[28:31], v[12:15], v[60:63]\n" \
  "ds_read_b128 v[12:15], v0 offset:24832\n" \
  "ds_read_b128 v[28:31], v1 offset:11264\n" \
  "v_mfma_f32_16x16x32_bf16 v[48:51], v[100:103], v[16:19], v[48:51]\n" \
  "v_mfma_f32_16x16x32_bf16 v[52:55], v[164:167], v[16:19], v[52:55]\n" \
  "v_mfma_f32_16x16x32_bf16 v[56:59], v[228:231], v[16:19], v[56:59]\n" \
  "s_waitcnt lgkmcnt(4)\n" \
  "v_mfma_f32_16x16x32_bf16 v[60:63], v[32:35], v[16:19], v[60:63]\n" \
  "ds_read_b128 v[16:19], v0 offset:26880\n" \
  "ds_read_b128 v[32:35], v1 offset:12288\n" \
  "v_mfma_f32_16x16x32_bf16 v[48:51], v[104:107], v[20:23], v[48:51]\n" \
  "v_mfma_f32_16x16x32_bf16 v[52:55], v[168:171], v[20:23], v[52:55]\n" \
  "v_mfma_f32_16x16x32_bf16 v[56:59], v[232:235], v[20:23], v[56:59]\n" \
  "s_waitcnt lgkmcnt(4)\n" \
  "v_mfma_f32_16x16x32_bf16 v[60:63], v[36:39], v[20:23], v[60:63]\n" \
  "ds_read_b128 v[20:23], v0 offset:28928\n" \
  "ds_read_b128 v[36:39], v1 offset:13312\n" \
  "v_mfma_f32_16x16x32_bf16 v[48:51], v[108:111], v[24:27], v[48:51]\n" \
  "v_mfma_f32_16x16x32_bf16 v[52:55], v[172:175], v[24:27], v[52:55]\n" \
  "v_mfma_f32_16x16x32_bf16 v[56:59], v[236:239], v[24:27], v[56:59]\n" \
  "s_waitcnt lgkmcnt(4)\n" \
  "v_mfma_f32_16x16x32_bf16 v[60:63], v[28:31], v[24:27], v[60:63]\n" \
  "ds_read_b128 v[24:27], v0 offset:30976\n" \
  "ds_read_b128 v[28:31], v1 offset:14336\n" \
  "v_mfma_f32_16x16x32_bf16 v[48:51], v[112:115], v[12:15], v[48:51]\n" \
  "v_mfma_f32_16x16x32_bf16 v[52:55], v[176:179], v[12:15], v[52:55]\n" \
  "v_mfma_f32_16x16x32_bf16 v[56:59], v[240:243], v[12:15], v[56:59]\n" \
  "s_waitcnt lgkmcnt(4)\n" \
  "v_mfma_f32_16x16x32_bf16 v[60:63], v[32:35], v[12:15], v[60:63]\n" \
  "ds_read_b128 v[32:35], v1 offset:15360\n" \
  "ds_read_b128 v[40:43], v4 offset:0\n" \
  "ds_read_b128 v[44:47], v4 offset:64\n" \
  "v_mfma_f32_16x16x32_bf16 v[48:51], v[116:119], v[16:19], v[48:51]\n" \
  "v_mfma_f32_16x16x32_bf16 v[52:55], v[180:183], v[16:19], v[52:55]\n" \
  "v_mfma_f32_16x16x32_bf16 v[56:59], v[244:247], v[16:19], v[56:59]\n" \
  "s_waitcnt lgkmcnt(5)\n" \
  "v_mfma_f32_16x16x32_bf16 v[60:63], v[36:39], v[16:19], v[60:63]\n" \
  "ds_read_b128 v[36:39], v4 offset:128\n" \
  "ds_read2_b32 v[8:9], v3 offset0:16 offset1:48\n" \
  "v_mfma_f32_16x16x32_bf16 v[48:51], v[120:123], v[20:23], v[48:51]\n" \
  "v_mfma_f32_16x16x32_bf16 v[52:55], v[184:187], v[20:23], v[52:55]\n" \
  "v_mfma_f32_16x16x32_bf16 v[56:59], v[248:251], v[20:23], v[56:59]\n" \
  "s_waitcnt lgkmcnt(5)\n" \
  "v_mfma_f32_16x16x32_bf16 v[60:63], v[28:31], v[20:23], v[60:63]\n" \
  "ds_read_b128 v[28:31], v4 offset:192\n" \
  "s_mov_b64 exec, 1\n" \
  "ds_add_u32 v6, v7 offset:8\n" \
  "s_mov_b64 exec, -1\n" \
  "v_xor_b32 v6, 4, v6\n" \
  "v_mfma_f32_16x16x32_bf16 v[48:51], v[124:127], v[24:27], v[48:51]\n" \
  "v_mfma_f32_16x16x32_bf16 v[52:55], v[188:191], v[24:27], v[52:55]\n" \
  "v_mfma_f32_16x16x32_bf16 v[56:59], v[252:255], v[24:27], v[56:59]\n" \
  "s_waitcnt lgkmcnt(6)\n" \
  "v_mfma_f32_16x16x32_bf16 v[60:63], v[32:35], v[24:27], v[60:63]\n" \
  "s_waitcnt lgkmcnt(1)\n" \
  "s_nop 4\n" \
  "ds_read_b32 v5, v6 offset:0\n" \
  "v_fma_f32 v48, -v8, v40, v48\n" \
  "v_fma_f32 v49, -v8, v41, v49\n" \
  "v_fma_f32 v50, -v8, v42, v50\n" \
  "v_fma_f32 v51, -v8, v43, v51\n" \
  "v_fma_f32 v52, -v8, v44, v52\n" \
  "v_fma_f32 v53, -v8, v45, v53\n" \
  "v_fma_f32 v54, -v8, v46, v54\n" \
  "v_fma_f32 v55, -v8, v47, v55\n" \
  "v_fma_f32 v56, -v8, v36, v56\n" \
  "v_fma_f32 v57, -v8, v37, v57\n" \
  "v_fma_f32 v58, -v8, v38, v58\n" \
  "v_fma_f32 v59, -v8, v39, v59\n" \
  "v_fma_f32 v60, -v8, v28, v60\n" \
  "v_fma_f32 v61, -v8, v29, v61\n" \
  "v_fma_f32 v62, -v8, v30, v62\n" \
  "v_fma_f32 v63, -v8, v31, v63\n" \
  "v_mul_f32 v48, v48, v9\n" \
  "v_mul_f32 v49, v49, v9\n" \
  "v_mul_f32 v50, v50, v9\n" \
  "v_mul_f32 v51, v51, v9\n" \
  "v_mul_f32 v52, v52, v9\n" \
  "v_mul_f32 v53, v53, v9\n" \
  "v_mul_f32 v54, v54, v9\n" \
  "v_mul_f32 v55, v55, v9\n" \
  "v_cvt_pk_bf16_f32 v40, v48, v49\n" \
  "v_cvt_pk_bf16_f32 v41, v50, v51\n" \
  "v_cvt_pk_bf16_f32 v42, v52, v53\n" \
  "v_cvt_pk_bf16_f32 v43, v54, v55\n" \
  "buffer_store_dwordx4 v[40:43], v10, s[40:43], s65 offen offset:0 sc1\n" \
  "v_mul_f32 v56, v56, v9\n" \
  "v_mul_f32 v57, v57, v9\n" \
  "v_mul_f32 v58, v58, v9\n" \
  "v_mul_f32 v59, v59, v9\n" \
  "v_mul_f32 v60, v60, v9\n" \
  "v_mul_f32 v61, v61, v9\n" \
  "v_mul_f32 v62, v62, v9\n" \
  "v_mul_f32 v63, v63, v9\n" \
  "v_cvt_pk_bf16_f32 v44, v56, v57\n" \
  "v_cvt_pk_bf16_f32 v45, v58, v59\n" \
  "v_cvt_pk_bf16_f32 v46, v60, v61\n" \
  "v_cvt_pk_bf16_f32 v47, v62, v63\n" \
  "buffer_store_dwordx4 v[44:47], v10, s[40:43], s65 offen offset:1024 sc1\n" \
  "s_waitcnt lgkmcnt(0)\n" \
  "s_add_u32 s65, s65, 0x80000\n" \
  "s_sub_u32 s64, s64, 1\n" \
  "s_mov_b32 s68, 1\n" \
  "s_cmp_lg_u32 s64, 0\n" \
  "s_cbranch_scc1 .LCP_TOP_%=\n" \
  "s_waitcnt vmcnt(0)\n" \
  "s_mov_b64 exec, 1\n" \
  "buffer_store_dword v7, off, s[56:59], s72 sc1\n" \
  "s_mov_b64 exec, -1\n" \
  "s_add_u32 s72, s72, 0x1000\n" \
  "s_mov_b32 s68, 0\n" \
  ".LCP_END_%=:\n" \
  "s_endpgm\n"
#define V_ASM \
  "s_mov_b32 s68, 0\n" \
  "s_mov_b32 s66, 3\n" \
  "s_mov_b32 s67, 0\n" \
  "s_mov_b32 s72, 0\n" \
  "v_mov_b32 v7, 1\n" \
  "v_mov_b32 v6, s70\n" \
  "v_mov_b32 v5, 0\n" \
  "ds_read_b32 v8, v4 offset:0\n" \
  "ds_read_b32 v9, v4 offset:64\n" \
  "ds_read_b32 v10, v4 offset:128\n" \
  "ds_read_b32 v4, v4 offset:192\n" \
  "s_waitcnt lgkmcnt(0)\n" \
  ".LCW_TOP_%=:\n" \
  "ds_read_b128 v[28:31], v1 offset:0\n" \
  "ds_read_b128 v[32:35], v1 offset:1024\n" \
  "s_add_u32 s66, s66, 3\n" \
  "s_add_u32 s67, s67, 1\n" \
  "s_mov_b32 s69, 0x400000\n" \
  "v_xor_b32 v0, 0x8000, v0\n" \
  "v_xor_b32 v3, 0x100, v3\n" \
  "v_readfirstlane_b32 s71, v5\n" \
  "s_cmp_ge_u32 s71, s66\n" \
  "s_cbranch_scc1 .LCW_GO_%=\n" \
  "s_cmp_eq_u32 s68, 1\n" \
  "s_cbranch_scc0 .LCW_X1_%=\n" \
  "s_waitcnt vmcnt(0)\n" \
  "s_mov_b64 exec, 1\n" \
  "buffer_store_dword v7, off, s[56:59], s72 sc1\n" \
  "s_mov_b64 exec, -1\n" \
  "s_add_u32 s72, s72, 0x1000\n" \
  "s_mov_b32 s68, 0\n" \
  ".LCW_X1_%=:\n" \
  ".LCW_S2_%=:\n" \
  "s_sleep 1\n" \
  "ds_read_b32 v5, v6 offset:0\n" \
  "s_sub_u32 s69, s69, 1\n" \
  "s_cbranch_scc1 .LCW_END_%=\n" \
  "s_waitcnt lgkmcnt(0)\n" \
  "v_readfirstlane_b32 s71, v5\n" \
  "s_cmp_ge_u32 s71, s66\n" \
  "s_cbranch_scc1 .LCW_GO_%=\n" \
  "s_branch .LCW_S2_%=\n" \
  ".LCW_GO_%=:\n" \
  "ds_read_b128 v[12:15], v0 offset:0\n" \
  "ds_read_b128 v[16:19], v0 offset:2048\n" \
  "ds_read_b128 v[20:23], v0 offset:4096\n" \
  "ds_read_b128 v[24:27], v0 offset:6144\n" \
  "ds_read_b128 v[36:39], v1 offset:2048\n" \
  "s_waitcnt lgkmcnt(4)\n" \
  "v_mfma_f32_16x16x32_bf16 v[42:45], v[12:15], v[64:67], 0\n" \
  "v_mfma_f32_16x16x32_bf16 v[48:51], v[12:15], v[128:131], 0\n" \
  "v_mfma_f32_16x16x32_bf16 v[54:57], v[12:15], v[192:195], 0\n" \
  "v_mfma_f32_16x16x32_bf16 v[60:63], v[12:15], v[28:31], 0\n" \
  "ds_read_b128 v[12:15], v0 offset:8192\n" \
  "ds_read_b128 v[28:31], v1 offset:3072\n" \
  "s_waitcnt lgkmcnt(5)\n" \
  "v_mfma_f32_16x16x32_bf16 v[42:45], v[16:19], v[68:71], v[42:45]\n" \
  "v_mfma_f32_16x16x32_bf16 v[48:51], v[16:19], v[132:135], v[48:51]\n" \
  "v_mfma_f32_16x16x32_bf16 v[54:57], v[16:19], v[196:199], v[54:57]\n" \
  "v_mfma_f32_16x16x32_bf16 v[60:63], v[16:19], v[32:35], v[60:63]\n" \
  "ds_read_b128 v[16:19], v0 offset:10240\n" \
  "ds_read_b128 v[32:35], v1 offset:4096\n" \
  "s_waitcnt lgkmcnt(6)\n" \
  "v_mfma_f32_16x16x32_bf16 v[42:45], v[20:23], v[72:75], v[42:45]\n" \
  "v_mfma_f32_16x16x32_bf16 v[48:51], v[20:23], v[136:139], v[48:51]\n" \
  "v_mfma_f32_16x16x32_bf16 v[54:57], v[20:23], v[200:203], v[54:57]\n" \
  "s_waitcnt lgkmcnt(4)\n" \
  "v_mfma_f32_16x16x32_bf16 v[60:63], v[20:23], v[36:39], v[60:63]\n" \
  "ds_read_b128 v[20:23], v0 offset:12288\n" \
  "ds_read_b128 v[36:39], v1 offset:5120\n" \
  "v_mfma_f32_16x16x32_bf16 v[42:45], v[24:27], v[76:79], v[42:45]\n" \
  "v_mfma_f32_16x16x32_bf16 v[48:51], v[24:27], v[140:143], v[48:51]\n" \
  "v_mfma_f32_16x16x32_bf16 v[54:57], v[24:27], v[204:207], v[54:57]\n" \
  "s_waitcnt lgkmcnt(4)\n" \
  "v_mfma_f32_16x16x32_bf16 v[60:63], v[24:27], v[28:31], v[60:63]\n" \
  "ds_read_b128 v[24:27], v0 offset:14336\n" \
  "ds_read_b128 v[28:31], v1 offset:6144\n" \
  "v_mfma_f32_16x16x32_bf16 v[42:45], v[12:15], v[80:83], v[42:45]\n" \
  "v_mfma_f32_16x16x32_bf16 v[48:51], v[12:15], v[144:147], v[48:51]\n" \
  "v_mfma_f32_16x16x32_bf16 v[54:57], v[12:15], v[208:211], v[54:57]\n" \
  "s_waitcnt lgkmcnt(4)\n" \
  "v_mfma_f32_16x16x32_bf16 v[60:63], v[12:15], v[32:35], v[60:63]\n" \
  "ds_read_b128 v[12:15], v0 offset:16384\n" \
  "ds_read_b128 v[32:35], v1 offset:7168\n" \
  "v_mfma_f32_16x16x32_bf16 v[42:45], v[16:19], v[84:87], v[42:45]\n" \
  "v_mfma_f32_16x16x32_bf16 v[48:51], v[16:19], v[148:151], v[48:51]\n" \
  "v_mfma_f32_16x16x32_bf16 v[54:57], v[16:19], v[212:215], v[54:57]\n" \
  "s_waitcnt lgkmcnt(4)\n" \
  "v_mfma_f32_16x16x32_bf16 v[60:63], v[16:19], v[36:39], v[60:63]\n" \
  "ds_read_b128 v[16:19], v0 offset:18432\n" \
  "ds_read_b128 v[36:39], v1 offset:8192\n" \
  "v_mfma_f32_16x16x32_bf16 v[42:45], v[20:23], v[88:91], v[42:45]\n" \
  "v_mfma_f32_16x16x32_bf16 v[48:51], v[20:23], v[152:155], v[48:51]\n" \
  "v_mfma_f32_16x16x32_bf16 v[54:57], v[20:23], v[216:219], v[54:57]\n" \
  "s_waitcnt lgkmcnt(4)\n" \
  "v_mfma_f32_16x16x32_bf16 v[60:63], v[20:23], v[28:31], v[60:63]\n" \
  "ds_read_b128 v[20:23], v0 offset:20480\n" \
  "ds_read_b128 v[28:31], v1 offset:9216\n" \
  "v_mfma_f32_16x16x32_bf16 v[42:45], v[24:27], v[92:95], v[42:45]\n" \
  "v_mfma_f32_16x16x32_bf16 v[48:51], v[24:27], v[156:159], v[48:51]\n" \
  "v_mfma_f32_16x16x32_bf16 v[54:57], v[24:27], v[220:223], v[54:57]\n" \
  "s_waitcnt lgkmcnt(4)\n" \
  "v_mfma_f32_16x16x32_bf16 v[60:63], v[24:27], v[32:35], v[60:63]\n" \
  "ds_read_b128 v[24:27], v0 offset:22528\n" \
  "ds_read_b128 v[32:35], v1 offset:10240\n" \
  "v_mfma_f32_16x16x32_bf16 v[42:45], v[12:15], v[96:99], v[42:45]\n" \
  "v_mfma_f32_16x16x32_bf16 v[48:51], v[12:15], v[160:163], v[48:51]\n" \
  "v_mfma_f32_16x16x32_bf16 v[54:57], v[12:15], v[224:227], v[54:57]\n" \
  "s_waitcnt lgkmcnt(4)\n" \
  "v_mfma_f32_16x16x32_bf16 v[60:63], v[12:15], v[36:39], v[60:63]\n" \
  "ds_read_b128 v[12:15], v0 offset:24576\n" \
  "ds_read_b128 v[36:39], v1 offset:11264\n" \
  "v_mfma_f32_16x16x32_bf16 v[42:45], v[16:19], v[100:103], v[42:45]\n" \
  "v_mfma_f32_16x16x32_bf16 v[48:51], v[16:19], v[164:167], v[48:51]\n" \
  "v_mfma_f32_16x16x32_bf16 v[54:57], v[16:19], v[228:231], v[54:57]\n" \
  "s_waitcnt lgkmcnt(4)\n" \
  "v_mfma_f32_16x16x32_bf16 v[60:63], v[16:19], v[28:31], v[60:63]\n" \
  "ds_read_b128 v[16:19], v0 offset:26624\n" \
  "ds_read_b128 v[28:31], v1 offset:12288\n" \
  "v_mfma_f32_16x16x32_bf16 v[42:45], v[20:23], v[104:107], v[42:45]\n" \
  "v_mfma_f32_16x16x32_bf16 v[48:51], v[20:23], v[168:171], v[48:51]\n" \
  "v_mfma_f32_16x16x32_bf16 v[54:57], v[20:23], v[232:235], v[54:57]\n" \
  "s_waitcnt lgkmcnt(4)\n" \
  "v_mfma_f32_16x16x32_bf16 v[60:63], v[20:23], v[32:35], v[60:63]\n" \
  "ds_read_b128 v[20:23], v0 offset:28672\n" \
  "ds_read_b128 v[32:35], v1 offset:13312\n" \
  "v_mfma_f32_16x16x32_bf16 v[42:45], v[24:27], v[108:111], v[42:45]\n" \
  "v_mfma_f32_16x16x32_bf16 v[48:51], v[24:27], v[172:175], v[48:51]\n" \
  "v_mfma_f32_16x16x32_bf16 v[54:57], v[24:27], v[236:239], v[54:57]\n" \
  "s_waitcnt lgkmcnt(4)\n" \
  "v_mfma_f32_16x16x32_bf16 v[60:63], v[24:27], v[36:39], v[60:63]\n" \
  "s_cmp_eq_u32 s68, 1\n" \
  "s_cbranch_scc0 .LCW_X3_%=\n" \
  "s_waitcnt vmcnt(0)\n" \
  "s_mov_b64 exec, 1\n" \
  "buffer_store_dword v7, off, s[56:59], s72 sc1\n" \
  "s_mov_b64 exec, -1\n" \
  "s_add_u32 s72, s72, 0x1000\n" \
  "s_mov_b32 s68, 0\n" \
  ".LCW_X3_%=:\n" \
  "ds_read_b32 v5, v6 offset:16\n" \
  "ds_read_b128 v[24:27], v0 offset:30720\n" \
  "ds_read_b128 v[36:39], v1 offset:14336\n" \
  "v_mfma_f32_16x16x32_bf16 v[42:45], v[12:15], v[112:115], v[42:45]\n" \
  "v_mfma_f32_16x16x32_bf16 v[48:51], v[12:15], v[176:179], v[48:51]\n" \
  "v_mfma_f32_16x16x32_bf16 v[54:57], v[12:15], v[240:243], v[54:57]\n" \
  "s_waitcnt lgkmcnt(5)\n" \
  "v_mfma_f32_16x16x32_bf16 v[60:63], v[12:15], v[28:31], v[60:63]\n" \
  "ds_read_b128 v[12:15], v0 offset:256\n" \
  "ds_read_b128 v[28:31], v1 offset:15360\n" \
  "v_mfma_f32_16x16x32_bf16 v[42:45], v[16:19], v[116:119], v[42:45]\n" \
  "v_mfma_f32_16x16x32_bf16 v[48:51], v[16:19], v[180:183], v[48:51]\n" \
  "v_mfma_f32_16x16x32_bf16 v[54:57], v[16:19], v[244:247], v[54:57]\n" \
  "s_waitcnt lgkmcnt(5)\n" \
  "v_mfma_f32_16x16x32_bf16 v[60:63], v[16:19], v[32:35], v[60:63]\n" \
  "s_waitcnt lgkmcnt(4)\n" \
  "v_readfirstlane_b32 s71, v5\n" \
  "s_cmp_ge_u32 s71, s67\n" \
  "s_cbranch_scc1 .LCW_ST_%=\n" \
  ".LCW_S4_%=:\n" \
  "s_sleep 1\n" \
  "ds_read_b32 v5, v6 offset:16\n" \
  "s_sub_u32 s69, s69, 1\n" \
  "s_cbranch_scc1 .LCW_END_%=\n" \
  "s_waitcnt lgkmcnt(0)\n" \
  "v_readfirstlane_b32 s71, v5\n" \
  "s_cmp_ge_u32 s71, s67\n" \
  "s_cbranch_scc1 .LCW_ST_%=\n" \
  "s_branch .LCW_S4_%=\n" \
  ".LCW_ST_%=:\n" \
  "ds_read_b128 v[16:19], v0 offset:2304\n" \
  "ds_read_b128 v[32:35], v3 offset:0\n" \
  "v_mfma_f32_16x16x32_bf16 v[42:45], v[20:23], v[120:123], v[42:45]\n" \
  "v_mfma_f32_16x16x32_bf16 v[48:51], v[20:23], v[184:187], v[48:51]\n" \
  "v_mfma_f32_16x16x32_bf16 v[54:57], v[20:23], v[248:251], v[54:57]\n" \
  "s_waitcnt lgkmcnt(4)\n" \
  "v_mfma_f32_16x16x32_bf16 v[60:63], v[20:23], v[36:39], v[60:63]\n" \
  "ds_read_b128 v[20:23], v0 offset:4352\n" \
  "ds_read_b128 v[36:39], v3 offset:128\n" \
  "v_mfma_f32_16x16x32_bf16 v[42:45], v[24:27], v[124:127], v[42:45]\n" \
  "v_mfma_f32_16x16x32_bf16 v[48:51], v[24:27], v[188:191], v[48:51]\n" \
  "v_mfma_f32_16x16x32_bf16 v[54:57], v[24:27], v[252:255], v[54:57]\n" \
  "s_waitcnt lgkmcnt(4)\n" \
  "v_mfma_f32_16x16x32_bf16 v[60:63], v[24:27], v[28:31], v[60:63]\n" \
  "s_waitcnt lgkmcnt(0)\n" \
  "s_nop 4\n" \
  "v_fma_f32 v42, -v32, v8, v42\n" \
  "v_fma_f32 v43, -v33, v8, v43\n" \
  "v_fma_f32 v44, -v34, v8, v44\n" \
  "v_fma_f32 v45, -v35, v8, v45\n" \
  "v_fma_f32 v48, -v32, v9, v48\n" \
  "v_fma_f32 v49, -v33, v9, v49\n" \
  "v_fma_f32 v50, -v34, v9, v50\n" \
  "v_fma_f32 v51, -v35, v9, v51\n" \
  "v_fma_f32 v54, -v32, v10, v54\n" \
  "v_fma_f32 v55, -v33, v10, v55\n" \
  "v_fma_f32 v56, -v34, v10, v56\n" \
  "v_fma_f32 v57, -v35, v10, v57\n" \
  "v_fma_f32 v60, -v32, v4, v60\n" \
  "v_fma_f32 v61, -v33, v4, v61\n" \
  "v_fma_f32 v62, -v34, v4, v62\n" \
  "v_fma_f32 v63, -v35, v4, v63\n" \
  "ds_read_b128 v[32:35], v1 offset:0\n" \
  "v_mul_f32 v42, v42, v36\n" \
  "v_mul_f32 v43, v43, v37\n" \
  "v_mul_f32 v44, v44, v38\n" \
  "v_mul_f32 v45, v45, v39\n" \
  "v_mul_f32 v48, v48, v36\n" \
  "v_mul_f32 v49, v49, v37\n" \
  "v_mul_f32 v50, v50, v38\n" \
  "v_mul_f32 v51, v51, v39\n" \
  "v_mul_f32 v54, v54, v36\n" \
  "v_mul_f32 v55, v55, v37\n" \
  "v_mul_f32 v56, v56, v38\n" \
  "v_mul_f32 v57, v57, v39\n" \
  "v_mul_f32 v60, v60, v36\n" \
  "v_mul_f32 v61, v61, v37\n" \
  "v_mul_f32 v62, v62, v38\n" \
  "v_mul_f32 v63, v63, v39\n" \
  "ds_read_b128 v[36:39], v1 offset:1024\n" \
  "v_cvt_pk_bf16_f32 v40, v42, v43\n" \
  "v_cvt_pk_bf16_f32 v41, v44, v45\n" \
  "v_cvt_pk_bf16_f32 v46, v48, v49\n" \
  "v_cvt_pk_bf16_f32 v47, v50, v51\n" \
  "v_cvt_pk_bf16_f32 v52, v54, v55\n" \
  "v_cvt_pk_bf16_f32 v53, v56, v57\n" \
  "v_cvt_pk_bf16_f32 v58, v60, v61\n" \
  "v_cvt_pk_bf16_f32 v59, v62, v63\n" \
  "ds_read_b128 v[24:27], v0 offset:6400\n" \
  "ds_read_b128 v[28:31], v1 offset:2048\n" \
  "v_mfma_f32_16x16x32_bf16 v[42:45], v[12:15], v[64:67], 0\n" \
  "v_mfma_f32_16x16x32_bf16 v[48:51], v[12:15], v[128:131], 0\n" \
  "v_mfma_f32_16x16x32_bf16 v[54:57], v[12:15], v[192:195], 0\n" \
  "s_waitcnt lgkmcnt(3)\n" \
  "v_mfma_f32_16x16x32_bf16 v[60:63], v[12:15], v[32:35], 0\n" \
  "ds_read_b128 v[12:15], v0 offset:8448\n" \
  "ds_read_b128 v[32:35], v1 offset:3072\n" \
  "v_mfma_f32_16x16x32_bf16 v[42:45], v[16:19], v[68:71], v[42:45]\n" \
  "v_mfma_f32_16x16x32_bf16 v[48:51], v[16:19], v[132:135], v[48:51]\n" \
  "v_mfma_f32_16x16x32_bf16 v[54:57], v[16:19], v[196:199], v[54:57]\n" \
  "s_waitcnt lgkmcnt(4)\n" \
  "v_mfma_f32_16x16x32_bf16 v[60:63], v[16:19], v[36:39], v[60:63]\n" \
  "ds_read_b128 v[16:19], v0 offset:10496\n" \
  "ds_read_b128 v[36:39], v1 offset:4096\n" \
  "v_mfma_f32_16x16x32_bf16 v[42:45], v[20:23], v[72:75], v[42:45]\n" \
  "v_mfma_f32_16x16x32_bf16 v[48:51], v[20:23], v[136:139], v[48:51]\n" \
  "v_mfma_f32_16x16x32_bf16 v[54:57], v[20:23], v[200:203], v[54:57]\n" \
  "s_waitcnt lgkmcnt(4)\n" \
  "v_mfma_f32_16x16x32_bf16 v[60:63], v[20:23], v[28:31], v[60:63]\n" \
  "ds_read_b128 v[20:23], v0 offset:12544\n" \
  "ds_read_b128 v[28:31], v1 offset:5120\n" \
  "v_mfma_f32_16x16x32_bf16 v[42:45], v[24:27], v[76:79], v[42:45]\n" \
  "v_mfma_f32_16x16x32_bf16 v[48:51], v[24:27], v[140:143], v[48:51]\n" \
  "v_mfma_f32_16x16x32_bf16 v[54:57], v[24:27], v[204:207], v[54:57]\n" \
  "s_waitcnt lgkmcnt(4)\n" \
  "v_mfma_f32_16x16x32_bf16 v[60:63], v[24:27], v[32:35], v[60:63]\n" \
  "ds_read_b128 v[24:27], v0 offset:14592\n" \
  "ds_read_b128 v[32:35], v1 offset:6144\n" \
  "v_mfma_f32_16x16x32_bf16 v[42:45], v[12:15], v[80:83], v[42:45]\n" \
  "v_mfma_f32_16x16x32_bf16 v[48:51], v[12:15], v[144:147], v[48:51]\n" \
  "v_mfma_f32_16x16x32_bf16 v[54:57], v[12:15], v[208:211], v[54:57]\n" \
  "s_waitcnt lgkmcnt(4)\n" \
  "v_mfma_f32_16x16x32_bf16 v[60:63], v[12:15], v[36:39], v[60:63]\n" \
  "ds_read_b128 v[12:15], v0 offset:16640\n" \
  "ds_read_b128 v[36:39], v1 offset:7168\n" \
  "v_mfma_f32_16x16x32_bf16 v[42:45], v[16:19], v[84:87], v[42:45]\n" \
  "v_mfma_f32_16x16x32_bf16 v[48:51], v[16:19], v[148:151], v[48:51]\n" \
  "v_mfma_f32_16x16x32_bf16 v[54:57], v[16:19], v[212:215], v[54:57]\n" \
  "s_waitcnt lgkmcnt(4)\n" \
  "v_mfma_f32_16x16x32_bf16 v[60:63], v[16:19], v[28:31], v[60:63]\n" \
  "ds_read_b128 v[16:19], v0 offset:18688\n" \
  "ds_read_b128 v[28:31], v1 offset:8192\n" \
  "v_mfma_f32_16x16x32_bf16 v[42:45], v[20:23], v[88:91], v[42:45]\n" \
  "v_mfma_f32_16x16x32_bf16 v[48:51], v[20:23], v[152:155], v[48:51]\n" \
  "v_mfma_f32_16x16x32_bf16 v[54:57], v[20:23], v[216:219], v[54:57]\n" \
  "s_waitcnt lgkmcnt(4)\n" \
  "v_mfma_f32_16x16x32_bf16 v[60:63], v[20:23], v[32:35], v[60:63]\n" \
  "ds_read_b128 v[20:23], v0 offset:20736\n" \
  "ds_read_b128 v[32:35], v1 offset:9216\n" \
  "v_mfma_f32_16x16x32_bf16 v[42:45], v[24:27], v[92:95], v[42:45]\n" \
  "v_mfma_f32_16x16x32_bf16 v[48:51], v[24:27], v[156:159], v[48:51]\n" \
  "v_mfma_f32_16x16x32_bf16 v[54:57], v[24:27], v[220:223], v[54:57]\n" \
  "s_waitcnt lgkmcnt(4)\n" \
  "v_mfma_f32_16x16x32_bf16 v[60:63], v[24:27], v[36:39], v[60:63]\n" \
  "ds_read_b128 v[24:27], v0 offset:22784\n" \
  "ds_read_b128 v[36:39], v1 offset:10240\n" \
  "v_mfma_f32_16x16x32_bf16 v[42:45], v[12:15], v[96:99], v[42:45]\n" \
  "v_mfma_f32_16x16x32_bf16 v[48:51], v[12:15], v[160:163], v[48:51]\n" \
  "v_mfma_f32_16x16x32_bf16 v[54:57], v[12:15], v[224:227], v[54:57]\n" \
  "s_waitcnt lgkmcnt(4)\n" \
  "v_mfma_f32_16x16x32_bf16 v[60:63], v[12:15], v[28:31], v[60:63]\n" \
  "ds_read_b128 v[12:15], v0 offset:24832\n" \
  "ds_read_b128 v[28:31], v1 offset:11264\n" \
  "v_mfma_f32_16x16x32_bf16 v[42:45], v[16:19], v[100:103], v[42:45]\n" \
  "v_mfma_f32_16x16x32_bf16 v[48:51], v[16:19], v[164:167], v[48:51]\n" \
  "v_mfma_f32_16x16x32_bf16 v[54:57], v[16:19], v[228:231], v[54:57]\n" \
  "s_waitcnt lgkmcnt(4)\n" \
  "v_mfma_f32_16x16x32_bf16 v[60:63], v[16:19], v[32:35], v[60:63]\n" \
  "ds_read_b128 v[16:19], v0 offset:26880\n" \
  "ds_read_b128 v[32:35], v1 offset:12288\n" \
  "v_mfma_f32_16x16x32_bf16 v[42:45], v[20:23], v[104:107], v[42:45]\n" \
  "v_mfma_f32_16x16x32_bf16 v[48:51], v[20:23], v[168:171], v[48:51]\n" \
  "v_mfma_f32_16x16x32_bf16 v[54:57], v[20:23], v[232:235], v[54:57]\n" \
  "s_waitcnt lgkmcnt(4)\n" \
  "v_mfma_f32_16x16x32_bf16 v[60:63], v[20:23], v[36:39], v[60:63]\n" \
  "ds_read_b128 v[20:23], v0 offset:28928\n" \
  "ds_read_b128 v[36:39], v1 offset:13312\n" \
  "v_mfma_f32_16x16x32_bf16 v[42:45], v[24:27], v[108:111], v[42:45]\n" \
  "v_mfma_f32_16x16x32_bf16 v[48:51], v[24:27], v[172:175], v[48:51]\n" \
  "v_mfma_f32_16x16x32_bf16 v[54:57], v[24:27], v[236:239], v[54:57]\n" \
  "s_waitcnt lgkmcnt(4)\n" \
  "v_mfma_f32_16x16x32_bf16 v[60:63], v[24:27], v[28:31], v[60:63]\n" \
  "ds_read_b128 v[24:27], v0 offset:30976\n" \
  "ds_read_b128 v[28:31], v1 offset:14336\n" \
  "v_mfma_f32_16x16x32_bf16 v[42:45], v[12:15], v[112:115], v[42:45]\n" \
  "v_mfma_f32_16x16x32_bf16 v[48:51], v[12:15], v[176:179], v[48:51]\n" \
  "v_mfma_f32_16x16x32_bf16 v[54:57], v[12:15], v[240:243], v[54:57]\n" \
  "s_waitcnt lgkmcnt(4)\n" \
  "v_mfma_f32_16x16x32_bf16 v[60:63], v[12:15], v[32:35], v[60:63]\n" \
  "ds_read_b128 v[32:35], v1 offset:15360\n" \
  "v_mfma_f32_16x16x32_bf16 v[42:45], v[16:19], v[116:119], v[42:45]\n" \
  "v_mfma_f32_16x16x32_bf16 v[48:51], v[16:19], v[180:183], v[48:51]\n" \
  "v_mfma_f32_16x16x32_bf16 v[54:57], v[16:19], v[244:247], v[54:57]\n" \
  "s_waitcnt lgkmcnt(3)\n" \
  "v_mfma_f32_16x16x32_bf16 v[60:63], v[16:19], v[36:39], v[60:63]\n" \
  "ds_read_b128 v[36:39], v3 offset:64\n" \
  "v_mfma_f32_16x16x32_bf16 v[42:45], v[20:23], v[120:123], v[42:45]\n" \
  "v_mfma_f32_16x16x32_bf16 v[48:51], v[20:23], v[184:187], v[48:51]\n" \
  "v_mfma_f32_16x16x32_bf16 v[54:57], v[20:23], v[248:251], v[54:57]\n" \
  "s_waitcnt lgkmcnt(2)\n" \
  "v_mfma_f32_16x16x32_bf16 v[60:63], v[20:23], v[28:31], v[60:63]\n" \
  "ds_read_b128 v[28:31], v3 offset:192\n" \
  "s_mov_b64 exec, 1\n" \
  "ds_add_u32 v6, v7 offset:8\n" \
  "s_mov_b64 exec, -1\n" \
  "v_xor_b32 v6, 4, v6\n" \
  "v_mfma_f32_16x16x32_bf16 v[42:45], v[24:27], v[124:127], v[42:45]\n" \
  "v_mfma_f32_16x16x32_bf16 v[48:51], v[24:27], v[188:191], v[48:51]\n" \
  "v_mfma_f32_16x16x32_bf16 v[54:57], v[24:27], v[252:255], v[54:57]\n" \
  "s_waitcnt lgkmcnt(3)\n" \
  "v_mfma_f32_16x16x32_bf16 v[60:63], v[24:27], v[32:35], v[60:63]\n" \
  "s_waitcnt lgkmcnt(1)\n" \
  "s_nop 4\n" \
  "ds_read_b32 v5, v6 offset:0\n" \
  "v_fma_f32 v42, -v36, v8, v42\n" \
  "v_fma_f32 v43, -v37, v8, v43\n" \
  "v_fma_f32 v44, -v38, v8, v44\n" \
  "v_fma_f32 v45, -v39, v8, v45\n" \
  "v_mul_f32 v42, v42, v28\n" \
  "v_mul_f32 v43, v43, v29\n" \
  "v_mul_f32 v44, v44, v30\n" \
  "v_mul_f32 v45, v45, v31\n" \
  "v_cvt_pk_bf16_f32 v42, v42, v43\n" \
  "v_cvt_pk_bf16_f32 v43, v44, v45\n" \
  "buffer_store_dwordx4 v[40:43], v2, s[40:43], s65 offen offset:0 sc1\n" \
  "v_fma_f32 v48, -v36, v9, v48\n" \
  "v_fma_f32 v49, -v37, v9, v49\n" \
  "v_fma_f32 v50, -v38, v9, v50\n" \
  "v_fma_f32 v51, -v39, v9, v51\n" \
  "v_mul_f32 v48, v48, v28\n" \
  "v_mul_f32 v49, v49, v29\n" \
  "v_mul_f32 v50, v50, v30\n" \
  "v_mul_f32 v51, v51, v31\n" \
  "v_cvt_pk_bf16_f32 v48, v48, v49\n" \
  "v_cvt_pk_bf16_f32 v49, v50, v51\n" \
  "buffer_store_dwordx4 v[46:49], v2, s[40:43], s65 offen offset:1024 sc1\n" \
  "v_fma_f32 v54, -v36, v10, v54\n" \
  "v_fma_f32 v55, -v37, v10, v55\n" \
  "v_fma_f32 v56, -v38, v10, v56\n" \
  "v_fma_f32 v57, -v39, v10, v57\n" \
  "v_mul_f32 v54, v54, v28\n" \
  "v_mul_f32 v55, v55, v29\n" \
  "v_mul_f32 v56, v56, v30\n" \
  "v_mul_f32 v57, v57, v31\n" \
  "v_cvt_pk_bf16_f32 v54, v54, v55\n" \
  "v_cvt_pk_bf16_f32 v55, v56, v57\n" \
  "buffer_store_dwordx4 v[52:55], v2, s[40:43], s65 offen offset:2048 sc1\n" \
  "v_fma_f32 v60, -v36, v4, v60\n" \
  "v_fma_f32 v61, -v37, v4, v61\n" \
  "v_fma_f32 v62, -v38, v4, v62\n" \
  "v_fma_f32 v63, -v39, v4, v63\n" \
  "v_mul_f32 v60, v60, v28\n" \
  "v_mul_f32 v61, v61, v29\n" \
  "v_mul_f32 v62, v62, v30\n" \
  "v_mul_f32 v63, v63, v31\n" \
  "v_cvt_pk_bf16_f32 v60, v60, v61\n" \
  "v_cvt_pk_bf16_f32 v61, v62, v63\n" \
  "buffer_store_dwordx4 v[58:61], v2, s[40:43], s65 offen offset:3072 sc1\n" \
  "s_waitcnt lgkmcnt(0)\n" \
  "s_add_u32 s65, s65, 0x40000\n" \
  "s_sub_u32 s64, s64, 1\n" \
  "s_mov_b32 s68, 1\n" \
  "s_cmp_lg_u32 s64, 0\n" \
  "s_cbranch_scc1 .LCW_TOP_%=\n" \
  "s_waitcnt vmcnt(0)\n" \
  "s_mov_b64 exec, 1\n" \
  "buffer_store_dword v7, off, s[56:59], s72 sc1\n" \
  "s_mov_b64 exec, -1\n" \
  "s_add_u32 s72, s72, 0x1000\n" \
  "s_mov_b32 s68, 0\n" \
  ".LCW_END_%=:\n" \
  "s_endpgm\n"
constexpr int RG = 2;
#define RFL(x) __builtin_amdgcn_readfirstlane(x)
#define HALF(x, i) BF8(((i) ? __builtin_shufflevector(x, x, 4, 5, 6, 7) : __builtin_shufflevector(x, x, 0, 1, 2, 3)))
constexpr int P_W = 65536, P_ST = 131072, P_SS = P_ST + 1024, P_CN = P_SS + 512;
template <bool PR>
__device__ __forceinline__ void role_proj_t(const MegaArgs& A, char* smem, const int L, const int idx, const bool kv) {
  const int w = A.wv;
  const int l = lane_id();
  const int c = l & 15, g = l >> 4;
  char* ws = A.ws;
  unsigned* FL = (unsigned*)(ws + OFF_FL);
  unsigned* TMO = FL + FL_TMO;
  const unsigned sb = lds_base(smem);
  auto cn = (volatile __attribute__((address_space(3))) unsigned*)(unsigned long)(sb + P_CN);
  u64 tag;
  {
    const int j = w & 3;
    const bool vt = kv && j >= 2;
    constexpr int NT = PR ? 3 : 1;
    v8u wq[NT][8];
    const u16* Wm = (kv ? (PR ? A.Wv : A.Wk) : A.Wq) - (PR ? 128 : 0) * HD;
    const int hm = kv ? idx : 2 * idx + PR;
    const int q = (l >> 2) & 3, p = l & 3, f = q | ((g & 1) << 2);
    unsigned ra[NT];
#pragma unroll
    for (int m = 0; m < NT; ++m) {
      const int ct = PR ? m : 3;
      ra[m] = sb + (j >> 1) * 65536 + (8 * g + q) * 256 + ((4 * (j & 1) + (vt ? ct : (ct & 2) + (p >> 1))) ^ f) * 32 + (vt ? p * 8 : (p & 1) * 16 + (ct & 1) * 8);
    }
#define W_ISSUE(n) wqkv_issue<false>(Wm + (128 * (n) - 256 * ((n) & 1)) * HD, (n) & 1, hm, w, l, sb)
#define W_STAGE(n, WAIT, NEXT)                                                                             \
  {                                                                                                        \
    WAIT;                                                                                                  \
    RAW_BAR();                                                                                             \
    _Pragma("unroll") for (int m = 0; m < NT; ++m) {                                                       \
      _Pragma("unroll") for (int t = 0; t < 2; ++t) {                                                      \
        const unsigned a_ = ra[m] + ((n) & 1) * 8192 + t * 32768;                                          \
        const v2u r0 = trd(a_), r1 = trd(a_ + 1024), r2 = trd(a_ + 16384), r3 = trd(a_ + 16384 + 1024);    \
        wq[m][2 * (n) + t] = v8u{r0.x, r0.y, r1.x, r1.y, r2.x, r2.y, r3.x, r3.y};                          \
      }                                                                                                    \
    }                                                                                                      \
    _Pragma("unroll") for (int m = 0; m < 2 * NT; ++m) asm volatile("" : "+v"(wq[m >> 1][2 * (n) + (m & 1)])); \
    RAW_BAR();                                                                                             \
    NEXT;                                                                                                  \
    _Pragma("unroll") for (int m = 0; m < 4 * NT; ++m)                                                     \
      cs[m % NT] = MFMA16(HALF(wq[m % NT][2 * (n) + m / NT / 2], (m / NT) & 1), BF8(one), cs[m % NT]);     \
    _Pragma("unroll") for (int m = 0; m < NT; ++m) asm volatile("" : "+v"(cs[m]));                         \
  }
    __builtin_amdgcn_s_waitcnt(0x0f70);
    W_ISSUE(0);
    W_ISSUE(1);
    tag = call_tag(A);
    if (w == 0 && l < 8) cn[l] = l == 1 ? 3u : 0u;
    const v4u one = {0x3f803f80u, 0x3f803f80u, 0x3f803f80u, 0x3f803f80u};
    f32x4 cs[NT] = {};
    W_STAGE(0, WAIT_VM(8), W_ISSUE(2));
    SST(4);
    W_STAGE(1, WAIT_VM(8), W_ISSUE(3));
    SST(5);
    W_STAGE(2, WAIT_VM(8), (void)0);
    SST(6);
    ready_wait(A, tag, ready_look(A));
    W_STAGE(3, WAIT_VM(0), if (!PR) {
      _Pragma("unroll") for (int ks = 0; ks < 16; ++ks)
        *reinterpret_cast<bf16x8*>(smem + P_W + j * 16384 + ks * 1024 + l * 16) = HALF(wq[0][ks >> 1], ks & 1);
    });
    SST(7);
#undef W_STAGE
#undef W_ISSUE
#pragma unroll
    for (int m = 0; m < NT; ++m) *reinterpret_cast<f32x4*>(smem + P_ST + j * 256 + (PR ? m : 3) * 64 + g * 16) = cs[m];
    RAW_BAR();
    SST(8);
    if constexpr (PR) {
      const v4u rsO = mk_srd(ws + RFL((unsigned)(vt ? OFF_VF + L * SZ_O : OFF_QK + L * SZ_QK)), RFL((unsigned)(vt ? SZ_O : SZ_QK)));
      const v4u rsP = mk_srd(FL + RFL(kv ? FL_FQ + (L * 64 * 32 + idx) * 32 + 2 * j : FL_FP + (L * 16 + idx) * 32 + j), 63 * 4096 + 4);
      const int so = RFL(vt ? idx * 8 + 4 * (j & 1) : (kv ? idx * 16 + 8 : (2 * idx + (j >> 1)) * 16) + 2 * (j & 1));
      const v8u pv = {sb + 32768 + g * 512 + c * 16, sb + P_W + j * 16384 + l * 16, (unsigned)l * 16, sb + P_SS + 256 + (vt ? g * 16 : c * 4),
                      sb + P_ST + j * 256 + (vt ? c * 4 : g * 16), 0u, 0u, 0u};
      const char* dbg = nullptr;
#define P_OPS                                                                                                                               \
  ::"{v[64:71]}"(wq[0][0]), "{v[72:79]}"(wq[0][1]), "{v[80:87]}"(wq[0][2]), "{v[88:95]}"(wq[0][3]), "{v[96:103]}"(wq[0][4]),                \
      "{v[104:111]}"(wq[0][5]), "{v[112:119]}"(wq[0][6]), "{v[120:127]}"(wq[0][7]), "{v[128:135]}"(wq[1][0]), "{v[136:143]}"(wq[1][1]),     \
      "{v[144:151]}"(wq[1][2]), "{v[152:159]}"(wq[1][3]), "{v[160:167]}"(wq[1][4]), "{v[168:175]}"(wq[1][5]), "{v[176:183]}"(wq[1][6]),     \
      "{v[184:191]}"(wq[1][7]), "{v[192:199]}"(wq[2][0]), "{v[200:207]}"(wq[2][1]), "{v[208:215]}"(wq[2][2]), "{v[216:223]}"(wq[2][3]),     \
      "{v[224:231]}"(wq[2][4]), "{v[232:239]}"(wq[2][5]), "{v[240:247]}"(wq[2][6]), "{v[248:255]}"(wq[2][7]), "{v[0:7]}"(pv),               \
      "{s[40:43]}"(rsO), "{s[56:59]}"(rsP), "{s64}"(NSEG), "{s65}"(so * 1024), "{s70}"(sb + P_CN), "{s[36:37]}"(dbg)                        \
      : "memory", "vcc", "scc", "s80", "v63"
      if (vt) asm volatile("s_mov_b64 exec, -1\ns_waitcnt vmcnt(0) lgkmcnt(0)\ns_nop 4\n" V_ASM P_OPS);
      else asm volatile("s_mov_b64 exec, -1\ns_waitcnt vmcnt(0) lgkmcnt(0)\ns_nop 4\n" P_ASM P_OPS);
#undef P_OPS
      return;
    }
  }
  __builtin_amdgcn_s_setprio(RPRIO);
  if (kv || w >= 2) {
    const char* Xb = ws + (L ? OFF_X3 : OFF_XA);
    const __amdgpu_buffer_rsrc_t rsS = MKRS(ws + OFF_H + L * (SZ_X / 2), SZ_X / 2);
    const __amdgpu_buffer_rsrc_t rsM = MKRS(ws + OFF_MEMK + L * SZ_MEM, kv && w == 2 ? 3 * SZ_MEM : 0);
    const int b0 = RFL(kv ? 3 * w : w == 2 ? 0 : HX0), b1 = RFL(kv ? (w == 2 ? 8 : 3 * w + 3 * (w < 2)) : w == 2 ? HX0 : 8);
    const __amdgpu_buffer_rsrc_t rsF = MKRS(FL + FL_FC + L * 64 * 32 * 32, 64 * 4096);
    const int fo = ((blockIdx.x & 3) * 8 + (w == 3 ? l >> 3 : min(b0 + (l >> 3), b1 - 1))) * 128 + (l & 7) * 4;
#define FC_LOOK(s) __builtin_amdgcn_raw_buffer_load_b32(rsF, fo, (s) * 4096, AUX_SC1)
    unsigned tn = now10(), nf = 0u;
    for (int s = 0; s < NSEG; ++s) {
      if (kv && w == 2) {
#pragma unroll
        for (int i = 0; i < 4; ++i) ST128((v4u{0u, 0u, 0u, 0u}), rsM, l * 16 + (i & 1) * 1024, s * 65536 + idx * 2048 + (i >> 1) * (int)(OFF_MEMV - OFF_MEMK));
      }
      for (unsigned spins = 0; !__all(nf != 0u); ++spins) {
        if (spin_timeout(spins, TMO)) return;
        if (spins) __builtin_amdgcn_s_sleep(SLP_H);
        nf = FC_LOOK(s);
        asm volatile("" ::: "memory");
      }
      for (unsigned spins = 0; cn[2 + (s & 1)] < 4u * (s >> 1); ++spins) {
        if (spins > 64u * SPIN_CAP) return;
        __builtin_amdgcn_s_sleep(2);
      }
      if (L == 0 && s >= PACE0) gate(tn);
      else tn = now10();
      tn += PACE;
      for (int i = b0; i < b1; ++i) glds64c(Xb + RFL(s * 32768 + i * 4096), l * 16, RFL(sb + (s & 1) * 32768 + i * 4096));
      if (w != 3) {
        nf = L ? 0u : FC_LOOK(s + 1);
        WAIT_VM(0);
        if (l == 0) __hip_atomic_fetch_add((__attribute__((address_space(3))) unsigned*)cn + (s & 1), kv ? 2u : 3u, __ATOMIC_RELAXED, __HIP_MEMORY_SCOPE_WORKGROUP);
      } else {
        v4u t[16];
#pragma unroll
        for (int i = 0; i < 16; ++i) t[i] = LD128(rsS, l * 16, s * 16384 + i * 1024);
        WAIT_VM(16);
        nf = L ? 0u : FC_LOOK(s + 1);
        if (l == 0 && !kv) __hip_atomic_fetch_add((__attribute__((address_space(3))) unsigned*)cn + (s & 1), 3u, __ATOMIC_RELAXED, __HIP_MEMORY_SCOPE_WORKGROUP);
        float a = 0.f, b = 0.f, qa = 0.f, qb = 0.f;
#pragma unroll
        for (int i = 0; i < 16; ++i) {
          a = one(a + __uint_as_float(t[i].x));
          b = one(b + __uint_as_float(t[i].z));
          qa = one(qa + __uint_as_float(t[i].y));
          qb = one(qb + __uint_as_float(t[i].w));
        }
        a = xsum32(xsum16(a));
        b = xsum32(xsum16(b));
        float da = 0.f, db = 0.f;
#pragma unroll
        for (int i = 0; i < 16; ++i) {
          const float u = one(__uint_as_float(t[i].x) - a * (1.0f / 64)), v = one(__uint_as_float(t[i].z) - b * (1.0f / 64));
          da = one(da + u * u);
          db = one(db + v * v);
        }
        const float var = xsum32(swap_add16(qa + da * 0.125f, qb + db * 0.125f)) * (1.0f / DM);
        const float ri = 1.0f / bfr1(sqrtf(bfr1(bfr1(var) + 1e-5f)));
        reinterpret_cast<float*>(smem + P_SS)[(s & 1) * 64 + (g >> 1) * 32 + 2 * c + (g & 1)] = g < 2 ? bfr1(((g & 1) ? b : a) * (1.0f / DM)) : ri;
        asm volatile("" ::: "memory");
        if (l == 0) cn[4 + (s & 1)] = s + 1;
      }
    }
    return;
  }
  const __amdgpu_buffer_rsrc_t rsW = MKRS(ws + OFF_PW, SZ_PW);
  const __amdgpu_buffer_rsrc_t rsX = MKRS(ws + (L ? OFF_X3 : OFF_XA), SZ_X);
  const __amdgpu_buffer_rsrc_t rsY = L ? MKRS(A.out, SZ_X) : MKRS(ws + OFF_X2, SZ_X);
  const int r = 2 * idx + (g >> 1);
  if (L == 0) {
    for (int s = 0; s < NSEG; ++s) ST128((v4u{0u, 0u, 0u, 0u}), rsY, l * 16, (s * SEG + 2 * idx + w) * DM * 2);
    DRAIN();
    if (l < 16) set_flag(FL + FL_FZ + (2 * (l & 7) + (idx >> 3)) * 32 + (idx & 7) * 4 + 2 * w + (l >> 3));
  }
  v4u sel;
#pragma unroll
  for (int e = 0; e < 4; ++e) sel[e] = (g >> 1) == (c >> 3) && ((c & 7) >> 1) == e ? 0x3f80u << (16 * (c & 1)) : 0u;
  unsigned tl = now10();
  for (int s = w; s < NSEG; s += RG) {
    grp_wait<SLP_R, 4, GATE_R>(true, nullptr, 0, FL + FL_FW + ((L * 64 + s) * 4 + (blockIdx.x & 3)) * 4 * 32, 16, TMO, tl);
    if (s >= NSEG - 6) tl = now10();
    TS(0, s, w * 64);
    v4u p[8][4], xr[2];
#pragma unroll
    for (int u = 0; u < 2; ++u) {
      xr[u] = LD128(rsX, (l & 31) * 512 + r * 16, s * 32768 + u * 16384);
#pragma unroll
      for (int k = 0; k < 8; ++k)
#pragma unroll
        for (int b = 2 * u; b < 2 * u + 2; ++b)
          p[k][b] = LD128(rsW, (g & 1) * 32768 + (c >> 2) * 128 + (g >> 1) * 64 + (c & 3) * 16 + b * 512, ((L * 64 + s) * 16 + 2 * k) * 32768 + idx * 2048);
      __builtin_amdgcn_sched_barrier(0);
    }
    __builtin_amdgcn_sched_barrier(0);
    f32x4 ac[4] = {};
#pragma unroll
    for (int k = 0; k < 8; ++k)
#pragma unroll
      for (int b = 0; b < 4; ++b) ac[b] = MFMA16(BF8(sel), BF8(p[k][b]), ac[b]);
    v4u xo[2];
    float sm = 0.f;
#pragma unroll
    for (int u = 0; u < 2; ++u) {
      const auto x = __builtin_amdgcn_permlane16_swap(pk2s(ac[2 * u][0], ac[2 * u][1]), pk2s(ac[2 * u + 1][0], ac[2 * u + 1][1]), false, false);
      const auto y = __builtin_amdgcn_permlane16_swap(pk2s(ac[2 * u][2], ac[2 * u][3]), pk2s(ac[2 * u + 1][2], ac[2 * u + 1][3]), false, false);
      const v4u a = {x[0], y[0], x[1], y[1]};
#pragma unroll
      for (int e = 0; e < 4; ++e) {
        xo[u][e] = pk2s(lo16(xr[u][e]) + lo16(a[e]), hi16(xr[u][e]) + hi16(a[e]));
        if (L == 0) xo[u][e] = xo[u][e] ? xo[u][e] : 0x8000u;
      }
      ST128(xo[u], rsY, r * (DM * 2) + (l & 31) * 16 + u * 512, s * SEG * DM * 2);
    }
    TS(1, s, w * 64);
    if (L == 0) {
#pragma unroll
      for (int e = 0; e < 8; ++e) sm = one(sm + one(lo16(xo[e >> 2][e & 3]) + hi16(xo[e >> 2][e & 3])));
      const float mean = half_sum(sm) * (1.0f / DM);
      float q0 = 0.f, q1 = 0.f;
#pragma unroll
      for (int e = 0; e < 8; ++e) {
        const float u = one(lo16(xo[e >> 2][e & 3]) - mean), v = one(hi16(xo[e >> 2][e & 3]) - mean);
        q0 = one(q0 + u * u);
        q1 = one(q1 + v * v);
      }
      const float var = half_sum(q0 + q1) * (1.0f / DM);
      const unsigned st = pk2(mean, sqrtf(bfr1(bfr1(var) + 1e-5f)));
      if ((l & 31) < 4) __hip_atomic_store((gu32*)(FL + FL_FR + ((s * 4 + (l & 3)) * 4 + (r >> 3)) * 32 + (r & 7)), st, RLX_AGENT);
    }
    TS(3, s, w * 64);
  }
  if (L == 1 && idx == 0 && w == 0 && l == 0) __hip_atomic_store((gu64*)(ws + OFF_EP), tag, RLX_AGENT);
}
__device__ __forceinline__ void role_proj(const MegaArgs& A, char* smem, const int L, const int idx, const bool kv) {
  if (A.wv < 4) role_proj_t<false>(A, smem, L, idx, kv);
  else role_proj_t<true>(A, smem, L, idx, kv);
}

constexpr int CUN = 2144;
__device__ __forceinline__ v2u part_stats(const v4u o) {
  float v[8];
#pragma unroll
  for (int e = 0; e < 4; ++e) { v[2 * e] = lo16(o[e]); v[2 * e + 1] = hi16(o[e]); }
  const float s = ((v[0] + v[1]) + (v[2] + v[3])) + ((v[4] + v[5]) + (v[6] + v[7])), m = s * 0.125f;
  float q0 = 0.f, q1 = 0.f;
#pragma unroll
  for (int e = 0; e < 4; ++e) {
    const float a = v[e] - m, b = v[4 + e] - m;
    q0 += a * a;
    q1 += b * b;
  }
  return v2u{__float_as_uint(s), __float_as_uint(q0 + q1)};
}
constexpr int CRN = 176;
constexpr int CV_LDS = (8 * 2 * CKN + 8 * 2 * CUN + 64 + 2 * 32 * 8 + 16 * CRN + 64 + 256 * 8) * 2 + 8 * 1568;
__device__ __forceinline__ void role_conv(const MegaArgs& A, char* smem, const int cv) {
  u16* Kr = reinterpret_cast<u16*>(smem);
  u16* Uc = Kr + 8 * 2 * CKN;
  u16* Us = Uc + 8 * CUN + 63;
  u16* To = Uc + 8 * 2 * CUN + 64;
  u16* Sx = To + 32 * 8;
  u16* Rc = Sx + 32 * 8;
  u16* Rs = Rc + 8 * CRN + 63;
  u16* Ta = Rc + 16 * CRN + 64;
  const int w = A.wv;
  const int l = lane_id(), tid = 64 * w + l, c = l & 15, g = l >> 4;
  const int d = 8 * cv + w;
  char* ws = A.ws;
  unsigned* FL = (unsigned*)(ws + OFF_FL);
  unsigned* TMO = FL + FL_TMO;
  u16* kr = Kr + w * 2 * CKN;
  const u16* ka = kr + (c >> 3) * CKN + 2080 - 16 * (c & 7) + 8 * g;
#define KFRAG(D0) (*reinterpret_cast<const bf16x8*>(ka - (D0)))
  typedef __attribute__((address_space(3))) const unsigned* lpu;
#define BFRAG(a) BF8((v4u{((lpu)(unsigned long)(a))[0], ((lpu)(unsigned long)(a))[1], ((lpu)(unsigned long)(a))[2], ((lpu)(unsigned long)(a))[3]}))
  f32x4 acc[16], far[8];
  v4u tx[4];
  const __amdgpu_buffer_rsrc_t rz1 = MKRS(ws + OFF_XA, SZ_X), rps = MKRS(ws + OFF_H, SZ_X);
  u16* uc = Uc + w * CUN;
  const unsigned ub = lds_base(reinterpret_cast<char*>(((c & 1) ? Us : Uc) + w * CUN + c + 8 * g + 32));
  const int t0 = 16 * (4 * (g & 1) + (g >> 1)) + c;
  float dsk;
#define SKIP(T) pk2(bf2f(uc[32 + 128 * (T) + t0]) * dsk, bf2f(uc[64 + 128 * (T) + t0]) * dsk)
#define TILE_OUT(T, a, uu)                                                                     \
  {                                                                                            \
    const unsigned yy = pk2(swap_add32(a[0], a[1]), swap_add32(a[2], a[3]));                   \
    const unsigned v_ = pk2(lo16(yy) + lo16(uu), hi16(yy) + hi16(uu));                         \
    Ta[(128 * ((T) & 7) + t0) * 8 + w] = (u16)v_;                                              \
    Ta[(128 * ((T) & 7) + t0 + 32) * 8 + w] = (u16)(v_ >> 16);                                 \
  }
#define CHUNK_ST(i)                                                                            \
  {                                                                                            \
    const v4u a_ = *reinterpret_cast<const v4u*>(Ta + (64 * w + l + 512 * ((i) & 1)) * 8);     \
    v4u o;                                                                                     \
    _Pragma("unroll") for (int e = 0; e < 4; ++e) o[e] = pk2(lo16(tx[i][e]) + lo16(a_[e]), hi16(tx[i][e]) + hi16(a_[e])); \
    ST128(o, rz1, (l >> 5) * 32768 + (l & 31) * 16, (2 * (w + 8 * (i)) * 64 + cv) * 512);      \
    ST64(part_stats(o), rps, (l >> 5) * 16384 + (l & 31) * 8, (2 * (w + 8 * (i)) * 64 + cv) * 256); \
  }
#define CHUNK_FLAG(i) set_flag(FL + FL_FC + (((2 * w + 16 * ((i) + (l >> 3)) + ((l >> 2) & 1)) * 4 + (l & 3)) * 8 + (cv >> 3)) * 32 + (cv & 7))
  {
    unsigned ta[3], k0p;
    v4u kn;
    u64 tag;
    {
      KGen G;
      G.load(A, d, l);
      const u16 pk = A.Dskip[d];
      asm volatile("" ::: "memory");
      tx[0] = *reinterpret_cast<const v4u*>(A.x0 + (size_t)tid * DM + 8 * cv);
      ask_slow(A, 16 * (w - 4) + (cv & 15), ta);
      tag = call_tag(A);
      G.setup();
      dsk = bf2f(pk);
      G.st = smem + w * 16640;
      SST(0);
      G.columns(l);
      const Cx z = G.power(1.f);
      G.rows(G.st + l * 8, cmul(G.C, z), z, 16);
      kn = G.tile(l);
      const float k0 = 2.0f * wave_sum(G.C.r.x + G.C.r.y);
      k0p = l == 0 ? pk2(k0, k0 - lo16(pk2(k0, k0))) : 0u;
    }
    SST(1);
    const __amdgpu_buffer_rsrc_t rst = MKRS(ws + OFF_GT, L_SEQ * 16);
#define U_VAL(i)                                                                               \
  {                                                                                            \
    const float mb_ = lo16(st[i]), ri_ = 1.0f / hi16(st[i]);                                   \
    _Pragma("unroll") for (int e = 0; e < 4; ++e) {                                            \
      const unsigned t_ = pk2(lo16(tx[i][e]) - mb_, hi16(tx[i][e]) - mb_);                     \
      uv[e] = pk2(lo16(t_) * ri_, hi16(t_) * ri_);                                             \
    }                                                                                          \
  }
#define U_PUT(i)                                                                               \
  _Pragma("unroll") for (int e = 0; e < 4; ++e) {                                              \
    ((lph)(unsigned long)up0)[(2 * e) * CUN + 512 * (i)] = (u16)uv[e];                         \
    ((lph)(unsigned long)up0)[(2 * e + 1) * CUN + 512 * (i)] = (u16)(uv[e] >> 16);             \
    ((lph)(unsigned long)up1)[(2 * e) * CUN + 512 * (i)] = (u16)uv[e];                         \
    ((lph)(unsigned long)up1)[(2 * e + 1) * CUN + 512 * (i)] = (u16)(uv[e] >> 16);             \
  }
#define U_ROW(i) { U_VAL(i) U_PUT(i) }
    typedef __attribute__((address_space(3))) u16* lph;
    unsigned st[4], up0 = lds_base((char*)(Uc + 32 + tid)), up1 = lds_base((char*)(Us + 32 + tid));
    asm volatile("" : "+v"(up0), "+v"(up1), "+v"(kn), "+v"(k0p));
    v4u uv;
#define ST_GET(I0, I1)                                                                                                        \
  for (unsigned spins = 0; spins < SPIN_CAP; ++spins) {                                                                       \
    bool ok = true;                                                                                                           \
    v4u t_[4];                                                                                                                \
    _Pragma("unroll") for (int i = I0; i < I1; ++i) t_[i] = LD128(rst, (tid + 512 * i) * 16, 0);                              \
    _Pragma("unroll") for (int i = I0; i < I1; ++i) {                                                                         \
      ok = ok && has_tag(tag, t_[i].x, t_[i].y) && has_tag(tag, t_[i].z, t_[i].w);                                            \
      st[i] = __builtin_amdgcn_perm(t_[i].w, t_[i].y, 0x07060302);                                                            \
    }                                                                                                                         \
    if (__all(ok)) break;                                                                                                     \
    __builtin_amdgcn_s_sleep(4);                                                                                              \
    asm volatile("" ::: "memory");                                                                                            \
  }
    if (w < 4) {
      ST_GET(0, 2);
      U_VAL(0);
      asm volatile("" : "+v"(uv));
    }
#pragma unroll
    for (int i = 1; i < 4; ++i) tx[i] = *reinterpret_cast<const v4u*>(A.x0 + (size_t)(tid + 512 * i) * DM + 8 * cv);
    RAW_BAR();
    SST(2);
    if (w < 4) { U_PUT(0) }
    *reinterpret_cast<v2u*>(kr + 2076 - 16 * c - 4 * g) = v2u{__builtin_amdgcn_perm(kn.z, kn.w, 0x05040100), __builtin_amdgcn_perm(kn.x, kn.y, 0x05040100)};
    *reinterpret_cast<v2u*>(kr + CKN + 2076 - 16 * c - 4 * g) = v2u{__builtin_amdgcn_perm(kn.z, kn.w, 0x07060302), __builtin_amdgcn_perm(kn.x, kn.y, 0x07060302)};
    if (l < 18) {
      reinterpret_cast<v4u*>(kr)[l < 16 ? 260 + l : 210 + l] = v4u{k0p & 0xffffu, 0u, 0u, 0u};
      reinterpret_cast<v4u*>(kr + CKN)[l < 16 ? 260 + l : 210 + l] = v4u{k0p >> 16, 0u, 0u, 0u};
    }
    (l < 32 ? uc : Us + w * CUN - 32)[l] = 0;
    uc[32 + L_SEQ + l] = Us[w * CUN + 32 + L_SEQ + l] = 0;
    uc[32 + 256 + l] = Us[w * CUN + 32 + 256 + l] = 0;
    RAW_BAR();
    SST(11);
    {
      f32x4 a0 = {}, a1 = {}, a2 = {};
      bf16x8 bf[9], af[9];
#pragma unroll
      for (int b = 0; b < 9; ++b) {
        af[(b + 4) % 9] = KFRAG(160 - 32 * ((b + 4) % 9));
        bf[b] = BFRAG(ub + 64 * (b - 1));
      }
      const unsigned k0 = SKIP(0), k1 = SKIP(1);
      __builtin_amdgcn_sched_barrier(0);
#pragma unroll
      for (int b = 0; b < 5; ++b) a0 = MFMA16(af[b + 4], bf[b], a0);
#pragma unroll
      for (int b = 0; b < 4; ++b) {
        a2 = MFMA16(af[b + 4], bf[b + 4], a2);
        a1 = MFMA16(af[b], bf[b], a1);
      }
      a2 = MFMA16(af[8], bf[8], a2);
      TILE_OUT(0, a0, k0);
      a1 += a2;
      TILE_OUT(1, a1, k1);
    }
    const u64 rv = ready_look(A);
    RAW_BAR();
    SST(3);
    if (w < 4) {
      CHUNK_ST(0);
      asm volatile("" : "+v"(tx[1]));
      U_ROW(1);
      DRAIN();
      ready_wait(A, tag, rv);
      if (l < 8) CHUNK_FLAG(0);
      ST_GET(2, 4);
    } else {
      ST_GET(0, 4);
      ready_wait(A, tag, rv);
      asm volatile("" : "+v"(tx[0]), "+v"(tx[1]));
      U_ROW(0);
      U_ROW(1);
    }
    ASKED(ta);
    if (tid < (16 * CRN + 64) / 8) reinterpret_cast<v4u*>(Rc)[tid] = v4u{0u, 0u, 0u, 0u};
    SST(4);
    const gu64* fk = (gu64*)(ws + OFF_KG) + cv * 16 + w;
    u64 kg = __hip_atomic_load(fk, RLX_AGENT);
    U_ROW(2);
    for (unsigned spins = 0; !__all(kg == tag) && spins < SPIN_CAP; ++spins) {
      __builtin_amdgcn_s_sleep(8);
      kg = __hip_atomic_load(fk, RLX_AGENT);
    }
    asm volatile("" : "+v"(tx[3]), "+v"(st[3])::"memory");
    SST(5);
    const unsigned* kd = reinterpret_cast<const unsigned*>(Ta + 256 * 8) + w * 392;
    glds16c(ws + OFF_KF + d * 2048, l * 16, lds_base((char*)kd));
    if (l < 34) glds16c(ws + OFF_KF + d * 2048 + 1024, l * 16, lds_base((char*)kd) + 1024);
    U_ROW(3);
    DRAIN();
    SST(6);
#pragma unroll 1
    for (int un = l; un < 228; un += 64) {
      const int kc = un < 132 ? 2 : un < 196 ? 1 : 0;
      v4u oh, ol;
#pragma unroll
      for (int e = 0; e < 4; ++e) {
        unsigned kv[2];
#pragma unroll
        for (int k = 0; k < 2; ++k) {
          const int lag = 2080 - (8 * un + 2 * e + k);
          const int q = (lag + (1 << kc) - 1 + ((lag >> (kc + 1)) & 1)) >> (kc + 1);
          kv[k] = kd[128 * kc - 128 + q];
          if (lag >= L_SEQ) kv[k] = 0u;
        }
        oh[e] = __builtin_amdgcn_perm(kv[1], kv[0], 0x05040100);
        ol[e] = __builtin_amdgcn_perm(kv[1], kv[0], 0x07060302);
      }
      reinterpret_cast<v4u*>(kr)[un] = oh;
      reinterpret_cast<v4u*>(kr + CKN)[un] = ol;
    }
    RAW_BAR();
    SST(7);
    {
      unsigned uq[2] = {ub, ub + 1024};
      bf16x8 bf[8], a[4];
      const bf16x8 bm = BFRAG(ub - 64);
      asm volatile("" : "+v"(uq[0]), "+v"(uq[1]));
#pragma unroll
      for (int j = 1; j < 8; ++j) bf[j] = BFRAG(uq[j >> 2] + 256 * (j & 3));
#pragma unroll
      for (int T = 2; T < 16; ++T) (T < 8 ? acc[T] : far[T & 7]) = MFMA16(KFRAG(128 * T + 32), bm, (f32x4{}));
#pragma unroll
      for (int e = 0; e < 3; ++e) a[e] = KFRAG(128 * e);
      const u16* kq = ka - 128 * 7;
#pragma unroll 1
      for (int r = 0; r < 4; ++r) {
#pragma unroll
        for (int e = 0; e < 8; ++e) {
          a[(e + 3) & 3] = *reinterpret_cast<const bf16x8*>(kq + 128 * (7 - ((e + 3) & 7)) + (e >= 5 ? 32 : 0));
          const int j = (8 - e) & 7;
          bf[j] = BFRAG(uq[j >> 2] + 256 * (j & 3) + (e ? 64 : 0));
          __builtin_amdgcn_sched_barrier(0);
#pragma unroll
          for (int T = 7; T >= (e > 2 ? e : 2); --T) acc[T] = MFMA16(a[e & 3], bf[T - e], acc[T]);
          __builtin_amdgcn_sched_barrier(0);
        }
        kq += 32;
        uq[0] += 64;
        uq[1] += 64;
        asm volatile("" : "+v"(uq[0]), "+v"(uq[1]));
      }
    }
    SST(8);
    {
      unsigned k[8];
#pragma unroll
      for (int T = 2; T < 8; ++T) k[T] = SKIP(T);
#pragma unroll
      for (int T = 2; T < 8; ++T) TILE_OUT(T, acc[T], k[T]);
    }
    RAW_BAR();
    SST(10);
    if (w >= 4) CHUNK_ST(0);
    CHUNK_ST(1);
    if (w == 1) (void)wait_n<SLP_V, 32>(FL + FL_FZ + (blockIdx.x & 7) * 64, 64, TMO);
    DRAIN();
    if (l < 16) CHUNK_FLAG(0);
    SST(9);
#undef U_ROW
#undef U_VAL
#undef U_PUT
#undef ST_GET
  }
#define FAR_HALF(r, J0, J1)                                                                     \
  {                                                                                            \
    bf16x8 bf[16], af[16];                                                                     \
    unsigned u_ = ub + 64 * (r) + 256 * (J0), v_ = u_ + 1024;                                  \
    asm volatile("" : "+v"(u_), "+v"(v_));                                                     \
    _Pragma("unroll") for (int j = (J0); j < (J1); ++j) bf[j] = BFRAG((j - (J0) < 4 ? u_ : v_) + 256 * ((j - (J0)) & 3)); \
    _Pragma("unroll") for (int e = (9 - (J1) > 0 ? 9 - (J1) : 0); e < 16 - (J0); ++e) af[e] = KFRAG(128 * e - 32 * (r)); \
    __builtin_amdgcn_sched_barrier(0);                                                         \
    _Pragma("unroll") for (int j = (J1) - 1; j >= (J0); --j)                                   \
      _Pragma("unroll") for (int T = (j > 8 ? j : 8); T < 16; ++T) far[T - 8] = MFMA16(af[T - j], bf[j], far[T - 8]); \
  }
#define FAR_PIECE(p)                                                                            \
  {                                                                                            \
    int l = tid & 63;                                                                          \
    asm volatile("" : "+v"(l), "+v"(tx[2]), "+v"(tx[3]));                                      \
    if ((p) < 16) {                                                                            \
      const int r_ = (p) >> 2;                                                                 \
      if (((p) & 3) == 0) FAR_HALF(r_, 0, 3) else if (((p) & 3) == 1) FAR_HALF(r_, 3, 6)       \
      else if (((p) & 3) == 2) FAR_HALF(r_, 6, 9) else FAR_HALF(r_, 9, 16)                     \
    } else if ((p) == 16) {                                                                    \
      unsigned k_[8];                                                                          \
      _Pragma("unroll") for (int T = 0; T < 8; ++T) k_[T] = SKIP(8 + T);                       \
      _Pragma("unroll") for (int T = 0; T < 8; ++T) TILE_OUT(T, far[T], k_[T]);                \
    } else if ((p) == 17) {                                                                    \
      CHUNK_ST(2);                                                                             \
    } else if ((p) == 18) {                                                                    \
      CHUNK_ST(3);                                                                             \
    } else {                                                                                   \
      DRAIN();                                                                                 \
      if (l < 16) CHUNK_FLAG(2);                                                               \
    }                                                                                          \
  }
  static_assert(CVPRE <= 16, "the later pieces need the barriers of the segments");
  const unsigned rb = lds_base(reinterpret_cast<char*>(((c & 1) ? Rs : Rc) + w * CRN + c + 8 * g));
#pragma unroll
  for (int j = 0; j < 16; ++j) acc[j] = f32x4{};
  const __amdgpu_buffer_rsrc_t rx = MKRS(ws + OFF_X2, SZ_X), rz = MKRS(ws + OFF_X3, SZ_X);
  const __amdgpu_buffer_rsrc_t rf = MKRS(FL + FL_FR, 64 * 16 * 128);
  unsigned st = 0u;
  v2u xx = {0u, 0u};
#define X2_LOAD(ss, far)                                                                                                      \
  {                                                                                                                      \
    xx = LD64(rx, (l >> 1) * (DM * 2) + (l & 1) * 8, (far) ? (int)SZ_X : ((ss) * SEG * DM + 8 * cv) * 2);                 \
    st = __builtin_amdgcn_raw_buffer_load_b32(rf, (l >> 4) * 128 + ((l >> 1) & 7) * 4, ((ss) * 4 + (blockIdx.x & 3)) * 512, AUX_SC1); \
  }
  unsigned tl = now10();
  bool late = false;
  for (int s = -CVPRE; s < NSEG; ++s) {
   if (s >= 0) {
    const int q = s & 3;
    if (w == 1) {
      late = __builtin_popcountll(__ballot(st != 0u)) >= CVLATE;
      for (unsigned spins = 0; !__all(umin3(xx.x, xx.y, st) != 0u); ++spins) {
        if (spin_timeout(spins, TMO)) break;
        if (GATE_V && spins == 0 && !__any(st != 0u)) gate(tl);
        if (spins && !__all(st != 0u)) __builtin_amdgcn_s_sleep(SLP_V);
        X2_LOAD(s, __builtin_popcountll(__ballot(st != 0u)) < CVTHR);
      }
      tl = now10() + GATE_V;
      TS(0, s, 64);
      const float mb = lo16(st), ri = 1.0f / hi16(st);
      const unsigned t0 = pk2(lo16(xx.x) - mb, hi16(xx.x) - mb), t1 = pk2(lo16(xx.y) - mb, hi16(xx.y) - mb);
      const unsigned u0 = pk2(lo16(t0) * ri, hi16(t0) * ri), u1 = pk2(lo16(t1) * ri, hi16(t1) * ri);
      u16* rp = Rc + (l & 1) * 4 * CRN + 32 * q + (l >> 1);
#define R_PUT(o) { rp[o] = (u16)u0; rp[(o) + CRN] = (u16)(u0 >> 16); rp[(o) + 2 * CRN] = (u16)u1; rp[(o) + 3 * CRN] = (u16)(u1 >> 16); }
      R_PUT(0) R_PUT(8 * CRN + 63)
      if (q == 0) { R_PUT(128) R_PUT(8 * CRN + 63 + 128) }
#undef R_PUT
      *reinterpret_cast<v2u*>(Sx + l * 4) = xx;
    }
    const u16* kq = ka + 32 * q;
    const bf16x8 a0 = *reinterpret_cast<const bf16x8*>(kq - 32), a1 = *reinterpret_cast<const bf16x8*>(kq);
    __syncthreads();
    TS(1, s, 0);
    const bf16x8 bp = BFRAG(rb + 64 * ((q + 3) & 3)), bc = BFRAG(rb + 64 * q);
    const float u0 = bf2f(Rc[w * CRN + 32 * q + c]), u1 = bf2f(Rc[w * CRN + 32 * q + 16 + c]);
    const float x0 = bf2f(Sx[c * 8 + w]), x1 = bf2f(Sx[(16 + c) * 8 + w]);
    acc[0] = MFMA16(a0, bp, (q == 0 ? acc[1] : acc[0]));
    const f32x4 tmp = MFMA16(a1, bc, acc[0]);
    const unsigned uu = pk2(u0 * dsk, u1 * dsk);
    const unsigned yy = pk2(xsum32((q & 1) ? tmp[2] : tmp[0]), xsum32((q & 1) ? tmp[3] : tmp[1]));
    const unsigned aa = pk2(lo16(yy) + lo16(uu), hi16(yy) + hi16(uu)), oo = pk2(x0 + lo16(aa), x1 + hi16(aa));
    if (g == (q >> 1)) {
      To[c * 8 + w] = (u16)oo;
      To[(16 + c) * 8 + w] = (u16)(oo >> 16);
    }
    __syncthreads();
    if (w == 0 && l < 32) {
      const v4u o = *reinterpret_cast<const v4u*>(To + l * 8);
      ST128(o, rz, l * 16, (s * 64 + cv) * 512);
      ST64(part_stats(o), rps, l * 8, ((64 + s) * 64 + cv) * 256);
    }
    if (w == 1) X2_LOAD(s + 1, !late);
    const int left = 15 - (s >> 2);
#define PUSH(J0, SH)                                                                                              \
  if (left >= (J0)) {                                                                                             \
    _Pragma("unroll") for (int j = (J0); j < (J0) + 4 && j < 16; ++j)                                             \
      acc[j] = MFMA16(*reinterpret_cast<const bf16x8*>(kq - 32 - 128 * j), bp, (j + (SH) < 16 ? acc[(j + (SH)) & 15] : f32x4{})); \
  }
    TS(2, s, 0);
    if (w == 0) {
      DRAIN();
      if (l < 4) set_flag(FL + FL_FC + (((64 + s) * 4 + l) * 8 + (cv >> 3)) * 32 + (cv & 7));
    }
    TS(3, s, 0);
    if (q == 0) { PUSH(1, 1) PUSH(5, 1) PUSH(9, 1) PUSH(13, 1) }
    else { PUSH(1, 0) PUSH(5, 0) PUSH(9, 0) PUSH(13, 0) }
#undef PUSH
    MSTAMP();
   }
    const int pc = s < 0 ? s + CVPRE : (s % CVSTEP) ? 20 : CVPRE + s / CVSTEP;
    if (pc < 20) FAR_PIECE(pc);
  }
#undef KFRAG
#undef BFRAG
#undef X2_LOAD
#undef FAR_PIECE
#undef FAR_HALF
#undef TILE_OUT
#undef SKIP
#undef CHUNK_ST
#undef CHUNK_FLAG
}

__device__ __forceinline__ v4u ln_row_packed(const v4u xr) {
  float v[8];
#pragma unroll
  for (int e = 0; e < 4; ++e) { v[2 * e] = lo16(xr[e]); v[2 * e + 1] = hi16(xr[e]); }
  float s = ((v[0] + v[1]) + (v[2] + v[3])) + ((v[4] + v[5]) + (v[6] + v[7]));
  s = wave_sum(s);
  const float mean = s * (1.0f / DM);
  float q = 0.f;
#pragma unroll
  for (int j = 0; j < 8; ++j) { float t = v[j] - mean; q += t * t; }
  q = wave_sum(q);
  const float var = q * (1.0f / DM);
  const float mb = bfr(mean);
  const float sd = bfr(sqrtf(bfr(bfr(var) + 1e-5f)));
  const float ri = 1.0f / sd;
  v4u o;
#pragma unroll
  for (int e = 0; e < 4; ++e) {
    const unsigned t = pk2(v[2 * e] - mb, v[2 * e + 1] - mb);
    const float x0 = lo16(t), x1 = hi16(t);
    float q0 = x0 * ri, q1 = x1 * ri;
    q0 = __builtin_fmaf(__builtin_fmaf(-q0, sd, x0), ri, q0);
    q1 = __builtin_fmaf(__builtin_fmaf(-q1, sd, x1), ri, q1);
    o[e] = pk2(q0, q1);
  }
  return o;
}

__device__ __forceinline__ void ln4_packed(v4u (&x)[4]) {
  f32x2_t v[16];
#pragma unroll
  for (int e = 0; e < 16; ++e) v[e] = f32x2_t{lo16(x[e >> 2][e & 3]), hi16(x[e >> 2][e & 3])};
  const f32x2_t t = (((v[0] + v[1]) + (v[2] + v[3])) + ((v[4] + v[5]) + (v[6] + v[7]))) +
                    (((v[8] + v[9]) + (v[10] + v[11])) + ((v[12] + v[13]) + (v[14] + v[15])));
  const float mean = red_mod4(t.x + t.y) * (1.0f / DM);
  const float mb = bfr1(mean);
  f32x2_t q0 = {0.f, 0.f}, q1 = {0.f, 0.f};
#pragma unroll
  for (int e = 0; e < 16; e += 2) {
    v[e] -= mb;
    v[e + 1] -= mb;
    q0 += v[e] * v[e];
    q1 += v[e + 1] * v[e + 1];
  }
  q0 += q1;
  const float dm = mean - mb;
  const float var = fmaxf(red_mod4(q0.x + q0.y) * (1.0f / DM) - dm * dm, 0.f);
  const float sd = bfr1(sqrtf(bfr1(bfr1(var) + 1e-5f)));
  const float ri = 1.0f / sd;
#pragma unroll
  for (int e = 0; e < 16; ++e) {
    const unsigned t2 = pk2(v[e].x, v[e].y);
    const f32x2_t q = f32x2_t{lo16(t2), hi16(t2)} * ri;
    x[e >> 2][e & 3] = pk2(q.x, q.y);
  }
}

constexpr int MEGA_WGS = 256;
constexpr size_t MEGA_LDS = (size_t)(32 * 4 * 64 * 8 + 8 * SMS + 15 * QS + 128 + 16 * 64 * 8 + 8 * SOS + 512) * 2;
static_assert(MEGA_LDS <= 163840, "lds");
static_assert(MEGA_LDS >= (size_t)AW_CNT + 128 && MEGA_LDS >= (size_t)P_CN + 512, "lds");
static_assert(MEGA_LDS >= (size_t)CV_LDS && 8 * 1568 >= 768 * 16, "lds");
static_assert(MEGA_LDS >= (size_t)2 * 32 * SMS * 2 && MEGA_LDS >= 131072, "lds");
static_assert(MEGA_LDS >= (size_t)8 * 16640, "lds");
__device__ __forceinline__ MegaArgs role_args(const int b, const int wv) {
  auto p = (const __attribute__((address_space(4))) u64*)__builtin_amdgcn_kernarg_segment_ptr();
  asm volatile("" : "+s"(p));
#define KA(i) ((u16*)(__attribute__((address_space(1))) u16*)p[i])
  return MegaArgs{KA(0), KA(1), (char*)KA(2), KA(3), KA(4), KA(5), KA(6), KA(7), KA(8), KA(9), KA(10), KA(11), KA(12), KA(13), b, wv};
#undef KA
}
__global__ __launch_bounds__(512, 2) void mega_kernel(const u16* mem0, const u16* Dskip, char* ws, u16* out, const u16* x0,
                                                      const u16* logAre, const u16* Aim, const u16* Cre, const u16* Cim,
                                                      const u16* logdt, const u16* Wq, const u16* Wk, const u16* Wv,
                                                      const u16* Wo) {
  extern __shared__ __attribute__((aligned(16))) char smem[];
  const int b = vbid(), wv = __builtin_amdgcn_readfirstlane(threadIdx.x >> 6);
#define A role_args(b, wv)
  u64 tag = 0;
  {
    const unsigned t0 = now10();
    if ((b >= 32 && b < 64) || (b >= 200 && b < 232)) start_kgen(A, smem, b < 64 ? b - 32 : b - 168);
    else if (b >= 64 && b < 96) tag = start_stats<4>(A, b - 64);
    else if (b >= 240) (void)start_stats<8>(A, b - 224);
    const unsigned dl = (b >= 32 && b < 64) ? DL_C : (b >= 64 && b < 96) ? DL_A : b >= 240 ? DL_R2 : ((b >= 96 && b < 104) || b >= 232) ? DL_R : (b >= 200 && b < 232) ? DL_Q : 0u;
    while (now10() - t0 < dl) __builtin_amdgcn_s_sleep(16);
  }
  if ((EXP_OFF & 1) && b >= 64 && b < 80) return;
  if ((EXP_OFF & 8) && b >= 200 && b < 232) return;
  if ((EXP_OFF & 16) && b >= 32 && b < 64) return;
  if ((EXP_OFF & 32) && b >= 80 && b < 96) return;
  if (b < 64) role_chain(A, smem, b >> 5, b & 31, 0);
  else if (b < 96) role_aw(A, smem, (b >> 4) & 1, b & 15, tag);
  else if (b >= 104 && b < 168) role_conv(A, smem, b - 104);
  else {
    const bool kv = b >= 168 && b < 232;
    role_proj(A, smem, b >= (kv ? 200 : 240), kv ? (b - 168) & 31 : b & 15, kv);
  }
#undef A
}

extern "C" void kernel_launch(void* const* d_in, const int* in_sizes, int n_in, void* d_out, int out_size, void* d_ws,
                              size_t ws_size, hipStream_t stream) {
  MegaArgs A;
  A.x0 = (const u16*)d_in[0];
  A.logAre = (const u16*)d_in[1];
  A.Aim = (const u16*)d_in[2];
  A.Cre = (const u16*)d_in[3];
  A.Cim = (const u16*)d_in[4];
  A.logdt = (const u16*)d_in[5];
  A.Dskip = (const u16*)d_in[6];
  A.mem0 = (const u16*)d_in[8];
  A.Wq = (const u16*)d_in[9];
  A.Wk = (const u16*)d_in[10];
  A.Wv = (const u16*)d_in[11];
  A.Wo = (const u16*)d_in[12];
  A.ws = (char*)d_ws;
  A.out = (u16*)d_out;

  (void)hipFuncSetAttribute((const void*)mega_kernel, hipFuncAttributeMaxDynamicSharedMemorySize, (int)MEGA_LDS);
  mega_kernel<<<MEGA_WGS, 512, MEGA_LDS, stream>>>(A.mem0, A.Dskip, A.ws, A.out, A.x0, A.logAre, A.Aim, A.Cre, A.Cim, A.logdt,
                                                   A.Wq, A.Wk, A.Wv, A.Wo);
}
```

```cpp
#include <hip/hip_runtime.h>
#include <hip/hip_bf16.h>
#include <stdint.h>

constexpr int L_SEQ = 2048;
constexpr int DM    = 512;
constexpr int NST   = 128;
constexpr int NH    = 32;
constexpr int DH    = 128;
constexpr int HD    = NH * DH;
constexpr int MM    = 8;
constexpr int SEG   = 32;
constexpr int NSEG  = L_SEQ / SEG;
constexpr int LK    = MM + SEG;

typedef unsigned short u16;
using bf16x8 = __attribute__((ext_vector_type(8))) short;
using f32x4  = __attribute__((ext_vector_type(4))) float;
using f32x16 = __attribute__((ext_vector_type(16))) float;

static __device__ __forceinline__ u16 f2bf(float f) {
  unsigned u = __float_as_uint(f);
  u += 0x7fffu + ((u >> 16) & 1u);
  return (u16)(u >> 16);
}
static __device__ __forceinline__ float bf2f(u16 h) { return __uint_as_float(((unsigned)h) << 16); }
static __device__ __forceinline__ float bfr(float f) { return bf2f(f2bf(f)); }

constexpr size_t SZ_X      = (size_t)L_SEQ * DM * 2;
constexpr size_t OFF_KF    = 0;
constexpr size_t OFF_UT    = OFF_KF + (size_t)DM * L_SEQ * 4;
constexpr size_t OFF_YT    = OFF_UT + SZ_X;
constexpr size_t OFF_XA    = OFF_YT + SZ_X;
constexpr size_t OFF_H     = OFF_XA + SZ_X;
constexpr size_t OFF_X2    = OFF_H + SZ_X;
constexpr size_t OFF_U2    = OFF_X2 + SZ_X;
constexpr size_t OFF_X3    = OFF_U2 + SZ_X;
constexpr size_t SZ_QK     = (size_t)L_SEQ * 2 * HD * 2;
constexpr size_t OFF_QK    = OFF_X3 + SZ_X;
constexpr size_t SZ_O      = (size_t)L_SEQ * HD * 2;
constexpr size_t OFF_VF    = OFF_QK + 2 * SZ_QK;
constexpr size_t SZ_PW     = (size_t)2 * NSEG * 16 * SEG * DM * 2;
constexpr size_t OFF_PW    = OFF_VF + 2 * SZ_O;
constexpr size_t SZ_MEM    = (size_t)NSEG * MM * HD * 2;
constexpr size_t OFF_MEMK  = OFF_PW + SZ_PW;
constexpr size_t OFF_MEMV  = OFF_MEMK + 2 * SZ_MEM;
constexpr size_t OFF_GT    = OFF_MEMV + 2 * SZ_MEM;
constexpr size_t OFF_EP    = OFF_GT + 65536;
constexpr size_t OFF_RD    = OFF_EP + 4096;
constexpr size_t OFF_KG    = OFF_RD + 4096;
constexpr size_t OFF_G     = OFF_KG + 8192;
constexpr size_t SZ_ACC    = (size_t)2 * 32768;
constexpr size_t OFF_Z     = OFF_GT + (size_t)64 * DM * 4;
constexpr size_t OFF_ACC   = OFF_Z;
constexpr size_t OFF_FL    = OFF_ACC + 2 * SZ_ACC;
constexpr int FL_FK  = 0;
constexpr int FL_FW  = FL_FK + 64 * 8 * 32;
constexpr int FL_FR  = FL_FW + 2 * 64 * 16 * 32;
constexpr int FL_FC  = FL_FR + 64 * 16 * 32;
constexpr int FL_FQ  = FL_FC + 2 * 64 * 32 * 32;
constexpr int FL_FP  = FL_FQ + 2 * 64 * 32 * 32;
constexpr int FL_FZ  = FL_FP + 64 * 32 * 32;
constexpr int FL_TMO = FL_FZ + 16 * 32;
constexpr int FL_END = FL_TMO + 64;
constexpr size_t Z_BYTES   = OFF_FL + (size_t)FL_END * 4 - OFF_Z;
constexpr size_t WS_TOTAL  = OFF_Z + Z_BYTES;
static_assert(Z_BYTES % 16 == 0 && OFF_Z % 256 == 0 && Z_BYTES <= 4 * 64 * 8192 && OFF_G + 2048 <= OFF_Z, "zero region");
static_assert(WS_TOTAL + (4 << 20) <= (size_t)256 * 1024 * 1024, "workspace too large");

static_assert(WS_TOTAL - OFF_XA <= (size_t)384 * 8 * 65536, "translation warm-up");
constexpr int CKN = 2208;
static_assert((size_t)DM * 2 * CKN * 2 <= (size_t)DM * L_SEQ * 4 + SZ_X, "K buffer");
__device__ __forceinline__ float pos_of_index(int p) {
  if (p < 256) return (float)p;
  if (p < 384) return (float)(256 + 2 * (p - 256));
  if (p < 512) return (float)(512 + 4 * (p - 384));
  if (p < 640) return (float)(1024 + 8 * (p - 512));
  return 2048.0f;
}
__device__ __forceinline__ int index_of_l(int l) {
  float pf = bfr((float)l);
  int p = (int)pf;
  if (p < 256) return p;
  if (p < 512) return 256 + ((p - 256) >> 1);
  if (p < 1024) return 384 + ((p - 512) >> 2);
  if (p < 2048) return 512 + ((p - 1024) >> 3);
  return 640;
}

template <int CTRL> __device__ __forceinline__ float dppr(float x) {
  return __builtin_bit_cast(float, __builtin_amdgcn_update_dpp(0, __builtin_bit_cast(int, x), CTRL, 0xf, 0xf, false));
}
__device__ __forceinline__ float wave_sum(float v) {
  v += dppr<0x121>(v);
  v += dppr<0x122>(v);
  v += dppr<0x124>(v);
  v += dppr<0x128>(v);
  {
    auto r = __builtin_amdgcn_permlane16_swap(__float_as_uint(v), __float_as_uint(v), false, false);
    v = __uint_as_float(r[0]) + __uint_as_float(r[1]);
  }
  {
    auto r = __builtin_amdgcn_permlane32_swap(__float_as_uint(v), __float_as_uint(v), false, false);
    v = __uint_as_float(r[0]) + __uint_as_float(r[1]);
  }
  return v;
}
__device__ __forceinline__ void ln_row8(float (&v)[8]) {
  float s = 0.f;
#pragma unroll
  for (int j = 0; j < 8; ++j) s += v[j];
  s = wave_sum(s);
  const float mean = s * (1.0f / DM);
  float q = 0.f;
#pragma unroll
  for (int j = 0; j < 8; ++j) { float t = v[j] - mean; q += t * t; }
  q = wave_sum(q);
  const float var = q * (1.0f / DM);
  const float mb = bfr(mean);
  const float sd = bfr(sqrtf(bfr(bfr(var) + 1e-5f)));
#pragma unroll
  for (int j = 0; j < 8; ++j) v[j] = bfr(bfr(v[j] - mb) / sd);
}

#define RLX_AGENT __ATOMIC_RELAXED, __HIP_MEMORY_SCOPE_AGENT
typedef __attribute__((address_space(1))) unsigned gu32;
typedef unsigned v4u __attribute__((ext_vector_type(4)));
constexpr int AUX_SC1 = 16;
constexpr unsigned SPIN_CAP = 1u << 18;

__device__ __forceinline__ bool wait_flags(const unsigned* flags, int lane, int nfl, unsigned want, unsigned* tmo) {
  for (unsigned spins = 0;; ++spins) {
    unsigned v = want;
    if (lane < nfl) v = __hip_atomic_load((gu32*)flags + lane * 32, RLX_AGENT);
    if (__all(v >= want)) break;
    if (spins > SPIN_CAP) {
      if (lane == 0) __hip_atomic_store((gu32*)tmo, 1u, RLX_AGENT);
      return false;
    }
  }
  __builtin_amdgcn_fence(__ATOMIC_ACQUIRE, "wavefront");
  return true;
}

#define STAMP(i)
#define STAMP_DUMP()
#define WSTAMP(i)
template <int CTRL> __device__ __forceinline__ float dppf(float old, float x) {
  return __builtin_bit_cast(float, __builtin_amdgcn_update_dpp(__builtin_bit_cast(int, old), __builtin_bit_cast(int, x), CTRL, 0xf, 0xf, false));
}
__device__ __forceinline__ float wave_max_dpp(float x) {
  const float ninf = -3e38f;
  x = fmaxf(x, dppf<0x111>(ninf, x));
  x = fmaxf(x, dppf<0x112>(ninf, x));
  x = fmaxf(x, dppf<0x114>(ninf, x));
  x = fmaxf(x, dppf<0x118>(ninf, x));
  x = fmaxf(x, dppf<0x142>(ninf, x));
  x = fmaxf(x, dppf<0x143>(ninf, x));
  return __builtin_bit_cast(float, __builtin_amdgcn_readlane(__builtin_bit_cast(int, x), 63));
}
__device__ __forceinline__ float wave_sum_dpp(float x) {
  x += dppf<0x111>(0.f, x);
  x += dppf<0x112>(0.f, x);
  x += dppf<0x114>(0.f, x);
  x += dppf<0x118>(0.f, x);
  x += dppf<0x142>(0.f, x);
  x += dppf<0x143>(0.f, x);
  return __builtin_bit_cast(float, __builtin_amdgcn_readlane(__builtin_bit_cast(int, x), 63));
}
constexpr int SMS = 528;
constexpr int QS = 144;
constexpr int VS = 80;
constexpr int SOS = 144;
#define VAR_NREP 4
constexpr int NREP = VAR_NREP;
__device__ __forceinline__ float xsum16(float x) {
  auto r = __builtin_amdgcn_permlane16_swap(__float_as_uint(x), __float_as_uint(x), false, false);
  return __uint_as_float(r[0]) + __uint_as_float(r[1]);
}
__device__ __forceinline__ float xsum32(float x) {
  auto r = __builtin_amdgcn_permlane32_swap(__float_as_uint(x), __float_as_uint(x), false, false);
  return __uint_as_float(r[0]) + __uint_as_float(r[1]);
}
__device__ __forceinline__ float xmax16(float x) {
  auto r = __builtin_amdgcn_permlane16_swap(__float_as_uint(x), __float_as_uint(x), false, false);
  return fmaxf(__uint_as_float(r[0]), __uint_as_float(r[1]));
}
__device__ __forceinline__ float xmax32(float x) {
  auto r = __builtin_amdgcn_permlane32_swap(__float_as_uint(x), __float_as_uint(x), false, false);
  return fmaxf(__uint_as_float(r[0]), __uint_as_float(r[1]));
}
__device__ __forceinline__ float red_mod4(float x) {
  x += dppf<0x124>(0.f, x);
  x += dppf<0x128>(0.f, x);
  return xsum32(xsum16(x));
}
typedef __bf16 bf16x2_t __attribute__((ext_vector_type(2)));
typedef float f32x2_t __attribute__((ext_vector_type(2)));
__device__ __forceinline__ unsigned pk2(float a, float b) {
  f32x2_t v = {a, b};
  bf16x2_t r = __builtin_convertvector(v, bf16x2_t);
  return __builtin_bit_cast(unsigned, r);
}
__device__ __forceinline__ float one(float x) {
  asm("" : "+v"(x));
  return x;
}
__device__ __forceinline__ unsigned pk2s(float a, float b) { return pk2(one(a), one(b)); }
__device__ __forceinline__ float dot2sq(const unsigned x, const float acc) {
  return __builtin_amdgcn_fdot2_f32_bf16(__builtin_bit_cast(bf16x2_t, x), __builtin_bit_cast(bf16x2_t, x), acc, false);
}
__device__ __forceinline__ float lo16(unsigned u) { return __uint_as_float(u << 16); }
__device__ __forceinline__ float hi16(unsigned u) { return __uint_as_float(u & 0xffff0000u); }
__device__ __forceinline__ float bfr1(float x) { return lo16(pk2(x, x)); }
__device__ __forceinline__ float half_sum(float v) {
  v += dppr<0x121>(v);
  v += dppr<0x122>(v);
  v += dppr<0x124>(v);
  v += dppr<0x128>(v);
  return xsum16(v);
}
__device__ __forceinline__ void scale_pair(float a, float b, float scale, float& oa, float& ob) {
  unsigned u = pk2(a, b);
  unsigned v = pk2s(lo16(u) * scale, hi16(u) * scale);
  oa = lo16(v);
  ob = hi16(v);
}

#define XMAP 2
__device__ __forceinline__ int vbid() {
  const int b = blockIdx.x, x = b & 7, t = b >> 3;
  if (XMAP == 0) {
    if (b < 64) return (b & 32) + 4 * x + (t & 3);
    if (b < 96) return (b & ~15) + 2 * x + (t & 1);
    if (b >= 104 && b < 168) return 104 + 8 * x + t - 13;
    if (b >= 168 && b < 232) return 168 + ((b - 168) & 32) + 4 * x + ((t - 21) & 3);
    return b;
  }
  if (XMAP == 2) {
    if (x < 4) return t < 16 ? 4 * (t & 8) + 8 * x + (t & 7) : 104 + 16 * x + t - 16;
    const int y = x - 4;
    if (t >= 24) return (t >= 28 ? 240 : y < 2 ? 96 : 224) + 4 * y + (t & 3);
    if (t < 16) return 168 + 4 * (t & 8) + 8 * y + (t & 7);
    return 64 + 4 * (t & 4) + 4 * y + (t & 3);
  }
  if (x < 2) return 2 * (t & 16) + 16 * x + (t & 15);
  const int i = 16 * (x - 2) + (t & 15);
  if (t < 16) {
    const int k = (i * 171) >> 9, r = i - 3 * k;
    return r == 0 ? 168 + k : r == 1 ? 200 + k : 64 + 16 * (k & 1) + (k >> 1);
  }
  const int g = i >> 3, e = i & 7;
  if (g == 3) return 96 + e;
  if (g == 7) return 232 + e;
  if (g == 9) return 240 + e;
  if (g == 11) return 248 + e;
  return 104 + 8 * (g - (g > 3) - (g > 7) - (g > 9)) + e;
}
#define MSTAMP()
#define WSTAMP2()
#define SST(k)
#define TS(k, ss, t)
#define LS(k, ss)
#define LS_DUMP()
struct MegaArgs {
  const u16* mem0;
  const u16* Dskip;
  char* ws;
  u16* out;
  const u16* x0;
  const u16 *logAre, *Aim, *Cre, *Cim, *logdt;
  const u16 *Wq, *Wk, *Wv, *Wo;
  int vb, wv;
};

__device__ __forceinline__ int lane_id() {
  int l;
  asm volatile("v_mbcnt_lo_u32_b32 %0, -1, 0\n\tv_mbcnt_hi_u32_b32 %0, -1, %0" : "=v"(l));
  __builtin_assume(l >= 0 && l < 64);
  return l;
}
__device__ __forceinline__ bool spin_timeout(unsigned spins, unsigned* tmo) {
  if (spins > SPIN_CAP) {
    if (lane_id() == 0) __hip_atomic_store((gu32*)tmo, 1u, RLX_AGENT);
    return true;
  }
  return false;
}
#define ROT_H (4 * (h & 1))
#define ROT_T (h & 2)
#define GF_R 400
#define GF_V 0
#define GF_H 150
#define CVT2 48
#define GF_P 0
#define GF0 8
#define SLP_H 12
#define HX0 6
#define CVSTEP 2
#define CVLATE 64
#define PACE 206
#define PACE0 6
#define RPRIO 3
#define DL_R2 1400
#define PNL 8
#define GSL 12
#define AWDIR 1
#define AWAT 0
#define AWRP 0
#define PEPS 4
#define FGATE 0
#define DL_C 0
#define DL_A 800
#define DL_R 0
#define DL_Q 0
#define KC0 0
#define CPRIO 1
#define P1PRIO 3
#define QLEAD 2
#define POLL_SLEEP 12
#define SLP_C POLL_SLEEP
#define SLP_A POLL_SLEEP
#define SLP_W POLL_SLEEP
#define SLP_R POLL_SLEEP
#define SLP_V POLL_SLEEP
#define SLP_Q POLL_SLEEP
#define SLP_M 4
#define AWPRIO 2
#define SLP_S 8
#define SPOS 4
#define CVPRE 12
#define QPD 3
#define QPRI 2
#define TLBW 1
#define QHV 0
#define QCHK 0
#define LEADN 99
#define GATE_V 170
#define GATE_R 380
#define GATE_N 380
#define AWAHEAD 1
#define GATE_M 150
#define PRI_A 3
#define PRI_WA 2
#define PRI_WB 1
#define AWSKIP 3
#define PRI_0 3
#define AWDEF 0
#define SLP_F 2
#define EXP_OFF 0
#define AWSPEC 1
#define CVTHR 48
#define NR_C 8
#define NR_R 8
#define NR_W 8
#define NR_N 2
#define QAHEAD 0
#define NS0 4
#define SLP_N POLL_SLEEP
template <int SLP, int WPL>
__device__ __forceinline__ bool wait_n(const unsigned* f, int n, unsigned* tmo) {
  const int l = lane_id();
  for (unsigned spins = 0;; ++spins) {
    unsigned v = 1u;
    if (l < n) v = __hip_atomic_load((gu32*)f + (l / WPL) * 32 + l % WPL, RLX_AGENT);
    if (__all(v != 0u)) break;
    if (spin_timeout(spins, tmo)) return false;
    __builtin_amdgcn_s_sleep(SLP);
  }
  asm volatile("" ::: "memory");
  return true;
}
__device__ __forceinline__ unsigned now10() { return (unsigned)__builtin_amdgcn_s_memrealtime(); }
__device__ __forceinline__ void gate(const unsigned t) {
  for (int i = 0; i < 64; ++i) {
    const int d = (int)(t - now10());
    if (d <= 0) break;
    if (d > 100) __builtin_amdgcn_s_sleep(32);
    else if (d > 25) __builtin_amdgcn_s_sleep(8);
    else __builtin_amdgcn_s_sleep(2);
  }
}
template <int SLP, int WPL, int GT>
__device__ __forceinline__ void grp_wait(const bool leader, volatile unsigned* sw, const unsigned val, const unsigned* f, int n,
                                         unsigned* tmo, unsigned& tl) {
  if (leader) {
    const int l = lane_id();
    for (unsigned spins = 0;; ++spins) {
      unsigned v = 1u;
      if (l < n) v = __hip_atomic_load((gu32*)f + (l / WPL) * 32 + l % WPL, RLX_AGENT);
      if (__all(v != 0u) || spin_timeout(spins, tmo)) break;
      if (GT && spins == 0) gate(tl);
      else __builtin_amdgcn_s_sleep(SLP);
    }
    if (GT) tl = now10() + GT;
    if (sw && l == 0) *sw = val;
  } else {
    for (unsigned spins = 0; *sw < val; ++spins) {
      if (spins > 64u * SPIN_CAP) break;
      if (FGATE && GT && spins == 0) gate(tl);
      __builtin_amdgcn_s_sleep(SLP_F);
    }
    if (FGATE && GT) tl = now10() + GT;
  }
  asm volatile("" ::: "memory");
}
template <int SLP, int WPL>
__device__ __forceinline__ bool wg_wait(const unsigned* f, int n, unsigned* tmo) {
  if (threadIdx.x < 64) (void)wait_n<SLP, WPL>(f, n, tmo);
  __syncthreads();
  asm volatile("" ::: "memory");
  return true;
}
__device__ __forceinline__ void set_flag(unsigned* f) { __hip_atomic_store((gu32*)f, 1u, RLX_AGENT); }
#define DRAIN() asm volatile("s_waitcnt vmcnt(0)" ::: "memory")
#define MKRS(ptr, bytes) __builtin_amdgcn_make_buffer_rsrc((void*)(ptr), 0, (int)(bytes), 0x00020000)
typedef unsigned v2u __attribute__((ext_vector_type(2)));
#define LD128(rs, voff, soff) __builtin_amdgcn_raw_buffer_load_b128(rs, voff, soff, AUX_SC1)
#define LD64(rs, voff, soff) __builtin_amdgcn_raw_buffer_load_b64(rs, voff, soff, AUX_SC1)
#define ST128(v, rs, voff, soff)                                              \
  do {                                                                        \
    const v4u st_d_ = (v);                                                    \
    __builtin_amdgcn_raw_buffer_store_b128(st_d_, rs, voff, soff, AUX_SC1);   \
    asm volatile("s_nop 1" ::"v"(st_d_) : "memory");                          \
  } while (0)
#define ST64(v, rs, voff, soff) __builtin_amdgcn_raw_buffer_store_b64(v, rs, voff, soff, AUX_SC1)
#define BF8(x) __builtin_bit_cast(bf16x8, x)
#define MFMA16(a, b, c) __builtin_amdgcn_mfma_f32_16x16x32_bf16(a, b, c, 0, 0, 0)
typedef short bf16x4 __attribute__((ext_vector_type(4)));
#define MFMA16K(a, b, c) \
  __builtin_amdgcn_mfma_f32_16x16x16bf16_1k(__builtin_bit_cast(bf16x4, a), __builtin_bit_cast(bf16x4, b), c, 0, 0, 0)
__device__ __forceinline__ float fmax_nn(float a, float b) { return __builtin_elementwise_maximum(a, b); }
typedef __bf16 bfv8 __attribute__((ext_vector_type(8)));
typedef __bf16 bfv16 __attribute__((ext_vector_type(16)));
typedef unsigned v8u __attribute__((ext_vector_type(8)));
#define SMF(a, b, c, idx) \
  __builtin_amdgcn_smfmac_f32_16x16x64_bf16(__builtin_bit_cast(bfv8, a), __builtin_bit_cast(bfv16, b), c, idx, 0, 0)
__device__ __forceinline__ float swap_add32(float a, float b) {
  auto r = __builtin_amdgcn_permlane32_swap(__float_as_uint(a), __float_as_uint(b), false, false);
  return __uint_as_float(r[0]) + __uint_as_float(r[1]);
}
__device__ __forceinline__ float swap_add16(float a, float b) {
  auto r = __builtin_amdgcn_permlane16_swap(__float_as_uint(a), __float_as_uint(b), false, false);
  return __uint_as_float(r[0]) + __uint_as_float(r[1]);
}
__device__ __forceinline__ v4u smf_fix(const v4u d) {
  auto x = __builtin_amdgcn_permlane16_swap(d.x, d.z, false, false);
  auto y = __builtin_amdgcn_permlane16_swap(d.y, d.w, false, false);
  return v4u{x[0], x[1], y[0], y[1]};
}
__device__ __forceinline__ v8u smf_bfrag(const u16* p) {
  const v2u a = *reinterpret_cast<const v2u*>(p), b = *reinterpret_cast<const v2u*>(p + 8);
  const v2u c = *reinterpret_cast<const v2u*>(p + 32), d = *reinterpret_cast<const v2u*>(p + 40);
  return v8u{a.x, b.x, a.y, b.y, c.x, d.x, c.y, d.y};
}

#define VAR_PPA 0
#define VAR_PPB 0
#define VAR_THR 0
#define VAR_THR2 0
#define CV_LEAD 100
#define VAR_EARLY 1
#define VAR_DRAIN 1
#define VAR_D1 0
#define VAR_D2 0
#define VAR_DR2 1
#define VAR_S1 0
#define VAR_S2 0
typedef __attribute__((address_space(1))) unsigned long long gu64;
typedef __attribute__((address_space(1))) char gch;
__device__ __forceinline__ int cvt_rpi(float x) {
  int r;
  asm("v_cvt_rpi_i32_f32 %0, %1" : "=v"(r) : "v"(x));
  return r;
}
typedef unsigned long long u64;
__device__ __forceinline__ u64 call_tag(const MegaArgs& A) {
  u64 t = *(const __attribute__((address_space(4))) u64*)(A.ws + OFF_EP) * 6364136223846793005ull + 1442695040888963407ull;
  asm volatile("" : "+s"(t)::"memory");
  return t;
}
__device__ __forceinline__ u64 tagged(const u64 tag, const unsigned v) { return (tag & 0xffffffffffffull) | (u64)(v >> 16) << 48; }
__device__ __forceinline__ bool has_tag(const u64 tag, const unsigned lo, const unsigned hi) {
  return lo == (unsigned)tag && (hi & 0xffffu) == ((unsigned)(tag >> 32) & 0xffffu);
}
__device__ __forceinline__ u64 ready_look(const MegaArgs& A) {
  return __hip_atomic_load((gu64*)(A.ws + OFF_RD) + (blockIdx.x & 7) * 64 + lane_id(), RLX_AGENT);
}
__device__ __forceinline__ void ready_wait(const MegaArgs& A, const u64 tag, u64 v) {
  for (unsigned spins = 0; !__all(v == tag) && spins < SPIN_CAP; ++spins) {
    __builtin_amdgcn_s_sleep(8);
    v = ready_look(A);
  }
  asm volatile("" ::: "memory");
}
typedef short v4s __attribute__((ext_vector_type(4)));
typedef __attribute__((address_space(3))) v4s* lp4s;
__device__ __forceinline__ void glds16(const void* sbase, const unsigned voff, const unsigned lds) {
  unsigned keep;
  asm volatile("s_nop 4\n\ts_mov_b32 %0, m0\n\ts_mov_b32 m0, %3\n\ts_nop 0\n\tglobal_load_lds_dwordx4 %1, %2\n\ts_mov_b32 m0, %0"
               : "=&s"(keep)
               : "v"(voff), "s"(sbase), "s"(lds)
               : "memory");
}
__device__ __forceinline__ void glds16c(const void* sbase, const unsigned voff, const unsigned lds) {
  unsigned keep;
  asm volatile("s_nop 4\n\ts_mov_b32 %0, m0\n\ts_mov_b32 m0, %3\n\ts_nop 0\n\tglobal_load_lds_dwordx4 %1, %2 sc1\n\ts_mov_b32 m0, %0"
               : "=&s"(keep)
               : "v"(voff), "s"(sbase), "s"(lds)
               : "memory");
}
__device__ __forceinline__ void glds64c(const void* sbase, const unsigned voff, const unsigned lds) {
  unsigned keep;
  asm volatile("s_nop 4\n\ts_mov_b32 %0, m0\n\ts_mov_b32 m0, %3\n\ts_nop 0\n\tglobal_load_lds_dwordx4 %1, %2 sc1\n\t"
               "global_load_lds_dwordx4 %1, %2 offset:1024 sc1\n\tglobal_load_lds_dwordx4 %1, %2 offset:2048 sc1\n\t"
               "global_load_lds_dwordx4 %1, %2 offset:3072 sc1\n\ts_mov_b32 m0, %0"
               : "=&s"(keep)
               : "v"(voff), "s"(sbase), "s"(lds)
               : "memory");
}
__device__ __forceinline__ v2u trd(const unsigned a) {
  return __builtin_bit_cast(v2u, __builtin_amdgcn_ds_read_tr16_b64_v4i16((lp4s)(unsigned long)a));
}
__device__ __forceinline__ unsigned lds_base(char* smem) { return (unsigned)(unsigned long)(__attribute__((address_space(3))) char*)smem; }
#define RAW_BAR()                                           \
  do {                                                      \
    asm volatile("s_waitcnt lgkmcnt(0)" ::: "memory");      \
    __builtin_amdgcn_s_barrier();                           \
    asm volatile("" ::: "memory");                          \
  } while (0)
#define WAIT_VM(n) asm volatile("s_waitcnt vmcnt(" #n ")" ::: "memory")
template <bool SP>
__device__ __forceinline__ void wqkv_issue(const u16* Wm, const int hf, const int h, const int w, const int l, const unsigned sb) {
  const char* base = (const char*)Wm + (size_t)(256 * hf + 32 * w) * (HD * 2) + h * (DH * 2);
#pragma unroll
  for (int i = 0; i < 8; ++i) {
    const int r = l >> 4;
    const int f = SP ? ((r & 1) | ((i & 1) << 1) | (((i >> 1) & 1) << 2)) : (r | (((i >> 1) & 1) << 2));
    glds16(base, (unsigned)((4 * i + r) * (HD * 2) + (((l & 15) ^ (f << 1)) << 4)), sb + w * 16384 + hf * 8192 + i * 1024);
  }
}

__device__ __forceinline__ unsigned row_stats(const v4u raw) {
  float v[8];
#pragma unroll
  for (int e = 0; e < 4; ++e) { v[2 * e] = lo16(raw[e]); v[2 * e + 1] = hi16(raw[e]); }
  float s = 0.f;
#pragma unroll
  for (int e = 0; e < 8; ++e) s += v[e];
  const float mean = wave_sum(s) * (1.0f / DM);
  float q = 0.f;
#pragma unroll
  for (int e = 0; e < 8; ++e) { const float t = v[e] - mean; q += t * t; }
  const float var = wave_sum(q) * (1.0f / DM);
  const float sd = bfr(sqrtf(bfr(bfr(var) + 1e-5f)));
  return (unsigned)f2bf(mean) | ((unsigned)f2bf(sd) << 16);
}
__device__ __forceinline__ void ask_slow(const MegaArgs& A, const int k, unsigned (&t)[3]) {
  const int l = lane_id(), x = blockIdx.x & 7;
  const char *wq = (const char*)A.Wq, *wv = (const char*)A.Wv;
  asm("" : "+s"(wq), "+s"(wv));
#pragma unroll
  for (int m = 0; m < 3; ++m) {
    const char* W = m == 1 ? (const char*)A.Wk : ((m == 0) == (x < 4)) ? wv : wq;
    const int a = (int)((unsigned long)W >> 8) + 8 * (x & 3), h = k < 32 ? ((1 - a) & 3) + 4 * (k & 1) : (31 - a) & 31;
    const int r = k < 32 ? k >> 1 : k - 32;
    t[m] = __builtin_amdgcn_raw_buffer_load_b32(MKRS(W, (unsigned)k < 48u && h < 8 ? DM * HD * 2 : 0), (l >> 1) * (HD * 2) + (l & 1) * 128,
                                                (32 * (r & 15)) * (HD * 2) + (8 * (x & 3) + (h & 7)) * 256, 0);
  }
}
#define ASKED(t) asm volatile("" ::"v"(t[0]), "v"(t[1]), "v"(t[2]))
template <int N>
__device__ __forceinline__ u64 start_stats(const MegaArgs& A, const int idx) {
  const int w = A.wv, l = lane_id();
  const int r0 = N * (8 * idx + w);
  unsigned ta[3], tb[3];
  v4u raw[N];
#pragma unroll
  for (int i = 0; i < N; ++i) raw[i] = *reinterpret_cast<const v4u*>(A.x0 + (size_t)(r0 + i) * DM + l * 8);
  asm volatile("" ::: "memory");
  ask_slow(A, 16 * w + (idx & 3) + (idx >> 4) * N, ta);
  if (N > 4) ask_slow(A, 16 * w + (idx & 3) + 12, tb);
  const u64 tag = call_tag(A);
  unsigned stw = 0u;
#pragma unroll
  for (int i = 0; i < N; ++i) {
    const unsigned t = row_stats(raw[i]);
    if ((l >> 1) == i) stw = t;
  }
  if (l < 2 * N) __hip_atomic_store((gu64*)(A.ws + OFF_GT) + 2 * r0 + l, tagged(tag, (l & 1) ? stw : stw << 16), RLX_AGENT);
  SST(0);
  ASKED(ta);
  if (N > 4) ASKED(tb);
  return tag;
}
struct Cx {
  f32x2_t r, i;
};
__device__ __forceinline__ Cx cmul(const Cx a, const Cx b) { return Cx{a.r * b.r - a.i * b.i, a.r * b.i + a.i * b.r}; }
struct KGen {
  unsigned pa, pb, pc, pd;
  u16 pe;
  f32x2_t ar, ai;
  Cx C;
  char* st;
  v4u bh[8], bl[8];
  __device__ __forceinline__ void load(const MegaArgs& A, const int d, const int l) {
    pa = *reinterpret_cast<const unsigned*>(A.logAre + d * NST + 2 * l);
    pb = *reinterpret_cast<const unsigned*>(A.Aim + d * NST + 2 * l);
    pc = *reinterpret_cast<const unsigned*>(A.Cre + d * NST + 2 * l);
    pd = *reinterpret_cast<const unsigned*>(A.Cim + d * NST + 2 * l);
    pe = A.logdt[d];
  }
  __device__ __forceinline__ void setup() {
    const float dt = bfr(expf(bf2f(pe)));
    ar = f32x2_t{-bfr(expf(lo16(pa))) * dt, -bfr(expf(hi16(pa))) * dt};
    ai = f32x2_t{lo16(pb) * dt, hi16(pb) * dt};
    C = Cx{f32x2_t{lo16(pc) * dt, hi16(pc) * dt}, f32x2_t{lo16(pd) * dt, hi16(pd) * dt}};
  }
  __device__ __forceinline__ Cx power(const float p) const {
    Cx z;
#pragma unroll
    for (int e = 0; e < 2; ++e) {
      const float x = ai[e] * p;
      const float f = __builtin_fmaf(x, 6.4206382e-9f, __builtin_fmaf(x, 0.15915494f, -__builtin_rintf(x * 0.15915494f)));
      const float m = __expf(ar[e] * p);
      z.r[e] = m * __builtin_amdgcn_cosf(f);
      z.i[e] = m * __builtin_amdgcn_sinf(f);
    }
    return z;
  }
  __device__ __forceinline__ char* rows(char* p, Cx y, const Cx s, const int n) const {
#pragma unroll 1
    for (int i = 0; i < n; i += 2) {
#pragma unroll
      for (int k = 0; k < 2; ++k) {
        const unsigned h0 = pk2(y.r.x, y.r.y), h1 = pk2(y.i.x, y.i.y);
        const f32x2_t a = y.r - f32x2_t{lo16(h0), hi16(h0)}, b = y.i - f32x2_t{lo16(h1), hi16(h1)};
        *reinterpret_cast<v2u*>(p + 1040 * k) = v2u{h0, h1};
        *reinterpret_cast<v2u*>(p + 1040 * k + 512) = v2u{pk2(a.x, a.y), pk2(b.x, b.y)};
        y = cmul(y, s);
      }
      p += 2080;
    }
    return p;
  }
  __device__ __forceinline__ void get(v4u (&h)[8], v4u (&o)[8], const int l) const {
    const char* p = st + (l & 15) * 1040 + (l >> 4) * 16;
    asm volatile("" ::: "memory");
#pragma unroll
    for (int k = 0; k < 8; ++k) {
      h[k] = *reinterpret_cast<const v4u*>(p + 64 * k);
      o[k] = *reinterpret_cast<const v4u*>(p + 512 + 64 * k);
    }
    asm volatile("" ::: "memory");
  }
  __device__ __forceinline__ void columns(const int l) {
    Cx s = power(16.f);
    s.i = -s.i;
    rows(st + l * 8, Cx{f32x2_t{1.f, 1.f}, f32x2_t{0.f, 0.f}}, s, 16);
    get(bh, bl, l);
  }
  __device__ __forceinline__ v4u tile(const int l) const {
    v4u h[8], o[8];
    get(h, o, l);
    f32x4 d = {}, e = {}, f = {};
#pragma unroll
    for (int k = 0; k < 8; ++k) {
      d = MFMA16(BF8(h[k]), BF8(bh[k]), d);
      e = MFMA16(BF8(o[k]), BF8(bh[k]), e);
      f = MFMA16(BF8(h[k]), BF8(bl[k]), f);
    }
    d = (d + (e + f)) * 2.0f;
    v4u r;
#pragma unroll
    for (int i = 0; i < 4; ++i) r[i] = pk2(d[i], d[i] - lo16(pk2(d[i], d[i])));
    return r;
  }
};
#define ACC_INIT 0xc9b26c86c9b26ca0ull
#define CHAIN_ASM_ATT \
  "s_mov_b32 s95, 272\n" \
  "s_mov_b32 s91, 0x4c7c2000\n" \
  "s_mov_b32 s85, 0xc9b26c9b\n" \
  "s_mov_b32 s92, 0xc9326c9b\n" \
  "s_mov_b32 s93, 0xc9326c9b\n" \
  "s_mov_b32 s78, 0x3e0293ee\n" \
  "s_mov_b32 s76, 0x33333333\n" \
  "s_mov_b32 s77, 0x33333333\n" \
  "s_lshl_b32 s98, s73, 10\n" \
  "s_add_u32 s98, s98, s74\n" \
  "s_add_u32 s98, s98, 131072\n" \
  "s_lshl_b32 s97, s73, 9\n" \
  "s_add_u32 s97, s97, s74\n" \
  "s_add_u32 s97, s97, 158720\n" \
  "s_lshl_b32 s99, s73, 10\n" \
  "v_mbcnt_lo_u32_b32 v48, -1, 0\n" \
  "v_mbcnt_hi_u32_b32 v48, -1, v48\n" \
  "s_add_u32 s88, s74, 157760\n" \
  "v_mov_b32 v59, s88\n" \
  "v_lshrrev_b32 v49, 2, v48\n" \
  "v_lshrrev_b32 v50, 4, v48\n" \
  "v_bitop3_b32 v50, v49, v50, 3 bitop3:0x28\n" \
  "v_lshl_add_u32 v8, v50, 3, s97\n" \
  "v_and_b32 v50, 3, v48\n" \
  "v_lshl_add_u32 v8, v50, 5, v8\n" \
  "v_and_b32 v49, 1, v49\n" \
  "v_lshl_add_u32 v8, v49, 8, v8\n" \
  "v_and_b32 v49, 8, v48\n" \
  "v_lshl_add_u32 v8, v49, 4, v8\n" \
  "v_lshrrev_b32 v49, 5, v48\n" \
  "v_add_u32 v10, s99, v1\n" \
  "s_lshr_b32 s88, s99, 2\n" \
  "s_add_u32 s88, s88, s74\n" \
  "s_add_u32 s88, s88, 139264\n" \
  "v_lshl_add_u32 v7, v49, 10, s88\n" \
  "v_and_b32 v49, 31, v48\n" \
  "v_lshl_add_u32 v7, v49, 3, v7\n" \
  "v_mov_b64 v[12:13], 0\n" \
  "v_mov_b64 v[14:15], 0\n" \
  "v_mov_b64 v[16:17], 0\n" \
  "v_mov_b64 v[18:19], 0\n" \
  "v_mov_b64 v[20:21], 0\n" \
  "v_mov_b64 v[22:23], 0\n" \
  "v_mov_b32 v34, 0xc9b26ca0\n" \
  "v_mov_b32 v35, 0xc9b26c86\n" \
  "v_mov_b32 v24, 0x44870000\n" \
  "v_mul_f32 v24, s72, v24\n" \
  "s_nop 0\n" \
  "v_readfirstlane_b32 s86, v24\n" \
  "s_lshr_b32 s86, s86, 23\n" \
  "s_addk_i32 s86, 0xff83\n" \
  "s_ashr_i32 s86, s86, 1\n" \
  "s_max_i32 s86, s86, -20\n" \
  "s_min_i32 s86, s86, 100\n" \
  "s_mov_b32 s82, s86\n" \
  "s_lshr_b32 s83, s72, 23\n" \
  "s_sub_i32 s83, s83, 242\n" \
  "s_lshl_b32 s88, s73, 10\n" \
  "v_add_u32 v24, s88, v1\n" \
  "ds_read_b64 v[32:33], v24 offset:0\n" \
  "ds_read_b64 v[36:37], v24 offset:8\n" \
  "s_waitcnt lgkmcnt(0)\n" \
  "v_mov_b32 v11, 0\n" \
  "v_dot2c_f32_bf16 v11, v32, v32\n" \
  "v_dot2c_f32_bf16 v11, v33, v33\n" \
  "v_dot2c_f32_bf16 v11, v36, v36\n" \
  "v_dot2c_f32_bf16 v11, v37, v37\n" \
  "s_nop 1\n" \
  "s_nop 0\n" \
  "v_cmpx_lt_f32 vcc, 2.0, v11\n" \
  "ds_max_u32 v59, v11\n" \
  "s_mov_b64 exec, -1\n" \
  ".LCA_TOP_%=:\n" \
  "s_waitcnt lgkmcnt(0)\n" \
  "s_barrier\n" \
  "ds_read_b128 v[40:43], v1 offset:0\n" \
  "ds_read_b128 v[44:47], v1 offset:1024\n" \
  "ds_read_b128 v[48:51], v1 offset:2048\n" \
  "ds_read_b128 v[52:55], v1 offset:3072\n" \
  "ds_read_b128 v[56:59], v1 offset:4096\n" \
  "ds_read_b128 v[60:63], v1 offset:5120\n" \
  "s_cmp_lg_u32 s73, 0\n" \
  "s_cbranch_scc1 .LCA_NOZ_%=\n" \
  "s_xor_b32 s88, s60, 0x8000\n" \
  "s_add_u32 s88, s88, s61\n" \
  "buffer_store_dwordx2 v[34:35], v5, s[40:43], s88 offen sc1\n" \
  ".LCA_NOZ_%=:\n" \
  "s_waitcnt lgkmcnt(5)\n" \
  "v_smfmac_f32_16x16x64_bf16 v[20:23], v[40:43], v[192:199], v0\n" \
  "v_smfmac_f32_16x16x64_bf16 v[12:15], v[40:43], v[64:71], v0\n" \
  "v_smfmac_f32_16x16x64_bf16 v[16:19], v[40:43], v[128:135], v0\n" \
  "ds_read_b128 v[40:43], v1 offset:6144\n" \
  "s_waitcnt lgkmcnt(5)\n" \
  "v_smfmac_f32_16x16x64_bf16 v[20:23], v[44:47], v[200:207], v0\n" \
  "v_smfmac_f32_16x16x64_bf16 v[12:15], v[44:47], v[72:79], v0\n" \
  "v_smfmac_f32_16x16x64_bf16 v[16:19], v[44:47], v[136:143], v0\n" \
  "ds_read_b128 v[44:47], v1 offset:7168\n" \
  "s_waitcnt lgkmcnt(5)\n" \
  "v_smfmac_f32_16x16x64_bf16 v[20:23], v[48:51], v[208:215], v0\n" \
  "v_smfmac_f32_16x16x64_bf16 v[12:15], v[48:51], v[80:87], v0\n" \
  "v_smfmac_f32_16x16x64_bf16 v[16:19], v[48:51], v[144:151], v0\n" \
  "ds_read_b128 v[24:27], v1 offset:10240\n" \
  "ds_read_b128 v[28:31], v1 offset:14336\n" \
  "s_waitcnt lgkmcnt(6)\n" \
  "v_smfmac_f32_16x16x64_bf16 v[20:23], v[52:55], v[216:223], v0\n" \
  "v_smfmac_f32_16x16x64_bf16 v[12:15], v[52:55], v[88:95], v0\n" \
  "v_smfmac_f32_16x16x64_bf16 v[16:19], v[52:55], v[152:159], v0\n" \
  "ds_read_b128 v[32:35], v1 offset:11264\n" \
  "ds_read_b128 v[36:39], v1 offset:15360\n" \
  "s_waitcnt lgkmcnt(7)\n" \
  "v_smfmac_f32_16x16x64_bf16 v[20:23], v[56:59], v[224:231], v0\n" \
  "v_smfmac_f32_16x16x64_bf16 v[12:15], v[56:59], v[96:103], v0\n" \
  "v_smfmac_f32_16x16x64_bf16 v[16:19], v[56:59], v[160:167], v0\n" \
  "ds_read_b128 v[56:59], v1 offset:12288\n" \
  "s_waitcnt lgkmcnt(7)\n" \
  "v_smfmac_f32_16x16x64_bf16 v[20:23], v[60:63], v[232:239], v0\n" \
  "v_smfmac_f32_16x16x64_bf16 v[12:15], v[60:63], v[104:111], v0\n" \
  "v_smfmac_f32_16x16x64_bf16 v[16:19], v[60:63], v[168:175], v0\n" \
  "ds_read_b128 v[60:63], v1 offset:16384\n" \
  "s_waitcnt lgkmcnt(7)\n" \
  "v_smfmac_f32_16x16x64_bf16 v[20:23], v[40:43], v[240:247], v0\n" \
  "v_smfmac_f32_16x16x64_bf16 v[12:15], v[40:43], v[112:119], v0\n" \
  "v_smfmac_f32_16x16x64_bf16 v[16:19], v[40:43], v[176:183], v0\n" \
  "s_waitcnt lgkmcnt(6)\n" \
  "v_smfmac_f32_16x16x64_bf16 v[20:23], v[44:47], v[248:255], v0\n" \
  "v_smfmac_f32_16x16x64_bf16 v[12:15], v[44:47], v[120:127], v0\n" \
  "v_smfmac_f32_16x16x64_bf16 v[16:19], v[44:47], v[184:191], v0\n" \
  "s_cmp_eq_u32 s71, 0\n" \
  "v_permlane32_swap_b32 v20, v22\n" \
  "v_permlane32_swap_b32 v21, v23\n" \
  "v_pk_add_f32 v[20:21], v[20:21], v[22:23]\n" \
  "s_mov_b64 vcc, s[76:77]\n" \
  "v_permlane32_swap_b32 v12, v14\n" \
  "v_permlane32_swap_b32 v13, v15\n" \
  "v_cvt_pk_bf16_f32 v20, v20, v21\n" \
  "v_pk_add_f32 v[12:13], v[12:13], v[14:15]\n" \
  "v_permlane32_swap_b32 v16, v18\n" \
  "v_permlane32_swap_b32 v17, v19\n" \
  "ds_write_b32 v8, v20 offset:2048\n" \
  "v_cvt_pk_bf16_f32 v12, v12, v13\n" \
  "v_pk_add_f32 v[16:17], v[16:17], v[18:19]\n" \
  "s_nop 0\n" \
  "v_cvt_pk_bf16_f32 v13, v16, v17\n" \
  "ds_write_b64 v8, v[12:13]\n" \
  "s_setprio 1\n" \
  "s_waitcnt lgkmcnt(0)\n" \
  "s_barrier\n" \
  "s_cbranch_scc1 .LCA_LAST_%=\n" \
  "ds_read_b64_tr_b16 v[40:41], v2 offset:0\n" \
  "ds_read_b64_tr_b16 v[42:43], v3 offset:0\n" \
  "ds_read_b64_tr_b16 v[44:45], v2 offset:1024\n" \
  "ds_read_b64_tr_b16 v[46:47], v3 offset:1024\n" \
  "ds_read_b64_tr_b16 v[48:49], v2 offset:2048\n" \
  "ds_read_b64_tr_b16 v[50:51], v3 offset:2048\n" \
  "ds_read_b64_tr_b16 v[52:53], v2 offset:3072\n" \
  "ds_read_b64_tr_b16 v[54:55], v3 offset:3072\n" \
  "ds_read_b32 v11, v1 offset:26688\n" \
  "s_waitcnt lgkmcnt(7)\n" \
  "v_mfma_f32_16x16x32_bf16 v[12:15], v[24:27], v[40:43], 0\n" \
  "v_mfma_f32_16x16x32_bf16 v[16:19], v[28:31], v[40:43], 0\n" \
  "ds_read_b128 v[24:27], v1 offset:13312\n" \
  "ds_read_b128 v[28:31], v1 offset:17408\n" \
  "v_mfma_f32_16x16x32_bf16 v[20:23], v[40:43], v[40:43], 0\n" \
  "s_waitcnt lgkmcnt(7)\n" \
  "v_mfma_f32_16x16x32_bf16 v[12:15], v[32:35], v[44:47], v[12:15]\n" \
  "v_mfma_f32_16x16x32_bf16 v[16:19], v[36:39], v[44:47], v[16:19]\n" \
  "v_mfma_f32_16x16x32_bf16 v[20:23], v[44:47], v[44:47], v[20:23]\n" \
  "s_waitcnt lgkmcnt(2)\n" \
  "v_mfma_f32_16x16x32_bf16 v[12:15], v[56:59], v[48:51], v[12:15]\n" \
  "v_readfirstlane_b32 s86, v11\n" \
  "v_mfma_f32_16x16x32_bf16 v[16:19], v[60:63], v[48:51], v[16:19]\n" \
  "ds_read_b128 v[32:35], v6 offset:8192\n" \
  "ds_read_b128 v[36:39], v6 offset:9216\n" \
  "v_mfma_f32_16x16x32_bf16 v[20:23], v[48:51], v[48:51], v[20:23]\n" \
  "s_lshr_b32 s86, s86, 23\n" \
  "s_add_i32 s86, s86, s83\n" \
  "s_ashr_i32 s86, s86, 1\n" \
  "ds_read_b128 v[48:51], v10 offset:18432\n" \
  "v_mfma_f32_16x16x32_bf16 v[20:23], v[52:55], v[52:55], v[20:23]\n" \
  "s_max_i32 s86, s86, s82\n" \
  "s_min_i32 s86, s86, 100\n" \
  "s_lshl_b32 s86, s86, 23\n" \
  "s_sub_i32 s84, 0xc8800000, s86\n" \
  "s_waitcnt lgkmcnt(3)\n" \
  "v_mfma_f32_16x16x32_bf16 v[12:15], v[24:27], v[52:55], v[12:15]\n" \
  "v_mfma_f32_16x16x32_bf16 v[16:19], v[28:31], v[52:55], v[16:19]\n" \
  "ds_read_b128 v[52:55], v10 offset:22528\n" \
  "ds_read_b128 v[56:59], v6 offset:0\n" \
  "ds_read_b128 v[60:63], v6 offset:1024\n" \
  "ds_read_b128 v[24:27], v6 offset:2048\n" \
  "ds_read_b128 v[28:31], v6 offset:3072\n" \
  "v_cndmask_b32_dpp v20, v23, v22, vcc quad_perm:[2,3,0,1] row_mask:0xf bank_mask:0xf\n" \
  "v_mov_b32 v41, 0\n" \
  "v_mov_b32 v42, 0\n" \
  "v_cndmask_b32_dpp v12, v16, v12, vcc quad_perm:[2,3,0,1] row_mask:0xf bank_mask:0xf\n" \
  "v_cndmask_b32_dpp v13, v17, v13, vcc quad_perm:[2,3,0,1] row_mask:0xf bank_mask:0xf\n" \
  "v_cndmask_b32_dpp v14, v18, v14, vcc quad_perm:[2,3,0,1] row_mask:0xf bank_mask:0xf\n" \
  "v_cndmask_b32_dpp v15, v19, v15, vcc quad_perm:[2,3,0,1] row_mask:0xf bank_mask:0xf\n" \
  "v_maximum3_f32 v16, v20, v12, v13\n" \
  "v_maximum3_f32 v16, v16, v14, v15\n" \
  "s_nop 1\n" \
  "v_max_f32_dpp v16, v16, v16 quad_perm:[2,3,0,1] row_mask:0xf bank_mask:0xf\n" \
  "v_mov_b32 v17, v16\n" \
  "s_nop 1\n" \
  "v_permlane16_swap_b32 v16, v17\n" \
  "v_max_f32 v16, v16, v17\n" \
  "v_mov_b32 v17, v16\n" \
  "s_nop 1\n" \
  "v_permlane32_swap_b32 v16, v17\n" \
  "v_max_f32 v16, v16, v17\n" \
  "v_mul_f32 v16, s78, v16\n" \
  "v_pk_fma_f32 v[12:13], v[12:13], s[78:79], v[16:17] op_sel_hi:[1,0,0] neg_lo:[0,0,1] neg_hi:[0,0,1]\n" \
  "v_pk_fma_f32 v[14:15], v[14:15], s[78:79], v[16:17] op_sel_hi:[1,0,0] neg_lo:[0,0,1] neg_hi:[0,0,1]\n" \
  "v_fma_f32 v20, v20, s78, -v16\n" \
  "v_exp_f32 v12, v12\n" \
  "v_exp_f32 v13, v13\n" \
  "v_exp_f32 v14, v14\n" \
  "v_exp_f32 v15, v15\n" \
  "v_exp_f32 v20, v20\n" \
  "s_waitcnt lgkmcnt(4)\n" \
  "v_cvt_pk_bf16_f32 v44, v12, v13\n" \
  "v_cvt_pk_bf16_f32 v45, v14, v15\n" \
  "v_pk_add_f32 v[8:9], v[12:13], v[14:15]\n" \
  "v_mov_b32_dpp v46, v44 quad_perm:[2,3,0,1] row_mask:0xf bank_mask:0xf\n" \
  "v_mov_b32_dpp v47, v45 quad_perm:[2,3,0,1] row_mask:0xf bank_mask:0xf\n" \
  "v_add_f32 v10, v8, v20\n" \
  "v_add_f32 v10, v10, v9\n" \
  "v_mfma_f32_16x16x32_bf16 v[12:15], v[48:51], v[44:47], 0\n" \
  "v_mov_b32_dpp v21, v20 quad_perm:[2,3,0,1] row_mask:0xf bank_mask:0xf\n" \
  "v_add_f32_dpp v10, v10, v10 quad_perm:[2,3,0,1] row_mask:0xf bank_mask:0xf\n" \
  "v_mov_b32 v11, v10\n" \
  "v_mfma_f32_16x16x32_bf16 v[16:19], v[52:55], v[44:47], 0\n" \
  "s_setprio 3\n" \
  "s_barrier\n" \
  "ds_read_b64 v[22:23], v7 offset:0\n" \
  "s_nop 0\n" \
  "v_permlane16_swap_b32 v10, v11\n" \
  "v_add_f32 v10, v10, v11\n" \
  "v_mov_b32 v11, v10\n" \
  "v_mov_b64 v[8:9], s[92:93]\n" \
  "s_nop 0\n" \
  "v_permlane32_swap_b32 v10, v11\n" \
  "v_add_f32 v10, v10, v11\n" \
  "v_rcp_f32 v10, v10\n" \
  "s_nop 0\n" \
  "v_mul_f32 v10, s84, v10\n" \
  "v_pk_mul_f32 v[20:21], v[20:21], v[10:11] op_sel_hi:[1,0]\n" \
  "v_pk_mul_f32 v[12:13], v[12:13], v[10:11] op_sel_hi:[1,0]\n" \
  "v_pk_mul_f32 v[14:15], v[14:15], v[10:11] op_sel_hi:[1,0]\n" \
  "v_cvt_pk_bf16_f32 v40, v20, v21\n" \
  "v_pk_mul_f32 v[16:17], v[16:17], v[10:11] op_sel_hi:[1,0]\n" \
  "v_cvt_pk_bf16_f32 v43, v20, v21\n" \
  "v_pk_mul_f32 v[18:19], v[18:19], v[10:11] op_sel_hi:[1,0]\n" \
  "s_waitcnt lgkmcnt(0)\n" \
  "v_mfma_f32_16x16x16_bf16 v[12:15], v[22:23], v[40:41], v[12:15]\n" \
  "v_mfma_f32_16x16x16_bf16 v[16:19], v[22:23], v[42:43], v[16:19]\n" \
  "v_mov_b64 v[10:11], s[92:93]\n" \
  "ds_read_b128 v[48:51], v6 offset:10240\n" \
  "ds_read_b128 v[52:55], v6 offset:11264\n" \
  "v_mov_b64 v[20:21], s[92:93]\n" \
  "v_mov_b64 v[22:23], s[92:93]\n" \
  "s_nop 1\n" \
  "v_cvt_pk_bf16_f32 v40, v12, v13\n" \
  "v_cvt_pk_bf16_f32 v41, v14, v15\n" \
  "v_cvt_pk_bf16_f32 v42, v16, v17\n" \
  "ds_write_b64 v4, v[40:41]\n" \
  "v_cvt_pk_bf16_f32 v43, v18, v19\n" \
  "ds_write_b64 v4, v[42:43] offset:1024\n" \
  "v_mov_b64 v[12:13], s[92:93]\n" \
  "v_mov_b64 v[14:15], s[92:93]\n" \
  "v_mov_b64 v[16:17], s[92:93]\n" \
  "v_mov_b64 v[18:19], s[92:93]\n" \
  "s_setprio 0\n" \
  "s_waitcnt lgkmcnt(0)\n" \
  "s_barrier\n" \
  "ds_read_b128 v[40:43], v1 offset:8192\n" \
  "ds_read_b128 v[44:47], v1 offset:9216\n" \
  "s_waitcnt lgkmcnt(1)\n" \
  "v_smfmac_f32_16x16x64_bf16 v[12:15], v[40:43], v[56:63], v0\n" \
  "ds_read_b128 v[56:59], v6 offset:4096\n" \
  "ds_read_b128 v[60:63], v6 offset:5120\n" \
  "v_smfmac_f32_16x16x64_bf16 v[16:19], v[40:43], v[24:31], v0\n" \
  "ds_read_b128 v[24:27], v6 offset:6144\n" \
  "ds_read_b128 v[28:31], v6 offset:7168\n" \
  "s_waitcnt lgkmcnt(4)\n" \
  "v_smfmac_f32_16x16x64_bf16 v[12:15], v[44:47], v[32:39], v0\n" \
  "ds_read_b128 v[32:35], v6 offset:12288\n" \
  "ds_read_b128 v[36:39], v6 offset:13312\n" \
  "v_smfmac_f32_16x16x64_bf16 v[16:19], v[44:47], v[48:55], v0\n" \
  "ds_read_b128 v[48:51], v6 offset:14336\n" \
  "ds_read_b128 v[52:55], v6 offset:15360\n" \
  "s_nop 5\n" \
  "v_permlane32_swap_b32 v12, v17\n" \
  "v_permlane32_swap_b32 v13, v16\n" \
  "v_permlane32_swap_b32 v14, v19\n" \
  "v_permlane32_swap_b32 v15, v18\n" \
  "v_pk_add_f32 v[12:13], v[12:13], v[16:17] op_sel:[0,1] op_sel_hi:[1,0]\n" \
  "v_pk_add_f32 v[14:15], v[14:15], v[18:19] op_sel:[0,1] op_sel_hi:[1,0]\n" \
  "buffer_atomic_add_x2 v[12:13], v5, s[40:43], s56 offen\n" \
  "buffer_atomic_add_x2 v[14:15], v5, s[40:43], s57 offen\n" \
  "s_waitcnt lgkmcnt(6)\n" \
  "v_smfmac_f32_16x16x64_bf16 v[20:23], v[40:43], v[56:63], v0\n" \
  "s_waitcnt lgkmcnt(4)\n" \
  "v_smfmac_f32_16x16x64_bf16 v[8:11], v[40:43], v[24:31], v0\n" \
  "s_waitcnt lgkmcnt(2)\n" \
  "v_smfmac_f32_16x16x64_bf16 v[20:23], v[44:47], v[32:39], v0\n" \
  "s_waitcnt lgkmcnt(0)\n" \
  "v_smfmac_f32_16x16x64_bf16 v[8:11], v[44:47], v[48:55], v0\n" \
  "s_nop 7\n" \
  "v_permlane32_swap_b32 v20, v9\n" \
  "v_permlane32_swap_b32 v21, v8\n" \
  "v_permlane32_swap_b32 v22, v11\n" \
  "v_permlane32_swap_b32 v23, v10\n" \
  "v_pk_add_f32 v[20:21], v[20:21], v[8:9] op_sel:[0,1] op_sel_hi:[1,0]\n" \
  "v_pk_add_f32 v[22:23], v[22:23], v[10:11] op_sel:[0,1] op_sel_hi:[1,0]\n" \
  "buffer_atomic_add_x2 v[20:21], v5, s[40:43], s58 offen\n" \
  "buffer_atomic_add_x2 v[22:23], v5, s[40:43], s59 offen\n" \
  "s_memrealtime s[38:39]\n" \
  "v_mbcnt_lo_u32_b32 v48, -1, 0\n" \
  "v_mbcnt_hi_u32_b32 v48, -1, v48\n" \
  "s_add_u32 s88, s74, 157760\n" \
  "v_mov_b32 v59, s88\n" \
  "v_lshl_add_u32 v52, v48, 2, v59\n" \
  "ds_read_b32 v52, v52 offset:192\n" \
  "v_lshrrev_b32 v49, 3, v48\n" \
  "v_and_b32 v50, 7, v48\n" \
  "v_lshlrev_b32 v50, 4, v50\n" \
  "v_lshl_or_b32 v44, v49, 8, v50\n" \
  "s_mov_b32 s87, 0x40000\n" \
  "v_lshrrev_b32 v49, 2, v48\n" \
  "v_lshrrev_b32 v50, 4, v48\n" \
  "v_bitop3_b32 v50, v49, v50, 3 bitop3:0x28\n" \
  "v_lshl_add_u32 v8, v50, 3, s97\n" \
  "v_and_b32 v50, 3, v48\n" \
  "v_lshl_add_u32 v8, v50, 5, v8\n" \
  "v_and_b32 v49, 1, v49\n" \
  "v_lshl_add_u32 v8, v49, 8, v8\n" \
  "v_and_b32 v49, 8, v48\n" \
  "v_lshl_add_u32 v8, v49, 4, v8\n" \
  "v_lshrrev_b32 v49, 5, v48\n" \
  "v_add_u32 v10, s99, v1\n" \
  "s_lshr_b32 s88, s99, 2\n" \
  "s_add_u32 s88, s88, s74\n" \
  "s_add_u32 s88, s88, 139264\n" \
  "v_lshl_add_u32 v7, v49, 10, s88\n" \
  "v_and_b32 v49, 31, v48\n" \
  "v_lshl_add_u32 v7, v49, 3, v7\n" \
  "v_mov_b64 v[12:13], 0\n" \
  "v_mov_b64 v[14:15], 0\n" \
  "v_mov_b64 v[16:17], 0\n" \
  "v_mov_b64 v[18:19], 0\n" \
  "v_mov_b64 v[20:21], 0\n" \
  "v_mov_b64 v[22:23], 0\n" \
  "v_mov_b32 v34, 0xc9b26ca0\n" \
  "v_mov_b32 v35, 0xc9b26c86\n" \
  "v_and_b32 v50, 3, v48\n" \
  "v_lshl_add_u32 v40, v50, 2, s98\n" \
  "v_lshrrev_b32 v50, 4, v48\n" \
  "v_mad_u32_u24 v40, v50, s95, v40\n" \
  "v_bfe_u32 v49, v48, 3, 1\n" \
  "v_lshl_add_u32 v40, v49, 6, v40\n" \
  "v_bfe_u32 v49, v48, 2, 1\n" \
  "v_lshl_add_u32 v40, v49, 7, v40\n" \
  "v_xor_b32 v41, 16, v40\n" \
  "v_xor_b32 v42, 32, v40\n" \
  "v_xor_b32 v43, 48, v40\n" \
  "s_add_i32 s80, s86, 0x35000000\n" \
  "v_mov_b32 v11, 0\n" \
  "s_mov_b32 s89, s60\n" \
  "s_bitcmp1_b32 s60, 7\n" \
  "s_cselect_b32 s88, 0x8000, 0\n" \
  "s_bitset1_b32 s88, 7\n" \
  "s_xor_b32 s56, s56, s88\n" \
  "s_xor_b32 s57, s57, s88\n" \
  "s_xor_b32 s58, s58, s88\n" \
  "s_xor_b32 s59, s59, s88\n" \
  "s_xor_b32 s60, s60, s88\n" \
  "s_sub_u32 s71, s71, 1\n" \
  "s_waitcnt lgkmcnt(0)\n" \
  "s_sub_u32 s39, s38, 150\n" \
  "v_subrev_u32 v52, s39, v52\n" \
  "v_cmp_lt_u32 vcc, 0x1000, v52\n" \
  "s_cmp_eq_u64 vcc, 0\n" \
  "s_cbranch_scc1 .LCA_OKM_%=\n" \
  ".LCA_AGN_%=:\n" \
  "buffer_load_dword v52, v5, s[0:3], s66 offen sc1\n" \
  "s_sub_u32 s87, s87, 1\n" \
  "s_cbranch_scc1 .LCA_END_%=\n" \
  "s_waitcnt vmcnt(0)\n" \
  "v_cmp_eq_u32 vcc, 0, v52\n" \
  "v_subrev_u32 v52, s39, v52\n" \
  "s_cmp_eq_u64 vcc, 0\n" \
  "s_cbranch_scc1 .LCA_OKM_%=\n" \
  "s_sleep 4\n" \
  "s_branch .LCA_AGN_%=\n" \
  ".LCA_OKM_%=:\n" \
  "s_nop 1\n" \
  "v_max_i32_dpp v52, v52, v52 row_ror:1 row_mask:0xf bank_mask:0xf\n" \
  "s_nop 1\n" \
  "v_max_i32_dpp v52, v52, v52 row_ror:2 row_mask:0xf bank_mask:0xf\n" \
  "s_nop 1\n" \
  "v_max_i32_dpp v52, v52, v52 row_ror:4 row_mask:0xf bank_mask:0xf\n" \
  "s_movk_i32 vcc_lo, 150\n" \
  "v_readlane_b32 s88, v52, 0\n" \
  "s_max_i32 vcc_lo, vcc_lo, s88\n" \
  "v_readlane_b32 s88, v52, 16\n" \
  "s_max_i32 vcc_lo, vcc_lo, s88\n" \
  "v_readlane_b32 s88, v52, 32\n" \
  "s_max_i32 vcc_lo, vcc_lo, s88\n" \
  "v_readlane_b32 s88, v52, 48\n" \
  "s_max_i32 vcc_lo, vcc_lo, s88\n" \
  "s_add_u32 s66, s66, 1024\n" \
  "s_add_u32 vcc_lo, vcc_lo, s39\n" \
  "s_add_u32 vcc_lo, vcc_lo, 26\n" \
  ".LCA_TW_%=:\n" \
  "s_memrealtime s[38:39]\n" \
  "s_waitcnt lgkmcnt(0)\n" \
  "s_sub_u32 s38, s38, vcc_lo\n" \
  "s_cmp_lt_i32 s38, 0\n" \
  "s_cbranch_scc1 .LCA_TW_%=\n" \
  "buffer_load_dwordx4 v[24:27], v44, s[40:43], s89 offen sc1\n" \
  "buffer_load_dwordx4 v[28:31], v44, s[40:43], s89 offen offset:2048 sc1\n" \
  ".LCA_SEE_%=:\n" \
  "s_waitcnt vmcnt(1)\n" \
  "v_cvt_f32_i32 v24, v24\n" \
  "v_cvt_f32_i32 v25, v25\n" \
  "v_cvt_f32_i32 v26, v26\n" \
  "v_cvt_f32_i32 v27, v27\n" \
  "v_max_f32 v45, |v24|, |v26|\n" \
  "v_pk_mul_f32 v[24:25], v[24:25], s[80:81] op_sel_hi:[1,0]\n" \
  "v_pk_mul_f32 v[26:27], v[26:27], s[80:81] op_sel_hi:[1,0]\n" \
  "s_nop 0\n" \
  "v_cvt_pk_bf16_f32 v32, v24, v26\n" \
  "v_cvt_pk_bf16_f32 v33, v25, v27\n" \
  "ds_write_b32 v40, v32\n" \
  "ds_write_b32 v41, v33\n" \
  "s_waitcnt vmcnt(0)\n" \
  "v_cvt_f32_i32 v28, v28\n" \
  "v_cvt_f32_i32 v30, v30\n" \
  "v_cvt_f32_i32 v29, v29\n" \
  "v_max3_f32 v45, v45, |v28|, |v30|\n" \
  "v_cvt_f32_i32 v31, v31\n" \
  "v_cmp_lt_f32 vcc, s91, v45\n" \
  "v_pk_mul_f32 v[28:29], v[28:29], s[80:81] op_sel_hi:[1,0]\n" \
  "v_pk_mul_f32 v[30:31], v[30:31], s[80:81] op_sel_hi:[1,0]\n" \
  "s_cmp_eq_u64 vcc, 0\n" \
  "s_cbranch_scc0 .LCA_POLL_%=\n" \
  "v_cvt_pk_bf16_f32 v36, v28, v30\n" \
  "v_cvt_pk_bf16_f32 v37, v29, v31\n" \
  "ds_write_b32 v42, v36\n" \
  "ds_write_b32 v43, v37\n" \
  "v_dot2c_f32_bf16 v11, v32, v32\n" \
  "v_dot2c_f32_bf16 v11, v33, v33\n" \
  "v_dot2c_f32_bf16 v11, v36, v36\n" \
  "v_dot2c_f32_bf16 v11, v37, v37\n" \
  "s_nop 1\n" \
  "s_nop 0\n" \
  "v_cmpx_lt_f32 vcc, 2.0, v11\n" \
  "ds_max_u32 v59, v11\n" \
  "s_mov_b64 exec, -1\n" \
  "s_branch .LCA_TOP_%=\n" \
  ".LCA_POLL_%=:\n" \
  "buffer_load_dwordx4 v[24:27], v44, s[40:43], s89 offen sc1\n" \
  "buffer_load_dwordx4 v[28:31], v44, s[40:43], s89 offen offset:2048 sc1\n" \
  "s_sub_u32 s87, s87, 1\n" \
  "s_cbranch_scc1 .LCA_END_%=\n" \
  "s_branch .LCA_SEE_%=\n" \
  ".LCA_LAST_%=:\n" \
  ".LCA_END_%=:\n" \
  "s_endpgm\n"
#define CHAIN_ASM_VO \
  "s_mov_b32 s95, 272\n" \
  "s_mov_b32 s91, 0x4c7c2000\n" \
  "s_mov_b32 s85, 0xc9b26c9b\n" \
  "s_mov_b32 s92, 0xc9326c9b\n" \
  "s_mov_b32 s93, 0xc9326c9b\n" \
  "s_mov_b32 s78, 0x3e0293ee\n" \
  "s_mov_b32 s76, 0x33333333\n" \
  "s_mov_b32 s77, 0x33333333\n" \
  "s_lshl_b32 s98, s73, 10\n" \
  "s_add_u32 s98, s98, s74\n" \
  "s_add_u32 s98, s98, 131072\n" \
  "s_lshl_b32 s97, s73, 9\n" \
  "s_add_u32 s97, s97, s74\n" \
  "s_add_u32 s97, s97, 158724\n" \
  "s_lshl_b32 s99, s73, 11\n" \
  "s_add_u32 s99, s99, s74\n" \
  "s_add_u32 s99, s99, 133120\n" \
  "s_lshl_b32 s94, s73, 4\n" \
  "s_add_u32 s94, s94, s74\n" \
  "s_add_u32 s94, s94, 157760\n" \
  "v_mbcnt_lo_u32_b32 v48, -1, 0\n" \
  "v_mbcnt_hi_u32_b32 v48, -1, v48\n" \
  "s_add_u32 s88, s74, 157760\n" \
  "v_mov_b32 v59, s88\n" \
  "s_sub_u32 s88, s73, 4\n" \
  "v_lshrrev_b32 v49, 2, v48\n" \
  "v_lshrrev_b32 v50, 4, v48\n" \
  "v_bitop3_b32 v50, v49, v50, 3 bitop3:0x28\n" \
  "v_lshl_add_u32 v4, v50, 3, s97\n" \
  "v_and_b32 v50, 3, v48\n" \
  "v_lshl_add_u32 v4, v50, 5, v4\n" \
  "v_and_b32 v49, 1, v49\n" \
  "v_lshl_add_u32 v4, v49, 8, v4\n" \
  "v_and_b32 v49, 8, v48\n" \
  "v_lshl_add_u32 v4, v49, 4, v4\n" \
  "v_and_b32 v51, 15, v48\n" \
  "v_bfe_u32 v49, v48, 4, 1\n" \
  "v_lshrrev_b32 v50, 5, v48\n" \
  "s_lshl_b32 s89, s88, 10\n" \
  "s_add_u32 s89, s89, s74\n" \
  "s_add_u32 s89, s89, 158720\n" \
  "v_lshrrev_b32 v9, 3, v51\n" \
  "v_mul_u32_u24 v6, 41, v9\n" \
  "v_xor_b32 v6, v6, v51\n" \
  "v_lshl_add_u32 v6, v6, 3, s89\n" \
  "v_lshl_add_u32 v6, v49, 6, v6\n" \
  "v_lshl_add_u32 v6, v50, 9, v6\n" \
  "v_xor_b32 v9, 0x90, v6\n" \
  "v_lshlrev_b32 v8, 4, v49\n" \
  "v_lshl_add_u32 v8, v51, 5, v8\n" \
  "v_lshl_add_u32 v8, v50, 3, v8\n" \
  "s_lshl_b32 s89, s88, 8\n" \
  "s_add_u32 s89, s89, s74\n" \
  "s_add_u32 s89, s89, 139264\n" \
  "v_and_b32 v49, 31, v48\n" \
  "v_lshl_add_u32 v5, v49, 3, s89\n" \
  "v_lshl_add_u32 v5, v50, 10, v5\n" \
  "v_lshrrev_b32 v49, 2, v51\n" \
  "v_and_b32 v50, 5, v51\n" \
  "v_and_or_b32 v49, v49, 2, v50\n" \
  "v_and_b32 v50, 48, v48\n" \
  "v_lshlrev_b32 v7, 3, v50\n" \
  "v_lshl_add_u32 v7, v49, 4, v7\n" \
  "v_bitop3_b32 v49, v51, 2, 2 bitop3:0x0c\n" \
  "v_lshl_add_u32 v7, v49, 29, v7\n" \
  "v_mov_b64 v[12:13], 0\n" \
  "v_mov_b64 v[14:15], 0\n" \
  "v_mov_b64 v[16:17], 0\n" \
  "v_mov_b64 v[18:19], 0\n" \
  "v_mov_b64 v[20:21], 0\n" \
  "v_mov_b64 v[22:23], 0\n" \
  "v_lshlrev_b32 v10, 4, v48\n" \
  "s_add_u32 s78, s74, 157952\n" \
  "v_lshl_add_u32 v52, v48, 2, v59\n" \
  "ds_write_b32 v52, v12 offset:192\n" \
  "s_movk_i32 s79, 105\n" \
  "v_mov_b32 v24, 0x44870000\n" \
  "v_mul_f32 v24, s72, v24\n" \
  "s_nop 0\n" \
  "v_readfirstlane_b32 s86, v24\n" \
  "s_lshr_b32 s86, s86, 23\n" \
  "s_addk_i32 s86, 0xff83\n" \
  "s_ashr_i32 s86, s86, 1\n" \
  "s_max_i32 s86, s86, -20\n" \
  "s_min_i32 s86, s86, 100\n" \
  "s_mov_b32 s82, s86\n" \
  "s_lshr_b32 s83, s72, 23\n" \
  "s_sub_i32 s83, s83, 242\n" \
  "s_lshl_b32 s88, s73, 10\n" \
  "v_add_u32 v24, s88, v1\n" \
  "ds_read_b64 v[32:33], v24 offset:0\n" \
  "ds_read_b64 v[36:37], v24 offset:8\n" \
  "s_waitcnt lgkmcnt(0)\n" \
  "v_mov_b32 v11, 0\n" \
  "v_dot2c_f32_bf16 v11, v32, v32\n" \
  "v_dot2c_f32_bf16 v11, v33, v33\n" \
  "v_dot2c_f32_bf16 v11, v36, v36\n" \
  "v_dot2c_f32_bf16 v11, v37, v37\n" \
  "s_nop 1\n" \
  "s_nop 0\n" \
  "v_cmpx_lt_f32 vcc, 2.0, v11\n" \
  "ds_max_u32 v59, v11\n" \
  "s_mov_b64 exec, -1\n" \
  "s_cmp_lg_u32 s61, 0\n" \
  "s_movk_i32 s76, -8000\n" \
  "s_cmovk_i32 s76, 9152\n" \
  "s_movk_i32 s77, 90\n" \
  "s_cmovk_i32 s77, 90\n" \
  "s_cselect_b32 s72, -1, 55\n" \
  ".LCV_TOP_%=:\n" \
  "s_waitcnt lgkmcnt(0)\n" \
  "s_barrier\n" \
  "s_setprio 3\n" \
  "ds_read_b128 v[24:27], v1 offset:0\n" \
  "ds_read_b128 v[28:31], v1 offset:1024\n" \
  "ds_read_b128 v[32:35], v1 offset:2048\n" \
  "ds_read_b128 v[36:39], v1 offset:3072\n" \
  "ds_read_b128 v[40:43], v1 offset:4096\n" \
  "ds_read_b128 v[44:47], v1 offset:5120\n" \
  "ds_read_b128 v[48:51], v1 offset:6144\n" \
  "ds_read_b128 v[52:55], v1 offset:7168\n" \
  "s_memrealtime s[38:39]\n" \
  "s_waitcnt lgkmcnt(7)\n" \
  "v_smfmac_f32_16x16x64_bf16 v[12:15], v[24:27], v[64:71], v0\n" \
  "s_waitcnt lgkmcnt(6)\n" \
  "v_smfmac_f32_16x16x64_bf16 v[12:15], v[28:31], v[72:79], v0\n" \
  "s_waitcnt lgkmcnt(5)\n" \
  "v_smfmac_f32_16x16x64_bf16 v[12:15], v[32:35], v[80:87], v0\n" \
  "s_waitcnt lgkmcnt(4)\n" \
  "v_smfmac_f32_16x16x64_bf16 v[12:15], v[36:39], v[88:95], v0\n" \
  "s_waitcnt lgkmcnt(3)\n" \
  "v_smfmac_f32_16x16x64_bf16 v[12:15], v[40:43], v[96:103], v0\n" \
  "s_waitcnt lgkmcnt(2)\n" \
  "v_smfmac_f32_16x16x64_bf16 v[12:15], v[44:47], v[104:111], v0\n" \
  "s_waitcnt lgkmcnt(1)\n" \
  "v_smfmac_f32_16x16x64_bf16 v[12:15], v[48:51], v[112:119], v0\n" \
  "s_waitcnt lgkmcnt(0)\n" \
  "v_smfmac_f32_16x16x64_bf16 v[12:15], v[52:55], v[120:127], v0\n" \
  "ds_read_b32 v11, v1 offset:26688\n" \
  "s_mov_b32 s61, s38\n" \
  "s_add_u32 s38, s38, s79\n" \
  "v_or_b32 v57, 1, s38\n" \
  "v_add_u32 v56, s67, v10\n" \
  "s_add_u32 s39, s67, s76\n" \
  "s_add_u32 s38, s38, s77\n" \
  "s_bitset1_b32 s38, 0\n" \
  "v_writelane_b32 v56, s39, 1\n" \
  "v_writelane_b32 v57, s38, 1\n" \
  "s_sub_u32 s39, s71, s72\n" \
  "s_cmp_lt_u32 s39, 9\n" \
  "s_cselect_b64 exec, 19, 3\n" \
  "buffer_store_dword v57, v56, s[0:3], 0 offen sc1\n" \
  "s_mov_b64 exec, -1\n" \
  "v_permlane32_swap_b32 v12, v14\n" \
  "v_permlane32_swap_b32 v13, v15\n" \
  "v_pk_add_f32 v[12:13], v[12:13], v[14:15]\n" \
  "s_nop 0\n" \
  "v_cvt_pk_bf16_f32 v12, v12, v13\n" \
  "ds_write_b32 v4, v12\n" \
  "s_setprio 2\n" \
  "v_mov_b64 v[12:13], s[92:93]\n" \
  "v_mov_b64 v[14:15], s[92:93]\n" \
  "s_waitcnt lgkmcnt(1)\n" \
  "v_readfirstlane_b32 s86, v11\n" \
  "s_lshr_b32 s86, s86, 23\n" \
  "s_add_i32 s86, s86, s83\n" \
  "s_ashr_i32 s86, s86, 1\n" \
  "s_max_i32 s86, s86, s82\n" \
  "s_min_i32 s86, s86, 100\n" \
  "s_lshl_b32 s86, s86, 23\n" \
  "s_sub_i32 s84, 0xc8800000, s86\n" \
  "s_cmp_eq_u32 s73, 5\n" \
  "v_smfmac_f32_16x16x64_bf16 v[16:19], v[24:27], v[128:135], v0\n" \
  "v_smfmac_f32_16x16x64_bf16 v[16:19], v[28:31], v[136:143], v0\n" \
  "v_smfmac_f32_16x16x64_bf16 v[16:19], v[32:35], v[144:151], v0\n" \
  "v_smfmac_f32_16x16x64_bf16 v[16:19], v[36:39], v[152:159], v0\n" \
  "v_smfmac_f32_16x16x64_bf16 v[16:19], v[40:43], v[160:167], v0\n" \
  "v_smfmac_f32_16x16x64_bf16 v[16:19], v[44:47], v[168:175], v0\n" \
  "s_waitcnt lgkmcnt(0)\n" \
  "s_barrier\n" \
  "ds_read_b64_tr_b16 v[60:61], v6 offset:0\n" \
  "ds_read_b64_tr_b16 v[62:63], v9 offset:0\n" \
  "v_smfmac_f32_16x16x64_bf16 v[16:19], v[48:51], v[176:183], v0\n" \
  "v_smfmac_f32_16x16x64_bf16 v[16:19], v[52:55], v[184:191], v0\n" \
  "v_smfmac_f32_16x16x64_bf16 v[20:23], v[24:27], v[192:199], v0\n" \
  "v_smfmac_f32_16x16x64_bf16 v[20:23], v[28:31], v[200:207], v0\n" \
  "v_smfmac_f32_16x16x64_bf16 v[20:23], v[32:35], v[208:215], v0\n" \
  "v_smfmac_f32_16x16x64_bf16 v[20:23], v[36:39], v[216:223], v0\n" \
  "v_smfmac_f32_16x16x64_bf16 v[20:23], v[40:43], v[224:231], v0\n" \
  "v_smfmac_f32_16x16x64_bf16 v[20:23], v[44:47], v[232:239], v0\n" \
  "v_smfmac_f32_16x16x64_bf16 v[20:23], v[48:51], v[240:247], v0\n" \
  "v_smfmac_f32_16x16x64_bf16 v[20:23], v[52:55], v[248:255], v0\n" \
  "s_cmp_eq_u32 s71, 0\n" \
  "v_permlane32_swap_b32 v16, v18\n" \
  "v_permlane32_swap_b32 v17, v19\n" \
  "v_add_f32 v16, v16, v18\n" \
  "v_add_f32 v17, v17, v19\n" \
  "v_cvt_pk_bf16_f32 v16, v16, v17\n" \
  "s_waitcnt lgkmcnt(0)\n" \
  "v_max_u32 v60, 1, v60\n" \
  "v_max_u32 v61, 1, v61\n" \
  "v_max_u32 v62, 1, v62\n" \
  "v_max_u32 v63, 1, v63\n" \
  "buffer_store_dwordx4 v[60:63], v7, s[48:51], s69 offen sc1\n" \
  "v_permlane32_swap_b32 v20, v22\n" \
  "v_permlane32_swap_b32 v21, v23\n" \
  "v_pk_add_f32 v[20:21], v[20:21], v[22:23]\n" \
  "v_max_u32 v18, 1, v16\n" \
  "v_cvt_pk_bf16_f32 v17, v20, v21\n" \
  "ds_write_b64 v5, v[16:17]\n" \
  "v_max_u32 v19, 1, v17\n" \
  "s_nop 1\n" \
  "v_permlane32_swap_b32 v18, v19\n" \
  "buffer_store_dwordx2 v[18:19], v8, s[52:55], s70 offen sc1\n" \
  "ds_read_b128 v[24:27], v3 offset:0\n" \
  "ds_read_b128 v[28:31], v3 offset:1024\n" \
  "ds_read_b128 v[32:35], v3 offset:2048\n" \
  "ds_read_b128 v[36:39], v3 offset:3072\n" \
  "ds_read_b128 v[40:43], v3 offset:8192\n" \
  "ds_read_b128 v[44:47], v3 offset:9216\n" \
  "ds_read_b128 v[48:51], v3 offset:10240\n" \
  "ds_read_b128 v[52:55], v3 offset:11264\n" \
  "s_cbranch_scc1 .LCV_LAST_%=\n" \
  "s_cmp_eq_u32 s73, 5\n" \
  "s_cbranch_scc0 .LCV_NMK_%=\n" \
  "s_mov_b32 m0, s78\n" \
  "s_nop 0\n" \
  "buffer_load_dword v2, s[0:3], s66 offen sc1 lds\n" \
  ".LCV_NMK_%=:\n" \
  "s_setprio 0\n" \
  "s_waitcnt lgkmcnt(8)\n" \
  "s_barrier\n" \
  "s_add_u32 s67, s67, 1024\n" \
  "v_mov_b32 v11, 0\n" \
  "ds_write_b32 v59, v11\n" \
  "ds_read_b128 v[56:59], v3 offset:4096\n" \
  "ds_read_b128 v[60:63], v3 offset:5120\n" \
  "v_mov_b64 v[20:21], s[92:93]\n" \
  "v_mov_b64 v[22:23], s[92:93]\n" \
  "v_mov_b64 v[16:17], s[92:93]\n" \
  "v_mov_b64 v[18:19], s[92:93]\n" \
  "s_mov_b32 m0, s94\n" \
  "s_mov_b64 exec, 15\n" \
  "buffer_load_dword v2, s[44:47], s68 offen sc1 lds\n" \
  "s_mov_b64 exec, -1\n" \
  "s_setprio 2\n" \
  "s_waitcnt lgkmcnt(0)\n" \
  "s_barrier\n" \
  "ds_read_b128 v[4:7], v1 offset:8192\n" \
  "ds_read_b128 v[8:11], v1 offset:9216\n" \
  "s_waitcnt lgkmcnt(1)\n" \
  "v_smfmac_f32_16x16x64_bf16 v[12:15], v[4:7], v[24:31], v0\n" \
  "ds_read_b128 v[24:27], v3 offset:6144\n" \
  "ds_read_b128 v[28:31], v3 offset:7168\n" \
  "v_smfmac_f32_16x16x64_bf16 v[16:19], v[4:7], v[32:39], v0\n" \
  "ds_read_b128 v[32:35], v3 offset:12288\n" \
  "ds_read_b128 v[36:39], v3 offset:13312\n" \
  "s_waitcnt lgkmcnt(4)\n" \
  "v_smfmac_f32_16x16x64_bf16 v[12:15], v[8:11], v[40:47], v0\n" \
  "v_smfmac_f32_16x16x64_bf16 v[16:19], v[8:11], v[48:55], v0\n" \
  "v_smfmac_f32_16x16x64_bf16 v[20:23], v[4:7], v[56:63], v0\n" \
  "ds_read_b128 v[56:59], v3 offset:14336\n" \
  "ds_read_b128 v[60:63], v3 offset:15360\n" \
  "v_mov_b64 v[40:41], s[92:93]\n" \
  "v_mov_b64 v[42:43], s[92:93]\n" \
  "s_nop 2\n" \
  "v_permlane32_swap_b32 v12, v17\n" \
  "v_permlane32_swap_b32 v13, v16\n" \
  "v_permlane32_swap_b32 v14, v19\n" \
  "v_permlane32_swap_b32 v15, v18\n" \
  "v_pk_add_f32 v[12:13], v[12:13], v[16:17] op_sel:[0,1] op_sel_hi:[1,0]\n" \
  "v_pk_add_f32 v[14:15], v[14:15], v[18:19] op_sel:[0,1] op_sel_hi:[1,0]\n" \
  "buffer_atomic_add_x2 v[12:13], v2, s[40:43], s56 offen\n" \
  "buffer_atomic_add_x2 v[14:15], v2, s[40:43], s57 offen\n" \
  "s_waitcnt lgkmcnt(2)\n" \
  "v_smfmac_f32_16x16x64_bf16 v[20:23], v[8:11], v[32:39], v0\n" \
  "v_smfmac_f32_16x16x64_bf16 v[40:43], v[4:7], v[24:31], v0\n" \
  "s_waitcnt lgkmcnt(0)\n" \
  "v_smfmac_f32_16x16x64_bf16 v[40:43], v[8:11], v[56:63], v0\n" \
  "s_nop 7\n" \
  "v_permlane32_swap_b32 v20, v41\n" \
  "v_permlane32_swap_b32 v21, v40\n" \
  "v_permlane32_swap_b32 v22, v43\n" \
  "v_permlane32_swap_b32 v23, v42\n" \
  "v_pk_add_f32 v[20:21], v[20:21], v[40:41] op_sel:[0,1] op_sel_hi:[1,0]\n" \
  "v_pk_add_f32 v[22:23], v[22:23], v[42:43] op_sel:[0,1] op_sel_hi:[1,0]\n" \
  "buffer_atomic_add_x2 v[20:21], v2, s[40:43], s58 offen\n" \
  "buffer_atomic_add_x2 v[22:23], v2, s[40:43], s59 offen\n" \
  "s_setprio 0\n" \
  "s_memrealtime s[38:39]\n" \
  "v_mbcnt_lo_u32_b32 v48, -1, 0\n" \
  "v_mbcnt_hi_u32_b32 v48, -1, v48\n" \
  "s_add_u32 s88, s74, 157760\n" \
  "v_mov_b32 v59, s88\n" \
  "v_lshl_add_u32 v52, v48, 2, v59\n" \
  "ds_read_b32 v52, v52 offset:192\n" \
  "v_lshrrev_b32 v49, 3, v48\n" \
  "v_and_b32 v50, 7, v48\n" \
  "v_lshlrev_b32 v50, 4, v50\n" \
  "v_lshl_or_b32 v44, v49, 8, v50\n" \
  "s_mov_b32 s87, 0x40000\n" \
  "v_lshlrev_b32 v10, 4, v48\n" \
  "v_lshl_add_u32 v58, v48, 2, s94\n" \
  "s_waitcnt vmcnt(4)\n" \
  "ds_read_b32 v58, v58\n" \
  "s_waitcnt lgkmcnt(0)\n" \
  "v_cmp_eq_u32 vcc, 0, v58\n" \
  "s_and_b32 vcc_lo, vcc_lo, 15\n" \
  "s_cbranch_scc0 .LCV_GOT_%=\n" \
  "s_branch .LCV_ASK_%=\n" \
  ".LCV_LATE_%=:\n" \
  "v_cmp_eq_u32 vcc, 0, v58\n" \
  "s_and_b32 vcc_lo, vcc_lo, 15\n" \
  "s_cbranch_scc0 .LCV_GOT_%=\n" \
  "s_sleep 12\n" \
  ".LCV_ASK_%=:\n" \
  "s_mov_b64 exec, 15\n" \
  "buffer_load_dword v58, v2, s[44:47], s68 offen sc1\n" \
  "s_mov_b64 exec, -1\n" \
  "s_sub_u32 s87, s87, 1\n" \
  "s_cbranch_scc1 .LCV_END_%=\n" \
  "s_waitcnt vmcnt(0)\n" \
  "s_branch .LCV_LATE_%=\n" \
  ".LCV_GOT_%=:\n" \
  "s_mov_b32 m0, s99\n" \
  "s_add_u32 s88, s99, 8192\n" \
  "global_load_lds_dwordx4 v10, s[62:63] sc1\n" \
  "global_load_lds_dwordx4 v10, s[62:63] offset:1024 sc1\n" \
  "s_mov_b32 m0, s88\n" \
  "s_nop 0\n" \
  "global_load_lds_dwordx4 v10, s[64:65] sc1\n" \
  "global_load_lds_dwordx4 v10, s[64:65] offset:1024 sc1\n" \
  ".LCV_HAVE_%=:\n" \
  "s_waitcnt lgkmcnt(0)\n" \
  "s_sub_u32 s61, s38, s61\n" \
  "s_add_u32 s79, s79, 2\n" \
  "s_min_u32 s79, s79, s61\n" \
  "s_sub_u32 s39, s38, 150\n" \
  "v_subrev_u32 v52, s39, v52\n" \
  "v_cmp_lt_u32 vcc, 0x1000, v52\n" \
  "s_cmp_eq_u64 vcc, 0\n" \
  "s_cbranch_scc1 .LCV_OKM_%=\n" \
  ".LCV_AGN_%=:\n" \
  "buffer_load_dword v52, v2, s[0:3], s66 offen sc1\n" \
  "s_sub_u32 s87, s87, 1\n" \
  "s_cbranch_scc1 .LCV_END_%=\n" \
  "s_waitcnt vmcnt(0)\n" \
  "v_cmp_eq_u32 vcc, 0, v52\n" \
  "v_subrev_u32 v52, s39, v52\n" \
  "s_cmp_eq_u64 vcc, 0\n" \
  "s_cbranch_scc1 .LCV_OKM_%=\n" \
  "s_sleep 4\n" \
  "s_branch .LCV_AGN_%=\n" \
  ".LCV_OKM_%=:\n" \
  "s_nop 1\n" \
  "v_max_i32_dpp v52, v52, v52 row_ror:1 row_mask:0xf bank_mask:0xf\n" \
  "s_nop 1\n" \
  "v_max_i32_dpp v52, v52, v52 row_ror:2 row_mask:0xf bank_mask:0xf\n" \
  "s_nop 1\n" \
  "v_max_i32_dpp v52, v52, v52 row_ror:4 row_mask:0xf bank_mask:0xf\n" \
  "s_movk_i32 vcc_lo, 150\n" \
  "v_readlane_b32 s88, v52, 0\n" \
  "s_max_i32 vcc_lo, vcc_lo, s88\n" \
  "v_readlane_b32 s88, v52, 16\n" \
  "s_max_i32 vcc_lo, vcc_lo, s88\n" \
  "v_readlane_b32 s88, v52, 32\n" \
  "s_max_i32 vcc_lo, vcc_lo, s88\n" \
  "v_readlane_b32 s88, v52, 48\n" \
  "s_max_i32 vcc_lo, vcc_lo, s88\n" \
  "s_add_u32 s66, s66, 1024\n" \
  "s_add_u32 vcc_lo, vcc_lo, s39\n" \
  "s_add_u32 vcc_lo, vcc_lo, 26\n" \
  ".LCV_TW_%=:\n" \
  "s_memrealtime s[38:39]\n" \
  "s_waitcnt lgkmcnt(0)\n" \
  "s_sub_u32 s38, s38, vcc_lo\n" \
  "s_cmp_lt_i32 s38, 0\n" \
  "s_cbranch_scc1 .LCV_TW_%=\n" \
  "buffer_load_dwordx4 v[24:27], v44, s[40:43], s60 offen sc1\n" \
  "buffer_load_dwordx4 v[28:31], v44, s[40:43], s60 offen offset:2048 sc1\n" \
  "s_add_u32 s62, s62, 0x80000\n" \
  "s_addc_u32 s63, s63, 0\n" \
  "s_add_u32 s64, s64, 0x40000\n" \
  "s_addc_u32 s65, s65, 0\n" \
  "s_add_u32 s68, s68, 0x1000\n" \
  "s_add_u32 s69, s69, 0x10000\n" \
  "s_add_u32 s70, s70, 0x10000\n" \
  "s_sub_u32 s88, s73, 4\n" \
  "v_lshrrev_b32 v49, 2, v48\n" \
  "v_lshrrev_b32 v50, 4, v48\n" \
  "v_bitop3_b32 v50, v49, v50, 3 bitop3:0x28\n" \
  "v_lshl_add_u32 v4, v50, 3, s97\n" \
  "v_and_b32 v50, 3, v48\n" \
  "v_lshl_add_u32 v4, v50, 5, v4\n" \
  "v_and_b32 v49, 1, v49\n" \
  "v_lshl_add_u32 v4, v49, 8, v4\n" \
  "v_and_b32 v49, 8, v48\n" \
  "v_lshl_add_u32 v4, v49, 4, v4\n" \
  "v_and_b32 v51, 15, v48\n" \
  "v_bfe_u32 v49, v48, 4, 1\n" \
  "v_lshrrev_b32 v50, 5, v48\n" \
  "s_lshl_b32 s89, s88, 10\n" \
  "s_add_u32 s89, s89, s74\n" \
  "s_add_u32 s89, s89, 158720\n" \
  "v_lshrrev_b32 v9, 3, v51\n" \
  "v_mul_u32_u24 v6, 41, v9\n" \
  "v_xor_b32 v6, v6, v51\n" \
  "v_lshl_add_u32 v6, v6, 3, s89\n" \
  "v_lshl_add_u32 v6, v49, 6, v6\n" \
  "v_lshl_add_u32 v6, v50, 9, v6\n" \
  "v_xor_b32 v9, 0x90, v6\n" \
  "v_lshlrev_b32 v8, 4, v49\n" \
  "v_lshl_add_u32 v8, v51, 5, v8\n" \
  "v_lshl_add_u32 v8, v50, 3, v8\n" \
  "s_lshl_b32 s89, s88, 8\n" \
  "s_add_u32 s89, s89, s74\n" \
  "s_add_u32 s89, s89, 139264\n" \
  "v_and_b32 v49, 31, v48\n" \
  "v_lshl_add_u32 v5, v49, 3, s89\n" \
  "v_lshl_add_u32 v5, v50, 10, v5\n" \
  "v_lshrrev_b32 v49, 2, v51\n" \
  "v_and_b32 v50, 5, v51\n" \
  "v_and_or_b32 v49, v49, 2, v50\n" \
  "v_and_b32 v50, 48, v48\n" \
  "v_lshlrev_b32 v7, 3, v50\n" \
  "v_lshl_add_u32 v7, v49, 4, v7\n" \
  "v_bitop3_b32 v49, v51, 2, 2 bitop3:0x0c\n" \
  "v_lshl_add_u32 v7, v49, 29, v7\n" \
  "v_mov_b64 v[12:13], 0\n" \
  "v_mov_b64 v[14:15], 0\n" \
  "v_mov_b64 v[16:17], 0\n" \
  "v_mov_b64 v[18:19], 0\n" \
  "v_mov_b64 v[20:21], 0\n" \
  "v_mov_b64 v[22:23], 0\n" \
  "v_and_b32 v50, 3, v48\n" \
  "v_lshl_add_u32 v40, v50, 2, s98\n" \
  "v_lshrrev_b32 v50, 4, v48\n" \
  "v_mad_u32_u24 v40, v50, s95, v40\n" \
  "v_bfe_u32 v49, v48, 3, 1\n" \
  "v_lshl_add_u32 v40, v49, 6, v40\n" \
  "v_bfe_u32 v49, v48, 2, 1\n" \
  "v_lshl_add_u32 v40, v49, 7, v40\n" \
  "v_xor_b32 v41, 16, v40\n" \
  "v_xor_b32 v42, 32, v40\n" \
  "v_xor_b32 v43, 48, v40\n" \
  "s_add_i32 s80, s86, 0x35000000\n" \
  "v_mov_b32 v11, 0\n" \
  "s_mov_b32 s89, s60\n" \
  "s_bitcmp1_b32 s60, 7\n" \
  "s_cselect_b32 s88, 0x8000, 0\n" \
  "s_bitset1_b32 s88, 7\n" \
  "s_xor_b32 s56, s56, s88\n" \
  "s_xor_b32 s57, s57, s88\n" \
  "s_xor_b32 s58, s58, s88\n" \
  "s_xor_b32 s59, s59, s88\n" \
  "s_xor_b32 s60, s60, s88\n" \
  "s_sub_u32 s71, s71, 1\n" \
  ".LCV_SEE_%=:\n" \
  "s_waitcnt vmcnt(1)\n" \
  "v_cvt_f32_i32 v24, v24\n" \
  "v_cvt_f32_i32 v25, v25\n" \
  "v_cvt_f32_i32 v26, v26\n" \
  "v_cvt_f32_i32 v27, v27\n" \
  "v_max_f32 v45, |v24|, |v26|\n" \
  "v_pk_mul_f32 v[24:25], v[24:25], s[80:81] op_sel_hi:[1,0]\n" \
  "v_pk_mul_f32 v[26:27], v[26:27], s[80:81] op_sel_hi:[1,0]\n" \
  "s_nop 0\n" \
  "v_cvt_pk_bf16_f32 v32, v24, v26\n" \
  "v_cvt_pk_bf16_f32 v33, v25, v27\n" \
  "ds_write_b32 v40, v32\n" \
  "ds_write_b32 v41, v33\n" \
  "s_waitcnt vmcnt(0)\n" \
  "v_cvt_f32_i32 v28, v28\n" \
  "v_cvt_f32_i32 v30, v30\n" \
  "v_cvt_f32_i32 v29, v29\n" \
  "v_max3_f32 v45, v45, |v28|, |v30|\n" \
  "v_cvt_f32_i32 v31, v31\n" \
  "v_cmp_lt_f32 vcc, s91, v45\n" \
  "v_pk_mul_f32 v[28:29], v[28:29], s[80:81] op_sel_hi:[1,0]\n" \
  "v_pk_mul_f32 v[30:31], v[30:31], s[80:81] op_sel_hi:[1,0]\n" \
  "s_cmp_eq_u64 vcc, 0\n" \
  "s_cbranch_scc0 .LCV_POLL_%=\n" \
  "v_cvt_pk_bf16_f32 v36, v28, v30\n" \
  "v_cvt_pk_bf16_f32 v37, v29, v31\n" \
  "ds_write_b32 v42, v36\n" \
  "ds_write_b32 v43, v37\n" \
  "v_dot2c_f32_bf16 v11, v32, v32\n" \
  "v_dot2c_f32_bf16 v11, v33, v33\n" \
  "v_dot2c_f32_bf16 v11, v36, v36\n" \
  "v_dot2c_f32_bf16 v11, v37, v37\n" \
  "s_nop 1\n" \
  "s_nop 0\n" \
  "v_cmpx_lt_f32 vcc, 2.0, v11\n" \
  "ds_max_u32 v59, v11\n" \
  "s_mov_b64 exec, -1\n" \
  "s_branch .LCV_TOP_%=\n" \
  ".LCV_POLL_%=:\n" \
  "buffer_load_dwordx4 v[24:27], v44, s[40:43], s89 offen sc1\n" \
  "buffer_load_dwordx4 v[28:31], v44, s[40:43], s89 offen offset:2048 sc1\n" \
  "s_sub_u32 s87, s87, 1\n" \
  "s_cbranch_scc1 .LCV_END_%=\n" \
  "s_branch .LCV_SEE_%=\n" \
  ".LCV_LAST_%=:\n" \
  "s_waitcnt vmcnt(0)\n" \
  ".LCV_END_%=:\n" \
  "s_endpgm\n"
__device__ __forceinline__ void start_kgen(const MegaArgs& A, char* smem, const int kg) {
  const int w = A.wv, l = lane_id(), tid = 64 * w + l, c = l & 15, g = l >> 4;
  KGen G;
  G.load(A, 8 * kg + w, l);
  {
    const __amdgpu_buffer_rsrc_t rz = MKRS(A.ws + OFF_Z, Z_BYTES);
#pragma unroll
    for (int i = 0; i < 4; ++i) {
      const u64 z = i == 0 && kg < (int)(2 * SZ_ACC / 8192) ? ACC_INIT : 0ull;
      ST128((v4u{(unsigned)z, (unsigned)(z >> 32), (unsigned)z, (unsigned)(z >> 32)}), rz, tid * 16, (i * 64 + kg) * 8192);
    }
  }
  const u64 tag = call_tag(A);
  DRAIN();
  __syncthreads();
  if (tid < 8) __hip_atomic_store((gu64*)(A.ws + OFF_RD) + tid * 64 + kg, tag, RLX_AGENT);
  asm volatile("" : "+v"(G.pa), "+v"(G.pb), "+v"(G.pe));
  G.setup();
  G.st = smem + w * 16640;
  const __amdgpu_buffer_rsrc_t rs = MKRS(A.ws + OFF_KF + (8 * kg + w) * 2048, 386 * 4);
  SST(1);
  unsigned tw = 0u;
  if (TLBW && tid < 384) {
    const unsigned o = (unsigned)OFF_XA + (unsigned)(tid * 8 + ((blockIdx.x >> 3) & 7)) * 65536u;
    if (o < WS_TOTAL) asm volatile("global_load_dword %0, %1, %2" : "=v"(tw) : "v"(o), "s"(A.ws) : "memory");
  }
  G.columns(l);
  const Cx zb = G.power(256.f);
  Cx b = G.C, z = G.power(2.f);
#pragma unroll 1
  for (int f = 1; f < 9;) {
    char* p = G.st + l * 8;
    const int fe = f == 1 ? 4 : 9;
#pragma unroll 1
    for (; f < fe; ++f) {
      b = cmul(b, zb);
      if (f == 2 || f == 4) z = cmul(z, z);
      p = G.rows(p, b, z, f == 1 ? 8 : f < 4 ? 4 : 2);
    }
    const v4u r = G.tile(l);
    if (f == 4) {
      ST128(r, rs, (g < 2 ? 8 * c + 4 * g : 64 * g + 4 * c) * 4, 0);
    } else {
      ST64((v2u{r.x, r.y}), rs, (256 + 64 * g + 2 * c) * 4, 0);
      ST64((v2u{r.z, r.w}), rs, (288 + 64 * g + 2 * c) * 4, 0);
    }
  }
  SST(2);
  DRAIN();
  asm volatile("" ::"v"(tw));
  if (l == 0) __hip_atomic_store((gu64*)(A.ws + OFF_KG) + kg * 16 + w, tag, RLX_AGENT);
  __syncthreads();
  SST(3);
}

constexpr int C_SM = 131072, C_SO = C_SM + 8192, C_KT = C_SO + 2048, C_X = C_KT + 16384, C_SQ = C_X + 1024;
static_assert(C_SQ + 4096 <= 163840 && C_SQ % 512 == 0, "lds");
__device__ __forceinline__ v4u mk_srd(const void* p, const unsigned bytes) {
  const unsigned long long a = (unsigned long long)p;
  return v4u{(unsigned)a, (unsigned)(a >> 32) & 0xffffu, bytes, 0x00020000u};
}
template <int R>
__device__ __forceinline__ void pin8(v8u& x) {
#define PIN_(a, b) if constexpr (R == a) asm volatile("" : "+{v[" #a ":" #b "]}"(x));
  PIN_(64, 71)  PIN_(72, 79)  PIN_(80, 87)  PIN_(88, 95)  PIN_(96, 103)  PIN_(104, 111)  PIN_(112, 119)  PIN_(120, 127)
  PIN_(128, 135)  PIN_(136, 143)  PIN_(144, 151)  PIN_(152, 159)  PIN_(160, 167)  PIN_(168, 175)  PIN_(176, 183)  PIN_(184, 191)
  PIN_(192, 199)  PIN_(200, 207)  PIN_(208, 215)  PIN_(216, 223)  PIN_(224, 231)  PIN_(232, 239)  PIN_(240, 247)  PIN_(248, 255)
#undef PIN_
}
template <int R>
__device__ __forceinline__ void pin4(v4u& x) {
#define PIN_(a, b) if constexpr (R == a) asm volatile("" : "+{v[" #a ":" #b "]}"(x));
  PIN_(32, 35)  PIN_(36, 39)  PIN_(40, 43)  PIN_(44, 47)  PIN_(48, 51)  PIN_(52, 55)  PIN_(56, 59)  PIN_(60, 63)
  PIN_(96, 99)  PIN_(100, 103)  PIN_(104, 107)  PIN_(108, 111)  PIN_(112, 115)  PIN_(116, 119)  PIN_(120, 123)  PIN_(124, 127)
  PIN_(160, 163)  PIN_(164, 167)  PIN_(168, 171)  PIN_(172, 175)  PIN_(176, 179)  PIN_(180, 183)  PIN_(184, 187)  PIN_(188, 191)
#undef PIN_
}
template <bool ATT>
__device__ __forceinline__ void role_chain_t(const MegaArgs& A, char* smem, const int L, const int h, const int rep) {
  u16* swo = reinterpret_cast<u16*>(smem);
  const int w = A.wv;
  char* ws = A.ws;
  unsigned* FL = (unsigned*)(ws + OFF_FL);
  unsigned* TMO = FL + FL_TMO;
#define LANE_IDS()                         \
  const int l = lane_id();                 \
  const int c = l & 15, g = l >> 4;        \
  (void)c; (void)g;
  float* sgf = reinterpret_cast<float*>(smem + C_X);
  unsigned* FQ = FL + FL_FQ + L * 64 * 32 * 32;
  const int wb = (w + ROT_H) & 7, tb = ROT_T;
  v8u w0[8], w1[8], w2[8];
  u64 tag;
  {
    LANE_IDS();
    const unsigned sb = lds_base(smem);
    const int q = (l >> 2) & 3, p = l & 3;
    const int f = (q & 1) | ((g & 1) << 1) | ((q >> 1) << 2);
    const unsigned ra = sb + (16 * (g >> 1) + 4 * (g & 1) + (q & 1) + 8 * (q >> 1)) * 256 + (w ^ f) * 32 + p * 8;
#define W_READ(N, hf, X)                                                       \
  {                                                                           \
    _Pragma("unroll") for (int t = 0; t < 4; ++t) {                           \
      const unsigned a_ = (ra ^ (X)) + (hf) * 8192 + t * 32768;               \
      const v2u r0 = trd(a_), r1 = trd(a_ + 512), r2 = trd(a_ + 16384), r3 = trd(a_ + 16384 + 512); \
      w##N[4 * (hf) + t] = v8u{r0.x, r0.y, r1.x, r1.y, r2.x, r2.y, r3.x, r3.y}; \
    }                                                                         \
    pin8<64 * N + 64 + 32 * hf>(w##N[4 * hf]);                                \
    pin8<64 * N + 72 + 32 * hf>(w##N[4 * hf + 1]);                            \
    pin8<64 * N + 80 + 32 * hf>(w##N[4 * hf + 2]);                            \
    pin8<64 * N + 88 + 32 * hf>(w##N[4 * hf + 3]);                            \
  }
#define W_STAGE(M, hf, WAIT, NEXT)                                             \
  {                                                                           \
    WAIT;                                                                     \
    RAW_BAR();                                                                \
    if ((M) == 0 && ATT) { W_READ(0, hf, 0); W_READ(2, hf, 128); }            \
    if ((M) == 1 && ATT) { W_READ(1, hf, 0); }                                \
    if ((M) == 1 && !ATT) { W_READ(0, hf, 0); }                               \
    if ((M) == 2 && !ATT) { W_READ(1, hf, 128); W_READ(2, hf, 0); }           \
    if ((M) == 3) {                                                           \
      _Pragma("unroll") for (int i = 0; i < 8; ++i) {                         \
        const unsigned a_ = rb + (hf) * 8192 + (i & 1) * 65536 + ((4 * wb + (i >> 1)) ^ f ^ tb) * 32; \
        const v2u r0 = trd(a_), r1 = trd(a_ + 2048);                          \
        fr[hf][i] = v4u{r0.x, r0.y, r1.x, r1.y};                              \
      }                                                                       \
      constexpr int R_ = hf ? 32 : ATT ? 160 : 96;                            \
      pin4<R_>(fr[hf][0]); pin4<R_ + 4>(fr[hf][1]); pin4<R_ + 8>(fr[hf][2]); pin4<R_ + 12>(fr[hf][3]); \
      pin4<R_ + 16>(fr[hf][4]); pin4<R_ + 20>(fr[hf][5]); pin4<R_ + 24>(fr[hf][6]); pin4<R_ + 28>(fr[hf][7]); \
    }                                                                         \
    RAW_BAR();                                                                \
    NEXT;                                                                     \
  }
#define WO_ISSUE(hf)                                                          \
  {                                                                           \
    const char* base_ = (const char*)A.Wo + (size_t)(h * DH + 64 * (hf) + 8 * w) * (DM * 2); \
    _Pragma("unroll") for (int i = 0; i < 8; ++i) {                           \
      const int fd = (i & 1) | (((i >> 2) & 1) << 1) | ((w & 1) << 2);        \
      glds16(base_, (unsigned)(i * 1024 + ((l ^ (fd << 1)) << 4)), sb + w * 16384 + (hf) * 8192 + i * 1024); \
    }                                                                         \
  }
    const unsigned rb = sb + (2 * (g >> 1) + (q >> 1)) * 16384 + (4 * (g & 1) + (q & 1)) * 1024 + p * 8;
    v4u fr[2][8];
#define WO_PUT(hf) \
  _Pragma("unroll") for (int i = 0; i < 8; ++i) *reinterpret_cast<v4u*>(swo + w * 8192 + (hf) * 4096 + i * 512 + l * 8) = fr[hf][i];
    __builtin_amdgcn_s_waitcnt(0x0f70);
    glds16(A.mem0, (unsigned)((c & 7) * DM + 64 * w + 16 * g + 8 * (c >> 3)) * 2, sb + C_SM + w * 1024);
    wqkv_issue<true>(A.Wv, 0, h, w, l, sb);
    wqkv_issue<true>(A.Wv, 1, h, w, l, sb);
    tag = call_tag(A);
    W_STAGE(2, 0, WAIT_VM(8), WO_ISSUE(0));
    W_STAGE(2, 1, WAIT_VM(8), WO_ISSUE(1));
    SST(4);
    if (!ATT) {
      float f2 = 0.f;
      asm volatile("" : "+v"(f2));
#pragma unroll
      for (int t = 0; t < 8; ++t)
#pragma unroll
        for (int e = 0; e < 8; ++e) f2 = dot2sq(w2[t][e], dot2sq(w1[t][e], f2));
      f2 = wave_sum_dpp(f2);
      if (l == 0) { sgf[w] = f2; sgf[16] = 0.f; }
    }
    W_STAGE(3, 0, WAIT_VM(8), wqkv_issue<true>(A.Wq, 0, h, w, l, sb));
    W_STAGE(3, 1, WAIT_VM(8), wqkv_issue<true>(A.Wq, 1, h, w, l, sb));
    SST(5);
    {
      float cn[4] = {0.f, 0.f, 0.f, 0.f};
      asm volatile("" : "+v"(cn[0]), "+v"(cn[1]), "+v"(cn[2]), "+v"(cn[3]));
#pragma unroll
      for (int e = 0; e < 64; ++e) cn[e & 3] = dot2sq(fr[e >> 5][2 * (e & 3) + ((e >> 4) & 1)][(e >> 2) & 3], cn[e & 3]);
      const float m2 = wave_max_dpp(swap_add32(swap_add16(cn[0], cn[1]), swap_add16(cn[2], cn[3])));
      if (l == 0) sgf[8 + w] = m2;
    }
#define G_VALUE()                                                                                                   \
  if (w == 4 && L == 0) {                                                                                           \
    const f32x4 a = *reinterpret_cast<const f32x4*>(sgf + 4), b = *reinterpret_cast<const f32x4*>(sgf + 8);         \
    const f32x4 d = *reinterpret_cast<const f32x4*>(sgf + 12);                                                      \
    const float m2 = fmaxf(fmaxf(fmaxf(b.x, b.y), fmaxf(b.z, b.w)), fmaxf(fmaxf(d.x, d.y), fmaxf(d.z, d.w)));       \
    if (l < 8) __hip_atomic_store((gu64*)(ws + OFF_G) + l * 32 + h, tagged(tag, __float_as_uint(((a.x + a.y) + (a.z + a.w)) * m2 * 1.01f)), RLX_AGENT); \
  }
    W_STAGE(0, 0, WAIT_VM(8), G_VALUE(); wqkv_issue<true>(A.Wk, 0, h, w, l, sb));
    W_STAGE(0, 1, WAIT_VM(8), if (ATT) {
      wqkv_issue<true>(A.Wk, 1, h, w, l, sb);
      wqkv_issue<true>(A.Wk, 1, h, w + 4, l, sb);
    });
    SST(6);
    W_STAGE(1, 0, if (ATT) WAIT_VM(16); else { ready_wait(A, tag, ready_look(A)); __builtin_amdgcn_s_waitcnt(0x0f70); }, WO_PUT(0));
    if (w == 4) {
      u64 gv;
      for (unsigned spins = 0;; ++spins) {
        gv = __hip_atomic_load((gu64*)(ws + OFF_G) + (blockIdx.x & 7) * 32 + (l & 31), RLX_AGENT);
        if (__all(has_tag(tag, (unsigned)gv, (unsigned)(gv >> 32))) || spins > SPIN_CAP) break;
        __builtin_amdgcn_s_sleep(GSL);
      }
      const float gm = wave_max_dpp(hi16((unsigned)(gv >> 32)));
      if (l == 0) sgf[0] = gm * 1.111f;
    }
    W_STAGE(1, 1, WAIT_VM(0), WO_PUT(1));
    SST(7);
#undef G_VALUE
#undef WO_PUT
#undef W_STAGE
#undef W_READ
#undef WO_ISSUE
  }
  WSTAMP2();
  const unsigned sb = lds_base(smem);
  const char* gK = ws + OFF_QK + L * SZ_QK + ((size_t)h * 16 + 2 * w) * 1024;
  const char* gV = ws + OFF_VF + L * SZ_O + ((size_t)h * 8 + 2 * w - 8) * 1024;
  v8u pv;
  {
    LANE_IDS();
    const unsigned voff = g * 256 + c * 8, wp = sb + w * 16384 + l * 16;
    pv[0] = (c & 8) ? 0xEEEE : 0x4444;
    pv[1] = sb + C_SM + l * 16;
    const unsigned ra = sb + C_SQ + (g >> 1) * 512 + (c ^ ((g & 1) * 18)) * 8;
    pv[2] = ATT ? ra : voff;
    pv[3] = ATT ? ra ^ 0x108 : wp;
    pv[4] = sb + ((c & 2) ? C_SQ : C_SO) + w * 256 + (((c & 5) | ((c >> 2) & 2)) + 8 * (g >> 1)) * 16 + 8 * ((g ^ (c >> 1)) & 1);
    pv[5] = voff;
    pv[6] = wp;
    pv[7] = sb + C_SO + (l >> 5) * 1024 + w * 256 + (l & 31) * 8;
  }
  if (!ATT) {
    for (unsigned spins = 0; !__all(__hip_atomic_load((gu32*)FQ + h * 32 + 2 * (lane_id() & 3), RLX_AGENT) != 0u); ++spins) {
      if (spin_timeout(spins, TMO)) return;
      __builtin_amdgcn_s_sleep(4);
    }
    asm volatile("" ::: "memory");
    const unsigned lo = lane_id() * 16, d = sb + C_KT + (2 * w - 8) * 1024;
    glds16c(gK, lo, d);
    glds16c(gK + 1024, lo, d + 1024);
    glds16c(gV, lo, d + 8192);
    glds16c(gV + 1024, lo, d + 9216);
  }
  const unsigned gw2 = __builtin_amdgcn_readfirstlane(__float_as_uint(sgf[0]));
  DRAIN();
  SST(8);
  const v4u rsA = mk_srd(ws + OFF_ACC + L * SZ_ACC, SZ_ACC);
  const v4u rsF = mk_srd(FQ, 64 * 32 * 32 * 4);
  const v4u rsMK = mk_srd(ws + OFF_MEMK + L * SZ_MEM, SZ_MEM);
  const v4u rsMV = mk_srd(ws + OFF_MEMV + L * SZ_MEM, SZ_MEM);
  const v4u pAB = (v4u{0u, 2048u, 3072u, 1024u} ^ (1536u * tb)) + wb * 4096;
  const char* pGK = gK + NH * 16 * 1024;
  const char* pGV = gV + NH * 8 * 1024;
  const v4u rsT = mk_srd(FL + FL_FK + L * 32, 64 * 1024 - 128);
  unsigned sc[8];
  sc[0] = (32 + h) * 128;
  sc[1] = (h * 4 + w - 4) * 512;
  sc[2] = (h * 4 + w - 4) * 512;
  sc[3] = NSEG - 1;
  sc[4] = gw2;
  sc[5] = w;
  sc[6] = sb;
  const char* dbg = ws;
#define CHAIN_OPS                                                                                                          \
  ::"{v[64:71]}"(w0[0]), "{v[72:79]}"(w0[1]), "{v[80:87]}"(w0[2]), "{v[88:95]}"(w0[3]), "{v[96:103]}"(w0[4]),              \
      "{v[104:111]}"(w0[5]), "{v[112:119]}"(w0[6]), "{v[120:127]}"(w0[7]), "{v[128:135]}"(w1[0]), "{v[136:143]}"(w1[1]),    \
      "{v[144:151]}"(w1[2]), "{v[152:159]}"(w1[3]), "{v[160:167]}"(w1[4]), "{v[168:175]}"(w1[5]), "{v[176:183]}"(w1[6]),    \
      "{v[184:191]}"(w1[7]), "{v[192:199]}"(w2[0]), "{v[200:207]}"(w2[1]), "{v[208:215]}"(w2[2]), "{v[216:223]}"(w2[3]),    \
      "{v[224:231]}"(w2[4]), "{v[232:239]}"(w2[5]), "{v[240:247]}"(w2[6]), "{v[248:255]}"(w2[7]), "{v[0:7]}"(pv),          \
      "{s[40:43]}"(rsA), "{s[44:47]}"(rsF), "{s[48:51]}"(rsMK), "{s[52:55]}"(rsMV), "{s[56:59]}"(pAB), "{s60}"(w * 4096), "{s61}"(ATT ? h * 1024 : L), \
      "{s[62:63]}"(pGK), "{s[64:65]}"(pGV), "{s66}"(0), "{s67}"(w == 4 ? (h >> 3) * 256 + (h & 7) * 8 : 0x40000000), "{s68}"(sc[0]), "{s69}"(sc[1]), "{s70}"(sc[2]),    \
      "{s71}"(sc[3]), "{s72}"(sc[4]), "{s73}"(sc[5]), "{s74}"(sc[6]), "{s[36:37]}"(dbg),                   \
      "{s[0:3]}"(rsT)                                                                                                      \
      : "memory", "vcc", "scc", "s99", "v63"
  if (ATT) asm volatile("s_mov_b64 exec, -1\ns_nop 4\n" CHAIN_ASM_ATT CHAIN_OPS);
  else asm volatile("s_mov_b64 exec, -1\ns_nop 4\n" CHAIN_ASM_VO CHAIN_OPS);
#undef CHAIN_OPS
#undef LANE_IDS
}

__device__ __forceinline__ void role_chain(const MegaArgs& A, char* smem, const int L, const int h, const int rep) {
  if (A.wv < 4) role_chain_t<true>(A, smem, L, h, rep);
  else role_chain_t<false>(A, smem, L, h, rep);
}

constexpr int AW_SO = 0, AW_TK = 32768, AW_CNT = AW_TK + 98304;
__device__ __forceinline__ unsigned umin3(unsigned a, unsigned b, unsigned c) {
  return __builtin_elementwise_min(__builtin_elementwise_min(a, b), c);
}
#define AW_ASM_X \
  "s_mov_b32 s61, 0\n" \
  "s_mov_b32 s67, 1\n" \
  "s_mov_b32 s68, 0\n" \
  "s_mov_b32 s70, 0\n" \
  "s_mov_b32 s73, 0\n" \
  "s_mov_b32 s71, 0x3e0293ee\n" \
  "s_mov_b32 s72, 0xbe0293ee\n" \
  "s_mov_b32 s65, 49152\n" \
  "s_bitcmp0_b32 s58, 2\n" \
  "s_cbranch_scc1 .LCX_OWN_%=\n" \
  "v_swap_b32 v192, v208\n" \
  "v_swap_b32 v193, v209\n" \
  "v_swap_b32 v194, v210\n" \
  "v_swap_b32 v195, v211\n" \
  "v_swap_b32 v196, v212\n" \
  "v_swap_b32 v197, v213\n" \
  "v_swap_b32 v198, v214\n" \
  "v_swap_b32 v199, v215\n" \
  "v_swap_b32 v200, v216\n" \
  "v_swap_b32 v201, v217\n" \
  "v_swap_b32 v202, v218\n" \
  "v_swap_b32 v203, v219\n" \
  "v_swap_b32 v204, v220\n" \
  "v_swap_b32 v205, v221\n" \
  "v_swap_b32 v206, v222\n" \
  "v_swap_b32 v207, v223\n" \
  "v_swap_b32 v224, v240\n" \
  "v_swap_b32 v225, v241\n" \
  "v_swap_b32 v226, v242\n" \
  "v_swap_b32 v227, v243\n" \
  "v_swap_b32 v228, v244\n" \
  "v_swap_b32 v229, v245\n" \
  "v_swap_b32 v230, v246\n" \
  "v_swap_b32 v231, v247\n" \
  "v_swap_b32 v232, v248\n" \
  "v_swap_b32 v233, v249\n" \
  "v_swap_b32 v234, v250\n" \
  "v_swap_b32 v235, v251\n" \
  "v_swap_b32 v236, v252\n" \
  "v_swap_b32 v237, v253\n" \
  "v_swap_b32 v238, v254\n" \
  "v_swap_b32 v239, v255\n" \
  ".LCX_OWN_%=:\n" \
  "v_mov_b32 v38, s58\n" \
  "s_memrealtime s[38:39]\n" \
  "s_waitcnt lgkmcnt(0)\n" \
  "s_mov_b32 s69, s38\n" \
  "s_setprio 3\n" \
  ".LCX_TOP_%=:\n" \
  "s_mov_b32 s62, 0x200000\n" \
  "s_cmp_ge_u32 s73, s67\n" \
  "s_cbranch_scc1 .LCX_TOK_%=\n" \
  "s_cmp_eq_u32 s61, 0\n" \
  "s_cbranch_scc1 .LCX_SPIN_%=\n" \
  ".LCX_RE_%=:\n" \
  "ds_read_b32 v37, v38\n" \
  "s_add_u32 s62, s62, 1\n" \
  "s_waitcnt lgkmcnt(0)\n" \
  "v_readfirstlane_b32 s73, v37\n" \
  "s_bfe_u32 s64, s73, 0x80008\n" \
  "s_and_b32 s73, s73, 0xff\n" \
  "s_min_u32 s73, s73, s64\n" \
  "s_cmp_ge_u32 s73, s67\n" \
  "s_cbranch_scc1 .LCX_TOK_%=\n" \
  "s_sleep 1\n" \
  "s_cmp_lt_u32 s62, 2097158\n" \
  "s_cbranch_scc1 .LCX_RE_%=\n" \
  "s_waitcnt vmcnt(0)\n" \
  "v_mov_b32 v144, s59\n" \
  "v_mov_b32 v145, 1\n" \
  "s_mov_b64 exec, 1\n" \
  "ds_add_rtn_u32 v144, v144, v145\n" \
  "s_mov_b64 exec, -1\n" \
  "s_xor_b32 s59, s59, 4\n" \
  "s_mov_b32 s61, 0\n" \
  "s_waitcnt lgkmcnt(0)\n" \
  "v_readfirstlane_b32 s63, v144\n" \
  "v_lshlrev_b32 v144, 5, v5\n" \
  "s_and_b32 s63, s63, 7\n" \
  "s_cmp_eq_u32 s63, 7\n" \
  "s_cbranch_scc0 .LCX_W1_%=\n" \
  "s_mov_b64 exec, 15\n" \
  "global_store_dword v144, v145, s[52:53] sc1\n" \
  "s_mov_b64 exec, -1\n" \
  ".LCX_W1_%=:\n" \
  "s_add_u32 s52, s52, 2048\n" \
  "s_addc_u32 s53, s53, 0\n" \
  ".LCX_SPIN_%=:\n" \
  "s_sleep 2\n" \
  "s_memrealtime s[38:39]\n" \
  "ds_read_b32 v37, v38\n" \
  "s_sub_u32 s62, s62, 1\n" \
  "s_cbranch_scc1 .LCX_END_%=\n" \
  "s_waitcnt lgkmcnt(0)\n" \
  "v_readfirstlane_b32 s73, v37\n" \
  "s_add_u32 s38, s38, 80\n" \
  "s_bfe_u32 s64, s73, 0x80008\n" \
  "s_and_b32 s73, s73, 0xff\n" \
  "s_min_u32 s73, s73, s64\n" \
  "s_cmp_ge_u32 s73, s67\n" \
  "s_cbranch_scc0 .LCX_SPIN_%=\n" \
  "s_cmp_eq_u32 s70, 0\n" \
  "s_cbranch_scc1 .LCX_TOK_%=\n" \
  "s_sub_u32 s63, s38, s69\n" \
  "s_max_i32 s63, s63, 0\n" \
  "s_add_u32 s69, s69, s63\n" \
  ".LCX_TOK_%=:\n" \
  "ds_read_b128 v[48:51], v6 offset:0\n" \
  "ds_read_b128 v[64:67], v0 offset:8192\n" \
  "ds_read_b128 v[80:83], v0 offset:12288\n" \
  "ds_read_b128 v[52:55], v6 offset:1024\n" \
  "ds_read_b128 v[68:71], v0 offset:9216\n" \
  "ds_read_b128 v[84:87], v0 offset:13312\n" \
  "ds_read_b128 v[56:59], v6 offset:2048\n" \
  "ds_read_b128 v[72:75], v0 offset:10240\n" \
  "ds_read_b128 v[88:91], v0 offset:14336\n" \
  "ds_read_b128 v[60:63], v6 offset:3072\n" \
  "ds_read_b128 v[76:79], v0 offset:11264\n" \
  "ds_read_b128 v[92:95], v0 offset:15360\n" \
  "s_waitcnt lgkmcnt(9)\n" \
  "v_mfma_f32_16x16x32_bf16 v[20:23], v[64:67], v[48:51], v[8:11]\n" \
  "v_mfma_f32_16x16x32_bf16 v[24:27], v[80:83], v[48:51], v[12:15]\n" \
  "ds_read_b128 v[96:99], v0 offset:16384\n" \
  "ds_read_b128 v[100:103], v0 offset:17408\n" \
  "s_waitcnt lgkmcnt(8)\n" \
  "v_mfma_f32_16x16x32_bf16 v[20:23], v[68:71], v[52:55], v[20:23]\n" \
  "v_mfma_f32_16x16x32_bf16 v[24:27], v[84:87], v[52:55], v[24:27]\n" \
  "ds_read_b128 v[104:107], v0 offset:18432\n" \
  "ds_read_b128 v[108:111], v0 offset:19456\n" \
  "s_waitcnt lgkmcnt(7)\n" \
  "v_mfma_f32_16x16x32_bf16 v[20:23], v[72:75], v[56:59], v[20:23]\n" \
  "v_mfma_f32_16x16x32_bf16 v[24:27], v[88:91], v[56:59], v[24:27]\n" \
  "ds_read_b128 v[112:115], v0 offset:20480\n" \
  "ds_read_b128 v[116:119], v0 offset:21504\n" \
  "s_waitcnt lgkmcnt(6)\n" \
  "v_mfma_f32_16x16x32_bf16 v[20:23], v[76:79], v[60:63], v[20:23]\n" \
  "v_mfma_f32_16x16x32_bf16 v[24:27], v[92:95], v[60:63], v[24:27]\n" \
  "ds_read_b128 v[120:123], v0 offset:22528\n" \
  "ds_read_b128 v[124:127], v0 offset:23552\n" \
  "s_add_u32 s67, s67, 1\n" \
  "v_add_u32 v0, s65, v0\n" \
  "v_add_u32 v6, s65, v6\n" \
  "s_sub_u32 s65, 0, s65\n" \
  "s_nop 1\n" \
  "v_max3_f32 v32, v20, v21, v22\n" \
  "v_max3_f32 v33, v23, v24, v25\n" \
  "v_max3_f32 v32, v32, v26, v27\n" \
  "v_max_f32 v32, v32, v33\n" \
  "v_mov_b32 v33, v32\n" \
  "s_nop 1\n" \
  "v_permlane16_swap_b32 v32, v33\n" \
  "v_max_f32 v32, v32, v33\n" \
  "v_mov_b32 v33, v32\n" \
  "s_nop 1\n" \
  "v_permlane32_swap_b32 v32, v33\n" \
  "v_max_f32 v32, v32, v33\n" \
  "v_mul_f32 v33, s72, v32\n" \
  "v_fma_f32 v20, v20, s71, v33\n" \
  "v_fma_f32 v21, v21, s71, v33\n" \
  "v_fma_f32 v22, v22, s71, v33\n" \
  "v_fma_f32 v23, v23, s71, v33\n" \
  "v_fma_f32 v24, v24, s71, v33\n" \
  "v_fma_f32 v25, v25, s71, v33\n" \
  "v_fma_f32 v26, v26, s71, v33\n" \
  "v_fma_f32 v27, v27, s71, v33\n" \
  "v_exp_f32 v20, v20\n" \
  "v_exp_f32 v21, v21\n" \
  "v_exp_f32 v22, v22\n" \
  "v_exp_f32 v23, v23\n" \
  "v_exp_f32 v24, v24\n" \
  "v_exp_f32 v25, v25\n" \
  "v_exp_f32 v26, v26\n" \
  "v_exp_f32 v27, v27\n" \
  "v_add_f32 v34, v20, v21\n" \
  "v_add_f32 v33, v22, v23\n" \
  "v_add_f32 v36, v24, v25\n" \
  "v_add_f32 v34, v34, v33\n" \
  "v_add_f32 v33, v26, v27\n" \
  "v_add_f32 v34, v34, v36\n" \
  "v_add_f32 v34, v34, v33\n" \
  "v_mov_b64 v[28:29], 0\n" \
  "v_mov_b64 v[30:31], 0\n" \
  "s_waitcnt vmcnt(0) lgkmcnt(0)\n" \
  "s_cmp_eq_u32 s61, 0\n" \
  "s_cbranch_scc1 .LCX_NP_%=\n" \
  "v_mov_b32 v144, s59\n" \
  "v_mov_b32 v145, 1\n" \
  "s_mov_b64 exec, 1\n" \
  "ds_add_rtn_u32 v144, v144, v145\n" \
  "s_mov_b64 exec, -1\n" \
  "s_xor_b32 s59, s59, 4\n" \
  "s_cmp_lg_u32 s68, 0\n" \
  "s_cbranch_scc1 .LCX_TEST_%=\n" \
  "s_mov_b32 s61, 0\n" \
  "s_waitcnt lgkmcnt(0)\n" \
  "v_readfirstlane_b32 s63, v144\n" \
  "v_lshlrev_b32 v144, 5, v5\n" \
  "s_and_b32 s63, s63, 7\n" \
  "s_cmp_eq_u32 s63, 7\n" \
  "s_cbranch_scc0 .LCX_W2_%=\n" \
  "s_mov_b64 exec, 15\n" \
  "global_store_dword v144, v145, s[52:53] sc1\n" \
  "s_mov_b64 exec, -1\n" \
  ".LCX_W2_%=:\n" \
  "s_add_u32 s52, s52, 2048\n" \
  "s_addc_u32 s53, s53, 0\n" \
  ".LCX_NP_%=:\n" \
  "s_cmp_lg_u32 s68, 0\n" \
  "s_cbranch_scc1 .LCX_TEST_%=\n" \
  ".LCX_WF_%=:\n" \
  "s_memrealtime s[38:39]\n" \
  "s_waitcnt lgkmcnt(0)\n" \
  "s_sub_u32 s63, s69, s38\n" \
  "s_cmp_le_i32 s63, 0\n" \
  "s_cbranch_scc1 .LCX_GO_%=\n" \
  "s_cmp_lt_i32 s63, 12\n" \
  "s_cbranch_scc1 .LCX_WF_%=\n" \
  "s_sleep 3\n" \
  "s_branch .LCX_WF_%=\n" \
  ".LCX_GO_%=:\n" \
  "v_mov_b32 v35, 0\n" \
  "global_load_dwordx4 v[128:131], v5, s[48:49] offset:0 sc1\n" \
  "global_load_dwordx4 v[132:135], v5, s[48:49] offset:1024 sc1\n" \
  "global_load_dwordx4 v[136:139], v3, s[48:49] offset:0 sc1\n" \
  "global_load_dwordx4 v[140:143], v3, s[48:49] offset:512 sc1\n" \
  "global_load_dword v35, v35, s[50:51] sc1\n" \
  "s_sub_u32 s62, s62, 8\n" \
  "s_cbranch_scc1 .LCX_END_%=\n" \
  "s_waitcnt vmcnt(0)\n" \
  ".LCX_TEST_%=:\n" \
  "v_smfmac_f32_16x16x64_bf16 v[28:31], v[128:131], v[48:55], v1\n" \
  "v_smfmac_f32_16x16x64_bf16 v[28:31], v[132:135], v[56:63], v1\n" \
  "v_min3_u32 v33, v128, v129, v130\n" \
  "v_min3_u32 v36, v131, v132, v133\n" \
  "v_min3_u32 v33, v33, v134, v135\n" \
  "v_min3_u32 v36, v36, v136, v137\n" \
  "v_min3_u32 v33, v33, v138, v139\n" \
  "v_min3_u32 v36, v36, v140, v141\n" \
  "v_min3_u32 v33, v33, v142, v143\n" \
  "v_min_u32 v33, v33, v36\n" \
  "v_cmp_eq_u32 vcc, 0, v33\n" \
  "v_readfirstlane_b32 s64, v35\n" \
  "s_mov_b32 s68, 0\n" \
  "s_cmp_eq_u64 vcc, 0\n" \
  "s_cbranch_scc0 .LCX_FAIL_%=\n" \
  "v_permlane32_swap_b32 v28, v30\n" \
  "v_permlane32_swap_b32 v29, v31\n" \
  "v_mov_b32 v33, s67\n" \
  "ds_write_b32 v38, v33 offset:16\n" \
  "v_add_f32 v28, v28, v30\n" \
  "v_add_f32 v29, v29, v31\n" \
  "v_max3_f32 v36, v28, v29, v32\n" \
  "v_mov_b32 v33, v36\n" \
  "s_cmp_eq_u32 s64, 0\n" \
  "s_cselect_b32 s64, s38, s64\n" \
  "v_permlane16_swap_b32 v36, v33\n" \
  "v_max_f32 v36, v36, v33\n" \
  "v_mov_b32 v33, v36\n" \
  "s_sub_u32 s63, s64, s70\n" \
  "s_mov_b32 s70, s64\n" \
  "v_permlane32_swap_b32 v36, v33\n" \
  "v_max_f32 v36, v36, v33\n" \
  "v_mul_f32 v36, s72, v36\n" \
  "v_fma_f32 v32, v32, s71, v36\n" \
  "v_fma_f32 v28, v28, s71, v36\n" \
  "v_fma_f32 v29, v29, s71, v36\n" \
  "v_exp_f32 v32, v32\n" \
  "v_exp_f32 v28, v28\n" \
  "v_exp_f32 v29, v29\n" \
  "v_fma_f32 v34, v34, v32, v28\n" \
  "v_add_f32 v34, v34, v29\n" \
  "v_mov_b32 v33, v34\n" \
  "s_min_u32 s63, s63, 250\n" \
  "s_add_u32 s69, s64, s63\n" \
  "v_permlane16_swap_b32 v34, v33\n" \
  "v_add_f32 v34, v34, v33\n" \
  "v_mov_b32 v33, v34\n" \
  "s_add_u32 s69, s69, -48\n" \
  "s_add_u32 s48, s48, 0x10000\n" \
  "s_addc_u32 s49, s49, 0\n" \
  "v_permlane32_swap_b32 v34, v33\n" \
  "v_add_f32 v34, v34, v33\n" \
  "v_rcp_f32 v34, v34\n" \
  "s_add_u32 s50, s50, 1024\n" \
  "s_addc_u32 s51, s51, 0\n" \
  "v_mul_f32 v32, v32, v34\n" \
  "v_mul_f32 v20, v20, v32\n" \
  "v_mul_f32 v21, v21, v32\n" \
  "v_mul_f32 v22, v22, v32\n" \
  "v_mul_f32 v23, v23, v32\n" \
  "v_mul_f32 v24, v24, v32\n" \
  "v_mul_f32 v25, v25, v32\n" \
  "v_mul_f32 v26, v26, v32\n" \
  "v_mul_f32 v27, v27, v32\n" \
  "v_cvt_pk_bf16_f32 v40, v20, v21\n" \
  "v_cvt_pk_bf16_f32 v41, v22, v23\n" \
  "v_cvt_pk_bf16_f32 v42, v24, v25\n" \
  "v_cvt_pk_bf16_f32 v43, v26, v27\n" \
  "v_mul_f32 v28, v28, v34\n" \
  "v_mul_f32 v29, v29, v34\n" \
  "v_mfma_f32_16x16x32_bf16 v[64:67], v[96:99], v[40:43], 0\n" \
  "v_cvt_pk_bf16_f32 v44, v28, v29\n" \
  "v_mov_b32 v45, 0\n" \
  "v_mfma_f32_16x16x32_bf16 v[68:71], v[100:103], v[40:43], 0\n" \
  "v_cvt_pk_bf16_f32 v47, v28, v29\n" \
  "v_mov_b32 v46, 0\n" \
  "v_mfma_f32_16x16x32_bf16 v[72:75], v[104:107], v[40:43], 0\n" \
  "v_permlane32_swap_b32 v44, v45\n" \
  "v_mfma_f32_16x16x32_bf16 v[76:79], v[108:111], v[40:43], 0\n" \
  "v_permlane32_swap_b32 v46, v47\n" \
  "v_mfma_f32_16x16x32_bf16 v[80:83], v[112:115], v[40:43], 0\n" \
  "v_xor_b32 v16, 4096, v7\n" \
  "v_mfma_f32_16x16x32_bf16 v[84:87], v[116:119], v[40:43], 0\n" \
  "v_xor_b32 v17, 8192, v7\n" \
  "v_mfma_f32_16x16x32_bf16 v[88:91], v[120:123], v[40:43], 0\n" \
  "v_xor_b32 v18, 12288, v7\n" \
  "v_mfma_f32_16x16x32_bf16 v[92:95], v[124:127], v[40:43], 0\n" \
  "s_cmp_eq_u32 s61, 0\n" \
  "s_cbranch_scc1 .LCX_NQ_%=\n" \
  "s_mov_b32 s61, 0\n" \
  "s_waitcnt lgkmcnt(0)\n" \
  "v_readfirstlane_b32 s63, v144\n" \
  "v_lshlrev_b32 v144, 5, v5\n" \
  "s_and_b32 s63, s63, 7\n" \
  "s_cmp_eq_u32 s63, 7\n" \
  "s_cbranch_scc0 .LCX_W3_%=\n" \
  "s_mov_b64 exec, 15\n" \
  "global_store_dword v144, v145, s[52:53] sc1\n" \
  "s_mov_b64 exec, -1\n" \
  ".LCX_W3_%=:\n" \
  "s_add_u32 s52, s52, 2048\n" \
  "s_addc_u32 s53, s53, 0\n" \
  ".LCX_NQ_%=:\n" \
  "s_memrealtime s[38:39]\n" \
  "ds_read_b32 v37, v38\n" \
  "v_mfma_f32_16x16x16_bf16 v[64:67], v[136:137], v[44:45], v[64:67]\n" \
  "v_mfma_f32_16x16x16_bf16 v[68:71], v[140:141], v[44:45], v[68:71]\n" \
  "v_mfma_f32_16x16x16_bf16 v[72:75], v[136:137], v[46:47], v[72:75]\n" \
  "v_mfma_f32_16x16x16_bf16 v[76:79], v[140:141], v[46:47], v[76:79]\n" \
  "v_mfma_f32_16x16x16_bf16 v[80:83], v[138:139], v[44:45], v[80:83]\n" \
  "v_mfma_f32_16x16x16_bf16 v[84:87], v[142:143], v[44:45], v[84:87]\n" \
  "v_mfma_f32_16x16x16_bf16 v[88:91], v[138:139], v[46:47], v[88:91]\n" \
  "v_mfma_f32_16x16x16_bf16 v[92:95], v[142:143], v[46:47], v[92:95]\n" \
  "s_nop 0\n" \
  "v_cvt_pk_bf16_f32 v160, v64, v65\n" \
  "v_cvt_pk_bf16_f32 v161, v66, v67\n" \
  "v_cvt_pk_bf16_f32 v162, v68, v69\n" \
  "v_cvt_pk_bf16_f32 v163, v70, v71\n" \
  "ds_write_b128 v7, v[160:163] offset:0\n" \
  "v_cvt_pk_bf16_f32 v164, v72, v73\n" \
  "v_cvt_pk_bf16_f32 v165, v74, v75\n" \
  "v_cvt_pk_bf16_f32 v166, v76, v77\n" \
  "v_cvt_pk_bf16_f32 v167, v78, v79\n" \
  "ds_write_b128 v7, v[164:167] offset:1024\n" \
  "v_cvt_pk_bf16_f32 v168, v80, v81\n" \
  "v_cvt_pk_bf16_f32 v169, v82, v83\n" \
  "v_cvt_pk_bf16_f32 v170, v84, v85\n" \
  "v_cvt_pk_bf16_f32 v171, v86, v87\n" \
  "ds_write_b128 v7, v[168:171] offset:2048\n" \
  "v_cvt_pk_bf16_f32 v172, v88, v89\n" \
  "v_cvt_pk_bf16_f32 v173, v90, v91\n" \
  "v_cvt_pk_bf16_f32 v174, v92, v93\n" \
  "v_cvt_pk_bf16_f32 v175, v94, v95\n" \
  "ds_write_b128 v7, v[172:175] offset:3072\n" \
  "v_xor_b32 v7, 0x4000, v7\n" \
  "v_mfma_f32_16x16x32_bf16 v[112:115], v[192:195], v[160:163], 0\n" \
  "v_mfma_f32_16x16x32_bf16 v[116:119], v[224:227], v[160:163], 0\n" \
  "s_waitcnt lgkmcnt(0)\n" \
  "s_barrier\n" \
  "ds_read_b128 v[64:67], v16 offset:0\n" \
  "ds_read_b128 v[68:71], v16 offset:1024\n" \
  "ds_read_b128 v[72:75], v16 offset:2048\n" \
  "ds_read_b128 v[76:79], v16 offset:3072\n" \
  "ds_read_b128 v[80:83], v17 offset:0\n" \
  "ds_read_b128 v[84:87], v17 offset:1024\n" \
  "ds_read_b128 v[88:91], v17 offset:2048\n" \
  "ds_read_b128 v[92:95], v17 offset:3072\n" \
  "ds_read_b128 v[96:99], v18 offset:0\n" \
  "ds_read_b128 v[100:103], v18 offset:1024\n" \
  "ds_read_b128 v[104:107], v18 offset:2048\n" \
  "ds_read_b128 v[108:111], v18 offset:3072\n" \
  "v_readfirstlane_b32 s73, v37\n" \
  "s_sub_u32 s63, s38, s69\n" \
  "s_bfe_u32 s64, s73, 0x80010\n" \
  "s_sub_u32 s64, s64, s67\n" \
  "s_or_b32 s63, s63, s64\n" \
  "s_bfe_u32 s64, s73, 0x80008\n" \
  "s_and_b32 s73, s73, 0xff\n" \
  "s_min_u32 s73, s73, s64\n" \
  "s_or_b32 s63, s63, s68\n" \
  "s_cmp_lt_i32 s63, 0\n" \
  "s_cbranch_scc1 .LCX_E4_%=\n" \
  "v_mov_b32 v35, 0\n" \
  "global_load_dwordx4 v[128:131], v5, s[48:49] offset:0 sc1\n" \
  "global_load_dwordx4 v[132:135], v5, s[48:49] offset:1024 sc1\n" \
  "global_load_dwordx4 v[136:139], v3, s[48:49] offset:0 sc1\n" \
  "global_load_dwordx4 v[140:143], v3, s[48:49] offset:512 sc1\n" \
  "global_load_dword v35, v35, s[50:51] sc1\n" \
  "v_mov_b32 v33, s67\n" \
  "ds_write_b32 v38, v33 offset:8\n" \
  "s_mov_b32 s68, -1\n" \
  ".LCX_E4_%=:\n" \
  "v_mfma_f32_16x16x32_bf16 v[112:115], v[196:199], v[164:167], v[112:115]\n" \
  "v_mfma_f32_16x16x32_bf16 v[116:119], v[228:231], v[164:167], v[116:119]\n" \
  "v_mfma_f32_16x16x32_bf16 v[112:115], v[200:203], v[168:171], v[112:115]\n" \
  "v_mfma_f32_16x16x32_bf16 v[116:119], v[232:235], v[168:171], v[116:119]\n" \
  "v_mfma_f32_16x16x32_bf16 v[112:115], v[204:207], v[172:175], v[112:115]\n" \
  "v_mfma_f32_16x16x32_bf16 v[116:119], v[236:239], v[172:175], v[116:119]\n" \
  "s_waitcnt lgkmcnt(11)\n" \
  "v_mfma_f32_16x16x32_bf16 v[120:123], v[192:195], v[64:67], 0\n" \
  "v_mfma_f32_16x16x32_bf16 v[124:127], v[224:227], v[64:67], 0\n" \
  "s_waitcnt lgkmcnt(10)\n" \
  "v_mfma_f32_16x16x32_bf16 v[120:123], v[196:199], v[68:71], v[120:123]\n" \
  "v_mfma_f32_16x16x32_bf16 v[124:127], v[228:231], v[68:71], v[124:127]\n" \
  "s_waitcnt lgkmcnt(9)\n" \
  "v_mfma_f32_16x16x32_bf16 v[120:123], v[200:203], v[72:75], v[120:123]\n" \
  "v_mfma_f32_16x16x32_bf16 v[124:127], v[232:235], v[72:75], v[124:127]\n" \
  "s_waitcnt lgkmcnt(8)\n" \
  "v_mfma_f32_16x16x32_bf16 v[120:123], v[204:207], v[76:79], v[120:123]\n" \
  "v_mfma_f32_16x16x32_bf16 v[124:127], v[236:239], v[76:79], v[124:127]\n" \
  "s_waitcnt lgkmcnt(7)\n" \
  "v_mfma_f32_16x16x32_bf16 v[112:115], v[208:211], v[80:83], v[112:115]\n" \
  "v_mfma_f32_16x16x32_bf16 v[116:119], v[240:243], v[80:83], v[116:119]\n" \
  "s_waitcnt lgkmcnt(6)\n" \
  "v_mfma_f32_16x16x32_bf16 v[112:115], v[212:215], v[84:87], v[112:115]\n" \
  "v_mfma_f32_16x16x32_bf16 v[116:119], v[244:247], v[84:87], v[116:119]\n" \
  "s_waitcnt lgkmcnt(5)\n" \
  "v_mfma_f32_16x16x32_bf16 v[112:115], v[216:219], v[88:91], v[112:115]\n" \
  "v_mfma_f32_16x16x32_bf16 v[116:119], v[248:251], v[88:91], v[116:119]\n" \
  "s_waitcnt lgkmcnt(4)\n" \
  "v_mfma_f32_16x16x32_bf16 v[112:115], v[220:223], v[92:95], v[112:115]\n" \
  "v_mfma_f32_16x16x32_bf16 v[116:119], v[252:255], v[92:95], v[116:119]\n" \
  "s_memrealtime s[38:39]\n" \
  "ds_read_b32 v37, v38\n" \
  "s_waitcnt lgkmcnt(4)\n" \
  "v_mfma_f32_16x16x32_bf16 v[120:123], v[208:211], v[96:99], v[120:123]\n" \
  "v_mfma_f32_16x16x32_bf16 v[124:127], v[240:243], v[96:99], v[124:127]\n" \
  "s_waitcnt lgkmcnt(3)\n" \
  "v_mfma_f32_16x16x32_bf16 v[120:123], v[212:215], v[100:103], v[120:123]\n" \
  "v_mfma_f32_16x16x32_bf16 v[124:127], v[244:247], v[100:103], v[124:127]\n" \
  "v_cvt_pk_bf16_f32 v20, v112, v113\n" \
  "v_cvt_pk_bf16_f32 v21, v114, v115\n" \
  "v_cvt_pk_bf16_f32 v22, v116, v117\n" \
  "v_cvt_pk_bf16_f32 v23, v118, v119\n" \
  "s_waitcnt lgkmcnt(2)\n" \
  "v_mfma_f32_16x16x32_bf16 v[120:123], v[216:219], v[104:107], v[120:123]\n" \
  "v_mfma_f32_16x16x32_bf16 v[124:127], v[248:251], v[104:107], v[124:127]\n" \
  "buffer_store_dwordx4 v[20:23], v4, s[40:43], s54 offen sc1\n" \
  "s_waitcnt lgkmcnt(0)\n" \
  "v_mfma_f32_16x16x32_bf16 v[120:123], v[220:223], v[108:111], v[120:123]\n" \
  "v_mfma_f32_16x16x32_bf16 v[124:127], v[252:255], v[108:111], v[124:127]\n" \
  "s_xor_b32 s64, s54, 16384\n" \
  "s_mov_b32 s61, 1\n" \
  "s_nop 3\n" \
  "v_cvt_pk_bf16_f32 v24, v120, v121\n" \
  "v_cvt_pk_bf16_f32 v25, v122, v123\n" \
  "v_cvt_pk_bf16_f32 v26, v124, v125\n" \
  "v_cvt_pk_bf16_f32 v27, v126, v127\n" \
  "buffer_store_dwordx4 v[24:27], v4, s[40:43], s64 offen sc1\n" \
  "v_readfirstlane_b32 s73, v37\n" \
  "s_sub_u32 s63, s38, s69\n" \
  "s_bfe_u32 s64, s73, 0x80010\n" \
  "s_sub_u32 s64, s64, s67\n" \
  "s_or_b32 s63, s63, s64\n" \
  "s_bfe_u32 s64, s73, 0x80008\n" \
  "s_and_b32 s73, s73, 0xff\n" \
  "s_min_u32 s73, s73, s64\n" \
  "s_or_b32 s63, s63, s68\n" \
  "s_cmp_lt_i32 s63, 0\n" \
  "s_cbranch_scc1 .LCX_E5_%=\n" \
  "v_mov_b32 v35, 0\n" \
  "global_load_dwordx4 v[128:131], v5, s[48:49] offset:0 sc1\n" \
  "global_load_dwordx4 v[132:135], v5, s[48:49] offset:1024 sc1\n" \
  "global_load_dwordx4 v[136:139], v3, s[48:49] offset:0 sc1\n" \
  "global_load_dwordx4 v[140:143], v3, s[48:49] offset:512 sc1\n" \
  "global_load_dword v35, v35, s[50:51] sc1\n" \
  "v_mov_b32 v33, s67\n" \
  "ds_write_b32 v38, v33 offset:8\n" \
  "s_mov_b32 s68, -1\n" \
  ".LCX_E5_%=:\n" \
  "s_add_u32 s54, s54, 0x80000\n" \
  "s_sub_u32 s60, s60, 1\n" \
  "s_cmp_lg_u32 s60, 0\n" \
  "s_cbranch_scc1 .LCX_TOP_%=\n" \
  "s_waitcnt vmcnt(0)\n" \
  "v_mov_b32 v144, s59\n" \
  "v_mov_b32 v145, 1\n" \
  "s_mov_b64 exec, 1\n" \
  "ds_add_rtn_u32 v144, v144, v145\n" \
  "s_mov_b64 exec, -1\n" \
  "s_xor_b32 s59, s59, 4\n" \
  "s_mov_b32 s61, 0\n" \
  "s_waitcnt lgkmcnt(0)\n" \
  "v_readfirstlane_b32 s63, v144\n" \
  "v_lshlrev_b32 v144, 5, v5\n" \
  "s_and_b32 s63, s63, 7\n" \
  "s_cmp_eq_u32 s63, 7\n" \
  "s_cbranch_scc0 .LCX_W6_%=\n" \
  "s_mov_b64 exec, 15\n" \
  "global_store_dword v144, v145, s[52:53] sc1\n" \
  "s_mov_b64 exec, -1\n" \
  ".LCX_W6_%=:\n" \
  "s_add_u32 s52, s52, 2048\n" \
  "s_addc_u32 s53, s53, 0\n" \
  ".LCX_END_%=:\n" \
  "s_endpgm\n" \
  ".LCX_FAIL_%=:\n" \
  "v_mov_b64 v[28:29], 0\n" \
  "v_mov_b64 v[30:31], 0\n" \
  "s_cmp_eq_u32 s61, 0\n" \
  "s_cbranch_scc1 .LCX_FAIL2_%=\n" \
  "s_mov_b32 s61, 0\n" \
  "s_waitcnt lgkmcnt(0)\n" \
  "v_readfirstlane_b32 s63, v144\n" \
  "v_lshlrev_b32 v144, 5, v5\n" \
  "s_and_b32 s63, s63, 7\n" \
  "s_cmp_eq_u32 s63, 7\n" \
  "s_cbranch_scc0 .LCX_W7_%=\n" \
  "s_mov_b64 exec, 15\n" \
  "global_store_dword v144, v145, s[52:53] sc1\n" \
  "s_mov_b64 exec, -1\n" \
  ".LCX_W7_%=:\n" \
  "s_add_u32 s52, s52, 2048\n" \
  "s_addc_u32 s53, s53, 0\n" \
  ".LCX_FAIL2_%=:\n" \
  "s_add_u32 s69, s64, -48\n" \
  "s_cmp_lg_u32 s64, 0\n" \
  "s_cbranch_scc1 .LCX_WF_%=\n" \
  "s_sleep 4\n" \
  "global_load_dword v35, v35, s[50:51] sc1\n" \
  "s_sub_u32 s62, s62, 8\n" \
  "s_cbranch_scc1 .LCX_END_%=\n" \
  "s_waitcnt vmcnt(0)\n" \
  "v_readfirstlane_b32 s64, v35\n" \
  "s_branch .LCX_FAIL2_%=\n"
#define AW_ASM_Y \
  "s_mov_b32 s72, 0\n" \
  "s_mov_b32 s67, 0\n" \
  "s_mov_b32 s68, 2\n" \
  "s_mov_b32 s69, 0\n" \
  "s_mov_b32 s65, 49152\n" \
  "s_mov_b32 s66, 16384\n" \
  "v_mov_b32 v5, s58\n" \
  "v_mov_b32 v60, v3\n" \
  "v_add_u32 v3, 4096, v2\n" \
  "s_mov_b32 s62, 0x40000\n" \
  "s_branch .LCY_ASK_%=\n" \
  ".LCY_BAR_%=:\n" \
  "s_barrier\n" \
  "ds_read_b128 v[8:11], v0 offset:0\n" \
  "ds_read_b128 v[12:15], v0 offset:1024\n" \
  "ds_read_b128 v[16:19], v0 offset:2048\n" \
  "s_add_u32 s68, s68, 1\n" \
  "s_add_u32 s64, s54, 16384\n" \
  "s_cmp_eq_u32 s69, 0\n" \
  "s_cbranch_scc1 .LCY_NI_%=\n" \
  "s_waitcnt vmcnt(1)\n" \
  "s_cmp_lg_u32 s61, 0\n" \
  "s_cbranch_scc1 .LCY_NJ_%=\n" \
  "s_waitcnt vmcnt(0)\n" \
  ".LCY_NJ_%=:\n" \
  "v_mov_b32 v6, s55\n" \
  "s_mov_b64 exec, 1\n" \
  "ds_add_u32 v5, v6\n" \
  "s_mov_b64 exec, -1\n" \
  "s_mov_b32 s69, 0\n" \
  "s_cmp_eq_u32 s72, 0\n" \
  "s_cbranch_scc1 .LCY_NI_%=\n" \
  "s_mov_b32 s72, 0\n" \
  "v_mov_b32 v6, s59\n" \
  "v_mov_b32 v7, 1\n" \
  "s_mov_b64 exec, 1\n" \
  "ds_add_rtn_u32 v6, v6, v7\n" \
  "s_mov_b64 exec, -1\n" \
  "s_xor_b32 s59, s59, 4\n" \
  "s_mov_b32 s61, 0\n" \
  "s_waitcnt lgkmcnt(0)\n" \
  "v_readfirstlane_b32 s63, v6\n" \
  "v_lshlrev_b32 v6, 5, v2\n" \
  "s_and_b32 s63, s63, 7\n" \
  "s_cmp_eq_u32 s63, 7\n" \
  "s_cbranch_scc0 .LCY_W1_%=\n" \
  "s_mov_b64 exec, 15\n" \
  "global_store_dword v6, v7, s[52:53] sc1\n" \
  "s_mov_b64 exec, -1\n" \
  ".LCY_W1_%=:\n" \
  "s_add_u32 s52, s52, 2048\n" \
  "s_addc_u32 s53, s53, 0\n" \
  ".LCY_NI_%=:\n" \
  "v_cmp_eq_u32 vcc, 0, v4\n" \
  "s_cmp_eq_u64 vcc, 0\n" \
  "s_cselect_b32 s70, 1, 0\n" \
  "s_cbranch_scc0 .LCY_NS_%=\n" \
  "v_lshlrev_b32 v6, 16, s55\n" \
  "s_mov_b64 exec, 1\n" \
  "ds_add_u32 v5, v6\n" \
  "s_mov_b64 exec, -1\n" \
  "s_mov_b32 m0, s56\n" \
  "s_add_u32 s71, s56, 4096\n" \
  "global_load_lds_dwordx4 v2, s[48:49] offset:0 sc1\n" \
  "global_load_lds_dwordx4 v2, s[48:49] offset:1024 sc1\n" \
  "global_load_lds_dwordx4 v2, s[48:49] offset:2048 sc1\n" \
  "global_load_lds_dwordx4 v2, s[48:49] offset:3072 sc1\n" \
  "s_mov_b32 m0, s71\n" \
  "s_add_u32 s56, s56, s65\n" \
  "global_load_lds_dwordx4 v3, s[48:49] offset:0 sc1\n" \
  "global_load_lds_dwordx4 v3, s[48:49] offset:1024 sc1\n" \
  ".LCY_NS_%=:\n" \
  "ds_read_b128 v[20:23], v0 offset:3072\n" \
  "s_waitcnt lgkmcnt(3)\n" \
  "v_mfma_f32_16x16x32_bf16 v[32:35], v[64:67], v[8:11], 0\n" \
  "v_mfma_f32_16x16x32_bf16 v[36:39], v[96:99], v[8:11], 0\n" \
  "v_mfma_f32_16x16x32_bf16 v[40:43], v[128:131], v[8:11], 0\n" \
  "v_mfma_f32_16x16x32_bf16 v[44:47], v[160:163], v[8:11], 0\n" \
  "v_mfma_f32_16x16x32_bf16 v[48:51], v[192:195], v[8:11], 0\n" \
  "v_mfma_f32_16x16x32_bf16 v[52:55], v[224:227], v[8:11], 0\n" \
  "ds_read_b128 v[8:11], v0 offset:8192\n" \
  "s_waitcnt lgkmcnt(3)\n" \
  "v_mfma_f32_16x16x32_bf16 v[32:35], v[68:71], v[12:15], v[32:35]\n" \
  "v_mfma_f32_16x16x32_bf16 v[36:39], v[100:103], v[12:15], v[36:39]\n" \
  "v_mfma_f32_16x16x32_bf16 v[40:43], v[132:135], v[12:15], v[40:43]\n" \
  "v_mfma_f32_16x16x32_bf16 v[44:47], v[164:167], v[12:15], v[44:47]\n" \
  "v_mfma_f32_16x16x32_bf16 v[48:51], v[196:199], v[12:15], v[48:51]\n" \
  "v_mfma_f32_16x16x32_bf16 v[52:55], v[228:231], v[12:15], v[52:55]\n" \
  "ds_read_b128 v[12:15], v0 offset:9216\n" \
  "s_waitcnt lgkmcnt(3)\n" \
  "v_mfma_f32_16x16x32_bf16 v[32:35], v[72:75], v[16:19], v[32:35]\n" \
  "v_mfma_f32_16x16x32_bf16 v[36:39], v[104:107], v[16:19], v[36:39]\n" \
  "v_mfma_f32_16x16x32_bf16 v[40:43], v[136:139], v[16:19], v[40:43]\n" \
  "v_mfma_f32_16x16x32_bf16 v[44:47], v[168:171], v[16:19], v[44:47]\n" \
  "v_mfma_f32_16x16x32_bf16 v[48:51], v[200:203], v[16:19], v[48:51]\n" \
  "v_mfma_f32_16x16x32_bf16 v[52:55], v[232:235], v[16:19], v[52:55]\n" \
  "ds_read_b128 v[16:19], v0 offset:10240\n" \
  "s_waitcnt lgkmcnt(3)\n" \
  "v_mfma_f32_16x16x32_bf16 v[32:35], v[76:79], v[20:23], v[32:35]\n" \
  "v_mfma_f32_16x16x32_bf16 v[36:39], v[108:111], v[20:23], v[36:39]\n" \
  "v_mfma_f32_16x16x32_bf16 v[40:43], v[140:143], v[20:23], v[40:43]\n" \
  "v_mfma_f32_16x16x32_bf16 v[44:47], v[172:175], v[20:23], v[44:47]\n" \
  "v_mfma_f32_16x16x32_bf16 v[48:51], v[204:207], v[20:23], v[48:51]\n" \
  "v_mfma_f32_16x16x32_bf16 v[52:55], v[236:239], v[20:23], v[52:55]\n" \
  "ds_read_b128 v[20:23], v0 offset:11264\n" \
  "s_waitcnt lgkmcnt(3)\n" \
  "v_mfma_f32_16x16x32_bf16 v[32:35], v[80:83], v[8:11], v[32:35]\n" \
  "v_mfma_f32_16x16x32_bf16 v[36:39], v[112:115], v[8:11], v[36:39]\n" \
  "v_mfma_f32_16x16x32_bf16 v[40:43], v[144:147], v[8:11], v[40:43]\n" \
  "v_mfma_f32_16x16x32_bf16 v[44:47], v[176:179], v[8:11], v[44:47]\n" \
  "v_mfma_f32_16x16x32_bf16 v[48:51], v[208:211], v[8:11], v[48:51]\n" \
  "v_mfma_f32_16x16x32_bf16 v[52:55], v[240:243], v[8:11], v[52:55]\n" \
  "ds_read_b128 v[8:11], v0 offset:4096\n" \
  "s_waitcnt lgkmcnt(3)\n" \
  "v_mfma_f32_16x16x32_bf16 v[32:35], v[84:87], v[12:15], v[32:35]\n" \
  "v_mfma_f32_16x16x32_bf16 v[36:39], v[116:119], v[12:15], v[36:39]\n" \
  "v_mfma_f32_16x16x32_bf16 v[40:43], v[148:151], v[12:15], v[40:43]\n" \
  "v_mfma_f32_16x16x32_bf16 v[44:47], v[180:183], v[12:15], v[44:47]\n" \
  "v_mfma_f32_16x16x32_bf16 v[48:51], v[212:215], v[12:15], v[48:51]\n" \
  "v_mfma_f32_16x16x32_bf16 v[52:55], v[244:247], v[12:15], v[52:55]\n" \
  "ds_read_b128 v[12:15], v0 offset:5120\n" \
  "s_waitcnt lgkmcnt(3)\n" \
  "v_mfma_f32_16x16x32_bf16 v[32:35], v[88:91], v[16:19], v[32:35]\n" \
  "v_mfma_f32_16x16x32_bf16 v[36:39], v[120:123], v[16:19], v[36:39]\n" \
  "v_mfma_f32_16x16x32_bf16 v[40:43], v[152:155], v[16:19], v[40:43]\n" \
  "v_mfma_f32_16x16x32_bf16 v[44:47], v[184:187], v[16:19], v[44:47]\n" \
  "v_mfma_f32_16x16x32_bf16 v[48:51], v[216:219], v[16:19], v[48:51]\n" \
  "v_mfma_f32_16x16x32_bf16 v[52:55], v[248:251], v[16:19], v[52:55]\n" \
  "ds_read_b128 v[16:19], v0 offset:6144\n" \
  "s_waitcnt lgkmcnt(3)\n" \
  "v_mfma_f32_16x16x32_bf16 v[32:35], v[92:95], v[20:23], v[32:35]\n" \
  "v_mfma_f32_16x16x32_bf16 v[36:39], v[124:127], v[20:23], v[36:39]\n" \
  "v_mfma_f32_16x16x32_bf16 v[40:43], v[156:159], v[20:23], v[40:43]\n" \
  "v_mfma_f32_16x16x32_bf16 v[44:47], v[188:191], v[20:23], v[44:47]\n" \
  "v_mfma_f32_16x16x32_bf16 v[48:51], v[220:223], v[20:23], v[48:51]\n" \
  "v_mfma_f32_16x16x32_bf16 v[52:55], v[252:255], v[20:23], v[52:55]\n" \
  "ds_read_b128 v[20:23], v0 offset:7168\n" \
  "s_waitcnt lgkmcnt(3)\n" \
  "s_nop 1\n" \
  "v_cvt_pk_bf16_f32 v24, v32, v33\n" \
  "v_cvt_pk_bf16_f32 v25, v34, v35\n" \
  "v_cvt_pk_bf16_f32 v26, v36, v37\n" \
  "v_cvt_pk_bf16_f32 v27, v38, v39\n" \
  "s_nop 0\n" \
  "v_mfma_f32_16x16x32_bf16 v[32:35], v[64:67], v[8:11], 0\n" \
  "v_mfma_f32_16x16x32_bf16 v[36:39], v[96:99], v[8:11], 0\n" \
  "buffer_store_dwordx4 v[24:27], v1, s[40:43], s54 offen offset:0 sc1\n" \
  "v_cvt_pk_bf16_f32 v28, v40, v41\n" \
  "v_cvt_pk_bf16_f32 v29, v42, v43\n" \
  "v_cvt_pk_bf16_f32 v30, v44, v45\n" \
  "v_cvt_pk_bf16_f32 v31, v46, v47\n" \
  "s_nop 0\n" \
  "v_mfma_f32_16x16x32_bf16 v[40:43], v[128:131], v[8:11], 0\n" \
  "v_mfma_f32_16x16x32_bf16 v[44:47], v[160:163], v[8:11], 0\n" \
  "buffer_store_dwordx4 v[28:31], v1, s[40:43], s54 offen offset:128 sc1\n" \
  "v_cvt_pk_bf16_f32 v56, v48, v49\n" \
  "v_cvt_pk_bf16_f32 v57, v50, v51\n" \
  "v_cvt_pk_bf16_f32 v58, v52, v53\n" \
  "v_cvt_pk_bf16_f32 v59, v54, v55\n" \
  "s_nop 0\n" \
  "v_mfma_f32_16x16x32_bf16 v[48:51], v[192:195], v[8:11], 0\n" \
  "v_mfma_f32_16x16x32_bf16 v[52:55], v[224:227], v[8:11], 0\n" \
  "buffer_store_dwordx4 v[56:59], v1, s[40:43], s54 offen offset:256 sc1\n" \
  "ds_read_b128 v[8:11], v0 offset:12288\n" \
  "s_waitcnt lgkmcnt(3)\n" \
  "v_mfma_f32_16x16x32_bf16 v[32:35], v[68:71], v[12:15], v[32:35]\n" \
  "v_mfma_f32_16x16x32_bf16 v[36:39], v[100:103], v[12:15], v[36:39]\n" \
  "v_mfma_f32_16x16x32_bf16 v[40:43], v[132:135], v[12:15], v[40:43]\n" \
  "v_mfma_f32_16x16x32_bf16 v[44:47], v[164:167], v[12:15], v[44:47]\n" \
  "v_mfma_f32_16x16x32_bf16 v[48:51], v[196:199], v[12:15], v[48:51]\n" \
  "v_mfma_f32_16x16x32_bf16 v[52:55], v[228:231], v[12:15], v[52:55]\n" \
  "ds_read_b128 v[12:15], v0 offset:13312\n" \
  "s_waitcnt lgkmcnt(3)\n" \
  "v_mfma_f32_16x16x32_bf16 v[32:35], v[72:75], v[16:19], v[32:35]\n" \
  "v_mfma_f32_16x16x32_bf16 v[36:39], v[104:107], v[16:19], v[36:39]\n" \
  "v_mfma_f32_16x16x32_bf16 v[40:43], v[136:139], v[16:19], v[40:43]\n" \
  "v_mfma_f32_16x16x32_bf16 v[44:47], v[168:171], v[16:19], v[44:47]\n" \
  "v_mfma_f32_16x16x32_bf16 v[48:51], v[200:203], v[16:19], v[48:51]\n" \
  "v_mfma_f32_16x16x32_bf16 v[52:55], v[232:235], v[16:19], v[52:55]\n" \
  "ds_read_b128 v[16:19], v0 offset:14336\n" \
  "s_waitcnt lgkmcnt(3)\n" \
  "v_mfma_f32_16x16x32_bf16 v[32:35], v[76:79], v[20:23], v[32:35]\n" \
  "v_mfma_f32_16x16x32_bf16 v[36:39], v[108:111], v[20:23], v[36:39]\n" \
  "v_mfma_f32_16x16x32_bf16 v[40:43], v[140:143], v[20:23], v[40:43]\n" \
  "v_mfma_f32_16x16x32_bf16 v[44:47], v[172:175], v[20:23], v[44:47]\n" \
  "v_mfma_f32_16x16x32_bf16 v[48:51], v[204:207], v[20:23], v[48:51]\n" \
  "v_mfma_f32_16x16x32_bf16 v[52:55], v[236:239], v[20:23], v[52:55]\n" \
  "ds_read_b32 v7, v5 offset:8\n" \
  "ds_read_b128 v[20:23], v0 offset:15360\n" \
  "s_waitcnt lgkmcnt(4)\n" \
  "v_mfma_f32_16x16x32_bf16 v[32:35], v[80:83], v[8:11], v[32:35]\n" \
  "v_mfma_f32_16x16x32_bf16 v[36:39], v[112:115], v[8:11], v[36:39]\n" \
  "v_mfma_f32_16x16x32_bf16 v[40:43], v[144:147], v[8:11], v[40:43]\n" \
  "v_mfma_f32_16x16x32_bf16 v[44:47], v[176:179], v[8:11], v[44:47]\n" \
  "v_mfma_f32_16x16x32_bf16 v[48:51], v[208:211], v[8:11], v[48:51]\n" \
  "v_mfma_f32_16x16x32_bf16 v[52:55], v[240:243], v[8:11], v[52:55]\n" \
  "s_waitcnt lgkmcnt(3)\n" \
  "v_mfma_f32_16x16x32_bf16 v[32:35], v[84:87], v[12:15], v[32:35]\n" \
  "v_mfma_f32_16x16x32_bf16 v[36:39], v[116:119], v[12:15], v[36:39]\n" \
  "v_mfma_f32_16x16x32_bf16 v[40:43], v[148:151], v[12:15], v[40:43]\n" \
  "v_mfma_f32_16x16x32_bf16 v[44:47], v[180:183], v[12:15], v[44:47]\n" \
  "v_mfma_f32_16x16x32_bf16 v[48:51], v[212:215], v[12:15], v[48:51]\n" \
  "v_mfma_f32_16x16x32_bf16 v[52:55], v[244:247], v[12:15], v[52:55]\n" \
  "s_waitcnt lgkmcnt(2)\n" \
  "v_mfma_f32_16x16x32_bf16 v[32:35], v[88:91], v[16:19], v[32:35]\n" \
  "v_mfma_f32_16x16x32_bf16 v[36:39], v[120:123], v[16:19], v[36:39]\n" \
  "v_mfma_f32_16x16x32_bf16 v[40:43], v[152:155], v[16:19], v[40:43]\n" \
  "v_mfma_f32_16x16x32_bf16 v[44:47], v[184:187], v[16:19], v[44:47]\n" \
  "v_mfma_f32_16x16x32_bf16 v[48:51], v[216:219], v[16:19], v[48:51]\n" \
  "v_mfma_f32_16x16x32_bf16 v[52:55], v[248:251], v[16:19], v[52:55]\n" \
  "s_waitcnt lgkmcnt(0)\n" \
  "v_mfma_f32_16x16x32_bf16 v[32:35], v[92:95], v[20:23], v[32:35]\n" \
  "v_mfma_f32_16x16x32_bf16 v[36:39], v[124:127], v[20:23], v[36:39]\n" \
  "v_mfma_f32_16x16x32_bf16 v[40:43], v[156:159], v[20:23], v[40:43]\n" \
  "v_mfma_f32_16x16x32_bf16 v[44:47], v[188:191], v[20:23], v[44:47]\n" \
  "v_mfma_f32_16x16x32_bf16 v[48:51], v[220:223], v[20:23], v[48:51]\n" \
  "v_mfma_f32_16x16x32_bf16 v[52:55], v[252:255], v[20:23], v[52:55]\n" \
  "v_add_u32 v0, s66, v0\n" \
  "s_sub_u32 s66, 0, s66\n" \
  "s_add_u32 s54, s54, 0x80000\n" \
  "s_mov_b32 s62, 0x40000\n" \
  "v_readfirstlane_b32 s63, v7\n" \
  "v_cvt_pk_bf16_f32 v24, v32, v33\n" \
  "v_cvt_pk_bf16_f32 v25, v34, v35\n" \
  "v_cvt_pk_bf16_f32 v26, v36, v37\n" \
  "v_cvt_pk_bf16_f32 v27, v38, v39\n" \
  "buffer_store_dwordx4 v[24:27], v1, s[40:43], s64 offen offset:0 sc1\n" \
  "v_cvt_pk_bf16_f32 v28, v40, v41\n" \
  "v_cvt_pk_bf16_f32 v29, v42, v43\n" \
  "v_cvt_pk_bf16_f32 v30, v44, v45\n" \
  "v_cvt_pk_bf16_f32 v31, v46, v47\n" \
  "buffer_store_dwordx4 v[28:31], v1, s[40:43], s64 offen offset:128 sc1\n" \
  "v_cvt_pk_bf16_f32 v56, v48, v49\n" \
  "v_cvt_pk_bf16_f32 v57, v50, v51\n" \
  "v_cvt_pk_bf16_f32 v58, v52, v53\n" \
  "v_cvt_pk_bf16_f32 v59, v54, v55\n" \
  "buffer_store_dwordx4 v[56:59], v1, s[40:43], s64 offen offset:256 sc1\n" \
  "s_cmp_eq_u32 s70, 0\n" \
  "s_cbranch_scc1 .LCY_ND_%=\n" \
  "global_load_lds_dwordx4 v3, s[48:49] offset:2048 sc1\n" \
  "global_load_lds_dwordx4 v3, s[48:49] offset:3072 sc1\n" \
  "s_add_u32 s48, s48, 0x80000\n" \
  "s_addc_u32 s49, s49, 0\n" \
  "s_mov_b32 m0, s57\n" \
  "s_add_u32 s57, s57, s65\n" \
  "global_load_lds_dwordx4 v2, s[50:51] offset:0 sc1\n" \
  "global_load_lds_dwordx4 v2, s[50:51] offset:1024 sc1\n" \
  "s_sub_u32 s65, 0, s65\n" \
  "s_add_u32 s67, s67, 4096\n" \
  "global_load_lds_dwordx4 v2, s[50:51] offset:2048 sc1\n" \
  "s_sub_u32 s68, s68, 1\n" \
  "s_mov_b32 s69, 1\n" \
  "global_load_lds_dwordx4 v2, s[50:51] offset:3072 sc1\n" \
  "s_add_u32 s50, s50, 0x40000\n" \
  "s_addc_u32 s51, s51, 0\n" \
  "v_mov_b32 v4, 0\n" \
  "buffer_load_dword v4, v60, s[44:47], s67 offen sc1\n" \
  "s_add_u32 s63, s63, s60\n" \
  "s_mov_b32 s61, 0\n" \
  "s_cmp_eq_u32 s63, 66\n" \
  "s_cselect_b32 s72, 1, 0\n" \
  "s_cbranch_scc1 .LCY_DEF_%=\n" \
  "s_waitcnt vmcnt(7)\n" \
  "s_branch .LCY_WC_%=\n" \
  ".LCY_ND_%=:\n" \
  "v_mov_b32 v4, 0\n" \
  "buffer_load_dword v4, v60, s[44:47], s67 offen sc1\n" \
  "s_waitcnt vmcnt(0)\n" \
  ".LCY_WC_%=:\n" \
  "v_mov_b32 v6, s59\n" \
  "v_mov_b32 v7, 1\n" \
  "s_mov_b64 exec, 1\n" \
  "ds_add_rtn_u32 v6, v6, v7\n" \
  "s_mov_b64 exec, -1\n" \
  "s_xor_b32 s59, s59, 4\n" \
  "s_mov_b32 s61, 0\n" \
  "s_waitcnt lgkmcnt(0)\n" \
  "v_readfirstlane_b32 s63, v6\n" \
  "v_lshlrev_b32 v6, 5, v2\n" \
  "s_and_b32 s63, s63, 7\n" \
  "s_cmp_eq_u32 s63, 7\n" \
  "s_cbranch_scc0 .LCY_W2_%=\n" \
  "s_mov_b64 exec, 15\n" \
  "global_store_dword v6, v7, s[52:53] sc1\n" \
  "s_mov_b64 exec, -1\n" \
  "s_mov_b32 s61, 1\n" \
  ".LCY_W2_%=:\n" \
  "s_add_u32 s52, s52, 2048\n" \
  "s_addc_u32 s53, s53, 0\n" \
  ".LCY_DEF_%=:\n" \
  "s_sub_u32 s60, s60, 1\n" \
  "s_cmp_eq_u32 s60, 0\n" \
  "s_cbranch_scc1 .LCY_END_%=\n" \
  ".LCY_NEED_%=:\n" \
  "s_cmp_eq_u32 s68, 0\n" \
  "s_cbranch_scc1 .LCY_BAR_%=\n" \
  "v_cmp_eq_u32 vcc, 0, v4\n" \
  "s_cmp_eq_u64 vcc, 0\n" \
  "s_cbranch_scc1 .LCY_GET_%=\n" \
  "s_cmp_ge_u32 s67, 0x40000\n" \
  "s_cbranch_scc1 .LCY_BAR_%=\n" \
  "s_sleep 4\n" \
  "ds_read_b32 v6, v5 offset:16\n" \
  "s_waitcnt lgkmcnt(0)\n" \
  "v_readfirstlane_b32 s63, v6\n" \
  "s_add_u32 s63, s63, s60\n" \
  "s_cmp_ge_u32 s63, 66\n" \
  "s_cbranch_scc1 .LCY_BAR_%=\n" \
  ".LCY_ASK_%=:\n" \
  "v_mov_b32 v4, 0\n" \
  "buffer_load_dword v4, v60, s[44:47], s67 offen sc1\n" \
  "s_sub_u32 s62, s62, 1\n" \
  "s_cbranch_scc1 .LCY_END_%=\n" \
  "s_waitcnt vmcnt(0)\n" \
  "s_branch .LCY_NEED_%=\n" \
  ".LCY_GET_%=:\n" \
  "v_lshlrev_b32 v6, 16, s55\n" \
  "s_mov_b64 exec, 1\n" \
  "ds_add_u32 v5, v6\n" \
  "s_mov_b64 exec, -1\n" \
  "s_mov_b32 m0, s56\n" \
  "s_add_u32 s71, s56, 4096\n" \
  "global_load_lds_dwordx4 v2, s[48:49] offset:0 sc1\n" \
  "global_load_lds_dwordx4 v2, s[48:49] offset:1024 sc1\n" \
  "global_load_lds_dwordx4 v2, s[48:49] offset:2048 sc1\n" \
  "global_load_lds_dwordx4 v2, s[48:49] offset:3072 sc1\n" \
  "s_mov_b32 m0, s71\n" \
  "s_add_u32 s56, s56, s65\n" \
  "global_load_lds_dwordx4 v3, s[48:49] offset:0 sc1\n" \
  "global_load_lds_dwordx4 v3, s[48:49] offset:1024 sc1\n" \
  "global_load_lds_dwordx4 v3, s[48:49] offset:2048 sc1\n" \
  "global_load_lds_dwordx4 v3, s[48:49] offset:3072 sc1\n" \
  "s_add_u32 s48, s48, 0x80000\n" \
  "s_addc_u32 s49, s49, 0\n" \
  "s_mov_b32 m0, s57\n" \
  "s_add_u32 s57, s57, s65\n" \
  "global_load_lds_dwordx4 v2, s[50:51] offset:0 sc1\n" \
  "global_load_lds_dwordx4 v2, s[50:51] offset:1024 sc1\n" \
  "s_sub_u32 s65, 0, s65\n" \
  "s_add_u32 s67, s67, 4096\n" \
  "global_load_lds_dwordx4 v2, s[50:51] offset:2048 sc1\n" \
  "s_sub_u32 s68, s68, 1\n" \
  "s_mov_b32 s69, 1\n" \
  "global_load_lds_dwordx4 v2, s[50:51] offset:3072 sc1\n" \
  "s_add_u32 s50, s50, 0x40000\n" \
  "s_addc_u32 s51, s51, 0\n" \
  "v_mov_b32 v4, 0\n" \
  "buffer_load_dword v4, v60, s[44:47], s67 offen sc1\n" \
  "s_mov_b32 s61, 0\n" \
  "s_cmp_eq_u32 s68, 0\n" \
  "s_cbranch_scc1 .LCY_BAR_%=\n" \
  "s_waitcnt vmcnt(0)\n" \
  "v_mov_b32 v6, s55\n" \
  "s_mov_b64 exec, 1\n" \
  "ds_add_u32 v5, v6\n" \
  "s_mov_b64 exec, -1\n" \
  "s_mov_b32 s69, 0\n" \
  "s_branch .LCY_NEED_%=\n" \
  ".LCY_END_%=:\n" \
  "s_endpgm\n"
template <bool AT>
__device__ __forceinline__ void role_aw_t(const MegaArgs& A, char* smem, const int L, const int a, const u64 tag) {
  const int w = A.wv;
  const int l = lane_id(), c = l & 15, g = l >> 4;
  const int qt = w & 1, hh = (w >> 1) & 1, h = 2 * a + hh;
  char* ws = A.ws;
  unsigned* FL = (unsigned*)(ws + OFF_FL);
  const unsigned sb = lds_base(smem);
  if (w == 0 && l < 8) reinterpret_cast<unsigned*>(smem + AW_CNT)[l] = 0u;
  constexpr int NP = AT ? 1 : 3;
  const int cp0 = AT ? w : 3 * w - 8;
  v8u wo[2 * NP][4];
  {
    const int q = (l >> 2) & 3, p = l & 3, f = q | ((g & 1) << 2);
    const unsigned ra = sb + (4 * g + q) * 256 + (p & 1) * 16;
#pragma unroll
    for (int r = 0; r < 2; ++r) {
#pragma unroll
      for (int k = 0; k < 2; ++k) {
        const char* base_ = (const char*)A.Wo + ((size_t)((2 * a + r) * DH + 64 * (w & 1) + 32 * k) * DM + 128 * (w >> 1)) * 2;
#pragma unroll
        for (int i = 0; i < 8; ++i) {
          const int fd = g | ((i & 1) << 2);
          glds16(base_, (unsigned)((4 * i + g) * (DM * 2) + ((c ^ (fd << 1)) << 4)), sb + w * 16384 + k * 8192 + i * 1024);
        }
      }
      SST(4 + 3 * r);
      if (r == 1) ready_wait(A, tag, ready_look(A));
      WAIT_VM(0);
      RAW_BAR();
      SST(5 + 3 * r);
#pragma unroll
      for (int k2 = 0; k2 < 2; ++k2) {
#pragma unroll
        for (int ct = 0; ct < 2 * NP; ++ct) {
          const int cp = cp0 + (ct >> 1);
          const unsigned a_ = ra + (cp >> 2) * 32768 + k2 * 16384 + ((2 * (cp & 3) + (p >> 1)) ^ f) * 32 + (ct & 1) * 8;
          const v2u r0 = trd(a_), r1 = trd(a_ + 4096), r2 = trd(a_ + 8192), r3 = trd(a_ + 12288);
          wo[ct][2 * r + k2] = v8u{r0.x, r0.y, r1.x, r1.y, r2.x, r2.y, r3.x, r3.y};
          asm volatile("" : "+v"(wo[ct][2 * r + k2]));
        }
      }
      RAW_BAR();
      SST(6 + 3 * r);
    }
  }
  WSTAMP2();
  const v4u rsW = mk_srd(ws + OFF_PW, SZ_PW);
  const int dp = (FL_FP - FL_FQ + (L * 16 + a - L * 64 * 32 - h) * 32 + 2 * hh) * 4;
  const v4u rsF = mk_srd(FL + FL_FQ + (L * 64 * 32 + h) * 32, (qt ? 0 : dp) + 64 * 4096 - 128);
  const char* p0 = AT ? ws + OFF_MEMK + L * SZ_MEM + h * 2048 : ws + OFF_QK + L * SZ_QK + ((size_t)h * 16 + 8 * qt) * 1024;
  const char* p1 = AT ? (const char*)(FL + FL_FK + L * 32 + (h >> 3) * 64 + (h & 7) * 2) : ws + OFF_VF + L * SZ_O + ((size_t)h * 8 + 4 * qt) * 1024;
  const unsigned* pFW = FL + FL_FW + (L * 64 * 16 + (a >> 2)) * 32 + (a & 3);
  const unsigned tk = sb + AW_TK + hh * 24 * 1024, voff = (c >> 1) * 2048 + (c & 1) * 64 + g * 16;
  const char* dbg = ws + WS_TOTAL + A.vb * 64 * 8;
#define AW_SOPS                                                                                                                 \
  "{s[40:43]}"(rsW), "{s[44:47]}"(rsF), "{s[48:49]}"(p0), "{s[50:51]}"(p1), "{s[52:53]}"(pFW),                                   \
      "{s54}"((L * 64 * 16 + a) * 32768 + cp0 * 128 + (AT ? qt * 16384 : 0)), "{s55}"(1 << (8 * qt)), "{s56}"(tk + qt * 8192), "{s57}"(tk + 16384 + qt * 4096),                  \
      "{s58}"(sb + AW_CNT + 8 + 4 * hh), "{s59}"(sb + AW_CNT), "{s60}"(NSEG), "{s[36:37]}"(dbg)                                  \
      : "memory", "vcc", "scc", "s80"
  if constexpr (AT) {
    const v8u pv = {tk + l * 16, (c & 8) ? 0xEEEEu : 0x4444u, 0u, (unsigned)(OFF_MEMV - OFF_MEMK) + c * 32 + (g & 1) * 16 + (g >> 1) * 1024,
                    voff, (unsigned)l * 16, tk + qt * 4096 + l * 16, sb + AW_SO + w * 4096 + l * 16};
    const unsigned neg = 0xff61b1e6u;
    v8u bt;
#pragma unroll
    for (int i = 0; i < 4; ++i) {
      const unsigned d = 4 * g + i > c ? neg : 0u;
      bt[i] = qt ? 0u : d;
      bt[4 + i] = qt ? d : neg;
    }
    asm volatile("s_mov_b64 exec, -1\ns_waitcnt vmcnt(0) lgkmcnt(0)\ns_nop 4\n" AW_ASM_X
                 ::"{v[192:199]}"(wo[0][0]), "{v[200:207]}"(wo[0][1]), "{v[208:215]}"(wo[0][2]), "{v[216:223]}"(wo[0][3]),
                 "{v[224:231]}"(wo[1][0]), "{v[232:239]}"(wo[1][1]), "{v[240:247]}"(wo[1][2]), "{v[248:255]}"(wo[1][3]),
                 "{v[0:7]}"(pv), "{v[8:15]}"(bt), AW_SOPS);
  } else {
    const v4u pv = {sb + AW_SO + l * 16, voff, (unsigned)l * 16, (l & 2) ? 16u + 8 * qt : qt ? 8u * (l & 1) : dp + 4 * (l & 1)};
    asm volatile("s_mov_b64 exec, -1\ns_waitcnt vmcnt(0) lgkmcnt(0)\ns_nop 4\n" AW_ASM_Y
                 ::"{v[64:71]}"(wo[0][0]), "{v[72:79]}"(wo[0][1]), "{v[80:87]}"(wo[0][2]), "{v[88:95]}"(wo[0][3]),
                 "{v[96:103]}"(wo[1][0]), "{v[104:111]}"(wo[1][1]), "{v[112:119]}"(wo[1][2]), "{v[120:127]}"(wo[1][3]),
                 "{v[128:135]}"(wo[2][0]), "{v[136:143]}"(wo[2][1]), "{v[144:151]}"(wo[2][2]), "{v[152:159]}"(wo[2][3]),
                 "{v[160:167]}"(wo[3][0]), "{v[168:175]}"(wo[3][1]), "{v[176:183]}"(wo[3][2]), "{v[184:191]}"(wo[3][3]),
                 "{v[192:199]}"(wo[4][0]), "{v[200:207]}"(wo[4][1]), "{v[208:215]}"(wo[4][2]), "{v[216:223]}"(wo[4][3]),
                 "{v[224:231]}"(wo[5][0]), "{v[232:239]}"(wo[5][1]), "{v[240:247]}"(wo[5][2]), "{v[248:255]}"(wo[5][3]),
                 "{v[0:3]}"(pv), AW_SOPS);
  }
#undef AW_SOPS
}
__device__ __forceinline__ void role_aw(const MegaArgs& A, char* smem, const int L, const int a, const u64 tag) {
  if (A.wv < 4) role_aw_t<true>(A, smem, L, a, tag);
  else role_aw_t<false>(A, smem, L, a, tag);
}

#define P_ASM \
  "s_mov_b32 s68, 0\n" \
  "s_mov_b32 s66, 3\n" \
  "s_mov_b32 s67, 0\n" \
  "s_mov_b32 s72, 0\n" \
  "v_mov_b32 v7, 1\n" \
  "v_mov_b32 v6, s70\n" \
  "v_mov_b32 v5, 0\n" \
  "v_add_u32 v10, 4096, v2\n" \
  ".LCP_TOP_%=:\n" \
  "ds_read_b128 v[28:31], v1 offset:0\n" \
  "ds_read_b128 v[32:35], v1 offset:1024\n" \
  "s_add_u32 s66, s66, 3\n" \
  "s_add_u32 s67, s67, 1\n" \
  "s_mov_b32 s69, 0x400000\n" \
  "v_xor_b32 v0, 0x8000, v0\n" \
  "v_xor_b32 v3, 0x100, v3\n" \
  "v_readfirstlane_b32 s71, v5\n" \
  "s_cmp_ge_u32 s71, s66\n" \
  "s_cbranch_scc1 .LCP_GO_%=\n" \
  "s_cmp_eq_u32 s68, 1\n" \
  "s_cbranch_scc0 .LCP_X1_%=\n" \
  "s_waitcnt vmcnt(0)\n" \
  "s_mov_b64 exec, 1\n" \
  "buffer_store_dword v7, off, s[56:59], s72 sc1\n" \
  "s_mov_b64 exec, -1\n" \
  "s_add_u32 s72, s72, 0x1000\n" \
  "s_mov_b32 s68, 0\n" \
  ".LCP_X1_%=:\n" \
  ".LCP_S2_%=:\n" \
  "s_sleep 1\n" \
  "ds_read_b32 v5, v6 offset:0\n" \
  "s_sub_u32 s69, s69, 1\n" \
  "s_cbranch_scc1 .LCP_END_%=\n" \
  "s_waitcnt lgkmcnt(0)\n" \
  "v_readfirstlane_b32 s71, v5\n" \
  "s_cmp_ge_u32 s71, s66\n" \
  "s_cbranch_scc1 .LCP_GO_%=\n" \
  "s_branch .LCP_S2_%=\n" \
  ".LCP_GO_%=:\n" \
  "ds_read_b128 v[12:15], v0 offset:0\n" \
  "ds_read_b128 v[16:19], v0 offset:2048\n" \
  "ds_read_b128 v[20:23], v0 offset:4096\n" \
  "ds_read_b128 v[24:27], v0 offset:6144\n" \
  "ds_read_b128 v[36:39], v1 offset:2048\n" \
  "s_waitcnt lgkmcnt(4)\n" \
  "v_mfma_f32_16x16x32_bf16 v[48:51], v[64:67], v[12:15], 0\n" \
  "v_mfma_f32_16x16x32_bf16 v[52:55], v[128:131], v[12:15], 0\n" \
  "v_mfma_f32_16x16x32_bf16 v[56:59], v[192:195], v[12:15], 0\n" \
  "v_mfma_f32_16x16x32_bf16 v[60:63], v[28:31], v[12:15], 0\n" \
  "ds_read_b128 v[12:15], v0 offset:8192\n" \
  "ds_read_b128 v[28:31], v1 offset:3072\n" \
  "s_waitcnt lgkmcnt(5)\n" \
  "v_mfma_f32_16x16x32_bf16 v[48:51], v[68:71], v[16:19], v[48:51]\n" \
  "v_mfma_f32_16x16x32_bf16 v[52:55], v[132:135], v[16:19], v[52:55]\n" \
  "v_mfma_f32_16x16x32_bf16 v[56:59], v[196:199], v[16:19], v[56:59]\n" \
  "v_mfma_f32_16x16x32_bf16 v[60:63], v[32:35], v[16:19], v[60:63]\n" \
  "ds_read_b128 v[16:19], v0 offset:10240\n" \
  "ds_read_b128 v[32:35], v1 offset:4096\n" \
  "s_waitcnt lgkmcnt(6)\n" \
  "v_mfma_f32_16x16x32_bf16 v[48:51], v[72:75], v[20:23], v[48:51]\n" \
  "v_mfma_f32_16x16x32_bf16 v[52:55], v[136:139], v[20:23], v[52:55]\n" \
  "v_mfma_f32_16x16x32_bf16 v[56:59], v[200:203], v[20:23], v[56:59]\n" \
  "s_waitcnt lgkmcnt(4)\n" \
  "v_mfma_f32_16x16x32_bf16 v[60:63], v[36:39], v[20:23], v[60:63]\n" \
  "ds_read_b128 v[20:23], v0 offset:12288\n" \
  "ds_read_b128 v[36:39], v1 offset:5120\n" \
  "v_mfma_f32_16x16x32_bf16 v[48:51], v[76:79], v[24:27], v[48:51]\n" \
  "v_mfma_f32_16x16x32_bf16 v[52:55], v[140:143], v[24:27], v[52:55]\n" \
  "v_mfma_f32_16x16x32_bf16 v[56:59], v[204:207], v[24:27], v[56:59]\n" \
  "s_waitcnt lgkmcnt(4)\n" \
  "v_mfma_f32_16x16x32_bf16 v[60:63], v[28:31], v[24:27], v[60:63]\n" \
  "ds_read_b128 v[24:27], v0 offset:14336\n" \
  "ds_read_b128 v[28:31], v1 offset:6144\n" \
  "v_mfma_f32_16x16x32_bf16 v[48:51], v[80:83], v[12:15], v[48:51]\n" \
  "v_mfma_f32_16x16x32_bf16 v[52:55], v[144:147], v[12:15], v[52:55]\n" \
  "v_mfma_f32_16x16x32_bf16 v[56:59], v[208:211], v[12:15], v[56:59]\n" \
  "s_waitcnt lgkmcnt(4)\n" \
  "v_mfma_f32_16x16x32_bf16 v[60:63], v[32:35], v[12:15], v[60:63]\n" \
  "ds_read_b128 v[12:15], v0 offset:16384\n" \
  "ds_read_b128 v[32:35], v1 offset:7168\n" \
  "v_mfma_f32_16x16x32_bf16 v[48:51], v[84:87], v[16:19], v[48:51]\n" \
  "v_mfma_f32_16x16x32_bf16 v[52:55], v[148:151], v[16:19], v[52:55]\n" \
  "v_mfma_f32_16x16x32_bf16 v[56:59], v[212:215], v[16:19], v[56:59]\n" \
  "s_waitcnt lgkmcnt(4)\n" \
  "v_mfma_f32_16x16x32_bf16 v[60:63], v[36:39], v[16:19], v[60:63]\n" \
  "ds_read_b128 v[16:19], v0 offset:18432\n" \
  "ds_read_b128 v[36:39], v1 offset:8192\n" \
  "v_mfma_f32_16x16x32_bf16 v[48:51], v[88:91], v[20:23], v[48:51]\n" \
  "v_mfma_f32_16x16x32_bf16 v[52:55], v[152:155], v[20:23], v[52:55]\n" \
  "v_mfma_f32_16x16x32_bf16 v[56:59], v[216:219], v[20:23], v[56:59]\n" \
  "s_waitcnt lgkmcnt(4)\n" \
  "v_mfma_f32_16x16x32_bf16 v[60:63], v[28:31], v[20:23], v[60:63]\n" \
  "ds_read_b128 v[20:23], v0 offset:20480\n" \
  "ds_read_b128 v[28:31], v1 offset:9216\n" \
  "v_mfma_f32_16x16x32_bf16 v[48:51], v[92:95], v[24:27], v[48:51]\n" \
  "v_mfma_f32_16x16x32_bf16 v[52:55], v[156:159], v[24:27], v[52:55]\n" \
  "v_mfma_f32_16x16x32_bf16 v[56:59], v[220:223], v[24:27], v[56:59]\n" \
  "s_waitcnt lgkmcnt(4)\n" \
  "v_mfma_f32_16x16x32_bf16 v[60:63], v[32:35], v[24:27], v[60:63]\n" \
  "ds_read_b128 v[24:27], v0 offset:22528\n" \
  "ds_read_b128 v[32:35], v1 offset:10240\n" \
  "v_mfma_f32_16x16x32_bf16 v[48:51], v[96:99], v[12:15], v[48:51]\n" \
  "v_mfma_f32_16x16x32_bf16 v[52:55], v[160:163], v[12:15], v[52:55]\n" \
  "v_mfma_f32_16x16x32_bf16 v[56:59], v[224:227], v[12:15], v[56:59]\n" \
  "s_waitcnt lgkmcnt(4)\n" \
  "v_mfma_f32_16x16x32_bf16 v[60:63], v[36:39], v[12:15], v[60:63]\n" \
  "ds_read_b128 v[12:15], v0 offset:24576\n" \
  "ds_read_b128 v[36:39], v1 offset:11264\n" \
  "v_mfma_f32_16x16x32_bf16 v[48:51], v[100:103], v[16:19], v[48:51]\n" \
  "v_mfma_f32_16x16x32_bf16 v[52:55], v[164:167], v[16:19], v[52:55]\n" \
  "v_mfma_f32_16x16x32_bf16 v[56:59], v[228:231], v[16:19], v[56:59]\n" \
  "s_waitcnt lgkmcnt(4)\n" \
  "v_mfma_f32_16x16x32_bf16 v[60:63], v[28:31], v[16:19], v[60:63]\n" \
  "ds_read_b128 v[16:19], v0 offset:26624\n" \
  "ds_read_b128 v[28:31], v1 offset:12288\n" \
  "v_mfma_f32_16x16x32_bf16 v[48:51], v[104:107], v[20:23], v[48:51]\n" \
  "v_mfma_f32_16x16x32_bf16 v[52:55], v[168:171], v[20:23], v[52:55]\n" \
  "v_mfma_f32_16x16x32_bf16 v[56:59], v[232:235], v[20:23], v[56:59]\n" \
  "s_waitcnt lgkmcnt(4)\n" \
  "v_mfma_f32_16x16x32_bf16 v[60:63], v[32:35], v[20:23], v[60:63]\n" \
  "ds_read_b128 v[20:23], v0 offset:28672\n" \
  "ds_read_b128 v[32:35], v1 offset:13312\n" \
  "v_mfma_f32_16x16x32_bf16 v[48:51], v[108:111], v[24:27], v[48:51]\n" \
  "v_mfma_f32_16x16x32_bf16 v[52:55], v[172:175], v[24:27], v[52:55]\n" \
  "v_mfma_f32_16x16x32_bf16 v[56:59], v[236:239], v[24:27], v[56:59]\n" \
  "s_waitcnt lgkmcnt(4)\n" \
  "v_mfma_f32_16x16x32_bf16 v[60:63], v[36:39], v[24:27], v[60:63]\n" \
  "s_cmp_eq_u32 s68, 1\n" \
  "s_cbranch_scc0 .LCP_X3_%=\n" \
  "s_waitcnt vmcnt(0)\n" \
  "s_mov_b64 exec, 1\n" \
  "buffer_store_dword v7, off, s[56:59], s72 sc1\n" \
  "s_mov_b64 exec, -1\n" \
  "s_add_u32 s72, s72, 0x1000\n" \
  "s_mov_b32 s68, 0\n" \
  ".LCP_X3_%=:\n" \
  "ds_read_b32 v5, v6 offset:16\n" \
  "ds_read_b128 v[24:27], v0 offset:30720\n" \
  "ds_read_b128 v[36:39], v1 offset:14336\n" \
  "v_mfma_f32_16x16x32_bf16 v[48:51], v[112:115], v[12:15], v[48:51]\n" \
  "v_mfma_f32_16x16x32_bf16 v[52:55], v[176:179], v[12:15], v[52:55]\n" \
  "v_mfma_f32_16x16x32_bf16 v[56:59], v[240:243], v[12:15], v[56:59]\n" \
  "s_waitcnt lgkmcnt(5)\n" \
  "v_mfma_f32_16x16x32_bf16 v[60:63], v[28:31], v[12:15], v[60:63]\n" \
  "ds_read_b128 v[12:15], v0 offset:256\n" \
  "ds_read_b128 v[28:31], v1 offset:15360\n" \
  "ds_read_b128 v[40:43], v4 offset:0\n" \
  "ds_read_b128 v[44:47], v4 offset:64\n" \
  "v_mfma_f32_16x16x32_bf16 v[48:51], v[116:119], v[16:19], v[48:51]\n" \
  "v_mfma_f32_16x16x32_bf16 v[52:55], v[180:183], v[16:19], v[52:55]\n" \
  "v_mfma_f32_16x16x32_bf16 v[56:59], v[244:247], v[16:19], v[56:59]\n" \
  "s_waitcnt lgkmcnt(7)\n" \
  "v_mfma_f32_16x16x32_bf16 v[60:63], v[32:35], v[16:19], v[60:63]\n" \
  "s_waitcnt lgkmcnt(6)\n" \
  "v_readfirstlane_b32 s71, v5\n" \
  "s_cmp_ge_u32 s71, s67\n" \
  "s_cbranch_scc1 .LCP_ST_%=\n" \
  ".LCP_S4_%=:\n" \
  "s_sleep 1\n" \
  "ds_read_b32 v5, v6 offset:16\n" \
  "s_sub_u32 s69, s69, 1\n" \
  "s_cbranch_scc1 .LCP_END_%=\n" \
  "s_waitcnt lgkmcnt(0)\n" \
  "v_readfirstlane_b32 s71, v5\n" \
  "s_cmp_ge_u32 s71, s67\n" \
  "s_cbranch_scc1 .LCP_ST_%=\n" \
  "s_branch .LCP_S4_%=\n" \
  ".LCP_ST_%=:\n" \
  "ds_read_b128 v[16:19], v0 offset:2304\n" \
  "ds_read_b128 v[32:35], v4 offset:128\n" \
  "ds_read2_b32 v[8:9], v3 offset0:0 offset1:32\n" \
  "v_mfma_f32_16x16x32_bf16 v[48:51], v[120:123], v[20:23], v[48:51]\n" \
  "v_mfma_f32_16x16x32_bf16 v[52:55], v[184:187], v[20:23], v[52:55]\n" \
  "v_mfma_f32_16x16x32_bf16 v[56:59], v[248:251], v[20:23], v[56:59]\n" \
  "s_waitcnt lgkmcnt(7)\n" \
  "v_mfma_f32_16x16x32_bf16 v[60:63], v[36:39], v[20:23], v[60:63]\n" \
  "ds_read_b128 v[20:23], v0 offset:4352\n" \
  "ds_read_b128 v[36:39], v4 offset:192\n" \
  "v_mfma_f32_16x16x32_bf16 v[48:51], v[124:127], v[24:27], v[48:51]\n" \
  "v_mfma_f32_16x16x32_bf16 v[52:55], v[188:191], v[24:27], v[52:55]\n" \
  "v_mfma_f32_16x16x32_bf16 v[56:59], v[252:255], v[24:27], v[56:59]\n" \
  "s_waitcnt lgkmcnt(7)\n" \
  "v_mfma_f32_16x16x32_bf16 v[60:63], v[28:31], v[24:27], v[60:63]\n" \
  "s_waitcnt lgkmcnt(0)\n" \
  "s_nop 4\n" \
  "v_fma_f32 v48, -v8, v40, v48\n" \
  "v_fma_f32 v49, -v8, v41, v49\n" \
  "v_fma_f32 v50, -v8, v42, v50\n" \
  "v_fma_f32 v51, -v8, v43, v51\n" \
  "v_fma_f32 v52, -v8, v44, v52\n" \
  "v_fma_f32 v53, -v8, v45, v53\n" \
  "v_fma_f32 v54, -v8, v46, v54\n" \
  "v_fma_f32 v55, -v8, v47, v55\n" \
  "v_fma_f32 v56, -v8, v32, v56\n" \
  "v_fma_f32 v57, -v8, v33, v57\n" \
  "v_fma_f32 v58, -v8, v34, v58\n" \
  "v_fma_f32 v59, -v8, v35, v59\n" \
  "v_fma_f32 v60, -v8, v36, v60\n" \
  "v_fma_f32 v61, -v8, v37, v61\n" \
  "v_fma_f32 v62, -v8, v38, v62\n" \
  "v_fma_f32 v63, -v8, v39, v63\n" \
  "ds_read_b128 v[32:35], v1 offset:0\n" \
  "ds_read_b128 v[36:39], v1 offset:1024\n" \
  "v_mul_f32 v48, v48, v9\n" \
  "v_mul_f32 v49, v49, v9\n" \
  "v_mul_f32 v50, v50, v9\n" \
  "v_mul_f32 v51, v51, v9\n" \
  "v_mul_f32 v52, v52, v9\n" \
  "v_mul_f32 v53, v53, v9\n" \
  "v_mul_f32 v54, v54, v9\n" \
  "v_mul_f32 v55, v55, v9\n" \
  "v_cvt_pk_bf16_f32 v40, v48, v49\n" \
  "v_cvt_pk_bf16_f32 v41, v50, v51\n" \
  "v_cvt_pk_bf16_f32 v42, v52, v53\n" \
  "v_cvt_pk_bf16_f32 v43, v54, v55\n" \
  "buffer_store_dwordx4 v[40:43], v2, s[40:43], s65 offen offset:0 sc1\n" \
  "v_mul_f32 v56, v56, v9\n" \
  "v_mul_f32 v57, v57, v9\n" \
  "v_mul_f32 v58, v58, v9\n" \
  "v_mul_f32 v59, v59, v9\n" \
  "v_mul_f32 v60, v60, v9\n" \
  "v_mul_f32 v61, v61, v9\n" \
  "v_mul_f32 v62, v62, v9\n" \
  "v_mul_f32 v63, v63, v9\n" \
  "v_cvt_pk_bf16_f32 v44, v56, v57\n" \
  "v_cvt_pk_bf16_f32 v45, v58, v59\n" \
  "v_cvt_pk_bf16_f32 v46, v60, v61\n" \
  "v_cvt_pk_bf16_f32 v47, v62, v63\n" \
  "buffer_store_dwordx4 v[44:47], v2, s[40:43], s65 offen offset:1024 sc1\n" \
  "ds_read_b128 v[24:27], v0 offset:6400\n" \
  "ds_read_b128 v[28:31], v1 offset:2048\n" \
  "v_mfma_f32_16x16x32_bf16 v[48:51], v[64:67], v[12:15], 0\n" \
  "v_mfma_f32_16x16x32_bf16 v[52:55], v[128:131], v[12:15], 0\n" \
  "v_mfma_f32_16x16x32_bf16 v[56:59], v[192:195], v[12:15], 0\n" \
  "s_waitcnt lgkmcnt(3)\n" \
  "v_mfma_f32_16x16x32_bf16 v[60:63], v[32:35], v[12:15], 0\n" \
  "ds_read_b128 v[12:15], v0 offset:8448\n" \
  "ds_read_b128 v[32:35], v1 offset:3072\n" \
  "v_mfma_f32_16x16x32_bf16 v[48:51], v[68:71], v[16:19], v[48:51]\n" \
  "v_mfma_f32_16x16x32_bf16 v[52:55], v[132:135], v[16:19], v[52:55]\n" \
  "v_mfma_f32_16x16x32_bf16 v[56:59], v[196:199], v[16:19], v[56:59]\n" \
  "s_waitcnt lgkmcnt(4)\n" \
  "v_mfma_f32_16x16x32_bf16 v[60:63], v[36:39], v[16:19], v[60:63]\n" \
  "ds_read_b128 v[16:19], v0 offset:10496\n" \
  "ds_read_b128 v[36:39], v1 offset:4096\n" \
  "v_mfma_f32_16x16x32_bf16 v[48:51], v[72:75], v[20:23], v[48:51]\n" \
  "v_mfma_f32_16x16x32_bf16 v[52:55], v[136:139], v[20:23], v[52:55]\n" \
  "v_mfma_f32_16x16x32_bf16 v[56:59], v[200:203], v[20:23], v[56:59]\n" \
  "s_waitcnt lgkmcnt(4)\n" \
  "v_mfma_f32_16x16x32_bf16 v[60:63], v[28:31], v[20:23], v[60:63]\n" \
  "ds_read_b128 v[20:23], v0 offset:12544\n" \
  "ds_read_b128 v[28:31], v1 offset:5120\n" \
  "v_mfma_f32_16x16x32_bf16 v[48:51], v[76:79], v[24:27], v[48:51]\n" \
  "v_mfma_f32_16x16x32_bf16 v[52:55], v[140:143], v[24:27], v[52:55]\n" \
  "v_mfma_f32_16x16x32_bf16 v[56:59], v[204:207], v[24:27], v[56:59]\n" \
  "s_waitcnt lgkmcnt(4)\n" \
  "v_mfma_f32_16x16x32_bf16 v[60:63], v[32:35], v[24:27], v[60:63]\n" \
  "ds_read_b128 v[24:27], v0 offset:14592\n" \
  "ds_read_b128 v[32:35], v1 offset:6144\n" \
  "v_mfma_f32_16x16x32_bf16 v[48:51], v[80:83], v[12:15], v[48:51]\n" \
  "v_mfma_f32_16x16x32_bf16 v[52:55], v[144:147], v[12:15], v[52:55]\n" \
  "v_mfma_f32_16x16x32_bf16 v[56:59], v[208:211], v[12:15], v[56:59]\n" \
  "s_waitcnt lgkmcnt(4)\n" \
  "v_mfma_f32_16x16x32_bf16 v[60:63], v[36:39], v[12:15], v[60:63]\n" \
  "ds_read_b128 v[12:15], v0 offset:16640\n" \
  "ds_read_b128 v[36:39], v1 offset:7168\n" \
  "v_mfma_f32_16x16x32_bf16 v[48:51], v[84:87], v[16:19], v[48:51]\n" \
  "v_mfma_f32_16x16x32_bf16 v[52:55], v[148:151], v[16:19], v[52:55]\n" \
  "v_mfma_f32_16x16x32_bf16 v[56:59], v[212:215], v[16:19], v[56:59]\n" \
  "s_waitcnt lgkmcnt(4)\n" \
  "v_mfma_f32_16x16x32_bf16 v[60:63], v[28:31], v[16:19], v[60:63]\n" \
  "ds_read_b128 v[16:19], v0 offset:18688\n" \
  "ds_read_b128 v[28:31], v1 offset:8192\n" \
  "v_mfma_f32_16x16x32_bf16 v[48:51], v[88:91], v[20:23], v[48:51]\n" \
  "v_mfma_f32_16x16x32_bf16 v[52:55], v[152:155], v[20:23], v[52:55]\n" \
  "v_mfma_f32_16x16x32_bf16 v[56:59], v[216:219], v[20:23], v[56:59]\n" \
  "s_waitcnt lgkmcnt(4)\n" \
  "v_mfma_f32_16x16x32_bf16 v[60:63], v[32:35], v[20:23], v[60:63]\n" \
  "ds_read_b128 v[20:23], v0 offset:20736\n" \
  "ds_read_b128 v[32:35], v1 offset:9216\n" \
  "v_mfma_f32_16x16x32_bf16 v[48:51], v[92:95], v[24:27], v[48:51]\n" \
  "v_mfma_f32_16x16x32_bf16 v[52:55], v[156:159], v[24:27], v[52:55]\n" \
  "v_mfma_f32_16x16x32_bf16 v[56:59], v[220:223], v[24:27], v[56:59]\n" \
  "s_waitcnt lgkmcnt(4)\n" \
  "v_mfma_f32_16x16x32_bf16 v[60:63], v[36:39], v[24:27], v[60:63]\n" \
  "ds_read_b128 v[24:27], v0 offset:22784\n" \
  "ds_read_b128 v[36:39], v1 offset:10240\n" \
  "v_mfma_f32_16x16x32_bf16 v[48:51], v[96:99], v[12:15], v[48:51]\n" \
  "v_mfma_f32_16x16x32_bf16 v[52:55], v[160:163], v[12:15], v[52:55]\n" \
  "v_mfma_f32_16x16x32_bf16 v[56:59], v[224:227], v[12:15], v[56:59]\n" \
  "s_waitcnt lgkmcnt(4)\n" \
  "v_mfma_f32_16x16x32_bf16 v[60:63], v[28:31], v[12:15], v[60:63]\n" \
  "ds_read_b128 v[12:15], v0 offset:24832\n" \
  "ds_read_b128 v[28:31], v1 offset:11264\n" \
  "v_mfma_f32_16x16x32_bf16 v[48:51], v[100:103], v[16:19], v[48:51]\n" \
  "v_mfma_f32_16x16x32_bf16 v[52:55], v[164:167], v[16:19], v[52:55]\n" \
  "v_mfma_f32_16x16x32_bf16 v[56:59], v[228:231], v[16:19], v[56:59]\n" \
  "s_waitcnt lgkmcnt(4)\n" \
  "v_mfma_f32_16x16x32_bf16 v[60:63], v[32:35], v[16:19], v[60:63]\n" \
  "ds_read_b128 v[16:19], v0 offset:26880\n" \
  "ds_read_b128 v[32:35], v1 offset:12288\n" \
  "v_mfma_f32_16x16x32_bf16 v[48:51], v[104:107], v[20:23], v[48:51]\n" \
  "v_mfma_f32_16x16x32_bf16 v[52:55], v[168:171], v[20:23], v[52:55]\n" \
  "v_mfma_f32_16x16x32_bf16 v[56:59], v[232:235], v[20:23], v[56:59]\n" \
  "s_waitcnt lgkmcnt(4)\n" \
  "v_mfma_f32_16x16x32_bf16 v[60:63], v[36:39], v[20:23], v[60:63]\n" \
  "ds_read_b128 v[20:23], v0 offset:28928\n" \
  "ds_read_b128 v[36:39], v1 offset:13312\n" \
  "v_mfma_f32_16x16x32_bf16 v[48:51], v[108:111], v[24:27], v[48:51]\n" \
  "v_mfma_f32_16x16x32_bf16 v[52:55], v[172:175], v[24:27], v[52:55]\n" \
  "v_mfma_f32_16x16x32_bf16 v[56:59], v[236:239], v[24:27], v[56:59]\n" \
  "s_waitcnt lgkmcnt(4)\n" \
  "v_mfma_f32_16x16x32_bf16 v[60:63], v[28:31], v[24:27], v[60:63]\n" \
  "ds_read_b128 v[24:27], v0 offset:30976\n" \
  "ds_read_b128 v[28:31], v1 offset:14336\n" \
  "v_mfma_f32_16x16x32_bf16 v[48:51], v[112:115], v[12:15], v[48:51]\n" \
  "v_mfma_f32_16x16x32_bf16 v[52:55], v[176:179], v[12:15], v[52:55]\n" \
  "v_mfma_f32_16x16x32_bf16 v[56:59], v[240:243], v[12:15], v[56:59]\n" \
  "s_waitcnt lgkmcnt(4)\n" \
  "v_mfma_f32_16x16x32_bf16 v[60:63], v[32:35], v[12:15], v[60:63]\n" \
  "ds_read_b128 v[32:35], v1 offset:15360\n" \
  "ds_read_b128 v[40:43], v4 offset:0\n" \
  "ds_read_b128 v[44:47], v4 offset:64\n" \
  "v_mfma_f32_16x16x32_bf16 v[48:51], v[116:119], v[16:19], v[48:51]\n" \
  "v_mfma_f32_16x16x32_bf16 v[52:55], v[180:183], v[16:19], v[52:55]\n" \
  "v_mfma_f32_16x16x32_bf16 v[56:59], v[244:247], v[16:19], v[56:59]\n" \
  "s_waitcnt lgkmcnt(5)\n" \
  "v_mfma_f32_16x16x32_bf16 v[60:63], v[36:39], v[16:19], v[60:63]\n" \
  "ds_read_b128 v[36:39], v4 offset:128\n" \
  "ds_read2_b32 v[8:9], v3 offset0:16 offset1:48\n" \
  "v_mfma_f32_16x16x32_bf16 v[48:51], v[120:123], v[20:23], v[48:51]\n" \
  "v_mfma_f32_16x16x32_bf16 v[52:55], v[184:187], v[20:23], v[52:55]\n" \
  "v_mfma_f32_16x16x32_bf16 v[56:59], v[248:251], v[20:23], v[56:59]\n" \
  "s_waitcnt lgkmcnt(5)\n" \
  "v_mfma_f32_16x16x32_bf16 v[60:63], v[28:31], v[20:23], v[60:63]\n" \
  "ds_read_b128 v[28:31], v4 offset:192\n" \
  "s_mov_b64 exec, 1\n" \
  "ds_add_u32 v6, v7 offset:8\n" \
  "s_mov_b64 exec, -1\n" \
  "v_xor_b32 v6, 4, v6\n" \
  "v_mfma_f32_16x16x32_bf16 v[48:51], v[124:127], v[24:27], v[48:51]\n" \
  "v_mfma_f32_16x16x32_bf16 v[52:55], v[188:191], v[24:27], v[52:55]\n" \
  "v_mfma_f32_16x16x32_bf16 v[56:59], v[252:255], v[24:27], v[56:59]\n" \
  "s_waitcnt lgkmcnt(6)\n" \
  "v_mfma_f32_16x16x32_bf16 v[60:63], v[32:35], v[24:27], v[60:63]\n" \
  "s_waitcnt lgkmcnt(1)\n" \
  "s_nop 4\n" \
  "ds_read_b32 v5, v6 offset:0\n" \
  "v_fma_f32 v48, -v8, v40, v48\n" \
  "v_fma_f32 v49, -v8, v41, v49\n" \
  "v_fma_f32 v50, -v8, v42, v50\n" \
  "v_fma_f32 v51, -v8, v43, v51\n" \
  "v_fma_f32 v52, -v8, v44, v52\n" \
  "v_fma_f32 v53, -v8, v45, v53\n" \
  "v_fma_f32 v54, -v8, v46, v54\n" \
  "v_fma_f32 v55, -v8, v47, v55\n" \
  "v_fma_f32 v56, -v8, v36, v56\n" \
  "v_fma_f32 v57, -v8, v37, v57\n" \
  "v_fma_f32 v58, -v8, v38, v58\n" \
  "v_fma_f32 v59, -v8, v39, v59\n" \
  "v_fma_f32 v60, -v8, v28, v60\n" \
  "v_fma_f32 v61, -v8, v29, v61\n" \
  "v_fma_f32 v62, -v8, v30, v62\n" \
  "v_fma_f32 v63, -v8, v31, v63\n" \
  "v_mul_f32 v48, v48, v9\n" \
  "v_mul_f32 v49, v49, v9\n" \
  "v_mul_f32 v50, v50, v9\n" \
  "v_mul_f32 v51, v51, v9\n" \
  "v_mul_f32 v52, v52, v9\n" \
  "v_mul_f32 v53, v53, v9\n" \
  "v_mul_f32 v54, v54, v9\n" \
  "v_mul_f32 v55, v55, v9\n" \
  "v_cvt_pk_bf16_f32 v40, v48, v49\n" \
  "v_cvt_pk_bf16_f32 v41, v50, v51\n" \
  "v_cvt_pk_bf16_f32 v42, v52, v53\n" \
  "v_cvt_pk_bf16_f32 v43, v54, v55\n" \
  "buffer_store_dwordx4 v[40:43], v10, s[40:43], s65 offen offset:0 sc1\n" \
  "v_mul_f32 v56, v56, v9\n" \
  "v_mul_f32 v57, v57, v9\n" \
  "v_mul_f32 v58, v58, v9\n" \
  "v_mul_f32 v59, v59, v9\n" \
  "v_mul_f32 v60, v60, v9\n" \
  "v_mul_f32 v61, v61, v9\n" \
  "v_mul_f32 v62, v62, v9\n" \
  "v_mul_f32 v63, v63, v9\n" \
  "v_cvt_pk_bf16_f32 v44, v56, v57\n" \
  "v_cvt_pk_bf16_f32 v45, v58, v59\n" \
  "v_cvt_pk_bf16_f32 v46, v60, v61\n" \
  "v_cvt_pk_bf16_f32 v47, v62, v63\n" \
  "buffer_store_dwordx4 v[44:47], v10, s[40:43], s65 offen offset:1024 sc1\n" \
  "s_waitcnt lgkmcnt(0)\n" \
  "s_add_u32 s65, s65, 0x80000\n" \
  "s_sub_u32 s64, s64, 1\n" \
  "s_mov_b32 s68, 1\n" \
  "s_cmp_lg_u32 s64, 0\n" \
  "s_cbranch_scc1 .LCP_TOP_%=\n" \
  "s_waitcnt vmcnt(0)\n" \
  "s_mov_b64 exec, 1\n" \
  "buffer_store_dword v7, off, s[56:59], s72 sc1\n" \
  "s_mov_b64 exec, -1\n" \
  "s_add_u32 s72, s72, 0x1000\n" \
  "s_mov_b32 s68, 0\n" \
  ".LCP_END_%=:\n" \
  "s_endpgm\n"
#define V_ASM \
  "s_mov_b32 s68, 0\n" \
  "s_mov_b32 s66, 3\n" \
  "s_mov_b32 s67, 0\n" \
  "s_mov_b32 s72, 0\n" \
  "v_mov_b32 v7, 1\n" \
  "v_mov_b32 v6, s70\n" \
  "v_mov_b32 v5, 0\n" \
  "ds_read_b32 v8, v4 offset:0\n" \
  "ds_read_b32 v9, v4 offset:64\n" \
  "ds_read_b32 v10, v4 offset:128\n" \
  "ds_read_b32 v4, v4 offset:192\n" \
  "s_waitcnt lgkmcnt(0)\n" \
  ".LCW_TOP_%=:\n" \
  "ds_read_b128 v[28:31], v1 offset:0\n" \
  "ds_read_b128 v[32:35], v1 offset:1024\n" \
  "s_add_u32 s66, s66, 3\n" \
  "s_add_u32 s67, s67, 1\n" \
  "s_mov_b32 s69, 0x400000\n" \
  "v_xor_b32 v0, 0x8000, v0\n" \
  "v_xor_b32 v3, 0x100, v3\n" \
  "v_readfirstlane_b32 s71, v5\n" \
  "s_cmp_ge_u32 s71, s66\n" \
  "s_cbranch_scc1 .LCW_GO_%=\n" \
  "s_cmp_eq_u32 s68, 1\n" \
  "s_cbranch_scc0 .LCW_X1_%=\n" \
  "s_waitcnt vmcnt(0)\n" \
  "s_mov_b64 exec, 1\n" \
  "buffer_store_dword v7, off, s[56:59], s72 sc1\n" \
  "s_mov_b64 exec, -1\n" \
  "s_add_u32 s72, s72, 0x1000\n" \
  "s_mov_b32 s68, 0\n" \
  ".LCW_X1_%=:\n" \
  ".LCW_S2_%=:\n" \
  "s_sleep 1\n" \
  "ds_read_b32 v5, v6 offset:0\n" \
  "s_sub_u32 s69, s69, 1\n" \
  "s_cbranch_scc1 .LCW_END_%=\n" \
  "s_waitcnt lgkmcnt(0)\n" \
  "v_readfirstlane_b32 s71, v5\n" \
  "s_cmp_ge_u32 s71, s66\n" \
  "s_cbranch_scc1 .LCW_GO_%=\n" \
  "s_branch .LCW_S2_%=\n" \
  ".LCW_GO_%=:\n" \
  "ds_read_b128 v[12:15], v0 offset:0\n" \
  "ds_read_b128 v[16:19], v0 offset:2048\n" \
  "ds_read_b128 v[20:23], v0 offset:4096\n" \
  "ds_read_b128 v[24:27], v0 offset:6144\n" \
  "ds_read_b128 v[36:39], v1 offset:2048\n" \
  "s_waitcnt lgkmcnt(4)\n" \
  "v_mfma_f32_16x16x32_bf16 v[42:45], v[12:15], v[64:67], 0\n" \
  "v_mfma_f32_16x16x32_bf16 v[48:51], v[12:15], v[128:131], 0\n" \
  "v_mfma_f32_16x16x32_bf16 v[54:57], v[12:15], v[192:195], 0\n" \
  "v_mfma_f32_16x16x32_bf16 v[60:63], v[12:15], v[28:31], 0\n" \
  "ds_read_b128 v[12:15], v0 offset:8192\n" \
  "ds_read_b128 v[28:31], v1 offset:3072\n" \
  "s_waitcnt lgkmcnt(5)\n" \
  "v_mfma_f32_16x16x32_bf16 v[42:45], v[16:19], v[68:71], v[42:45]\n" \
  "v_mfma_f32_16x16x32_bf16 v[48:51], v[16:19], v[132:135], v[48:51]\n" \
  "v_mfma_f32_16x16x32_bf16 v[54:57], v[16:19], v[196:199], v[54:57]\n" \
  "v_mfma_f32_16x16x32_bf16 v[60:63], v[16:19], v[32:35], v[60:63]\n" \
  "ds_read_b128 v[16:19], v0 offset:10240\n" \
  "ds_read_b128 v[32:35], v1 offset:4096\n" \
  "s_waitcnt lgkmcnt(6)\n" \
  "v_mfma_f32_16x16x32_bf16 v[42:45], v[20:23], v[72:75], v[42:45]\n" \
  "v_mfma_f32_16x16x32_bf16 v[48:51], v[20:23], v[136:139], v[48:51]\n" \
  "v_mfma_f32_16x16x32_bf16 v[54:57], v[20:23], v[200:203], v[54:57]\n" \
  "s_waitcnt lgkmcnt(4)\n" \
  "v_mfma_f32_16x16x32_bf16 v[60:63], v[20:23], v[36:39], v[60:63]\n" \
  "ds_read_b128 v[20:23], v0 offset:12288\n" \
  "ds_read_b128 v[36:39], v1 offset:5120\n" \
  "v_mfma_f32_16x16x32_bf16 v[42:45], v[24:27], v[76:79], v[42:45]\n" \
  "v_mfma_f32_16x16x32_bf16 v[48:51], v[24:27], v[140:143], v[48:51]\n" \
  "v_mfma_f32_16x16x32_bf16 v[54:57], v[24:27], v[204:207], v[54:57]\n" \
  "s_waitcnt lgkmcnt(4)\n" \
  "v_mfma_f32_16x16x32_bf16 v[60:63], v[24:27], v[28:31], v[60:63]\n" \
  "ds_read_b128 v[24:27], v0 offset:14336\n" \
  "ds_read_b128 v[28:31], v1 offset:6144\n" \
  "v_mfma_f32_16x16x32_bf16 v[42:45], v[12:15], v[80:83], v[42:45]\n" \
  "v_mfma_f32_16x16x32_bf16 v[48:51], v[12:15], v[144:147], v[48:51]\n" \
  "v_mfma_f32_16x16x32_bf16 v[54:57], v[12:15], v[208:211], v[54:57]\n" \
  "s_waitcnt lgkmcnt(4)\n" \
  "v_mfma_f32_16x16x32_bf16 v[60:63], v[12:15], v[32:35], v[60:63]\n" \
  "ds_read_b128 v[12:15], v0 offset:16384\n" \
  "ds_read_b128 v[32:35], v1 offset:7168\n" \
  "v_mfma_f32_16x16x32_bf16 v[42:45], v[16:19], v[84:87], v[42:45]\n" \
  "v_mfma_f32_16x16x32_bf16 v[48:51], v[16:19], v[148:151], v[48:51]\n" \
  "v_mfma_f32_16x16x32_bf16 v[54:57], v[16:19], v[212:215], v[54:57]\n" \
  "s_waitcnt lgkmcnt(4)\n" \
  "v_mfma_f32_16x16x32_bf16 v[60:63], v[16:19], v[36:39], v[60:63]\n" \
  "ds_read_b128 v[16:19], v0 offset:18432\n" \
  "ds_read_b128 v[36:39], v1 offset:8192\n" \
  "v_mfma_f32_16x16x32_bf16 v[42:45], v[20:23], v[88:91], v[42:45]\n" \
  "v_mfma_f32_16x16x32_bf16 v[48:51], v[20:23], v[152:155], v[48:51]\n" \
  "v_mfma_f32_16x16x32_bf16 v[54:57], v[20:23], v[216:219], v[54:57]\n" \
  "s_waitcnt lgkmcnt(4)\n" \
  "v_mfma_f32_16x16x32_bf16 v[60:63], v[20:23], v[28:31], v[60:63]\n" \
  "ds_read_b128 v[20:23], v0 offset:20480\n" \
  "ds_read_b128 v[28:31], v1 offset:9216\n" \
  "v_mfma_f32_16x16x32_bf16 v[42:45], v[24:27], v[92:95], v[42:45]\n" \
  "v_mfma_f32_16x16x32_bf16 v[48:51], v[24:27], v[156:159], v[48:51]\n" \
  "v_mfma_f32_16x16x32_bf16 v[54:57], v[24:27], v[220:223], v[54:57]\n" \
  "s_waitcnt lgkmcnt(4)\n" \
  "v_mfma_f32_16x16x32_bf16 v[60:63], v[24:27], v[32:35], v[60:63]\n" \
  "ds_read_b128 v[24:27], v0 offset:22528\n" \
  "ds_read_b128 v[32:35], v1 offset:10240\n" \
  "v_mfma_f32_16x16x32_bf16 v[42:45], v[12:15], v[96:99], v[42:45]\n" \
  "v_mfma_f32_16x16x32_bf16 v[48:51], v[12:15], v[160:163], v[48:51]\n" \
  "v_mfma_f32_16x16x32_bf16 v[54:57], v[12:15], v[224:227], v[54:57]\n" \
  "s_waitcnt lgkmcnt(4)\n" \
  "v_mfma_f32_16x16x32_bf16 v[60:63], v[12:15], v[36:39], v[60:63]\n" \
  "ds_read_b128 v[12:15], v0 offset:24576\n" \
  "ds_read_b128 v[36:39], v1 offset:11264\n" \
  "v_mfma_f32_16x16x32_bf16 v[42:45], v[16:19], v[100:103], v[42:45]\n" \
  "v_mfma_f32_16x16x32_bf16 v[48:51], v[16:19], v[164:167], v[48:51]\n" \
  "v_mfma_f32_16x16x32_bf16 v[54:57], v[16:19], v[228:231], v[54:57]\n" \
  "s_waitcnt lgkmcnt(4)\n" \
  "v_mfma_f32_16x16x32_bf16 v[60:63], v[16:19], v[28:31], v[60:63]\n" \
  "ds_read_b128 v[16:19], v0 offset:26624\n" \
  "ds_read_b128 v[28:31], v1 offset:12288\n" \
  "v_mfma_f32_16x16x32_bf16 v[42:45], v[20:23], v[104:107], v[42:45]\n" \
  "v_mfma_f32_16x16x32_bf16 v[48:51], v[20:23], v[168:171], v[48:51]\n" \
  "v_mfma_f32_16x16x32_bf16 v[54:57], v[20:23], v[232:235], v[54:57]\n" \
  "s_waitcnt lgkmcnt(4)\n" \
  "v_mfma_f32_16x16x32_bf16 v[60:63], v[20:23], v[32:35], v[60:63]\n" \
  "ds_read_b128 v[20:23], v0 offset:28672\n" \
  "ds_read_b128 v[32:35], v1 offset:13312\n" \
  "v_mfma_f32_16x16x32_bf16 v[42:45], v[24:27], v[108:111], v[42:45]\n" \
  "v_mfma_f32_16x16x32_bf16 v[48:51], v[24:27], v[172:175], v[48:51]\n" \
  "v_mfma_f32_16x16x32_bf16 v[54:57], v[24:27], v[236:239], v[54:57]\n" \
  "s_waitcnt lgkmcnt(4)\n" \
  "v_mfma_f32_16x16x32_bf16 v[60:63], v[24:27], v[36:39], v[60:63]\n" \
  "s_cmp_eq_u32 s68, 1\n" \
  "s_cbranch_scc0 .LCW_X3_%=\n" \
  "s_waitcnt vmcnt(0)\n" \
  "s_mov_b64 exec, 1\n" \
  "buffer_store_dword v7, off, s[56:59], s72 sc1\n" \
  "s_mov_b64 exec, -1\n" \
  "s_add_u32 s72, s72, 0x1000\n" \
  "s_mov_b32 s68, 0\n" \
  ".LCW_X3_%=:\n" \
  "ds_read_b32 v5, v6 offset:16\n" \
  "ds_read_b128 v[24:27], v0 offset:30720\n" \
  "ds_read_b128 v[36:39], v1 offset:14336\n" \
  "v_mfma_f32_16x16x32_bf16 v[42:45], v[12:15], v[112:115], v[42:45]\n" \
  "v_mfma_f32_16x16x32_bf16 v[48:51], v[12:15], v[176:179], v[48:51]\n" \
  "v_mfma_f32_16x16x32_bf16 v[54:57], v[12:15], v[240:243], v[54:57]\n" \
  "s_waitcnt lgkmcnt(5)\n" \
  "v_mfma_f32_16x16x32_bf16 v[60:63], v[12:15], v[28:31], v[60:63]\n" \
  "ds_read_b128 v[12:15], v0 offset:256\n" \
  "ds_read_b128 v[28:31], v1 offset:15360\n" \
  "v_mfma_f32_16x16x32_bf16 v[42:45], v[16:19], v[116:119], v[42:45]\n" \
  "v_mfma_f32_16x16x32_bf16 v[48:51], v[16:19], v[180:183], v[48:51]\n" \
  "v_mfma_f32_16x16x32_bf16 v[54:57], v[16:19], v[244:247], v[54:57]\n" \
  "s_waitcnt lgkmcnt(5)\n" \
  "v_mfma_f32_16x16x32_bf16 v[60:63], v[16:19], v[32:35], v[60:63]\n" \
  "s_waitcnt lgkmcnt(4)\n" \
  "v_readfirstlane_b32 s71, v5\n" \
  "s_cmp_ge_u32 s71, s67\n" \
  "s_cbranch_scc1 .LCW_ST_%=\n" \
  ".LCW_S4_%=:\n" \
  "s_sleep 1\n" \
  "ds_read_b32 v5, v6 offset:16\n" \
  "s_sub_u32 s69, s69, 1\n" \
  "s_cbranch_scc1 .LCW_END_%=\n" \
  "s_waitcnt lgkmcnt(0)\n" \
  "v_readfirstlane_b32 s71, v5\n" \
  "s_cmp_ge_u32 s71, s67\n" \
  "s_cbranch_scc1 .LCW_ST_%=\n" \
  "s_branch .LCW_S4_%=\n" \
  ".LCW_ST_%=:\n" \
  "ds_read_b128 v[16:19], v0 offset:2304\n" \
  "ds_read_b128 v[32:35], v3 offset:0\n" \
  "v_mfma_f32_16x16x32_bf16 v[42:45], v[20:23], v[120:123], v[42:45]\n" \
  "v_mfma_f32_16x16x32_bf16 v[48:51], v[20:23], v[184:187], v[48:51]\n" \
  "v_mfma_f32_16x16x32_bf16 v[54:57], v[20:23], v[248:251], v[54:57]\n" \
  "s_waitcnt lgkmcnt(4)\n" \
  "v_mfma_f32_16x16x32_bf16 v[60:63], v[20:23], v[36:39], v[60:63]\n" \
  "ds_read_b128 v[20:23], v0 offset:4352\n" \
  "ds_read_b128 v[36:39], v3 offset:128\n" \
  "v_mfma_f32_16x16x32_bf16 v[42:45], v[24:27], v[124:127], v[42:45]\n" \
  "v_mfma_f32_16x16x32_bf16 v[48:51], v[24:27], v[188:191], v[48:51]\n" \
  "v_mfma_f32_16x16x32_bf16 v[54:57], v[24:27], v[252:255], v[54:57]\n" \
  "s_waitcnt lgkmcnt(4)\n" \
  "v_mfma_f32_16x16x32_bf16 v[60:63], v[24:27], v[28:31], v[60:63]\n" \
  "s_waitcnt lgkmcnt(0)\n" \
  "s_nop 4\n" \
  "v_fma_f32 v42, -v32, v8, v42\n" \
  "v_fma_f32 v43, -v33, v8, v43\n" \
  "v_fma_f32 v44, -v34, v8, v44\n" \
  "v_fma_f32 v45, -v35, v8, v45\n" \
  "v_fma_f32 v48, -v32, v9, v48\n" \
  "v_fma_f32 v49, -v33, v9, v49\n" \
  "v_fma_f32 v50, -v34, v9, v50\n" \
  "v_fma_f32 v51, -v35, v9, v51\n" \
  "v_fma_f32 v54, -v32, v10, v54\n" \
  "v_fma_f32 v55, -v33, v10, v55\n" \
  "v_fma_f32 v56, -v34, v10, v56\n" \
  "v_fma_f32 v57, -v35, v10, v57\n" \
  "v_fma_f32 v60, -v32, v4, v60\n" \
  "v_fma_f32 v61, -v33, v4, v61\n" \
  "v_fma_f32 v62, -v34, v4, v62\n" \
  "v_fma_f32 v63, -v35, v4, v63\n" \
  "ds_read_b128 v[32:35], v1 offset:0\n" \
  "v_mul_f32 v42, v42, v36\n" \
  "v_mul_f32 v43, v43, v37\n" \
  "v_mul_f32 v44, v44, v38\n" \
  "v_mul_f32 v45, v45, v39\n" \
  "v_mul_f32 v48, v48, v36\n" \
  "v_mul_f32 v49, v49, v37\n" \
  "v_mul_f32 v50, v50, v38\n" \
  "v_mul_f32 v51, v51, v39\n" \
  "v_mul_f32 v54, v54, v36\n" \
  "v_mul_f32 v55, v55, v37\n" \
  "v_mul_f32 v56, v56, v38\n" \
  "v_mul_f32 v57, v57, v39\n" \
  "v_mul_f32 v60, v60, v36\n" \
  "v_mul_f32 v61, v61, v37\n" \
  "v_mul_f32 v62, v62, v38\n" \
  "v_mul_f32 v63, v63, v39\n" \
  "ds_read_b128 v[36:39], v1 offset:1024\n" \
  "v_cvt_pk_bf16_f32 v40, v42, v43\n" \
  "v_cvt_pk_bf16_f32 v41, v44, v45\n" \
  "v_cvt_pk_bf16_f32 v46, v48, v49\n" \
  "v_cvt_pk_bf16_f32 v47, v50, v51\n" \
  "v_cvt_pk_bf16_f32 v52, v54, v55\n" \
  "v_cvt_pk_bf16_f32 v53, v56, v57\n" \
  "v_cvt_pk_bf16_f32 v58, v60, v61\n" \
  "v_cvt_pk_bf16_f32 v59, v62, v63\n" \
  "ds_read_b128 v[24:27], v0 offset:6400\n" \
  "ds_read_b128 v[28:31], v1 offset:2048\n" \
  "v_mfma_f32_16x16x32_bf16 v[42:45], v[12:15], v[64:67], 0\n" \
  "v_mfma_f32_16x16x32_bf16 v[48:51], v[12:15], v[128:131], 0\n" \
  "v_mfma_f32_16x16x32_bf16 v[54:57], v[12:15], v[192:195], 0\n" \
  "s_waitcnt lgkmcnt(3)\n" \
  "v_mfma_f32_16x16x32_bf16 v[60:63], v[12:15], v[32:35], 0\n" \
  "ds_read_b128 v[12:15], v0 offset:8448\n" \
  "ds_read_b128 v[32:35], v1 offset:3072\n" \
  "v_mfma_f32_16x16x32_bf16 v[42:45], v[16:19], v[68:71], v[42:45]\n" \
  "v_mfma_f32_16x16x32_bf16 v[48:51], v[16:19], v[132:135], v[48:51]\n" \
  "v_mfma_f32_16x16x32_bf16 v[54:57], v[16:19], v[196:199], v[54:57]\n" \
  "s_waitcnt lgkmcnt(4)\n" \
  "v_mfma_f32_16x16x32_bf16 v[60:63], v[16:19], v[36:39], v[60:63]\n" \
  "ds_read_b128 v[16:19], v0 offset:10496\n" \
  "ds_read_b128 v[36:39], v1 offset:4096\n" \
  "v_mfma_f32_16x16x32_bf16 v[42:45], v[20:23], v[72:75], v[42:45]\n" \
  "v_mfma_f32_16x16x32_bf16 v[48:51], v[20:23], v[136:139], v[48:51]\n" \
  "v_mfma_f32_16x16x32_bf16 v[54:57], v[20:23], v[200:203], v[54:57]\n" \
  "s_waitcnt lgkmcnt(4)\n" \
  "v_mfma_f32_16x16x32_bf16 v[60:63], v[20:23], v[28:31], v[60:63]\n" \
  "ds_read_b128 v[20:23], v0 offset:12544\n" \
  "ds_read_b128 v[28:31], v1 offset:5120\n" \
  "v_mfma_f32_16x16x32_bf16 v[42:45], v[24:27], v[76:79], v[42:45]\n" \
  "v_mfma_f32_16x16x32_bf16 v[48:51], v[24:27], v[140:143], v[48:51]\n" \
  "v_mfma_f32_16x16x32_bf16 v[54:57], v[24:27], v[204:207], v[54:57]\n" \
  "s_waitcnt lgkmcnt(4)\n" \
  "v_mfma_f32_16x16x32_bf16 v[60:63], v[24:27], v[32:35], v[60:63]\n" \
  "ds_read_b128 v[24:27], v0 offset:14592\n" \
  "ds_read_b128 v[32:35], v1 offset:6144\n" \
  "v_mfma_f32_16x16x32_bf16 v[42:45], v[12:15], v[80:83], v[42:45]\n" \
  "v_mfma_f32_16x16x32_bf16 v[48:51], v[12:15], v[144:147], v[48:51]\n" \
  "v_mfma_f32_16x16x32_bf16 v[54:57], v[12:15], v[208:211], v[54:57]\n" \
  "s_waitcnt lgkmcnt(4)\n" \
  "v_mfma_f32_16x16x32_bf16 v[60:63], v[12:15], v[36:39], v[60:63]\n" \
  "ds_read_b128 v[12:15], v0 offset:16640\n" \
  "ds_read_b128 v[36:39], v1 offset:7168\n" \
  "v_mfma_f32_16x16x32_bf16 v[42:45], v[16:19], v[84:87], v[42:45]\n" \
  "v_mfma_f32_16x16x32_bf16 v[48:51], v[16:19], v[148:151], v[48:51]\n" \
  "v_mfma_f32_16x16x32_bf16 v[54:57], v[16:19], v[212:215], v[54:57]\n" \
  "s_waitcnt lgkmcnt(4)\n" \
  "v_mfma_f32_16x16x32_bf16 v[60:63], v[16:19], v[28:31], v[60:63]\n" \
  "ds_read_b128 v[16:19], v0 offset:18688\n" \
  "ds_read_b128 v[28:31], v1 offset:8192\n" \
  "v_mfma_f32_16x16x32_bf16 v[42:45], v[20:23], v[88:91], v[42:45]\n" \
  "v_mfma_f32_16x16x32_bf16 v[48:51], v[20:23], v[152:155], v[48:51]\n" \
  "v_mfma_f32_16x16x32_bf16 v[54:57], v[20:23], v[216:219], v[54:57]\n" \
  "s_waitcnt lgkmcnt(4)\n" \
  "v_mfma_f32_16x16x32_bf16 v[60:63], v[20:23], v[32:35], v[60:63]\n" \
  "ds_read_b128 v[20:23], v0 offset:20736\n" \
  "ds_read_b128 v[32:35], v1 offset:9216\n" \
  "v_mfma_f32_16x16x32_bf16 v[42:45], v[24:27], v[92:95], v[42:45]\n" \
  "v_mfma_f32_16x16x32_bf16 v[48:51], v[24:27], v[156:159], v[48:51]\n" \
  "v_mfma_f32_16x16x32_bf16 v[54:57], v[24:27], v[220:223], v[54:57]\n" \
  "s_waitcnt lgkmcnt(4)\n" \
  "v_mfma_f32_16x16x32_bf16 v[60:63], v[24:27], v[36:39], v[60:63]\n" \
  "ds_read_b128 v[24:27], v0 offset:22784\n" \
  "ds_read_b128 v[36:39], v1 offset:10240\n" \
  "v_mfma_f32_16x16x32_bf16 v[42:45], v[12:15], v[96:99], v[42:45]\n" \
  "v_mfma_f32_16x16x32_bf16 v[48:51], v[12:15], v[160:163], v[48:51]\n" \
  "v_mfma_f32_16x16x32_bf16 v[54:57], v[12:15], v[224:227], v[54:57]\n" \
  "s_waitcnt lgkmcnt(4)\n" \
  "v_mfma_f32_16x16x32_bf16 v[60:63], v[12:15], v[28:31], v[60:63]\n" \
  "ds_read_b128 v[12:15], v0 offset:24832\n" \
  "ds_read_b128 v[28:31], v1 offset:11264\n" \
  "v_mfma_f32_16x16x32_bf16 v[42:45], v[16:19], v[100:103], v[42:45]\n" \
  "v_mfma_f32_16x16x32_bf16 v[48:51], v[16:19], v[164:167], v[48:51]\n" \
  "v_mfma_f32_16x16x32_bf16 v[54:57], v[16:19], v[228:231], v[54:57]\n" \
  "s_waitcnt lgkmcnt(4)\n" \
  "v_mfma_f32_16x16x32_bf16 v[60:63], v[16:19], v[32:35], v[60:63]\n" \
  "ds_read_b128 v[16:19], v0 offset:26880\n" \
  "ds_read_b128 v[32:35], v1 offset:12288\n" \
  "v_mfma_f32_16x16x32_bf16 v[42:45], v[20:23], v[104:107], v[42:45]\n" \
  "v_mfma_f32_16x16x32_bf16 v[48:51], v[20:23], v[168:171], v[48:51]\n" \
  "v_mfma_f32_16x16x32_bf16 v[54:57], v[20:23], v[232:235], v[54:57]\n" \
  "s_waitcnt lgkmcnt(4)\n" \
  "v_mfma_f32_16x16x32_bf16 v[60:63], v[20:23], v[36:39], v[60:63]\n" \
  "ds_read_b128 v[20:23], v0 offset:28928\n" \
  "ds_read_b128 v[36:39], v1 offset:13312\n" \
  "v_mfma_f32_16x16x32_bf16 v[42:45], v[24:27], v[108:111], v[42:45]\n" \
  "v_mfma_f32_16x16x32_bf16 v[48:51], v[24:27], v[172:175], v[48:51]\n" \
  "v_mfma_f32_16x16x32_bf16 v[54:57], v[24:27], v[236:239], v[54:57]\n" \
  "s_waitcnt lgkmcnt(4)\n" \
  "v_mfma_f32_16x16x32_bf16 v[60:63], v[24:27], v[28:31], v[60:63]\n" \
  "ds_read_b128 v[24:27], v0 offset:30976\n" \
  "ds_read_b128 v[28:31], v1 offset:14336\n" \
  "v_mfma_f32_16x16x32_bf16 v[42:45], v[12:15], v[112:115], v[42:45]\n" \
  "v_mfma_f32_16x16x32_bf16 v[48:51], v[12:15], v[176:179], v[48:51]\n" \
  "v_mfma_f32_16x16x32_bf16 v[54:57], v[12:15], v[240:243], v[54:57]\n" \
  "s_waitcnt lgkmcnt(4)\n" \
  "v_mfma_f32_16x16x32_bf16 v[60:63], v[12:15], v[32:35], v[60:63]\n" \
  "ds_read_b128 v[32:35], v1 offset:15360\n" \
  "v_mfma_f32_16x16x32_bf16 v[42:45], v[16:19], v[116:119], v[42:45]\n" \
  "v_mfma_f32_16x16x32_bf16 v[48:51], v[16:19], v[180:183], v[48:51]\n" \
  "v_mfma_f32_16x16x32_bf16 v[54:57], v[16:19], v[244:247], v[54:57]\n" \
  "s_waitcnt lgkmcnt(3)\n" \
  "v_mfma_f32_16x16x32_bf16 v[60:63], v[16:19], v[36:39], v[60:63]\n" \
  "ds_read_b128 v[36:39], v3 offset:64\n" \
  "v_mfma_f32_16x16x32_bf16 v[42:45], v[20:23], v[120:123], v[42:45]\n" \
  "v_mfma_f32_16x16x32_bf16 v[48:51], v[20:23], v[184:187], v[48:51]\n" \
  "v_mfma_f32_16x16x32_bf16 v[54:57], v[20:23], v[248:251], v[54:57]\n" \
  "s_waitcnt lgkmcnt(2)\n" \
  "v_mfma_f32_16x16x32_bf16 v[60:63], v[20:23], v[28:31], v[60:63]\n" \
  "ds_read_b128 v[28:31], v3 offset:192\n" \
  "s_mov_b64 exec, 1\n" \
  "ds_add_u32 v6, v7 offset:8\n" \
  "s_mov_b64 exec, -1\n" \
  "v_xor_b32 v6, 4, v6\n" \
  "v_mfma_f32_16x16x32_bf16 v[42:45], v[24:27], v[124:127], v[42:45]\n" \
  "v_mfma_f32_16x16x32_bf16 v[48:51], v[24:27], v[188:191], v[48:51]\n" \
  "v_mfma_f32_16x16x32_bf16 v[54:57], v[24:27], v[252:255], v[54:57]\n" \
  "s_waitcnt lgkmcnt(3)\n" \
  "v_mfma_f32_16x16x32_bf16 v[60:63], v[24:27], v[32:35], v[60:63]\n" \
  "s_waitcnt lgkmcnt(1)\n" \
  "s_nop 4\n" \
  "ds_read_b32 v5, v6 offset:0\n" \
  "v_fma_f32 v42, -v36, v8, v42\n" \
  "v_fma_f32 v43, -v37, v8, v43\n" \
  "v_fma_f32 v44, -v38, v8, v44\n" \
  "v_fma_f32 v45, -v39, v8, v45\n" \
  "v_mul_f32 v42, v42, v28\n" \
  "v_mul_f32 v43, v43, v29\n" \
  "v_mul_f32 v44, v44, v30\n" \
  "v_mul_f32 v45, v45, v31\n" \
  "v_cvt_pk_bf16_f32 v42, v42, v43\n" \
  "v_cvt_pk_bf16_f32 v43, v44, v45\n" \
  "buffer_store_dwordx4 v[40:43], v2, s[40:43], s65 offen offset:0 sc1\n" \
  "v_fma_f32 v48, -v36, v9, v48\n" \
  "v_fma_f32 v49, -v37, v9, v49\n" \
  "v_fma_f32 v50, -v38, v9, v50\n" \
  "v_fma_f32 v51, -v39, v9, v51\n" \
  "v_mul_f32 v48, v48, v28\n" \
  "v_mul_f32 v49, v49, v29\n" \
  "v_mul_f32 v50, v50, v30\n" \
  "v_mul_f32 v51, v51, v31\n" \
  "v_cvt_pk_bf16_f32 v48, v48, v49\n" \
  "v_cvt_pk_bf16_f32 v49, v50, v51\n" \
  "buffer_store_dwordx4 v[46:49], v2, s[40:43], s65 offen offset:1024 sc1\n" \
  "v_fma_f32 v54, -v36, v10, v54\n" \
  "v_fma_f32 v55, -v37, v10, v55\n" \
  "v_fma_f32 v56, -v38, v10, v56\n" \
  "v_fma_f32 v57, -v39, v10, v57\n" \
  "v_mul_f32 v54, v54, v28\n" \
  "v_mul_f32 v55, v55, v29\n" \
  "v_mul_f32 v56, v56, v30\n" \
  "v_mul_f32 v57, v57, v31\n" \
  "v_cvt_pk_bf16_f32 v54, v54, v55\n" \
  "v_cvt_pk_bf16_f32 v55, v56, v57\n" \
  "buffer_store_dwordx4 v[52:55], v2, s[40:43], s65 offen offset:2048 sc1\n" \
  "v_fma_f32 v60, -v36, v4, v60\n" \
  "v_fma_f32 v61, -v37, v4, v61\n" \
  "v_fma_f32 v62, -v38, v4, v62\n" \
  "v_fma_f32 v63, -v39, v4, v63\n" \
  "v_mul_f32 v60, v60, v28\n" \
  "v_mul_f32 v61, v61, v29\n" \
  "v_mul_f32 v62, v62, v30\n" \
  "v_mul_f32 v63, v63, v31\n" \
  "v_cvt_pk_bf16_f32 v60, v60, v61\n" \
  "v_cvt_pk_bf16_f32 v61, v62, v63\n" \
  "buffer_store_dwordx4 v[58:61], v2, s[40:43], s65 offen offset:3072 sc1\n" \
  "s_waitcnt lgkmcnt(0)\n" \
  "s_add_u32 s65, s65, 0x40000\n" \
  "s_sub_u32 s64, s64, 1\n" \
  "s_mov_b32 s68, 1\n" \
  "s_cmp_lg_u32 s64, 0\n" \
  "s_cbranch_scc1 .LCW_TOP_%=\n" \
  "s_waitcnt vmcnt(0)\n" \
  "s_mov_b64 exec, 1\n" \
  "buffer_store_dword v7, off, s[56:59], s72 sc1\n" \
  "s_mov_b64 exec, -1\n" \
  "s_add_u32 s72, s72, 0x1000\n" \
  "s_mov_b32 s68, 0\n" \
  ".LCW_END_%=:\n" \
  "s_endpgm\n"
constexpr int RG = 2;
#define RFL(x) __builtin_amdgcn_readfirstlane(x)
#define HALF(x, i) BF8(((i) ? __builtin_shufflevector(x, x, 4, 5, 6, 7) : __builtin_shufflevector(x, x, 0, 1, 2, 3)))
constexpr int P_W = 65536, P_ST = 131072, P_SS = P_ST + 1024, P_CN = P_SS + 512;
template <bool PR>
__device__ __forceinline__ void role_proj_t(const MegaArgs& A, char* smem, const int L, const int idx, const bool kv) {
  const int w = A.wv;
  const int l = lane_id();
  const int c = l & 15, g = l >> 4;
  char* ws = A.ws;
  unsigned* FL = (unsigned*)(ws + OFF_FL);
  unsigned* TMO = FL + FL_TMO;
  const unsigned sb = lds_base(smem);
  auto cn = (volatile __attribute__((address_space(3))) unsigned*)(unsigned long)(sb + P_CN);
  u64 tag;
  {
    const int j = w & 3;
    const bool vt = kv && j >= 2;
    constexpr int NT = PR ? 3 : 1;
    v8u wq[NT][8];
    const u16* Wm = (kv ? (PR ? A.Wv : A.Wk) : A.Wq) - (PR ? 128 : 0) * HD;
    const int hm = kv ? idx : 2 * idx + PR;
    const int q = (l >> 2) & 3, p = l & 3, f = q | ((g & 1) << 2);
    unsigned ra[NT];
#pragma unroll
    for (int m = 0; m < NT; ++m) {
      const int ct = PR ? m : 3;
      ra[m] = sb + (j >> 1) * 65536 + (8 * g + q) * 256 + ((4 * (j & 1) + (vt ? ct : (ct & 2) + (p >> 1))) ^ f) * 32 + (vt ? p * 8 : (p & 1) * 16 + (ct & 1) * 8);
    }
#define W_ISSUE(n) wqkv_issue<false>(Wm + (128 * (n) - 256 * ((n) & 1)) * HD, (n) & 1, hm, w, l, sb)
#define W_STAGE(n, WAIT, NEXT)                                                                             \
  {                                                                                                        \
    WAIT;                                                                                                  \
    RAW_BAR();                                                                                             \
    _Pragma("unroll") for (int m = 0; m < NT; ++m) {                                                       \
      _Pragma("unroll") for (int t = 0; t < 2; ++t) {                                                      \
        const unsigned a_ = ra[m] + ((n) & 1) * 8192 + t * 32768;                                          \
        const v2u r0 = trd(a_), r1 = trd(a_ + 1024), r2 = trd(a_ + 16384), r3 = trd(a_ + 16384 + 1024);    \
        wq[m][2 * (n) + t] = v8u{r0.x, r0.y, r1.x, r1.y, r2.x, r2.y, r3.x, r3.y};                          \
      }                                                                                                    \
    }                                                                                                      \
    _Pragma("unroll") for (int m = 0; m < 2 * NT; ++m) asm volatile("" : "+v"(wq[m >> 1][2 * (n) + (m & 1)])); \
    RAW_BAR();                                                                                             \
    NEXT;                                                                                                  \
    _Pragma("unroll") for (int m = 0; m < 4 * NT; ++m)                                                     \
      cs[m % NT] = MFMA16(HALF(wq[m % NT][2 * (n) + m / NT / 2], (m / NT) & 1), BF8(one), cs[m % NT]);     \
    _Pragma("unroll") for (int m = 0; m < NT; ++m) asm volatile("" : "+v"(cs[m]));                         \
  }
    __builtin_amdgcn_s_waitcnt(0x0f70);
    W_ISSUE(0);
    W_ISSUE(1);
    tag = call_tag(A);
    if (w == 0 && l < 8) cn[l] = l == 1 ? 3u : 0u;
    const v4u one = {0x3f803f80u, 0x3f803f80u, 0x3f803f80u, 0x3f803f80u};
    f32x4 cs[NT] = {};
    W_STAGE(0, WAIT_VM(8), W_ISSUE(2));
    SST(4);
    W_STAGE(1, WAIT_VM(8), W_ISSUE(3));
    SST(5);
    W_STAGE(2, WAIT_VM(8), (void)0);
    SST(6);
    ready_wait(A, tag, ready_look(A));
    W_STAGE(3, WAIT_VM(0), if (!PR) {
      _Pragma("unroll") for (int ks = 0; ks < 16; ++ks)
        *reinterpret_cast<bf16x8*>(smem + P_W + j * 16384 + ks * 1024 + l * 16) = HALF(wq[0][ks >> 1], ks & 1);
    });
    SST(7);
#undef W_STAGE
#undef W_ISSUE
#pragma unroll
    for (int m = 0; m < NT; ++m) *reinterpret_cast<f32x4*>(smem + P_ST + j * 256 + (PR ? m : 3) * 64 + g * 16) = cs[m];
    RAW_BAR();
    SST(8);
    if constexpr (PR) {
      const v4u rsO = mk_srd(ws + RFL((unsigned)(vt ? OFF_VF + L * SZ_O : OFF_QK + L * SZ_QK)), RFL((unsigned)(vt ? SZ_O : SZ_QK)));
      const v4u rsP = mk_srd(FL + RFL(kv ? FL_FQ + (L * 64 * 32 + idx) * 32 + 2 * j : FL_FP + (L * 16 + idx) * 32 + j), 63 * 4096 + 4);
      const int so = RFL(vt ? idx * 8 + 4 * (j & 1) : (kv ? idx * 16 + 8 : (2 * idx + (j >> 1)) * 16) + 2 * (j & 1));
      const v8u pv = {sb + 32768 + g * 512 + c * 16, sb + P_W + j * 16384 + l * 16, (unsigned)l * 16, sb + P_SS + 256 + (vt ? g * 16 : c * 4),
                      sb + P_ST + j * 256 + (vt ? c * 4 : g * 16), 0u, 0u, 0u};
      const char* dbg = nullptr;
#define P_OPS                                                                                                                               \
  ::"{v[64:71]}"(wq[0][0]), "{v[72:79]}"(wq[0][1]), "{v[80:87]}"(wq[0][2]), "{v[88:95]}"(wq[0][3]), "{v[96:103]}"(wq[0][4]),                \
      "{v[104:111]}"(wq[0][5]), "{v[112:119]}"(wq[0][6]), "{v[120:127]}"(wq[0][7]), "{v[128:135]}"(wq[1][0]), "{v[136:143]}"(wq[1][1]),     \
      "{v[144:151]}"(wq[1][2]), "{v[152:159]}"(wq[1][3]), "{v[160:167]}"(wq[1][4]), "{v[168:175]}"(wq[1][5]), "{v[176:183]}"(wq[1][6]),     \
      "{v[184:191]}"(wq[1][7]), "{v[192:199]}"(wq[2][0]), "{v[200:207]}"(wq[2][1]), "{v[208:215]}"(wq[2][2]), "{v[216:223]}"(wq[2][3]),     \
      "{v[224:231]}"(wq[2][4]), "{v[232:239]}"(wq[2][5]), "{v[240:247]}"(wq[2][6]), "{v[248:255]}"(wq[2][7]), "{v[0:7]}"(pv),               \
      "{s[40:43]}"(rsO), "{s[56:59]}"(rsP), "{s64}"(NSEG), "{s65}"(so * 1024), "{s70}"(sb + P_CN), "{s[36:37]}"(dbg)                        \
      : "memory", "vcc", "scc", "s80", "v63"
      if (vt) asm volatile("s_mov_b64 exec, -1\ns_waitcnt vmcnt(0) lgkmcnt(0)\ns_nop 4\n" V_ASM P_OPS);
      else asm volatile("s_mov_b64 exec, -1\ns_waitcnt vmcnt(0) lgkmcnt(0)\ns_nop 4\n" P_ASM P_OPS);
#undef P_OPS
      return;
    }
  }
  __builtin_amdgcn_s_setprio(RPRIO);
  const bool h4 = kv && L;
  if (h4 || w >= 2) {
    const char* Xb = ws + (L ? OFF_X3 : OFF_XA);
    const __amdgpu_buffer_rsrc_t rsS = MKRS(ws + OFF_H + L * (SZ_X / 2), SZ_X / 2);
    const __amdgpu_buffer_rsrc_t rsM = MKRS(ws + OFF_MEMK + L * SZ_MEM, kv && w == 2 ? 3 * SZ_MEM : 0);
    const int b0 = RFL(h4 ? 3 * w : w == 2 ? 0 : HX0), b1 = RFL(h4 ? (w == 2 ? 8 : 3 * w + 3 * (w < 2)) : w == 2 ? HX0 : 8);
    const __amdgpu_buffer_rsrc_t rsF = MKRS(FL + FL_FC + L * 64 * 32 * 32, 64 * 4096);
    const int fo = ((blockIdx.x & 3) * 8 + (w == 3 ? l >> 3 : min(b0 + (l >> 3), b1 - 1))) * 128 + (l & 7) * 4;
#define FC_LOOK(s) __builtin_amdgcn_raw_buffer_load_b32(rsF, fo, (s) * 4096, AUX_SC1)
#define MEM_CLEAR(s)                                                                                                          \
  _Pragma("unroll") for (int i = 0; i < 4; ++i)                                                                               \
    ST128((v4u{0u, 0u, 0u, 0u}), rsM, l * 16 + (i & 1) * 1024, (s) * 65536 + idx * 2048 + (i >> 1) * (int)(OFF_MEMV - OFF_MEMK));
    if (kv && w == 2) { MEM_CLEAR(0) }
    unsigned tn = now10(), nf = 0u, tg = tn;
    for (int s = 0; s < NSEG; ++s) {
      for (unsigned spins = 0; !__all(nf != 0u); ++spins) {
        if (spin_timeout(spins, TMO)) return;
        if (spins) __builtin_amdgcn_s_sleep(SLP_H);
        else if (GF_H && (h4 || (GF_P && L)) && s >= GF0 && s < NSEG - 6) gate(tg);
        tg = now10() + GF_H;
        nf = FC_LOOK(s);
        asm volatile("" ::: "memory");
      }
      TS(4, s, 128);
      for (unsigned spins = 0; cn[2 + (s & 1)] < 4u * (s >> 1); ++spins) {
        if (spins > 64u * SPIN_CAP) return;
        __builtin_amdgcn_s_sleep(2);
      }
      if (L == 0 && s >= PACE0) gate(tn);
      else tn = now10();
      tn += PACE;
      TS(5, s, 128);
      for (int i = b0; i < b1; ++i) glds64c(Xb + RFL(s * 32768 + i * 4096), l * 16, RFL(sb + (s & 1) * 32768 + i * 4096));
      if (w != 3) {
        nf = L ? 0u : FC_LOOK(s + 1);
        if (kv && w == 2 && s < NSEG - 1) {
          MEM_CLEAR(s + 1)
          WAIT_VM(4);
        } else {
          WAIT_VM(0);
        }
        if (l == 0) __hip_atomic_fetch_add((__attribute__((address_space(3))) unsigned*)cn + (s & 1), h4 ? 2u : 3u, __ATOMIC_RELAXED, __HIP_MEMORY_SCOPE_WORKGROUP);
        TS(6, s, 128);
      } else {
        v4u t[16];
#pragma unroll
        for (int i = 0; i < 16; ++i) t[i] = LD128(rsS, l * 16, s * 16384 + i * 1024);
        WAIT_VM(16);
        nf = L ? 0u : FC_LOOK(s + 1);
        if (l == 0 && !h4) __hip_atomic_fetch_add((__attribute__((address_space(3))) unsigned*)cn + (s & 1), 3u, __ATOMIC_RELAXED, __HIP_MEMORY_SCOPE_WORKGROUP);
        float a = 0.f, b = 0.f, qa = 0.f, qb = 0.f;
#pragma unroll
        for (int i = 0; i < 16; ++i) {
          a = one(a + __uint_as_float(t[i].x));
          b = one(b + __uint_as_float(t[i].z));
          qa = one(qa + __uint_as_float(t[i].y));
          qb = one(qb + __uint_as_float(t[i].w));
        }
        a = xsum32(xsum16(a));
        b = xsum32(xsum16(b));
        float da = 0.f, db = 0.f;
#pragma unroll
        for (int i = 0; i < 16; ++i) {
          const float u = one(__uint_as_float(t[i].x) - a * (1.0f / 64)), v = one(__uint_as_float(t[i].z) - b * (1.0f / 64));
          da = one(da + u * u);
          db = one(db + v * v);
        }
        const float var = xsum32(swap_add16(qa + da * 0.125f, qb + db * 0.125f)) * (1.0f / DM);
        const float ri = 1.0f / bfr1(sqrtf(bfr1(bfr1(var) + 1e-5f)));
        reinterpret_cast<float*>(smem + P_SS)[(s & 1) * 64 + (g >> 1) * 32 + 2 * c + (g & 1)] = g < 2 ? bfr1(((g & 1) ? b : a) * (1.0f / DM)) : ri;
        asm volatile("" ::: "memory");
        if (l == 0) cn[4 + (s & 1)] = s + 1;
        TS(7, s, 192);
      }
    }
    return;
  }
  const int RL = !kv, rp = idx & 15, u = RFL(kv ? idx >> 4 : L);
  const __amdgpu_buffer_rsrc_t rsW = MKRS(ws + OFF_PW, SZ_PW);
  const __amdgpu_buffer_rsrc_t rsX = MKRS(ws + (RL ? OFF_X3 : OFF_XA), SZ_X);
  const __amdgpu_buffer_rsrc_t rsY = RL ? MKRS(A.out, SZ_X) : MKRS(ws + OFF_X2, SZ_X);
  const int r = 2 * rp + (g >> 1), yo = r * (DM * 2) + (l & 31) * 16 + u * 512;
  if (kv) {
    for (int s = w; s < NSEG; s += RG) ST128((v4u{0u, 0u, 0u, 0u}), rsY, yo, s * SEG * DM * 2);
    DRAIN();
    if (l < 8) set_flag(FL + FL_FZ + l * 64 + 2 * idx + w);
  }
  v4u sel;
#pragma unroll
  for (int e = 0; e < 4; ++e) sel[e] = (g >> 1) == (c >> 3) && ((c & 7) >> 1) == e ? 0x3f80u << (16 * (c & 1)) : 0u;
  unsigned tl = now10();
  for (int s = w; s < NSEG; s += RG) {
    if (GF_R) {
      gate(tl);
      const gu32* f = (gu32*)(FL + FL_FW) + ((RL * 64 + s) * 4 + (blockIdx.x & 3)) * 4 * 32 + (c >> 2) * 32 + (c & 3);
      for (unsigned spins = 0;; ++spins) {
        tl = now10() + GF_R;
        if (__all(__hip_atomic_load(f, RLX_AGENT) != 0u) || spin_timeout(spins, TMO)) break;
        __builtin_amdgcn_s_sleep(SLP_R);
      }
      asm volatile("" ::: "memory");
    } else
    grp_wait<SLP_R, 4, GATE_R>(true, nullptr, 0, FL + FL_FW + ((RL * 64 + s) * 4 + (blockIdx.x & 3)) * 4 * 32, 16, TMO, tl);
    if (s >= NSEG - 6) tl = now10();
    TS(0, s, w * 64);
    v4u p[8][2];
    const v4u xr = LD128(rsX, (l & 31) * 512 + r * 16, s * 32768 + u * 16384);
#pragma unroll
    for (int k = 0; k < 8; ++k)
#pragma unroll
      for (int b = 0; b < 2; ++b)
        p[k][b] = LD128(rsW, (g & 1) * 32768 + (c >> 2) * 128 + (g >> 1) * 64 + (c & 3) * 16 + b * 512, ((RL * 64 + s) * 16 + 2 * k) * 32768 + rp * 2048 + u * 1024);
    __builtin_amdgcn_sched_barrier(0);
    f32x4 ac[2] = {};
#pragma unroll
    for (int k = 0; k < 8; ++k)
#pragma unroll
      for (int b = 0; b < 2; ++b) ac[b] = MFMA16(BF8(sel), BF8(p[k][b]), ac[b]);
    v4u xo;
    {
      const auto x = __builtin_amdgcn_permlane16_swap(pk2s(ac[0][0], ac[0][1]), pk2s(ac[1][0], ac[1][1]), false, false);
      const auto y = __builtin_amdgcn_permlane16_swap(pk2s(ac[0][2], ac[0][3]), pk2s(ac[1][2], ac[1][3]), false, false);
      const v4u a = {x[0], y[0], x[1], y[1]};
#pragma unroll
      for (int e = 0; e < 4; ++e) {
        xo[e] = pk2s(lo16(xr[e]) + lo16(a[e]), hi16(xr[e]) + hi16(a[e]));
        if (kv) xo[e] = xo[e] ? xo[e] : 0x8000u;
      }
      ST128(xo, rsY, yo, s * SEG * DM * 2);
    }
    TS(1, s, w * 64);
    if (kv) {
      const float pv = lo16(g < 2 ? __builtin_amdgcn_readlane(xo[0], 0) : __builtin_amdgcn_readlane(xo[0], 32));
      float sm = 0.f, q0 = 0.f, q1 = 0.f;
#pragma unroll
      for (int e = 0; e < 4; ++e) {
        const float a = one(lo16(xo[e]) - pv), b = one(hi16(xo[e]) - pv);
        sm = one(sm + one(a + b));
        q0 = one(q0 + a * a);
        q1 = one(q1 + b * b);
      }
      sm = half_sum(sm);
      const float q = fmaxf(one(half_sum(q0 + q1)) - sm * sm * (1.0f / 256), 0.f);
      const u64 st = (u64)__float_as_uint(sm + pv * 256.f) | (u64)(__float_as_uint(q) | 1u) << 32;
      if ((l & 31) < 4) __hip_atomic_store((gu64*)(FL + FL_FR + ((s * 4 + (l & 3)) * 4 + (r >> 3)) * 32 + (r & 7) * 4 + 2 * u), st, RLX_AGENT);
    }
    TS(3, s, w * 64);
  }
  if (L == 1 && idx == 0 && w == 0 && l == 0) __hip_atomic_store((gu64*)(ws + OFF_EP), tag, RLX_AGENT);
}
__device__ __forceinline__ void role_proj(const MegaArgs& A, char* smem, const int L, const int idx, const bool kv) {
  if (A.wv < 4) role_proj_t<false>(A, smem, L, idx, kv);
  else role_proj_t<true>(A, smem, L, idx, kv);
}

constexpr int CUN = 2144;
__device__ __forceinline__ v2u part_stats(const v4u o) {
  float v[8];
#pragma unroll
  for (int e = 0; e < 4; ++e) { v[2 * e] = lo16(o[e]); v[2 * e + 1] = hi16(o[e]); }
  const float s = ((v[0] + v[1]) + (v[2] + v[3])) + ((v[4] + v[5]) + (v[6] + v[7])), m = s * 0.125f;
  float q0 = 0.f, q1 = 0.f;
#pragma unroll
  for (int e = 0; e < 4; ++e) {
    const float a = v[e] - m, b = v[4 + e] - m;
    q0 += a * a;
    q1 += b * b;
  }
  return v2u{__float_as_uint(s), __float_as_uint(q0 + q1)};
}
constexpr int CRN = 176;
constexpr int CV_LDS = (8 * 2 * CKN + 8 * 2 * CUN + 64 + 2 * 32 * 8 + 16 * CRN + 64 + 256 * 8) * 2 + 8 * 1568;
__device__ __forceinline__ void role_conv(const MegaArgs& A, char* smem, const int cv) {
  u16* Kr = reinterpret_cast<u16*>(smem);
  u16* Uc = Kr + 8 * 2 * CKN;
  u16* Us = Uc + 8 * CUN + 63;
  u16* To = Uc + 8 * 2 * CUN + 64;
  u16* Sx = To + 32 * 8;
  u16* Rc = Sx + 32 * 8;
  u16* Rs = Rc + 8 * CRN + 63;
  u16* Ta = Rc + 16 * CRN + 64;
  const int w = A.wv;
  const int l = lane_id(), tid = 64 * w + l, c = l & 15, g = l >> 4;
  const int d = 8 * cv + w;
  char* ws = A.ws;
  unsigned* FL = (unsigned*)(ws + OFF_FL);
  unsigned* TMO = FL + FL_TMO;
  u16* kr = Kr + w * 2 * CKN;
  const u16* ka = kr + (c >> 3) * CKN + 2080 - 16 * (c & 7) + 8 * g;
#define KFRAG(D0) (*reinterpret_cast<const bf16x8*>(ka - (D0)))
  typedef __attribute__((address_space(3))) const unsigned* lpu;
#define BFRAG(a) BF8((v4u{((lpu)(unsigned long)(a))[0], ((lpu)(unsigned long)(a))[1], ((lpu)(unsigned long)(a))[2], ((lpu)(unsigned long)(a))[3]}))
  f32x4 acc[16], far[8];
  v4u tx[4];
  const __amdgpu_buffer_rsrc_t rz1 = MKRS(ws + OFF_XA, SZ_X), rps = MKRS(ws + OFF_H, SZ_X);
  u16* uc = Uc + w * CUN;
  const unsigned ub = lds_base(reinterpret_cast<char*>(((c & 1) ? Us : Uc) + w * CUN + c + 8 * g + 32));
  const int t0 = 16 * (4 * (g & 1) + (g >> 1)) + c;
  float dsk;
#define SKIP(T) pk2(bf2f(uc[32 + 128 * (T) + t0]) * dsk, bf2f(uc[64 + 128 * (T) + t0]) * dsk)
#define TILE_OUT(T, a, uu)                                                                     \
  {                                                                                            \
    const unsigned yy = pk2(swap_add32(a[0], a[1]), swap_add32(a[2], a[3]));                   \
    const unsigned v_ = pk2(lo16(yy) + lo16(uu), hi16(yy) + hi16(uu));                         \
    Ta[(128 * ((T) & 7) + t0) * 8 + w] = (u16)v_;                                              \
    Ta[(128 * ((T) & 7) + t0 + 32) * 8 + w] = (u16)(v_ >> 16);                                 \
  }
#define CHUNK_ST(i)                                                                            \
  {                                                                                            \
    const v4u a_ = *reinterpret_cast<const v4u*>(Ta + (64 * w + l + 512 * ((i) & 1)) * 8);     \
    v4u o;                                                                                     \
    _Pragma("unroll") for (int e = 0; e < 4; ++e) o[e] = pk2(lo16(tx[i][e]) + lo16(a_[e]), hi16(tx[i][e]) + hi16(a_[e])); \
    ST128(o, rz1, (l >> 5) * 32768 + (l & 31) * 16, (2 * (w + 8 * (i)) * 64 + cv) * 512);      \
    ST64(part_stats(o), rps, (l >> 5) * 16384 + (l & 31) * 8, (2 * (w + 8 * (i)) * 64 + cv) * 256); \
  }
#define CHUNK_FLAG(i) set_flag(FL + FL_FC + (((2 * w + 16 * ((i) + (l >> 3)) + ((l >> 2) & 1)) * 4 + (l & 3)) * 8 + (cv >> 3)) * 32 + (cv & 7))
  {
    unsigned ta[3], k0p;
    v4u kn;
    u64 tag;
    {
      KGen G;
      G.load(A, d, l);
      const u16 pk = A.Dskip[d];
      asm volatile("" ::: "memory");
      tx[0] = *reinterpret_cast<const v4u*>(A.x0 + (size_t)tid * DM + 8 * cv);
      ask_slow(A, 16 * (w - 4) + (cv & 15), ta);
      tag = call_tag(A);
      G.setup();
      dsk = bf2f(pk);
      G.st = smem + w * 16640;
      SST(0);
      G.columns(l);
      const Cx z = G.power(1.f);
      G.rows(G.st + l * 8, cmul(G.C, z), z, 16);
      kn = G.tile(l);
      const float k0 = 2.0f * wave_sum(G.C.r.x + G.C.r.y);
      k0p = l == 0 ? pk2(k0, k0 - lo16(pk2(k0, k0))) : 0u;
    }
    SST(1);
    const __amdgpu_buffer_rsrc_t rst = MKRS(ws + OFF_GT, L_SEQ * 16);
#define U_VAL(i)                                                                               \
  {                                                                                            \
    const float mb_ = lo16(st[i]), ri_ = 1.0f / hi16(st[i]);                                   \
    _Pragma("unroll") for (int e = 0; e < 4; ++e) {                                            \
      const unsigned t_ = pk2(lo16(tx[i][e]) - mb_, hi16(tx[i][e]) - mb_);                     \
      uv[e] = pk2(lo16(t_) * ri_, hi16(t_) * ri_);                                             \
    }                                                                                          \
  }
#define U_PUT(i)                                                                               \
  _Pragma("unroll") for (int e = 0; e < 4; ++e) {                                              \
    ((lph)(unsigned long)up0)[(2 * e) * CUN + 512 * (i)] = (u16)uv[e];                         \
    ((lph)(unsigned long)up0)[(2 * e + 1) * CUN + 512 * (i)] = (u16)(uv[e] >> 16);             \
    ((lph)(unsigned long)up1)[(2 * e) * CUN + 512 * (i)] = (u16)uv[e];                         \
    ((lph)(unsigned long)up1)[(2 * e + 1) * CUN + 512 * (i)] = (u16)(uv[e] >> 16);             \
  }
#define U_ROW(i) { U_VAL(i) U_PUT(i) }
    typedef __attribute__((address_space(3))) u16* lph;
    unsigned st[4], up0 = lds_base((char*)(Uc + 32 + tid)), up1 = lds_base((char*)(Us + 32 + tid));
    asm volatile("" : "+v"(up0), "+v"(up1), "+v"(kn), "+v"(k0p));
    v4u uv;
#define ST_GET(I0, I1)                                                                                                        \
  for (unsigned spins = 0; spins < SPIN_CAP; ++spins) {                                                                       \
    bool ok = true;                                                                                                           \
    v4u t_[4];                                                                                                                \
    _Pragma("unroll") for (int i = I0; i < I1; ++i) t_[i] = LD128(rst, (tid + 512 * i) * 16, 0);                              \
    _Pragma("unroll") for (int i = I0; i < I1; ++i) {                                                                         \
      ok = ok && has_tag(tag, t_[i].x, t_[i].y) && has_tag(tag, t_[i].z, t_[i].w);                                            \
      st[i] = __builtin_amdgcn_perm(t_[i].w, t_[i].y, 0x07060302);                                                            \
    }                                                                                                                         \
    if (__all(ok)) break;                                                                                                     \
    __builtin_amdgcn_s_sleep(4);                                                                                              \
    asm volatile("" ::: "memory");                                                                                            \
  }
    if (w < 4) {
      ST_GET(0, 2);
      U_VAL(0);
      asm volatile("" : "+v"(uv));
    }
#pragma unroll
    for (int i = 1; i < 4; ++i) tx[i] = *reinterpret_cast<const v4u*>(A.x0 + (size_t)(tid + 512 * i) * DM + 8 * cv);
    RAW_BAR();
    SST(2);
    if (w < 4) { U_PUT(0) }
    *reinterpret_cast<v2u*>(kr + 2076 - 16 * c - 4 * g) = v2u{__builtin_amdgcn_perm(kn.z, kn.w, 0x05040100), __builtin_amdgcn_perm(kn.x, kn.y, 0x05040100)};
    *reinterpret_cast<v2u*>(kr + CKN + 2076 - 16 * c - 4 * g) = v2u{__builtin_amdgcn_perm(kn.z, kn.w, 0x07060302), __builtin_amdgcn_perm(kn.x, kn.y, 0x07060302)};
    if (l < 18) {
      reinterpret_cast<v4u*>(kr)[l < 16 ? 260 + l : 210 + l] = v4u{k0p & 0xffffu, 0u, 0u, 0u};
      reinterpret_cast<v4u*>(kr + CKN)[l < 16 ? 260 + l : 210 + l] = v4u{k0p >> 16, 0u, 0u, 0u};
    }
    (l < 32 ? uc : Us + w * CUN - 32)[l] = 0;
    uc[32 + L_SEQ + l] = Us[w * CUN + 32 + L_SEQ + l] = 0;
    uc[32 + 256 + l] = Us[w * CUN + 32 + 256 + l] = 0;
    RAW_BAR();
    SST(11);
    {
      f32x4 a0 = {}, a1 = {}, a2 = {};
      bf16x8 bf[9], af[9];
#pragma unroll
      for (int b = 0; b < 9; ++b) {
        af[(b + 4) % 9] = KFRAG(160 - 32 * ((b + 4) % 9));
        bf[b] = BFRAG(ub + 64 * (b - 1));
      }
      const unsigned k0 = SKIP(0), k1 = SKIP(1);
      __builtin_amdgcn_sched_barrier(0);
#pragma unroll
      for (int b = 0; b < 5; ++b) a0 = MFMA16(af[b + 4], bf[b], a0);
#pragma unroll
      for (int b = 0; b < 4; ++b) {
        a2 = MFMA16(af[b + 4], bf[b + 4], a2);
        a1 = MFMA16(af[b], bf[b], a1);
      }
      a2 = MFMA16(af[8], bf[8], a2);
      TILE_OUT(0, a0, k0);
      a1 += a2;
      TILE_OUT(1, a1, k1);
    }
    const u64 rv = ready_look(A);
    RAW_BAR();
    SST(3);
    if (w < 4) {
      CHUNK_ST(0);
      asm volatile("" : "+v"(tx[1]));
      U_ROW(1);
      DRAIN();
      ready_wait(A, tag, rv);
      if (l < 8) CHUNK_FLAG(0);
      ST_GET(2, 4);
    } else {
      ST_GET(0, 4);
      ready_wait(A, tag, rv);
      asm volatile("" : "+v"(tx[0]), "+v"(tx[1]));
      U_ROW(0);
      U_ROW(1);
    }
    ASKED(ta);
    if (tid < (16 * CRN + 64) / 8) reinterpret_cast<v4u*>(Rc)[tid] = v4u{0u, 0u, 0u, 0u};
    SST(4);
    const gu64* fk = (gu64*)(ws + OFF_KG) + cv * 16 + w;
    u64 kg = __hip_atomic_load(fk, RLX_AGENT);
    U_ROW(2);
    for (unsigned spins = 0; !__all(kg == tag) && spins < SPIN_CAP; ++spins) {
      __builtin_amdgcn_s_sleep(8);
      kg = __hip_atomic_load(fk, RLX_AGENT);
    }
    asm volatile("" : "+v"(tx[3]), "+v"(st[3])::"memory");
    SST(5);
    const unsigned* kd = reinterpret_cast<const unsigned*>(Ta + 256 * 8) + w * 392;
    glds16c(ws + OFF_KF + d * 2048, l * 16, lds_base((char*)kd));
    if (l < 34) glds16c(ws + OFF_KF + d * 2048 + 1024, l * 16, lds_base((char*)kd) + 1024);
    U_ROW(3);
    DRAIN();
    SST(6);
#pragma unroll 1
    for (int un = l; un < 228; un += 64) {
      const int kc = un < 132 ? 2 : un < 196 ? 1 : 0;
      v4u oh, ol;
#pragma unroll
      for (int e = 0; e < 4; ++e) {
        unsigned kv[2];
#pragma unroll
        for (int k = 0; k < 2; ++k) {
          const int lag = 2080 - (8 * un + 2 * e + k);
          const int q = (lag + (1 << kc) - 1 + ((lag >> (kc + 1)) & 1)) >> (kc + 1);
          kv[k] = kd[128 * kc - 128 + q];
          if (lag >= L_SEQ) kv[k] = 0u;
        }
        oh[e] = __builtin_amdgcn_perm(kv[1], kv[0], 0x05040100);
        ol[e] = __builtin_amdgcn_perm(kv[1], kv[0], 0x07060302);
      }
      reinterpret_cast<v4u*>(kr)[un] = oh;
      reinterpret_cast<v4u*>(kr + CKN)[un] = ol;
    }
    RAW_BAR();
    SST(7);
    {
      unsigned uq[2] = {ub, ub + 1024};
      bf16x8 bf[8], a[4];
      const bf16x8 bm = BFRAG(ub - 64);
      asm volatile("" : "+v"(uq[0]), "+v"(uq[1]));
#pragma unroll
      for (int j = 1; j < 8; ++j) bf[j] = BFRAG(uq[j >> 2] + 256 * (j & 3));
#pragma unroll
      for (int T = 2; T < 16; ++T) (T < 8 ? acc[T] : far[T & 7]) = MFMA16(KFRAG(128 * T + 32), bm, (f32x4{}));
#pragma unroll
      for (int e = 0; e < 3; ++e) a[e] = KFRAG(128 * e);
      const u16* kq = ka - 128 * 7;
#pragma unroll 1
      for (int r = 0; r < 4; ++r) {
#pragma unroll
        for (int e = 0; e < 8; ++e) {
          a[(e + 3) & 3] = *reinterpret_cast<const bf16x8*>(kq + 128 * (7 - ((e + 3) & 7)) + (e >= 5 ? 32 : 0));
          const int j = (8 - e) & 7;
          bf[j] = BFRAG(uq[j >> 2] + 256 * (j & 3) + (e ? 64 : 0));
          __builtin_amdgcn_sched_barrier(0);
#pragma unroll
          for (int T = 7; T >= (e > 2 ? e : 2); --T) acc[T] = MFMA16(a[e & 3], bf[T - e], acc[T]);
          __builtin_amdgcn_sched_barrier(0);
        }
        kq += 32;
        uq[0] += 64;
        uq[1] += 64;
        asm volatile("" : "+v"(uq[0]), "+v"(uq[1]));
      }
    }
    SST(8);
    {
      unsigned k[8];
#pragma unroll
      for (int T = 2; T < 8; ++T) k[T] = SKIP(T);
#pragma unroll
      for (int T = 2; T < 8; ++T) TILE_OUT(T, acc[T], k[T]);
    }
    RAW_BAR();
    SST(10);
    if (w >= 4) CHUNK_ST(0);
    CHUNK_ST(1);
    if (w == 1) (void)wait_n<SLP_V, 32>(FL + FL_FZ + (blockIdx.x & 7) * 64, 64, TMO);
    DRAIN();
    if (l < 16) CHUNK_FLAG(0);
    SST(9);
#undef U_ROW
#undef U_VAL
#undef U_PUT
#undef ST_GET
  }
#define FAR_HALF(r, J0, J1)                                                                     \
  {                                                                                            \
    bf16x8 bf[16], af[16];                                                                     \
    unsigned u_ = ub + 64 * (r) + 256 * (J0), v_ = u_ + 1024;                                  \
    asm volatile("" : "+v"(u_), "+v"(v_));                                                     \
    _Pragma("unroll") for (int j = (J0); j < (J1); ++j) bf[j] = BFRAG((j - (J0) < 4 ? u_ : v_) + 256 * ((j - (J0)) & 3)); \
    _Pragma("unroll") for (int e = (9 - (J1) > 0 ? 9 - (J1) : 0); e < 16 - (J0); ++e) af[e] = KFRAG(128 * e - 32 * (r)); \
    __builtin_amdgcn_sched_barrier(0);                                                         \
    _Pragma("unroll") for (int j = (J1) - 1; j >= (J0); --j)                                   \
      _Pragma("unroll") for (int T = (j > 8 ? j : 8); T < 16; ++T) far[T - 8] = MFMA16(af[T - j], bf[j], far[T - 8]); \
  }
#define FAR_PIECE(p)                                                                            \
  {                                                                                            \
    int l = tid & 63;                                                                          \
    asm volatile("" : "+v"(l), "+v"(tx[2]), "+v"(tx[3]));                                      \
    if ((p) < 16) {                                                                            \
      const int r_ = (p) >> 2;                                                                 \
      if (((p) & 3) == 0) FAR_HALF(r_, 0, 3) else if (((p) & 3) == 1) FAR_HALF(r_, 3, 6)       \
      else if (((p) & 3) == 2) FAR_HALF(r_, 6, 9) else FAR_HALF(r_, 9, 16)                     \
    } else if ((p) == 16) {                                                                    \
      unsigned k_[8];                                                                          \
      _Pragma("unroll") for (int T = 0; T < 8; ++T) k_[T] = SKIP(8 + T);                       \
      _Pragma("unroll") for (int T = 0; T < 8; ++T) TILE_OUT(T, far[T], k_[T]);                \
    } else if ((p) == 17) {                                                                    \
      CHUNK_ST(2);                                                                             \
    } else if ((p) == 18) {                                                                    \
      CHUNK_ST(3);                                                                             \
    } else {                                                                                   \
      DRAIN();                                                                                 \
      if (l < 16) CHUNK_FLAG(2);                                                               \
    }                                                                                          \
  }
  static_assert(CVPRE <= 16, "the later pieces need the barriers of the segments");
  const unsigned rb = lds_base(reinterpret_cast<char*>(((c & 1) ? Rs : Rc) + w * CRN + c + 8 * g));
#pragma unroll
  for (int j = 0; j < 16; ++j) acc[j] = f32x4{};
  const __amdgpu_buffer_rsrc_t rx = MKRS(ws + OFF_X2, SZ_X), rz = MKRS(ws + OFF_X3, SZ_X);
  const __amdgpu_buffer_rsrc_t rf = MKRS(FL + FL_FR, 64 * 16 * 128);
  v2u st = {0u, 0u}, xx = {0u, 0u};
#define X2_LOAD(ss, far)                                                                                                      \
  {                                                                                                                      \
    xx = LD64(rx, (l >> 1) * (DM * 2) + (l & 1) * 8, (far) ? (int)SZ_X : ((ss) * SEG * DM + 8 * cv) * 2);                 \
    st = LD64(rf, l * 8, ((ss) * 4 + (blockIdx.x & 3)) * 512);                                                           \
  }
  unsigned tl = now10();
  bool late = false;
  for (int s = -CVPRE; s < NSEG; ++s) {
   if (s >= 0) {
    const int q = s & 3;
    if (w == 1) {
      late = __builtin_popcountll(__ballot(st.y != 0u)) >= CVLATE;
      for (unsigned spins = 0; !__all(umin3(xx.x, xx.y, st.y) != 0u); ++spins) {
        if (spin_timeout(spins, TMO)) break;
        if (GF_V && s >= GF0) {
          if (spins) __builtin_amdgcn_s_sleep(SLP_V);
          else if (s < NSEG - 6) gate(tl);
          tl = now10() + GF_V;
          X2_LOAD(s, CVT2 > 64 ? 0 : spins && __builtin_popcountll(__ballot(st.y != 0u)) < CVT2);
          continue;
        }
        if (GATE_V && spins == 0 && !__any(st.y != 0u)) gate(tl);
        if (spins && !__all(st.y != 0u)) __builtin_amdgcn_s_sleep(SLP_V);
        X2_LOAD(s, __builtin_popcountll(__ballot(st.y != 0u)) < CVTHR);
      }
      if (!GF_V || s < GF0) tl = now10() + GATE_V;
      TS(0, s, 64);
      const float s0 = __uint_as_float(st.x), s1 = dppr<0xb1>(s0), m2 = __uint_as_float(st.y);
      const float var = (m2 + dppr<0xb1>(m2) + (s0 - s1) * (s0 - s1) * (1.0f / DM)) * (1.0f / DM);
      const float mb = bfr1((s0 + s1) * (1.0f / DM)), ri = __builtin_amdgcn_rcpf(bfr1(__builtin_amdgcn_sqrtf(bfr1(bfr1(var) + 1e-5f))));
      const unsigned t0 = pk2(lo16(xx.x) - mb, hi16(xx.x) - mb), t1 = pk2(lo16(xx.y) - mb, hi16(xx.y) - mb);
      const unsigned u0 = pk2(lo16(t0) * ri, hi16(t0) * ri), u1 = pk2(lo16(t1) * ri, hi16(t1) * ri);
      u16* rp = Rc + (l & 1) * 4 * CRN + 32 * q + (l >> 1);
#define R_PUT(o) { rp[o] = (u16)u0; rp[(o) + CRN] = (u16)(u0 >> 16); rp[(o) + 2 * CRN] = (u16)u1; rp[(o) + 3 * CRN] = (u16)(u1 >> 16); }
      R_PUT(0) R_PUT(8 * CRN + 63)
      if (q == 0) { R_PUT(128) R_PUT(8 * CRN + 63 + 128) }
#undef R_PUT
      *reinterpret_cast<v2u*>(Sx + l * 4) = xx;
    }
    const u16* kq = ka + 32 * q;
    const bf16x8 a0 = *reinterpret_cast<const bf16x8*>(kq - 32), a1 = *reinterpret_cast<const bf16x8*>(kq);
    __syncthreads();
    TS(1, s, 0);
    const bf16x8 bp = BFRAG(rb + 64 * ((q + 3) & 3)), bc = BFRAG(rb + 64 * q);
    const float u0 = bf2f(Rc[w * CRN + 32 * q + c]), u1 = bf2f(Rc[w * CRN + 32 * q + 16 + c]);
    const float x0 = bf2f(Sx[c * 8 + w]), x1 = bf2f(Sx[(16 + c) * 8 + w]);
    acc[0] = MFMA16(a0, bp, (q == 0 ? acc[1] : acc[0]));
    const f32x4 tmp = MFMA16(a1, bc, acc[0]);
    const unsigned uu = pk2(u0 * dsk, u1 * dsk);
    const unsigned yy = pk2(xsum32((q & 1) ? tmp[2] : tmp[0]), xsum32((q & 1) ? tmp[3] : tmp[1]));
    const unsigned aa = pk2(lo16(yy) + lo16(uu), hi16(yy) + hi16(uu)), oo = pk2(x0 + lo16(aa), x1 + hi16(aa));
    if (g == (q >> 1)) {
      To[c * 8 + w] = (u16)oo;
      To[(16 + c) * 8 + w] = (u16)(oo >> 16);
    }
    __syncthreads();
    if (w == 0 && l < 32) {
      const v4u o = *reinterpret_cast<const v4u*>(To + l * 8);
      ST128(o, rz, l * 16, (s * 64 + cv) * 512);
      ST64(part_stats(o), rps, l * 8, ((64 + s) * 64 + cv) * 256);
    }
    if (w == 1) {
      if (GF_V && s + 1 >= GF0) st.y = 0u;
      else X2_LOAD(s + 1, !late);
    }
    const int left = 15 - (s >> 2);
#define PUSH(J0, SH)                                                                                              \
  if (left >= (J0)) {                                                                                             \
    _Pragma("unroll") for (int j = (J0); j < (J0) + 4 && j < 16; ++j)                                             \
      acc[j] = MFMA16(*reinterpret_cast<const bf16x8*>(kq - 32 - 128 * j), bp, (j + (SH) < 16 ? acc[(j + (SH)) & 15] : f32x4{})); \
  }
    TS(2, s, 0);
    if (w == 0) {
      DRAIN();
      if (l < 4) set_flag(FL + FL_FC + (((64 + s) * 4 + l) * 8 + (cv >> 3)) * 32 + (cv & 7));
    }
    TS(3, s, 0);
    if (q == 0) { PUSH(1, 1) PUSH(5, 1) PUSH(9, 1) PUSH(13, 1) }
    else { PUSH(1, 0) PUSH(5, 0) PUSH(9, 0) PUSH(13, 0) }
#undef PUSH
    MSTAMP();
   }
    const int pc = s < 0 ? s + CVPRE : (s % CVSTEP) ? 20 : CVPRE + s / CVSTEP;
    if (pc < 20) FAR_PIECE(pc);
  }
#undef KFRAG
#undef BFRAG
#undef X2_LOAD
#undef FAR_PIECE
#undef FAR_HALF
#undef TILE_OUT
#undef SKIP
#undef CHUNK_ST
#undef CHUNK_FLAG
}

__device__ __forceinline__ v4u ln_row_packed(const v4u xr) {
  float v[8];
#pragma unroll
  for (int e = 0; e < 4; ++e) { v[2 * e] = lo16(xr[e]); v[2 * e + 1] = hi16(xr[e]); }
  float s = ((v[0] + v[1]) + (v[2] + v[3])) + ((v[4] + v[5]) + (v[6] + v[7]));
  s = wave_sum(s);
  const float mean = s * (1.0f / DM);
  float q = 0.f;
#pragma unroll
  for (int j = 0; j < 8; ++j) { float t = v[j] - mean; q += t * t; }
  q = wave_sum(q);
  const float var = q * (1.0f / DM);
  const float mb = bfr(mean);
  const float sd = bfr(sqrtf(bfr(bfr(var) + 1e-5f)));
  const float ri = 1.0f / sd;
  v4u o;
#pragma unroll
  for (int e = 0; e < 4; ++e) {
    const unsigned t = pk2(v[2 * e] - mb, v[2 * e + 1] - mb);
    const float x0 = lo16(t), x1 = hi16(t);
    float q0 = x0 * ri, q1 = x1 * ri;
    q0 = __builtin_fmaf(__builtin_fmaf(-q0, sd, x0), ri, q0);
    q1 = __builtin_fmaf(__builtin_fmaf(-q1, sd, x1), ri, q1);
    o[e] = pk2(q0, q1);
  }
  return o;
}

__device__ __forceinline__ void ln4_packed(v4u (&x)[4]) {
  f32x2_t v[16];
#pragma unroll
  for (int e = 0; e < 16; ++e) v[e] = f32x2_t{lo16(x[e >> 2][e & 3]), hi16(x[e >> 2][e & 3])};
  const f32x2_t t = (((v[0] + v[1]) + (v[2] + v[3])) + ((v[4] + v[5]) + (v[6] + v[7]))) +
                    (((v[8] + v[9]) + (v[10] + v[11])) + ((v[12] + v[13]) + (v[14] + v[15])));
  const float mean = red_mod4(t.x + t.y) * (1.0f / DM);
  const float mb = bfr1(mean);
  f32x2_t q0 = {0.f, 0.f}, q1 = {0.f, 0.f};
#pragma unroll
  for (int e = 0; e < 16; e += 2) {
    v[e] -= mb;
    v[e + 1] -= mb;
    q0 += v[e] * v[e];
    q1 += v[e + 1] * v[e + 1];
  }
  q0 += q1;
  const float dm = mean - mb;
  const float var = fmaxf(red_mod4(q0.x + q0.y) * (1.0f / DM) - dm * dm, 0.f);
  const float sd = bfr1(sqrtf(bfr1(bfr1(var) + 1e-5f)));
  const float ri = 1.0f / sd;
#pragma unroll
  for (int e = 0; e < 16; ++e) {
    const unsigned t2 = pk2(v[e].x, v[e].y);
    const f32x2_t q = f32x2_t{lo16(t2), hi16(t2)} * ri;
    x[e >> 2][e & 3] = pk2(q.x, q.y);
  }
}

constexpr int MEGA_WGS = 256;
constexpr size_t MEGA_LDS = (size_t)(32 * 4 * 64 * 8 + 8 * SMS + 15 * QS + 128 + 16 * 64 * 8 + 8 * SOS + 512) * 2;
static_assert(MEGA_LDS <= 163840, "lds");
static_assert(MEGA_LDS >= (size_t)AW_CNT + 128 && MEGA_LDS >= (size_t)P_CN + 512, "lds");
static_assert(MEGA_LDS >= (size_t)CV_LDS && 8 * 1568 >= 768 * 16, "lds");
static_assert(MEGA_LDS >= (size_t)2 * 32 * SMS * 2 && MEGA_LDS >= 131072, "lds");
static_assert(MEGA_LDS >= (size_t)8 * 16640, "lds");
__device__ __forceinline__ MegaArgs role_args(const int b, const int wv) {
  auto p = (const __attribute__((address_space(4))) u64*)__builtin_amdgcn_kernarg_segment_ptr();
  asm volatile("" : "+s"(p));
#define KA(i) ((u16*)(__attribute__((address_space(1))) u16*)p[i])
  return MegaArgs{KA(0), KA(1), (char*)KA(2), KA(3), KA(4), KA(5), KA(6), KA(7), KA(8), KA(9), KA(10), KA(11), KA(12), KA(13), b, wv};
#undef KA
}
__global__ __launch_bounds__(512, 2) void mega_kernel(const u16* mem0, const u16* Dskip, char* ws, u16* out, const u16* x0,
                                                      const u16* logAre, const u16* Aim, const u16* Cre, const u16* Cim,
                                                      const u16* logdt, const u16* Wq, const u16* Wk, const u16* Wv,
                                                      const u16* Wo) {
  extern __shared__ __attribute__((aligned(16))) char smem[];
  const int b = vbid(), wv = __builtin_amdgcn_readfirstlane(threadIdx.x >> 6);
#define A role_args(b, wv)
  u64 tag = 0;
  {
    const unsigned t0 = now10();
    if ((b >= 32 && b < 64) || (b >= 200 && b < 232)) start_kgen(A, smem, b < 64 ? b - 32 : b - 168);
    else if (b >= 64 && b < 96) tag = start_stats<4>(A, b - 64);
    else if (b >= 240) (void)start_stats<8>(A, b - 224);
    const unsigned dl = (b >= 32 && b < 64) ? DL_C : (b >= 64 && b < 96) ? DL_A : b >= 240 ? DL_R2 : ((b >= 96 && b < 104) || b >= 232) ? DL_R : (b >= 200 && b < 232) ? DL_Q : 0u;
    while (now10() - t0 < dl) __builtin_amdgcn_s_sleep(16);
  }
  if ((EXP_OFF & 1) && b >= 64 && b < 80) return;
  if ((EXP_OFF & 8) && b >= 200 && b < 232) return;
  if ((EXP_OFF & 16) && b >= 32 && b < 64) return;
  if ((EXP_OFF & 32) && b >= 80 && b < 96) return;
  if (b < 64) role_chain(A, smem, b >> 5, b & 31, 0);
  else if (b < 96) role_aw(A, smem, (b >> 4) & 1, b & 15, tag);
  else if (b >= 104 && b < 168) role_conv(A, smem, b - 104);
  else {
    const bool kv = b >= 168 && b < 232;
    role_proj(A, smem, b >= (kv ? 200 : 240), kv ? (b - 168) & 31 : b & 15, kv);
  }
#undef A
}

extern "C" void kernel_launch(void* const* d_in, const int* in_sizes, int n_in, void* d_out, int out_size, void* d_ws,
                              size_t ws_size, hipStream_t stream) {
  MegaArgs A;
  A.x0 = (const u16*)d_in[0];
  A.logAre = (const u16*)d_in[1];
  A.Aim = (const u16*)d_in[2];
  A.Cre = (const u16*)d_in[3];
  A.Cim = (const u16*)d_in[4];
  A.logdt = (const u16*)d_in[5];
  A.Dskip = (const u16*)d_in[6];
  A.mem0 = (const u16*)d_in[8];
  A.Wq = (const u16*)d_in[9];
  A.Wk = (const u16*)d_in[10];
  A.Wv = (const u16*)d_in[11];
  A.Wo = (const u16*)d_in[12];
  A.ws = (char*)d_ws;
  A.out = (u16*)d_out;

  (void)hipFuncSetAttribute((const void*)mega_kernel, hipFuncAttributeMaxDynamicSharedMemorySize, (int)MEGA_LDS);
  mega_kernel<<<MEGA_WGS, 512, MEGA_LDS, stream>>>(A.mem0, A.Dskip, A.ws, A.out, A.x0, A.logAre, A.Aim, A.Cre, A.Cim, A.logdt,
                                                   A.Wq, A.Wk, A.Wv, A.Wo);
}
```

```cpp
#include <hip/hip_runtime.h>
#include <hip/hip_bf16.h>
#include <stdint.h>

constexpr int L_SEQ = 2048;
constexpr int DM    = 512;
constexpr int NST   = 128;
constexpr int NH    = 32;
constexpr int DH    = 128;
constexpr int HD    = NH * DH;
constexpr int MM    = 8;
constexpr int SEG   = 32;
constexpr int NSEG  = L_SEQ / SEG;
constexpr int LK    = MM + SEG;

typedef unsigned short u16;
using bf16x8 = __attribute__((ext_vector_type(8))) short;
using f32x4  = __attribute__((ext_vector_type(4))) float;
using f32x16 = __attribute__((ext_vector_type(16))) float;

static __device__ __forceinline__ u16 f2bf(float f) {
  unsigned u = __float_as_uint(f);
  u += 0x7fffu + ((u >> 16) & 1u);
  return (u16)(u >> 16);
}
static __device__ __forceinline__ float bf2f(u16 h) { return __uint_as_float(((unsigned)h) << 16); }
static __device__ __forceinline__ float bfr(float f) { return bf2f(f2bf(f)); }

constexpr size_t SZ_X      = (size_t)L_SEQ * DM * 2;
constexpr size_t OFF_KF    = 0;
constexpr size_t OFF_UT    = OFF_KF + (size_t)DM * L_SEQ * 4;
constexpr size_t OFF_YT    = OFF_UT + SZ_X;
constexpr size_t OFF_XA    = OFF_YT + SZ_X;
constexpr size_t OFF_H     = OFF_XA + SZ_X;
constexpr size_t OFF_X2    = OFF_H + SZ_X;
constexpr size_t OFF_U2    = OFF_X2 + SZ_X;
constexpr size_t OFF_X3    = OFF_U2 + SZ_X;
constexpr size_t SZ_QK     = (size_t)L_SEQ * 2 * HD * 2;
constexpr size_t OFF_QK    = OFF_X3 + SZ_X;
constexpr size_t SZ_O      = (size_t)L_SEQ * HD * 2;
constexpr size_t OFF_VF    = OFF_QK + 2 * SZ_QK;
constexpr size_t SZ_PW     = (size_t)2 * NSEG * 16 * SEG * DM * 2;
constexpr size_t OFF_PW    = OFF_VF + 2 * SZ_O;
constexpr size_t SZ_MEM    = (size_t)NSEG * MM * HD * 2;
constexpr size_t OFF_MEMK  = OFF_PW + SZ_PW;
constexpr size_t OFF_MEMV  = OFF_MEMK + 2 * SZ_MEM;
constexpr size_t OFF_GT    = OFF_MEMV + 2 * SZ_MEM;
constexpr size_t OFF_EP    = OFF_GT + 65536;
constexpr size_t OFF_RD    = OFF_EP + 4096;
constexpr size_t OFF_KG    = OFF_RD + 4096;
constexpr size_t OFF_G     = OFF_KG + 8192;
constexpr size_t SZ_ACC    = (size_t)2 * 65536;
constexpr size_t OFF_Z     = OFF_GT + (size_t)128 * DM * 4;
constexpr size_t OFF_ACC   = OFF_Z;
constexpr size_t OFF_FL    = OFF_ACC + 2 * SZ_ACC;
constexpr int FL_FK  = 0;
constexpr int FL_FW  = FL_FK + 64 * 8 * 32;
constexpr int FL_FR  = FL_FW + 2 * 64 * 16 * 32;
constexpr int FL_FC  = FL_FR + 64 * 16 * 32;
constexpr int FL_FQ  = FL_FC + 2 * 64 * 32 * 32;
constexpr int FL_FP  = FL_FQ + 2 * 64 * 32 * 32;
constexpr int FL_FZ  = FL_FP + 64 * 32 * 32;
constexpr int FL_TMO = FL_FZ + 16 * 32;
constexpr int FL_END = FL_TMO + 64;
constexpr size_t Z_BYTES   = OFF_FL + (size_t)FL_END * 4 - OFF_Z;
constexpr size_t WS_TOTAL  = OFF_Z + Z_BYTES;
static_assert(Z_BYTES % 16 == 0 && OFF_Z % 256 == 0 && Z_BYTES <= 4 * 64 * 8192 && OFF_G + 2048 <= OFF_Z, "zero region");
static_assert(WS_TOTAL + (4 << 20) <= (size_t)256 * 1024 * 1024, "workspace too large");

static_assert(WS_TOTAL - OFF_XA <= (size_t)384 * 8 * 65536, "translation warm-up");
constexpr int CKN = 2208;
static_assert((size_t)DM * 2 * CKN * 2 <= (size_t)DM * L_SEQ * 4 + SZ_X, "K buffer");
__device__ __forceinline__ float pos_of_index(int p) {
  if (p < 256) return (float)p;
  if (p < 384) return (float)(256 + 2 * (p - 256));
  if (p < 512) return (float)(512 + 4 * (p - 384));
  if (p < 640) return (float)(1024 + 8 * (p - 512));
  return 2048.0f;
}
__device__ __forceinline__ int index_of_l(int l) {
  float pf = bfr((float)l);
  int p = (int)pf;
  if (p < 256) return p;
  if (p < 512) return 256 + ((p - 256) >> 1);
  if (p < 1024) return 384 + ((p - 512) >> 2);
  if (p < 2048) return 512 + ((p - 1024) >> 3);
  return 640;
}

template <int CTRL> __device__ __forceinline__ float dppr(float x) {
  return __builtin_bit_cast(float, __builtin_amdgcn_update_dpp(0, __builtin_bit_cast(int, x), CTRL, 0xf, 0xf, false));
}
__device__ __forceinline__ float wave_sum(float v) {
  v += dppr<0x121>(v);
  v += dppr<0x122>(v);
  v += dppr<0x124>(v);
  v += dppr<0x128>(v);
  {
    auto r = __builtin_amdgcn_permlane16_swap(__float_as_uint(v), __float_as_uint(v), false, false);
    v = __uint_as_float(r[0]) + __uint_as_float(r[1]);
  }
  {
    auto r = __builtin_amdgcn_permlane32_swap(__float_as_uint(v), __float_as_uint(v), false, false);
    v = __uint_as_float(r[0]) + __uint_as_float(r[1]);
  }
  return v;
}
__device__ __forceinline__ void ln_row8(float (&v)[8]) {
  float s = 0.f;
#pragma unroll
  for (int j = 0; j < 8; ++j) s += v[j];
  s = wave_sum(s);
  const float mean = s * (1.0f / DM);
  float q = 0.f;
#pragma unroll
  for (int j = 0; j < 8; ++j) { float t = v[j] - mean; q += t * t; }
  q = wave_sum(q);
  const float var = q * (1.0f / DM);
  const float mb = bfr(mean);
  const float sd = bfr(sqrtf(bfr(bfr(var) + 1e-5f)));
#pragma unroll
  for (int j = 0; j < 8; ++j) v[j] = bfr(bfr(v[j] - mb) / sd);
}

#define RLX_AGENT __ATOMIC_RELAXED, __HIP_MEMORY_SCOPE_AGENT
typedef __attribute__((address_space(1))) unsigned gu32;
typedef unsigned v4u __attribute__((ext_vector_type(4)));
constexpr int AUX_SC1 = 16;
constexpr unsigned SPIN_CAP = 1u << 18;

__device__ __forceinline__ bool wait_flags(const unsigned* flags, int lane, int nfl, unsigned want, unsigned* tmo) {
  for (unsigned spins = 0;; ++spins) {
    unsigned v = want;
    if (lane < nfl) v = __hip_atomic_load((gu32*)flags + lane * 32, RLX_AGENT);
    if (__all(v >= want)) break;
    if (spins > SPIN_CAP) {
      if (lane == 0) __hip_atomic_store((gu32*)tmo, 1u, RLX_AGENT);
      return false;
    }
  }
  __builtin_amdgcn_fence(__ATOMIC_ACQUIRE, "wavefront");
  return true;
}

#define STAMP(i)
#define STAMP_DUMP()
#define WSTAMP(i)
template <int CTRL> __device__ __forceinline__ float dppf(float old, float x) {
  return __builtin_bit_cast(float, __builtin_amdgcn_update_dpp(__builtin_bit_cast(int, old), __builtin_bit_cast(int, x), CTRL, 0xf, 0xf, false));
}
__device__ __forceinline__ float wave_max_dpp(float x) {
  const float ninf = -3e38f;
  x = fmaxf(x, dppf<0x111>(ninf, x));
  x = fmaxf(x, dppf<0x112>(ninf, x));
  x = fmaxf(x, dppf<0x114>(ninf, x));
  x = fmaxf(x, dppf<0x118>(ninf, x));
  x = fmaxf(x, dppf<0x142>(ninf, x));
  x = fmaxf(x, dppf<0x143>(ninf, x));
  return __builtin_bit_cast(float, __builtin_amdgcn_readlane(__builtin_bit_cast(int, x), 63));
}
__device__ __forceinline__ float wave_sum_dpp(float x) {
  x += dppf<0x111>(0.f, x);
  x += dppf<0x112>(0.f, x);
  x += dppf<0x114>(0.f, x);
  x += dppf<0x118>(0.f, x);
  x += dppf<0x142>(0.f, x);
  x += dppf<0x143>(0.f, x);
  return __builtin_bit_cast(float, __builtin_amdgcn_readlane(__builtin_bit_cast(int, x), 63));
}
constexpr int SMS = 528;
constexpr int QS = 144;
constexpr int VS = 80;
constexpr int SOS = 144;
#define VAR_NREP 4
constexpr int NREP = VAR_NREP;
__device__ __forceinline__ float xsum16(float x) {
  auto r = __builtin_amdgcn_permlane16_swap(__float_as_uint(x), __float_as_uint(x), false, false);
  return __uint_as_float(r[0]) + __uint_as_float(r[1]);
}
__device__ __forceinline__ float xsum32(float x) {
  auto r = __builtin_amdgcn_permlane32_swap(__float_as_uint(x), __float_as_uint(x), false, false);
  return __uint_as_float(r[0]) + __uint_as_float(r[1]);
}
__device__ __forceinline__ float xmax16(float x) {
  auto r = __builtin_amdgcn_permlane16_swap(__float_as_uint(x), __float_as_uint(x), false, false);
  return fmaxf(__uint_as_float(r[0]), __uint_as_float(r[1]));
}
__device__ __forceinline__ float xmax32(float x) {
  auto r = __builtin_amdgcn_permlane32_swap(__float_as_uint(x), __float_as_uint(x), false, false);
  return fmaxf(__uint_as_float(r[0]), __uint_as_float(r[1]));
}
__device__ __forceinline__ float red_mod4(float x) {
  x += dppf<0x124>(0.f, x);
  x += dppf<0x128>(0.f, x);
  return xsum32(xsum16(x));
}
typedef __bf16 bf16x2_t __attribute__((ext_vector_type(2)));
typedef float f32x2_t __attribute__((ext_vector_type(2)));
__device__ __forceinline__ unsigned pk2(float a, float b) {
  f32x2_t v = {a, b};
  bf16x2_t r = __builtin_convertvector(v, bf16x2_t);
  return __builtin_bit_cast(unsigned, r);
}
__device__ __forceinline__ float one(float x) {
  asm("" : "+v"(x));
  return x;
}
__device__ __forceinline__ unsigned pk2s(float a, float b) { return pk2(one(a), one(b)); }
__device__ __forceinline__ float dot2sq(const unsigned x, const float acc) {
  return __builtin_amdgcn_fdot2_f32_bf16(__builtin_bit_cast(bf16x2_t, x), __builtin_bit_cast(bf16x2_t, x), acc, false);
}
__device__ __forceinline__ float lo16(unsigned u) { return __uint_as_float(u << 16); }
__device__ __forceinline__ float hi16(unsigned u) { return __uint_as_float(u & 0xffff0000u); }
__device__ __forceinline__ float bfr1(float x) { return lo16(pk2(x, x)); }
__device__ __forceinline__ float half_sum(float v) {
  v += dppr<0x121>(v);
  v += dppr<0x122>(v);
  v += dppr<0x124>(v);
  v += dppr<0x128>(v);
  return xsum16(v);
}
__device__ __forceinline__ void scale_pair(float a, float b, float scale, float& oa, float& ob) {
  unsigned u = pk2(a, b);
  unsigned v = pk2s(lo16(u) * scale, hi16(u) * scale);
  oa = lo16(v);
  ob = hi16(v);
}

#define XMAP 2
__device__ __forceinline__ int vbid() {
  const int b = blockIdx.x, x = b & 7, t = b >> 3;
  if (XMAP == 0) {
    if (b < 64) return (b & 32) + 4 * x + (t & 3);
    if (b < 96) return (b & ~15) + 2 * x + (t & 1);
    if (b >= 104 && b < 168) return 104 + 8 * x + t - 13;
    if (b >= 168 && b < 232) return 168 + ((b - 168) & 32) + 4 * x + ((t - 21) & 3);
    return b;
  }
  if (XMAP == 3) {
    const int y = x & 3;
    if (t >= 16 && t < 24) return 104 + 16 * y + 2 * (x & 4) + t - 16;
    if (x < 4) return t < 16 ? 4 * (t & 8) + 8 * x + (t & 7) : 64 + 4 * (t & 4) + 4 * y + (t & 3);
    if (t >= 24) return (t >= 28 ? 240 : y < 2 ? 96 : 224) + 4 * y + (t & 3);
    return 168 + 4 * (t & 8) + 8 * y + (t & 7);
  }
  if (XMAP == 4) {
    const int y = x & 3;
    if (x < 4) return t < 16 ? 4 * (t & 8) + 8 * x + (t & 7) : t < 28 ? 104 + 16 * y + t - 16 : 64 + 4 * y + (t & 3);
    if (t >= 24) return (t >= 28 ? 240 : y < 2 ? 96 : 224) + 4 * y + (t & 3);
    if (t < 16) return 168 + 4 * (t & 8) + 8 * y + (t & 7);
    return t < 20 ? 104 + 16 * y + t - 4 : 80 + 4 * y + (t & 3);
  }
  if (XMAP == 2) {
    if (x < 4) return t < 16 ? 4 * (t & 8) + 8 * x + (t & 7) : 104 + 16 * x + t - 16;
    const int y = x - 4;
    if (t >= 24) return (t >= 28 ? 240 : y < 2 ? 96 : 224) + 4 * y + (t & 3);
    if (t < 16) return 168 + 4 * (t & 8) + 8 * y + (t & 7);
    return 64 + 4 * (t & 4) + 4 * y + (t & 3);
  }
  if (x < 2) return 2 * (t & 16) + 16 * x + (t & 15);
  const int i = 16 * (x - 2) + (t & 15);
  if (t < 16) {
    const int k = (i * 171) >> 9, r = i - 3 * k;
    return r == 0 ? 168 + k : r == 1 ? 200 + k : 64 + 16 * (k & 1) + (k >> 1);
  }
  const int g = i >> 3, e = i & 7;
  if (g == 3) return 96 + e;
  if (g == 7) return 232 + e;
  if (g == 9) return 240 + e;
  if (g == 11) return 248 + e;
  return 104 + 8 * (g - (g > 3) - (g > 7) - (g > 9)) + e;
}
#define MSTAMP()
#define WSTAMP2()
#define SST(k)
#define TS(k, ss, t)
#define LS(k, ss)
#define LS_DUMP()
struct MegaArgs {
  const u16* mem0;
  const u16* Dskip;
  char* ws;
  u16* out;
  const u16* x0;
  const u16 *logAre, *Aim, *Cre, *Cim, *logdt;
  const u16 *Wq, *Wk, *Wv, *Wo;
  int vb, wv;
};

__device__ __forceinline__ int lane_id() {
  int l;
  asm volatile("v_mbcnt_lo_u32_b32 %0, -1, 0\n\tv_mbcnt_hi_u32_b32 %0, -1, %0" : "=v"(l));
  __builtin_assume(l >= 0 && l < 64);
  return l;
}
__device__ __forceinline__ bool spin_timeout(unsigned spins, unsigned* tmo) {
  if (spins > SPIN_CAP) {
    if (lane_id() == 0) __hip_atomic_store((gu32*)tmo, 1u, RLX_AGENT);
    return true;
  }
  return false;
}
#define ROT_H (4 * (h & 1))
#define ROT_T (h & 2)
#define GF_R 400
#define GF_V 0
#define GF_H 150
#define CVT2 48
#define GF_P 0
#define GF0 8
#define SLP_H 12
#define HX0 6
#define CVSTEP 2
#define CVLATE 64
#define PACE 203
#define PACE0 6
#define RPRIO 3
#define DL_R2 1400
#define PNL 8
#define GSL 12
#define AWDIR 1
#define AWAT 0
#define AWRP 0
#define PEPS 4
#define FGATE 0
#define DL_C 0
#define DL_A 800
#define DL_R 0
#define DL_Q 0
#define KC0 0
#define CPRIO 1
#define P1PRIO 3
#define QLEAD 2
#define POLL_SLEEP 12
#define SLP_C POLL_SLEEP
#define SLP_A POLL_SLEEP
#define SLP_W POLL_SLEEP
#define SLP_R POLL_SLEEP
#define SLP_V POLL_SLEEP
#define SLP_Q POLL_SLEEP
#define SLP_M 4
#define AWPRIO 2
#define SLP_S 8
#define SPOS 4
#define CVPRE 12
#define QPD 3
#define QPRI 2
#define TLBW 1
#define QHV 0
#define QCHK 0
#define LEADN 99
#define GATE_V 170
#define GATE_R 380
#define GATE_N 380
#define AWAHEAD 1
#define GATE_M 150
#define PRI_A 3
#define PRI_WA 2
#define PRI_WB 1
#define AWSKIP 3
#define PRI_0 3
#define AWDEF 0
#define SLP_F 2
#define EXP_OFF 0
#define AWSPEC 1
#define CVTHR 48
#define NR_C 8
#define NR_R 8
#define NR_W 8
#define NR_N 2
#define QAHEAD 0
#define NS0 4
#define SLP_N POLL_SLEEP
template <int SLP, int WPL>
__device__ __forceinline__ bool wait_n(const unsigned* f, int n, unsigned* tmo) {
  const int l = lane_id();
  for (unsigned spins = 0;; ++spins) {
    unsigned v = 1u;
    if (l < n) v = __hip_atomic_load((gu32*)f + (l / WPL) * 32 + l % WPL, RLX_AGENT);
    if (__all(v != 0u)) break;
    if (spin_timeout(spins, tmo)) return false;
    __builtin_amdgcn_s_sleep(SLP);
  }
  asm volatile("" ::: "memory");
  return true;
}
__device__ __forceinline__ unsigned now10() { return (unsigned)__builtin_amdgcn_s_memrealtime(); }
__device__ __forceinline__ void gate(const unsigned t) {
  for (int i = 0; i < 64; ++i) {
    const int d = (int)(t - now10());
    if (d <= 0) break;
    if (d > 100) __builtin_amdgcn_s_sleep(32);
    else if (d > 25) __builtin_amdgcn_s_sleep(8);
    else __builtin_amdgcn_s_sleep(2);
  }
}
template <int SLP, int WPL, int GT>
__device__ __forceinline__ void grp_wait(const bool leader, volatile unsigned* sw, const unsigned val, const unsigned* f, int n,
                                         unsigned* tmo, unsigned& tl) {
  if (leader) {
    const int l = lane_id();
    for (unsigned spins = 0;; ++spins) {
      unsigned v = 1u;
      if (l < n) v = __hip_atomic_load((gu32*)f + (l / WPL) * 32 + l % WPL, RLX_AGENT);
      if (__all(v != 0u) || spin_timeout(spins, tmo)) break;
      if (GT && spins == 0) gate(tl);
      else __builtin_amdgcn_s_sleep(SLP);
    }
    if (GT) tl = now10() + GT;
    if (sw && l == 0) *sw = val;
  } else {
    for (unsigned spins = 0; *sw < val; ++spins) {
      if (spins > 64u * SPIN_CAP) break;
      if (FGATE && GT && spins == 0) gate(tl);
      __builtin_amdgcn_s_sleep(SLP_F);
    }
    if (FGATE && GT) tl = now10() + GT;
  }
  asm volatile("" ::: "memory");
}
template <int SLP, int WPL>
__device__ __forceinline__ bool wg_wait(const unsigned* f, int n, unsigned* tmo) {
  if (threadIdx.x < 64) (void)wait_n<SLP, WPL>(f, n, tmo);
  __syncthreads();
  asm volatile("" ::: "memory");
  return true;
}
__device__ __forceinline__ void set_flag(unsigned* f) { __hip_atomic_store((gu32*)f, 1u, RLX_AGENT); }
#define DRAIN() asm volatile("s_waitcnt vmcnt(0)" ::: "memory")
#define MKRS(ptr, bytes) __builtin_amdgcn_make_buffer_rsrc((void*)(ptr), 0, (int)(bytes), 0x00020000)
typedef unsigned v2u __attribute__((ext_vector_type(2)));
#define LD128(rs, voff, soff) __builtin_amdgcn_raw_buffer_load_b128(rs, voff, soff, AUX_SC1)
#define LD64(rs, voff, soff) __builtin_amdgcn_raw_buffer_load_b64(rs, voff, soff, AUX_SC1)
#define ST128(v, rs, voff, soff)                                              \
  do {                                                                        \
    const v4u st_d_ = (v);                                                    \
    __builtin_amdgcn_raw_buffer_store_b128(st_d_, rs, voff, soff, AUX_SC1);   \
    asm volatile("s_nop 1" ::"v"(st_d_) : "memory");                          \
  } while (0)
#define ST64(v, rs, voff, soff) __builtin_amdgcn_raw_buffer_store_b64(v, rs, voff, soff, AUX_SC1)
#define BF8(x) __builtin_bit_cast(bf16x8, x)
#define MFMA16(a, b, c) __builtin_amdgcn_mfma_f32_16x16x32_bf16(a, b, c, 0, 0, 0)
typedef short bf16x4 __attribute__((ext_vector_type(4)));
#define MFMA16K(a, b, c) \
  __builtin_amdgcn_mfma_f32_16x16x16bf16_1k(__builtin_bit_cast(bf16x4, a), __builtin_bit_cast(bf16x4, b), c, 0, 0, 0)
__device__ __forceinline__ float fmax_nn(float a, float b) { return __builtin_elementwise_maximum(a, b); }
typedef __bf16 bfv8 __attribute__((ext_vector_type(8)));
typedef __bf16 bfv16 __attribute__((ext_vector_type(16)));
typedef unsigned v8u __attribute__((ext_vector_type(8)));
#define SMF(a, b, c, idx) \
  __builtin_amdgcn_smfmac_f32_16x16x64_bf16(__builtin_bit_cast(bfv8, a), __builtin_bit_cast(bfv16, b), c, idx, 0, 0)
__device__ __forceinline__ float swap_add32(float a, float b) {
  auto r = __builtin_amdgcn_permlane32_swap(__float_as_uint(a), __float_as_uint(b), false, false);
  return __uint_as_float(r[0]) + __uint_as_float(r[1]);
}
__device__ __forceinline__ float swap_add16(float a, float b) {
  auto r = __builtin_amdgcn_permlane16_swap(__float_as_uint(a), __float_as_uint(b), false, false);
  return __uint_as_float(r[0]) + __uint_as_float(r[1]);
}
__device__ __forceinline__ v4u smf_fix(const v4u d) {
  auto x = __builtin_amdgcn_permlane16_swap(d.x, d.z, false, false);
  auto y = __builtin_amdgcn_permlane16_swap(d.y, d.w, false, false);
  return v4u{x[0], x[1], y[0], y[1]};
}
__device__ __forceinline__ v8u smf_bfrag(const u16* p) {
  const v2u a = *reinterpret_cast<const v2u*>(p), b = *reinterpret_cast<const v2u*>(p + 8);
  const v2u c = *reinterpret_cast<const v2u*>(p + 32), d = *reinterpret_cast<const v2u*>(p + 40);
  return v8u{a.x, b.x, a.y, b.y, c.x, d.x, c.y, d.y};
}

#define VAR_PPA 0
#define VAR_PPB 0
#define VAR_THR 0
#define VAR_THR2 0
#define CV_LEAD 100
#define VAR_EARLY 1
#define VAR_DRAIN 1
#define VAR_D1 0
#define VAR_D2 0
#define VAR_DR2 1
#define VAR_S1 0
#define VAR_S2 0
typedef __attribute__((address_space(1))) unsigned long long gu64;
typedef __attribute__((address_space(1))) char gch;
__device__ __forceinline__ int cvt_rpi(float x) {
  int r;
  asm("v_cvt_rpi_i32_f32 %0, %1" : "=v"(r) : "v"(x));
  return r;
}
typedef unsigned long long u64;
__device__ __forceinline__ u64 call_tag(const MegaArgs& A) {
  u64 t = *(const __attribute__((address_space(4))) u64*)(A.ws + OFF_EP) * 6364136223846793005ull + 1442695040888963407ull;
  asm volatile("" : "+s"(t)::"memory");
  return t;
}
__device__ __forceinline__ u64 tagged(const u64 tag, const unsigned v) { return (tag & 0xffffffffffffull) | (u64)(v >> 16) << 48; }
__device__ __forceinline__ bool has_tag(const u64 tag, const unsigned lo, const unsigned hi) {
  return lo == (unsigned)tag && (hi & 0xffffu) == ((unsigned)(tag >> 32) & 0xffffu);
}
__device__ __forceinline__ u64 ready_look(const MegaArgs& A) {
  return __hip_atomic_load((gu64*)(A.ws + OFF_RD) + (blockIdx.x & 7) * 64 + lane_id(), RLX_AGENT);
}
__device__ __forceinline__ void ready_wait(const MegaArgs& A, const u64 tag, u64 v) {
  for (unsigned spins = 0; !__all(v == tag) && spins < SPIN_CAP; ++spins) {
    __builtin_amdgcn_s_sleep(8);
    v = ready_look(A);
  }
  asm volatile("" ::: "memory");
}
typedef short v4s __attribute__((ext_vector_type(4)));
typedef __attribute__((address_space(3))) v4s* lp4s;
__device__ __forceinline__ void glds16(const void* sbase, const unsigned voff, const unsigned lds) {
  unsigned keep;
  asm volatile("s_nop 4\n\ts_mov_b32 %0, m0\n\ts_mov_b32 m0, %3\n\ts_nop 0\n\tglobal_load_lds_dwordx4 %1, %2\n\ts_mov_b32 m0, %0"
               : "=&s"(keep)
               : "v"(voff), "s"(sbase), "s"(lds)
               : "memory");
}
__device__ __forceinline__ void glds16c(const void* sbase, const unsigned voff, const unsigned lds) {
  unsigned keep;
  asm volatile("s_nop 4\n\ts_mov_b32 %0, m0\n\ts_mov_b32 m0, %3\n\ts_nop 0\n\tglobal_load_lds_dwordx4 %1, %2 sc1\n\ts_mov_b32 m0, %0"
               : "=&s"(keep)
               : "v"(voff), "s"(sbase), "s"(lds)
               : "memory");
}
__device__ __forceinline__ void glds64c(const void* sbase, const unsigned voff, const unsigned lds) {
  unsigned keep;
  asm volatile("s_nop 4\n\ts_mov_b32 %0, m0\n\ts_mov_b32 m0, %3\n\ts_nop 0\n\tglobal_load_lds_dwordx4 %1, %2 sc1\n\t"
               "global_load_lds_dwordx4 %1, %2 offset:1024 sc1\n\tglobal_load_lds_dwordx4 %1, %2 offset:2048 sc1\n\t"
               "global_load_lds_dwordx4 %1, %2 offset:3072 sc1\n\ts_mov_b32 m0, %0"
               : "=&s"(keep)
               : "v"(voff), "s"(sbase), "s"(lds)
               : "memory");
}
__device__ __forceinline__ v2u trd(const unsigned a) {
  return __builtin_bit_cast(v2u, __builtin_amdgcn_ds_read_tr16_b64_v4i16((lp4s)(unsigned long)a));
}
__device__ __forceinline__ unsigned lds_base(char* smem) { return (unsigned)(unsigned long)(__attribute__((address_space(3))) char*)smem; }
#define RAW_BAR()                                           \
  do {                                                      \
    asm volatile("s_waitcnt lgkmcnt(0)" ::: "memory");      \
    __builtin_amdgcn_s_barrier();                           \
    asm volatile("" ::: "memory");                          \
  } while (0)
#define WAIT_VM(n) asm volatile("s_waitcnt vmcnt(" #n ")" ::: "memory")
template <bool SP>
__device__ __forceinline__ void wqkv_issue(const u16* Wm, const int hf, const int h, const int w, const int l, const unsigned sb) {
  const char* base = (const char*)Wm + (size_t)(256 * hf + 32 * w) * (HD * 2) + h * (DH * 2);
#pragma unroll
  for (int i = 0; i < 8; ++i) {
    const int r = l >> 4;
    const int f = SP ? ((r & 1) | ((i & 1) << 1) | (((i >> 1) & 1) << 2)) : (r | (((i >> 1) & 1) << 2));
    glds16(base, (unsigned)((4 * i + r) * (HD * 2) + (((l & 15) ^ (f << 1)) << 4)), sb + w * 16384 + hf * 8192 + i * 1024);
  }
}

__device__ __forceinline__ unsigned row_stats(const v4u raw) {
  float v[8];
#pragma unroll
  for (int e = 0; e < 4; ++e) { v[2 * e] = lo16(raw[e]); v[2 * e + 1] = hi16(raw[e]); }
  float s = 0.f;
#pragma unroll
  for (int e = 0; e < 8; ++e) s += v[e];
  const float mean = wave_sum(s) * (1.0f / DM);
  float q = 0.f;
#pragma unroll
  for (int e = 0; e < 8; ++e) { const float t = v[e] - mean; q += t * t; }
  const float var = wave_sum(q) * (1.0f / DM);
  const float sd = bfr(sqrtf(bfr(bfr(var) + 1e-5f)));
  return (unsigned)f2bf(mean) | ((unsigned)f2bf(sd) << 16);
}
__device__ __forceinline__ void ask_slow(const MegaArgs& A, const int k, unsigned (&t)[3]) {
  const int l = lane_id(), x = blockIdx.x & 7;
  const char *wq = (const char*)A.Wq, *wv = (const char*)A.Wv;
  asm("" : "+s"(wq), "+s"(wv));
#pragma unroll
  for (int m = 0; m < 3; ++m) {
    const char* W = m == 1 ? (const char*)A.Wk : ((m == 0) == (x < 4)) ? wv : wq;
    const int a = (int)((unsigned long)W >> 8) + 8 * (x & 3), h = k < 32 ? ((1 - a) & 3) + 4 * (k & 1) : (31 - a) & 31;
    const int r = k < 32 ? k >> 1 : k - 32;
    t[m] = __builtin_amdgcn_raw_buffer_load_b32(MKRS(W, (unsigned)k < 48u && h < 8 ? DM * HD * 2 : 0), (l >> 1) * (HD * 2) + (l & 1) * 128,
                                                (32 * (r & 15)) * (HD * 2) + (8 * (x & 3) + (h & 7)) * 256, 0);
  }
}
#define ASKED(t) asm volatile("" ::"v"(t[0]), "v"(t[1]), "v"(t[2]))
template <int N>
__device__ __forceinline__ u64 start_stats(const MegaArgs& A, const int idx) {
  const int w = A.wv, l = lane_id();
  const int r0 = N * (8 * idx + w);
  unsigned ta[3], tb[3];
  v4u raw[N];
#pragma unroll
  for (int i = 0; i < N; ++i) raw[i] = *reinterpret_cast<const v4u*>(A.x0 + (size_t)(r0 + i) * DM + l * 8);
  asm volatile("" ::: "memory");
  ask_slow(A, 16 * w + (idx & 3) + (idx >> 4) * N, ta);
  if (N > 4) ask_slow(A, 16 * w + (idx & 3) + 12, tb);
  const u64 tag = call_tag(A);
  unsigned stw = 0u;
#pragma unroll
  for (int i = 0; i < N; ++i) {
    const unsigned t = row_stats(raw[i]);
    if ((l >> 1) == i) stw = t;
  }
  if (l < 2 * N) __hip_atomic_store((gu64*)(A.ws + OFF_GT) + 2 * r0 + l, tagged(tag, (l & 1) ? stw : stw << 16), RLX_AGENT);
  SST(0);
  ASKED(ta);
  if (N > 4) ASKED(tb);
  return tag;
}
struct Cx {
  f32x2_t r, i;
};
__device__ __forceinline__ Cx cmul(const Cx a, const Cx b) { return Cx{a.r * b.r - a.i * b.i, a.r * b.i + a.i * b.r}; }
struct KGen {
  unsigned pa, pb, pc, pd;
  u16 pe;
  f32x2_t ar, ai;
  Cx C;
  char* st;
  v4u bh[8], bl[8];
  __device__ __forceinline__ void load(const MegaArgs& A, const int d, const int l) {
    pa = *reinterpret_cast<const unsigned*>(A.logAre + d * NST + 2 * l);
    pb = *reinterpret_cast<const unsigned*>(A.Aim + d * NST + 2 * l);
    pc = *reinterpret_cast<const unsigned*>(A.Cre + d * NST + 2 * l);
    pd = *reinterpret_cast<const unsigned*>(A.Cim + d * NST + 2 * l);
    pe = A.logdt[d];
  }
  __device__ __forceinline__ void setup() {
    const float dt = bfr(expf(bf2f(pe)));
    ar = f32x2_t{-bfr(expf(lo16(pa))) * dt, -bfr(expf(hi16(pa))) * dt};
    ai = f32x2_t{lo16(pb) * dt, hi16(pb) * dt};
    C = Cx{f32x2_t{lo16(pc) * dt, hi16(pc) * dt}, f32x2_t{lo16(pd) * dt, hi16(pd) * dt}};
  }
  __device__ __forceinline__ Cx power(const float p) const {
    Cx z;
#pragma unroll
    for (int e = 0; e < 2; ++e) {
      const float x = ai[e] * p;
      const float f = __builtin_fmaf(x, 6.4206382e-9f, __builtin_fmaf(x, 0.15915494f, -__builtin_rintf(x * 0.15915494f)));
      const float m = __expf(ar[e] * p);
      z.r[e] = m * __builtin_amdgcn_cosf(f);
      z.i[e] = m * __builtin_amdgcn_sinf(f);
    }
    return z;
  }
  __device__ __forceinline__ char* rows(char* p, Cx y, const Cx s, const int n) const {
#pragma unroll 1
    for (int i = 0; i < n; i += 2) {
#pragma unroll
      for (int k = 0; k < 2; ++k) {
        const unsigned h0 = pk2(y.r.x, y.r.y), h1 = pk2(y.i.x, y.i.y);
        const f32x2_t a = y.r - f32x2_t{lo16(h0), hi16(h0)}, b = y.i - f32x2_t{lo16(h1), hi16(h1)};
        *reinterpret_cast<v2u*>(p + 1040 * k) = v2u{h0, h1};
        *reinterpret_cast<v2u*>(p + 1040 * k + 512) = v2u{pk2(a.x, a.y), pk2(b.x, b.y)};
        y = cmul(y, s);
      }
      p += 2080;
    }
    return p;
  }
  __device__ __forceinline__ void get(v4u (&h)[8], v4u (&o)[8], const int l) const {
    const char* p = st + (l & 15) * 1040 + (l >> 4) * 16;
    asm volatile("" ::: "memory");
#pragma unroll
    for (int k = 0; k < 8; ++k) {
      h[k] = *reinterpret_cast<const v4u*>(p + 64 * k);
      o[k] = *reinterpret_cast<const v4u*>(p + 512 + 64 * k);
    }
    asm volatile("" ::: "memory");
  }
  __device__ __forceinline__ void columns(const int l) {
    Cx s = power(16.f);
    s.i = -s.i;
    rows(st + l * 8, Cx{f32x2_t{1.f, 1.f}, f32x2_t{0.f, 0.f}}, s, 16);
    get(bh, bl, l);
  }
  __device__ __forceinline__ v4u tile(const int l) const {
    v4u h[8], o[8];
    get(h, o, l);
    f32x4 d = {}, e = {}, f = {};
#pragma unroll
    for (int k = 0; k < 8; ++k) {
      d = MFMA16(BF8(h[k]), BF8(bh[k]), d);
      e = MFMA16(BF8(o[k]), BF8(bh[k]), e);
      f = MFMA16(BF8(h[k]), BF8(bl[k]), f);
    }
    d = (d + (e + f)) * 2.0f;
    v4u r;
#pragma unroll
    for (int i = 0; i < 4; ++i) r[i] = pk2(d[i], d[i] - lo16(pk2(d[i], d[i])));
    return r;
  }
};
#define ACC_INIT 0xc9b26c86c9b26ca0ull
#define CHAIN_ASM_ATT \
  "s_mov_b32 s95, 272\n" \
  "s_mov_b32 s91, 0x4c7c2000\n" \
  "s_mov_b32 s85, 0xc9b26c9b\n" \
  "s_mov_b32 s92, 0xc9326c9b\n" \
  "s_mov_b32 s93, 0xc9326c9b\n" \
  "s_mov_b32 s78, 0x3e0293ee\n" \
  "s_mov_b32 s76, 0x33333333\n" \
  "s_mov_b32 s77, 0x33333333\n" \
  "s_lshl_b32 s98, s73, 10\n" \
  "s_add_u32 s98, s98, s74\n" \
  "s_add_u32 s98, s98, 131072\n" \
  "s_lshl_b32 s97, s73, 9\n" \
  "s_add_u32 s97, s97, s74\n" \
  "s_add_u32 s97, s97, 158720\n" \
  "s_lshl_b32 s99, s73, 10\n" \
  "v_mbcnt_lo_u32_b32 v48, -1, 0\n" \
  "v_mbcnt_hi_u32_b32 v48, -1, v48\n" \
  "s_add_u32 s88, s74, 157760\n" \
  "v_mov_b32 v59, s88\n" \
  "v_lshrrev_b32 v49, 2, v48\n" \
  "v_lshrrev_b32 v50, 4, v48\n" \
  "v_bitop3_b32 v50, v49, v50, 3 bitop3:0x28\n" \
  "v_lshl_add_u32 v8, v50, 3, s97\n" \
  "v_and_b32 v50, 3, v48\n" \
  "v_lshl_add_u32 v8, v50, 5, v8\n" \
  "v_and_b32 v49, 1, v49\n" \
  "v_lshl_add_u32 v8, v49, 8, v8\n" \
  "v_and_b32 v49, 8, v48\n" \
  "v_lshl_add_u32 v8, v49, 4, v8\n" \
  "v_lshrrev_b32 v49, 5, v48\n" \
  "v_add_u32 v10, s99, v1\n" \
  "s_lshr_b32 s88, s99, 2\n" \
  "s_add_u32 s88, s88, s74\n" \
  "s_add_u32 s88, s88, 139264\n" \
  "v_lshl_add_u32 v7, v49, 10, s88\n" \
  "v_and_b32 v49, 31, v48\n" \
  "v_lshl_add_u32 v7, v49, 3, v7\n" \
  "v_mov_b64 v[12:13], 0\n" \
  "v_mov_b64 v[14:15], 0\n" \
  "v_mov_b64 v[16:17], 0\n" \
  "v_mov_b64 v[18:19], 0\n" \
  "v_mov_b64 v[20:21], 0\n" \
  "v_mov_b64 v[22:23], 0\n" \
  "v_mov_b32 v34, 0xc9b26ca0\n" \
  "v_mov_b32 v35, 0xc9b26c86\n" \
  "v_mov_b32 v24, 0x44870000\n" \
  "v_mul_f32 v24, s72, v24\n" \
  "s_nop 0\n" \
  "v_readfirstlane_b32 s86, v24\n" \
  "s_lshr_b32 s86, s86, 23\n" \
  "s_addk_i32 s86, 0xff83\n" \
  "s_ashr_i32 s86, s86, 1\n" \
  "s_max_i32 s86, s86, -20\n" \
  "s_min_i32 s86, s86, 100\n" \
  "s_mov_b32 s82, s86\n" \
  "s_lshr_b32 s83, s72, 23\n" \
  "s_sub_i32 s83, s83, 242\n" \
  "s_lshl_b32 s88, s73, 10\n" \
  "v_add_u32 v24, s88, v1\n" \
  "ds_read_b64 v[32:33], v24 offset:0\n" \
  "ds_read_b64 v[36:37], v24 offset:8\n" \
  "s_waitcnt lgkmcnt(0)\n" \
  "v_mov_b32 v11, 0\n" \
  "v_dot2c_f32_bf16 v11, v32, v32\n" \
  "v_dot2c_f32_bf16 v11, v33, v33\n" \
  "v_dot2c_f32_bf16 v11, v36, v36\n" \
  "v_dot2c_f32_bf16 v11, v37, v37\n" \
  "s_nop 1\n" \
  "s_nop 0\n" \
  "v_cmpx_lt_f32 vcc, 2.0, v11\n" \
  "ds_max_u32 v59, v11\n" \
  "s_mov_b64 exec, -1\n" \
  ".LCA_TOP_%=:\n" \
  "s_waitcnt lgkmcnt(0)\n" \
  "s_barrier\n" \
  "ds_read_b128 v[40:43], v1 offset:0\n" \
  "ds_read_b128 v[44:47], v1 offset:1024\n" \
  "ds_read_b128 v[48:51], v1 offset:2048\n" \
  "ds_read_b128 v[52:55], v1 offset:3072\n" \
  "ds_read_b128 v[56:59], v1 offset:4096\n" \
  "ds_read_b128 v[60:63], v1 offset:5120\n" \
  "s_cmp_lg_u32 s73, 0\n" \
  "s_cbranch_scc1 .LCA_NOZ_%=\n" \
  "s_xor_b32 s88, s60, 0x10000\n" \
  "s_add_u32 s88, s88, s61\n" \
  "buffer_store_dwordx2 v[34:35], v5, s[40:43], s88 offen sc1\n" \
  ".LCA_NOZ_%=:\n" \
  "s_waitcnt lgkmcnt(5)\n" \
  "v_smfmac_f32_16x16x64_bf16 v[20:23], v[40:43], v[192:199], v0\n" \
  "v_smfmac_f32_16x16x64_bf16 v[12:15], v[40:43], v[64:71], v0\n" \
  "v_smfmac_f32_16x16x64_bf16 v[16:19], v[40:43], v[128:135], v0\n" \
  "ds_read_b128 v[40:43], v1 offset:6144\n" \
  "s_waitcnt lgkmcnt(5)\n" \
  "v_smfmac_f32_16x16x64_bf16 v[20:23], v[44:47], v[200:207], v0\n" \
  "v_smfmac_f32_16x16x64_bf16 v[12:15], v[44:47], v[72:79], v0\n" \
  "v_smfmac_f32_16x16x64_bf16 v[16:19], v[44:47], v[136:143], v0\n" \
  "ds_read_b128 v[44:47], v1 offset:7168\n" \
  "s_waitcnt lgkmcnt(5)\n" \
  "v_smfmac_f32_16x16x64_bf16 v[20:23], v[48:51], v[208:215], v0\n" \
  "v_smfmac_f32_16x16x64_bf16 v[12:15], v[48:51], v[80:87], v0\n" \
  "v_smfmac_f32_16x16x64_bf16 v[16:19], v[48:51], v[144:151], v0\n" \
  "ds_read_b128 v[24:27], v1 offset:10240\n" \
  "ds_read_b128 v[28:31], v1 offset:14336\n" \
  "s_waitcnt lgkmcnt(6)\n" \
  "v_smfmac_f32_16x16x64_bf16 v[20:23], v[52:55], v[216:223], v0\n" \
  "v_smfmac_f32_16x16x64_bf16 v[12:15], v[52:55], v[88:95], v0\n" \
  "v_smfmac_f32_16x16x64_bf16 v[16:19], v[52:55], v[152:159], v0\n" \
  "ds_read_b128 v[32:35], v1 offset:11264\n" \
  "ds_read_b128 v[36:39], v1 offset:15360\n" \
  "s_waitcnt lgkmcnt(7)\n" \
  "v_smfmac_f32_16x16x64_bf16 v[20:23], v[56:59], v[224:231], v0\n" \
  "v_smfmac_f32_16x16x64_bf16 v[12:15], v[56:59], v[96:103], v0\n" \
  "v_smfmac_f32_16x16x64_bf16 v[16:19], v[56:59], v[160:167], v0\n" \
  "ds_read_b128 v[56:59], v1 offset:12288\n" \
  "s_waitcnt lgkmcnt(7)\n" \
  "v_smfmac_f32_16x16x64_bf16 v[20:23], v[60:63], v[232:239], v0\n" \
  "v_smfmac_f32_16x16x64_bf16 v[12:15], v[60:63], v[104:111], v0\n" \
  "v_smfmac_f32_16x16x64_bf16 v[16:19], v[60:63], v[168:175], v0\n" \
  "ds_read_b128 v[60:63], v1 offset:16384\n" \
  "s_waitcnt lgkmcnt(7)\n" \
  "v_smfmac_f32_16x16x64_bf16 v[20:23], v[40:43], v[240:247], v0\n" \
  "v_smfmac_f32_16x16x64_bf16 v[12:15], v[40:43], v[112:119], v0\n" \
  "v_smfmac_f32_16x16x64_bf16 v[16:19], v[40:43], v[176:183], v0\n" \
  "s_waitcnt lgkmcnt(6)\n" \
  "v_smfmac_f32_16x16x64_bf16 v[20:23], v[44:47], v[248:255], v0\n" \
  "v_smfmac_f32_16x16x64_bf16 v[12:15], v[44:47], v[120:127], v0\n" \
  "v_smfmac_f32_16x16x64_bf16 v[16:19], v[44:47], v[184:191], v0\n" \
  "s_cmp_eq_u32 s71, 0\n" \
  "v_permlane32_swap_b32 v20, v22\n" \
  "v_permlane32_swap_b32 v21, v23\n" \
  "v_pk_add_f32 v[20:21], v[20:21], v[22:23]\n" \
  "s_mov_b64 vcc, s[76:77]\n" \
  "v_permlane32_swap_b32 v12, v14\n" \
  "v_permlane32_swap_b32 v13, v15\n" \
  "v_cvt_pk_bf16_f32 v20, v20, v21\n" \
  "v_pk_add_f32 v[12:13], v[12:13], v[14:15]\n" \
  "v_permlane32_swap_b32 v16, v18\n" \
  "v_permlane32_swap_b32 v17, v19\n" \
  "ds_write_b32 v8, v20 offset:2048\n" \
  "v_cvt_pk_bf16_f32 v12, v12, v13\n" \
  "v_pk_add_f32 v[16:17], v[16:17], v[18:19]\n" \
  "s_nop 0\n" \
  "v_cvt_pk_bf16_f32 v13, v16, v17\n" \
  "ds_write_b64 v8, v[12:13]\n" \
  "s_setprio 1\n" \
  "s_waitcnt lgkmcnt(0)\n" \
  "s_barrier\n" \
  "s_cbranch_scc1 .LCA_LAST_%=\n" \
  "ds_read_b64_tr_b16 v[40:41], v2 offset:0\n" \
  "ds_read_b64_tr_b16 v[42:43], v3 offset:0\n" \
  "ds_read_b64_tr_b16 v[44:45], v2 offset:1024\n" \
  "ds_read_b64_tr_b16 v[46:47], v3 offset:1024\n" \
  "ds_read_b64_tr_b16 v[48:49], v2 offset:2048\n" \
  "ds_read_b64_tr_b16 v[50:51], v3 offset:2048\n" \
  "ds_read_b64_tr_b16 v[52:53], v2 offset:3072\n" \
  "ds_read_b64_tr_b16 v[54:55], v3 offset:3072\n" \
  "ds_read_b32 v11, v1 offset:26688\n" \
  "s_waitcnt lgkmcnt(7)\n" \
  "v_mfma_f32_16x16x32_bf16 v[12:15], v[24:27], v[40:43], 0\n" \
  "v_mfma_f32_16x16x32_bf16 v[16:19], v[28:31], v[40:43], 0\n" \
  "ds_read_b128 v[24:27], v1 offset:13312\n" \
  "ds_read_b128 v[28:31], v1 offset:17408\n" \
  "v_mfma_f32_16x16x32_bf16 v[20:23], v[40:43], v[40:43], 0\n" \
  "s_waitcnt lgkmcnt(7)\n" \
  "v_mfma_f32_16x16x32_bf16 v[12:15], v[32:35], v[44:47], v[12:15]\n" \
  "v_mfma_f32_16x16x32_bf16 v[16:19], v[36:39], v[44:47], v[16:19]\n" \
  "v_mfma_f32_16x16x32_bf16 v[20:23], v[44:47], v[44:47], v[20:23]\n" \
  "s_waitcnt lgkmcnt(2)\n" \
  "v_mfma_f32_16x16x32_bf16 v[12:15], v[56:59], v[48:51], v[12:15]\n" \
  "v_readfirstlane_b32 s86, v11\n" \
  "v_mfma_f32_16x16x32_bf16 v[16:19], v[60:63], v[48:51], v[16:19]\n" \
  "ds_read_b128 v[32:35], v6 offset:8192\n" \
  "ds_read_b128 v[36:39], v6 offset:9216\n" \
  "v_mfma_f32_16x16x32_bf16 v[20:23], v[48:51], v[48:51], v[20:23]\n" \
  "s_lshr_b32 s86, s86, 23\n" \
  "s_add_i32 s86, s86, s83\n" \
  "s_ashr_i32 s86, s86, 1\n" \
  "ds_read_b128 v[48:51], v10 offset:18432\n" \
  "v_mfma_f32_16x16x32_bf16 v[20:23], v[52:55], v[52:55], v[20:23]\n" \
  "s_max_i32 s86, s86, s82\n" \
  "s_min_i32 s86, s86, 100\n" \
  "s_lshl_b32 s86, s86, 23\n" \
  "s_sub_i32 s84, 0xc8800000, s86\n" \
  "s_waitcnt lgkmcnt(3)\n" \
  "v_mfma_f32_16x16x32_bf16 v[12:15], v[24:27], v[52:55], v[12:15]\n" \
  "v_mfma_f32_16x16x32_bf16 v[16:19], v[28:31], v[52:55], v[16:19]\n" \
  "ds_read_b128 v[52:55], v10 offset:22528\n" \
  "ds_read_b128 v[56:59], v6 offset:0\n" \
  "ds_read_b128 v[60:63], v6 offset:1024\n" \
  "ds_read_b128 v[24:27], v6 offset:2048\n" \
  "ds_read_b128 v[28:31], v6 offset:3072\n" \
  "v_cndmask_b32_dpp v20, v23, v22, vcc quad_perm:[2,3,0,1] row_mask:0xf bank_mask:0xf\n" \
  "v_mov_b32 v41, 0\n" \
  "v_mov_b32 v42, 0\n" \
  "v_cndmask_b32_dpp v12, v16, v12, vcc quad_perm:[2,3,0,1] row_mask:0xf bank_mask:0xf\n" \
  "v_cndmask_b32_dpp v13, v17, v13, vcc quad_perm:[2,3,0,1] row_mask:0xf bank_mask:0xf\n" \
  "v_cndmask_b32_dpp v14, v18, v14, vcc quad_perm:[2,3,0,1] row_mask:0xf bank_mask:0xf\n" \
  "v_cndmask_b32_dpp v15, v19, v15, vcc quad_perm:[2,3,0,1] row_mask:0xf bank_mask:0xf\n" \
  "v_maximum3_f32 v16, v20, v12, v13\n" \
  "v_maximum3_f32 v16, v16, v14, v15\n" \
  "s_nop 1\n" \
  "v_max_f32_dpp v16, v16, v16 quad_perm:[2,3,0,1] row_mask:0xf bank_mask:0xf\n" \
  "v_mov_b32 v17, v16\n" \
  "s_nop 1\n" \
  "v_permlane16_swap_b32 v16, v17\n" \
  "v_max_f32 v16, v16, v17\n" \
  "v_mov_b32 v17, v16\n" \
  "s_nop 1\n" \
  "v_permlane32_swap_b32 v16, v17\n" \
  "v_max_f32 v16, v16, v17\n" \
  "v_mul_f32 v16, s78, v16\n" \
  "v_pk_fma_f32 v[12:13], v[12:13], s[78:79], v[16:17] op_sel_hi:[1,0,0] neg_lo:[0,0,1] neg_hi:[0,0,1]\n" \
  "v_pk_fma_f32 v[14:15], v[14:15], s[78:79], v[16:17] op_sel_hi:[1,0,0] neg_lo:[0,0,1] neg_hi:[0,0,1]\n" \
  "v_fma_f32 v20, v20, s78, -v16\n" \
  "v_exp_f32 v12, v12\n" \
  "v_exp_f32 v13, v13\n" \
  "v_exp_f32 v14, v14\n" \
  "v_exp_f32 v15, v15\n" \
  "v_exp_f32 v20, v20\n" \
  "s_waitcnt lgkmcnt(4)\n" \
  "v_cvt_pk_bf16_f32 v44, v12, v13\n" \
  "v_cvt_pk_bf16_f32 v45, v14, v15\n" \
  "v_pk_add_f32 v[8:9], v[12:13], v[14:15]\n" \
  "v_mov_b32_dpp v46, v44 quad_perm:[2,3,0,1] row_mask:0xf bank_mask:0xf\n" \
  "v_mov_b32_dpp v47, v45 quad_perm:[2,3,0,1] row_mask:0xf bank_mask:0xf\n" \
  "v_add_f32 v10, v8, v20\n" \
  "v_add_f32 v10, v10, v9\n" \
  "v_mfma_f32_16x16x32_bf16 v[12:15], v[48:51], v[44:47], 0\n" \
  "v_mov_b32_dpp v21, v20 quad_perm:[2,3,0,1] row_mask:0xf bank_mask:0xf\n" \
  "v_add_f32_dpp v10, v10, v10 quad_perm:[2,3,0,1] row_mask:0xf bank_mask:0xf\n" \
  "v_mov_b32 v11, v10\n" \
  "v_mfma_f32_16x16x32_bf16 v[16:19], v[52:55], v[44:47], 0\n" \
  "s_setprio 3\n" \
  "s_barrier\n" \
  "ds_read_b64 v[22:23], v7 offset:0\n" \
  "s_nop 0\n" \
  "v_permlane16_swap_b32 v10, v11\n" \
  "v_add_f32 v10, v10, v11\n" \
  "v_mov_b32 v11, v10\n" \
  "v_mov_b64 v[8:9], s[92:93]\n" \
  "s_nop 0\n" \
  "v_permlane32_swap_b32 v10, v11\n" \
  "v_add_f32 v10, v10, v11\n" \
  "v_rcp_f32 v10, v10\n" \
  "s_nop 0\n" \
  "v_mul_f32 v10, s84, v10\n" \
  "v_pk_mul_f32 v[20:21], v[20:21], v[10:11] op_sel_hi:[1,0]\n" \
  "v_pk_mul_f32 v[12:13], v[12:13], v[10:11] op_sel_hi:[1,0]\n" \
  "v_pk_mul_f32 v[14:15], v[14:15], v[10:11] op_sel_hi:[1,0]\n" \
  "v_cvt_pk_bf16_f32 v40, v20, v21\n" \
  "v_pk_mul_f32 v[16:17], v[16:17], v[10:11] op_sel_hi:[1,0]\n" \
  "v_cvt_pk_bf16_f32 v43, v20, v21\n" \
  "v_pk_mul_f32 v[18:19], v[18:19], v[10:11] op_sel_hi:[1,0]\n" \
  "s_waitcnt lgkmcnt(0)\n" \
  "v_mfma_f32_16x16x16_bf16 v[12:15], v[22:23], v[40:41], v[12:15]\n" \
  "v_mfma_f32_16x16x16_bf16 v[16:19], v[22:23], v[42:43], v[16:19]\n" \
  "v_mov_b64 v[10:11], s[92:93]\n" \
  "ds_read_b128 v[48:51], v6 offset:10240\n" \
  "ds_read_b128 v[52:55], v6 offset:11264\n" \
  "v_mov_b64 v[20:21], s[92:93]\n" \
  "v_mov_b64 v[22:23], s[92:93]\n" \
  "s_nop 1\n" \
  "v_cvt_pk_bf16_f32 v40, v12, v13\n" \
  "v_cvt_pk_bf16_f32 v41, v14, v15\n" \
  "v_cvt_pk_bf16_f32 v42, v16, v17\n" \
  "ds_write_b64 v4, v[40:41]\n" \
  "v_cvt_pk_bf16_f32 v43, v18, v19\n" \
  "ds_write_b64 v4, v[42:43] offset:1024\n" \
  "v_mov_b64 v[12:13], s[92:93]\n" \
  "v_mov_b64 v[14:15], s[92:93]\n" \
  "v_mov_b64 v[16:17], s[92:93]\n" \
  "v_mov_b64 v[18:19], s[92:93]\n" \
  "s_setprio 0\n" \
  "s_waitcnt lgkmcnt(0)\n" \
  "s_barrier\n" \
  "ds_read_b128 v[40:43], v1 offset:8192\n" \
  "ds_read_b128 v[44:47], v1 offset:9216\n" \
  "s_waitcnt lgkmcnt(1)\n" \
  "v_smfmac_f32_16x16x64_bf16 v[12:15], v[40:43], v[56:63], v0\n" \
  "ds_read_b128 v[56:59], v6 offset:4096\n" \
  "ds_read_b128 v[60:63], v6 offset:5120\n" \
  "v_smfmac_f32_16x16x64_bf16 v[16:19], v[40:43], v[24:31], v0\n" \
  "ds_read_b128 v[24:27], v6 offset:6144\n" \
  "ds_read_b128 v[28:31], v6 offset:7168\n" \
  "s_waitcnt lgkmcnt(4)\n" \
  "v_smfmac_f32_16x16x64_bf16 v[12:15], v[44:47], v[32:39], v0\n" \
  "ds_read_b128 v[32:35], v6 offset:12288\n" \
  "ds_read_b128 v[36:39], v6 offset:13312\n" \
  "v_smfmac_f32_16x16x64_bf16 v[16:19], v[44:47], v[48:55], v0\n" \
  "ds_read_b128 v[48:51], v6 offset:14336\n" \
  "ds_read_b128 v[52:55], v6 offset:15360\n" \
  "s_nop 5\n" \
  "v_permlane32_swap_b32 v12, v17\n" \
  "v_permlane32_swap_b32 v13, v16\n" \
  "v_permlane32_swap_b32 v14, v19\n" \
  "v_permlane32_swap_b32 v15, v18\n" \
  "v_pk_add_f32 v[12:13], v[12:13], v[16:17] op_sel:[0,1] op_sel_hi:[1,0]\n" \
  "v_pk_add_f32 v[14:15], v[14:15], v[18:19] op_sel:[0,1] op_sel_hi:[1,0]\n" \
  "buffer_atomic_add_x2 v[12:13], v5, s[40:43], s56 offen\n" \
  "buffer_atomic_add_x2 v[14:15], v5, s[40:43], s57 offen\n" \
  "s_waitcnt lgkmcnt(6)\n" \
  "v_smfmac_f32_16x16x64_bf16 v[20:23], v[40:43], v[56:63], v0\n" \
  "s_waitcnt lgkmcnt(4)\n" \
  "v_smfmac_f32_16x16x64_bf16 v[8:11], v[40:43], v[24:31], v0\n" \
  "s_waitcnt lgkmcnt(2)\n" \
  "v_smfmac_f32_16x16x64_bf16 v[20:23], v[44:47], v[32:39], v0\n" \
  "s_waitcnt lgkmcnt(0)\n" \
  "v_smfmac_f32_16x16x64_bf16 v[8:11], v[44:47], v[48:55], v0\n" \
  "s_nop 7\n" \
  "v_permlane32_swap_b32 v20, v9\n" \
  "v_permlane32_swap_b32 v21, v8\n" \
  "v_permlane32_swap_b32 v22, v11\n" \
  "v_permlane32_swap_b32 v23, v10\n" \
  "v_pk_add_f32 v[20:21], v[20:21], v[8:9] op_sel:[0,1] op_sel_hi:[1,0]\n" \
  "v_pk_add_f32 v[22:23], v[22:23], v[10:11] op_sel:[0,1] op_sel_hi:[1,0]\n" \
  "buffer_atomic_add_x2 v[20:21], v5, s[40:43], s58 offen\n" \
  "buffer_atomic_add_x2 v[22:23], v5, s[40:43], s59 offen\n" \
  "s_memrealtime s[38:39]\n" \
  "v_mbcnt_lo_u32_b32 v48, -1, 0\n" \
  "v_mbcnt_hi_u32_b32 v48, -1, v48\n" \
  "s_add_u32 s88, s74, 157760\n" \
  "v_mov_b32 v59, s88\n" \
  "v_lshl_add_u32 v52, v48, 2, v59\n" \
  "ds_read_b32 v52, v52 offset:192\n" \
  "v_lshrrev_b32 v49, 3, v48\n" \
  "v_and_b32 v50, 7, v48\n" \
  "v_lshlrev_b32 v50, 4, v50\n" \
  "v_lshl_or_b32 v44, v49, 8, v50\n" \
  "s_mov_b32 s87, 0x40000\n" \
  "v_lshrrev_b32 v49, 2, v48\n" \
  "v_lshrrev_b32 v50, 4, v48\n" \
  "v_bitop3_b32 v50, v49, v50, 3 bitop3:0x28\n" \
  "v_lshl_add_u32 v8, v50, 3, s97\n" \
  "v_and_b32 v50, 3, v48\n" \
  "v_lshl_add_u32 v8, v50, 5, v8\n" \
  "v_and_b32 v49, 1, v49\n" \
  "v_lshl_add_u32 v8, v49, 8, v8\n" \
  "v_and_b32 v49, 8, v48\n" \
  "v_lshl_add_u32 v8, v49, 4, v8\n" \
  "v_lshrrev_b32 v49, 5, v48\n" \
  "v_add_u32 v10, s99, v1\n" \
  "s_lshr_b32 s88, s99, 2\n" \
  "s_add_u32 s88, s88, s74\n" \
  "s_add_u32 s88, s88, 139264\n" \
  "v_lshl_add_u32 v7, v49, 10, s88\n" \
  "v_and_b32 v49, 31, v48\n" \
  "v_lshl_add_u32 v7, v49, 3, v7\n" \
  "v_mov_b64 v[12:13], 0\n" \
  "v_mov_b64 v[14:15], 0\n" \
  "v_mov_b64 v[16:17], 0\n" \
  "v_mov_b64 v[18:19], 0\n" \
  "v_mov_b64 v[20:21], 0\n" \
  "v_mov_b64 v[22:23], 0\n" \
  "v_mov_b32 v34, 0xc9b26ca0\n" \
  "v_mov_b32 v35, 0xc9b26c86\n" \
  "v_and_b32 v50, 3, v48\n" \
  "v_lshl_add_u32 v40, v50, 2, s98\n" \
  "v_lshrrev_b32 v50, 4, v48\n" \
  "v_mad_u32_u24 v40, v50, s95, v40\n" \
  "v_bfe_u32 v49, v48, 3, 1\n" \
  "v_lshl_add_u32 v40, v49, 6, v40\n" \
  "v_bfe_u32 v49, v48, 2, 1\n" \
  "v_lshl_add_u32 v40, v49, 7, v40\n" \
  "v_xor_b32 v41, 16, v40\n" \
  "v_xor_b32 v42, 32, v40\n" \
  "v_xor_b32 v43, 48, v40\n" \
  "s_add_i32 s80, s86, 0x35000000\n" \
  "v_mov_b32 v11, 0\n" \
  "s_mov_b32 s89, s60\n" \
  "s_bitcmp1_b32 s60, 7\n" \
  "s_cselect_b32 s88, 0x10000, 0\n" \
  "s_bitset1_b32 s88, 7\n" \
  "s_xor_b32 s56, s56, s88\n" \
  "s_xor_b32 s57, s57, s88\n" \
  "s_xor_b32 s58, s58, s88\n" \
  "s_xor_b32 s59, s59, s88\n" \
  "s_xor_b32 s60, s60, s88\n" \
  "s_sub_u32 s71, s71, 1\n" \
  "s_waitcnt lgkmcnt(0)\n" \
  "s_sub_u32 s39, s38, 150\n" \
  "v_subrev_u32 v52, s39, v52\n" \
  "v_cmp_lt_u32 vcc, 0x1000, v52\n" \
  "s_cmp_eq_u64 vcc, 0\n" \
  "s_cbranch_scc1 .LCA_OKM_%=\n" \
  ".LCA_AGN_%=:\n" \
  "buffer_load_dword v52, v5, s[0:3], s66 offen sc1\n" \
  "s_sub_u32 s87, s87, 1\n" \
  "s_cbranch_scc1 .LCA_END_%=\n" \
  "s_waitcnt vmcnt(0)\n" \
  "v_cmp_eq_u32 vcc, 0, v52\n" \
  "v_subrev_u32 v52, s39, v52\n" \
  "s_cmp_eq_u64 vcc, 0\n" \
  "s_cbranch_scc1 .LCA_OKM_%=\n" \
  "s_sleep 4\n" \
  "s_branch .LCA_AGN_%=\n" \
  ".LCA_OKM_%=:\n" \
  "s_nop 1\n" \
  "v_max_i32_dpp v52, v52, v52 row_ror:1 row_mask:0xf bank_mask:0xf\n" \
  "s_nop 1\n" \
  "v_max_i32_dpp v52, v52, v52 row_ror:2 row_mask:0xf bank_mask:0xf\n" \
  "s_nop 1\n" \
  "v_max_i32_dpp v52, v52, v52 row_ror:4 row_mask:0xf bank_mask:0xf\n" \
  "s_nop 1\n" \
  "v_max_i32_dpp v52, v52, v52 row_ror:8 row_mask:0xf bank_mask:0xf\n" \
  "s_movk_i32 vcc_lo, 150\n" \
  "v_readlane_b32 s88, v52, 0\n" \
  "s_max_i32 vcc_lo, vcc_lo, s88\n" \
  "v_readlane_b32 s88, v52, 16\n" \
  "s_max_i32 vcc_lo, vcc_lo, s88\n" \
  "v_readlane_b32 s88, v52, 32\n" \
  "s_max_i32 vcc_lo, vcc_lo, s88\n" \
  "v_readlane_b32 s88, v52, 48\n" \
  "s_max_i32 vcc_lo, vcc_lo, s88\n" \
  "s_add_u32 s66, s66, 1024\n" \
  "s_add_u32 vcc_lo, vcc_lo, s39\n" \
  "s_add_u32 vcc_lo, vcc_lo, 23\n" \
  ".LCA_TW_%=:\n" \
  "s_memrealtime s[38:39]\n" \
  "s_waitcnt lgkmcnt(0)\n" \
  "s_sub_u32 s38, s38, vcc_lo\n" \
  "s_cmp_lt_i32 s38, 0\n" \
  "s_cbranch_scc1 .LCA_TW_%=\n" \
  "buffer_load_dwordx4 v[24:27], v44, s[40:43], s89 offen sc1\n" \
  "buffer_load_dwordx4 v[28:31], v44, s[40:43], s89 offen offset:2048 sc1\n" \
  ".LCA_SEE_%=:\n" \
  "s_waitcnt vmcnt(1)\n" \
  "v_cvt_f32_i32 v24, v24\n" \
  "v_cvt_f32_i32 v25, v25\n" \
  "v_cvt_f32_i32 v26, v26\n" \
  "v_cvt_f32_i32 v27, v27\n" \
  "v_max_f32 v45, |v24|, |v26|\n" \
  "v_pk_mul_f32 v[24:25], v[24:25], s[80:81] op_sel_hi:[1,0]\n" \
  "v_pk_mul_f32 v[26:27], v[26:27], s[80:81] op_sel_hi:[1,0]\n" \
  "s_nop 0\n" \
  "v_cvt_pk_bf16_f32 v32, v24, v26\n" \
  "v_cvt_pk_bf16_f32 v33, v25, v27\n" \
  "ds_write_b32 v40, v32\n" \
  "ds_write_b32 v41, v33\n" \
  "s_waitcnt vmcnt(0)\n" \
  "v_cvt_f32_i32 v28, v28\n" \
  "v_cvt_f32_i32 v30, v30\n" \
  "v_cvt_f32_i32 v29, v29\n" \
  "v_max3_f32 v45, v45, |v28|, |v30|\n" \
  "v_cvt_f32_i32 v31, v31\n" \
  "v_cmp_lt_f32 vcc, s91, v45\n" \
  "v_pk_mul_f32 v[28:29], v[28:29], s[80:81] op_sel_hi:[1,0]\n" \
  "v_pk_mul_f32 v[30:31], v[30:31], s[80:81] op_sel_hi:[1,0]\n" \
  "s_cmp_eq_u64 vcc, 0\n" \
  "s_cbranch_scc0 .LCA_POLL_%=\n" \
  "v_cvt_pk_bf16_f32 v36, v28, v30\n" \
  "v_cvt_pk_bf16_f32 v37, v29, v31\n" \
  "ds_write_b32 v42, v36\n" \
  "ds_write_b32 v43, v37\n" \
  "v_dot2c_f32_bf16 v11, v32, v32\n" \
  "v_dot2c_f32_bf16 v11, v33, v33\n" \
  "v_dot2c_f32_bf16 v11, v36, v36\n" \
  "v_dot2c_f32_bf16 v11, v37, v37\n" \
  "s_nop 1\n" \
  "s_nop 0\n" \
  "v_cmpx_lt_f32 vcc, 2.0, v11\n" \
  "ds_max_u32 v59, v11\n" \
  "s_mov_b64 exec, -1\n" \
  "s_branch .LCA_TOP_%=\n" \
  ".LCA_POLL_%=:\n" \
  "buffer_load_dwordx4 v[24:27], v44, s[40:43], s89 offen sc1\n" \
  "buffer_load_dwordx4 v[28:31], v44, s[40:43], s89 offen offset:2048 sc1\n" \
  "s_sub_u32 s87, s87, 1\n" \
  "s_cbranch_scc1 .LCA_END_%=\n" \
  "s_branch .LCA_SEE_%=\n" \
  ".LCA_LAST_%=:\n" \
  ".LCA_END_%=:\n" \
  "s_endpgm\n"
#define CHAIN_ASM_VO \
  "s_mov_b32 s95, 272\n" \
  "s_mov_b32 s91, 0x4c7c2000\n" \
  "s_mov_b32 s85, 0xc9b26c9b\n" \
  "s_mov_b32 s92, 0xc9326c9b\n" \
  "s_mov_b32 s93, 0xc9326c9b\n" \
  "s_mov_b32 s78, 0x3e0293ee\n" \
  "s_mov_b32 s76, 0x33333333\n" \
  "s_mov_b32 s77, 0x33333333\n" \
  "s_lshl_b32 s98, s73, 10\n" \
  "s_add_u32 s98, s98, s74\n" \
  "s_add_u32 s98, s98, 131072\n" \
  "s_lshl_b32 s97, s73, 9\n" \
  "s_add_u32 s97, s97, s74\n" \
  "s_add_u32 s97, s97, 158724\n" \
  "s_lshl_b32 s99, s73, 11\n" \
  "s_add_u32 s99, s99, s74\n" \
  "s_add_u32 s99, s99, 133120\n" \
  "s_lshl_b32 s94, s73, 4\n" \
  "s_add_u32 s94, s94, s74\n" \
  "s_add_u32 s94, s94, 157760\n" \
  "v_mbcnt_lo_u32_b32 v48, -1, 0\n" \
  "v_mbcnt_hi_u32_b32 v48, -1, v48\n" \
  "s_add_u32 s88, s74, 157760\n" \
  "v_mov_b32 v59, s88\n" \
  "s_sub_u32 s88, s73, 4\n" \
  "v_lshrrev_b32 v49, 2, v48\n" \
  "v_lshrrev_b32 v50, 4, v48\n" \
  "v_bitop3_b32 v50, v49, v50, 3 bitop3:0x28\n" \
  "v_lshl_add_u32 v4, v50, 3, s97\n" \
  "v_and_b32 v50, 3, v48\n" \
  "v_lshl_add_u32 v4, v50, 5, v4\n" \
  "v_and_b32 v49, 1, v49\n" \
  "v_lshl_add_u32 v4, v49, 8, v4\n" \
  "v_and_b32 v49, 8, v48\n" \
  "v_lshl_add_u32 v4, v49, 4, v4\n" \
  "v_and_b32 v51, 15, v48\n" \
  "v_bfe_u32 v49, v48, 4, 1\n" \
  "v_lshrrev_b32 v50, 5, v48\n" \
  "s_lshl_b32 s89, s88, 10\n" \
  "s_add_u32 s89, s89, s74\n" \
  "s_add_u32 s89, s89, 158720\n" \
  "v_lshrrev_b32 v9, 3, v51\n" \
  "v_mul_u32_u24 v6, 41, v9\n" \
  "v_xor_b32 v6, v6, v51\n" \
  "v_lshl_add_u32 v6, v6, 3, s89\n" \
  "v_lshl_add_u32 v6, v49, 6, v6\n" \
  "v_lshl_add_u32 v6, v50, 9, v6\n" \
  "v_xor_b32 v9, 0x90, v6\n" \
  "v_lshlrev_b32 v8, 4, v49\n" \
  "v_lshl_add_u32 v8, v51, 5, v8\n" \
  "v_lshl_add_u32 v8, v50, 3, v8\n" \
  "s_lshl_b32 s89, s88, 8\n" \
  "s_add_u32 s89, s89, s74\n" \
  "s_add_u32 s89, s89, 139264\n" \
  "v_and_b32 v49, 31, v48\n" \
  "v_lshl_add_u32 v5, v49, 3, s89\n" \
  "v_lshl_add_u32 v5, v50, 10, v5\n" \
  "v_lshrrev_b32 v49, 2, v51\n" \
  "v_and_b32 v50, 5, v51\n" \
  "v_and_or_b32 v49, v49, 2, v50\n" \
  "v_and_b32 v50, 48, v48\n" \
  "v_lshlrev_b32 v7, 3, v50\n" \
  "v_lshl_add_u32 v7, v49, 4, v7\n" \
  "v_bitop3_b32 v49, v51, 2, 2 bitop3:0x0c\n" \
  "v_lshl_add_u32 v7, v49, 29, v7\n" \
  "v_mov_b64 v[12:13], 0\n" \
  "v_mov_b64 v[14:15], 0\n" \
  "v_mov_b64 v[16:17], 0\n" \
  "v_mov_b64 v[18:19], 0\n" \
  "v_mov_b64 v[20:21], 0\n" \
  "v_mov_b64 v[22:23], 0\n" \
  "v_lshlrev_b32 v10, 4, v48\n" \
  "s_add_u32 s78, s74, 157952\n" \
  "v_lshl_add_u32 v52, v48, 2, v59\n" \
  "ds_write_b32 v52, v12 offset:192\n" \
  "s_movk_i32 s79, 105\n" \
  "v_mov_b32 v24, 0x44870000\n" \
  "v_mul_f32 v24, s72, v24\n" \
  "s_nop 0\n" \
  "v_readfirstlane_b32 s86, v24\n" \
  "s_lshr_b32 s86, s86, 23\n" \
  "s_addk_i32 s86, 0xff83\n" \
  "s_ashr_i32 s86, s86, 1\n" \
  "s_max_i32 s86, s86, -20\n" \
  "s_min_i32 s86, s86, 100\n" \
  "s_mov_b32 s82, s86\n" \
  "s_lshr_b32 s83, s72, 23\n" \
  "s_sub_i32 s83, s83, 242\n" \
  "s_lshl_b32 s88, s73, 10\n" \
  "v_add_u32 v24, s88, v1\n" \
  "ds_read_b64 v[32:33], v24 offset:0\n" \
  "ds_read_b64 v[36:37], v24 offset:8\n" \
  "s_waitcnt lgkmcnt(0)\n" \
  "v_mov_b32 v11, 0\n" \
  "v_dot2c_f32_bf16 v11, v32, v32\n" \
  "v_dot2c_f32_bf16 v11, v33, v33\n" \
  "v_dot2c_f32_bf16 v11, v36, v36\n" \
  "v_dot2c_f32_bf16 v11, v37, v37\n" \
  "s_nop 1\n" \
  "s_nop 0\n" \
  "v_cmpx_lt_f32 vcc, 2.0, v11\n" \
  "ds_max_u32 v59, v11\n" \
  "s_mov_b64 exec, -1\n" \
  "s_cmp_lg_u32 s61, 0\n" \
  "s_movk_i32 s76, -8000\n" \
  "s_cmovk_i32 s76, 9152\n" \
  "s_movk_i32 s77, 90\n" \
  "s_cmovk_i32 s77, 90\n" \
  "s_cselect_b32 s72, -1, 55\n" \
  ".LCV_TOP_%=:\n" \
  "s_waitcnt lgkmcnt(0)\n" \
  "s_barrier\n" \
  "s_setprio 3\n" \
  "ds_read_b128 v[24:27], v1 offset:0\n" \
  "ds_read_b128 v[28:31], v1 offset:1024\n" \
  "ds_read_b128 v[32:35], v1 offset:2048\n" \
  "ds_read_b128 v[36:39], v1 offset:3072\n" \
  "ds_read_b128 v[40:43], v1 offset:4096\n" \
  "ds_read_b128 v[44:47], v1 offset:5120\n" \
  "ds_read_b128 v[48:51], v1 offset:6144\n" \
  "ds_read_b128 v[52:55], v1 offset:7168\n" \
  "s_memrealtime s[38:39]\n" \
  "s_waitcnt lgkmcnt(7)\n" \
  "v_smfmac_f32_16x16x64_bf16 v[12:15], v[24:27], v[64:71], v0\n" \
  "s_waitcnt lgkmcnt(6)\n" \
  "v_smfmac_f32_16x16x64_bf16 v[12:15], v[28:31], v[72:79], v0\n" \
  "s_waitcnt lgkmcnt(5)\n" \
  "v_smfmac_f32_16x16x64_bf16 v[12:15], v[32:35], v[80:87], v0\n" \
  "s_waitcnt lgkmcnt(4)\n" \
  "v_smfmac_f32_16x16x64_bf16 v[12:15], v[36:39], v[88:95], v0\n" \
  "s_waitcnt lgkmcnt(3)\n" \
  "v_smfmac_f32_16x16x64_bf16 v[12:15], v[40:43], v[96:103], v0\n" \
  "s_waitcnt lgkmcnt(2)\n" \
  "v_smfmac_f32_16x16x64_bf16 v[12:15], v[44:47], v[104:111], v0\n" \
  "s_waitcnt lgkmcnt(1)\n" \
  "v_smfmac_f32_16x16x64_bf16 v[12:15], v[48:51], v[112:119], v0\n" \
  "s_waitcnt lgkmcnt(0)\n" \
  "v_smfmac_f32_16x16x64_bf16 v[12:15], v[52:55], v[120:127], v0\n" \
  "ds_read_b32 v11, v1 offset:26688\n" \
  "s_mov_b32 s61, s38\n" \
  "s_add_u32 s38, s38, s79\n" \
  "v_or_b32 v57, 1, s38\n" \
  "v_add_u32 v56, s67, v10\n" \
  "s_add_u32 s39, s67, s76\n" \
  "s_add_u32 s38, s38, s77\n" \
  "s_bitset1_b32 s38, 0\n" \
  "v_writelane_b32 v56, s39, 1\n" \
  "v_writelane_b32 v57, s38, 1\n" \
  "s_sub_u32 s39, s71, s72\n" \
  "s_cmp_lt_u32 s39, 9\n" \
  "s_cselect_b64 exec, 19, 3\n" \
  "buffer_store_dword v57, v56, s[0:3], 0 offen sc1\n" \
  "s_mov_b64 exec, -1\n" \
  "v_permlane32_swap_b32 v12, v14\n" \
  "v_permlane32_swap_b32 v13, v15\n" \
  "v_pk_add_f32 v[12:13], v[12:13], v[14:15]\n" \
  "s_nop 0\n" \
  "v_cvt_pk_bf16_f32 v12, v12, v13\n" \
  "ds_write_b32 v4, v12\n" \
  "s_setprio 2\n" \
  "v_mov_b64 v[12:13], s[92:93]\n" \
  "v_mov_b64 v[14:15], s[92:93]\n" \
  "s_waitcnt lgkmcnt(1)\n" \
  "v_readfirstlane_b32 s86, v11\n" \
  "s_lshr_b32 s86, s86, 23\n" \
  "s_add_i32 s86, s86, s83\n" \
  "s_ashr_i32 s86, s86, 1\n" \
  "s_max_i32 s86, s86, s82\n" \
  "s_min_i32 s86, s86, 100\n" \
  "s_lshl_b32 s86, s86, 23\n" \
  "s_sub_i32 s84, 0xc8800000, s86\n" \
  "s_cmp_eq_u32 s73, 5\n" \
  "v_smfmac_f32_16x16x64_bf16 v[16:19], v[24:27], v[128:135], v0\n" \
  "v_smfmac_f32_16x16x64_bf16 v[16:19], v[28:31], v[136:143], v0\n" \
  "v_smfmac_f32_16x16x64_bf16 v[16:19], v[32:35], v[144:151], v0\n" \
  "v_smfmac_f32_16x16x64_bf16 v[16:19], v[36:39], v[152:159], v0\n" \
  "v_smfmac_f32_16x16x64_bf16 v[16:19], v[40:43], v[160:167], v0\n" \
  "v_smfmac_f32_16x16x64_bf16 v[16:19], v[44:47], v[168:175], v0\n" \
  "s_waitcnt lgkmcnt(0)\n" \
  "s_barrier\n" \
  "ds_read_b64_tr_b16 v[60:61], v6 offset:0\n" \
  "ds_read_b64_tr_b16 v[62:63], v9 offset:0\n" \
  "v_smfmac_f32_16x16x64_bf16 v[16:19], v[48:51], v[176:183], v0\n" \
  "v_smfmac_f32_16x16x64_bf16 v[16:19], v[52:55], v[184:191], v0\n" \
  "v_smfmac_f32_16x16x64_bf16 v[20:23], v[24:27], v[192:199], v0\n" \
  "v_smfmac_f32_16x16x64_bf16 v[20:23], v[28:31], v[200:207], v0\n" \
  "v_smfmac_f32_16x16x64_bf16 v[20:23], v[32:35], v[208:215], v0\n" \
  "v_smfmac_f32_16x16x64_bf16 v[20:23], v[36:39], v[216:223], v0\n" \
  "v_smfmac_f32_16x16x64_bf16 v[20:23], v[40:43], v[224:231], v0\n" \
  "v_smfmac_f32_16x16x64_bf16 v[20:23], v[44:47], v[232:239], v0\n" \
  "v_smfmac_f32_16x16x64_bf16 v[20:23], v[48:51], v[240:247], v0\n" \
  "v_smfmac_f32_16x16x64_bf16 v[20:23], v[52:55], v[248:255], v0\n" \
  "s_cmp_eq_u32 s71, 0\n" \
  "v_permlane32_swap_b32 v16, v18\n" \
  "v_permlane32_swap_b32 v17, v19\n" \
  "v_add_f32 v16, v16, v18\n" \
  "v_add_f32 v17, v17, v19\n" \
  "v_cvt_pk_bf16_f32 v16, v16, v17\n" \
  "s_waitcnt lgkmcnt(0)\n" \
  "v_max_u32 v60, 1, v60\n" \
  "v_max_u32 v61, 1, v61\n" \
  "v_max_u32 v62, 1, v62\n" \
  "v_max_u32 v63, 1, v63\n" \
  "buffer_store_dwordx4 v[60:63], v7, s[48:51], s69 offen sc1\n" \
  "v_permlane32_swap_b32 v20, v22\n" \
  "v_permlane32_swap_b32 v21, v23\n" \
  "v_pk_add_f32 v[20:21], v[20:21], v[22:23]\n" \
  "v_max_u32 v18, 1, v16\n" \
  "v_cvt_pk_bf16_f32 v17, v20, v21\n" \
  "ds_write_b64 v5, v[16:17]\n" \
  "v_max_u32 v19, 1, v17\n" \
  "s_nop 1\n" \
  "v_permlane32_swap_b32 v18, v19\n" \
  "buffer_store_dwordx2 v[18:19], v8, s[52:55], s70 offen sc1\n" \
  "ds_read_b128 v[24:27], v3 offset:0\n" \
  "ds_read_b128 v[28:31], v3 offset:1024\n" \
  "ds_read_b128 v[32:35], v3 offset:2048\n" \
  "ds_read_b128 v[36:39], v3 offset:3072\n" \
  "ds_read_b128 v[40:43], v3 offset:8192\n" \
  "ds_read_b128 v[44:47], v3 offset:9216\n" \
  "ds_read_b128 v[48:51], v3 offset:10240\n" \
  "ds_read_b128 v[52:55], v3 offset:11264\n" \
  "s_cbranch_scc1 .LCV_LAST_%=\n" \
  "s_cmp_eq_u32 s73, 5\n" \
  "s_cbranch_scc0 .LCV_NMK_%=\n" \
  "s_mov_b32 m0, s78\n" \
  "s_nop 0\n" \
  "buffer_load_dword v2, s[0:3], s66 offen sc1 lds\n" \
  ".LCV_NMK_%=:\n" \
  "s_setprio 0\n" \
  "s_waitcnt lgkmcnt(8)\n" \
  "s_barrier\n" \
  "s_add_u32 s67, s67, 1024\n" \
  "v_mov_b32 v11, 0\n" \
  "ds_write_b32 v59, v11\n" \
  "ds_read_b128 v[56:59], v3 offset:4096\n" \
  "ds_read_b128 v[60:63], v3 offset:5120\n" \
  "v_mov_b64 v[20:21], s[92:93]\n" \
  "v_mov_b64 v[22:23], s[92:93]\n" \
  "v_mov_b64 v[16:17], s[92:93]\n" \
  "v_mov_b64 v[18:19], s[92:93]\n" \
  "s_mov_b32 m0, s94\n" \
  "s_mov_b64 exec, 15\n" \
  "buffer_load_dword v2, s[44:47], s68 offen sc1 lds\n" \
  "s_mov_b64 exec, -1\n" \
  "s_setprio 2\n" \
  "s_waitcnt lgkmcnt(0)\n" \
  "s_barrier\n" \
  "ds_read_b128 v[4:7], v1 offset:8192\n" \
  "ds_read_b128 v[8:11], v1 offset:9216\n" \
  "s_waitcnt lgkmcnt(1)\n" \
  "v_smfmac_f32_16x16x64_bf16 v[12:15], v[4:7], v[24:31], v0\n" \
  "ds_read_b128 v[24:27], v3 offset:6144\n" \
  "ds_read_b128 v[28:31], v3 offset:7168\n" \
  "v_smfmac_f32_16x16x64_bf16 v[16:19], v[4:7], v[32:39], v0\n" \
  "ds_read_b128 v[32:35], v3 offset:12288\n" \
  "ds_read_b128 v[36:39], v3 offset:13312\n" \
  "s_waitcnt lgkmcnt(4)\n" \
  "v_smfmac_f32_16x16x64_bf16 v[12:15], v[8:11], v[40:47], v0\n" \
  "v_smfmac_f32_16x16x64_bf16 v[16:19], v[8:11], v[48:55], v0\n" \
  "v_smfmac_f32_16x16x64_bf16 v[20:23], v[4:7], v[56:63], v0\n" \
  "ds_read_b128 v[56:59], v3 offset:14336\n" \
  "ds_read_b128 v[60:63], v3 offset:15360\n" \
  "v_mov_b64 v[40:41], s[92:93]\n" \
  "v_mov_b64 v[42:43], s[92:93]\n" \
  "s_nop 2\n" \
  "v_permlane32_swap_b32 v12, v17\n" \
  "v_permlane32_swap_b32 v13, v16\n" \
  "v_permlane32_swap_b32 v14, v19\n" \
  "v_permlane32_swap_b32 v15, v18\n" \
  "v_pk_add_f32 v[12:13], v[12:13], v[16:17] op_sel:[0,1] op_sel_hi:[1,0]\n" \
  "v_pk_add_f32 v[14:15], v[14:15], v[18:19] op_sel:[0,1] op_sel_hi:[1,0]\n" \
  "buffer_atomic_add_x2 v[12:13], v2, s[40:43], s56 offen\n" \
  "buffer_atomic_add_x2 v[14:15], v2, s[40:43], s57 offen\n" \
  "s_waitcnt lgkmcnt(2)\n" \
  "v_smfmac_f32_16x16x64_bf16 v[20:23], v[8:11], v[32:39], v0\n" \
  "v_smfmac_f32_16x16x64_bf16 v[40:43], v[4:7], v[24:31], v0\n" \
  "s_waitcnt lgkmcnt(0)\n" \
  "v_smfmac_f32_16x16x64_bf16 v[40:43], v[8:11], v[56:63], v0\n" \
  "s_nop 7\n" \
  "v_permlane32_swap_b32 v20, v41\n" \
  "v_permlane32_swap_b32 v21, v40\n" \
  "v_permlane32_swap_b32 v22, v43\n" \
  "v_permlane32_swap_b32 v23, v42\n" \
  "v_pk_add_f32 v[20:21], v[20:21], v[40:41] op_sel:[0,1] op_sel_hi:[1,0]\n" \
  "v_pk_add_f32 v[22:23], v[22:23], v[42:43] op_sel:[0,1] op_sel_hi:[1,0]\n" \
  "buffer_atomic_add_x2 v[20:21], v2, s[40:43], s58 offen\n" \
  "buffer_atomic_add_x2 v[22:23], v2, s[40:43], s59 offen\n" \
  "s_setprio 0\n" \
  "s_memrealtime s[38:39]\n" \
  "v_mbcnt_lo_u32_b32 v48, -1, 0\n" \
  "v_mbcnt_hi_u32_b32 v48, -1, v48\n" \
  "s_add_u32 s88, s74, 157760\n" \
  "v_mov_b32 v59, s88\n" \
  "v_lshl_add_u32 v52, v48, 2, v59\n" \
  "ds_read_b32 v52, v52 offset:192\n" \
  "v_lshrrev_b32 v49, 3, v48\n" \
  "v_and_b32 v50, 7, v48\n" \
  "v_lshlrev_b32 v50, 4, v50\n" \
  "v_lshl_or_b32 v44, v49, 8, v50\n" \
  "s_mov_b32 s87, 0x40000\n" \
  "v_lshlrev_b32 v10, 4, v48\n" \
  "v_lshl_add_u32 v58, v48, 2, s94\n" \
  "s_waitcnt vmcnt(4)\n" \
  "ds_read_b32 v58, v58\n" \
  "s_waitcnt lgkmcnt(0)\n" \
  "v_cmp_eq_u32 vcc, 0, v58\n" \
  "s_and_b32 vcc_lo, vcc_lo, 15\n" \
  "s_cbranch_scc0 .LCV_GOT_%=\n" \
  "s_branch .LCV_ASK_%=\n" \
  ".LCV_LATE_%=:\n" \
  "v_cmp_eq_u32 vcc, 0, v58\n" \
  "s_and_b32 vcc_lo, vcc_lo, 15\n" \
  "s_cbranch_scc0 .LCV_GOT_%=\n" \
  "s_sleep 12\n" \
  ".LCV_ASK_%=:\n" \
  "s_mov_b64 exec, 15\n" \
  "buffer_load_dword v58, v2, s[44:47], s68 offen sc1\n" \
  "s_mov_b64 exec, -1\n" \
  "s_sub_u32 s87, s87, 1\n" \
  "s_cbranch_scc1 .LCV_END_%=\n" \
  "s_waitcnt vmcnt(0)\n" \
  "s_branch .LCV_LATE_%=\n" \
  ".LCV_GOT_%=:\n" \
  "s_mov_b32 m0, s99\n" \
  "s_add_u32 s88, s99, 8192\n" \
  "global_load_lds_dwordx4 v10, s[62:63] sc1\n" \
  "global_load_lds_dwordx4 v10, s[62:63] offset:1024 sc1\n" \
  "s_mov_b32 m0, s88\n" \
  "s_nop 0\n" \
  "global_load_lds_dwordx4 v10, s[64:65] sc1\n" \
  "global_load_lds_dwordx4 v10, s[64:65] offset:1024 sc1\n" \
  ".LCV_HAVE_%=:\n" \
  "s_waitcnt lgkmcnt(0)\n" \
  "s_sub_u32 s61, s38, s61\n" \
  "s_add_u32 s79, s79, 2\n" \
  "s_min_u32 s79, s79, s61\n" \
  "s_sub_u32 s39, s38, 150\n" \
  "v_subrev_u32 v52, s39, v52\n" \
  "v_cmp_lt_u32 vcc, 0x1000, v52\n" \
  "s_cmp_eq_u64 vcc, 0\n" \
  "s_cbranch_scc1 .LCV_OKM_%=\n" \
  ".LCV_AGN_%=:\n" \
  "buffer_load_dword v52, v2, s[0:3], s66 offen sc1\n" \
  "s_sub_u32 s87, s87, 1\n" \
  "s_cbranch_scc1 .LCV_END_%=\n" \
  "s_waitcnt vmcnt(0)\n" \
  "v_cmp_eq_u32 vcc, 0, v52\n" \
  "v_subrev_u32 v52, s39, v52\n" \
  "s_cmp_eq_u64 vcc, 0\n" \
  "s_cbranch_scc1 .LCV_OKM_%=\n" \
  "s_sleep 4\n" \
  "s_branch .LCV_AGN_%=\n" \
  ".LCV_OKM_%=:\n" \
  "s_nop 1\n" \
  "v_max_i32_dpp v52, v52, v52 row_ror:1 row_mask:0xf bank_mask:0xf\n" \
  "s_nop 1\n" \
  "v_max_i32_dpp v52, v52, v52 row_ror:2 row_mask:0xf bank_mask:0xf\n" \
  "s_nop 1\n" \
  "v_max_i32_dpp v52, v52, v52 row_ror:4 row_mask:0xf bank_mask:0xf\n" \
  "s_nop 1\n" \
  "v_max_i32_dpp v52, v52, v52 row_ror:8 row_mask:0xf bank_mask:0xf\n" \
  "s_movk_i32 vcc_lo, 150\n" \
  "v_readlane_b32 s88, v52, 0\n" \
  "s_max_i32 vcc_lo, vcc_lo, s88\n" \
  "v_readlane_b32 s88, v52, 16\n" \
  "s_max_i32 vcc_lo, vcc_lo, s88\n" \
  "v_readlane_b32 s88, v52, 32\n" \
  "s_max_i32 vcc_lo, vcc_lo, s88\n" \
  "v_readlane_b32 s88, v52, 48\n" \
  "s_max_i32 vcc_lo, vcc_lo, s88\n" \
  "s_add_u32 s66, s66, 1024\n" \
  "s_add_u32 vcc_lo, vcc_lo, s39\n" \
  "s_add_u32 vcc_lo, vcc_lo, 23\n" \
  ".LCV_TW_%=:\n" \
  "s_memrealtime s[38:39]\n" \
  "s_waitcnt lgkmcnt(0)\n" \
  "s_sub_u32 s38, s38, vcc_lo\n" \
  "s_cmp_lt_i32 s38, 0\n" \
  "s_cbranch_scc1 .LCV_TW_%=\n" \
  "buffer_load_dwordx4 v[24:27], v44, s[40:43], s60 offen sc1\n" \
  "buffer_load_dwordx4 v[28:31], v44, s[40:43], s60 offen offset:2048 sc1\n" \
  "s_add_u32 s62, s62, 0x80000\n" \
  "s_addc_u32 s63, s63, 0\n" \
  "s_add_u32 s64, s64, 0x40000\n" \
  "s_addc_u32 s65, s65, 0\n" \
  "s_add_u32 s68, s68, 0x1000\n" \
  "s_add_u32 s69, s69, 0x10000\n" \
  "s_add_u32 s70, s70, 0x10000\n" \
  "s_sub_u32 s88, s73, 4\n" \
  "v_lshrrev_b32 v49, 2, v48\n" \
  "v_lshrrev_b32 v50, 4, v48\n" \
  "v_bitop3_b32 v50, v49, v50, 3 bitop3:0x28\n" \
  "v_lshl_add_u32 v4, v50, 3, s97\n" \
  "v_and_b32 v50, 3, v48\n" \
  "v_lshl_add_u32 v4, v50, 5, v4\n" \
  "v_and_b32 v49, 1, v49\n" \
  "v_lshl_add_u32 v4, v49, 8, v4\n" \
  "v_and_b32 v49, 8, v48\n" \
  "v_lshl_add_u32 v4, v49, 4, v4\n" \
  "v_and_b32 v51, 15, v48\n" \
  "v_bfe_u32 v49, v48, 4, 1\n" \
  "v_lshrrev_b32 v50, 5, v48\n" \
  "s_lshl_b32 s89, s88, 10\n" \
  "s_add_u32 s89, s89, s74\n" \
  "s_add_u32 s89, s89, 158720\n" \
  "v_lshrrev_b32 v9, 3, v51\n" \
  "v_mul_u32_u24 v6, 41, v9\n" \
  "v_xor_b32 v6, v6, v51\n" \
  "v_lshl_add_u32 v6, v6, 3, s89\n" \
  "v_lshl_add_u32 v6, v49, 6, v6\n" \
  "v_lshl_add_u32 v6, v50, 9, v6\n" \
  "v_xor_b32 v9, 0x90, v6\n" \
  "v_lshlrev_b32 v8, 4, v49\n" \
  "v_lshl_add_u32 v8, v51, 5, v8\n" \
  "v_lshl_add_u32 v8, v50, 3, v8\n" \
  "s_lshl_b32 s89, s88, 8\n" \
  "s_add_u32 s89, s89, s74\n" \
  "s_add_u32 s89, s89, 139264\n" \
  "v_and_b32 v49, 31, v48\n" \
  "v_lshl_add_u32 v5, v49, 3, s89\n" \
  "v_lshl_add_u32 v5, v50, 10, v5\n" \
  "v_lshrrev_b32 v49, 2, v51\n" \
  "v_and_b32 v50, 5, v51\n" \
  "v_and_or_b32 v49, v49, 2, v50\n" \
  "v_and_b32 v50, 48, v48\n" \
  "v_lshlrev_b32 v7, 3, v50\n" \
  "v_lshl_add_u32 v7, v49, 4, v7\n" \
  "v_bitop3_b32 v49, v51, 2, 2 bitop3:0x0c\n" \
  "v_lshl_add_u32 v7, v49, 29, v7\n" \
  "v_mov_b64 v[12:13], 0\n" \
  "v_mov_b64 v[14:15], 0\n" \
  "v_mov_b64 v[16:17], 0\n" \
  "v_mov_b64 v[18:19], 0\n" \
  "v_mov_b64 v[20:21], 0\n" \
  "v_mov_b64 v[22:23], 0\n" \
  "v_and_b32 v50, 3, v48\n" \
  "v_lshl_add_u32 v40, v50, 2, s98\n" \
  "v_lshrrev_b32 v50, 4, v48\n" \
  "v_mad_u32_u24 v40, v50, s95, v40\n" \
  "v_bfe_u32 v49, v48, 3, 1\n" \
  "v_lshl_add_u32 v40, v49, 6, v40\n" \
  "v_bfe_u32 v49, v48, 2, 1\n" \
  "v_lshl_add_u32 v40, v49, 7, v40\n" \
  "v_xor_b32 v41, 16, v40\n" \
  "v_xor_b32 v42, 32, v40\n" \
  "v_xor_b32 v43, 48, v40\n" \
  "s_add_i32 s80, s86, 0x35000000\n" \
  "v_mov_b32 v11, 0\n" \
  "s_mov_b32 s89, s60\n" \
  "s_bitcmp1_b32 s60, 7\n" \
  "s_cselect_b32 s88, 0x10000, 0\n" \
  "s_bitset1_b32 s88, 7\n" \
  "s_xor_b32 s56, s56, s88\n" \
  "s_xor_b32 s57, s57, s88\n" \
  "s_xor_b32 s58, s58, s88\n" \
  "s_xor_b32 s59, s59, s88\n" \
  "s_xor_b32 s60, s60, s88\n" \
  "s_sub_u32 s71, s71, 1\n" \
  ".LCV_SEE_%=:\n" \
  "s_waitcnt vmcnt(1)\n" \
  "v_cvt_f32_i32 v24, v24\n" \
  "v_cvt_f32_i32 v25, v25\n" \
  "v_cvt_f32_i32 v26, v26\n" \
  "v_cvt_f32_i32 v27, v27\n" \
  "v_max_f32 v45, |v24|, |v26|\n" \
  "v_pk_mul_f32 v[24:25], v[24:25], s[80:81] op_sel_hi:[1,0]\n" \
  "v_pk_mul_f32 v[26:27], v[26:27], s[80:81] op_sel_hi:[1,0]\n" \
  "s_nop 0\n" \
  "v_cvt_pk_bf16_f32 v32, v24, v26\n" \
  "v_cvt_pk_bf16_f32 v33, v25, v27\n" \
  "ds_write_b32 v40, v32\n" \
  "ds_write_b32 v41, v33\n" \
  "s_waitcnt vmcnt(0)\n" \
  "v_cvt_f32_i32 v28, v28\n" \
  "v_cvt_f32_i32 v30, v30\n" \
  "v_cvt_f32_i32 v29, v29\n" \
  "v_max3_f32 v45, v45, |v28|, |v30|\n" \
  "v_cvt_f32_i32 v31, v31\n" \
  "v_cmp_lt_f32 vcc, s91, v45\n" \
  "v_pk_mul_f32 v[28:29], v[28:29], s[80:81] op_sel_hi:[1,0]\n" \
  "v_pk_mul_f32 v[30:31], v[30:31], s[80:81] op_sel_hi:[1,0]\n" \
  "s_cmp_eq_u64 vcc, 0\n" \
  "s_cbranch_scc0 .LCV_POLL_%=\n" \
  "v_cvt_pk_bf16_f32 v36, v28, v30\n" \
  "v_cvt_pk_bf16_f32 v37, v29, v31\n" \
  "ds_write_b32 v42, v36\n" \
  "ds_write_b32 v43, v37\n" \
  "v_dot2c_f32_bf16 v11, v32, v32\n" \
  "v_dot2c_f32_bf16 v11, v33, v33\n" \
  "v_dot2c_f32_bf16 v11, v36, v36\n" \
  "v_dot2c_f32_bf16 v11, v37, v37\n" \
  "s_nop 1\n" \
  "s_nop 0\n" \
  "v_cmpx_lt_f32 vcc, 2.0, v11\n" \
  "ds_max_u32 v59, v11\n" \
  "s_mov_b64 exec, -1\n" \
  "s_branch .LCV_TOP_%=\n" \
  ".LCV_POLL_%=:\n" \
  "buffer_load_dwordx4 v[24:27], v44, s[40:43], s89 offen sc1\n" \
  "buffer_load_dwordx4 v[28:31], v44, s[40:43], s89 offen offset:2048 sc1\n" \
  "s_sub_u32 s87, s87, 1\n" \
  "s_cbranch_scc1 .LCV_END_%=\n" \
  "s_branch .LCV_SEE_%=\n" \
  ".LCV_LAST_%=:\n" \
  "s_waitcnt vmcnt(0)\n" \
  ".LCV_END_%=:\n" \
  "s_endpgm\n"
__device__ __forceinline__ void start_kgen(const MegaArgs& A, char* smem, const int kg) {
  const int w = A.wv, l = lane_id(), tid = 64 * w + l, c = l & 15, g = l >> 4;
  KGen G;
  G.load(A, 8 * kg + w, l);
  {
    const __amdgpu_buffer_rsrc_t rz = MKRS(A.ws + OFF_Z, Z_BYTES);
#pragma unroll
    for (int i = 0; i < 4; ++i) {
      const u64 z = i == 0 && kg < (int)(2 * SZ_ACC / 8192) ? ACC_INIT : 0ull;
      ST128((v4u{(unsigned)z, (unsigned)(z >> 32), (unsigned)z, (unsigned)(z >> 32)}), rz, tid * 16, (i * 64 + kg) * 8192);
    }
  }
  const u64 tag = call_tag(A);
  DRAIN();
  __syncthreads();
  if (tid < 8) __hip_atomic_store((gu64*)(A.ws + OFF_RD) + tid * 64 + kg, tag, RLX_AGENT);
  asm volatile("" : "+v"(G.pa), "+v"(G.pb), "+v"(G.pe));
  G.setup();
  G.st = smem + w * 16640;
  const __amdgpu_buffer_rsrc_t rs = MKRS(A.ws + OFF_KF + (8 * kg + w) * 2048, 386 * 4);
  SST(1);
  unsigned tw = 0u;
  if (TLBW && tid < 384) {
    const unsigned o = (unsigned)OFF_XA + (unsigned)(tid * 8 + ((blockIdx.x >> 3) & 7)) * 65536u;
    if (o < WS_TOTAL) asm volatile("global_load_dword %0, %1, %2" : "=v"(tw) : "v"(o), "s"(A.ws) : "memory");
  }
  G.columns(l);
  const Cx zb = G.power(256.f);
  Cx b = G.C, z = G.power(2.f);
#pragma unroll 1
  for (int f = 1; f < 9;) {
    char* p = G.st + l * 8;
    const int fe = f == 1 ? 4 : 9;
#pragma unroll 1
    for (; f < fe; ++f) {
      b = cmul(b, zb);
      if (f == 2 || f == 4) z = cmul(z, z);
      p = G.rows(p, b, z, f == 1 ? 8 : f < 4 ? 4 : 2);
    }
    const v4u r = G.tile(l);
    if (f == 4) {
      ST128(r, rs, (g < 2 ? 8 * c + 4 * g : 64 * g + 4 * c) * 4, 0);
    } else {
      ST64((v2u{r.x, r.y}), rs, (256 + 64 * g + 2 * c) * 4, 0);
      ST64((v2u{r.z, r.w}), rs, (288 + 64 * g + 2 * c) * 4, 0);
    }
  }
  SST(2);
  DRAIN();
  asm volatile("" ::"v"(tw));
  if (l == 0) __hip_atomic_store((gu64*)(A.ws + OFF_KG) + kg * 16 + w, tag, RLX_AGENT);
  __syncthreads();
  SST(3);
}

constexpr int C_SM = 131072, C_SO = C_SM + 8192, C_KT = C_SO + 2048, C_X = C_KT + 16384, C_SQ = C_X + 1024;
static_assert(C_SQ + 4096 <= 163840 && C_SQ % 512 == 0, "lds");
__device__ __forceinline__ v4u mk_srd(const void* p, const unsigned bytes) {
  const unsigned long long a = (unsigned long long)p;
  return v4u{(unsigned)a, (unsigned)(a >> 32) & 0xffffu, bytes, 0x00020000u};
}
template <int R>
__device__ __forceinline__ void pin8(v8u& x) {
#define PIN_(a, b) if constexpr (R == a) asm volatile("" : "+{v[" #a ":" #b "]}"(x));
  PIN_(64, 71)  PIN_(72, 79)  PIN_(80, 87)  PIN_(88, 95)  PIN_(96, 103)  PIN_(104, 111)  PIN_(112, 119)  PIN_(120, 127)
  PIN_(128, 135)  PIN_(136, 143)  PIN_(144, 151)  PIN_(152, 159)  PIN_(160, 167)  PIN_(168, 175)  PIN_(176, 183)  PIN_(184, 191)
  PIN_(192, 199)  PIN_(200, 207)  PIN_(208, 215)  PIN_(216, 223)  PIN_(224, 231)  PIN_(232, 239)  PIN_(240, 247)  PIN_(248, 255)
#undef PIN_
}
template <int R>
__device__ __forceinline__ void pin4(v4u& x) {
#define PIN_(a, b) if constexpr (R == a) asm volatile("" : "+{v[" #a ":" #b "]}"(x));
  PIN_(32, 35)  PIN_(36, 39)  PIN_(40, 43)  PIN_(44, 47)  PIN_(48, 51)  PIN_(52, 55)  PIN_(56, 59)  PIN_(60, 63)
  PIN_(96, 99)  PIN_(100, 103)  PIN_(104, 107)  PIN_(108, 111)  PIN_(112, 115)  PIN_(116, 119)  PIN_(120, 123)  PIN_(124, 127)
  PIN_(160, 163)  PIN_(164, 167)  PIN_(168, 171)  PIN_(172, 175)  PIN_(176, 179)  PIN_(180, 183)  PIN_(184, 187)  PIN_(188, 191)
#undef PIN_
}
template <bool ATT>
__device__ __forceinline__ void role_chain_t(const MegaArgs& A, char* smem, const int L, const int h, const int rep) {
  u16* swo = reinterpret_cast<u16*>(smem);
  const int w = A.wv;
  char* ws = A.ws;
  unsigned* FL = (unsigned*)(ws + OFF_FL);
  unsigned* TMO = FL + FL_TMO;
#define LANE_IDS()                         \
  const int l = lane_id();                 \
  const int c = l & 15, g = l >> 4;        \
  (void)c; (void)g;
  float* sgf = reinterpret_cast<float*>(smem + C_X);
  unsigned* FQ = FL + FL_FQ + L * 64 * 32 * 32;
  const int wb = (w + ROT_H) & 7, tb = ROT_T;
  v8u w0[8], w1[8], w2[8];
  u64 tag;
  {
    LANE_IDS();
    const unsigned sb = lds_base(smem);
    const int q = (l >> 2) & 3, p = l & 3;
    const int f = (q & 1) | ((g & 1) << 1) | ((q >> 1) << 2);
    const unsigned ra = sb + (16 * (g >> 1) + 4 * (g & 1) + (q & 1) + 8 * (q >> 1)) * 256 + (w ^ f) * 32 + p * 8;
#define W_READ(N, hf, X)                                                       \
  {                                                                           \
    _Pragma("unroll") for (int t = 0; t < 4; ++t) {                           \
      const unsigned a_ = (ra ^ (X)) + (hf) * 8192 + t * 32768;               \
      const v2u r0 = trd(a_), r1 = trd(a_ + 512), r2 = trd(a_ + 16384), r3 = trd(a_ + 16384 + 512); \
      w##N[4 * (hf) + t] = v8u{r0.x, r0.y, r1.x, r1.y, r2.x, r2.y, r3.x, r3.y}; \
    }                                                                         \
    pin8<64 * N + 64 + 32 * hf>(w##N[4 * hf]);                                \
    pin8<64 * N + 72 + 32 * hf>(w##N[4 * hf + 1]);                            \
    pin8<64 * N + 80 + 32 * hf>(w##N[4 * hf + 2]);                            \
    pin8<64 * N + 88 + 32 * hf>(w##N[4 * hf + 3]);                            \
  }
#define W_STAGE(M, hf, WAIT, NEXT)                                             \
  {                                                                           \
    WAIT;                                                                     \
    RAW_BAR();                                                                \
    if ((M) == 0 && ATT) { W_READ(0, hf, 0); W_READ(2, hf, 128); }            \
    if ((M) == 1 && ATT) { W_READ(1, hf, 0); }                                \
    if ((M) == 1 && !ATT) { W_READ(0, hf, 0); }                               \
    if ((M) == 2 && !ATT) { W_READ(1, hf, 128); W_READ(2, hf, 0); }           \
    if ((M) == 3) {                                                           \
      _Pragma("unroll") for (int i = 0; i < 8; ++i) {                         \
        const unsigned a_ = rb + (hf) * 8192 + (i & 1) * 65536 + ((4 * wb + (i >> 1)) ^ f ^ tb) * 32; \
        const v2u r0 = trd(a_), r1 = trd(a_ + 2048);                          \
        fr[hf][i] = v4u{r0.x, r0.y, r1.x, r1.y};                              \
      }                                                                       \
      constexpr int R_ = hf ? 32 : ATT ? 160 : 96;                            \
      pin4<R_>(fr[hf][0]); pin4<R_ + 4>(fr[hf][1]); pin4<R_ + 8>(fr[hf][2]); pin4<R_ + 12>(fr[hf][3]); \
      pin4<R_ + 16>(fr[hf][4]); pin4<R_ + 20>(fr[hf][5]); pin4<R_ + 24>(fr[hf][6]); pin4<R_ + 28>(fr[hf][7]); \
    }                                                                         \
    RAW_BAR();                                                                \
    NEXT;                                                                     \
  }
#define WO_ISSUE(hf)                                                          \
  {                                                                           \
    const char* base_ = (const char*)A.Wo + (size_t)(h * DH + 64 * (hf) + 8 * w) * (DM * 2); \
    _Pragma("unroll") for (int i = 0; i < 8; ++i) {                           \
      const int fd = (i & 1) | (((i >> 2) & 1) << 1) | ((w & 1) << 2);        \
      glds16(base_, (unsigned)(i * 1024 + ((l ^ (fd << 1)) << 4)), sb + w * 16384 + (hf) * 8192 + i * 1024); \
    }                                                                         \
  }
    const unsigned rb = sb + (2 * (g >> 1) + (q >> 1)) * 16384 + (4 * (g & 1) + (q & 1)) * 1024 + p * 8;
    v4u fr[2][8];
#define WO_PUT(hf) \
  _Pragma("unroll") for (int i = 0; i < 8; ++i) *reinterpret_cast<v4u*>(swo + w * 8192 + (hf) * 4096 + i * 512 + l * 8) = fr[hf][i];
    __builtin_amdgcn_s_waitcnt(0x0f70);
    glds16(A.mem0, (unsigned)((c & 7) * DM + 64 * w + 16 * g + 8 * (c >> 3)) * 2, sb + C_SM + w * 1024);
    wqkv_issue<true>(A.Wv, 0, h, w, l, sb);
    wqkv_issue<true>(A.Wv, 1, h, w, l, sb);
    tag = call_tag(A);
    W_STAGE(2, 0, WAIT_VM(8), WO_ISSUE(0));
    W_STAGE(2, 1, WAIT_VM(8), WO_ISSUE(1));
    SST(4);
    if (!ATT) {
      float f2 = 0.f;
      asm volatile("" : "+v"(f2));
#pragma unroll
      for (int t = 0; t < 8; ++t)
#pragma unroll
        for (int e = 0; e < 8; ++e) f2 = dot2sq(w2[t][e], dot2sq(w1[t][e], f2));
      f2 = wave_sum_dpp(f2);
      if (l == 0) { sgf[w] = f2; sgf[16] = 0.f; }
    }
    W_STAGE(3, 0, WAIT_VM(8), wqkv_issue<true>(A.Wq, 0, h, w, l, sb));
    W_STAGE(3, 1, WAIT_VM(8), wqkv_issue<true>(A.Wq, 1, h, w, l, sb));
    SST(5);
    {
      float cn[4] = {0.f, 0.f, 0.f, 0.f};
      asm volatile("" : "+v"(cn[0]), "+v"(cn[1]), "+v"(cn[2]), "+v"(cn[3]));
#pragma unroll
      for (int e = 0; e < 64; ++e) cn[e & 3] = dot2sq(fr[e >> 5][2 * (e & 3) + ((e >> 4) & 1)][(e >> 2) & 3], cn[e & 3]);
      const float m2 = wave_max_dpp(swap_add32(swap_add16(cn[0], cn[1]), swap_add16(cn[2], cn[3])));
      if (l == 0) sgf[8 + w] = m2;
    }
#define G_VALUE()                                                                                                   \
  if (w == 4 && L == 0) {                                                                                           \
    const f32x4 a = *reinterpret_cast<const f32x4*>(sgf + 4), b = *reinterpret_cast<const f32x4*>(sgf + 8);         \
    const f32x4 d = *reinterpret_cast<const f32x4*>(sgf + 12);                                                      \
    const float m2 = fmaxf(fmaxf(fmaxf(b.x, b.y), fmaxf(b.z, b.w)), fmaxf(fmaxf(d.x, d.y), fmaxf(d.z, d.w)));       \
    if (l < 8) __hip_atomic_store((gu64*)(ws + OFF_G) + l * 32 + h, tagged(tag, __float_as_uint(((a.x + a.y) + (a.z + a.w)) * m2 * 1.01f)), RLX_AGENT); \
  }
    W_STAGE(0, 0, WAIT_VM(8), G_VALUE(); wqkv_issue<true>(A.Wk, 0, h, w, l, sb));
    W_STAGE(0, 1, WAIT_VM(8), if (ATT) {
      wqkv_issue<true>(A.Wk, 1, h, w, l, sb);
      wqkv_issue<true>(A.Wk, 1, h, w + 4, l, sb);
    });
    SST(6);
    W_STAGE(1, 0, if (ATT) WAIT_VM(16); else { ready_wait(A, tag, ready_look(A)); __builtin_amdgcn_s_waitcnt(0x0f70); }, WO_PUT(0));
    if (w == 4) {
      u64 gv;
      for (unsigned spins = 0;; ++spins) {
        gv = __hip_atomic_load((gu64*)(ws + OFF_G) + (blockIdx.x & 7) * 32 + (l & 31), RLX_AGENT);
        if (__all(has_tag(tag, (unsigned)gv, (unsigned)(gv >> 32))) || spins > SPIN_CAP) break;
        __builtin_amdgcn_s_sleep(GSL);
      }
      const float gm = wave_max_dpp(hi16((unsigned)(gv >> 32)));
      if (l == 0) sgf[0] = gm * 1.111f;
    }
    W_STAGE(1, 1, WAIT_VM(0), WO_PUT(1));
    SST(7);
#undef G_VALUE
#undef WO_PUT
#undef W_STAGE
#undef W_READ
#undef WO_ISSUE
  }
  WSTAMP2();
  const unsigned sb = lds_base(smem);
  const char* gK = ws + OFF_QK + L * SZ_QK + ((size_t)h * 16 + 2 * w) * 1024;
  const char* gV = ws + OFF_VF + L * SZ_O + ((size_t)h * 8 + 2 * w - 8) * 1024;
  v8u pv;
  {
    LANE_IDS();
    const unsigned voff = g * 256 + c * 8, wp = sb + w * 16384 + l * 16;
    pv[0] = (c & 8) ? 0xEEEE : 0x4444;
    pv[1] = sb + C_SM + l * 16;
    const unsigned ra = sb + C_SQ + (g >> 1) * 512 + (c ^ ((g & 1) * 18)) * 8;
    pv[2] = ATT ? ra : voff;
    pv[3] = ATT ? ra ^ 0x108 : wp;
    pv[4] = sb + ((c & 2) ? C_SQ : C_SO) + w * 256 + (((c & 5) | ((c >> 2) & 2)) + 8 * (g >> 1)) * 16 + 8 * ((g ^ (c >> 1)) & 1);
    pv[5] = voff;
    pv[6] = wp;
    pv[7] = sb + C_SO + (l >> 5) * 1024 + w * 256 + (l & 31) * 8;
  }
  if (!ATT) {
    for (unsigned spins = 0; !__all(__hip_atomic_load((gu32*)FQ + h * 32 + 2 * (lane_id() & 3), RLX_AGENT) != 0u); ++spins) {
      if (spin_timeout(spins, TMO)) return;
      __builtin_amdgcn_s_sleep(4);
    }
    asm volatile("" ::: "memory");
    const unsigned lo = lane_id() * 16, d = sb + C_KT + (2 * w - 8) * 1024;
    glds16c(gK, lo, d);
    glds16c(gK + 1024, lo, d + 1024);
    glds16c(gV, lo, d + 8192);
    glds16c(gV + 1024, lo, d + 9216);
  }
  const unsigned gw2 = __builtin_amdgcn_readfirstlane(__float_as_uint(sgf[0]));
  DRAIN();
  SST(8);
#define POL 2
#define PG(w) (POL == 2 ? (w) * 4096 : ((w) & 1) * 4096 + ((w) >> 1) * 16384)
  const v4u rsA = mk_srd(ws + OFF_ACC + L * SZ_ACC + (POL == 1 ? 8192 : 0), SZ_ACC - 8192);
  const v4u rsF = mk_srd(FQ, 64 * 32 * 32 * 4);
  const v4u rsMK = mk_srd(ws + OFF_MEMK + L * SZ_MEM, SZ_MEM);
  const v4u rsMV = mk_srd(ws + OFF_MEMV + L * SZ_MEM, SZ_MEM);
  const v4u pAB = (v4u{0u, 2048u, 3072u, 1024u} ^ (1536u * tb)) + PG(wb);
  const char* pGK = gK + NH * 16 * 1024;
  const char* pGV = gV + NH * 8 * 1024;
  const v4u rsT = mk_srd(FL + FL_FK + L * 32, 64 * 1024 - 128);
  unsigned sc[8];
  sc[0] = (32 + h) * 128;
  sc[1] = (h * 4 + w - 4) * 512;
  sc[2] = (h * 4 + w - 4) * 512;
  sc[3] = NSEG - 1;
  sc[4] = gw2;
  sc[5] = w;
  sc[6] = sb;
  const char* dbg = ws;
#define CHAIN_OPS                                                                                                          \
  ::"{v[64:71]}"(w0[0]), "{v[72:79]}"(w0[1]), "{v[80:87]}"(w0[2]), "{v[88:95]}"(w0[3]), "{v[96:103]}"(w0[4]),              \
      "{v[104:111]}"(w0[5]), "{v[112:119]}"(w0[6]), "{v[120:127]}"(w0[7]), "{v[128:135]}"(w1[0]), "{v[136:143]}"(w1[1]),    \
      "{v[144:151]}"(w1[2]), "{v[152:159]}"(w1[3]), "{v[160:167]}"(w1[4]), "{v[168:175]}"(w1[5]), "{v[176:183]}"(w1[6]),    \
      "{v[184:191]}"(w1[7]), "{v[192:199]}"(w2[0]), "{v[200:207]}"(w2[1]), "{v[208:215]}"(w2[2]), "{v[216:223]}"(w2[3]),    \
      "{v[224:231]}"(w2[4]), "{v[232:239]}"(w2[5]), "{v[240:247]}"(w2[6]), "{v[248:255]}"(w2[7]), "{v[0:7]}"(pv),          \
      "{s[40:43]}"(rsA), "{s[44:47]}"(rsF), "{s[48:51]}"(rsMK), "{s[52:55]}"(rsMV), "{s[56:59]}"(pAB), "{s60}"(PG(w)), "{s61}"(ATT ? PG(h >> 2) + (h & 3) * 1024 : L), \
      "{s[62:63]}"(pGK), "{s[64:65]}"(pGV), "{s66}"(0), "{s67}"(w == 4 ? (h >> 3) * 256 + (h & 7) * 8 : 0x40000000), "{s68}"(sc[0]), "{s69}"(sc[1]), "{s70}"(sc[2]),    \
      "{s71}"(sc[3]), "{s72}"(sc[4]), "{s73}"(sc[5]), "{s74}"(sc[6]), "{s[36:37]}"(dbg),                   \
      "{s[0:3]}"(rsT)                                                                                                      \
      : "memory", "vcc", "scc", "s99", "v63"
  if (ATT) asm volatile("s_mov_b64 exec, -1\ns_nop 4\n" CHAIN_ASM_ATT CHAIN_OPS);
  else asm volatile("s_mov_b64 exec, -1\ns_nop 4\n" CHAIN_ASM_VO CHAIN_OPS);
#undef CHAIN_OPS
#undef LANE_IDS
}

__device__ __forceinline__ void role_chain(const MegaArgs& A, char* smem, const int L, const int h, const int rep) {
  if (A.wv < 4) role_chain_t<true>(A, smem, L, h, rep);
  else role_chain_t<false>(A, smem, L, h, rep);
}

constexpr int AW_SO = 0, AW_TK = 32768, AW_CNT = AW_TK + 98304;
__device__ __forceinline__ unsigned umin3(unsigned a, unsigned b, unsigned c) {
  return __builtin_elementwise_min(__builtin_elementwise_min(a, b), c);
}
#define AW_ASM_X \
  "s_mov_b32 s61, 0\n" \
  "s_mov_b32 s67, 1\n" \
  "s_mov_b32 s68, 0\n" \
  "s_mov_b32 s70, 0\n" \
  "s_mov_b32 s73, 0\n" \
  "s_mov_b32 s71, 0x3e0293ee\n" \
  "s_mov_b32 s72, 0xbe0293ee\n" \
  "s_mov_b32 s65, 49152\n" \
  "s_bitcmp0_b32 s58, 2\n" \
  "s_cbranch_scc1 .LCX_OWN_%=\n" \
  "v_swap_b32 v192, v208\n" \
  "v_swap_b32 v193, v209\n" \
  "v_swap_b32 v194, v210\n" \
  "v_swap_b32 v195, v211\n" \
  "v_swap_b32 v196, v212\n" \
  "v_swap_b32 v197, v213\n" \
  "v_swap_b32 v198, v214\n" \
  "v_swap_b32 v199, v215\n" \
  "v_swap_b32 v200, v216\n" \
  "v_swap_b32 v201, v217\n" \
  "v_swap_b32 v202, v218\n" \
  "v_swap_b32 v203, v219\n" \
  "v_swap_b32 v204, v220\n" \
  "v_swap_b32 v205, v221\n" \
  "v_swap_b32 v206, v222\n" \
  "v_swap_b32 v207, v223\n" \
  "v_swap_b32 v224, v240\n" \
  "v_swap_b32 v225, v241\n" \
  "v_swap_b32 v226, v242\n" \
  "v_swap_b32 v227, v243\n" \
  "v_swap_b32 v228, v244\n" \
  "v_swap_b32 v229, v245\n" \
  "v_swap_b32 v230, v246\n" \
  "v_swap_b32 v231, v247\n" \
  "v_swap_b32 v232, v248\n" \
  "v_swap_b32 v233, v249\n" \
  "v_swap_b32 v234, v250\n" \
  "v_swap_b32 v235, v251\n" \
  "v_swap_b32 v236, v252\n" \
  "v_swap_b32 v237, v253\n" \
  "v_swap_b32 v238, v254\n" \
  "v_swap_b32 v239, v255\n" \
  ".LCX_OWN_%=:\n" \
  "v_mov_b32 v38, s58\n" \
  "s_memrealtime s[38:39]\n" \
  "s_waitcnt lgkmcnt(0)\n" \
  "s_mov_b32 s69, s38\n" \
  "s_setprio 3\n" \
  ".LCX_TOP_%=:\n" \
  "s_mov_b32 s62, 0x200000\n" \
  "s_cmp_ge_u32 s73, s67\n" \
  "s_cbranch_scc1 .LCX_TOK_%=\n" \
  "s_cmp_eq_u32 s61, 0\n" \
  "s_cbranch_scc1 .LCX_SPIN_%=\n" \
  ".LCX_RE_%=:\n" \
  "ds_read_b32 v37, v38\n" \
  "s_add_u32 s62, s62, 1\n" \
  "s_waitcnt lgkmcnt(0)\n" \
  "v_readfirstlane_b32 s73, v37\n" \
  "s_bfe_u32 s64, s73, 0x80008\n" \
  "s_and_b32 s73, s73, 0xff\n" \
  "s_min_u32 s73, s73, s64\n" \
  "s_cmp_ge_u32 s73, s67\n" \
  "s_cbranch_scc1 .LCX_TOK_%=\n" \
  "s_sleep 1\n" \
  "s_cmp_lt_u32 s62, 2097158\n" \
  "s_cbranch_scc1 .LCX_RE_%=\n" \
  "s_waitcnt vmcnt(0)\n" \
  "v_mov_b32 v144, s59\n" \
  "v_mov_b32 v145, 1\n" \
  "s_mov_b64 exec, 1\n" \
  "ds_add_rtn_u32 v144, v144, v145\n" \
  "s_mov_b64 exec, -1\n" \
  "s_xor_b32 s59, s59, 4\n" \
  "s_mov_b32 s61, 0\n" \
  "s_waitcnt lgkmcnt(0)\n" \
  "v_readfirstlane_b32 s63, v144\n" \
  "v_lshlrev_b32 v144, 5, v5\n" \
  "s_and_b32 s63, s63, 7\n" \
  "s_cmp_eq_u32 s63, 7\n" \
  "s_cbranch_scc0 .LCX_W1_%=\n" \
  "s_mov_b64 exec, 15\n" \
  "global_store_dword v144, v145, s[52:53] sc1\n" \
  "s_mov_b64 exec, -1\n" \
  ".LCX_W1_%=:\n" \
  "s_add_u32 s52, s52, 2048\n" \
  "s_addc_u32 s53, s53, 0\n" \
  ".LCX_SPIN_%=:\n" \
  "s_sleep 2\n" \
  "s_memrealtime s[38:39]\n" \
  "ds_read_b32 v37, v38\n" \
  "s_sub_u32 s62, s62, 1\n" \
  "s_cbranch_scc1 .LCX_END_%=\n" \
  "s_waitcnt lgkmcnt(0)\n" \
  "v_readfirstlane_b32 s73, v37\n" \
  "s_add_u32 s38, s38, 80\n" \
  "s_bfe_u32 s64, s73, 0x80008\n" \
  "s_and_b32 s73, s73, 0xff\n" \
  "s_min_u32 s73, s73, s64\n" \
  "s_cmp_ge_u32 s73, s67\n" \
  "s_cbranch_scc0 .LCX_SPIN_%=\n" \
  "s_cmp_eq_u32 s70, 0\n" \
  "s_cbranch_scc1 .LCX_TOK_%=\n" \
  "s_sub_u32 s63, s38, s69\n" \
  "s_max_i32 s63, s63, 0\n" \
  "s_add_u32 s69, s69, s63\n" \
  ".LCX_TOK_%=:\n" \
  "ds_read_b128 v[48:51], v6 offset:0\n" \
  "ds_read_b128 v[64:67], v0 offset:8192\n" \
  "ds_read_b128 v[80:83], v0 offset:12288\n" \
  "ds_read_b128 v[52:55], v6 offset:1024\n" \
  "ds_read_b128 v[68:71], v0 offset:9216\n" \
  "ds_read_b128 v[84:87], v0 offset:13312\n" \
  "ds_read_b128 v[56:59], v6 offset:2048\n" \
  "ds_read_b128 v[72:75], v0 offset:10240\n" \
  "ds_read_b128 v[88:91], v0 offset:14336\n" \
  "ds_read_b128 v[60:63], v6 offset:3072\n" \
  "ds_read_b128 v[76:79], v0 offset:11264\n" \
  "ds_read_b128 v[92:95], v0 offset:15360\n" \
  "s_waitcnt lgkmcnt(9)\n" \
  "v_mfma_f32_16x16x32_bf16 v[20:23], v[64:67], v[48:51], v[8:11]\n" \
  "v_mfma_f32_16x16x32_bf16 v[24:27], v[80:83], v[48:51], v[12:15]\n" \
  "ds_read_b128 v[96:99], v0 offset:16384\n" \
  "ds_read_b128 v[100:103], v0 offset:17408\n" \
  "s_waitcnt lgkmcnt(8)\n" \
  "v_mfma_f32_16x16x32_bf16 v[20:23], v[68:71], v[52:55], v[20:23]\n" \
  "v_mfma_f32_16x16x32_bf16 v[24:27], v[84:87], v[52:55], v[24:27]\n" \
  "ds_read_b128 v[104:107], v0 offset:18432\n" \
  "ds_read_b128 v[108:111], v0 offset:19456\n" \
  "s_waitcnt lgkmcnt(7)\n" \
  "v_mfma_f32_16x16x32_bf16 v[20:23], v[72:75], v[56:59], v[20:23]\n" \
  "v_mfma_f32_16x16x32_bf16 v[24:27], v[88:91], v[56:59], v[24:27]\n" \
  "ds_read_b128 v[112:115], v0 offset:20480\n" \
  "ds_read_b128 v[116:119], v0 offset:21504\n" \
  "s_waitcnt lgkmcnt(6)\n" \
  "v_mfma_f32_16x16x32_bf16 v[20:23], v[76:79], v[60:63], v[20:23]\n" \
  "v_mfma_f32_16x16x32_bf16 v[24:27], v[92:95], v[60:63], v[24:27]\n" \
  "ds_read_b128 v[120:123], v0 offset:22528\n" \
  "ds_read_b128 v[124:127], v0 offset:23552\n" \
  "s_add_u32 s67, s67, 1\n" \
  "v_add_u32 v0, s65, v0\n" \
  "v_add_u32 v6, s65, v6\n" \
  "s_sub_u32 s65, 0, s65\n" \
  "s_nop 1\n" \
  "v_max3_f32 v32, v20, v21, v22\n" \
  "v_max3_f32 v33, v23, v24, v25\n" \
  "v_max3_f32 v32, v32, v26, v27\n" \
  "v_max_f32 v32, v32, v33\n" \
  "v_mov_b32 v33, v32\n" \
  "s_nop 1\n" \
  "v_permlane16_swap_b32 v32, v33\n" \
  "v_max_f32 v32, v32, v33\n" \
  "v_mov_b32 v33, v32\n" \
  "s_nop 1\n" \
  "v_permlane32_swap_b32 v32, v33\n" \
  "v_max_f32 v32, v32, v33\n" \
  "v_mul_f32 v33, s72, v32\n" \
  "v_fma_f32 v20, v20, s71, v33\n" \
  "v_fma_f32 v21, v21, s71, v33\n" \
  "v_fma_f32 v22, v22, s71, v33\n" \
  "v_fma_f32 v23, v23, s71, v33\n" \
  "v_fma_f32 v24, v24, s71, v33\n" \
  "v_fma_f32 v25, v25, s71, v33\n" \
  "v_fma_f32 v26, v26, s71, v33\n" \
  "v_fma_f32 v27, v27, s71, v33\n" \
  "v_exp_f32 v20, v20\n" \
  "v_exp_f32 v21, v21\n" \
  "v_exp_f32 v22, v22\n" \
  "v_exp_f32 v23, v23\n" \
  "v_exp_f32 v24, v24\n" \
  "v_exp_f32 v25, v25\n" \
  "v_exp_f32 v26, v26\n" \
  "v_exp_f32 v27, v27\n" \
  "v_add_f32 v34, v20, v21\n" \
  "v_add_f32 v33, v22, v23\n" \
  "v_add_f32 v36, v24, v25\n" \
  "v_add_f32 v34, v34, v33\n" \
  "v_add_f32 v33, v26, v27\n" \
  "v_add_f32 v34, v34, v36\n" \
  "v_add_f32 v34, v34, v33\n" \
  "v_mov_b64 v[28:29], 0\n" \
  "v_mov_b64 v[30:31], 0\n" \
  "s_waitcnt vmcnt(0) lgkmcnt(0)\n" \
  "s_cmp_eq_u32 s61, 0\n" \
  "s_cbranch_scc1 .LCX_NP_%=\n" \
  "v_mov_b32 v144, s59\n" \
  "v_mov_b32 v145, 1\n" \
  "s_mov_b64 exec, 1\n" \
  "ds_add_rtn_u32 v144, v144, v145\n" \
  "s_mov_b64 exec, -1\n" \
  "s_xor_b32 s59, s59, 4\n" \
  "s_cmp_lg_u32 s68, 0\n" \
  "s_cbranch_scc1 .LCX_TEST_%=\n" \
  "s_mov_b32 s61, 0\n" \
  "s_waitcnt lgkmcnt(0)\n" \
  "v_readfirstlane_b32 s63, v144\n" \
  "v_lshlrev_b32 v144, 5, v5\n" \
  "s_and_b32 s63, s63, 7\n" \
  "s_cmp_eq_u32 s63, 7\n" \
  "s_cbranch_scc0 .LCX_W2_%=\n" \
  "s_mov_b64 exec, 15\n" \
  "global_store_dword v144, v145, s[52:53] sc1\n" \
  "s_mov_b64 exec, -1\n" \
  ".LCX_W2_%=:\n" \
  "s_add_u32 s52, s52, 2048\n" \
  "s_addc_u32 s53, s53, 0\n" \
  ".LCX_NP_%=:\n" \
  "s_cmp_lg_u32 s68, 0\n" \
  "s_cbranch_scc1 .LCX_TEST_%=\n" \
  ".LCX_WF_%=:\n" \
  "s_memrealtime s[38:39]\n" \
  "s_waitcnt lgkmcnt(0)\n" \
  "s_sub_u32 s63, s69, s38\n" \
  "s_cmp_le_i32 s63, 0\n" \
  "s_cbranch_scc1 .LCX_GO_%=\n" \
  "s_cmp_lt_i32 s63, 12\n" \
  "s_cbranch_scc1 .LCX_WF_%=\n" \
  "s_sleep 3\n" \
  "s_branch .LCX_WF_%=\n" \
  ".LCX_GO_%=:\n" \
  "v_mov_b32 v35, 0\n" \
  "global_load_dwordx4 v[128:131], v5, s[48:49] offset:0 sc1\n" \
  "global_load_dwordx4 v[132:135], v5, s[48:49] offset:1024 sc1\n" \
  "global_load_dwordx4 v[136:139], v3, s[48:49] offset:0 sc1\n" \
  "global_load_dwordx4 v[140:143], v3, s[48:49] offset:512 sc1\n" \
  "global_load_dword v35, v35, s[50:51] sc1\n" \
  "s_sub_u32 s62, s62, 8\n" \
  "s_cbranch_scc1 .LCX_END_%=\n" \
  "s_waitcnt vmcnt(0)\n" \
  ".LCX_TEST_%=:\n" \
  "v_smfmac_f32_16x16x64_bf16 v[28:31], v[128:131], v[48:55], v1\n" \
  "v_smfmac_f32_16x16x64_bf16 v[28:31], v[132:135], v[56:63], v1\n" \
  "v_min3_u32 v33, v128, v129, v130\n" \
  "v_min3_u32 v36, v131, v132, v133\n" \
  "v_min3_u32 v33, v33, v134, v135\n" \
  "v_min3_u32 v36, v36, v136, v137\n" \
  "v_min3_u32 v33, v33, v138, v139\n" \
  "v_min3_u32 v36, v36, v140, v141\n" \
  "v_min3_u32 v33, v33, v142, v143\n" \
  "v_min_u32 v33, v33, v36\n" \
  "v_cmp_eq_u32 vcc, 0, v33\n" \
  "v_readfirstlane_b32 s64, v35\n" \
  "s_mov_b32 s68, 0\n" \
  "s_cmp_eq_u64 vcc, 0\n" \
  "s_cbranch_scc0 .LCX_FAIL_%=\n" \
  "v_permlane32_swap_b32 v28, v30\n" \
  "v_permlane32_swap_b32 v29, v31\n" \
  "v_mov_b32 v33, s67\n" \
  "ds_write_b32 v38, v33 offset:16\n" \
  "v_add_f32 v28, v28, v30\n" \
  "v_add_f32 v29, v29, v31\n" \
  "v_max3_f32 v36, v28, v29, v32\n" \
  "v_mov_b32 v33, v36\n" \
  "s_cmp_eq_u32 s64, 0\n" \
  "s_cselect_b32 s64, s38, s64\n" \
  "v_permlane16_swap_b32 v36, v33\n" \
  "v_max_f32 v36, v36, v33\n" \
  "v_mov_b32 v33, v36\n" \
  "s_sub_u32 s63, s64, s70\n" \
  "s_mov_b32 s70, s64\n" \
  "v_permlane32_swap_b32 v36, v33\n" \
  "v_max_f32 v36, v36, v33\n" \
  "v_mul_f32 v36, s72, v36\n" \
  "v_fma_f32 v32, v32, s71, v36\n" \
  "v_fma_f32 v28, v28, s71, v36\n" \
  "v_fma_f32 v29, v29, s71, v36\n" \
  "v_exp_f32 v32, v32\n" \
  "v_exp_f32 v28, v28\n" \
  "v_exp_f32 v29, v29\n" \
  "v_fma_f32 v34, v34, v32, v28\n" \
  "v_add_f32 v34, v34, v29\n" \
  "v_mov_b32 v33, v34\n" \
  "s_min_u32 s63, s63, 250\n" \
  "s_add_u32 s69, s64, s63\n" \
  "v_permlane16_swap_b32 v34, v33\n" \
  "v_add_f32 v34, v34, v33\n" \
  "v_mov_b32 v33, v34\n" \
  "s_add_u32 s69, s69, -48\n" \
  "s_add_u32 s48, s48, 0x10000\n" \
  "s_addc_u32 s49, s49, 0\n" \
  "v_permlane32_swap_b32 v34, v33\n" \
  "v_add_f32 v34, v34, v33\n" \
  "v_rcp_f32 v34, v34\n" \
  "s_add_u32 s50, s50, 1024\n" \
  "s_addc_u32 s51, s51, 0\n" \
  "v_mul_f32 v32, v32, v34\n" \
  "v_mul_f32 v20, v20, v32\n" \
  "v_mul_f32 v21, v21, v32\n" \
  "v_mul_f32 v22, v22, v32\n" \
  "v_mul_f32 v23, v23, v32\n" \
  "v_mul_f32 v24, v24, v32\n" \
  "v_mul_f32 v25, v25, v32\n" \
  "v_mul_f32 v26, v26, v32\n" \
  "v_mul_f32 v27, v27, v32\n" \
  "v_cvt_pk_bf16_f32 v40, v20, v21\n" \
  "v_cvt_pk_bf16_f32 v41, v22, v23\n" \
  "v_cvt_pk_bf16_f32 v42, v24, v25\n" \
  "v_cvt_pk_bf16_f32 v43, v26, v27\n" \
  "v_mul_f32 v28, v28, v34\n" \
  "v_mul_f32 v29, v29, v34\n" \
  "v_mfma_f32_16x16x32_bf16 v[64:67], v[96:99], v[40:43], 0\n" \
  "v_cvt_pk_bf16_f32 v44, v28, v29\n" \
  "v_mov_b32 v45, 0\n" \
  "v_mfma_f32_16x16x32_bf16 v[68:71], v[100:103], v[40:43], 0\n" \
  "v_cvt_pk_bf16_f32 v47, v28, v29\n" \
  "v_mov_b32 v46, 0\n" \
  "v_mfma_f32_16x16x32_bf16 v[72:75], v[104:107], v[40:43], 0\n" \
  "v_permlane32_swap_b32 v44, v45\n" \
  "v_mfma_f32_16x16x32_bf16 v[76:79], v[108:111], v[40:43], 0\n" \
  "v_permlane32_swap_b32 v46, v47\n" \
  "v_mfma_f32_16x16x32_bf16 v[80:83], v[112:115], v[40:43], 0\n" \
  "v_xor_b32 v16, 4096, v7\n" \
  "v_mfma_f32_16x16x32_bf16 v[84:87], v[116:119], v[40:43], 0\n" \
  "v_xor_b32 v17, 8192, v7\n" \
  "v_mfma_f32_16x16x32_bf16 v[88:91], v[120:123], v[40:43], 0\n" \
  "v_xor_b32 v18, 12288, v7\n" \
  "v_mfma_f32_16x16x32_bf16 v[92:95], v[124:127], v[40:43], 0\n" \
  "s_cmp_eq_u32 s61, 0\n" \
  "s_cbranch_scc1 .LCX_NQ_%=\n" \
  "s_mov_b32 s61, 0\n" \
  "s_waitcnt lgkmcnt(0)\n" \
  "v_readfirstlane_b32 s63, v144\n" \
  "v_lshlrev_b32 v144, 5, v5\n" \
  "s_and_b32 s63, s63, 7\n" \
  "s_cmp_eq_u32 s63, 7\n" \
  "s_cbranch_scc0 .LCX_W3_%=\n" \
  "s_mov_b64 exec, 15\n" \
  "global_store_dword v144, v145, s[52:53] sc1\n" \
  "s_mov_b64 exec, -1\n" \
  ".LCX_W3_%=:\n" \
  "s_add_u32 s52, s52, 2048\n" \
  "s_addc_u32 s53, s53, 0\n" \
  ".LCX_NQ_%=:\n" \
  "s_memrealtime s[38:39]\n" \
  "ds_read_b32 v37, v38\n" \
  "v_mfma_f32_16x16x16_bf16 v[64:67], v[136:137], v[44:45], v[64:67]\n" \
  "v_mfma_f32_16x16x16_bf16 v[68:71], v[140:141], v[44:45], v[68:71]\n" \
  "v_mfma_f32_16x16x16_bf16 v[72:75], v[136:137], v[46:47], v[72:75]\n" \
  "v_mfma_f32_16x16x16_bf16 v[76:79], v[140:141], v[46:47], v[76:79]\n" \
  "v_mfma_f32_16x16x16_bf16 v[80:83], v[138:139], v[44:45], v[80:83]\n" \
  "v_mfma_f32_16x16x16_bf16 v[84:87], v[142:143], v[44:45], v[84:87]\n" \
  "v_mfma_f32_16x16x16_bf16 v[88:91], v[138:139], v[46:47], v[88:91]\n" \
  "v_mfma_f32_16x16x16_bf16 v[92:95], v[142:143], v[46:47], v[92:95]\n" \
  "s_nop 0\n" \
  "v_cvt_pk_bf16_f32 v160, v64, v65\n" \
  "v_cvt_pk_bf16_f32 v161, v66, v67\n" \
  "v_cvt_pk_bf16_f32 v162, v68, v69\n" \
  "v_cvt_pk_bf16_f32 v163, v70, v71\n" \
  "ds_write_b128 v7, v[160:163] offset:0\n" \
  "v_cvt_pk_bf16_f32 v164, v72, v73\n" \
  "v_cvt_pk_bf16_f32 v165, v74, v75\n" \
  "v_cvt_pk_bf16_f32 v166, v76, v77\n" \
  "v_cvt_pk_bf16_f32 v167, v78, v79\n" \
  "ds_write_b128 v7, v[164:167] offset:1024\n" \
  "v_cvt_pk_bf16_f32 v168, v80, v81\n" \
  "v_cvt_pk_bf16_f32 v169, v82, v83\n" \
  "v_cvt_pk_bf16_f32 v170, v84, v85\n" \
  "v_cvt_pk_bf16_f32 v171, v86, v87\n" \
  "ds_write_b128 v7, v[168:171] offset:2048\n" \
  "v_cvt_pk_bf16_f32 v172, v88, v89\n" \
  "v_cvt_pk_bf16_f32 v173, v90, v91\n" \
  "v_cvt_pk_bf16_f32 v174, v92, v93\n" \
  "v_cvt_pk_bf16_f32 v175, v94, v95\n" \
  "ds_write_b128 v7, v[172:175] offset:3072\n" \
  "v_xor_b32 v7, 0x4000, v7\n" \
  "v_mfma_f32_16x16x32_bf16 v[112:115], v[192:195], v[160:163], 0\n" \
  "v_mfma_f32_16x16x32_bf16 v[116:119], v[224:227], v[160:163], 0\n" \
  "s_waitcnt lgkmcnt(0)\n" \
  "s_barrier\n" \
  "ds_read_b128 v[64:67], v16 offset:0\n" \
  "ds_read_b128 v[68:71], v16 offset:1024\n" \
  "ds_read_b128 v[72:75], v16 offset:2048\n" \
  "ds_read_b128 v[76:79], v16 offset:3072\n" \
  "ds_read_b128 v[80:83], v17 offset:0\n" \
  "ds_read_b128 v[84:87], v17 offset:1024\n" \
  "ds_read_b128 v[88:91], v17 offset:2048\n" \
  "ds_read_b128 v[92:95], v17 offset:3072\n" \
  "ds_read_b128 v[96:99], v18 offset:0\n" \
  "ds_read_b128 v[100:103], v18 offset:1024\n" \
  "ds_read_b128 v[104:107], v18 offset:2048\n" \
  "ds_read_b128 v[108:111], v18 offset:3072\n" \
  "v_readfirstlane_b32 s73, v37\n" \
  "s_sub_u32 s63, s38, s69\n" \
  "s_bfe_u32 s64, s73, 0x80010\n" \
  "s_sub_u32 s64, s64, s67\n" \
  "s_or_b32 s63, s63, s64\n" \
  "s_bfe_u32 s64, s73, 0x80008\n" \
  "s_and_b32 s73, s73, 0xff\n" \
  "s_min_u32 s73, s73, s64\n" \
  "s_or_b32 s63, s63, s68\n" \
  "s_cmp_lt_i32 s63, 0\n" \
  "s_cbranch_scc1 .LCX_E4_%=\n" \
  "v_mov_b32 v35, 0\n" \
  "global_load_dwordx4 v[128:131], v5, s[48:49] offset:0 sc1\n" \
  "global_load_dwordx4 v[132:135], v5, s[48:49] offset:1024 sc1\n" \
  "global_load_dwordx4 v[136:139], v3, s[48:49] offset:0 sc1\n" \
  "global_load_dwordx4 v[140:143], v3, s[48:49] offset:512 sc1\n" \
  "global_load_dword v35, v35, s[50:51] sc1\n" \
  "v_mov_b32 v33, s67\n" \
  "ds_write_b32 v38, v33 offset:8\n" \
  "s_mov_b32 s68, -1\n" \
  ".LCX_E4_%=:\n" \
  "v_mfma_f32_16x16x32_bf16 v[112:115], v[196:199], v[164:167], v[112:115]\n" \
  "v_mfma_f32_16x16x32_bf16 v[116:119], v[228:231], v[164:167], v[116:119]\n" \
  "v_mfma_f32_16x16x32_bf16 v[112:115], v[200:203], v[168:171], v[112:115]\n" \
  "v_mfma_f32_16x16x32_bf16 v[116:119], v[232:235], v[168:171], v[116:119]\n" \
  "v_mfma_f32_16x16x32_bf16 v[112:115], v[204:207], v[172:175], v[112:115]\n" \
  "v_mfma_f32_16x16x32_bf16 v[116:119], v[236:239], v[172:175], v[116:119]\n" \
  "s_waitcnt lgkmcnt(11)\n" \
  "v_mfma_f32_16x16x32_bf16 v[120:123], v[192:195], v[64:67], 0\n" \
  "v_mfma_f32_16x16x32_bf16 v[124:127], v[224:227], v[64:67], 0\n" \
  "s_waitcnt lgkmcnt(10)\n" \
  "v_mfma_f32_16x16x32_bf16 v[120:123], v[196:199], v[68:71], v[120:123]\n" \
  "v_mfma_f32_16x16x32_bf16 v[124:127], v[228:231], v[68:71], v[124:127]\n" \
  "s_waitcnt lgkmcnt(9)\n" \
  "v_mfma_f32_16x16x32_bf16 v[120:123], v[200:203], v[72:75], v[120:123]\n" \
  "v_mfma_f32_16x16x32_bf16 v[124:127], v[232:235], v[72:75], v[124:127]\n" \
  "s_waitcnt lgkmcnt(8)\n" \
  "v_mfma_f32_16x16x32_bf16 v[120:123], v[204:207], v[76:79], v[120:123]\n" \
  "v_mfma_f32_16x16x32_bf16 v[124:127], v[236:239], v[76:79], v[124:127]\n" \
  "s_waitcnt lgkmcnt(7)\n" \
  "v_mfma_f32_16x16x32_bf16 v[112:115], v[208:211], v[80:83], v[112:115]\n" \
  "v_mfma_f32_16x16x32_bf16 v[116:119], v[240:243], v[80:83], v[116:119]\n" \
  "s_waitcnt lgkmcnt(6)\n" \
  "v_mfma_f32_16x16x32_bf16 v[112:115], v[212:215], v[84:87], v[112:115]\n" \
  "v_mfma_f32_16x16x32_bf16 v[116:119], v[244:247], v[84:87], v[116:119]\n" \
  "s_waitcnt lgkmcnt(5)\n" \
  "v_mfma_f32_16x16x32_bf16 v[112:115], v[216:219], v[88:91], v[112:115]\n" \
  "v_mfma_f32_16x16x32_bf16 v[116:119], v[248:251], v[88:91], v[116:119]\n" \
  "s_waitcnt lgkmcnt(4)\n" \
  "v_mfma_f32_16x16x32_bf16 v[112:115], v[220:223], v[92:95], v[112:115]\n" \
  "v_mfma_f32_16x16x32_bf16 v[116:119], v[252:255], v[92:95], v[116:119]\n" \
  "s_memrealtime s[38:39]\n" \
  "ds_read_b32 v37, v38\n" \
  "s_waitcnt lgkmcnt(4)\n" \
  "v_mfma_f32_16x16x32_bf16 v[120:123], v[208:211], v[96:99], v[120:123]\n" \
  "v_mfma_f32_16x16x32_bf16 v[124:127], v[240:243], v[96:99], v[124:127]\n" \
  "s_waitcnt lgkmcnt(3)\n" \
  "v_mfma_f32_16x16x32_bf16 v[120:123], v[212:215], v[100:103], v[120:123]\n" \
  "v_mfma_f32_16x16x32_bf16 v[124:127], v[244:247], v[100:103], v[124:127]\n" \
  "v_cvt_pk_bf16_f32 v20, v112, v113\n" \
  "v_cvt_pk_bf16_f32 v21, v114, v115\n" \
  "v_cvt_pk_bf16_f32 v22, v116, v117\n" \
  "v_cvt_pk_bf16_f32 v23, v118, v119\n" \
  "s_waitcnt lgkmcnt(2)\n" \
  "v_mfma_f32_16x16x32_bf16 v[120:123], v[216:219], v[104:107], v[120:123]\n" \
  "v_mfma_f32_16x16x32_bf16 v[124:127], v[248:251], v[104:107], v[124:127]\n" \
  "buffer_store_dwordx4 v[20:23], v4, s[40:43], s54 offen sc1\n" \
  "s_waitcnt lgkmcnt(0)\n" \
  "v_mfma_f32_16x16x32_bf16 v[120:123], v[220:223], v[108:111], v[120:123]\n" \
  "v_mfma_f32_16x16x32_bf16 v[124:127], v[252:255], v[108:111], v[124:127]\n" \
  "s_xor_b32 s64, s54, 16384\n" \
  "s_mov_b32 s61, 1\n" \
  "s_nop 3\n" \
  "v_cvt_pk_bf16_f32 v24, v120, v121\n" \
  "v_cvt_pk_bf16_f32 v25, v122, v123\n" \
  "v_cvt_pk_bf16_f32 v26, v124, v125\n" \
  "v_cvt_pk_bf16_f32 v27, v126, v127\n" \
  "buffer_store_dwordx4 v[24:27], v4, s[40:43], s64 offen sc1\n" \
  "v_readfirstlane_b32 s73, v37\n" \
  "s_sub_u32 s63, s38, s69\n" \
  "s_bfe_u32 s64, s73, 0x80010\n" \
  "s_sub_u32 s64, s64, s67\n" \
  "s_or_b32 s63, s63, s64\n" \
  "s_bfe_u32 s64, s73, 0x80008\n" \
  "s_and_b32 s73, s73, 0xff\n" \
  "s_min_u32 s73, s73, s64\n" \
  "s_or_b32 s63, s63, s68\n" \
  "s_cmp_lt_i32 s63, 0\n" \
  "s_cbranch_scc1 .LCX_E5_%=\n" \
  "v_mov_b32 v35, 0\n" \
  "global_load_dwordx4 v[128:131], v5, s[48:49] offset:0 sc1\n" \
  "global_load_dwordx4 v[132:135], v5, s[48:49] offset:1024 sc1\n" \
  "global_load_dwordx4 v[136:139], v3, s[48:49] offset:0 sc1\n" \
  "global_load_dwordx4 v[140:143], v3, s[48:49] offset:512 sc1\n" \
  "global_load_dword v35, v35, s[50:51] sc1\n" \
  "v_mov_b32 v33, s67\n" \
  "ds_write_b32 v38, v33 offset:8\n" \
  "s_mov_b32 s68, -1\n" \
  ".LCX_E5_%=:\n" \
  "s_add_u32 s54, s54, 0x80000\n" \
  "s_sub_u32 s60, s60, 1\n" \
  "s_cmp_lg_u32 s60, 0\n" \
  "s_cbranch_scc1 .LCX_TOP_%=\n" \
  "s_waitcnt vmcnt(0)\n" \
  "v_mov_b32 v144, s59\n" \
  "v_mov_b32 v145, 1\n" \
  "s_mov_b64 exec, 1\n" \
  "ds_add_rtn_u32 v144, v144, v145\n" \
  "s_mov_b64 exec, -1\n" \
  "s_xor_b32 s59, s59, 4\n" \
  "s_mov_b32 s61, 0\n" \
  "s_waitcnt lgkmcnt(0)\n" \
  "v_readfirstlane_b32 s63, v144\n" \
  "v_lshlrev_b32 v144, 5, v5\n" \
  "s_and_b32 s63, s63, 7\n" \
  "s_cmp_eq_u32 s63, 7\n" \
  "s_cbranch_scc0 .LCX_W6_%=\n" \
  "s_mov_b64 exec, 15\n" \
  "global_store_dword v144, v145, s[52:53] sc1\n" \
  "s_mov_b64 exec, -1\n" \
  ".LCX_W6_%=:\n" \
  "s_add_u32 s52, s52, 2048\n" \
  "s_addc_u32 s53, s53, 0\n" \
  ".LCX_END_%=:\n" \
  "s_endpgm\n" \
  ".LCX_FAIL_%=:\n" \
  "v_mov_b64 v[28:29], 0\n" \
  "v_mov_b64 v[30:31], 0\n" \
  "s_cmp_eq_u32 s61, 0\n" \
  "s_cbranch_scc1 .LCX_FAIL2_%=\n" \
  "s_mov_b32 s61, 0\n" \
  "s_waitcnt lgkmcnt(0)\n" \
  "v_readfirstlane_b32 s63, v144\n" \
  "v_lshlrev_b32 v144, 5, v5\n" \
  "s_and_b32 s63, s63, 7\n" \
  "s_cmp_eq_u32 s63, 7\n" \
  "s_cbranch_scc0 .LCX_W7_%=\n" \
  "s_mov_b64 exec, 15\n" \
  "global_store_dword v144, v145, s[52:53] sc1\n" \
  "s_mov_b64 exec, -1\n" \
  ".LCX_W7_%=:\n" \
  "s_add_u32 s52, s52, 2048\n" \
  "s_addc_u32 s53, s53, 0\n" \
  ".LCX_FAIL2_%=:\n" \
  "s_add_u32 s69, s64, -48\n" \
  "s_cmp_lg_u32 s64, 0\n" \
  "s_cbranch_scc1 .LCX_WF_%=\n" \
  "s_sleep 4\n" \
  "global_load_dword v35, v35, s[50:51] sc1\n" \
  "s_sub_u32 s62, s62, 8\n" \
  "s_cbranch_scc1 .LCX_END_%=\n" \
  "s_waitcnt vmcnt(0)\n" \
  "v_readfirstlane_b32 s64, v35\n" \
  "s_branch .LCX_FAIL2_%=\n"
#define AW_ASM_Y \
  "s_mov_b32 s72, 0\n" \
  "s_mov_b32 s67, 0\n" \
  "s_mov_b32 s68, 2\n" \
  "s_mov_b32 s69, 0\n" \
  "s_mov_b32 s65, 49152\n" \
  "s_mov_b32 s66, 16384\n" \
  "v_mov_b32 v5, s58\n" \
  "v_mov_b32 v60, v3\n" \
  "v_add_u32 v3, 4096, v2\n" \
  "s_mov_b32 s62, 0x40000\n" \
  "s_branch .LCY_ASK_%=\n" \
  ".LCY_BAR_%=:\n" \
  "s_barrier\n" \
  "ds_read_b128 v[8:11], v0 offset:0\n" \
  "ds_read_b128 v[12:15], v0 offset:1024\n" \
  "ds_read_b128 v[16:19], v0 offset:2048\n" \
  "s_add_u32 s68, s68, 1\n" \
  "s_add_u32 s64, s54, 16384\n" \
  "s_cmp_eq_u32 s69, 0\n" \
  "s_cbranch_scc1 .LCY_NI_%=\n" \
  "s_waitcnt vmcnt(3)\n" \
  "s_cmp_lg_u32 s72, 0\n" \
  "s_cbranch_scc1 .LCY_NJ_%=\n" \
  "s_waitcnt vmcnt(0)\n" \
  ".LCY_NJ_%=:\n" \
  "v_mov_b32 v6, s55\n" \
  "s_mov_b64 exec, 1\n" \
  "ds_add_u32 v5, v6\n" \
  "s_mov_b64 exec, -1\n" \
  "s_mov_b32 s69, 0\n" \
  ".LCY_NI_%=:\n" \
  "v_cmp_eq_u32 vcc, 0, v4\n" \
  "s_cmp_eq_u64 vcc, 0\n" \
  "s_cbranch_scc0 .LCY_NS_%=\n" \
  "v_lshlrev_b32 v6, 16, s55\n" \
  "s_mov_b64 exec, 1\n" \
  "ds_add_u32 v5, v6\n" \
  "s_mov_b64 exec, -1\n" \
  "s_mov_b32 m0, s56\n" \
  "s_add_u32 s71, s56, 4096\n" \
  "global_load_lds_dwordx4 v2, s[48:49] offset:0 sc1\n" \
  "global_load_lds_dwordx4 v2, s[48:49] offset:1024 sc1\n" \
  "global_load_lds_dwordx4 v2, s[48:49] offset:2048 sc1\n" \
  "global_load_lds_dwordx4 v2, s[48:49] offset:3072 sc1\n" \
  "s_mov_b32 m0, s71\n" \
  "s_add_u32 s56, s56, s65\n" \
  "global_load_lds_dwordx4 v3, s[48:49] offset:0 sc1\n" \
  "global_load_lds_dwordx4 v3, s[48:49] offset:1024 sc1\n" \
  ".LCY_NS_%=:\n" \
  "s_cmp_eq_u32 s72, 0\n" \
  "s_cbranch_scc1 .LCY_NK_%=\n" \
  "s_waitcnt vmcnt(6)\n" \
  "s_mov_b32 s72, 0\n" \
  "s_cbranch_vccz .LCY_NL_%=\n" \
  "s_waitcnt vmcnt(0)\n" \
  ".LCY_NL_%=:\n" \
  "v_mov_b32 v6, s59\n" \
  "v_mov_b32 v7, 1\n" \
  "s_mov_b64 exec, 1\n" \
  "ds_add_rtn_u32 v6, v6, v7\n" \
  "s_mov_b64 exec, -1\n" \
  "s_xor_b32 s59, s59, 4\n" \
  "s_mov_b32 s61, 0\n" \
  "s_waitcnt lgkmcnt(0)\n" \
  "v_readfirstlane_b32 s63, v6\n" \
  "v_lshlrev_b32 v6, 5, v2\n" \
  "s_and_b32 s63, s63, 7\n" \
  "s_cmp_eq_u32 s63, 7\n" \
  "s_cbranch_scc0 .LCY_W1_%=\n" \
  "s_mov_b64 exec, 15\n" \
  "global_store_dword v6, v7, s[52:53] sc1\n" \
  "s_mov_b64 exec, -1\n" \
  ".LCY_W1_%=:\n" \
  "s_add_u32 s52, s52, 2048\n" \
  "s_addc_u32 s53, s53, 0\n" \
  ".LCY_NK_%=:\n" \
  "ds_read_b128 v[20:23], v0 offset:3072\n" \
  "s_waitcnt lgkmcnt(3)\n" \
  "v_mfma_f32_16x16x32_bf16 v[32:35], v[64:67], v[8:11], 0\n" \
  "v_mfma_f32_16x16x32_bf16 v[36:39], v[96:99], v[8:11], 0\n" \
  "v_mfma_f32_16x16x32_bf16 v[40:43], v[128:131], v[8:11], 0\n" \
  "v_mfma_f32_16x16x32_bf16 v[44:47], v[160:163], v[8:11], 0\n" \
  "v_mfma_f32_16x16x32_bf16 v[48:51], v[192:195], v[8:11], 0\n" \
  "v_mfma_f32_16x16x32_bf16 v[52:55], v[224:227], v[8:11], 0\n" \
  "ds_read_b128 v[8:11], v0 offset:8192\n" \
  "s_waitcnt lgkmcnt(3)\n" \
  "v_mfma_f32_16x16x32_bf16 v[32:35], v[68:71], v[12:15], v[32:35]\n" \
  "v_mfma_f32_16x16x32_bf16 v[36:39], v[100:103], v[12:15], v[36:39]\n" \
  "v_mfma_f32_16x16x32_bf16 v[40:43], v[132:135], v[12:15], v[40:43]\n" \
  "v_mfma_f32_16x16x32_bf16 v[44:47], v[164:167], v[12:15], v[44:47]\n" \
  "v_mfma_f32_16x16x32_bf16 v[48:51], v[196:199], v[12:15], v[48:51]\n" \
  "v_mfma_f32_16x16x32_bf16 v[52:55], v[228:231], v[12:15], v[52:55]\n" \
  "s_cbranch_vccnz .LCY_D2_%=\n" \
  "global_load_lds_dwordx4 v3, s[48:49] offset:2048 sc1\n" \
  ".LCY_D2_%=:\n" \
  "ds_read_b128 v[12:15], v0 offset:9216\n" \
  "s_waitcnt lgkmcnt(3)\n" \
  "v_mfma_f32_16x16x32_bf16 v[32:35], v[72:75], v[16:19], v[32:35]\n" \
  "v_mfma_f32_16x16x32_bf16 v[36:39], v[104:107], v[16:19], v[36:39]\n" \
  "v_mfma_f32_16x16x32_bf16 v[40:43], v[136:139], v[16:19], v[40:43]\n" \
  "v_mfma_f32_16x16x32_bf16 v[44:47], v[168:171], v[16:19], v[44:47]\n" \
  "v_mfma_f32_16x16x32_bf16 v[48:51], v[200:203], v[16:19], v[48:51]\n" \
  "v_mfma_f32_16x16x32_bf16 v[52:55], v[232:235], v[16:19], v[52:55]\n" \
  "s_cbranch_vccnz .LCY_D3_%=\n" \
  "global_load_lds_dwordx4 v3, s[48:49] offset:3072 sc1\n" \
  "s_add_u32 s48, s48, 0x80000\n" \
  "s_addc_u32 s49, s49, 0\n" \
  ".LCY_D3_%=:\n" \
  "ds_read_b128 v[16:19], v0 offset:10240\n" \
  "s_waitcnt lgkmcnt(3)\n" \
  "v_mfma_f32_16x16x32_bf16 v[32:35], v[76:79], v[20:23], v[32:35]\n" \
  "v_mfma_f32_16x16x32_bf16 v[36:39], v[108:111], v[20:23], v[36:39]\n" \
  "v_mfma_f32_16x16x32_bf16 v[40:43], v[140:143], v[20:23], v[40:43]\n" \
  "v_mfma_f32_16x16x32_bf16 v[44:47], v[172:175], v[20:23], v[44:47]\n" \
  "v_mfma_f32_16x16x32_bf16 v[48:51], v[204:207], v[20:23], v[48:51]\n" \
  "v_mfma_f32_16x16x32_bf16 v[52:55], v[236:239], v[20:23], v[52:55]\n" \
  "s_cbranch_vccnz .LCY_D4_%=\n" \
  "s_mov_b32 m0, s57\n" \
  "s_add_u32 s57, s57, s65\n" \
  "global_load_lds_dwordx4 v2, s[50:51] offset:0 sc1\n" \
  ".LCY_D4_%=:\n" \
  "ds_read_b128 v[20:23], v0 offset:11264\n" \
  "s_waitcnt lgkmcnt(3)\n" \
  "v_mfma_f32_16x16x32_bf16 v[32:35], v[80:83], v[8:11], v[32:35]\n" \
  "v_mfma_f32_16x16x32_bf16 v[36:39], v[112:115], v[8:11], v[36:39]\n" \
  "v_mfma_f32_16x16x32_bf16 v[40:43], v[144:147], v[8:11], v[40:43]\n" \
  "v_mfma_f32_16x16x32_bf16 v[44:47], v[176:179], v[8:11], v[44:47]\n" \
  "v_mfma_f32_16x16x32_bf16 v[48:51], v[208:211], v[8:11], v[48:51]\n" \
  "v_mfma_f32_16x16x32_bf16 v[52:55], v[240:243], v[8:11], v[52:55]\n" \
  "s_cbranch_vccnz .LCY_D5_%=\n" \
  "global_load_lds_dwordx4 v2, s[50:51] offset:1024 sc1\n" \
  "s_sub_u32 s65, 0, s65\n" \
  "s_add_u32 s67, s67, 4096\n" \
  ".LCY_D5_%=:\n" \
  "ds_read_b128 v[8:11], v0 offset:4096\n" \
  "s_waitcnt lgkmcnt(3)\n" \
  "v_mfma_f32_16x16x32_bf16 v[32:35], v[84:87], v[12:15], v[32:35]\n" \
  "v_mfma_f32_16x16x32_bf16 v[36:39], v[116:119], v[12:15], v[36:39]\n" \
  "v_mfma_f32_16x16x32_bf16 v[40:43], v[148:151], v[12:15], v[40:43]\n" \
  "v_mfma_f32_16x16x32_bf16 v[44:47], v[180:183], v[12:15], v[44:47]\n" \
  "v_mfma_f32_16x16x32_bf16 v[48:51], v[212:215], v[12:15], v[48:51]\n" \
  "v_mfma_f32_16x16x32_bf16 v[52:55], v[244:247], v[12:15], v[52:55]\n" \
  "s_cbranch_vccnz .LCY_D6_%=\n" \
  "global_load_lds_dwordx4 v2, s[50:51] offset:2048 sc1\n" \
  "s_sub_u32 s68, s68, 1\n" \
  "s_mov_b32 s69, 1\n" \
  ".LCY_D6_%=:\n" \
  "ds_read_b128 v[12:15], v0 offset:5120\n" \
  "s_waitcnt lgkmcnt(3)\n" \
  "v_mfma_f32_16x16x32_bf16 v[32:35], v[88:91], v[16:19], v[32:35]\n" \
  "v_mfma_f32_16x16x32_bf16 v[36:39], v[120:123], v[16:19], v[36:39]\n" \
  "v_mfma_f32_16x16x32_bf16 v[40:43], v[152:155], v[16:19], v[40:43]\n" \
  "v_mfma_f32_16x16x32_bf16 v[44:47], v[184:187], v[16:19], v[44:47]\n" \
  "v_mfma_f32_16x16x32_bf16 v[48:51], v[216:219], v[16:19], v[48:51]\n" \
  "v_mfma_f32_16x16x32_bf16 v[52:55], v[248:251], v[16:19], v[52:55]\n" \
  "s_cbranch_vccnz .LCY_D7_%=\n" \
  "global_load_lds_dwordx4 v2, s[50:51] offset:3072 sc1\n" \
  "s_add_u32 s50, s50, 0x40000\n" \
  "s_addc_u32 s51, s51, 0\n" \
  ".LCY_D7_%=:\n" \
  "ds_read_b128 v[16:19], v0 offset:6144\n" \
  "s_waitcnt lgkmcnt(3)\n" \
  "v_mfma_f32_16x16x32_bf16 v[32:35], v[92:95], v[20:23], v[32:35]\n" \
  "v_mfma_f32_16x16x32_bf16 v[36:39], v[124:127], v[20:23], v[36:39]\n" \
  "v_mfma_f32_16x16x32_bf16 v[40:43], v[156:159], v[20:23], v[40:43]\n" \
  "v_mfma_f32_16x16x32_bf16 v[44:47], v[188:191], v[20:23], v[44:47]\n" \
  "v_mfma_f32_16x16x32_bf16 v[48:51], v[220:223], v[20:23], v[48:51]\n" \
  "v_mfma_f32_16x16x32_bf16 v[52:55], v[252:255], v[20:23], v[52:55]\n" \
  "ds_read_b128 v[20:23], v0 offset:7168\n" \
  "s_waitcnt lgkmcnt(3)\n" \
  "s_nop 1\n" \
  "v_cvt_pk_bf16_f32 v24, v32, v33\n" \
  "v_cvt_pk_bf16_f32 v25, v34, v35\n" \
  "v_cvt_pk_bf16_f32 v26, v36, v37\n" \
  "v_cvt_pk_bf16_f32 v27, v38, v39\n" \
  "s_nop 0\n" \
  "v_mfma_f32_16x16x32_bf16 v[32:35], v[64:67], v[8:11], 0\n" \
  "v_mfma_f32_16x16x32_bf16 v[36:39], v[96:99], v[8:11], 0\n" \
  "buffer_store_dwordx4 v[24:27], v1, s[40:43], s54 offen offset:0 sc1\n" \
  "v_cvt_pk_bf16_f32 v28, v40, v41\n" \
  "v_cvt_pk_bf16_f32 v29, v42, v43\n" \
  "v_cvt_pk_bf16_f32 v30, v44, v45\n" \
  "v_cvt_pk_bf16_f32 v31, v46, v47\n" \
  "s_nop 0\n" \
  "v_mfma_f32_16x16x32_bf16 v[40:43], v[128:131], v[8:11], 0\n" \
  "v_mfma_f32_16x16x32_bf16 v[44:47], v[160:163], v[8:11], 0\n" \
  "buffer_store_dwordx4 v[28:31], v1, s[40:43], s54 offen offset:128 sc1\n" \
  "v_cvt_pk_bf16_f32 v56, v48, v49\n" \
  "v_cvt_pk_bf16_f32 v57, v50, v51\n" \
  "v_cvt_pk_bf16_f32 v58, v52, v53\n" \
  "v_cvt_pk_bf16_f32 v59, v54, v55\n" \
  "s_nop 0\n" \
  "v_mfma_f32_16x16x32_bf16 v[48:51], v[192:195], v[8:11], 0\n" \
  "v_mfma_f32_16x16x32_bf16 v[52:55], v[224:227], v[8:11], 0\n" \
  "buffer_store_dwordx4 v[56:59], v1, s[40:43], s54 offen offset:256 sc1\n" \
  "ds_read_b128 v[8:11], v0 offset:12288\n" \
  "s_waitcnt lgkmcnt(3)\n" \
  "v_mfma_f32_16x16x32_bf16 v[32:35], v[68:71], v[12:15], v[32:35]\n" \
  "v_mfma_f32_16x16x32_bf16 v[36:39], v[100:103], v[12:15], v[36:39]\n" \
  "v_mfma_f32_16x16x32_bf16 v[40:43], v[132:135], v[12:15], v[40:43]\n" \
  "v_mfma_f32_16x16x32_bf16 v[44:47], v[164:167], v[12:15], v[44:47]\n" \
  "v_mfma_f32_16x16x32_bf16 v[48:51], v[196:199], v[12:15], v[48:51]\n" \
  "v_mfma_f32_16x16x32_bf16 v[52:55], v[228:231], v[12:15], v[52:55]\n" \
  "ds_read_b128 v[12:15], v0 offset:13312\n" \
  "s_waitcnt lgkmcnt(3)\n" \
  "v_mfma_f32_16x16x32_bf16 v[32:35], v[72:75], v[16:19], v[32:35]\n" \
  "v_mfma_f32_16x16x32_bf16 v[36:39], v[104:107], v[16:19], v[36:39]\n" \
  "v_mfma_f32_16x16x32_bf16 v[40:43], v[136:139], v[16:19], v[40:43]\n" \
  "v_mfma_f32_16x16x32_bf16 v[44:47], v[168:171], v[16:19], v[44:47]\n" \
  "v_mfma_f32_16x16x32_bf16 v[48:51], v[200:203], v[16:19], v[48:51]\n" \
  "v_mfma_f32_16x16x32_bf16 v[52:55], v[232:235], v[16:19], v[52:55]\n" \
  "ds_read_b128 v[16:19], v0 offset:14336\n" \
  "s_waitcnt lgkmcnt(3)\n" \
  "v_mfma_f32_16x16x32_bf16 v[32:35], v[76:79], v[20:23], v[32:35]\n" \
  "v_mfma_f32_16x16x32_bf16 v[36:39], v[108:111], v[20:23], v[36:39]\n" \
  "v_mfma_f32_16x16x32_bf16 v[40:43], v[140:143], v[20:23], v[40:43]\n" \
  "v_mfma_f32_16x16x32_bf16 v[44:47], v[172:175], v[20:23], v[44:47]\n" \
  "v_mfma_f32_16x16x32_bf16 v[48:51], v[204:207], v[20:23], v[48:51]\n" \
  "v_mfma_f32_16x16x32_bf16 v[52:55], v[236:239], v[20:23], v[52:55]\n" \
  "ds_read_b32 v7, v5 offset:8\n" \
  "ds_read_b128 v[20:23], v0 offset:15360\n" \
  "s_waitcnt lgkmcnt(4)\n" \
  "v_mfma_f32_16x16x32_bf16 v[32:35], v[80:83], v[8:11], v[32:35]\n" \
  "v_mfma_f32_16x16x32_bf16 v[36:39], v[112:115], v[8:11], v[36:39]\n" \
  "v_mfma_f32_16x16x32_bf16 v[40:43], v[144:147], v[8:11], v[40:43]\n" \
  "v_mfma_f32_16x16x32_bf16 v[44:47], v[176:179], v[8:11], v[44:47]\n" \
  "v_mfma_f32_16x16x32_bf16 v[48:51], v[208:211], v[8:11], v[48:51]\n" \
  "v_mfma_f32_16x16x32_bf16 v[52:55], v[240:243], v[8:11], v[52:55]\n" \
  "s_waitcnt lgkmcnt(3)\n" \
  "v_mfma_f32_16x16x32_bf16 v[32:35], v[84:87], v[12:15], v[32:35]\n" \
  "v_mfma_f32_16x16x32_bf16 v[36:39], v[116:119], v[12:15], v[36:39]\n" \
  "v_mfma_f32_16x16x32_bf16 v[40:43], v[148:151], v[12:15], v[40:43]\n" \
  "v_mfma_f32_16x16x32_bf16 v[44:47], v[180:183], v[12:15], v[44:47]\n" \
  "v_mfma_f32_16x16x32_bf16 v[48:51], v[212:215], v[12:15], v[48:51]\n" \
  "v_mfma_f32_16x16x32_bf16 v[52:55], v[244:247], v[12:15], v[52:55]\n" \
  "s_waitcnt lgkmcnt(2)\n" \
  "v_mfma_f32_16x16x32_bf16 v[32:35], v[88:91], v[16:19], v[32:35]\n" \
  "v_mfma_f32_16x16x32_bf16 v[36:39], v[120:123], v[16:19], v[36:39]\n" \
  "v_mfma_f32_16x16x32_bf16 v[40:43], v[152:155], v[16:19], v[40:43]\n" \
  "v_mfma_f32_16x16x32_bf16 v[44:47], v[184:187], v[16:19], v[44:47]\n" \
  "v_mfma_f32_16x16x32_bf16 v[48:51], v[216:219], v[16:19], v[48:51]\n" \
  "v_mfma_f32_16x16x32_bf16 v[52:55], v[248:251], v[16:19], v[52:55]\n" \
  "s_cbranch_vccnz .LCY_NT_%=\n" \
  "v_mov_b32 v4, 0\n" \
  "buffer_load_dword v4, v60, s[44:47], s67 offen sc1\n" \
  ".LCY_NT_%=:\n" \
  "s_waitcnt lgkmcnt(0)\n" \
  "v_mfma_f32_16x16x32_bf16 v[32:35], v[92:95], v[20:23], v[32:35]\n" \
  "v_mfma_f32_16x16x32_bf16 v[36:39], v[124:127], v[20:23], v[36:39]\n" \
  "v_mfma_f32_16x16x32_bf16 v[40:43], v[156:159], v[20:23], v[40:43]\n" \
  "v_mfma_f32_16x16x32_bf16 v[44:47], v[188:191], v[20:23], v[44:47]\n" \
  "v_mfma_f32_16x16x32_bf16 v[48:51], v[220:223], v[20:23], v[48:51]\n" \
  "v_mfma_f32_16x16x32_bf16 v[52:55], v[252:255], v[20:23], v[52:55]\n" \
  "v_add_u32 v0, s66, v0\n" \
  "s_sub_u32 s66, 0, s66\n" \
  "s_add_u32 s54, s54, 0x80000\n" \
  "s_mov_b32 s62, 0x40000\n" \
  "v_readfirstlane_b32 s63, v7\n" \
  "v_cvt_pk_bf16_f32 v24, v32, v33\n" \
  "v_cvt_pk_bf16_f32 v25, v34, v35\n" \
  "v_cvt_pk_bf16_f32 v26, v36, v37\n" \
  "v_cvt_pk_bf16_f32 v27, v38, v39\n" \
  "buffer_store_dwordx4 v[24:27], v1, s[40:43], s64 offen offset:0 sc1\n" \
  "v_cvt_pk_bf16_f32 v28, v40, v41\n" \
  "v_cvt_pk_bf16_f32 v29, v42, v43\n" \
  "v_cvt_pk_bf16_f32 v30, v44, v45\n" \
  "v_cvt_pk_bf16_f32 v31, v46, v47\n" \
  "buffer_store_dwordx4 v[28:31], v1, s[40:43], s64 offen offset:128 sc1\n" \
  "v_cvt_pk_bf16_f32 v56, v48, v49\n" \
  "v_cvt_pk_bf16_f32 v57, v50, v51\n" \
  "v_cvt_pk_bf16_f32 v58, v52, v53\n" \
  "v_cvt_pk_bf16_f32 v59, v54, v55\n" \
  "buffer_store_dwordx4 v[56:59], v1, s[40:43], s64 offen offset:256 sc1\n" \
  "s_cbranch_vccnz .LCY_ND_%=\n" \
  "s_add_u32 s63, s63, s60\n" \
  "s_cmp_eq_u32 s63, 66\n" \
  "s_cselect_b32 s72, 1, 0\n" \
  "s_cbranch_scc1 .LCY_DEF_%=\n" \
  "s_waitcnt vmcnt(0)\n" \
  "v_mov_b32 v6, s55\n" \
  "s_mov_b64 exec, 1\n" \
  "ds_add_u32 v5, v6\n" \
  "s_mov_b64 exec, -1\n" \
  "s_mov_b32 s69, 0\n" \
  "s_branch .LCY_WC_%=\n" \
  ".LCY_ND_%=:\n" \
  "v_mov_b32 v4, 0\n" \
  "buffer_load_dword v4, v60, s[44:47], s67 offen sc1\n" \
  "s_waitcnt vmcnt(0)\n" \
  ".LCY_WC_%=:\n" \
  "v_mov_b32 v6, s59\n" \
  "v_mov_b32 v7, 1\n" \
  "s_mov_b64 exec, 1\n" \
  "ds_add_rtn_u32 v6, v6, v7\n" \
  "s_mov_b64 exec, -1\n" \
  "s_xor_b32 s59, s59, 4\n" \
  "s_mov_b32 s61, 0\n" \
  "s_waitcnt lgkmcnt(0)\n" \
  "v_readfirstlane_b32 s63, v6\n" \
  "v_lshlrev_b32 v6, 5, v2\n" \
  "s_and_b32 s63, s63, 7\n" \
  "s_cmp_eq_u32 s63, 7\n" \
  "s_cbranch_scc0 .LCY_W8_%=\n" \
  "s_mov_b64 exec, 15\n" \
  "global_store_dword v6, v7, s[52:53] sc1\n" \
  "s_mov_b64 exec, -1\n" \
  ".LCY_W8_%=:\n" \
  "s_add_u32 s52, s52, 2048\n" \
  "s_addc_u32 s53, s53, 0\n" \
  ".LCY_DEF_%=:\n" \
  "s_sub_u32 s60, s60, 1\n" \
  "s_cmp_eq_u32 s60, 0\n" \
  "s_cbranch_scc1 .LCY_END_%=\n" \
  ".LCY_NEED_%=:\n" \
  "s_cmp_eq_u32 s68, 0\n" \
  "s_cbranch_scc1 .LCY_BAR_%=\n" \
  "v_cmp_eq_u32 vcc, 0, v4\n" \
  "s_cmp_eq_u64 vcc, 0\n" \
  "s_cbranch_scc1 .LCY_GET_%=\n" \
  "s_cmp_ge_u32 s67, 0x40000\n" \
  "s_cbranch_scc1 .LCY_BAR_%=\n" \
  "s_sleep 4\n" \
  "ds_read_b32 v6, v5 offset:16\n" \
  "s_waitcnt lgkmcnt(0)\n" \
  "v_readfirstlane_b32 s63, v6\n" \
  "s_add_u32 s63, s63, s60\n" \
  "s_cmp_ge_u32 s63, 66\n" \
  "s_cbranch_scc1 .LCY_BAR_%=\n" \
  ".LCY_ASK_%=:\n" \
  "v_mov_b32 v4, 0\n" \
  "buffer_load_dword v4, v60, s[44:47], s67 offen sc1\n" \
  "s_sub_u32 s62, s62, 1\n" \
  "s_cbranch_scc1 .LCY_END_%=\n" \
  "s_waitcnt vmcnt(0)\n" \
  "s_branch .LCY_NEED_%=\n" \
  ".LCY_GET_%=:\n" \
  "v_lshlrev_b32 v6, 16, s55\n" \
  "s_mov_b64 exec, 1\n" \
  "ds_add_u32 v5, v6\n" \
  "s_mov_b64 exec, -1\n" \
  "s_mov_b32 m0, s56\n" \
  "s_add_u32 s71, s56, 4096\n" \
  "global_load_lds_dwordx4 v2, s[48:49] offset:0 sc1\n" \
  "global_load_lds_dwordx4 v2, s[48:49] offset:1024 sc1\n" \
  "global_load_lds_dwordx4 v2, s[48:49] offset:2048 sc1\n" \
  "global_load_lds_dwordx4 v2, s[48:49] offset:3072 sc1\n" \
  "s_mov_b32 m0, s71\n" \
  "s_add_u32 s56, s56, s65\n" \
  "global_load_lds_dwordx4 v3, s[48:49] offset:0 sc1\n" \
  "global_load_lds_dwordx4 v3, s[48:49] offset:1024 sc1\n" \
  "global_load_lds_dwordx4 v3, s[48:49] offset:2048 sc1\n" \
  "global_load_lds_dwordx4 v3, s[48:49] offset:3072 sc1\n" \
  "s_add_u32 s48, s48, 0x80000\n" \
  "s_addc_u32 s49, s49, 0\n" \
  "s_mov_b32 m0, s57\n" \
  "s_add_u32 s57, s57, s65\n" \
  "global_load_lds_dwordx4 v2, s[50:51] offset:0 sc1\n" \
  "global_load_lds_dwordx4 v2, s[50:51] offset:1024 sc1\n" \
  "s_sub_u32 s65, 0, s65\n" \
  "s_add_u32 s67, s67, 4096\n" \
  "global_load_lds_dwordx4 v2, s[50:51] offset:2048 sc1\n" \
  "s_sub_u32 s68, s68, 1\n" \
  "s_mov_b32 s69, 1\n" \
  "global_load_lds_dwordx4 v2, s[50:51] offset:3072 sc1\n" \
  "s_add_u32 s50, s50, 0x40000\n" \
  "s_addc_u32 s51, s51, 0\n" \
  "v_mov_b32 v4, 0\n" \
  "buffer_load_dword v4, v60, s[44:47], s67 offen sc1\n" \
  "s_mov_b32 s61, 0\n" \
  "s_cmp_eq_u32 s68, 0\n" \
  "s_cbranch_scc1 .LCY_BAR_%=\n" \
  "s_waitcnt vmcnt(0)\n" \
  "v_mov_b32 v6, s55\n" \
  "s_mov_b64 exec, 1\n" \
  "ds_add_u32 v5, v6\n" \
  "s_mov_b64 exec, -1\n" \
  "s_mov_b32 s69, 0\n" \
  "s_branch .LCY_NEED_%=\n" \
  ".LCY_END_%=:\n" \
  "s_endpgm\n"
template <bool AT>
__device__ __forceinline__ void role_aw_t(const MegaArgs& A, char* smem, const int L, const int a, const u64 tag) {
  const int w = A.wv;
  const int l = lane_id(), c = l & 15, g = l >> 4;
  const int qt = w & 1, hh = (w >> 1) & 1, h = 2 * a + hh;
  char* ws = A.ws;
  unsigned* FL = (unsigned*)(ws + OFF_FL);
  const unsigned sb = lds_base(smem);
  if (w == 0 && l < 8) reinterpret_cast<unsigned*>(smem + AW_CNT)[l] = 0u;
  constexpr int NP = AT ? 1 : 3;
  const int cp0 = AT ? w : 3 * w - 8;
  v8u wo[2 * NP][4];
  {
    const int q = (l >> 2) & 3, p = l & 3, f = q | ((g & 1) << 2);
    const unsigned ra = sb + (4 * g + q) * 256 + (p & 1) * 16;
#pragma unroll
    for (int r = 0; r < 2; ++r) {
#pragma unroll
      for (int k = 0; k < 2; ++k) {
        const char* base_ = (const char*)A.Wo + ((size_t)((2 * a + r) * DH + 64 * (w & 1) + 32 * k) * DM + 128 * (w >> 1)) * 2;
#pragma unroll
        for (int i = 0; i < 8; ++i) {
          const int fd = g | ((i & 1) << 2);
          glds16(base_, (unsigned)((4 * i + g) * (DM * 2) + ((c ^ (fd << 1)) << 4)), sb + w * 16384 + k * 8192 + i * 1024);
        }
      }
      SST(4 + 3 * r);
      if (r == 1) ready_wait(A, tag, ready_look(A));
      WAIT_VM(0);
      RAW_BAR();
      SST(5 + 3 * r);
#pragma unroll
      for (int k2 = 0; k2 < 2; ++k2) {
#pragma unroll
        for (int ct = 0; ct < 2 * NP; ++ct) {
          const int cp = cp0 + (ct >> 1);
          const unsigned a_ = ra + (cp >> 2) * 32768 + k2 * 16384 + ((2 * (cp & 3) + (p >> 1)) ^ f) * 32 + (ct & 1) * 8;
          const v2u r0 = trd(a_), r1 = trd(a_ + 4096), r2 = trd(a_ + 8192), r3 = trd(a_ + 12288);
          wo[ct][2 * r + k2] = v8u{r0.x, r0.y, r1.x, r1.y, r2.x, r2.y, r3.x, r3.y};
          asm volatile("" : "+v"(wo[ct][2 * r + k2]));
        }
      }
      RAW_BAR();
      SST(6 + 3 * r);
    }
  }
  WSTAMP2();
  const v4u rsW = mk_srd(ws + OFF_PW, SZ_PW);
  const int dp = (FL_FP - FL_FQ + (L * 16 + a - L * 64 * 32 - h) * 32 + 2 * hh) * 4;
  const v4u rsF = mk_srd(FL + FL_FQ + (L * 64 * 32 + h) * 32, (qt ? 0 : dp) + 64 * 4096 - 128);
  const char* p0 = AT ? ws + OFF_MEMK + L * SZ_MEM + h * 2048 : ws + OFF_QK + L * SZ_QK + ((size_t)h * 16 + 8 * qt) * 1024;
  const char* p1 = AT ? (const char*)(FL + FL_FK + L * 32 + (h >> 3) * 64 + (h & 7) * 2) : ws + OFF_VF + L * SZ_O + ((size_t)h * 8 + 4 * qt) * 1024;
  const unsigned* pFW = FL + FL_FW + (L * 64 * 16 + (a >> 2)) * 32 + (a & 3);
  const unsigned tk = sb + AW_TK + hh * 24 * 1024, voff = (c >> 1) * 2048 + (c & 1) * 64 + g * 16;
  const char* dbg = ws + WS_TOTAL + A.vb * 64 * 8;
#define AW_SOPS                                                                                                                 \
  "{s[40:43]}"(rsW), "{s[44:47]}"(rsF), "{s[48:49]}"(p0), "{s[50:51]}"(p1), "{s[52:53]}"(pFW),                                   \
      "{s54}"((L * 64 * 16 + a) * 32768 + cp0 * 128 + (AT ? qt * 16384 : 0)), "{s55}"(1 << (8 * qt)), "{s56}"(tk + qt * 8192), "{s57}"(tk + 16384 + qt * 4096),                  \
      "{s58}"(sb + AW_CNT + 8 + 4 * hh), "{s59}"(sb + AW_CNT), "{s60}"(NSEG), "{s[36:37]}"(dbg)                                  \
      : "memory", "vcc", "scc", "s80"
  if constexpr (AT) {
    const v8u pv = {tk + l * 16, (c & 8) ? 0xEEEEu : 0x4444u, 0u, (unsigned)(OFF_MEMV - OFF_MEMK) + c * 32 + (g & 1) * 16 + (g >> 1) * 1024,
                    voff, (unsigned)l * 16, tk + qt * 4096 + l * 16, sb + AW_SO + w * 4096 + l * 16};
    const unsigned neg = 0xff61b1e6u;
    v8u bt;
#pragma unroll
    for (int i = 0; i < 4; ++i) {
      const unsigned d = 4 * g + i > c ? neg : 0u;
      bt[i] = qt ? 0u : d;
      bt[4 + i] = qt ? d : neg;
    }
    asm volatile("s_mov_b64 exec, -1\ns_waitcnt vmcnt(0) lgkmcnt(0)\ns_nop 4\n" AW_ASM_X
                 ::"{v[192:199]}"(wo[0][0]), "{v[200:207]}"(wo[0][1]), "{v[208:215]}"(wo[0][2]), "{v[216:223]}"(wo[0][3]),
                 "{v[224:231]}"(wo[1][0]), "{v[232:239]}"(wo[1][1]), "{v[240:247]}"(wo[1][2]), "{v[248:255]}"(wo[1][3]),
                 "{v[0:7]}"(pv), "{v[8:15]}"(bt), AW_SOPS);
  } else {
    const v4u pv = {sb + AW_SO + l * 16, voff, (unsigned)l * 16, (l & 2) ? 16u + 8 * qt : qt ? 8u * (l & 1) : dp + 4 * (l & 1)};
    asm volatile("s_mov_b64 exec, -1\ns_waitcnt vmcnt(0) lgkmcnt(0)\ns_nop 4\n" AW_ASM_Y
                 ::"{v[64:71]}"(wo[0][0]), "{v[72:79]}"(wo[0][1]), "{v[80:87]}"(wo[0][2]), "{v[88:95]}"(wo[0][3]),
                 "{v[96:103]}"(wo[1][0]), "{v[104:111]}"(wo[1][1]), "{v[112:119]}"(wo[1][2]), "{v[120:127]}"(wo[1][3]),
                 "{v[128:135]}"(wo[2][0]), "{v[136:143]}"(wo[2][1]), "{v[144:151]}"(wo[2][2]), "{v[152:159]}"(wo[2][3]),
                 "{v[160:167]}"(wo[3][0]), "{v[168:175]}"(wo[3][1]), "{v[176:183]}"(wo[3][2]), "{v[184:191]}"(wo[3][3]),
                 "{v[192:199]}"(wo[4][0]), "{v[200:207]}"(wo[4][1]), "{v[208:215]}"(wo[4][2]), "{v[216:223]}"(wo[4][3]),
                 "{v[224:231]}"(wo[5][0]), "{v[232:239]}"(wo[5][1]), "{v[240:247]}"(wo[5][2]), "{v[248:255]}"(wo[5][3]),
                 "{v[0:3]}"(pv), AW_SOPS);
  }
#undef AW_SOPS
}
__device__ __forceinline__ void role_aw(const MegaArgs& A, char* smem, const int L, const int a, const u64 tag) {
  if (A.wv < 4) role_aw_t<true>(A, smem, L, a, tag);
  else role_aw_t<false>(A, smem, L, a, tag);
}

#define P_ASM \
  "s_mov_b32 s68, 0\n" \
  "s_mov_b32 s66, 3\n" \
  "s_mov_b32 s67, 0\n" \
  "s_mov_b32 s72, 0\n" \
  "v_mov_b32 v7, 1\n" \
  "v_mov_b32 v6, s70\n" \
  "v_mov_b32 v5, 0\n" \
  "v_add_u32 v10, 4096, v2\n" \
  ".LCP_TOP_%=:\n" \
  "ds_read_b128 v[28:31], v1 offset:0\n" \
  "ds_read_b128 v[32:35], v1 offset:1024\n" \
  "s_add_u32 s66, s66, 3\n" \
  "s_add_u32 s67, s67, 1\n" \
  "s_mov_b32 s69, 0x400000\n" \
  "v_xor_b32 v0, 0x8000, v0\n" \
  "v_xor_b32 v3, 0x100, v3\n" \
  "v_readfirstlane_b32 s71, v5\n" \
  "s_cmp_ge_u32 s71, s66\n" \
  "s_cbranch_scc1 .LCP_GO_%=\n" \
  "s_cmp_eq_u32 s68, 1\n" \
  "s_cbranch_scc0 .LCP_X1_%=\n" \
  "s_waitcnt vmcnt(0)\n" \
  "s_mov_b64 exec, 1\n" \
  "buffer_store_dword v7, off, s[56:59], s72 sc1\n" \
  "s_mov_b64 exec, -1\n" \
  "s_add_u32 s72, s72, 0x1000\n" \
  "s_mov_b32 s68, 0\n" \
  ".LCP_X1_%=:\n" \
  ".LCP_S2_%=:\n" \
  "s_sleep 1\n" \
  "ds_read_b32 v5, v6 offset:0\n" \
  "s_sub_u32 s69, s69, 1\n" \
  "s_cbranch_scc1 .LCP_END_%=\n" \
  "s_waitcnt lgkmcnt(0)\n" \
  "v_readfirstlane_b32 s71, v5\n" \
  "s_cmp_ge_u32 s71, s66\n" \
  "s_cbranch_scc1 .LCP_GO_%=\n" \
  "s_branch .LCP_S2_%=\n" \
  ".LCP_GO_%=:\n" \
  "ds_read_b128 v[12:15], v0 offset:0\n" \
  "ds_read_b128 v[16:19], v0 offset:2048\n" \
  "ds_read_b128 v[20:23], v0 offset:4096\n" \
  "ds_read_b128 v[24:27], v0 offset:6144\n" \
  "ds_read_b128 v[36:39], v1 offset:2048\n" \
  "s_waitcnt lgkmcnt(4)\n" \
  "v_mfma_f32_16x16x32_bf16 v[48:51], v[64:67], v[12:15], 0\n" \
  "v_mfma_f32_16x16x32_bf16 v[52:55], v[128:131], v[12:15], 0\n" \
  "v_mfma_f32_16x16x32_bf16 v[56:59], v[192:195], v[12:15], 0\n" \
  "v_mfma_f32_16x16x32_bf16 v[60:63], v[28:31], v[12:15], 0\n" \
  "ds_read_b128 v[12:15], v0 offset:8192\n" \
  "ds_read_b128 v[28:31], v1 offset:3072\n" \
  "s_waitcnt lgkmcnt(5)\n" \
  "v_mfma_f32_16x16x32_bf16 v[48:51], v[68:71], v[16:19], v[48:51]\n" \
  "v_mfma_f32_16x16x32_bf16 v[52:55], v[132:135], v[16:19], v[52:55]\n" \
  "v_mfma_f32_16x16x32_bf16 v[56:59], v[196:199], v[16:19], v[56:59]\n" \
  "v_mfma_f32_16x16x32_bf16 v[60:63], v[32:35], v[16:19], v[60:63]\n" \
  "ds_read_b128 v[16:19], v0 offset:10240\n" \
  "ds_read_b128 v[32:35], v1 offset:4096\n" \
  "s_waitcnt lgkmcnt(6)\n" \
  "v_mfma_f32_16x16x32_bf16 v[48:51], v[72:75], v[20:23], v[48:51]\n" \
  "v_mfma_f32_16x16x32_bf16 v[52:55], v[136:139], v[20:23], v[52:55]\n" \
  "v_mfma_f32_16x16x32_bf16 v[56:59], v[200:203], v[20:23], v[56:59]\n" \
  "s_waitcnt lgkmcnt(4)\n" \
  "v_mfma_f32_16x16x32_bf16 v[60:63], v[36:39], v[20:23], v[60:63]\n" \
  "ds_read_b128 v[20:23], v0 offset:12288\n" \
  "ds_read_b128 v[36:39], v1 offset:5120\n" \
  "v_mfma_f32_16x16x32_bf16 v[48:51], v[76:79], v[24:27], v[48:51]\n" \
  "v_mfma_f32_16x16x32_bf16 v[52:55], v[140:143], v[24:27], v[52:55]\n" \
  "v_mfma_f32_16x16x32_bf16 v[56:59], v[204:207], v[24:27], v[56:59]\n" \
  "s_waitcnt lgkmcnt(4)\n" \
  "v_mfma_f32_16x16x32_bf16 v[60:63], v[28:31], v[24:27], v[60:63]\n" \
  "ds_read_b128 v[24:27], v0 offset:14336\n" \
  "ds_read_b128 v[28:31], v1 offset:6144\n" \
  "v_mfma_f32_16x16x32_bf16 v[48:51], v[80:83], v[12:15], v[48:51]\n" \
  "v_mfma_f32_16x16x32_bf16 v[52:55], v[144:147], v[12:15], v[52:55]\n" \
  "v_mfma_f32_16x16x32_bf16 v[56:59], v[208:211], v[12:15], v[56:59]\n" \
  "s_waitcnt lgkmcnt(4)\n" \
  "v_mfma_f32_16x16x32_bf16 v[60:63], v[32:35], v[12:15], v[60:63]\n" \
  "ds_read_b128 v[12:15], v0 offset:16384\n" \
  "ds_read_b128 v[32:35], v1 offset:7168\n" \
  "v_mfma_f32_16x16x32_bf16 v[48:51], v[84:87], v[16:19], v[48:51]\n" \
  "v_mfma_f32_16x16x32_bf16 v[52:55], v[148:151], v[16:19], v[52:55]\n" \
  "v_mfma_f32_16x16x32_bf16 v[56:59], v[212:215], v[16:19], v[56:59]\n" \
  "s_waitcnt lgkmcnt(4)\n" \
  "v_mfma_f32_16x16x32_bf16 v[60:63], v[36:39], v[16:19], v[60:63]\n" \
  "ds_read_b128 v[16:19], v0 offset:18432\n" \
  "ds_read_b128 v[36:39], v1 offset:8192\n" \
  "v_mfma_f32_16x16x32_bf16 v[48:51], v[88:91], v[20:23], v[48:51]\n" \
  "v_mfma_f32_16x16x32_bf16 v[52:55], v[152:155], v[20:23], v[52:55]\n" \
  "v_mfma_f32_16x16x32_bf16 v[56:59], v[216:219], v[20:23], v[56:59]\n" \
  "s_waitcnt lgkmcnt(4)\n" \
  "v_mfma_f32_16x16x32_bf16 v[60:63], v[28:31], v[20:23], v[60:63]\n" \
  "ds_read_b128 v[20:23], v0 offset:20480\n" \
  "ds_read_b128 v[28:31], v1 offset:9216\n" \
  "v_mfma_f32_16x16x32_bf16 v[48:51], v[92:95], v[24:27], v[48:51]\n" \
  "v_mfma_f32_16x16x32_bf16 v[52:55], v[156:159], v[24:27], v[52:55]\n" \
  "v_mfma_f32_16x16x32_bf16 v[56:59], v[220:223], v[24:27], v[56:59]\n" \
  "s_waitcnt lgkmcnt(4)\n" \
  "v_mfma_f32_16x16x32_bf16 v[60:63], v[32:35], v[24:27], v[60:63]\n" \
  "ds_read_b128 v[24:27], v0 offset:22528\n" \
  "ds_read_b128 v[32:35], v1 offset:10240\n" \
  "v_mfma_f32_16x16x32_bf16 v[48:51], v[96:99], v[12:15], v[48:51]\n" \
  "v_mfma_f32_16x16x32_bf16 v[52:55], v[160:163], v[12:15], v[52:55]\n" \
  "v_mfma_f32_16x16x32_bf16 v[56:59], v[224:227], v[12:15], v[56:59]\n" \
  "s_waitcnt lgkmcnt(4)\n" \
  "v_mfma_f32_16x16x32_bf16 v[60:63], v[36:39], v[12:15], v[60:63]\n" \
  "ds_read_b128 v[12:15], v0 offset:24576\n" \
  "ds_read_b128 v[36:39], v1 offset:11264\n" \
  "v_mfma_f32_16x16x32_bf16 v[48:51], v[100:103], v[16:19], v[48:51]\n" \
  "v_mfma_f32_16x16x32_bf16 v[52:55], v[164:167], v[16:19], v[52:55]\n" \
  "v_mfma_f32_16x16x32_bf16 v[56:59], v[228:231], v[16:19], v[56:59]\n" \
  "s_waitcnt lgkmcnt(4)\n" \
  "v_mfma_f32_16x16x32_bf16 v[60:63], v[28:31], v[16:19], v[60:63]\n" \
  "ds_read_b128 v[16:19], v0 offset:26624\n" \
  "ds_read_b128 v[28:31], v1 offset:12288\n" \
  "v_mfma_f32_16x16x32_bf16 v[48:51], v[104:107], v[20:23], v[48:51]\n" \
  "v_mfma_f32_16x16x32_bf16 v[52:55], v[168:171], v[20:23], v[52:55]\n" \
  "v_mfma_f32_16x16x32_bf16 v[56:59], v[232:235], v[20:23], v[56:59]\n" \
  "s_waitcnt lgkmcnt(4)\n" \
  "v_mfma_f32_16x16x32_bf16 v[60:63], v[32:35], v[20:23], v[60:63]\n" \
  "ds_read_b128 v[20:23], v0 offset:28672\n" \
  "ds_read_b128 v[32:35], v1 offset:13312\n" \
  "v_mfma_f32_16x16x32_bf16 v[48:51], v[108:111], v[24:27], v[48:51]\n" \
  "v_mfma_f32_16x16x32_bf16 v[52:55], v[172:175], v[24:27], v[52:55]\n" \
  "v_mfma_f32_16x16x32_bf16 v[56:59], v[236:239], v[24:27], v[56:59]\n" \
  "s_waitcnt lgkmcnt(4)\n" \
  "v_mfma_f32_16x16x32_bf16 v[60:63], v[36:39], v[24:27], v[60:63]\n" \
  "s_cmp_eq_u32 s68, 1\n" \
  "s_cbranch_scc0 .LCP_X3_%=\n" \
  "s_waitcnt vmcnt(0)\n" \
  "s_mov_b64 exec, 1\n" \
  "buffer_store_dword v7, off, s[56:59], s72 sc1\n" \
  "s_mov_b64 exec, -1\n" \
  "s_add_u32 s72, s72, 0x1000\n" \
  "s_mov_b32 s68, 0\n" \
  ".LCP_X3_%=:\n" \
  "ds_read_b32 v5, v6 offset:16\n" \
  "ds_read_b128 v[24:27], v0 offset:30720\n" \
  "ds_read_b128 v[36:39], v1 offset:14336\n" \
  "v_mfma_f32_16x16x32_bf16 v[48:51], v[112:115], v[12:15], v[48:51]\n" \
  "v_mfma_f32_16x16x32_bf16 v[52:55], v[176:179], v[12:15], v[52:55]\n" \
  "v_mfma_f32_16x16x32_bf16 v[56:59], v[240:243], v[12:15], v[56:59]\n" \
  "s_waitcnt lgkmcnt(5)\n" \
  "v_mfma_f32_16x16x32_bf16 v[60:63], v[28:31], v[12:15], v[60:63]\n" \
  "ds_read_b128 v[12:15], v0 offset:256\n" \
  "ds_read_b128 v[28:31], v1 offset:15360\n" \
  "ds_read_b128 v[40:43], v4 offset:0\n" \
  "ds_read_b128 v[44:47], v4 offset:64\n" \
  "v_mfma_f32_16x16x32_bf16 v[48:51], v[116:119], v[16:19], v[48:51]\n" \
  "v_mfma_f32_16x16x32_bf16 v[52:55], v[180:183], v[16:19], v[52:55]\n" \
  "v_mfma_f32_16x16x32_bf16 v[56:59], v[244:247], v[16:19], v[56:59]\n" \
  "s_waitcnt lgkmcnt(7)\n" \
  "v_mfma_f32_16x16x32_bf16 v[60:63], v[32:35], v[16:19], v[60:63]\n" \
  "s_waitcnt lgkmcnt(6)\n" \
  "v_readfirstlane_b32 s71, v5\n" \
  "s_cmp_ge_u32 s71, s67\n" \
  "s_cbranch_scc1 .LCP_ST_%=\n" \
  ".LCP_S4_%=:\n" \
  "s_sleep 1\n" \
  "ds_read_b32 v5, v6 offset:16\n" \
  "s_sub_u32 s69, s69, 1\n" \
  "s_cbranch_scc1 .LCP_END_%=\n" \
  "s_waitcnt lgkmcnt(0)\n" \
  "v_readfirstlane_b32 s71, v5\n" \
  "s_cmp_ge_u32 s71, s67\n" \
  "s_cbranch_scc1 .LCP_ST_%=\n" \
  "s_branch .LCP_S4_%=\n" \
  ".LCP_ST_%=:\n" \
  "ds_read_b128 v[16:19], v0 offset:2304\n" \
  "ds_read_b128 v[32:35], v4 offset:128\n" \
  "ds_read2_b32 v[8:9], v3 offset0:0 offset1:32\n" \
  "v_mfma_f32_16x16x32_bf16 v[48:51], v[120:123], v[20:23], v[48:51]\n" \
  "v_mfma_f32_16x16x32_bf16 v[52:55], v[184:187], v[20:23], v[52:55]\n" \
  "v_mfma_f32_16x16x32_bf16 v[56:59], v[248:251], v[20:23], v[56:59]\n" \
  "s_waitcnt lgkmcnt(7)\n" \
  "v_mfma_f32_16x16x32_bf16 v[60:63], v[36:39], v[20:23], v[60:63]\n" \
  "ds_read_b128 v[20:23], v0 offset:4352\n" \
  "ds_read_b128 v[36:39], v4 offset:192\n" \
  "v_mfma_f32_16x16x32_bf16 v[48:51], v[124:127], v[24:27], v[48:51]\n" \
  "v_mfma_f32_16x16x32_bf16 v[52:55], v[188:191], v[24:27], v[52:55]\n" \
  "v_mfma_f32_16x16x32_bf16 v[56:59], v[252:255], v[24:27], v[56:59]\n" \
  "s_waitcnt lgkmcnt(7)\n" \
  "v_mfma_f32_16x16x32_bf16 v[60:63], v[28:31], v[24:27], v[60:63]\n" \
  "s_waitcnt lgkmcnt(0)\n" \
  "s_nop 4\n" \
  "v_fma_f32 v48, -v8, v40, v48\n" \
  "v_fma_f32 v49, -v8, v41, v49\n" \
  "v_fma_f32 v50, -v8, v42, v50\n" \
  "v_fma_f32 v51, -v8, v43, v51\n" \
  "v_fma_f32 v52, -v8, v44, v52\n" \
  "v_fma_f32 v53, -v8, v45, v53\n" \
  "v_fma_f32 v54, -v8, v46, v54\n" \
  "v_fma_f32 v55, -v8, v47, v55\n" \
  "v_fma_f32 v56, -v8, v32, v56\n" \
  "v_fma_f32 v57, -v8, v33, v57\n" \
  "v_fma_f32 v58, -v8, v34, v58\n" \
  "v_fma_f32 v59, -v8, v35, v59\n" \
  "v_fma_f32 v60, -v8, v36, v60\n" \
  "v_fma_f32 v61, -v8, v37, v61\n" \
  "v_fma_f32 v62, -v8, v38, v62\n" \
  "v_fma_f32 v63, -v8, v39, v63\n" \
  "ds_read_b128 v[32:35], v1 offset:0\n" \
  "ds_read_b128 v[36:39], v1 offset:1024\n" \
  "v_mul_f32 v48, v48, v9\n" \
  "v_mul_f32 v49, v49, v9\n" \
  "v_mul_f32 v50, v50, v9\n" \
  "v_mul_f32 v51, v51, v9\n" \
  "v_mul_f32 v52, v52, v9\n" \
  "v_mul_f32 v53, v53, v9\n" \
  "v_mul_f32 v54, v54, v9\n" \
  "v_mul_f32 v55, v55, v9\n" \
  "v_cvt_pk_bf16_f32 v40, v48, v49\n" \
  "v_cvt_pk_bf16_f32 v41, v50, v51\n" \
  "v_cvt_pk_bf16_f32 v42, v52, v53\n" \
  "v_cvt_pk_bf16_f32 v43, v54, v55\n" \
  "buffer_store_dwordx4 v[40:43], v2, s[40:43], s65 offen offset:0 sc1\n" \
  "v_mul_f32 v56, v56, v9\n" \
  "v_mul_f32 v57, v57, v9\n" \
  "v_mul_f32 v58, v58, v9\n" \
  "v_mul_f32 v59, v59, v9\n" \
  "v_mul_f32 v60, v60, v9\n" \
  "v_mul_f32 v61, v61, v9\n" \
  "v_mul_f32 v62, v62, v9\n" \
  "v_mul_f32 v63, v63, v9\n" \
  "v_cvt_pk_bf16_f32 v44, v56, v57\n" \
  "v_cvt_pk_bf16_f32 v45, v58, v59\n" \
  "v_cvt_pk_bf16_f32 v46, v60, v61\n" \
  "v_cvt_pk_bf16_f32 v47, v62, v63\n" \
  "buffer_store_dwordx4 v[44:47], v2, s[40:43], s65 offen offset:1024 sc1\n" \
  "ds_read_b128 v[24:27], v0 offset:6400\n" \
  "ds_read_b128 v[28:31], v1 offset:2048\n" \
  "v_mfma_f32_16x16x32_bf16 v[48:51], v[64:67], v[12:15], 0\n" \
  "v_mfma_f32_16x16x32_bf16 v[52:55], v[128:131], v[12:15], 0\n" \
  "v_mfma_f32_16x16x32_bf16 v[56:59], v[192:195], v[12:15], 0\n" \
  "s_waitcnt lgkmcnt(3)\n" \
  "v_mfma_f32_16x16x32_bf16 v[60:63], v[32:35], v[12:15], 0\n" \
  "ds_read_b128 v[12:15], v0 offset:8448\n" \
  "ds_read_b128 v[32:35], v1 offset:3072\n" \
  "v_mfma_f32_16x16x32_bf16 v[48:51], v[68:71], v[16:19], v[48:51]\n" \
  "v_mfma_f32_16x16x32_bf16 v[52:55], v[132:135], v[16:19], v[52:55]\n" \
  "v_mfma_f32_16x16x32_bf16 v[56:59], v[196:199], v[16:19], v[56:59]\n" \
  "s_waitcnt lgkmcnt(4)\n" \
  "v_mfma_f32_16x16x32_bf16 v[60:63], v[36:39], v[16:19], v[60:63]\n" \
  "ds_read_b128 v[16:19], v0 offset:10496\n" \
  "ds_read_b128 v[36:39], v1 offset:4096\n" \
  "v_mfma_f32_16x16x32_bf16 v[48:51], v[72:75], v[20:23], v[48:51]\n" \
  "v_mfma_f32_16x16x32_bf16 v[52:55], v[136:139], v[20:23], v[52:55]\n" \
  "v_mfma_f32_16x16x32_bf16 v[56:59], v[200:203], v[20:23], v[56:59]\n" \
  "s_waitcnt lgkmcnt(4)\n" \
  "v_mfma_f32_16x16x32_bf16 v[60:63], v[28:31], v[20:23], v[60:63]\n" \
  "ds_read_b128 v[20:23], v0 offset:12544\n" \
  "ds_read_b128 v[28:31], v1 offset:5120\n" \
  "v_mfma_f32_16x16x32_bf16 v[48:51], v[76:79], v[24:27], v[48:51]\n" \
  "v_mfma_f32_16x16x32_bf16 v[52:55], v[140:143], v[24:27], v[52:55]\n" \
  "v_mfma_f32_16x16x32_bf16 v[56:59], v[204:207], v[24:27], v[56:59]\n" \
  "s_waitcnt lgkmcnt(4)\n" \
  "v_mfma_f32_16x16x32_bf16 v[60:63], v[32:35], v[24:27], v[60:63]\n" \
  "ds_read_b128 v[24:27], v0 offset:14592\n" \
  "ds_read_b128 v[32:35], v1 offset:6144\n" \
  "v_mfma_f32_16x16x32_bf16 v[48:51], v[80:83], v[12:15], v[48:51]\n" \
  "v_mfma_f32_16x16x32_bf16 v[52:55], v[144:147], v[12:15], v[52:55]\n" \
  "v_mfma_f32_16x16x32_bf16 v[56:59], v[208:211], v[12:15], v[56:59]\n" \
  "s_waitcnt lgkmcnt(4)\n" \
  "v_mfma_f32_16x16x32_bf16 v[60:63], v[36:39], v[12:15], v[60:63]\n" \
  "ds_read_b128 v[12:15], v0 offset:16640\n" \
  "ds_read_b128 v[36:39], v1 offset:7168\n" \
  "v_mfma_f32_16x16x32_bf16 v[48:51], v[84:87], v[16:19], v[48:51]\n" \
  "v_mfma_f32_16x16x32_bf16 v[52:55], v[148:151], v[16:19], v[52:55]\n" \
  "v_mfma_f32_16x16x32_bf16 v[56:59], v[212:215], v[16:19], v[56:59]\n" \
  "s_waitcnt lgkmcnt(4)\n" \
  "v_mfma_f32_16x16x32_bf16 v[60:63], v[28:31], v[16:19], v[60:63]\n" \
  "ds_read_b128 v[16:19], v0 offset:18688\n" \
  "ds_read_b128 v[28:31], v1 offset:8192\n" \
  "v_mfma_f32_16x16x32_bf16 v[48:51], v[88:91], v[20:23], v[48:51]\n" \
  "v_mfma_f32_16x16x32_bf16 v[52:55], v[152:155], v[20:23], v[52:55]\n" \
  "v_mfma_f32_16x16x32_bf16 v[56:59], v[216:219], v[20:23], v[56:59]\n" \
  "s_waitcnt lgkmcnt(4)\n" \
  "v_mfma_f32_16x16x32_bf16 v[60:63], v[32:35], v[20:23], v[60:63]\n" \
  "ds_read_b128 v[20:23], v0 offset:20736\n" \
  "ds_read_b128 v[32:35], v1 offset:9216\n" \
  "v_mfma_f32_16x16x32_bf16 v[48:51], v[92:95], v[24:27], v[48:51]\n" \
  "v_mfma_f32_16x16x32_bf16 v[52:55], v[156:159], v[24:27], v[52:55]\n" \
  "v_mfma_f32_16x16x32_bf16 v[56:59], v[220:223], v[24:27], v[56:59]\n" \
  "s_waitcnt lgkmcnt(4)\n" \
  "v_mfma_f32_16x16x32_bf16 v[60:63], v[36:39], v[24:27], v[60:63]\n" \
  "ds_read_b128 v[24:27], v0 offset:22784\n" \
  "ds_read_b128 v[36:39], v1 offset:10240\n" \
  "v_mfma_f32_16x16x32_bf16 v[48:51], v[96:99], v[12:15], v[48:51]\n" \
  "v_mfma_f32_16x16x32_bf16 v[52:55], v[160:163], v[12:15], v[52:55]\n" \
  "v_mfma_f32_16x16x32_bf16 v[56:59], v[224:227], v[12:15], v[56:59]\n" \
  "s_waitcnt lgkmcnt(4)\n" \
  "v_mfma_f32_16x16x32_bf16 v[60:63], v[28:31], v[12:15], v[60:63]\n" \
  "ds_read_b128 v[12:15], v0 offset:24832\n" \
  "ds_read_b128 v[28:31], v1 offset:11264\n" \
  "v_mfma_f32_16x16x32_bf16 v[48:51], v[100:103], v[16:19], v[48:51]\n" \
  "v_mfma_f32_16x16x32_bf16 v[52:55], v[164:167], v[16:19], v[52:55]\n" \
  "v_mfma_f32_16x16x32_bf16 v[56:59], v[228:231], v[16:19], v[56:59]\n" \
  "s_waitcnt lgkmcnt(4)\n" \
  "v_mfma_f32_16x16x32_bf16 v[60:63], v[32:35], v[16:19], v[60:63]\n" \
  "ds_read_b128 v[16:19], v0 offset:26880\n" \
  "ds_read_b128 v[32:35], v1 offset:12288\n" \
  "v_mfma_f32_16x16x32_bf16 v[48:51], v[104:107], v[20:23], v[48:51]\n" \
  "v_mfma_f32_16x16x32_bf16 v[52:55], v[168:171], v[20:23], v[52:55]\n" \
  "v_mfma_f32_16x16x32_bf16 v[56:59], v[232:235], v[20:23], v[56:59]\n" \
  "s_waitcnt lgkmcnt(4)\n" \
  "v_mfma_f32_16x16x32_bf16 v[60:63], v[36:39], v[20:23], v[60:63]\n" \
  "ds_read_b128 v[20:23], v0 offset:28928\n" \
  "ds_read_b128 v[36:39], v1 offset:13312\n" \
  "v_mfma_f32_16x16x32_bf16 v[48:51], v[108:111], v[24:27], v[48:51]\n" \
  "v_mfma_f32_16x16x32_bf16 v[52:55], v[172:175], v[24:27], v[52:55]\n" \
  "v_mfma_f32_16x16x32_bf16 v[56:59], v[236:239], v[24:27], v[56:59]\n" \
  "s_waitcnt lgkmcnt(4)\n" \
  "v_mfma_f32_16x16x32_bf16 v[60:63], v[28:31], v[24:27], v[60:63]\n" \
  "ds_read_b128 v[24:27], v0 offset:30976\n" \
  "ds_read_b128 v[28:31], v1 offset:14336\n" \
  "v_mfma_f32_16x16x32_bf16 v[48:51], v[112:115], v[12:15], v[48:51]\n" \
  "v_mfma_f32_16x16x32_bf16 v[52:55], v[176:179], v[12:15], v[52:55]\n" \
  "v_mfma_f32_16x16x32_bf16 v[56:59], v[240:243], v[12:15], v[56:59]\n" \
  "s_waitcnt lgkmcnt(4)\n" \
  "v_mfma_f32_16x16x32_bf16 v[60:63], v[32:35], v[12:15], v[60:63]\n" \
  "ds_read_b128 v[32:35], v1 offset:15360\n" \
  "ds_read_b128 v[40:43], v4 offset:0\n" \
  "ds_read_b128 v[44:47], v4 offset:64\n" \
  "v_mfma_f32_16x16x32_bf16 v[48:51], v[116:119], v[16:19], v[48:51]\n" \
  "v_mfma_f32_16x16x32_bf16 v[52:55], v[180:183], v[16:19], v[52:55]\n" \
  "v_mfma_f32_16x16x32_bf16 v[56:59], v[244:247], v[16:19], v[56:59]\n" \
  "s_waitcnt lgkmcnt(5)\n" \
  "v_mfma_f32_16x16x32_bf16 v[60:63], v[36:39], v[16:19], v[60:63]\n" \
  "ds_read_b128 v[36:39], v4 offset:128\n" \
  "ds_read2_b32 v[8:9], v3 offset0:16 offset1:48\n" \
  "v_mfma_f32_16x16x32_bf16 v[48:51], v[120:123], v[20:23], v[48:51]\n" \
  "v_mfma_f32_16x16x32_bf16 v[52:55], v[184:187], v[20:23], v[52:55]\n" \
  "v_mfma_f32_16x16x32_bf16 v[56:59], v[248:251], v[20:23], v[56:59]\n" \
  "s_waitcnt lgkmcnt(5)\n" \
  "v_mfma_f32_16x16x32_bf16 v[60:63], v[28:31], v[20:23], v[60:63]\n" \
  "ds_read_b128 v[28:31], v4 offset:192\n" \
  "s_mov_b64 exec, 1\n" \
  "ds_add_u32 v6, v7 offset:8\n" \
  "s_mov_b64 exec, -1\n" \
  "v_xor_b32 v6, 4, v6\n" \
  "v_mfma_f32_16x16x32_bf16 v[48:51], v[124:127], v[24:27], v[48:51]\n" \
  "v_mfma_f32_16x16x32_bf16 v[52:55], v[188:191], v[24:27], v[52:55]\n" \
  "v_mfma_f32_16x16x32_bf16 v[56:59], v[252:255], v[24:27], v[56:59]\n" \
  "s_waitcnt lgkmcnt(6)\n" \
  "v_mfma_f32_16x16x32_bf16 v[60:63], v[32:35], v[24:27], v[60:63]\n" \
  "s_waitcnt lgkmcnt(1)\n" \
  "s_nop 4\n" \
  "ds_read_b32 v5, v6 offset:0\n" \
  "v_fma_f32 v48, -v8, v40, v48\n" \
  "v_fma_f32 v49, -v8, v41, v49\n" \
  "v_fma_f32 v50, -v8, v42, v50\n" \
  "v_fma_f32 v51, -v8, v43, v51\n" \
  "v_fma_f32 v52, -v8, v44, v52\n" \
  "v_fma_f32 v53, -v8, v45, v53\n" \
  "v_fma_f32 v54, -v8, v46, v54\n" \
  "v_fma_f32 v55, -v8, v47, v55\n" \
  "v_fma_f32 v56, -v8, v36, v56\n" \
  "v_fma_f32 v57, -v8, v37, v57\n" \
  "v_fma_f32 v58, -v8, v38, v58\n" \
  "v_fma_f32 v59, -v8, v39, v59\n" \
  "v_fma_f32 v60, -v8, v28, v60\n" \
  "v_fma_f32 v61, -v8, v29, v61\n" \
  "v_fma_f32 v62, -v8, v30, v62\n" \
  "v_fma_f32 v63, -v8, v31, v63\n" \
  "v_mul_f32 v48, v48, v9\n" \
  "v_mul_f32 v49, v49, v9\n" \
  "v_mul_f32 v50, v50, v9\n" \
  "v_mul_f32 v51, v51, v9\n" \
  "v_mul_f32 v52, v52, v9\n" \
  "v_mul_f32 v53, v53, v9\n" \
  "v_mul_f32 v54, v54, v9\n" \
  "v_mul_f32 v55, v55, v9\n" \
  "v_cvt_pk_bf16_f32 v40, v48, v49\n" \
  "v_cvt_pk_bf16_f32 v41, v50, v51\n" \
  "v_cvt_pk_bf16_f32 v42, v52, v53\n" \
  "v_cvt_pk_bf16_f32 v43, v54, v55\n" \
  "buffer_store_dwordx4 v[40:43], v10, s[40:43], s65 offen offset:0 sc1\n" \
  "v_mul_f32 v56, v56, v9\n" \
  "v_mul_f32 v57, v57, v9\n" \
  "v_mul_f32 v58, v58, v9\n" \
  "v_mul_f32 v59, v59, v9\n" \
  "v_mul_f32 v60, v60, v9\n" \
  "v_mul_f32 v61, v61, v9\n" \
  "v_mul_f32 v62, v62, v9\n" \
  "v_mul_f32 v63, v63, v9\n" \
  "v_cvt_pk_bf16_f32 v44, v56, v57\n" \
  "v_cvt_pk_bf16_f32 v45, v58, v59\n" \
  "v_cvt_pk_bf16_f32 v46, v60, v61\n" \
  "v_cvt_pk_bf16_f32 v47, v62, v63\n" \
  "buffer_store_dwordx4 v[44:47], v10, s[40:43], s65 offen offset:1024 sc1\n" \
  "s_waitcnt lgkmcnt(0)\n" \
  "s_add_u32 s65, s65, 0x80000\n" \
  "s_sub_u32 s64, s64, 1\n" \
  "s_mov_b32 s68, 1\n" \
  "s_cmp_lg_u32 s64, 0\n" \
  "s_cbranch_scc1 .LCP_TOP_%=\n" \
  "s_waitcnt vmcnt(0)\n" \
  "s_mov_b64 exec, 1\n" \
  "buffer_store_dword v7, off, s[56:59], s72 sc1\n" \
  "s_mov_b64 exec, -1\n" \
  "s_add_u32 s72, s72, 0x1000\n" \
  "s_mov_b32 s68, 0\n" \
  ".LCP_END_%=:\n" \
  "s_endpgm\n"
#define V_ASM \
  "s_mov_b32 s68, 0\n" \
  "s_mov_b32 s66, 3\n" \
  "s_mov_b32 s67, 0\n" \
  "s_mov_b32 s72, 0\n" \
  "v_mov_b32 v7, 1\n" \
  "v_mov_b32 v6, s70\n" \
  "v_mov_b32 v5, 0\n" \
  "ds_read_b32 v8, v4 offset:0\n" \
  "ds_read_b32 v9, v4 offset:64\n" \
  "ds_read_b32 v10, v4 offset:128\n" \
  "ds_read_b32 v4, v4 offset:192\n" \
  "s_waitcnt lgkmcnt(0)\n" \
  ".LCW_TOP_%=:\n" \
  "ds_read_b128 v[28:31], v1 offset:0\n" \
  "ds_read_b128 v[32:35], v1 offset:1024\n" \
  "s_add_u32 s66, s66, 3\n" \
  "s_add_u32 s67, s67, 1\n" \
  "s_mov_b32 s69, 0x400000\n" \
  "v_xor_b32 v0, 0x8000, v0\n" \
  "v_xor_b32 v3, 0x100, v3\n" \
  "v_readfirstlane_b32 s71, v5\n" \
  "s_cmp_ge_u32 s71, s66\n" \
  "s_cbranch_scc1 .LCW_GO_%=\n" \
  "s_cmp_eq_u32 s68, 1\n" \
  "s_cbranch_scc0 .LCW_X1_%=\n" \
  "s_waitcnt vmcnt(0)\n" \
  "s_mov_b64 exec, 1\n" \
  "buffer_store_dword v7, off, s[56:59], s72 sc1\n" \
  "s_mov_b64 exec, -1\n" \
  "s_add_u32 s72, s72, 0x1000\n" \
  "s_mov_b32 s68, 0\n" \
  ".LCW_X1_%=:\n" \
  ".LCW_S2_%=:\n" \
  "s_sleep 1\n" \
  "ds_read_b32 v5, v6 offset:0\n" \
  "s_sub_u32 s69, s69, 1\n" \
  "s_cbranch_scc1 .LCW_END_%=\n" \
  "s_waitcnt lgkmcnt(0)\n" \
  "v_readfirstlane_b32 s71, v5\n" \
  "s_cmp_ge_u32 s71, s66\n" \
  "s_cbranch_scc1 .LCW_GO_%=\n" \
  "s_branch .LCW_S2_%=\n" \
  ".LCW_GO_%=:\n" \
  "ds_read_b128 v[12:15], v0 offset:0\n" \
  "ds_read_b128 v[16:19], v0 offset:2048\n" \
  "ds_read_b128 v[20:23], v0 offset:4096\n" \
  "ds_read_b128 v[24:27], v0 offset:6144\n" \
  "ds_read_b128 v[36:39], v1 offset:2048\n" \
  "s_waitcnt lgkmcnt(4)\n" \
  "v_mfma_f32_16x16x32_bf16 v[42:45], v[12:15], v[64:67], 0\n" \
  "v_mfma_f32_16x16x32_bf16 v[48:51], v[12:15], v[128:131], 0\n" \
  "v_mfma_f32_16x16x32_bf16 v[54:57], v[12:15], v[192:195], 0\n" \
  "v_mfma_f32_16x16x32_bf16 v[60:63], v[12:15], v[28:31], 0\n" \
  "ds_read_b128 v[12:15], v0 offset:8192\n" \
  "ds_read_b128 v[28:31], v1 offset:3072\n" \
  "s_waitcnt lgkmcnt(5)\n" \
  "v_mfma_f32_16x16x32_bf16 v[42:45], v[16:19], v[68:71], v[42:45]\n" \
  "v_mfma_f32_16x16x32_bf16 v[48:51], v[16:19], v[132:135], v[48:51]\n" \
  "v_mfma_f32_16x16x32_bf16 v[54:57], v[16:19], v[196:199], v[54:57]\n" \
  "v_mfma_f32_16x16x32_bf16 v[60:63], v[16:19], v[32:35], v[60:63]\n" \
  "ds_read_b128 v[16:19], v0 offset:10240\n" \
  "ds_read_b128 v[32:35], v1 offset:4096\n" \
  "s_waitcnt lgkmcnt(6)\n" \
  "v_mfma_f32_16x16x32_bf16 v[42:45], v[20:23], v[72:75], v[42:45]\n" \
  "v_mfma_f32_16x16x32_bf16 v[48:51], v[20:23], v[136:139], v[48:51]\n" \
  "v_mfma_f32_16x16x32_bf16 v[54:57], v[20:23], v[200:203], v[54:57]\n" \
  "s_waitcnt lgkmcnt(4)\n" \
  "v_mfma_f32_16x16x32_bf16 v[60:63], v[20:23], v[36:39], v[60:63]\n" \
  "ds_read_b128 v[20:23], v0 offset:12288\n" \
  "ds_read_b128 v[36:39], v1 offset:5120\n" \
  "v_mfma_f32_16x16x32_bf16 v[42:45], v[24:27], v[76:79], v[42:45]\n" \
  "v_mfma_f32_16x16x32_bf16 v[48:51], v[24:27], v[140:143], v[48:51]\n" \
  "v_mfma_f32_16x16x32_bf16 v[54:57], v[24:27], v[204:207], v[54:57]\n" \
  "s_waitcnt lgkmcnt(4)\n" \
  "v_mfma_f32_16x16x32_bf16 v[60:63], v[24:27], v[28:31], v[60:63]\n" \
  "ds_read_b128 v[24:27], v0 offset:14336\n" \
  "ds_read_b128 v[28:31], v1 offset:6144\n" \
  "v_mfma_f32_16x16x32_bf16 v[42:45], v[12:15], v[80:83], v[42:45]\n" \
  "v_mfma_f32_16x16x32_bf16 v[48:51], v[12:15], v[144:147], v[48:51]\n" \
  "v_mfma_f32_16x16x32_bf16 v[54:57], v[12:15], v[208:211], v[54:57]\n" \
  "s_waitcnt lgkmcnt(4)\n" \
  "v_mfma_f32_16x16x32_bf16 v[60:63], v[12:15], v[32:35], v[60:63]\n" \
  "ds_read_b128 v[12:15], v0 offset:16384\n" \
  "ds_read_b128 v[32:35], v1 offset:7168\n" \
  "v_mfma_f32_16x16x32_bf16 v[42:45], v[16:19], v[84:87], v[42:45]\n" \
  "v_mfma_f32_16x16x32_bf16 v[48:51], v[16:19], v[148:151], v[48:51]\n" \
  "v_mfma_f32_16x16x32_bf16 v[54:57], v[16:19], v[212:215], v[54:57]\n" \
  "s_waitcnt lgkmcnt(4)\n" \
  "v_mfma_f32_16x16x32_bf16 v[60:63], v[16:19], v[36:39], v[60:63]\n" \
  "ds_read_b128 v[16:19], v0 offset:18432\n" \
  "ds_read_b128 v[36:39], v1 offset:8192\n" \
  "v_mfma_f32_16x16x32_bf16 v[42:45], v[20:23], v[88:91], v[42:45]\n" \
  "v_mfma_f32_16x16x32_bf16 v[48:51], v[20:23], v[152:155], v[48:51]\n" \
  "v_mfma_f32_16x16x32_bf16 v[54:57], v[20:23], v[216:219], v[54:57]\n" \
  "s_waitcnt lgkmcnt(4)\n" \
  "v_mfma_f32_16x16x32_bf16 v[60:63], v[20:23], v[28:31], v[60:63]\n" \
  "ds_read_b128 v[20:23], v0 offset:20480\n" \
  "ds_read_b128 v[28:31], v1 offset:9216\n" \
  "v_mfma_f32_16x16x32_bf16 v[42:45], v[24:27], v[92:95], v[42:45]\n" \
  "v_mfma_f32_16x16x32_bf16 v[48:51], v[24:27], v[156:159], v[48:51]\n" \
  "v_mfma_f32_16x16x32_bf16 v[54:57], v[24:27], v[220:223], v[54:57]\n" \
  "s_waitcnt lgkmcnt(4)\n" \
  "v_mfma_f32_16x16x32_bf16 v[60:63], v[24:27], v[32:35], v[60:63]\n" \
  "ds_read_b128 v[24:27], v0 offset:22528\n" \
  "ds_read_b128 v[32:35], v1 offset:10240\n" \
  "v_mfma_f32_16x16x32_bf16 v[42:45], v[12:15], v[96:99], v[42:45]\n" \
  "v_mfma_f32_16x16x32_bf16 v[48:51], v[12:15], v[160:163], v[48:51]\n" \
  "v_mfma_f32_16x16x32_bf16 v[54:57], v[12:15], v[224:227], v[54:57]\n" \
  "s_waitcnt lgkmcnt(4)\n" \
  "v_mfma_f32_16x16x32_bf16 v[60:63], v[12:15], v[36:39], v[60:63]\n" \
  "ds_read_b128 v[12:15], v0 offset:24576\n" \
  "ds_read_b128 v[36:39], v1 offset:11264\n" \
  "v_mfma_f32_16x16x32_bf16 v[42:45], v[16:19], v[100:103], v[42:45]\n" \
  "v_mfma_f32_16x16x32_bf16 v[48:51], v[16:19], v[164:167], v[48:51]\n" \
  "v_mfma_f32_16x16x32_bf16 v[54:57], v[16:19], v[228:231], v[54:57]\n" \
  "s_waitcnt lgkmcnt(4)\n" \
  "v_mfma_f32_16x16x32_bf16 v[60:63], v[16:19], v[28:31], v[60:63]\n" \
  "ds_read_b128 v[16:19], v0 offset:26624\n" \
  "ds_read_b128 v[28:31], v1 offset:12288\n" \
  "v_mfma_f32_16x16x32_bf16 v[42:45], v[20:23], v[104:107], v[42:45]\n" \
  "v_mfma_f32_16x16x32_bf16 v[48:51], v[20:23], v[168:171], v[48:51]\n" \
  "v_mfma_f32_16x16x32_bf16 v[54:57], v[20:23], v[232:235], v[54:57]\n" \
  "s_waitcnt lgkmcnt(4)\n" \
  "v_mfma_f32_16x16x32_bf16 v[60:63], v[20:23], v[32:35], v[60:63]\n" \
  "ds_read_b128 v[20:23], v0 offset:28672\n" \
  "ds_read_b128 v[32:35], v1 offset:13312\n" \
  "v_mfma_f32_16x16x32_bf16 v[42:45], v[24:27], v[108:111], v[42:45]\n" \
  "v_mfma_f32_16x16x32_bf16 v[48:51], v[24:27], v[172:175], v[48:51]\n" \
  "v_mfma_f32_16x16x32_bf16 v[54:57], v[24:27], v[236:239], v[54:57]\n" \
  "s_waitcnt lgkmcnt(4)\n" \
  "v_mfma_f32_16x16x32_bf16 v[60:63], v[24:27], v[36:39], v[60:63]\n" \
  "s_cmp_eq_u32 s68, 1\n" \
  "s_cbranch_scc0 .LCW_X3_%=\n" \
  "s_waitcnt vmcnt(0)\n" \
  "s_mov_b64 exec, 1\n" \
  "buffer_store_dword v7, off, s[56:59], s72 sc1\n" \
  "s_mov_b64 exec, -1\n" \
  "s_add_u32 s72, s72, 0x1000\n" \
  "s_mov_b32 s68, 0\n" \
  ".LCW_X3_%=:\n" \
  "ds_read_b32 v5, v6 offset:16\n" \
  "ds_read_b128 v[24:27], v0 offset:30720\n" \
  "ds_read_b128 v[36:39], v1 offset:14336\n" \
  "v_mfma_f32_16x16x32_bf16 v[42:45], v[12:15], v[112:115], v[42:45]\n" \
  "v_mfma_f32_16x16x32_bf16 v[48:51], v[12:15], v[176:179], v[48:51]\n" \
  "v_mfma_f32_16x16x32_bf16 v[54:57], v[12:15], v[240:243], v[54:57]\n" \
  "s_waitcnt lgkmcnt(5)\n" \
  "v_mfma_f32_16x16x32_bf16 v[60:63], v[12:15], v[28:31], v[60:63]\n" \
  "ds_read_b128 v[12:15], v0 offset:256\n" \
  "ds_read_b128 v[28:31], v1 offset:15360\n" \
  "v_mfma_f32_16x16x32_bf16 v[42:45], v[16:19], v[116:119], v[42:45]\n" \
  "v_mfma_f32_16x16x32_bf16 v[48:51], v[16:19], v[180:183], v[48:51]\n" \
  "v_mfma_f32_16x16x32_bf16 v[54:57], v[16:19], v[244:247], v[54:57]\n" \
  "s_waitcnt lgkmcnt(5)\n" \
  "v_mfma_f32_16x16x32_bf16 v[60:63], v[16:19], v[32:35], v[60:63]\n" \
  "s_waitcnt lgkmcnt(4)\n" \
  "v_readfirstlane_b32 s71, v5\n" \
  "s_cmp_ge_u32 s71, s67\n" \
  "s_cbranch_scc1 .LCW_ST_%=\n" \
  ".LCW_S4_%=:\n" \
  "s_sleep 1\n" \
  "ds_read_b32 v5, v6 offset:16\n" \
  "s_sub_u32 s69, s69, 1\n" \
  "s_cbranch_scc1 .LCW_END_%=\n" \
  "s_waitcnt lgkmcnt(0)\n" \
  "v_readfirstlane_b32 s71, v5\n" \
  "s_cmp_ge_u32 s71, s67\n" \
  "s_cbranch_scc1 .LCW_ST_%=\n" \
  "s_branch .LCW_S4_%=\n" \
  ".LCW_ST_%=:\n" \
  "ds_read_b128 v[16:19], v0 offset:2304\n" \
  "ds_read_b128 v[32:35], v3 offset:0\n" \
  "v_mfma_f32_16x16x32_bf16 v[42:45], v[20:23], v[120:123], v[42:45]\n" \
  "v_mfma_f32_16x16x32_bf16 v[48:51], v[20:23], v[184:187], v[48:51]\n" \
  "v_mfma_f32_16x16x32_bf16 v[54:57], v[20:23], v[248:251], v[54:57]\n" \
  "s_waitcnt lgkmcnt(4)\n" \
  "v_mfma_f32_16x16x32_bf16 v[60:63], v[20:23], v[36:39], v[60:63]\n" \
  "ds_read_b128 v[20:23], v0 offset:4352\n" \
  "ds_read_b128 v[36:39], v3 offset:128\n" \
  "v_mfma_f32_16x16x32_bf16 v[42:45], v[24:27], v[124:127], v[42:45]\n" \
  "v_mfma_f32_16x16x32_bf16 v[48:51], v[24:27], v[188:191], v[48:51]\n" \
  "v_mfma_f32_16x16x32_bf16 v[54:57], v[24:27], v[252:255], v[54:57]\n" \
  "s_waitcnt lgkmcnt(4)\n" \
  "v_mfma_f32_16x16x32_bf16 v[60:63], v[24:27], v[28:31], v[60:63]\n" \
  "s_waitcnt lgkmcnt(0)\n" \
  "s_nop 4\n" \
  "v_fma_f32 v42, -v32, v8, v42\n" \
  "v_fma_f32 v43, -v33, v8, v43\n" \
  "v_fma_f32 v44, -v34, v8, v44\n" \
  "v_fma_f32 v45, -v35, v8, v45\n" \
  "v_fma_f32 v48, -v32, v9, v48\n" \
  "v_fma_f32 v49, -v33, v9, v49\n" \
  "v_fma_f32 v50, -v34, v9, v50\n" \
  "v_fma_f32 v51, -v35, v9, v51\n" \
  "v_fma_f32 v54, -v32, v10, v54\n" \
  "v_fma_f32 v55, -v33, v10, v55\n" \
  "v_fma_f32 v56, -v34, v10, v56\n" \
  "v_fma_f32 v57, -v35, v10, v57\n" \
  "v_fma_f32 v60, -v32, v4, v60\n" \
  "v_fma_f32 v61, -v33, v4, v61\n" \
  "v_fma_f32 v62, -v34, v4, v62\n" \
  "v_fma_f32 v63, -v35, v4, v63\n" \
  "ds_read_b128 v[32:35], v1 offset:0\n" \
  "v_mul_f32 v42, v42, v36\n" \
  "v_mul_f32 v43, v43, v37\n" \
  "v_mul_f32 v44, v44, v38\n" \
  "v_mul_f32 v45, v45, v39\n" \
  "v_mul_f32 v48, v48, v36\n" \
  "v_mul_f32 v49, v49, v37\n" \
  "v_mul_f32 v50, v50, v38\n" \
  "v_mul_f32 v51, v51, v39\n" \
  "v_mul_f32 v54, v54, v36\n" \
  "v_mul_f32 v55, v55, v37\n" \
  "v_mul_f32 v56, v56, v38\n" \
  "v_mul_f32 v57, v57, v39\n" \
  "v_mul_f32 v60, v60, v36\n" \
  "v_mul_f32 v61, v61, v37\n" \
  "v_mul_f32 v62, v62, v38\n" \
  "v_mul_f32 v63, v63, v39\n" \
  "ds_read_b128 v[36:39], v1 offset:1024\n" \
  "v_cvt_pk_bf16_f32 v40, v42, v43\n" \
  "v_cvt_pk_bf16_f32 v41, v44, v45\n" \
  "v_cvt_pk_bf16_f32 v46, v48, v49\n" \
  "v_cvt_pk_bf16_f32 v47, v50, v51\n" \
  "v_cvt_pk_bf16_f32 v52, v54, v55\n" \
  "v_cvt_pk_bf16_f32 v53, v56, v57\n" \
  "v_cvt_pk_bf16_f32 v58, v60, v61\n" \
  "v_cvt_pk_bf16_f32 v59, v62, v63\n" \
  "ds_read_b128 v[24:27], v0 offset:6400\n" \
  "ds_read_b128 v[28:31], v1 offset:2048\n" \
  "v_mfma_f32_16x16x32_bf16 v[42:45], v[12:15], v[64:67], 0\n" \
  "v_mfma_f32_16x16x32_bf16 v[48:51], v[12:15], v[128:131], 0\n" \
  "v_mfma_f32_16x16x32_bf16 v[54:57], v[12:15], v[192:195], 0\n" \
  "s_waitcnt lgkmcnt(3)\n" \
  "v_mfma_f32_16x16x32_bf16 v[60:63], v[12:15], v[32:35], 0\n" \
  "ds_read_b128 v[12:15], v0 offset:8448\n" \
  "ds_read_b128 v[32:35], v1 offset:3072\n" \
  "v_mfma_f32_16x16x32_bf16 v[42:45], v[16:19], v[68:71], v[42:45]\n" \
  "v_mfma_f32_16x16x32_bf16 v[48:51], v[16:19], v[132:135], v[48:51]\n" \
  "v_mfma_f32_16x16x32_bf16 v[54:57], v[16:19], v[196:199], v[54:57]\n" \
  "s_waitcnt lgkmcnt(4)\n" \
  "v_mfma_f32_16x16x32_bf16 v[60:63], v[16:19], v[36:39], v[60:63]\n" \
  "ds_read_b128 v[16:19], v0 offset:10496\n" \
  "ds_read_b128 v[36:39], v1 offset:4096\n" \
  "v_mfma_f32_16x16x32_bf16 v[42:45], v[20:23], v[72:75], v[42:45]\n" \
  "v_mfma_f32_16x16x32_bf16 v[48:51], v[20:23], v[136:139], v[48:51]\n" \
  "v_mfma_f32_16x16x32_bf16 v[54:57], v[20:23], v[200:203], v[54:57]\n" \
  "s_waitcnt lgkmcnt(4)\n" \
  "v_mfma_f32_16x16x32_bf16 v[60:63], v[20:23], v[28:31], v[60:63]\n" \
  "ds_read_b128 v[20:23], v0 offset:12544\n" \
  "ds_read_b128 v[28:31], v1 offset:5120\n" \
  "v_mfma_f32_16x16x32_bf16 v[42:45], v[24:27], v[76:79], v[42:45]\n" \
  "v_mfma_f32_16x16x32_bf16 v[48:51], v[24:27], v[140:143], v[48:51]\n" \
  "v_mfma_f32_16x16x32_bf16 v[54:57], v[24:27], v[204:207], v[54:57]\n" \
  "s_waitcnt lgkmcnt(4)\n" \
  "v_mfma_f32_16x16x32_bf16 v[60:63], v[24:27], v[32:35], v[60:63]\n" \
  "ds_read_b128 v[24:27], v0 offset:14592\n" \
  "ds_read_b128 v[32:35], v1 offset:6144\n" \
  "v_mfma_f32_16x16x32_bf16 v[42:45], v[12:15], v[80:83], v[42:45]\n" \
  "v_mfma_f32_16x16x32_bf16 v[48:51], v[12:15], v[144:147], v[48:51]\n" \
  "v_mfma_f32_16x16x32_bf16 v[54:57], v[12:15], v[208:211], v[54:57]\n" \
  "s_waitcnt lgkmcnt(4)\n" \
  "v_mfma_f32_16x16x32_bf16 v[60:63], v[12:15], v[36:39], v[60:63]\n" \
  "ds_read_b128 v[12:15], v0 offset:16640\n" \
  "ds_read_b128 v[36:39], v1 offset:7168\n" \
  "v_mfma_f32_16x16x32_bf16 v[42:45], v[16:19], v[84:87], v[42:45]\n" \
  "v_mfma_f32_16x16x32_bf16 v[48:51], v[16:19], v[148:151], v[48:51]\n" \
  "v_mfma_f32_16x16x32_bf16 v[54:57], v[16:19], v[212:215], v[54:57]\n" \
  "s_waitcnt lgkmcnt(4)\n" \
  "v_mfma_f32_16x16x32_bf16 v[60:63], v[16:19], v[28:31], v[60:63]\n" \
  "ds_read_b128 v[16:19], v0 offset:18688\n" \
  "ds_read_b128 v[28:31], v1 offset:8192\n" \
  "v_mfma_f32_16x16x32_bf16 v[42:45], v[20:23], v[88:91], v[42:45]\n" \
  "v_mfma_f32_16x16x32_bf16 v[48:51], v[20:23], v[152:155], v[48:51]\n" \
  "v_mfma_f32_16x16x32_bf16 v[54:57], v[20:23], v[216:219], v[54:57]\n" \
  "s_waitcnt lgkmcnt(4)\n" \
  "v_mfma_f32_16x16x32_bf16 v[60:63], v[20:23], v[32:35], v[60:63]\n" \
  "ds_read_b128 v[20:23], v0 offset:20736\n" \
  "ds_read_b128 v[32:35], v1 offset:9216\n" \
  "v_mfma_f32_16x16x32_bf16 v[42:45], v[24:27], v[92:95], v[42:45]\n" \
  "v_mfma_f32_16x16x32_bf16 v[48:51], v[24:27], v[156:159], v[48:51]\n" \
  "v_mfma_f32_16x16x32_bf16 v[54:57], v[24:27], v[220:223], v[54:57]\n" \
  "s_waitcnt lgkmcnt(4)\n" \
  "v_mfma_f32_16x16x32_bf16 v[60:63], v[24:27], v[36:39], v[60:63]\n" \
  "ds_read_b128 v[24:27], v0 offset:22784\n" \
  "ds_read_b128 v[36:39], v1 offset:10240\n" \
  "v_mfma_f32_16x16x32_bf16 v[42:45], v[12:15], v[96:99], v[42:45]\n" \
  "v_mfma_f32_16x16x32_bf16 v[48:51], v[12:15], v[160:163], v[48:51]\n" \
  "v_mfma_f32_16x16x32_bf16 v[54:57], v[12:15], v[224:227], v[54:57]\n" \
  "s_waitcnt lgkmcnt(4)\n" \
  "v_mfma_f32_16x16x32_bf16 v[60:63], v[12:15], v[28:31], v[60:63]\n" \
  "ds_read_b128 v[12:15], v0 offset:24832\n" \
  "ds_read_b128 v[28:31], v1 offset:11264\n" \
  "v_mfma_f32_16x16x32_bf16 v[42:45], v[16:19], v[100:103], v[42:45]\n" \
  "v_mfma_f32_16x16x32_bf16 v[48:51], v[16:19], v[164:167], v[48:51]\n" \
  "v_mfma_f32_16x16x32_bf16 v[54:57], v[16:19], v[228:231], v[54:57]\n" \
  "s_waitcnt lgkmcnt(4)\n" \
  "v_mfma_f32_16x16x32_bf16 v[60:63], v[16:19], v[32:35], v[60:63]\n" \
  "ds_read_b128 v[16:19], v0 offset:26880\n" \
  "ds_read_b128 v[32:35], v1 offset:12288\n" \
  "v_mfma_f32_16x16x32_bf16 v[42:45], v[20:23], v[104:107], v[42:45]\n" \
  "v_mfma_f32_16x16x32_bf16 v[48:51], v[20:23], v[168:171], v[48:51]\n" \
  "v_mfma_f32_16x16x32_bf16 v[54:57], v[20:23], v[232:235], v[54:57]\n" \
  "s_waitcnt lgkmcnt(4)\n" \
  "v_mfma_f32_16x16x32_bf16 v[60:63], v[20:23], v[36:39], v[60:63]\n" \
  "ds_read_b128 v[20:23], v0 offset:28928\n" \
  "ds_read_b128 v[36:39], v1 offset:13312\n" \
  "v_mfma_f32_16x16x32_bf16 v[42:45], v[24:27], v[108:111], v[42:45]\n" \
  "v_mfma_f32_16x16x32_bf16 v[48:51], v[24:27], v[172:175], v[48:51]\n" \
  "v_mfma_f32_16x16x32_bf16 v[54:57], v[24:27], v[236:239], v[54:57]\n" \
  "s_waitcnt lgkmcnt(4)\n" \
  "v_mfma_f32_16x16x32_bf16 v[60:63], v[24:27], v[28:31], v[60:63]\n" \
  "ds_read_b128 v[24:27], v0 offset:30976\n" \
  "ds_read_b128 v[28:31], v1 offset:14336\n" \
  "v_mfma_f32_16x16x32_bf16 v[42:45], v[12:15], v[112:115], v[42:45]\n" \
  "v_mfma_f32_16x16x32_bf16 v[48:51], v[12:15], v[176:179], v[48:51]\n" \
  "v_mfma_f32_16x16x32_bf16 v[54:57], v[12:15], v[240:243], v[54:57]\n" \
  "s_waitcnt lgkmcnt(4)\n" \
  "v_mfma_f32_16x16x32_bf16 v[60:63], v[12:15], v[32:35], v[60:63]\n" \
  "ds_read_b128 v[32:35], v1 offset:15360\n" \
  "v_mfma_f32_16x16x32_bf16 v[42:45], v[16:19], v[116:119], v[42:45]\n" \
  "v_mfma_f32_16x16x32_bf16 v[48:51], v[16:19], v[180:183], v[48:51]\n" \
  "v_mfma_f32_16x16x32_bf16 v[54:57], v[16:19], v[244:247], v[54:57]\n" \
  "s_waitcnt lgkmcnt(3)\n" \
  "v_mfma_f32_16x16x32_bf16 v[60:63], v[16:19], v[36:39], v[60:63]\n" \
  "ds_read_b128 v[36:39], v3 offset:64\n" \
  "v_mfma_f32_16x16x32_bf16 v[42:45], v[20:23], v[120:123], v[42:45]\n" \
  "v_mfma_f32_16x16x32_bf16 v[48:51], v[20:23], v[184:187], v[48:51]\n" \
  "v_mfma_f32_16x16x32_bf16 v[54:57], v[20:23], v[248:251], v[54:57]\n" \
  "s_waitcnt lgkmcnt(2)\n" \
  "v_mfma_f32_16x16x32_bf16 v[60:63], v[20:23], v[28:31], v[60:63]\n" \
  "ds_read_b128 v[28:31], v3 offset:192\n" \
  "s_mov_b64 exec, 1\n" \
  "ds_add_u32 v6, v7 offset:8\n" \
  "s_mov_b64 exec, -1\n" \
  "v_xor_b32 v6, 4, v6\n" \
  "v_mfma_f32_16x16x32_bf16 v[42:45], v[24:27], v[124:127], v[42:45]\n" \
  "v_mfma_f32_16x16x32_bf16 v[48:51], v[24:27], v[188:191], v[48:51]\n" \
  "v_mfma_f32_16x16x32_bf16 v[54:57], v[24:27], v[252:255], v[54:57]\n" \
  "s_waitcnt lgkmcnt(3)\n" \
  "v_mfma_f32_16x16x32_bf16 v[60:63], v[24:27], v[32:35], v[60:63]\n" \
  "s_waitcnt lgkmcnt(1)\n" \
  "s_nop 4\n" \
  "ds_read_b32 v5, v6 offset:0\n" \
  "v_fma_f32 v42, -v36, v8, v42\n" \
  "v_fma_f32 v43, -v37, v8, v43\n" \
  "v_fma_f32 v44, -v38, v8, v44\n" \
  "v_fma_f32 v45, -v39, v8, v45\n" \
  "v_mul_f32 v42, v42, v28\n" \
  "v_mul_f32 v43, v43, v29\n" \
  "v_mul_f32 v44, v44, v30\n" \
  "v_mul_f32 v45, v45, v31\n" \
  "v_cvt_pk_bf16_f32 v42, v42, v43\n" \
  "v_cvt_pk_bf16_f32 v43, v44, v45\n" \
  "buffer_store_dwordx4 v[40:43], v2, s[40:43], s65 offen offset:0 sc1\n" \
  "v_fma_f32 v48, -v36, v9, v48\n" \
  "v_fma_f32 v49, -v37, v9, v49\n" \
  "v_fma_f32 v50, -v38, v9, v50\n" \
  "v_fma_f32 v51, -v39, v9, v51\n" \
  "v_mul_f32 v48, v48, v28\n" \
  "v_mul_f32 v49, v49, v29\n" \
  "v_mul_f32 v50, v50, v30\n" \
  "v_mul_f32 v51, v51, v31\n" \
  "v_cvt_pk_bf16_f32 v48, v48, v49\n" \
  "v_cvt_pk_bf16_f32 v49, v50, v51\n" \
  "buffer_store_dwordx4 v[46:49], v2, s[40:43], s65 offen offset:1024 sc1\n" \
  "v_fma_f32 v54, -v36, v10, v54\n" \
  "v_fma_f32 v55, -v37, v10, v55\n" \
  "v_fma_f32 v56, -v38, v10, v56\n" \
  "v_fma_f32 v57, -v39, v10, v57\n" \
  "v_mul_f32 v54, v54, v28\n" \
  "v_mul_f32 v55, v55, v29\n" \
  "v_mul_f32 v56, v56, v30\n" \
  "v_mul_f32 v57, v57, v31\n" \
  "v_cvt_pk_bf16_f32 v54, v54, v55\n" \
  "v_cvt_pk_bf16_f32 v55, v56, v57\n" \
  "buffer_store_dwordx4 v[52:55], v2, s[40:43], s65 offen offset:2048 sc1\n" \
  "v_fma_f32 v60, -v36, v4, v60\n" \
  "v_fma_f32 v61, -v37, v4, v61\n" \
  "v_fma_f32 v62, -v38, v4, v62\n" \
  "v_fma_f32 v63, -v39, v4, v63\n" \
  "v_mul_f32 v60, v60, v28\n" \
  "v_mul_f32 v61, v61, v29\n" \
  "v_mul_f32 v62, v62, v30\n" \
  "v_mul_f32 v63, v63, v31\n" \
  "v_cvt_pk_bf16_f32 v60, v60, v61\n" \
  "v_cvt_pk_bf16_f32 v61, v62, v63\n" \
  "buffer_store_dwordx4 v[58:61], v2, s[40:43], s65 offen offset:3072 sc1\n" \
  "s_waitcnt lgkmcnt(0)\n" \
  "s_add_u32 s65, s65, 0x40000\n" \
  "s_sub_u32 s64, s64, 1\n" \
  "s_mov_b32 s68, 1\n" \
  "s_cmp_lg_u32 s64, 0\n" \
  "s_cbranch_scc1 .LCW_TOP_%=\n" \
  "s_waitcnt vmcnt(0)\n" \
  "s_mov_b64 exec, 1\n" \
  "buffer_store_dword v7, off, s[56:59], s72 sc1\n" \
  "s_mov_b64 exec, -1\n" \
  "s_add_u32 s72, s72, 0x1000\n" \
  "s_mov_b32 s68, 0\n" \
  ".LCW_END_%=:\n" \
  "s_endpgm\n"
constexpr int RG = 2;
#define RFL(x) __builtin_amdgcn_readfirstlane(x)
#define HALF(x, i) BF8(((i) ? __builtin_shufflevector(x, x, 4, 5, 6, 7) : __builtin_shufflevector(x, x, 0, 1, 2, 3)))
constexpr int P_W = 65536, P_ST = 131072, P_SS = P_ST + 1024, P_CN = P_SS + 512;
template <bool PR>
__device__ __forceinline__ void role_proj_t(const MegaArgs& A, char* smem, const int L, const int idx, const bool kv) {
  const int w = A.wv;
  const int l = lane_id();
  const int c = l & 15, g = l >> 4;
  char* ws = A.ws;
  unsigned* FL = (unsigned*)(ws + OFF_FL);
  unsigned* TMO = FL + FL_TMO;
  const unsigned sb = lds_base(smem);
  auto cn = (volatile __attribute__((address_space(3))) unsigned*)(unsigned long)(sb + P_CN);
  u64 tag;
  {
    const int j = w & 3;
    const bool vt = kv && j >= 2;
    constexpr int NT = PR ? 3 : 1;
    v8u wq[NT][8];
    const u16* Wm = (kv ? (PR ? A.Wv : A.Wk) : A.Wq) - (PR ? 128 : 0) * HD;
    const int hm = kv ? idx : 2 * idx + PR;
    const int q = (l >> 2) & 3, p = l & 3, f = q | ((g & 1) << 2);
    unsigned ra[NT];
#pragma unroll
    for (int m = 0; m < NT; ++m) {
      const int ct = PR ? m : 3;
      ra[m] = sb + (j >> 1) * 65536 + (8 * g + q) * 256 + ((4 * (j & 1) + (vt ? ct : (ct & 2) + (p >> 1))) ^ f) * 32 + (vt ? p * 8 : (p & 1) * 16 + (ct & 1) * 8);
    }
#define W_ISSUE(n) wqkv_issue<false>(Wm + (128 * (n) - 256 * ((n) & 1)) * HD, (n) & 1, hm, w, l, sb)
#define W_STAGE(n, WAIT, NEXT)                                                                             \
  {                                                                                                        \
    WAIT;                                                                                                  \
    RAW_BAR();                                                                                             \
    _Pragma("unroll") for (int m = 0; m < NT; ++m) {                                                       \
      _Pragma("unroll") for (int t = 0; t < 2; ++t) {                                                      \
        const unsigned a_ = ra[m] + ((n) & 1) * 8192 + t * 32768;                                          \
        const v2u r0 = trd(a_), r1 = trd(a_ + 1024), r2 = trd(a_ + 16384), r3 = trd(a_ + 16384 + 1024);    \
        wq[m][2 * (n) + t] = v8u{r0.x, r0.y, r1.x, r1.y, r2.x, r2.y, r3.x, r3.y};                          \
      }                                                                                                    \
    }                                                                                                      \
    _Pragma("unroll") for (int m = 0; m < 2 * NT; ++m) asm volatile("" : "+v"(wq[m >> 1][2 * (n) + (m & 1)])); \
    RAW_BAR();                                                                                             \
    NEXT;                                                                                                  \
    _Pragma("unroll") for (int m = 0; m < 4 * NT; ++m)                                                     \
      cs[m % NT] = MFMA16(HALF(wq[m % NT][2 * (n) + m / NT / 2], (m / NT) & 1), BF8(one), cs[m % NT]);     \
    _Pragma("unroll") for (int m = 0; m < NT; ++m) asm volatile("" : "+v"(cs[m]));                         \
  }
    __builtin_amdgcn_s_waitcnt(0x0f70);
    W_ISSUE(0);
    W_ISSUE(1);
    tag = call_tag(A);
    if (w == 0 && l < 8) cn[l] = l == 1 ? 3u : 0u;
    const v4u one = {0x3f803f80u, 0x3f803f80u, 0x3f803f80u, 0x3f803f80u};
    f32x4 cs[NT] = {};
    W_STAGE(0, WAIT_VM(8), W_ISSUE(2));
    SST(4);
    W_STAGE(1, WAIT_VM(8), W_ISSUE(3));
    SST(5);
    W_STAGE(2, WAIT_VM(8), (void)0);
    SST(6);
    ready_wait(A, tag, ready_look(A));
    W_STAGE(3, WAIT_VM(0), if (!PR) {
      _Pragma("unroll") for (int ks = 0; ks < 16; ++ks)
        *reinterpret_cast<bf16x8*>(smem + P_W + j * 16384 + ks * 1024 + l * 16) = HALF(wq[0][ks >> 1], ks & 1);
    });
    SST(7);
#undef W_STAGE
#undef W_ISSUE
#pragma unroll
    for (int m = 0; m < NT; ++m) *reinterpret_cast<f32x4*>(smem + P_ST + j * 256 + (PR ? m : 3) * 64 + g * 16) = cs[m];
    RAW_BAR();
    SST(8);
    if constexpr (PR) {
      const v4u rsO = mk_srd(ws + RFL((unsigned)(vt ? OFF_VF + L * SZ_O : OFF_QK + L * SZ_QK)), RFL((unsigned)(vt ? SZ_O : SZ_QK)));
      const v4u rsP = mk_srd(FL + RFL(kv ? FL_FQ + (L * 64 * 32 + idx) * 32 + 2 * j : FL_FP + (L * 16 + idx) * 32 + j), 63 * 4096 + 4);
      const int so = RFL(vt ? idx * 8 + 4 * (j & 1) : (kv ? idx * 16 + 8 : (2 * idx + (j >> 1)) * 16) + 2 * (j & 1));
      const v8u pv = {sb + 32768 + g * 512 + c * 16, sb + P_W + j * 16384 + l * 16, (unsigned)l * 16, sb + P_SS + 256 + (vt ? g * 16 : c * 4),
                      sb + P_ST + j * 256 + (vt ? c * 4 : g * 16), 0u, 0u, 0u};
      const char* dbg = nullptr;
#define P_OPS                                                                                                                               \
  ::"{v[64:71]}"(wq[0][0]), "{v[72:79]}"(wq[0][1]), "{v[80:87]}"(wq[0][2]), "{v[88:95]}"(wq[0][3]), "{v[96:103]}"(wq[0][4]),                \
      "{v[104:111]}"(wq[0][5]), "{v[112:119]}"(wq[0][6]), "{v[120:127]}"(wq[0][7]), "{v[128:135]}"(wq[1][0]), "{v[136:143]}"(wq[1][1]),     \
      "{v[144:151]}"(wq[1][2]), "{v[152:159]}"(wq[1][3]), "{v[160:167]}"(wq[1][4]), "{v[168:175]}"(wq[1][5]), "{v[176:183]}"(wq[1][6]),     \
      "{v[184:191]}"(wq[1][7]), "{v[192:199]}"(wq[2][0]), "{v[200:207]}"(wq[2][1]), "{v[208:215]}"(wq[2][2]), "{v[216:223]}"(wq[2][3]),     \
      "{v[224:231]}"(wq[2][4]), "{v[232:239]}"(wq[2][5]), "{v[240:247]}"(wq[2][6]), "{v[248:255]}"(wq[2][7]), "{v[0:7]}"(pv),               \
      "{s[40:43]}"(rsO), "{s[56:59]}"(rsP), "{s64}"(NSEG), "{s65}"(so * 1024), "{s70}"(sb + P_CN), "{s[36:37]}"(dbg)                        \
      : "memory", "vcc", "scc", "s80", "v63"
      if (vt) asm volatile("s_mov_b64 exec, -1\ns_waitcnt vmcnt(0) lgkmcnt(0)\ns_nop 4\n" V_ASM P_OPS);
      else asm volatile("s_mov_b64 exec, -1\ns_waitcnt vmcnt(0) lgkmcnt(0)\ns_nop 4\n" P_ASM P_OPS);
#undef P_OPS
      return;
    }
  }
  __builtin_amdgcn_s_setprio(RPRIO);
  const bool h4 = kv && L;
  if (h4 || w >= 2) {
    const char* Xb = ws + (L ? OFF_X3 : OFF_XA);
    const __amdgpu_buffer_rsrc_t rsS = MKRS(ws + OFF_H + L * (SZ_X / 2), SZ_X / 2);
    const __amdgpu_buffer_rsrc_t rsM = MKRS(ws + OFF_MEMK + L * SZ_MEM, kv && w == 2 ? 3 * SZ_MEM : 0);
    const int b0 = RFL(h4 ? 3 * w : w == 2 ? 0 : HX0), b1 = RFL(h4 ? (w == 2 ? 8 : 3 * w + 3 * (w < 2)) : w == 2 ? HX0 : 8);
    const __amdgpu_buffer_rsrc_t rsF = MKRS(FL + FL_FC + L * 64 * 32 * 32, 64 * 4096);
    const int fo = ((blockIdx.x & 3) * 8 + (w == 3 ? l >> 3 : min(b0 + (l >> 3), b1 - 1))) * 128 + (l & 7) * 4;
#define FC_LOOK(s) __builtin_amdgcn_raw_buffer_load_b32(rsF, fo, (s) * 4096, AUX_SC1)
#define MEM_CLEAR(s)                                                                                                          \
  _Pragma("unroll") for (int i = 0; i < 4; ++i)                                                                               \
    ST128((v4u{0u, 0u, 0u, 0u}), rsM, l * 16 + (i & 1) * 1024, (s) * 65536 + idx * 2048 + (i >> 1) * (int)(OFF_MEMV - OFF_MEMK));
    if (kv && w == 2) { MEM_CLEAR(0) }
    unsigned tn = now10(), nf = 0u, tg = tn;
    for (int s = 0; s < NSEG; ++s) {
      for (unsigned spins = 0; !__all(nf != 0u); ++spins) {
        if (spin_timeout(spins, TMO)) return;
        if (spins) __builtin_amdgcn_s_sleep(SLP_H);
        else if (GF_H && (h4 || (GF_P && L)) && s >= GF0 && s < NSEG - 6) gate(tg);
        tg = now10() + GF_H;
        nf = FC_LOOK(s);
        asm volatile("" ::: "memory");
      }
      TS(4, s, 128);
      for (unsigned spins = 0; cn[2 + (s & 1)] < 4u * (s >> 1); ++spins) {
        if (spins > 64u * SPIN_CAP) return;
        __builtin_amdgcn_s_sleep(2);
      }
      if (L == 0 && s >= PACE0) gate(tn);
      else tn = now10();
      tn += PACE;
      TS(5, s, 128);
      for (int i = b0; i < b1; ++i) glds64c(Xb + RFL(s * 32768 + i * 4096), l * 16, RFL(sb + (s & 1) * 32768 + i * 4096));
      if (w != 3) {
        nf = L ? 0u : FC_LOOK(s + 1);
        if (kv && w == 2 && s < NSEG - 1) {
          MEM_CLEAR(s + 1)
          WAIT_VM(4);
        } else {
          WAIT_VM(0);
        }
        if (l == 0) __hip_atomic_fetch_add((__attribute__((address_space(3))) unsigned*)cn + (s & 1), h4 ? 2u : 3u, __ATOMIC_RELAXED, __HIP_MEMORY_SCOPE_WORKGROUP);
        TS(6, s, 128);
      } else {
        v4u t[16];
#pragma unroll
        for (int i = 0; i < 16; ++i) t[i] = LD128(rsS, l * 16, s * 16384 + i * 1024);
        WAIT_VM(16);
        nf = L ? 0u : FC_LOOK(s + 1);
        if (l == 0 && !h4) __hip_atomic_fetch_add((__attribute__((address_space(3))) unsigned*)cn + (s & 1), 3u, __ATOMIC_RELAXED, __HIP_MEMORY_SCOPE_WORKGROUP);
        float a = 0.f, b = 0.f, qa = 0.f, qb = 0.f;
#pragma unroll
        for (int i = 0; i < 16; ++i) {
          a = one(a + __uint_as_float(t[i].x));
          b = one(b + __uint_as_float(t[i].z));
          qa = one(qa + __uint_as_float(t[i].y));
          qb = one(qb + __uint_as_float(t[i].w));
        }
        a = xsum32(xsum16(a));
        b = xsum32(xsum16(b));
        float da = 0.f, db = 0.f;
#pragma unroll
        for (int i = 0; i < 16; ++i) {
          const float u = one(__uint_as_float(t[i].x) - a * (1.0f / 64)), v = one(__uint_as_float(t[i].z) - b * (1.0f / 64));
          da = one(da + u * u);
          db = one(db + v * v);
        }
        const float var = xsum32(swap_add16(qa + da * 0.125f, qb + db * 0.125f)) * (1.0f / DM);
        const float ri = 1.0f / bfr1(sqrtf(bfr1(bfr1(var) + 1e-5f)));
        reinterpret_cast<float*>(smem + P_SS)[(s & 1) * 64 + (g >> 1) * 32 + 2 * c + (g & 1)] = g < 2 ? bfr1(((g & 1) ? b : a) * (1.0f / DM)) : ri;
        asm volatile("" ::: "memory");
        if (l == 0) cn[4 + (s & 1)] = s + 1;
        TS(7, s, 192);
      }
    }
    return;
  }
  const int RL = !kv, rp = idx & 15, u = RFL(kv ? idx >> 4 : L);
  const __amdgpu_buffer_rsrc_t rsW = MKRS(ws + OFF_PW, SZ_PW);
  const __amdgpu_buffer_rsrc_t rsX = MKRS(ws + (RL ? OFF_X3 : OFF_XA), SZ_X);
  const __amdgpu_buffer_rsrc_t rsY = RL ? MKRS(A.out, SZ_X) : MKRS(ws + OFF_X2, SZ_X);
  const int r = 2 * rp + (g >> 1), yo = r * (DM * 2) + (l & 31) * 16 + u * 512;
  if (kv) {
    for (int s = w; s < NSEG; s += RG) ST128((v4u{0u, 0u, 0u, 0u}), rsY, yo, s * SEG * DM * 2);
    DRAIN();
    if (l < 8) set_flag(FL + FL_FZ + l * 64 + 2 * idx + w);
  }
  v4u sel;
#pragma unroll
  for (int e = 0; e < 4; ++e) sel[e] = (g >> 1) == (c >> 3) && ((c & 7) >> 1) == e ? 0x3f80u << (16 * (c & 1)) : 0u;
  unsigned tl = now10();
  for (int s = w; s < NSEG; s += RG) {
    if (GF_R) {
      gate(tl);
      const gu32* f = (gu32*)(FL + FL_FW) + ((RL * 64 + s) * 4 + (blockIdx.x & 3)) * 4 * 32 + (c >> 2) * 32 + (c & 3);
      for (unsigned spins = 0;; ++spins) {
        tl = now10() + GF_R;
        if (__all(__hip_atomic_load(f, RLX_AGENT) != 0u) || spin_timeout(spins, TMO)) break;
        __builtin_amdgcn_s_sleep(SLP_R);
      }
      asm volatile("" ::: "memory");
    } else
    grp_wait<SLP_R, 4, GATE_R>(true, nullptr, 0, FL + FL_FW + ((RL * 64 + s) * 4 + (blockIdx.x & 3)) * 4 * 32, 16, TMO, tl);
    if (s >= NSEG - 6) tl = now10();
    TS(0, s, w * 64);
    v4u p[8][2];
    const v4u xr = LD128(rsX, (l & 31) * 512 + r * 16, s * 32768 + u * 16384);
#pragma unroll
    for (int k = 0; k < 8; ++k)
#pragma unroll
      for (int b = 0; b < 2; ++b)
        p[k][b] = LD128(rsW, (g & 1) * 32768 + (c >> 2) * 128 + (g >> 1) * 64 + (c & 3) * 16 + b * 512, ((RL * 64 + s) * 16 + 2 * k) * 32768 + rp * 2048 + u * 1024);
    __builtin_amdgcn_sched_barrier(0);
    f32x4 ac[2] = {};
#pragma unroll
    for (int k = 0; k < 8; ++k)
#pragma unroll
      for (int b = 0; b < 2; ++b) ac[b] = MFMA16(BF8(sel), BF8(p[k][b]), ac[b]);
    v4u xo;
    {
      const auto x = __builtin_amdgcn_permlane16_swap(pk2s(ac[0][0], ac[0][1]), pk2s(ac[1][0], ac[1][1]), false, false);
      const auto y = __builtin_amdgcn_permlane16_swap(pk2s(ac[0][2], ac[0][3]), pk2s(ac[1][2], ac[1][3]), false, false);
      const v4u a = {x[0], y[0], x[1], y[1]};
#pragma unroll
      for (int e = 0; e < 4; ++e) {
        xo[e] = pk2s(lo16(xr[e]) + lo16(a[e]), hi16(xr[e]) + hi16(a[e]));
        if (kv) xo[e] = xo[e] ? xo[e] : 0x8000u;
      }
      ST128(xo, rsY, yo, s * SEG * DM * 2);
    }
    TS(1, s, w * 64);
    if (kv) {
      const float pv = lo16(g < 2 ? __builtin_amdgcn_readlane(xo[0], 0) : __builtin_amdgcn_readlane(xo[0], 32));
      float sm = 0.f, q0 = 0.f, q1 = 0.f;
#pragma unroll
      for (int e = 0; e < 4; ++e) {
        const float a = one(lo16(xo[e]) - pv), b = one(hi16(xo[e]) - pv);
        sm = one(sm + one(a + b));
        q0 = one(q0 + a * a);
        q1 = one(q1 + b * b);
      }
      sm = half_sum(sm);
      const float q = fmaxf(one(half_sum(q0 + q1)) - sm * sm * (1.0f / 256), 0.f);
      const u64 st = (u64)__float_as_uint(sm + pv * 256.f) | (u64)(__float_as_uint(q) | 1u) << 32;
      if ((l & 31) < 4) __hip_atomic_store((gu64*)(FL + FL_FR + ((s * 4 + (l & 3)) * 4 + (r >> 3)) * 32 + (r & 7) * 4 + 2 * u), st, RLX_AGENT);
    }
    TS(3, s, w * 64);
  }
  if (L == 1 && idx == 0 && w == 0 && l == 0) __hip_atomic_store((gu64*)(ws + OFF_EP), tag, RLX_AGENT);
}
__device__ __forceinline__ void role_proj(const MegaArgs& A, char* smem, const int L, const int idx, const bool kv) {
  if (A.wv < 4) role_proj_t<false>(A, smem, L, idx, kv);
  else role_proj_t<true>(A, smem, L, idx, kv);
}

constexpr int CUN = 2144;
__device__ __forceinline__ v2u part_stats(const v4u o) {
  float v[8];
#pragma unroll
  for (int e = 0; e < 4; ++e) { v[2 * e] = lo16(o[e]); v[2 * e + 1] = hi16(o[e]); }
  const float s = ((v[0] + v[1]) + (v[2] + v[3])) + ((v[4] + v[5]) + (v[6] + v[7])), m = s * 0.125f;
  float q0 = 0.f, q1 = 0.f;
#pragma unroll
  for (int e = 0; e < 4; ++e) {
    const float a = v[e] - m, b = v[4 + e] - m;
    q0 += a * a;
    q1 += b * b;
  }
  return v2u{__float_as_uint(s), __float_as_uint(q0 + q1)};
}
constexpr int CRN = 176;
constexpr int CV_LDS = (8 * 2 * CKN + 8 * 2 * CUN + 64 + 2 * 32 * 8 + 16 * CRN + 64 + 256 * 8) * 2 + 8 * 1568;
__device__ __forceinline__ void role_conv(const MegaArgs& A, char* smem, const int cv) {
  u16* Kr = reinterpret_cast<u16*>(smem);
  u16* Uc = Kr + 8 * 2 * CKN;
  u16* Us = Uc + 8 * CUN + 63;
  u16* To = Uc + 8 * 2 * CUN + 64;
  u16* Sx = To + 32 * 8;
  u16* Rc = Sx + 32 * 8;
  u16* Rs = Rc + 8 * CRN + 63;
  u16* Ta = Rc + 16 * CRN + 64;
  const int w = A.wv;
  const int l = lane_id(), tid = 64 * w + l, c = l & 15, g = l >> 4;
  const int d = 8 * cv + w;
  char* ws = A.ws;
  unsigned* FL = (unsigned*)(ws + OFF_FL);
  unsigned* TMO = FL + FL_TMO;
  u16* kr = Kr + w * 2 * CKN;
  const u16* ka = kr + (c >> 3) * CKN + 2080 - 16 * (c & 7) + 8 * g;
#define KFRAG(D0) (*reinterpret_cast<const bf16x8*>(ka - (D0)))
  typedef __attribute__((address_space(3))) const unsigned* lpu;
#define BFRAG(a) BF8((v4u{((lpu)(unsigned long)(a))[0], ((lpu)(unsigned long)(a))[1], ((lpu)(unsigned long)(a))[2], ((lpu)(unsigned long)(a))[3]}))
  f32x4 acc[16], far[8];
  v4u tx[4];
  const __amdgpu_buffer_rsrc_t rz1 = MKRS(ws + OFF_XA, SZ_X), rps = MKRS(ws + OFF_H, SZ_X);
  u16* uc = Uc + w * CUN;
  const unsigned ub = lds_base(reinterpret_cast<char*>(((c & 1) ? Us : Uc) + w * CUN + c + 8 * g + 32));
  const int t0 = 16 * (4 * (g & 1) + (g >> 1)) + c;
  float dsk;
#define SKIP(T) pk2(bf2f(uc[32 + 128 * (T) + t0]) * dsk, bf2f(uc[64 + 128 * (T) + t0]) * dsk)
#define TILE_OUT(T, a, uu)                                                                     \
  {                                                                                            \
    const unsigned yy = pk2(swap_add32(a[0], a[1]), swap_add32(a[2], a[3]));                   \
    const unsigned v_ = pk2(lo16(yy) + lo16(uu), hi16(yy) + hi16(uu));                         \
    Ta[(128 * ((T) & 7) + t0) * 8 + w] = (u16)v_;                                              \
    Ta[(128 * ((T) & 7) + t0 + 32) * 8 + w] = (u16)(v_ >> 16);                                 \
  }
#define CHUNK_ST(i)                                                                            \
  {                                                                                            \
    const v4u a_ = *reinterpret_cast<const v4u*>(Ta + (64 * w + l + 512 * ((i) & 1)) * 8);     \
    v4u o;                                                                                     \
    _Pragma("unroll") for (int e = 0; e < 4; ++e) o[e] = pk2(lo16(tx[i][e]) + lo16(a_[e]), hi16(tx[i][e]) + hi16(a_[e])); \
    ST128(o, rz1, (l >> 5) * 32768 + (l & 31) * 16, (2 * (w + 8 * (i)) * 64 + cv) * 512);      \
    ST64(part_stats(o), rps, (l >> 5) * 16384 + (l & 31) * 8, (2 * (w + 8 * (i)) * 64 + cv) * 256); \
  }
#define CHUNK_FLAG(i) set_flag(FL + FL_FC + (((2 * w + 16 * ((i) + (l >> 3)) + ((l >> 2) & 1)) * 4 + (l & 3)) * 8 + (cv >> 3)) * 32 + (cv & 7))
  {
    unsigned ta[3], k0p;
    v4u kn;
    u64 tag;
    {
      KGen G;
      G.load(A, d, l);
      const u16 pk = A.Dskip[d];
      asm volatile("" ::: "memory");
      tx[0] = *reinterpret_cast<const v4u*>(A.x0 + (size_t)tid * DM + 8 * cv);
      ask_slow(A, 16 * (w - 4) + (cv & 15), ta);
      tag = call_tag(A);
      G.setup();
      dsk = bf2f(pk);
      G.st = smem + w * 16640;
      SST(0);
      G.columns(l);
      const Cx z = G.power(1.f);
      G.rows(G.st + l * 8, cmul(G.C, z), z, 16);
      kn = G.tile(l);
      const float k0 = 2.0f * wave_sum(G.C.r.x + G.C.r.y);
      k0p = l == 0 ? pk2(k0, k0 - lo16(pk2(k0, k0))) : 0u;
    }
    SST(1);
    const __amdgpu_buffer_rsrc_t rst = MKRS(ws + OFF_GT, L_SEQ * 16);
#define U_VAL(i)                                                                               \
  {                                                                                            \
    const float mb_ = lo16(st[i]), ri_ = 1.0f / hi16(st[i]);                                   \
    _Pragma("unroll") for (int e = 0; e < 4; ++e) {                                            \
      const unsigned t_ = pk2(lo16(tx[i][e]) - mb_, hi16(tx[i][e]) - mb_);                     \
      uv[e] = pk2(lo16(t_) * ri_, hi16(t_) * ri_);                                             \
    }                                                                                          \
  }
#define U_PUT(i)                                                                               \
  _Pragma("unroll") for (int e = 0; e < 4; ++e) {                                              \
    ((lph)(unsigned long)up0)[(2 * e) * CUN + 512 * (i)] = (u16)uv[e];                         \
    ((lph)(unsigned long)up0)[(2 * e + 1) * CUN + 512 * (i)] = (u16)(uv[e] >> 16);             \
    ((lph)(unsigned long)up1)[(2 * e) * CUN + 512 * (i)] = (u16)uv[e];                         \
    ((lph)(unsigned long)up1)[(2 * e + 1) * CUN + 512 * (i)] = (u16)(uv[e] >> 16);             \
  }
#define U_ROW(i) { U_VAL(i) U_PUT(i) }
    typedef __attribute__((address_space(3))) u16* lph;
    unsigned st[4], up0 = lds_base((char*)(Uc + 32 + tid)), up1 = lds_base((char*)(Us + 32 + tid));
    asm volatile("" : "+v"(up0), "+v"(up1), "+v"(kn), "+v"(k0p));
    v4u uv;
#define ST_GET(I0, I1)                                                                                                        \
  for (unsigned spins = 0; spins < SPIN_CAP; ++spins) {                                                                       \
    bool ok = true;                                                                                                           \
    v4u t_[4];                                                                                                                \
    _Pragma("unroll") for (int i = I0; i < I1; ++i) t_[i] = LD128(rst, (tid + 512 * i) * 16, 0);                              \
    _Pragma("unroll") for (int i = I0; i < I1; ++i) {                                                                         \
      ok = ok && has_tag(tag, t_[i].x, t_[i].y) && has_tag(tag, t_[i].z, t_[i].w);                                            \
      st[i] = __builtin_amdgcn_perm(t_[i].w, t_[i].y, 0x07060302);                                                            \
    }                                                                                                                         \
    if (__all(ok)) break;                                                                                                     \
    __builtin_amdgcn_s_sleep(4);                                                                                              \
    asm volatile("" ::: "memory");                                                                                            \
  }
    if (w < 4) {
      ST_GET(0, 2);
      U_VAL(0);
      asm volatile("" : "+v"(uv));
    }
#pragma unroll
    for (int i = 1; i < 4; ++i) tx[i] = *reinterpret_cast<const v4u*>(A.x0 + (size_t)(tid + 512 * i) * DM + 8 * cv);
    RAW_BAR();
    SST(2);
    if (w < 4) { U_PUT(0) }
    *reinterpret_cast<v2u*>(kr + 2076 - 16 * c - 4 * g) = v2u{__builtin_amdgcn_perm(kn.z, kn.w, 0x05040100), __builtin_amdgcn_perm(kn.x, kn.y, 0x05040100)};
    *reinterpret_cast<v2u*>(kr + CKN + 2076 - 16 * c - 4 * g) = v2u{__builtin_amdgcn_perm(kn.z, kn.w, 0x07060302), __builtin_amdgcn_perm(kn.x, kn.y, 0x07060302)};
    if (l < 18) {
      reinterpret_cast<v4u*>(kr)[l < 16 ? 260 + l : 210 + l] = v4u{k0p & 0xffffu, 0u, 0u, 0u};
      reinterpret_cast<v4u*>(kr + CKN)[l < 16 ? 260 + l : 210 + l] = v4u{k0p >> 16, 0u, 0u, 0u};
    }
    (l < 32 ? uc : Us + w * CUN - 32)[l] = 0;
    uc[32 + L_SEQ + l] = Us[w * CUN + 32 + L_SEQ + l] = 0;
    uc[32 + 256 + l] = Us[w * CUN + 32 + 256 + l] = 0;
    RAW_BAR();
    SST(11);
    {
      f32x4 a0 = {}, a1 = {}, a2 = {};
      bf16x8 bf[9], af[9];
#pragma unroll
      for (int b = 0; b < 9; ++b) {
        af[(b + 4) % 9] = KFRAG(160 - 32 * ((b + 4) % 9));
        bf[b] = BFRAG(ub + 64 * (b - 1));
      }
      const unsigned k0 = SKIP(0), k1 = SKIP(1);
      __builtin_amdgcn_sched_barrier(0);
#pragma unroll
      for (int b = 0; b < 5; ++b) a0 = MFMA16(af[b + 4], bf[b], a0);
#pragma unroll
      for (int b = 0; b < 4; ++b) {
        a2 = MFMA16(af[b + 4], bf[b + 4], a2);
        a1 = MFMA16(af[b], bf[b], a1);
      }
      a2 = MFMA16(af[8], bf[8], a2);
      TILE_OUT(0, a0, k0);
      a1 += a2;
      TILE_OUT(1, a1, k1);
    }
    const u64 rv = ready_look(A);
    RAW_BAR();
    SST(3);
    if (w < 4) {
      CHUNK_ST(0);
      asm volatile("" : "+v"(tx[1]));
      U_ROW(1);
      DRAIN();
      ready_wait(A, tag, rv);
      if (l < 8) CHUNK_FLAG(0);
      ST_GET(2, 4);
    } else {
      ST_GET(0, 4);
      ready_wait(A, tag, rv);
      asm volatile("" : "+v"(tx[0]), "+v"(tx[1]));
      U_ROW(0);
      U_ROW(1);
    }
    ASKED(ta);
    if (tid < (16 * CRN + 64) / 8) reinterpret_cast<v4u*>(Rc)[tid] = v4u{0u, 0u, 0u, 0u};
    SST(4);
    const gu64* fk = (gu64*)(ws + OFF_KG) + cv * 16 + w;
    u64 kg = __hip_atomic_load(fk, RLX_AGENT);
    U_ROW(2);
    for (unsigned spins = 0; !__all(kg == tag) && spins < SPIN_CAP; ++spins) {
      __builtin_amdgcn_s_sleep(8);
      kg = __hip_atomic_load(fk, RLX_AGENT);
    }
    asm volatile("" : "+v"(tx[3]), "+v"(st[3])::"memory");
    SST(5);
    const unsigned* kd = reinterpret_cast<const unsigned*>(Ta + 256 * 8) + w * 392;
    glds16c(ws + OFF_KF + d * 2048, l * 16, lds_base((char*)kd));
    if (l < 34) glds16c(ws + OFF_KF + d * 2048 + 1024, l * 16, lds_base((char*)kd) + 1024);
    U_ROW(3);
    DRAIN();
    SST(6);
#pragma unroll 1
    for (int un = l; un < 228; un += 64) {
      const int kc = un < 132 ? 2 : un < 196 ? 1 : 0;
      v4u oh, ol;
#pragma unroll
      for (int e = 0; e < 4; ++e) {
        unsigned kv[2];
#pragma unroll
        for (int k = 0; k < 2; ++k) {
          const int lag = 2080 - (8 * un + 2 * e + k);
          const int q = (lag + (1 << kc) - 1 + ((lag >> (kc + 1)) & 1)) >> (kc + 1);
          kv[k] = kd[128 * kc - 128 + q];
          if (lag >= L_SEQ) kv[k] = 0u;
        }
        oh[e] = __builtin_amdgcn_perm(kv[1], kv[0], 0x05040100);
        ol[e] = __builtin_amdgcn_perm(kv[1], kv[0], 0x07060302);
      }
      reinterpret_cast<v4u*>(kr)[un] = oh;
      reinterpret_cast<v4u*>(kr + CKN)[un] = ol;
    }
    RAW_BAR();
    SST(7);
    {
      unsigned uq[2] = {ub, ub + 1024};
      bf16x8 bf[8], a[4];
      const bf16x8 bm = BFRAG(ub - 64);
      asm volatile("" : "+v"(uq[0]), "+v"(uq[1]));
#pragma unroll
      for (int j = 1; j < 8; ++j) bf[j] = BFRAG(uq[j >> 2] + 256 * (j & 3));
#pragma unroll
      for (int T = 2; T < 16; ++T) (T < 8 ? acc[T] : far[T & 7]) = MFMA16(KFRAG(128 * T + 32), bm, (f32x4{}));
#pragma unroll
      for (int e = 0; e < 3; ++e) a[e] = KFRAG(128 * e);
      const u16* kq = ka - 128 * 7;
#pragma unroll 1
      for (int r = 0; r < 4; ++r) {
#pragma unroll
        for (int e = 0; e < 8; ++e) {
          a[(e + 3) & 3] = *reinterpret_cast<const bf16x8*>(kq + 128 * (7 - ((e + 3) & 7)) + (e >= 5 ? 32 : 0));
          const int j = (8 - e) & 7;
          bf[j] = BFRAG(uq[j >> 2] + 256 * (j & 3) + (e ? 64 : 0));
          __builtin_amdgcn_sched_barrier(0);
#pragma unroll
          for (int T = 7; T >= (e > 2 ? e : 2); --T) acc[T] = MFMA16(a[e & 3], bf[T - e], acc[T]);
          __builtin_amdgcn_sched_barrier(0);
        }
        kq += 32;
        uq[0] += 64;
        uq[1] += 64;
        asm volatile("" : "+v"(uq[0]), "+v"(uq[1]));
      }
    }
    SST(8);
    {
      unsigned k[8];
#pragma unroll
      for (int T = 2; T < 8; ++T) k[T] = SKIP(T);
#pragma unroll
      for (int T = 2; T < 8; ++T) TILE_OUT(T, acc[T], k[T]);
    }
    RAW_BAR();
    SST(10);
    if (w >= 4) CHUNK_ST(0);
    CHUNK_ST(1);
    if (w == 1) (void)wait_n<SLP_V, 32>(FL + FL_FZ + (blockIdx.x & 7) * 64, 64, TMO);
    DRAIN();
    if (l < 16) CHUNK_FLAG(0);
    SST(9);
#undef U_ROW
#undef U_VAL
#undef U_PUT
#undef ST_GET
  }
#define FAR_HALF(r, J0, J1)                                                                     \
  {                                                                                            \
    bf16x8 bf[16], af[16];                                                                     \
    unsigned u_ = ub + 64 * (r) + 256 * (J0), v_ = u_ + 1024;                                  \
    asm volatile("" : "+v"(u_), "+v"(v_));                                                     \
    _Pragma("unroll") for (int j = (J0); j < (J1); ++j) bf[j] = BFRAG((j - (J0) < 4 ? u_ : v_) + 256 * ((j - (J0)) & 3)); \
    _Pragma("unroll") for (int e = (9 - (J1) > 0 ? 9 - (J1) : 0); e < 16 - (J0); ++e) af[e] = KFRAG(128 * e - 32 * (r)); \
    __builtin_amdgcn_sched_barrier(0);                                                         \
    _Pragma("unroll") for (int j = (J1) - 1; j >= (J0); --j)                                   \
      _Pragma("unroll") for (int T = (j > 8 ? j : 8); T < 16; ++T) far[T - 8] = MFMA16(af[T - j], bf[j], far[T - 8]); \
  }
#define FAR_PIECE(p)                                                                            \
  {                                                                                            \
    int l = tid & 63;                                                                          \
    asm volatile("" : "+v"(l), "+v"(tx[2]), "+v"(tx[3]));                                      \
    if ((p) < 16) {                                                                            \
      const int r_ = (p) >> 2;                                                                 \
      if (((p) & 3) == 0) FAR_HALF(r_, 0, 3) else if (((p) & 3) == 1) FAR_HALF(r_, 3, 6)       \
      else if (((p) & 3) == 2) FAR_HALF(r_, 6, 9) else FAR_HALF(r_, 9, 16)                     \
    } else if ((p) == 16) {                                                                    \
      unsigned k_[8];                                                                          \
      _Pragma("unroll") for (int T = 0; T < 8; ++T) k_[T] = SKIP(8 + T);                       \
      _Pragma("unroll") for (int T = 0; T < 8; ++T) TILE_OUT(T, far[T], k_[T]);                \
    } else if ((p) == 17) {                                                                    \
      CHUNK_ST(2);                                                                             \
    } else if ((p) == 18) {                                                                    \
      CHUNK_ST(3);                                                                             \
    } else {                                                                                   \
      DRAIN();                                                                                 \
      if (l < 16) CHUNK_FLAG(2);                                                               \
    }                                                                                          \
  }
  static_assert(CVPRE <= 16, "the later pieces need the barriers of the segments");
  const unsigned rb = lds_base(reinterpret_cast<char*>(((c & 1) ? Rs : Rc) + w * CRN + c + 8 * g));
#pragma unroll
  for (int j = 0; j < 16; ++j) acc[j] = f32x4{};
  const __amdgpu_buffer_rsrc_t rx = MKRS(ws + OFF_X2, SZ_X), rz = MKRS(ws + OFF_X3, SZ_X);
  const __amdgpu_buffer_rsrc_t rf = MKRS(FL + FL_FR, 64 * 16 * 128);
  v2u st = {0u, 0u}, xx = {0u, 0u};
#define X2_LOAD(ss, far)                                                                                                      \
  {                                                                                                                      \
    xx = LD64(rx, (l >> 1) * (DM * 2) + (l & 1) * 8, (far) ? (int)SZ_X : ((ss) * SEG * DM + 8 * cv) * 2);                 \
    st = LD64(rf, l * 8, ((ss) * 4 + (blockIdx.x & 3)) * 512);                                                           \
  }
  unsigned tl = now10();
  bool late = false;
  for (int s = -CVPRE; s < NSEG; ++s) {
   if (s >= 0) {
    const int q = s & 3;
    if (w == 1) {
      late = __builtin_popcountll(__ballot(st.y != 0u)) >= CVLATE;
      for (unsigned spins = 0; !__all(umin3(xx.x, xx.y, st.y) != 0u); ++spins) {
        if (spin_timeout(spins, TMO)) break;
        if (GF_V && s >= GF0) {
          if (spins) __builtin_amdgcn_s_sleep(SLP_V);
          else if (s < NSEG - 6) gate(tl);
          tl = now10() + GF_V;
          X2_LOAD(s, CVT2 > 64 ? 0 : spins && __builtin_popcountll(__ballot(st.y != 0u)) < CVT2);
          continue;
        }
        if (GATE_V && spins == 0 && !__any(st.y != 0u)) gate(tl);
        if (spins && !__all(st.y != 0u)) __builtin_amdgcn_s_sleep(SLP_V);
        X2_LOAD(s, __builtin_popcountll(__ballot(st.y != 0u)) < CVTHR);
      }
      if (!GF_V || s < GF0) tl = now10() + GATE_V;
      TS(0, s, 64);
      const float s0 = __uint_as_float(st.x), s1 = dppr<0xb1>(s0), m2 = __uint_as_float(st.y);
      const float var = (m2 + dppr<0xb1>(m2) + (s0 - s1) * (s0 - s1) * (1.0f / DM)) * (1.0f / DM);
      const float mb = bfr1((s0 + s1) * (1.0f / DM)), ri = __builtin_amdgcn_rcpf(bfr1(__builtin_amdgcn_sqrtf(bfr1(bfr1(var) + 1e-5f))));
      const unsigned t0 = pk2(lo16(xx.x) - mb, hi16(xx.x) - mb), t1 = pk2(lo16(xx.y) - mb, hi16(xx.y) - mb);
      const unsigned u0 = pk2(lo16(t0) * ri, hi16(t0) * ri), u1 = pk2(lo16(t1) * ri, hi16(t1) * ri);
      u16* rp = Rc + (l & 1) * 4 * CRN + 32 * q + (l >> 1);
#define R_PUT(o) { rp[o] = (u16)u0; rp[(o) + CRN] = (u16)(u0 >> 16); rp[(o) + 2 * CRN] = (u16)u1; rp[(o) + 3 * CRN] = (u16)(u1 >> 16); }
      R_PUT(0) R_PUT(8 * CRN + 63)
      if (q == 0) { R_PUT(128) R_PUT(8 * CRN + 63 + 128) }
#undef R_PUT
      *reinterpret_cast<v2u*>(Sx + l * 4) = xx;
    }
    const u16* kq = ka + 32 * q;
    const bf16x8 a0 = *reinterpret_cast<const bf16x8*>(kq - 32), a1 = *reinterpret_cast<const bf16x8*>(kq);
    __syncthreads();
    TS(1, s, 0);
    const bf16x8 bp = BFRAG(rb + 64 * ((q + 3) & 3)), bc = BFRAG(rb + 64 * q);
    const float u0 = bf2f(Rc[w * CRN + 32 * q + c]), u1 = bf2f(Rc[w * CRN + 32 * q + 16 + c]);
    const float x0 = bf2f(Sx[c * 8 + w]), x1 = bf2f(Sx[(16 + c) * 8 + w]);
    acc[0] = MFMA16(a0, bp, (q == 0 ? acc[1] : acc[0]));
    const f32x4 tmp = MFMA16(a1, bc, acc[0]);
    const unsigned uu = pk2(u0 * dsk, u1 * dsk);
    const unsigned yy = pk2(xsum32((q & 1) ? tmp[2] : tmp[0]), xsum32((q & 1) ? tmp[3] : tmp[1]));
    const unsigned aa = pk2(lo16(yy) + lo16(uu), hi16(yy) + hi16(uu)), oo = pk2(x0 + lo16(aa), x1 + hi16(aa));
    if (g == (q >> 1)) {
      To[c * 8 + w] = (u16)oo;
      To[(16 + c) * 8 + w] = (u16)(oo >> 16);
    }
    __syncthreads();
    if (w == 0 && l < 32) {
      const v4u o = *reinterpret_cast<const v4u*>(To + l * 8);
      ST128(o, rz, l * 16, (s * 64 + cv) * 512);
      ST64(part_stats(o), rps, l * 8, ((64 + s) * 64 + cv) * 256);
    }
    if (w == 1) {
      if (GF_V && s + 1 >= GF0) st.y = 0u;
      else X2_LOAD(s + 1, !late);
    }
    const int left = 15 - (s >> 2);
#define PUSH(J0, SH)                                                                                              \
  if (left >= (J0)) {                                                                                             \
    _Pragma("unroll") for (int j = (J0); j < (J0) + 4 && j < 16; ++j)                                             \
      acc[j] = MFMA16(*reinterpret_cast<const bf16x8*>(kq - 32 - 128 * j), bp, (j + (SH) < 16 ? acc[(j + (SH)) & 15] : f32x4{})); \
  }
    TS(2, s, 0);
    if (w == 0) {
      DRAIN();
      if (l < 4) set_flag(FL + FL_FC + (((64 + s) * 4 + l) * 8 + (cv >> 3)) * 32 + (cv & 7));
    }
    TS(3, s, 0);
    if (q == 0) { PUSH(1, 1) PUSH(5, 1) PUSH(9, 1) PUSH(13, 1) }
    else { PUSH(1, 0) PUSH(5, 0) PUSH(9, 0) PUSH(13, 0) }
#undef PUSH
    MSTAMP();
   }
    const int pc = s < 0 ? s + CVPRE : (s % CVSTEP) ? 20 : CVPRE + s / CVSTEP;
    if (pc < 20) FAR_PIECE(pc);
  }
#undef KFRAG
#undef BFRAG
#undef X2_LOAD
#undef FAR_PIECE
#undef FAR_HALF
#undef TILE_OUT
#undef SKIP
#undef CHUNK_ST
#undef CHUNK_FLAG
}

__device__ __forceinline__ v4u ln_row_packed(const v4u xr) {
  float v[8];
#pragma unroll
  for (int e = 0; e < 4; ++e) { v[2 * e] = lo16(xr[e]); v[2 * e + 1] = hi16(xr[e]); }
  float s = ((v[0] + v[1]) + (v[2] + v[3])) + ((v[4] + v[5]) + (v[6] + v[7]));
  s = wave_sum(s);
  const float mean = s * (1.0f / DM);
  float q = 0.f;
#pragma unroll
  for (int j = 0; j < 8; ++j) { float t = v[j] - mean; q += t * t; }
  q = wave_sum(q);
  const float var = q * (1.0f / DM);
  const float mb = bfr(mean);
  const float sd = bfr(sqrtf(bfr(bfr(var) + 1e-5f)));
  const float ri = 1.0f / sd;
  v4u o;
#pragma unroll
  for (int e = 0; e < 4; ++e) {
    const unsigned t = pk2(v[2 * e] - mb, v[2 * e + 1] - mb);
    const float x0 = lo16(t), x1 = hi16(t);
    float q0 = x0 * ri, q1 = x1 * ri;
    q0 = __builtin_fmaf(__builtin_fmaf(-q0, sd, x0), ri, q0);
    q1 = __builtin_fmaf(__builtin_fmaf(-q1, sd, x1), ri, q1);
    o[e] = pk2(q0, q1);
  }
  return o;
}

__device__ __forceinline__ void ln4_packed(v4u (&x)[4]) {
  f32x2_t v[16];
#pragma unroll
  for (int e = 0; e < 16; ++e) v[e] = f32x2_t{lo16(x[e >> 2][e & 3]), hi16(x[e >> 2][e & 3])};
  const f32x2_t t = (((v[0] + v[1]) + (v[2] + v[3])) + ((v[4] + v[5]) + (v[6] + v[7]))) +
                    (((v[8] + v[9]) + (v[10] + v[11])) + ((v[12] + v[13]) + (v[14] + v[15])));
  const float mean = red_mod4(t.x + t.y) * (1.0f / DM);
  const float mb = bfr1(mean);
  f32x2_t q0 = {0.f, 0.f}, q1 = {0.f, 0.f};
#pragma unroll
  for (int e = 0; e < 16; e += 2) {
    v[e] -= mb;
    v[e + 1] -= mb;
    q0 += v[e] * v[e];
    q1 += v[e + 1] * v[e + 1];
  }
  q0 += q1;
  const float dm = mean - mb;
  const float var = fmaxf(red_mod4(q0.x + q0.y) * (1.0f / DM) - dm * dm, 0.f);
  const float sd = bfr1(sqrtf(bfr1(bfr1(var) + 1e-5f)));
  const float ri = 1.0f / sd;
#pragma unroll
  for (int e = 0; e < 16; ++e) {
    const unsigned t2 = pk2(v[e].x, v[e].y);
    const f32x2_t q = f32x2_t{lo16(t2), hi16(t2)} * ri;
    x[e >> 2][e & 3] = pk2(q.x, q.y);
  }
}

constexpr int MEGA_WGS = 256;
constexpr size_t MEGA_LDS = (size_t)(32 * 4 * 64 * 8 + 8 * SMS + 15 * QS + 128 + 16 * 64 * 8 + 8 * SOS + 512) * 2;
static_assert(MEGA_LDS <= 163840, "lds");
static_assert(MEGA_LDS >= (size_t)AW_CNT + 128 && MEGA_LDS >= (size_t)P_CN + 512, "lds");
static_assert(MEGA_LDS >= (size_t)CV_LDS && 8 * 1568 >= 768 * 16, "lds");
static_assert(MEGA_LDS >= (size_t)2 * 32 * SMS * 2 && MEGA_LDS >= 131072, "lds");
static_assert(MEGA_LDS >= (size_t)8 * 16640, "lds");
__device__ __forceinline__ MegaArgs role_args(const int b, const int wv) {
  auto p = (const __attribute__((address_space(4))) u64*)__builtin_amdgcn_kernarg_segment_ptr();
  asm volatile("" : "+s"(p));
#define KA(i) ((u16*)(__attribute__((address_space(1))) u16*)p[i])
  return MegaArgs{KA(0), KA(1), (char*)KA(2), KA(3), KA(4), KA(5), KA(6), KA(7), KA(8), KA(9), KA(10), KA(11), KA(12), KA(13), b, wv};
#undef KA
}
__global__ __launch_bounds__(512, 2) void mega_kernel(const u16* mem0, const u16* Dskip, char* ws, u16* out, const u16* x0,
                                                      const u16* logAre, const u16* Aim, const u16* Cre, const u16* Cim,
                                                      const u16* logdt, const u16* Wq, const u16* Wk, const u16* Wv,
                                                      const u16* Wo) {
  extern __shared__ __attribute__((aligned(16))) char smem[];
  const int b = vbid(), wv = __builtin_amdgcn_readfirstlane(threadIdx.x >> 6);
#define A role_args(b, wv)
  u64 tag = 0;
  {
    const unsigned t0 = now10();
    if ((b >= 32 && b < 64) || (b >= 200 && b < 232)) start_kgen(A, smem, b < 64 ? b - 32 : b - 168);
    else if (b >= 64 && b < 96) tag = start_stats<4>(A, b - 64);
    else if (b >= 240) (void)start_stats<8>(A, b - 224);
    const unsigned dl = (b >= 32 && b < 64) ? DL_C : (b >= 64 && b < 96) ? DL_A : b >= 240 ? DL_R2 : ((b >= 96 && b < 104) || b >= 232) ? DL_R : (b >= 200 && b < 232) ? DL_Q : 0u;
    while (now10() - t0 < dl) __builtin_amdgcn_s_sleep(16);
  }
  if ((EXP_OFF & 1) && b >= 64 && b < 80) return;
  if ((EXP_OFF & 8) && b >= 200 && b < 232) return;
  if ((EXP_OFF & 16) && b >= 32 && b < 64) return;
  if ((EXP_OFF & 32) && b >= 80 && b < 96) return;
  if (b < 64) role_chain(A, smem, b >> 5, b & 31, 0);
  else if (b < 96) role_aw(A, smem, (b >> 4) & 1, b & 15, tag);
  else if (b >= 104 && b < 168) role_conv(A, smem, b - 104);
  else {
    const bool kv = b >= 168 && b < 232;
    role_proj(A, smem, b >= (kv ? 200 : 240), kv ? (b - 168) & 31 : b & 15, kv);
  }
#undef A
}

extern "C" void kernel_launch(void* const* d_in, const int* in_sizes, int n_in, void* d_out, int out_size, void* d_ws,
                              size_t ws_size, hipStream_t stream) {
  MegaArgs A;
  A.x0 = (const u16*)d_in[0];
  A.logAre = (const u16*)d_in[1];
  A.Aim = (const u16*)d_in[2];
  A.Cre = (const u16*)d_in[3];
  A.Cim = (const u16*)d_in[4];
  A.logdt = (const u16*)d_in[5];
  A.Dskip = (const u16*)d_in[6];
  A.mem0 = (const u16*)d_in[8];
  A.Wq = (const u16*)d_in[9];
  A.Wk = (const u16*)d_in[10];
  A.Wv = (const u16*)d_in[11];
  A.Wo = (const u16*)d_in[12];
  A.ws = (char*)d_ws;
  A.out = (u16*)d_out;

  (void)hipFuncSetAttribute((const void*)mega_kernel, hipFuncAttributeMaxDynamicSharedMemorySize, (int)MEGA_LDS);
  mega_kernel<<<MEGA_WGS, 512, MEGA_LDS, stream>>>(A.mem0, A.Dskip, A.ws, A.out, A.x0, A.logAre, A.Aim, A.Cre, A.Cim, A.logdt,
                                                   A.Wq, A.Wk, A.Wv, A.Wo);
}
```

```cpp
#include <hip/hip_runtime.h>
#include <hip/hip_bf16.h>
#include <stdint.h>

constexpr int L_SEQ = 2048;
constexpr int DM    = 512;
constexpr int NST   = 128;
constexpr int NH    = 32;
constexpr int DH    = 128;
constexpr int HD    = NH * DH;
constexpr int MM    = 8;
constexpr int SEG   = 32;
constexpr int NSEG  = L_SEQ / SEG;
constexpr int LK    = MM + SEG;

typedef unsigned short u16;
using bf16x8 = __attribute__((ext_vector_type(8))) short;
using f32x4  = __attribute__((ext_vector_type(4))) float;
using f32x16 = __attribute__((ext_vector_type(16))) float;

static __device__ __forceinline__ u16 f2bf(float f) {
  unsigned u = __float_as_uint(f);
  u += 0x7fffu + ((u >> 16) & 1u);
  return (u16)(u >> 16);
}
static __device__ __forceinline__ float bf2f(u16 h) { return __uint_as_float(((unsigned)h) << 16); }
static __device__ __forceinline__ float bfr(float f) { return bf2f(f2bf(f)); }

constexpr size_t SZ_X      = (size_t)L_SEQ * DM * 2;
constexpr size_t OFF_KF    = 0;
constexpr size_t OFF_UT    = OFF_KF + (size_t)DM * L_SEQ * 4;
constexpr size_t OFF_YT    = OFF_UT + SZ_X;
constexpr size_t OFF_XA    = OFF_YT + SZ_X;
constexpr size_t OFF_H     = OFF_XA + SZ_X;
constexpr size_t OFF_X2    = OFF_H + SZ_X;
constexpr size_t OFF_U2    = OFF_X2 + SZ_X;
constexpr size_t OFF_X3    = OFF_U2 + SZ_X;
constexpr size_t SZ_QK     = (size_t)L_SEQ * 2 * HD * 2;
constexpr size_t OFF_QK    = OFF_X3 + SZ_X;
constexpr size_t SZ_O      = (size_t)L_SEQ * HD * 2;
constexpr size_t OFF_VF    = OFF_QK + 2 * SZ_QK;
constexpr size_t SZ_PW     = (size_t)2 * NSEG * 16 * SEG * DM * 2;
constexpr size_t OFF_PW    = OFF_VF + 2 * SZ_O;
constexpr size_t SZ_MEM    = (size_t)NSEG * MM * HD * 2;
constexpr size_t OFF_MEMK  = OFF_PW + SZ_PW;
constexpr size_t OFF_MEMV  = OFF_MEMK + 2 * SZ_MEM;
constexpr size_t OFF_GT    = OFF_MEMV + 2 * SZ_MEM;
constexpr size_t OFF_EP    = OFF_GT + 65536;
constexpr size_t OFF_RD    = OFF_EP + 4096;
constexpr size_t OFF_KG    = OFF_RD + 4096;
constexpr size_t OFF_G     = OFF_KG + 8192;
constexpr size_t SZ_ACC    = (size_t)2 * 65536;
constexpr size_t OFF_Z     = OFF_GT + (size_t)128 * DM * 4;
constexpr size_t OFF_ACC   = OFF_Z;
constexpr size_t OFF_FL    = OFF_ACC + 2 * SZ_ACC;
constexpr int FL_FK  = 0;
constexpr int FL_FW  = FL_FK + 64 * 8 * 32;
constexpr int FL_FR  = FL_FW + 2 * 64 * 16 * 32;
constexpr int FL_FC  = FL_FR + 64 * 16 * 32;
constexpr int FL_FQ  = FL_FC + 2 * 64 * 32 * 32;
constexpr int FL_FP  = FL_FQ + 2 * 64 * 32 * 32;
constexpr int FL_FZ  = FL_FP + 64 * 32 * 32;
constexpr int FL_TMO = FL_FZ + 16 * 32;
constexpr int FL_END = FL_TMO + 64;
constexpr size_t Z_BYTES   = OFF_FL + (size_t)FL_END * 4 - OFF_Z;
constexpr size_t WS_TOTAL  = OFF_Z + Z_BYTES;
static_assert(Z_BYTES % 16 == 0 && OFF_Z % 256 == 0 && Z_BYTES <= 4 * 64 * 8192 && OFF_G + 2048 <= OFF_Z, "zero region");
static_assert(WS_TOTAL + (4 << 20) <= (size_t)256 * 1024 * 1024, "workspace too large");

static_assert(WS_TOTAL - OFF_XA <= (size_t)384 * 8 * 65536, "translation warm-up");
constexpr int CKN = 2336;
static_assert((size_t)DM * 2 * CKN * 2 <= (size_t)DM * L_SEQ * 4 + SZ_X, "K buffer");
__device__ __forceinline__ float pos_of_index(int p) {
  if (p < 256) return (float)p;
  if (p < 384) return (float)(256 + 2 * (p - 256));
  if (p < 512) return (float)(512 + 4 * (p - 384));
  if (p < 640) return (float)(1024 + 8 * (p - 512));
  return 2048.0f;
}
__device__ __forceinline__ int index_of_l(int l) {
  float pf = bfr((float)l);
  int p = (int)pf;
  if (p < 256) return p;
  if (p < 512) return 256 + ((p - 256) >> 1);
  if (p < 1024) return 384 + ((p - 512) >> 2);
  if (p < 2048) return 512 + ((p - 1024) >> 3);
  return 640;
}

template <int CTRL> __device__ __forceinline__ float dppr(float x) {
  return __builtin_bit_cast(float, __builtin_amdgcn_update_dpp(0, __builtin_bit_cast(int, x), CTRL, 0xf, 0xf, false));
}
__device__ __forceinline__ float wave_sum(float v) {
  v += dppr<0x121>(v);
  v += dppr<0x122>(v);
  v += dppr<0x124>(v);
  v += dppr<0x128>(v);
  {
    auto r = __builtin_amdgcn_permlane16_swap(__float_as_uint(v), __float_as_uint(v), false, false);
    v = __uint_as_float(r[0]) + __uint_as_float(r[1]);
  }
  {
    auto r = __builtin_amdgcn_permlane32_swap(__float_as_uint(v), __float_as_uint(v), false, false);
    v = __uint_as_float(r[0]) + __uint_as_float(r[1]);
  }
  return v;
}
__device__ __forceinline__ void ln_row8(float (&v)[8]) {
  float s = 0.f;
#pragma unroll
  for (int j = 0; j < 8; ++j) s += v[j];
  s = wave_sum(s);
  const float mean = s * (1.0f / DM);
  float q = 0.f;
#pragma unroll
  for (int j = 0; j < 8; ++j) { float t = v[j] - mean; q += t * t; }
  q = wave_sum(q);
  const float var = q * (1.0f / DM);
  const float mb = bfr(mean);
  const float sd = bfr(sqrtf(bfr(bfr(var) + 1e-5f)));
#pragma unroll
  for (int j = 0; j < 8; ++j) v[j] = bfr(bfr(v[j] - mb) / sd);
}

#define RLX_AGENT __ATOMIC_RELAXED, __HIP_MEMORY_SCOPE_AGENT
typedef __attribute__((address_space(1))) unsigned gu32;
typedef unsigned v4u __attribute__((ext_vector_type(4)));
constexpr int AUX_SC1 = 16;
constexpr unsigned SPIN_CAP = 1u << 18;

__device__ __forceinline__ bool wait_flags(const unsigned* flags, int lane, int nfl, unsigned want, unsigned* tmo) {
  for (unsigned spins = 0;; ++spins) {
    unsigned v = want;
    if (lane < nfl) v = __hip_atomic_load((gu32*)flags + lane * 32, RLX_AGENT);
    if (__all(v >= want)) break;
    if (spins > SPIN_CAP) {
      if (lane == 0) __hip_atomic_store((gu32*)tmo, 1u, RLX_AGENT);
      return false;
    }
  }
  __builtin_amdgcn_fence(__ATOMIC_ACQUIRE, "wavefront");
  return true;
}

#define STAMP(i)
#define STAMP_DUMP()
#define WSTAMP(i)
template <int CTRL> __device__ __forceinline__ float dppf(float old, float x) {
  return __builtin_bit_cast(float, __builtin_amdgcn_update_dpp(__builtin_bit_cast(int, old), __builtin_bit_cast(int, x), CTRL, 0xf, 0xf, false));
}
__device__ __forceinline__ float wave_max_dpp(float x) {
  const float ninf = -3e38f;
  x = fmaxf(x, dppf<0x111>(ninf, x));
  x = fmaxf(x, dppf<0x112>(ninf, x));
  x = fmaxf(x, dppf<0x114>(ninf, x));
  x = fmaxf(x, dppf<0x118>(ninf, x));
  x = fmaxf(x, dppf<0x142>(ninf, x));
  x = fmaxf(x, dppf<0x143>(ninf, x));
  return __builtin_bit_cast(float, __builtin_amdgcn_readlane(__builtin_bit_cast(int, x), 63));
}
__device__ __forceinline__ float wave_sum_dpp(float x) {
  x += dppf<0x111>(0.f, x);
  x += dppf<0x112>(0.f, x);
  x += dppf<0x114>(0.f, x);
  x += dppf<0x118>(0.f, x);
  x += dppf<0x142>(0.f, x);
  x += dppf<0x143>(0.f, x);
  return __builtin_bit_cast(float, __builtin_amdgcn_readlane(__builtin_bit_cast(int, x), 63));
}
constexpr int SMS = 528;
constexpr int QS = 144;
constexpr int VS = 80;
constexpr int SOS = 144;
#define VAR_NREP 4
constexpr int NREP = VAR_NREP;
__device__ __forceinline__ float xsum16(float x) {
  auto r = __builtin_amdgcn_permlane16_swap(__float_as_uint(x), __float_as_uint(x), false, false);
  return __uint_as_float(r[0]) + __uint_as_float(r[1]);
}
__device__ __forceinline__ float xsum32(float x) {
  auto r = __builtin_amdgcn_permlane32_swap(__float_as_uint(x), __float_as_uint(x), false, false);
  return __uint_as_float(r[0]) + __uint_as_float(r[1]);
}
__device__ __forceinline__ float xmax16(float x) {
  auto r = __builtin_amdgcn_permlane16_swap(__float_as_uint(x), __float_as_uint(x), false, false);
  return fmaxf(__uint_as_float(r[0]), __uint_as_float(r[1]));
}
__device__ __forceinline__ float xmax32(float x) {
  auto r = __builtin_amdgcn_permlane32_swap(__float_as_uint(x), __float_as_uint(x), false, false);
  return fmaxf(__uint_as_float(r[0]), __uint_as_float(r[1]));
}
__device__ __forceinline__ float red_mod4(float x) {
  x += dppf<0x124>(0.f, x);
  x += dppf<0x128>(0.f, x);
  return xsum32(xsum16(x));
}
typedef __bf16 bf16x2_t __attribute__((ext_vector_type(2)));
typedef float f32x2_t __attribute__((ext_vector_type(2)));
__device__ __forceinline__ unsigned pk2(float a, float b) {
  f32x2_t v = {a, b};
  bf16x2_t r = __builtin_convertvector(v, bf16x2_t);
  return __builtin_bit_cast(unsigned, r);
}
typedef _Float16 f16x2_t __attribute__((ext_vector_type(2)));
typedef _Float16 f16x8 __attribute__((ext_vector_type(8)));
__device__ __forceinline__ unsigned pkh(float a, float b) {
  f32x2_t v = {a, b};
  return __builtin_bit_cast(unsigned, __builtin_convertvector(v, f16x2_t));
}
__device__ __forceinline__ float h2f(u16 h) { return (float)__builtin_bit_cast(_Float16, h); }
__device__ __forceinline__ float one(float x) {
  asm("" : "+v"(x));
  return x;
}
__device__ __forceinline__ unsigned pk2s(float a, float b) { return pk2(one(a), one(b)); }
__device__ __forceinline__ float dot2sq(const unsigned x, const float acc) {
  return __builtin_amdgcn_fdot2_f32_bf16(__builtin_bit_cast(bf16x2_t, x), __builtin_bit_cast(bf16x2_t, x), acc, false);
}
__device__ __forceinline__ float lo16(unsigned u) { return __uint_as_float(u << 16); }
__device__ __forceinline__ float hi16(unsigned u) { return __uint_as_float(u & 0xffff0000u); }
__device__ __forceinline__ float bfr1(float x) { return lo16(pk2(x, x)); }
__device__ __forceinline__ float half_sum(float v) {
  v += dppr<0x121>(v);
  v += dppr<0x122>(v);
  v += dppr<0x124>(v);
  v += dppr<0x128>(v);
  return xsum16(v);
}
__device__ __forceinline__ void scale_pair(float a, float b, float scale, float& oa, float& ob) {
  unsigned u = pk2(a, b);
  unsigned v = pk2s(lo16(u) * scale, hi16(u) * scale);
  oa = lo16(v);
  ob = hi16(v);
}

#define XMAP 2
__device__ __forceinline__ int vbid() {
  const int b = blockIdx.x, x = b & 7, t = b >> 3;
  if (XMAP == 0) {
    if (b < 64) return (b & 32) + 4 * x + (t & 3);
    if (b < 96) return (b & ~15) + 2 * x + (t & 1);
    if (b >= 104 && b < 168) return 104 + 8 * x + t - 13;
    if (b >= 168 && b < 232) return 168 + ((b - 168) & 32) + 4 * x + ((t - 21) & 3);
    return b;
  }
  if (XMAP == 3) {
    const int y = x & 3;
    if (t >= 16 && t < 24) return 104 + 16 * y + 2 * (x & 4) + t - 16;
    if (x < 4) return t < 16 ? 4 * (t & 8) + 8 * x + (t & 7) : 64 + 4 * (t & 4) + 4 * y + (t & 3);
    if (t >= 24) return (t >= 28 ? 240 : y < 2 ? 96 : 224) + 4 * y + (t & 3);
    return 168 + 4 * (t & 8) + 8 * y + (t & 7);
  }
  if (XMAP == 4) {
    const int y = x & 3;
    if (x < 4) return t < 16 ? 4 * (t & 8) + 8 * x + (t & 7) : t < 28 ? 104 + 16 * y + t - 16 : 64 + 4 * y + (t & 3);
    if (t >= 24) return (t >= 28 ? 240 : y < 2 ? 96 : 224) + 4 * y + (t & 3);
    if (t < 16) return 168 + 4 * (t & 8) + 8 * y + (t & 7);
    return t < 20 ? 104 + 16 * y + t - 4 : 80 + 4 * y + (t & 3);
  }
  if (XMAP == 2 || XMAP >= 5) {
    if (XMAP == 5 && x < 4 && t < 16) return 16 * x + t;
    if (XMAP == 6 && x < 4 && t < 16) return 32 * (x & 1) + 8 * (x & 2) + t;
    if (XMAP == 7 && x < 4) return (x & 1) ? 104 + 16 * (x & 2) + t : 16 * x + t;
    if (x < 4) return t < 16 ? 4 * (t & 8) + 8 * x + (t & 7) : 104 + 16 * x + t - 16;
    const int y = x - 4;
    if (t >= 24) return (t >= 28 ? 240 : y < 2 ? 96 : 224) + 4 * y + (t & 3);
    if (t < 16) return 168 + 4 * (t & 8) + 8 * y + (t & 7);
    return 64 + 4 * (t & 4) + 4 * y + (t & 3);
  }
  if (x < 2) return 2 * (t & 16) + 16 * x + (t & 15);
  const int i = 16 * (x - 2) + (t & 15);
  if (t < 16) {
    const int k = (i * 171) >> 9, r = i - 3 * k;
    return r == 0 ? 168 + k : r == 1 ? 200 + k : 64 + 16 * (k & 1) + (k >> 1);
  }
  const int g = i >> 3, e = i & 7;
  if (g == 3) return 96 + e;
  if (g == 7) return 232 + e;
  if (g == 9) return 240 + e;
  if (g == 11) return 248 + e;
  return 104 + 8 * (g - (g > 3) - (g > 7) - (g > 9)) + e;
}
#define MSTAMP()
#define WSTAMP2()
#define SST(k)
#define SSW(k, v)
#define TS(k, ss, t)
#define LS(k, ss)
#define LS_DUMP()
struct MegaArgs {
  const u16* mem0;
  const u16* Dskip;
  char* ws;
  u16* out;
  const u16* x0;
  const u16 *logAre, *Aim, *Cre, *Cim, *logdt;
  const u16 *Wq, *Wk, *Wv, *Wo;
  int vb, wv;
};

__device__ __forceinline__ int lane_id() {
  int l;
  asm volatile("v_mbcnt_lo_u32_b32 %0, -1, 0\n\tv_mbcnt_hi_u32_b32 %0, -1, %0" : "=v"(l));
  __builtin_assume(l >= 0 && l < 64);
  return l;
}
__device__ __forceinline__ bool spin_timeout(unsigned spins, unsigned* tmo) {
  if (spins > SPIN_CAP) {
    if (lane_id() == 0) __hip_atomic_store((gu32*)tmo, 1u, RLX_AGENT);
    return true;
  }
  return false;
}
#define ROT_H (4 * (h & 1))
#define ROT_T (h & 2)
#define C2G 420
#define CVSP 40
#define PFA 0
#define PFB 0
#define WO1 1
#define AWH 0
#define PEEK0 1
#define SLP0 2
#define GF_R 400
#define GF_V 0
#define GF_H 150
#define CVT2 48
#define GF_P 0
#define GF0 8
#define SLP_H 12
#define HX0 6
#define CVSTEP 2
#define CVLATE 64
#define PACE 203
#define PACE0 6
#define RPRIO 3
#define DL_R2 1400
#define PNL 8
#define GSL 12
#define AWDIR 1
#define AWAT 0
#define AWRP 0
#define PEPS 4
#define FGATE 0
#define DL_C 0
#define DL_A 800
#define DL_R 0
#define DL_Q 0
#define KC0 0
#define CPRIO 1
#define P1PRIO 3
#define QLEAD 2
#define POLL_SLEEP 12
#define SLP_C POLL_SLEEP
#define SLP_A POLL_SLEEP
#define SLP_W POLL_SLEEP
#define SLP_R POLL_SLEEP
#define SLP_V POLL_SLEEP
#define SLP_Q POLL_SLEEP
#define SLP_M 4
#define AWPRIO 2
#define SLP_S 8
#define SPOS 4
#define CVPRE 12
#define QPD 3
#define QPRI 2
#define TLBW 3
#define QHV 0
#define QCHK 0
#define LEADN 99
#define GATE_V 170
#define GATE_R 380
#define GATE_N 380
#define AWAHEAD 1
#define GATE_M 150
#define PRI_A 3
#define PRI_WA 2
#define PRI_WB 1
#define AWSKIP 3
#define PRI_0 3
#define AWDEF 0
#define SLP_F 2
#define EXP_OFF 0
#define AWSPEC 1
#define CVTHR 48
#define NR_C 8
#define NR_R 8
#define NR_W 8
#define NR_N 2
#define QAHEAD 0
#define NS0 4
#define SLP_N POLL_SLEEP
template <int SLP, int WPL>
__device__ __forceinline__ bool wait_n(const unsigned* f, int n, unsigned* tmo) {
  const int l = lane_id();
  for (unsigned spins = 0;; ++spins) {
    unsigned v = 1u;
    if (l < n) v = __hip_atomic_load((gu32*)f + (l / WPL) * 32 + l % WPL, RLX_AGENT);
    if (__all(v != 0u)) break;
    if (spin_timeout(spins, tmo)) return false;
    __builtin_amdgcn_s_sleep(SLP);
  }
  asm volatile("" ::: "memory");
  return true;
}
__device__ __forceinline__ unsigned peek(const unsigned* base, const unsigned off) {
  unsigned r;
  asm volatile("global_atomic_or %0, %1, %2, %3 sc0\n\ts_waitcnt vmcnt(0)" : "=&v"(r) : "v"(off), "v"(0u), "s"(base) : "memory");
  return r;
}
__device__ __forceinline__ unsigned now10() { return (unsigned)__builtin_amdgcn_s_memrealtime(); }
__device__ __forceinline__ void gate(const unsigned t) {
  for (int i = 0; i < 64; ++i) {
    const int d = (int)(t - now10());
    if (d <= 0) break;
    if (d > 100) __builtin_amdgcn_s_sleep(32);
    else if (d > 25) __builtin_amdgcn_s_sleep(8);
    else __builtin_amdgcn_s_sleep(2);
  }
}
template <int SLP, int WPL, int GT>
__device__ __forceinline__ void grp_wait(const bool leader, volatile unsigned* sw, const unsigned val, const unsigned* f, int n,
                                         unsigned* tmo, unsigned& tl) {
  if (leader) {
    const int l = lane_id();
    for (unsigned spins = 0;; ++spins) {
      unsigned v = 1u;
      if (l < n) v = __hip_atomic_load((gu32*)f + (l / WPL) * 32 + l % WPL, RLX_AGENT);
      if (__all(v != 0u) || spin_timeout(spins, tmo)) break;
      if (GT && spins == 0) gate(tl);
      else __builtin_amdgcn_s_sleep(SLP);
    }
    if (GT) tl = now10() + GT;
    if (sw && l == 0) *sw = val;
  } else {
    for (unsigned spins = 0; *sw < val; ++spins) {
      if (spins > 64u * SPIN_CAP) break;
      if (FGATE && GT && spins == 0) gate(tl);
      __builtin_amdgcn_s_sleep(SLP_F);
    }
    if (FGATE && GT) tl = now10() + GT;
  }
  asm volatile("" ::: "memory");
}
template <int SLP, int WPL>
__device__ __forceinline__ bool wg_wait(const unsigned* f, int n, unsigned* tmo) {
  if (threadIdx.x < 64) (void)wait_n<SLP, WPL>(f, n, tmo);
  __syncthreads();
  asm volatile("" ::: "memory");
  return true;
}
__device__ __forceinline__ void set_flag(unsigned* f) { __hip_atomic_store((gu32*)f, 1u, RLX_AGENT); }
#define DRAIN() asm volatile("s_waitcnt vmcnt(0)" ::: "memory")
#define MKRS(ptr, bytes) __builtin_amdgcn_make_buffer_rsrc((void*)(ptr), 0, (int)(bytes), 0x00020000)
typedef unsigned v2u __attribute__((ext_vector_type(2)));
#define LD128(rs, voff, soff) __builtin_amdgcn_raw_buffer_load_b128(rs, voff, soff, AUX_SC1)
#define LD64(rs, voff, soff) __builtin_amdgcn_raw_buffer_load_b64(rs, voff, soff, AUX_SC1)
#define ST128(v, rs, voff, soff)                                              \
  do {                                                                        \
    const v4u st_d_ = (v);                                                    \
    __builtin_amdgcn_raw_buffer_store_b128(st_d_, rs, voff, soff, AUX_SC1);   \
    asm volatile("s_nop 1" ::"v"(st_d_) : "memory");                          \
  } while (0)
#define ST64(v, rs, voff, soff) __builtin_amdgcn_raw_buffer_store_b64(v, rs, voff, soff, AUX_SC1)
#define BF8(x) __builtin_bit_cast(bf16x8, x)
#define MFMA16(a, b, c) __builtin_amdgcn_mfma_f32_16x16x32_bf16(a, b, c, 0, 0, 0)
typedef short bf16x4 __attribute__((ext_vector_type(4)));
#define MFMA16K(a, b, c) \
  __builtin_amdgcn_mfma_f32_16x16x16bf16_1k(__builtin_bit_cast(bf16x4, a), __builtin_bit_cast(bf16x4, b), c, 0, 0, 0)
__device__ __forceinline__ float fmax_nn(float a, float b) { return __builtin_elementwise_maximum(a, b); }
typedef __bf16 bfv8 __attribute__((ext_vector_type(8)));
typedef __bf16 bfv16 __attribute__((ext_vector_type(16)));
typedef unsigned v8u __attribute__((ext_vector_type(8)));
#define SMF(a, b, c, idx) \
  __builtin_amdgcn_smfmac_f32_16x16x64_bf16(__builtin_bit_cast(bfv8, a), __builtin_bit_cast(bfv16, b), c, idx, 0, 0)
__device__ __forceinline__ float swap_add32(float a, float b) {
  auto r = __builtin_amdgcn_permlane32_swap(__float_as_uint(a), __float_as_uint(b), false, false);
  return __uint_as_float(r[0]) + __uint_as_float(r[1]);
}
__device__ __forceinline__ float swap_add16(float a, float b) {
  auto r = __builtin_amdgcn_permlane16_swap(__float_as_uint(a), __float_as_uint(b), false, false);
  return __uint_as_float(r[0]) + __uint_as_float(r[1]);
}
__device__ __forceinline__ v4u smf_fix(const v4u d) {
  auto x = __builtin_amdgcn_permlane16_swap(d.x, d.z, false, false);
  auto y = __builtin_amdgcn_permlane16_swap(d.y, d.w, false, false);
  return v4u{x[0], x[1], y[0], y[1]};
}
__device__ __forceinline__ v8u smf_bfrag(const u16* p) {
  const v2u a = *reinterpret_cast<const v2u*>(p), b = *reinterpret_cast<const v2u*>(p + 8);
  const v2u c = *reinterpret_cast<const v2u*>(p + 32), d = *reinterpret_cast<const v2u*>(p + 40);
  return v8u{a.x, b.x, a.y, b.y, c.x, d.x, c.y, d.y};
}

#define VAR_PPA 0
#define VAR_PPB 0
#define VAR_THR 0
#define VAR_THR2 0
#define CV_LEAD 100
#define VAR_EARLY 1
#define VAR_DRAIN 1
#define VAR_D1 0
#define VAR_D2 0
#define VAR_DR2 1
#define VAR_S1 0
#define VAR_S2 0
typedef __attribute__((address_space(1))) unsigned long long gu64;
typedef __attribute__((address_space(1))) char gch;
__device__ __forceinline__ int cvt_rpi(float x) {
  int r;
  asm("v_cvt_rpi_i32_f32 %0, %1" : "=v"(r) : "v"(x));
  return r;
}
typedef unsigned long long u64;
__device__ __forceinline__ u64 call_tag(const MegaArgs& A) {
  u64 t = *(const __attribute__((address_space(4))) u64*)(A.ws + OFF_EP) * 6364136223846793005ull + 1442695040888963407ull;
  asm volatile("" : "+s"(t)::"memory");
  return t;
}
__device__ __forceinline__ u64 tagged(const u64 tag, const unsigned v) { return (tag & 0xffffffffffffull) | (u64)(v >> 16) << 48; }
__device__ __forceinline__ bool has_tag(const u64 tag, const unsigned lo, const unsigned hi) {
  return lo == (unsigned)tag && (hi & 0xffffu) == ((unsigned)(tag >> 32) & 0xffffu);
}
__device__ __forceinline__ u64 ready_look(const MegaArgs& A) {
  return __hip_atomic_load((gu64*)(A.ws + OFF_RD) + (blockIdx.x & 7) * 64 + lane_id(), RLX_AGENT);
}
__device__ __forceinline__ void ready_wait(const MegaArgs& A, const u64 tag, u64 v) {
  for (unsigned spins = 0; !__all(v == tag) && spins < SPIN_CAP; ++spins) {
    __builtin_amdgcn_s_sleep(8);
    v = ready_look(A);
  }
  asm volatile("" ::: "memory");
}
typedef short v4s __attribute__((ext_vector_type(4)));
typedef __attribute__((address_space(3))) v4s* lp4s;
__device__ __forceinline__ void glds16(const void* sbase, const unsigned voff, const unsigned lds) {
  unsigned keep;
  asm volatile("s_nop 4\n\ts_mov_b32 %0, m0\n\ts_mov_b32 m0, %3\n\ts_nop 0\n\tglobal_load_lds_dwordx4 %1, %2\n\ts_mov_b32 m0, %0"
               : "=&s"(keep)
               : "v"(voff), "s"(sbase), "s"(lds)
               : "memory");
}
__device__ __forceinline__ void glds16c(const void* sbase, const unsigned voff, const unsigned lds) {
  unsigned keep;
  asm volatile("s_nop 4\n\ts_mov_b32 %0, m0\n\ts_mov_b32 m0, %3\n\ts_nop 0\n\tglobal_load_lds_dwordx4 %1, %2 sc1\n\ts_mov_b32 m0, %0"
               : "=&s"(keep)
               : "v"(voff), "s"(sbase), "s"(lds)
               : "memory");
}
__device__ __forceinline__ void glds64c(const void* sbase, const unsigned voff, const unsigned lds) {
  unsigned keep;
  asm volatile("s_nop 4\n\ts_mov_b32 %0, m0\n\ts_mov_b32 m0, %3\n\ts_nop 0\n\tglobal_load_lds_dwordx4 %1, %2 sc1\n\t"
               "global_load_lds_dwordx4 %1, %2 offset:1024 sc1\n\tglobal_load_lds_dwordx4 %1, %2 offset:2048 sc1\n\t"
               "global_load_lds_dwordx4 %1, %2 offset:3072 sc1\n\ts_mov_b32 m0, %0"
               : "=&s"(keep)
               : "v"(voff), "s"(sbase), "s"(lds)
               : "memory");
}
__device__ __forceinline__ v2u trd(const unsigned a) {
  return __builtin_bit_cast(v2u, __builtin_amdgcn_ds_read_tr16_b64_v4i16((lp4s)(unsigned long)a));
}
__device__ __forceinline__ unsigned lds_base(char* smem) { return (unsigned)(unsigned long)(__attribute__((address_space(3))) char*)smem; }
#define RAW_BAR()                                           \
  do {                                                      \
    asm volatile("s_waitcnt lgkmcnt(0)" ::: "memory");      \
    __builtin_amdgcn_s_barrier();                           \
    asm volatile("" ::: "memory");                          \
  } while (0)
#define WAIT_VM(n) asm volatile("s_waitcnt vmcnt(" #n ")" ::: "memory")
template <bool SP>
__device__ __forceinline__ void wqkv_issue(const u16* Wm, const int hf, const int h, const int w, const int l, const unsigned sb) {
  const char* base = (const char*)Wm + (size_t)(256 * hf + 32 * w) * (HD * 2) + h * (DH * 2);
#pragma unroll
  for (int i = 0; i < 8; ++i) {
    const int r = l >> 4;
    const int f = SP ? ((r & 1) | ((i & 1) << 1) | (((i >> 1) & 1) << 2)) : (r | (((i >> 1) & 1) << 2));
    glds16(base, (unsigned)((4 * i + r) * (HD * 2) + (((l & 15) ^ (f << 1)) << 4)), sb + w * 16384 + hf * 8192 + i * 1024);
  }
}

__device__ __forceinline__ unsigned row_stats(const v4u raw) {
  float v[8];
#pragma unroll
  for (int e = 0; e < 4; ++e) { v[2 * e] = lo16(raw[e]); v[2 * e + 1] = hi16(raw[e]); }
  float s = 0.f;
#pragma unroll
  for (int e = 0; e < 8; ++e) s += v[e];
  const float mean = wave_sum(s) * (1.0f / DM);
  float q = 0.f;
#pragma unroll
  for (int e = 0; e < 8; ++e) { const float t = v[e] - mean; q += t * t; }
  const float var = wave_sum(q) * (1.0f / DM);
  const float sd = bfr(sqrtf(bfr(bfr(var) + 1e-5f)));
  return (unsigned)f2bf(mean) | ((unsigned)f2bf(sd) << 16);
}
__device__ __forceinline__ void ask_slow(const MegaArgs& A, const int k, unsigned (&t)[3]) {
  const int l = lane_id(), x = blockIdx.x & 7;
  const char *wq = (const char*)A.Wq, *wv = (const char*)A.Wv;
  asm("" : "+s"(wq), "+s"(wv));
#pragma unroll
  for (int m = 0; m < 3; ++m) {
    const char* W = m == 1 ? (const char*)A.Wk : ((m == 0) == (x < 4)) ? wv : wq;
    const int a = (int)((unsigned long)W >> 8) + 8 * (x & 3), h = k < 32 ? ((1 - a) & 3) + 4 * (k & 1) : (31 - a) & 31;
    const int r = k < 32 ? k >> 1 : k - 32;
    t[m] = __builtin_amdgcn_raw_buffer_load_b32(MKRS(W, (unsigned)k < 48u && h < 8 ? DM * HD * 2 : 0), (l >> 1) * (HD * 2) + (l & 1) * 128,
                                                (32 * (r & 15)) * (HD * 2) + (8 * (x & 3) + (h & 7)) * 256, 0);
  }
}
#define ASKED(t) asm volatile("" ::"v"(t[0]), "v"(t[1]), "v"(t[2]))
template <int N>
__device__ __forceinline__ u64 start_stats(const MegaArgs& A, const int idx) {
  const int w = A.wv, l = lane_id();
  const int r0 = N * (8 * idx + w);
  unsigned ta[3], tb[3];
  v4u raw[N];
#pragma unroll
  for (int i = 0; i < N; ++i) raw[i] = *reinterpret_cast<const v4u*>(A.x0 + (size_t)(r0 + i) * DM + l * 8);
  asm volatile("" ::: "memory");
  ask_slow(A, 16 * w + (idx & 3) + (idx >> 4) * N, ta);
  if (N > 4) ask_slow(A, 16 * w + (idx & 3) + 12, tb);
  unsigned tc[2] = {};
  if (N == 4 && AWH > 0) tc[0] = *reinterpret_cast<const unsigned*>(A.Wo + (size_t)idx * 65536 + 32768 + (64 * w + l) * 64);
  if (N == 4 && AWH > 1 && w >= 4) tc[1] = *reinterpret_cast<const unsigned*>(A.Wo + (size_t)idx * 65536 + (64 * w + l) * 64);
  const u64 tag = call_tag(A);
  unsigned stw = 0u;
#pragma unroll
  for (int i = 0; i < N; ++i) {
    const unsigned t = row_stats(raw[i]);
    if ((l >> 1) == i) stw = t;
  }
  if (l < 2 * N) __hip_atomic_store((gu64*)(A.ws + OFF_GT) + 2 * r0 + l, tagged(tag, (l & 1) ? stw : stw << 16), RLX_AGENT);
  SST(0);
  ASKED(ta);
  if (N > 4) ASKED(tb);
  asm volatile("" ::"v"(tc[0]), "v"(tc[1]));
  return tag;
}
struct Cx {
  f32x2_t r, i;
};
__device__ __forceinline__ Cx cmul(const Cx a, const Cx b) { return Cx{a.r * b.r - a.i * b.i, a.r * b.i + a.i * b.r}; }
struct KGen {
  unsigned pa, pb, pc, pd;
  u16 pe;
  f32x2_t ar, ai;
  Cx C;
  char* st;
  v4u bh[8], bl[8];
  __device__ __forceinline__ void load(const MegaArgs& A, const int d, const int l) {
    pa = *reinterpret_cast<const unsigned*>(A.logAre + d * NST + 2 * l);
    pb = *reinterpret_cast<const unsigned*>(A.Aim + d * NST + 2 * l);
    pc = *reinterpret_cast<const unsigned*>(A.Cre + d * NST + 2 * l);
    pd = *reinterpret_cast<const unsigned*>(A.Cim + d * NST + 2 * l);
    pe = A.logdt[d];
  }
  __device__ __forceinline__ void setup() {
    const float dt = bfr(expf(bf2f(pe)));
    ar = f32x2_t{-bfr(expf(lo16(pa))) * dt, -bfr(expf(hi16(pa))) * dt};
    ai = f32x2_t{lo16(pb) * dt, hi16(pb) * dt};
    C = Cx{f32x2_t{lo16(pc) * dt, hi16(pc) * dt}, f32x2_t{lo16(pd) * dt, hi16(pd) * dt}};
  }
  __device__ __forceinline__ float scale() {
    const float b = 2.0f * wave_sum((fabsf(C.r.x) + fabsf(C.r.y)) + (fabsf(C.i.x) + fabsf(C.i.y)));
    const unsigned eb = max(__float_as_uint(b) >> 23, 25u);
    const float ks = __uint_as_float((268u - eb) << 23);
    C.r *= ks;
    C.i *= ks;
    return __uint_as_float((eb - 24u) << 23);
  }
  __device__ __forceinline__ Cx power(const float p) const {
    Cx z;
#pragma unroll
    for (int e = 0; e < 2; ++e) {
      const float x = ai[e] * p;
      const float f = __builtin_fmaf(x, 6.4206382e-9f, __builtin_fmaf(x, 0.15915494f, -__builtin_rintf(x * 0.15915494f)));
      const float m = __expf(ar[e] * p);
      z.r[e] = m * __builtin_amdgcn_cosf(f);
      z.i[e] = m * __builtin_amdgcn_sinf(f);
    }
    return z;
  }
  __device__ __forceinline__ char* rows(char* p, Cx y, const Cx s, const int n) const {
#pragma unroll 1
    for (int i = 0; i < n; i += 2) {
#pragma unroll
      for (int k = 0; k < 2; ++k) {
        const unsigned h0 = pk2(y.r.x, y.r.y), h1 = pk2(y.i.x, y.i.y);
        const f32x2_t a = y.r - f32x2_t{lo16(h0), hi16(h0)}, b = y.i - f32x2_t{lo16(h1), hi16(h1)};
        *reinterpret_cast<v2u*>(p + 1040 * k) = v2u{h0, h1};
        *reinterpret_cast<v2u*>(p + 1040 * k + 512) = v2u{pk2(a.x, a.y), pk2(b.x, b.y)};
        y = cmul(y, s);
      }
      p += 2080;
    }
    return p;
  }
  __device__ __forceinline__ void get(v4u (&h)[8], v4u (&o)[8], const int l) const {
    const char* p = st + (l & 15) * 1040 + (l >> 4) * 16;
    asm volatile("" ::: "memory");
#pragma unroll
    for (int k = 0; k < 8; ++k) {
      h[k] = *reinterpret_cast<const v4u*>(p + 64 * k);
      o[k] = *reinterpret_cast<const v4u*>(p + 512 + 64 * k);
    }
    asm volatile("" ::: "memory");
  }
  __device__ __forceinline__ void columns(const int l) {
    Cx s = power(16.f);
    s.i = -s.i;
    rows(st + l * 8, Cx{f32x2_t{1.f, 1.f}, f32x2_t{0.f, 0.f}}, s, 16);
    get(bh, bl, l);
  }
  __device__ __forceinline__ f32x4 tile(const int l) const {
    v4u h[8], o[8];
    get(h, o, l);
    f32x4 d = {}, e = {}, f = {};
#pragma unroll
    for (int k = 0; k < 8; ++k) {
      d = MFMA16(BF8(h[k]), BF8(bh[k]), d);
      e = MFMA16(BF8(o[k]), BF8(bh[k]), e);
      f = MFMA16(BF8(h[k]), BF8(bl[k]), f);
    }
    return (d + (e + f)) * 2.0f;
  }
};
#define ACC_INIT 0xc9b26c86c9b26ca0ull
#define CHAIN_ASM_ATT \
  "s_mov_b32 s95, 272\n" \
  "s_mov_b32 s91, 0x4c7c2000\n" \
  "s_mov_b32 s85, 0xc9b26c9b\n" \
  "s_mov_b32 s92, 0xc9326c9b\n" \
  "s_mov_b32 s93, 0xc9326c9b\n" \
  "s_mov_b32 s78, 0x3e0293ee\n" \
  "s_mov_b32 s76, 0x33333333\n" \
  "s_mov_b32 s77, 0x33333333\n" \
  "s_lshl_b32 s98, s73, 10\n" \
  "s_add_u32 s98, s98, s74\n" \
  "s_add_u32 s98, s98, 131072\n" \
  "s_lshl_b32 s97, s73, 9\n" \
  "s_add_u32 s97, s97, s74\n" \
  "s_add_u32 s97, s97, 158720\n" \
  "s_lshl_b32 s99, s73, 10\n" \
  "v_mbcnt_lo_u32_b32 v48, -1, 0\n" \
  "v_mbcnt_hi_u32_b32 v48, -1, v48\n" \
  "s_add_u32 s88, s74, 157760\n" \
  "v_mov_b32 v59, s88\n" \
  "v_lshrrev_b32 v49, 2, v48\n" \
  "v_lshrrev_b32 v50, 4, v48\n" \
  "v_bitop3_b32 v50, v49, v50, 3 bitop3:0x28\n" \
  "v_lshl_add_u32 v8, v50, 3, s97\n" \
  "v_and_b32 v50, 3, v48\n" \
  "v_lshl_add_u32 v8, v50, 5, v8\n" \
  "v_and_b32 v49, 1, v49\n" \
  "v_lshl_add_u32 v8, v49, 8, v8\n" \
  "v_and_b32 v49, 8, v48\n" \
  "v_lshl_add_u32 v8, v49, 4, v8\n" \
  "v_lshrrev_b32 v49, 5, v48\n" \
  "v_add_u32 v10, s99, v1\n" \
  "s_lshr_b32 s88, s99, 2\n" \
  "s_add_u32 s88, s88, s74\n" \
  "s_add_u32 s88, s88, 139264\n" \
  "v_lshl_add_u32 v7, v49, 10, s88\n" \
  "v_and_b32 v49, 31, v48\n" \
  "v_lshl_add_u32 v7, v49, 3, v7\n" \
  "v_mov_b64 v[12:13], 0\n" \
  "v_mov_b64 v[14:15], 0\n" \
  "v_mov_b64 v[16:17], 0\n" \
  "v_mov_b64 v[18:19], 0\n" \
  "v_mov_b64 v[20:21], 0\n" \
  "v_mov_b64 v[22:23], 0\n" \
  "v_mov_b32 v34, 0xc9b26ca0\n" \
  "v_mov_b32 v35, 0xc9b26c86\n" \
  "s_mov_b32 s87, 0x40000\n" \
  "v_subrev_u32 v30, 64, v59\n" \
  "v_add_u32 v17, 0x10000, v28\n" \
  "v_xor_b32 v18, 128, v28\n" \
  "v_xor_b32 v19, 128, v17\n" \
  "ds_write_b32 v30, v12 offset:0\n" \
  "ds_write_b32 v30, v12 offset:32\n" \
  "ds_write_b32 v30, v12 offset:64\n" \
  "v_xor_b32 v20, 0, v29\n" \
  "v_xor_b32 v21, 32, v29\n" \
  "v_xor_b32 v22, 64, v29\n" \
  "v_xor_b32 v23, 96, v29\n" \
  "s_waitcnt vmcnt(12) lgkmcnt(0)\n" \
  "s_barrier\n" \
  "ds_read_b64_tr_b16 v[160:161], v20 offset:0\n" \
  "ds_read_b64_tr_b16 v[162:163], v20 offset:2048\n" \
  "ds_read_b64_tr_b16 v[168:169], v21 offset:0\n" \
  "ds_read_b64_tr_b16 v[170:171], v21 offset:2048\n" \
  "ds_read_b64_tr_b16 v[176:177], v22 offset:0\n" \
  "ds_read_b64_tr_b16 v[178:179], v22 offset:2048\n" \
  "ds_read_b64_tr_b16 v[184:185], v23 offset:0\n" \
  "ds_read_b64_tr_b16 v[186:187], v23 offset:2048\n" \
  "s_lshl_b32 s88, s73, 10\n" \
  "v_add_u32 v48, s88, v1\n" \
  "ds_read_b128 v[52:55], v48 offset:0\n" \
  "s_waitcnt lgkmcnt(0)\n" \
  "v_mov_b32 v11, 0\n" \
  "v_dot2c_f32_bf16 v11, v52, v52\n" \
  "v_dot2c_f32_bf16 v11, v53, v53\n" \
  "v_dot2c_f32_bf16 v11, v54, v54\n" \
  "v_dot2c_f32_bf16 v11, v55, v55\n" \
  "s_nop 0\n" \
  "s_nop 1\n" \
  "v_max_u32_dpp v11, v11, v11 row_ror:1 row_mask:0xf bank_mask:0xf\n" \
  "s_nop 1\n" \
  "v_max_u32_dpp v11, v11, v11 row_ror:2 row_mask:0xf bank_mask:0xf\n" \
  "s_nop 1\n" \
  "v_max_u32_dpp v11, v11, v11 row_ror:4 row_mask:0xf bank_mask:0xf\n" \
  "s_nop 1\n" \
  "v_max_u32_dpp v11, v11, v11 row_ror:8 row_mask:0xf bank_mask:0xf\n" \
  "v_mov_b32 v31, v11\n" \
  "s_nop 1\n" \
  "v_permlane16_swap_b32 v11, v31\n" \
  "v_max_u32 v11, v11, v31\n" \
  "v_mov_b32 v31, v11\n" \
  "s_nop 1\n" \
  "v_permlane32_swap_b32 v11, v31\n" \
  "v_max_u32 v11, v11, v31\n" \
  "s_mov_b64 exec, 1\n" \
  "ds_max_u32 v59, v11\n" \
  "s_mov_b64 exec, -1\n" \
  "s_waitcnt vmcnt(8) lgkmcnt(0)\n" \
  "s_barrier\n" \
  "ds_read_b64_tr_b16 v[164:165], v20 offset:4096\n" \
  "ds_read_b64_tr_b16 v[166:167], v20 offset:6144\n" \
  "ds_read_b64_tr_b16 v[172:173], v21 offset:4096\n" \
  "ds_read_b64_tr_b16 v[174:175], v21 offset:6144\n" \
  "ds_read_b64_tr_b16 v[180:181], v22 offset:4096\n" \
  "ds_read_b64_tr_b16 v[182:183], v22 offset:6144\n" \
  "ds_read_b64_tr_b16 v[188:189], v23 offset:4096\n" \
  "ds_read_b64_tr_b16 v[190:191], v23 offset:6144\n" \
  "s_add_u32 m0, s19, 0\n" \
  "s_nop 0\n" \
  "global_load_lds_dwordx4 v24, s[4:5] offset:0\n" \
  "global_load_lds_dwordx4 v25, s[4:5] offset:1024\n" \
  "global_load_lds_dwordx4 v26, s[4:5] offset:2048\n" \
  "global_load_lds_dwordx4 v27, s[4:5] offset:3072\n" \
  "s_add_u32 s4, s4, 0x100000\n" \
  "s_addc_u32 s5, s5, 0\n" \
  "v_mfma_f32_16x16x32_bf16 v[64:67], v[160:163], v[160:163], 0\n" \
  "v_mfma_f32_16x16x32_bf16 v[68:71], v[168:171], v[168:171], 0\n" \
  "v_mfma_f32_16x16x32_bf16 v[72:75], v[176:179], v[176:179], 0\n" \
  "v_mfma_f32_16x16x32_bf16 v[76:79], v[184:187], v[184:187], 0\n" \
  "s_waitcnt vmcnt(8) lgkmcnt(0)\n" \
  "s_barrier\n" \
  "ds_read_b64_tr_b16 v[32:33], v20 offset:8192\n" \
  "ds_read_b64_tr_b16 v[34:35], v20 offset:10240\n" \
  "ds_read_b64_tr_b16 v[40:41], v21 offset:8192\n" \
  "ds_read_b64_tr_b16 v[42:43], v21 offset:10240\n" \
  "ds_read_b64_tr_b16 v[48:49], v22 offset:8192\n" \
  "ds_read_b64_tr_b16 v[50:51], v22 offset:10240\n" \
  "ds_read_b64_tr_b16 v[56:57], v23 offset:8192\n" \
  "ds_read_b64_tr_b16 v[58:59], v23 offset:10240\n" \
  "s_add_u32 m0, s19, 4096\n" \
  "s_nop 0\n" \
  "global_load_lds_dwordx4 v24, s[4:5] offset:0\n" \
  "global_load_lds_dwordx4 v25, s[4:5] offset:1024\n" \
  "global_load_lds_dwordx4 v26, s[4:5] offset:2048\n" \
  "global_load_lds_dwordx4 v27, s[4:5] offset:3072\n" \
  "s_add_u32 s4, s4, 0x100000\n" \
  "s_addc_u32 s5, s5, 0\n" \
  "v_mfma_f32_16x16x32_bf16 v[64:67], v[164:167], v[164:167], v[64:67]\n" \
  "v_mfma_f32_16x16x32_bf16 v[68:71], v[172:175], v[172:175], v[68:71]\n" \
  "v_mfma_f32_16x16x32_bf16 v[72:75], v[180:183], v[180:183], v[72:75]\n" \
  "v_mfma_f32_16x16x32_bf16 v[76:79], v[188:191], v[188:191], v[76:79]\n" \
  "s_waitcnt vmcnt(8) lgkmcnt(0)\n" \
  "s_barrier\n" \
  "ds_read_b64_tr_b16 v[36:37], v20 offset:12288\n" \
  "ds_read_b64_tr_b16 v[38:39], v20 offset:14336\n" \
  "ds_read_b64_tr_b16 v[44:45], v21 offset:12288\n" \
  "ds_read_b64_tr_b16 v[46:47], v21 offset:14336\n" \
  "ds_read_b64_tr_b16 v[52:53], v22 offset:12288\n" \
  "ds_read_b64_tr_b16 v[54:55], v22 offset:14336\n" \
  "ds_read_b64_tr_b16 v[60:61], v23 offset:12288\n" \
  "ds_read_b64_tr_b16 v[62:63], v23 offset:14336\n" \
  "s_add_u32 m0, s19, 8192\n" \
  "s_nop 0\n" \
  "global_load_lds_dwordx4 v24, s[4:5] offset:0\n" \
  "global_load_lds_dwordx4 v25, s[4:5] offset:1024\n" \
  "global_load_lds_dwordx4 v26, s[4:5] offset:2048\n" \
  "global_load_lds_dwordx4 v27, s[4:5] offset:3072\n" \
  "s_add_u32 s4, s4, 0x100000\n" \
  "s_addc_u32 s5, s5, 0\n" \
  "v_mfma_f32_16x16x32_bf16 v[64:67], v[32:35], v[32:35], v[64:67]\n" \
  "v_mfma_f32_16x16x32_bf16 v[68:71], v[40:43], v[40:43], v[68:71]\n" \
  "v_mfma_f32_16x16x32_bf16 v[72:75], v[48:51], v[48:51], v[72:75]\n" \
  "v_mfma_f32_16x16x32_bf16 v[76:79], v[56:59], v[56:59], v[76:79]\n" \
  "s_waitcnt vmcnt(8) lgkmcnt(0)\n" \
  "s_barrier\n" \
  "s_add_u32 m0, s19, 12288\n" \
  "s_nop 0\n" \
  "global_load_lds_dwordx4 v24, s[4:5] offset:0\n" \
  "global_load_lds_dwordx4 v25, s[4:5] offset:1024\n" \
  "global_load_lds_dwordx4 v26, s[4:5] offset:2048\n" \
  "global_load_lds_dwordx4 v27, s[4:5] offset:3072\n" \
  "s_add_u32 s4, s4, 0x100000\n" \
  "s_addc_u32 s5, s5, 0\n" \
  "v_mfma_f32_16x16x32_bf16 v[64:67], v[36:39], v[36:39], v[64:67]\n" \
  "v_mfma_f32_16x16x32_bf16 v[68:71], v[44:47], v[44:47], v[68:71]\n" \
  "v_mfma_f32_16x16x32_bf16 v[72:75], v[52:55], v[52:55], v[72:75]\n" \
  "v_mfma_f32_16x16x32_bf16 v[76:79], v[60:63], v[60:63], v[76:79]\n" \
  "s_nop 7\n" \
  "v_max3_f32 v13, v64, v65, v66\n" \
  "v_max3_f32 v13, v13, v67, v68\n" \
  "v_max3_f32 v13, v13, v69, v70\n" \
  "v_max3_f32 v13, v13, v71, v72\n" \
  "v_max3_f32 v13, v13, v73, v74\n" \
  "v_max3_f32 v13, v13, v75, v76\n" \
  "v_max3_f32 v13, v13, v77, v78\n" \
  "v_max_f32 v13, v13, v79\n" \
  "s_nop 1\n" \
  "v_max_f32_dpp v13, v13, v13 row_ror:1 row_mask:0xf bank_mask:0xf\n" \
  "s_nop 1\n" \
  "v_max_f32_dpp v13, v13, v13 row_ror:2 row_mask:0xf bank_mask:0xf\n" \
  "s_nop 1\n" \
  "v_max_f32_dpp v13, v13, v13 row_ror:4 row_mask:0xf bank_mask:0xf\n" \
  "s_nop 1\n" \
  "v_max_f32_dpp v13, v13, v13 row_ror:8 row_mask:0xf bank_mask:0xf\n" \
  "v_mov_b32 v31, v13\n" \
  "s_nop 1\n" \
  "v_permlane16_swap_b32 v13, v31\n" \
  "v_max_f32 v13, v13, v31\n" \
  "v_mov_b32 v31, v13\n" \
  "s_nop 1\n" \
  "v_permlane32_swap_b32 v13, v31\n" \
  "v_max_f32 v13, v13, v31\n" \
  "s_mov_b64 exec, 1\n" \
  "ds_max_u32 v30, v13 offset:32\n" \
  "s_mov_b64 exec, -1\n" \
  "s_waitcnt vmcnt(8) lgkmcnt(0)\n" \
  "s_barrier\n" \
  "s_add_u32 m0, s19, 0\n" \
  "s_nop 0\n" \
  "global_load_lds_dwordx4 v24, s[6:7] offset:0\n" \
  "global_load_lds_dwordx4 v25, s[6:7] offset:1024\n" \
  "global_load_lds_dwordx4 v26, s[6:7] offset:2048\n" \
  "global_load_lds_dwordx4 v27, s[6:7] offset:3072\n" \
  "s_add_u32 s6, s6, 0x100000\n" \
  "s_addc_u32 s7, s7, 0\n" \
  "s_waitcnt vmcnt(8) lgkmcnt(0)\n" \
  "s_barrier\n" \
  "s_add_u32 m0, s19, 4096\n" \
  "s_nop 0\n" \
  "global_load_lds_dwordx4 v24, s[6:7] offset:0\n" \
  "global_load_lds_dwordx4 v25, s[6:7] offset:1024\n" \
  "global_load_lds_dwordx4 v26, s[6:7] offset:2048\n" \
  "global_load_lds_dwordx4 v27, s[6:7] offset:3072\n" \
  "s_add_u32 s6, s6, 0x100000\n" \
  "s_addc_u32 s7, s7, 0\n" \
  "s_waitcnt vmcnt(8) lgkmcnt(0)\n" \
  "s_barrier\n" \
  "s_add_u32 m0, s19, 8192\n" \
  "s_nop 0\n" \
  "global_load_lds_dwordx4 v24, s[6:7] offset:0\n" \
  "global_load_lds_dwordx4 v25, s[6:7] offset:1024\n" \
  "global_load_lds_dwordx4 v26, s[6:7] offset:2048\n" \
  "global_load_lds_dwordx4 v27, s[6:7] offset:3072\n" \
  "s_add_u32 s6, s6, 0x100000\n" \
  "s_addc_u32 s7, s7, 0\n" \
  "s_waitcnt vmcnt(8) lgkmcnt(0)\n" \
  "s_barrier\n" \
  "ds_read_b64_tr_b16 v[64:65], v28 offset:0\n" \
  "ds_read_b64_tr_b16 v[66:67], v28 offset:512\n" \
  "ds_read_b64_tr_b16 v[68:69], v28 offset:32768\n" \
  "ds_read_b64_tr_b16 v[70:71], v28 offset:33280\n" \
  "ds_read_b64_tr_b16 v[72:73], v17 offset:0\n" \
  "ds_read_b64_tr_b16 v[74:75], v17 offset:512\n" \
  "ds_read_b64_tr_b16 v[76:77], v17 offset:32768\n" \
  "ds_read_b64_tr_b16 v[78:79], v17 offset:33280\n" \
  "ds_read_b64_tr_b16 v[192:193], v18 offset:0\n" \
  "ds_read_b64_tr_b16 v[194:195], v18 offset:512\n" \
  "ds_read_b64_tr_b16 v[196:197], v18 offset:32768\n" \
  "ds_read_b64_tr_b16 v[198:199], v18 offset:33280\n" \
  "ds_read_b64_tr_b16 v[200:201], v19 offset:0\n" \
  "ds_read_b64_tr_b16 v[202:203], v19 offset:512\n" \
  "ds_read_b64_tr_b16 v[204:205], v19 offset:32768\n" \
  "ds_read_b64_tr_b16 v[206:207], v19 offset:33280\n" \
  "s_add_u32 m0, s19, 12288\n" \
  "s_nop 0\n" \
  "global_load_lds_dwordx4 v24, s[6:7] offset:0\n" \
  "global_load_lds_dwordx4 v25, s[6:7] offset:1024\n" \
  "global_load_lds_dwordx4 v26, s[6:7] offset:2048\n" \
  "global_load_lds_dwordx4 v27, s[6:7] offset:3072\n" \
  "s_add_u32 s6, s6, 0x100000\n" \
  "s_addc_u32 s7, s7, 0\n" \
  "s_waitcnt vmcnt(8) lgkmcnt(0)\n" \
  "s_barrier\n" \
  "ds_read_b64_tr_b16 v[80:81], v28 offset:4096\n" \
  "ds_read_b64_tr_b16 v[82:83], v28 offset:4608\n" \
  "ds_read_b64_tr_b16 v[84:85], v28 offset:36864\n" \
  "ds_read_b64_tr_b16 v[86:87], v28 offset:37376\n" \
  "ds_read_b64_tr_b16 v[88:89], v17 offset:4096\n" \
  "ds_read_b64_tr_b16 v[90:91], v17 offset:4608\n" \
  "ds_read_b64_tr_b16 v[92:93], v17 offset:36864\n" \
  "ds_read_b64_tr_b16 v[94:95], v17 offset:37376\n" \
  "ds_read_b64_tr_b16 v[208:209], v18 offset:4096\n" \
  "ds_read_b64_tr_b16 v[210:211], v18 offset:4608\n" \
  "ds_read_b64_tr_b16 v[212:213], v18 offset:36864\n" \
  "ds_read_b64_tr_b16 v[214:215], v18 offset:37376\n" \
  "ds_read_b64_tr_b16 v[216:217], v19 offset:4096\n" \
  "ds_read_b64_tr_b16 v[218:219], v19 offset:4608\n" \
  "ds_read_b64_tr_b16 v[220:221], v19 offset:36864\n" \
  "ds_read_b64_tr_b16 v[222:223], v19 offset:37376\n" \
  "s_add_u32 m0, s19, 0\n" \
  "s_nop 0\n" \
  "global_load_lds_dwordx4 v24, s[8:9] offset:0\n" \
  "global_load_lds_dwordx4 v25, s[8:9] offset:1024\n" \
  "global_load_lds_dwordx4 v26, s[8:9] offset:2048\n" \
  "global_load_lds_dwordx4 v27, s[8:9] offset:3072\n" \
  "s_add_u32 s8, s8, 0x100000\n" \
  "s_addc_u32 s9, s9, 0\n" \
  "s_waitcnt vmcnt(8) lgkmcnt(0)\n" \
  "s_barrier\n" \
  "ds_read_b64_tr_b16 v[96:97], v28 offset:8192\n" \
  "ds_read_b64_tr_b16 v[98:99], v28 offset:8704\n" \
  "ds_read_b64_tr_b16 v[100:101], v28 offset:40960\n" \
  "ds_read_b64_tr_b16 v[102:103], v28 offset:41472\n" \
  "ds_read_b64_tr_b16 v[104:105], v17 offset:8192\n" \
  "ds_read_b64_tr_b16 v[106:107], v17 offset:8704\n" \
  "ds_read_b64_tr_b16 v[108:109], v17 offset:40960\n" \
  "ds_read_b64_tr_b16 v[110:111], v17 offset:41472\n" \
  "ds_read_b64_tr_b16 v[224:225], v18 offset:8192\n" \
  "ds_read_b64_tr_b16 v[226:227], v18 offset:8704\n" \
  "ds_read_b64_tr_b16 v[228:229], v18 offset:40960\n" \
  "ds_read_b64_tr_b16 v[230:231], v18 offset:41472\n" \
  "ds_read_b64_tr_b16 v[232:233], v19 offset:8192\n" \
  "ds_read_b64_tr_b16 v[234:235], v19 offset:8704\n" \
  "ds_read_b64_tr_b16 v[236:237], v19 offset:40960\n" \
  "ds_read_b64_tr_b16 v[238:239], v19 offset:41472\n" \
  "s_add_u32 m0, s19, 4096\n" \
  "s_nop 0\n" \
  "global_load_lds_dwordx4 v24, s[8:9] offset:0\n" \
  "global_load_lds_dwordx4 v25, s[8:9] offset:1024\n" \
  "global_load_lds_dwordx4 v26, s[8:9] offset:2048\n" \
  "global_load_lds_dwordx4 v27, s[8:9] offset:3072\n" \
  "s_add_u32 s8, s8, 0x100000\n" \
  "s_addc_u32 s9, s9, 0\n" \
  "s_waitcnt vmcnt(8) lgkmcnt(0)\n" \
  "s_barrier\n" \
  "ds_read_b64_tr_b16 v[112:113], v28 offset:12288\n" \
  "ds_read_b64_tr_b16 v[114:115], v28 offset:12800\n" \
  "ds_read_b64_tr_b16 v[116:117], v28 offset:45056\n" \
  "ds_read_b64_tr_b16 v[118:119], v28 offset:45568\n" \
  "ds_read_b64_tr_b16 v[120:121], v17 offset:12288\n" \
  "ds_read_b64_tr_b16 v[122:123], v17 offset:12800\n" \
  "ds_read_b64_tr_b16 v[124:125], v17 offset:45056\n" \
  "ds_read_b64_tr_b16 v[126:127], v17 offset:45568\n" \
  "ds_read_b64_tr_b16 v[240:241], v18 offset:12288\n" \
  "ds_read_b64_tr_b16 v[242:243], v18 offset:12800\n" \
  "ds_read_b64_tr_b16 v[244:245], v18 offset:45056\n" \
  "ds_read_b64_tr_b16 v[246:247], v18 offset:45568\n" \
  "ds_read_b64_tr_b16 v[248:249], v19 offset:12288\n" \
  "ds_read_b64_tr_b16 v[250:251], v19 offset:12800\n" \
  "ds_read_b64_tr_b16 v[252:253], v19 offset:45056\n" \
  "ds_read_b64_tr_b16 v[254:255], v19 offset:45568\n" \
  "s_add_u32 m0, s19, 8192\n" \
  "s_nop 0\n" \
  "global_load_lds_dwordx4 v24, s[8:9] offset:0\n" \
  "global_load_lds_dwordx4 v25, s[8:9] offset:1024\n" \
  "global_load_lds_dwordx4 v26, s[8:9] offset:2048\n" \
  "global_load_lds_dwordx4 v27, s[8:9] offset:3072\n" \
  "s_add_u32 s8, s8, 0x100000\n" \
  "s_addc_u32 s9, s9, 0\n" \
  "s_waitcnt vmcnt(8) lgkmcnt(0)\n" \
  "s_barrier\n" \
  "ds_read_b64_tr_b16 v[128:129], v28 offset:0\n" \
  "ds_read_b64_tr_b16 v[130:131], v28 offset:512\n" \
  "ds_read_b64_tr_b16 v[132:133], v28 offset:32768\n" \
  "ds_read_b64_tr_b16 v[134:135], v28 offset:33280\n" \
  "ds_read_b64_tr_b16 v[136:137], v17 offset:0\n" \
  "ds_read_b64_tr_b16 v[138:139], v17 offset:512\n" \
  "ds_read_b64_tr_b16 v[140:141], v17 offset:32768\n" \
  "ds_read_b64_tr_b16 v[142:143], v17 offset:33280\n" \
  "s_add_u32 m0, s19, 12288\n" \
  "s_nop 0\n" \
  "global_load_lds_dwordx4 v24, s[8:9] offset:0\n" \
  "global_load_lds_dwordx4 v25, s[8:9] offset:1024\n" \
  "global_load_lds_dwordx4 v26, s[8:9] offset:2048\n" \
  "global_load_lds_dwordx4 v27, s[8:9] offset:3072\n" \
  "s_add_u32 s8, s8, 0x100000\n" \
  "s_addc_u32 s9, s9, 0\n" \
  "s_waitcnt vmcnt(8) lgkmcnt(0)\n" \
  "s_barrier\n" \
  "ds_write_b128 v6, v[160:163] offset:0\n" \
  "ds_write_b128 v6, v[164:167] offset:1024\n" \
  "ds_write_b128 v6, v[168:171] offset:2048\n" \
  "ds_write_b128 v6, v[172:175] offset:3072\n" \
  "ds_read_b64_tr_b16 v[144:145], v28 offset:4096\n" \
  "ds_read_b64_tr_b16 v[146:147], v28 offset:4608\n" \
  "ds_read_b64_tr_b16 v[148:149], v28 offset:36864\n" \
  "ds_read_b64_tr_b16 v[150:151], v28 offset:37376\n" \
  "ds_read_b64_tr_b16 v[152:153], v17 offset:4096\n" \
  "ds_read_b64_tr_b16 v[154:155], v17 offset:4608\n" \
  "ds_read_b64_tr_b16 v[156:157], v17 offset:36864\n" \
  "ds_read_b64_tr_b16 v[158:159], v17 offset:37376\n" \
  "s_waitcnt vmcnt(4) lgkmcnt(0)\n" \
  "s_barrier\n" \
  "ds_write_b128 v6, v[176:179] offset:4096\n" \
  "ds_write_b128 v6, v[180:183] offset:5120\n" \
  "ds_write_b128 v6, v[184:187] offset:6144\n" \
  "ds_write_b128 v6, v[188:191] offset:7168\n" \
  "ds_read_b64_tr_b16 v[160:161], v28 offset:8192\n" \
  "ds_read_b64_tr_b16 v[162:163], v28 offset:8704\n" \
  "ds_read_b64_tr_b16 v[164:165], v28 offset:40960\n" \
  "ds_read_b64_tr_b16 v[166:167], v28 offset:41472\n" \
  "ds_read_b64_tr_b16 v[168:169], v17 offset:8192\n" \
  "ds_read_b64_tr_b16 v[170:171], v17 offset:8704\n" \
  "ds_read_b64_tr_b16 v[172:173], v17 offset:40960\n" \
  "ds_read_b64_tr_b16 v[174:175], v17 offset:41472\n" \
  "s_waitcnt vmcnt(0) lgkmcnt(0)\n" \
  "s_barrier\n" \
  "ds_write_b128 v6, v[32:35] offset:8192\n" \
  "ds_write_b128 v6, v[36:39] offset:9216\n" \
  "ds_write_b128 v6, v[40:43] offset:10240\n" \
  "ds_write_b128 v6, v[44:47] offset:11264\n" \
  "ds_read_b64_tr_b16 v[176:177], v28 offset:12288\n" \
  "ds_read_b64_tr_b16 v[178:179], v28 offset:12800\n" \
  "ds_read_b64_tr_b16 v[180:181], v28 offset:45056\n" \
  "ds_read_b64_tr_b16 v[182:183], v28 offset:45568\n" \
  "ds_read_b64_tr_b16 v[184:185], v17 offset:12288\n" \
  "ds_read_b64_tr_b16 v[186:187], v17 offset:12800\n" \
  "ds_read_b64_tr_b16 v[188:189], v17 offset:45056\n" \
  "ds_read_b64_tr_b16 v[190:191], v17 offset:45568\n" \
  "ds_read_b32 v31, v30 offset:0\n" \
  "s_waitcnt lgkmcnt(0)\n" \
  "s_barrier\n" \
  "ds_write_b128 v6, v[48:51] offset:12288\n" \
  "ds_write_b128 v6, v[52:55] offset:13312\n" \
  "ds_write_b128 v6, v[56:59] offset:14336\n" \
  "ds_write_b128 v6, v[60:63] offset:15360\n" \
  "v_mul_f32 v31, 0x3f8e353f, v31\n" \
  "v_mul_f32 v30, 0x44870000, v31\n" \
  "v_mov_b64 v[12:13], 0\n" \
  "v_mov_b64 v[14:15], 0\n" \
  "v_mov_b64 v[16:17], 0\n" \
  "v_mov_b64 v[18:19], 0\n" \
  "v_mov_b64 v[20:21], 0\n" \
  "v_mov_b64 v[22:23], 0\n" \
  "v_readfirstlane_b32 s72, v31\n" \
  "v_readfirstlane_b32 s86, v30\n" \
  "s_lshr_b32 s86, s86, 23\n" \
  "s_addk_i32 s86, 0xff83\n" \
  "s_ashr_i32 s86, s86, 1\n" \
  "s_max_i32 s86, s86, -20\n" \
  "s_min_i32 s86, s86, 100\n" \
  "s_mov_b32 s82, s86\n" \
  "s_lshr_b32 s83, s72, 23\n" \
  "s_sub_i32 s83, s83, 243\n" \
  "s_waitcnt lgkmcnt(0)\n" \
  "s_add_u32 s88, s74, 157760\n" \
  "v_mov_b32 v59, s88\n" \
  "v_mov_b32 v34, 0xc9b26ca0\n" \
  "v_mov_b32 v35, 0xc9b26c86\n" \
  ".p2align 6\n" \
  "s_nop 0\n" \
  "s_nop 0\n" \
  "s_nop 0\n" \
  ".LCA_TOP_%=:\n" \
  "s_waitcnt lgkmcnt(0)\n" \
  "s_barrier\n" \
  "ds_read_b128 v[40:43], v1 offset:0\n" \
  "ds_read_b128 v[44:47], v1 offset:1024\n" \
  "ds_read_b128 v[48:51], v1 offset:2048\n" \
  "ds_read_b128 v[52:55], v1 offset:3072\n" \
  "ds_read_b128 v[56:59], v1 offset:4096\n" \
  "ds_read_b128 v[60:63], v1 offset:5120\n" \
  "s_cmp_lg_u32 s73, 0\n" \
  "s_cbranch_scc1 .LCA_NOZ_%=\n" \
  "s_xor_b32 s88, s60, 0x10000\n" \
  "s_add_u32 s88, s88, s61\n" \
  "buffer_store_dwordx2 v[34:35], v5, s[40:43], s88 offen sc1\n" \
  ".LCA_NOZ_%=:\n" \
  "s_waitcnt lgkmcnt(5)\n" \
  "v_smfmac_f32_16x16x64_bf16 v[20:23], v[40:43], v[192:199], v0\n" \
  "v_smfmac_f32_16x16x64_bf16 v[12:15], v[40:43], v[64:71], v0\n" \
  "v_smfmac_f32_16x16x64_bf16 v[16:19], v[40:43], v[128:135], v0\n" \
  "ds_read_b128 v[40:43], v1 offset:6144\n" \
  "s_waitcnt lgkmcnt(5)\n" \
  "v_smfmac_f32_16x16x64_bf16 v[20:23], v[44:47], v[200:207], v0\n" \
  "v_smfmac_f32_16x16x64_bf16 v[12:15], v[44:47], v[72:79], v0\n" \
  "v_smfmac_f32_16x16x64_bf16 v[16:19], v[44:47], v[136:143], v0\n" \
  "ds_read_b128 v[44:47], v1 offset:7168\n" \
  "s_waitcnt lgkmcnt(5)\n" \
  "v_smfmac_f32_16x16x64_bf16 v[20:23], v[48:51], v[208:215], v0\n" \
  "v_smfmac_f32_16x16x64_bf16 v[12:15], v[48:51], v[80:87], v0\n" \
  "v_smfmac_f32_16x16x64_bf16 v[16:19], v[48:51], v[144:151], v0\n" \
  "ds_read_b128 v[24:27], v1 offset:10240\n" \
  "ds_read_b128 v[28:31], v1 offset:14336\n" \
  "s_waitcnt lgkmcnt(6)\n" \
  "v_smfmac_f32_16x16x64_bf16 v[20:23], v[52:55], v[216:223], v0\n" \
  "v_smfmac_f32_16x16x64_bf16 v[12:15], v[52:55], v[88:95], v0\n" \
  "v_smfmac_f32_16x16x64_bf16 v[16:19], v[52:55], v[152:159], v0\n" \
  "ds_read_b128 v[32:35], v1 offset:11264\n" \
  "ds_read_b128 v[36:39], v1 offset:15360\n" \
  "s_waitcnt lgkmcnt(7)\n" \
  "v_smfmac_f32_16x16x64_bf16 v[20:23], v[56:59], v[224:231], v0\n" \
  "v_smfmac_f32_16x16x64_bf16 v[12:15], v[56:59], v[96:103], v0\n" \
  "v_smfmac_f32_16x16x64_bf16 v[16:19], v[56:59], v[160:167], v0\n" \
  "ds_read_b128 v[56:59], v1 offset:12288\n" \
  "s_waitcnt lgkmcnt(7)\n" \
  "v_smfmac_f32_16x16x64_bf16 v[20:23], v[60:63], v[232:239], v0\n" \
  "v_smfmac_f32_16x16x64_bf16 v[12:15], v[60:63], v[104:111], v0\n" \
  "v_smfmac_f32_16x16x64_bf16 v[16:19], v[60:63], v[168:175], v0\n" \
  "ds_read_b128 v[60:63], v1 offset:16384\n" \
  "s_waitcnt lgkmcnt(7)\n" \
  "v_smfmac_f32_16x16x64_bf16 v[20:23], v[40:43], v[240:247], v0\n" \
  "v_smfmac_f32_16x16x64_bf16 v[12:15], v[40:43], v[112:119], v0\n" \
  "v_smfmac_f32_16x16x64_bf16 v[16:19], v[40:43], v[176:183], v0\n" \
  "s_waitcnt lgkmcnt(6)\n" \
  "v_smfmac_f32_16x16x64_bf16 v[20:23], v[44:47], v[248:255], v0\n" \
  "v_smfmac_f32_16x16x64_bf16 v[12:15], v[44:47], v[120:127], v0\n" \
  "v_smfmac_f32_16x16x64_bf16 v[16:19], v[44:47], v[184:191], v0\n" \
  "s_cmp_eq_u32 s71, 0\n" \
  "v_permlane32_swap_b32 v20, v22\n" \
  "v_permlane32_swap_b32 v21, v23\n" \
  "v_pk_add_f32 v[20:21], v[20:21], v[22:23]\n" \
  "s_mov_b64 vcc, s[76:77]\n" \
  "v_permlane32_swap_b32 v12, v14\n" \
  "v_permlane32_swap_b32 v13, v15\n" \
  "v_cvt_pk_bf16_f32 v20, v20, v21\n" \
  "v_pk_add_f32 v[12:13], v[12:13], v[14:15]\n" \
  "v_permlane32_swap_b32 v16, v18\n" \
  "v_permlane32_swap_b32 v17, v19\n" \
  "ds_write_b32 v8, v20 offset:2048\n" \
  "v_cvt_pk_bf16_f32 v12, v12, v13\n" \
  "v_pk_add_f32 v[16:17], v[16:17], v[18:19]\n" \
  "s_nop 0\n" \
  "v_cvt_pk_bf16_f32 v13, v16, v17\n" \
  "ds_write_b64 v8, v[12:13]\n" \
  "s_setprio 1\n" \
  "s_waitcnt lgkmcnt(0)\n" \
  "s_barrier\n" \
  "s_cbranch_scc1 .LCA_LAST_%=\n" \
  "ds_read_b64_tr_b16 v[40:41], v2 offset:0\n" \
  "ds_read_b64_tr_b16 v[42:43], v3 offset:0\n" \
  "ds_read_b64_tr_b16 v[44:45], v2 offset:1024\n" \
  "ds_read_b64_tr_b16 v[46:47], v3 offset:1024\n" \
  "ds_read_b64_tr_b16 v[48:49], v2 offset:2048\n" \
  "ds_read_b64_tr_b16 v[50:51], v3 offset:2048\n" \
  "ds_read_b64_tr_b16 v[52:53], v2 offset:3072\n" \
  "ds_read_b64_tr_b16 v[54:55], v3 offset:3072\n" \
  "ds_read_b32 v11, v1 offset:26688\n" \
  "s_waitcnt lgkmcnt(7)\n" \
  "v_mfma_f32_16x16x32_bf16 v[12:15], v[24:27], v[40:43], 0\n" \
  "v_mfma_f32_16x16x32_bf16 v[16:19], v[28:31], v[40:43], 0\n" \
  "ds_read_b128 v[24:27], v1 offset:13312\n" \
  "ds_read_b128 v[28:31], v1 offset:17408\n" \
  "v_mfma_f32_16x16x32_bf16 v[20:23], v[40:43], v[40:43], 0\n" \
  "s_waitcnt lgkmcnt(7)\n" \
  "v_mfma_f32_16x16x32_bf16 v[12:15], v[32:35], v[44:47], v[12:15]\n" \
  "v_mfma_f32_16x16x32_bf16 v[16:19], v[36:39], v[44:47], v[16:19]\n" \
  "v_mfma_f32_16x16x32_bf16 v[20:23], v[44:47], v[44:47], v[20:23]\n" \
  "s_waitcnt lgkmcnt(2)\n" \
  "v_mfma_f32_16x16x32_bf16 v[12:15], v[56:59], v[48:51], v[12:15]\n" \
  "v_readfirstlane_b32 s86, v11\n" \
  "v_mfma_f32_16x16x32_bf16 v[16:19], v[60:63], v[48:51], v[16:19]\n" \
  "ds_read_b128 v[32:35], v6 offset:8192\n" \
  "ds_read_b128 v[36:39], v6 offset:9216\n" \
  "v_mfma_f32_16x16x32_bf16 v[20:23], v[48:51], v[48:51], v[20:23]\n" \
  "s_lshr_b32 s86, s86, 23\n" \
  "s_add_i32 s86, s86, s83\n" \
  "s_ashr_i32 s86, s86, 1\n" \
  "ds_read_b128 v[48:51], v10 offset:18432\n" \
  "v_mfma_f32_16x16x32_bf16 v[20:23], v[52:55], v[52:55], v[20:23]\n" \
  "s_max_i32 s86, s86, s82\n" \
  "s_min_i32 s86, s86, 100\n" \
  "s_lshl_b32 s86, s86, 23\n" \
  "s_sub_i32 s84, 0xc8800000, s86\n" \
  "s_waitcnt lgkmcnt(3)\n" \
  "v_mfma_f32_16x16x32_bf16 v[12:15], v[24:27], v[52:55], v[12:15]\n" \
  "v_mfma_f32_16x16x32_bf16 v[16:19], v[28:31], v[52:55], v[16:19]\n" \
  "ds_read_b128 v[52:55], v10 offset:22528\n" \
  "ds_read_b128 v[56:59], v6 offset:0\n" \
  "ds_read_b128 v[60:63], v6 offset:1024\n" \
  "ds_read_b128 v[24:27], v6 offset:2048\n" \
  "ds_read_b128 v[28:31], v6 offset:3072\n" \
  "v_cndmask_b32_dpp v20, v23, v22, vcc quad_perm:[2,3,0,1] row_mask:0xf bank_mask:0xf\n" \
  "v_mov_b32 v41, 0\n" \
  "v_mov_b32 v42, 0\n" \
  "v_cndmask_b32_dpp v12, v16, v12, vcc quad_perm:[2,3,0,1] row_mask:0xf bank_mask:0xf\n" \
  "v_cndmask_b32_dpp v13, v17, v13, vcc quad_perm:[2,3,0,1] row_mask:0xf bank_mask:0xf\n" \
  "v_cndmask_b32_dpp v14, v18, v14, vcc quad_perm:[2,3,0,1] row_mask:0xf bank_mask:0xf\n" \
  "v_cndmask_b32_dpp v15, v19, v15, vcc quad_perm:[2,3,0,1] row_mask:0xf bank_mask:0xf\n" \
  "v_maximum3_f32 v16, v20, v12, v13\n" \
  "v_maximum3_f32 v16, v16, v14, v15\n" \
  "s_nop 1\n" \
  "v_max_f32_dpp v16, v16, v16 quad_perm:[2,3,0,1] row_mask:0xf bank_mask:0xf\n" \
  "v_mov_b32 v17, v16\n" \
  "s_nop 1\n" \
  "v_permlane16_swap_b32 v16, v17\n" \
  "v_max_f32 v16, v16, v17\n" \
  "v_mov_b32 v17, v16\n" \
  "s_nop 1\n" \
  "v_permlane32_swap_b32 v16, v17\n" \
  "v_max_f32 v16, v16, v17\n" \
  "v_mul_f32 v16, s78, v16\n" \
  "v_pk_fma_f32 v[12:13], v[12:13], s[78:79], v[16:17] op_sel_hi:[1,0,0] neg_lo:[0,0,1] neg_hi:[0,0,1]\n" \
  "v_pk_fma_f32 v[14:15], v[14:15], s[78:79], v[16:17] op_sel_hi:[1,0,0] neg_lo:[0,0,1] neg_hi:[0,0,1]\n" \
  "v_fma_f32 v20, v20, s78, -v16\n" \
  "v_exp_f32 v12, v12\n" \
  "v_exp_f32 v13, v13\n" \
  "v_exp_f32 v14, v14\n" \
  "v_exp_f32 v15, v15\n" \
  "v_exp_f32 v20, v20\n" \
  "s_waitcnt lgkmcnt(4)\n" \
  "v_cvt_pk_bf16_f32 v44, v12, v13\n" \
  "v_cvt_pk_bf16_f32 v45, v14, v15\n" \
  "v_pk_add_f32 v[8:9], v[12:13], v[14:15]\n" \
  "v_mov_b32_dpp v46, v44 quad_perm:[2,3,0,1] row_mask:0xf bank_mask:0xf\n" \
  "v_mov_b32_dpp v47, v45 quad_perm:[2,3,0,1] row_mask:0xf bank_mask:0xf\n" \
  "v_add_f32 v10, v8, v20\n" \
  "v_add_f32 v10, v10, v9\n" \
  "v_mfma_f32_16x16x32_bf16 v[12:15], v[48:51], v[44:47], 0\n" \
  "v_mov_b32_dpp v21, v20 quad_perm:[2,3,0,1] row_mask:0xf bank_mask:0xf\n" \
  "v_add_f32_dpp v10, v10, v10 quad_perm:[2,3,0,1] row_mask:0xf bank_mask:0xf\n" \
  "v_mov_b32 v11, v10\n" \
  "v_mfma_f32_16x16x32_bf16 v[16:19], v[52:55], v[44:47], 0\n" \
  "s_setprio 3\n" \
  "s_barrier\n" \
  "ds_read_b64 v[22:23], v7 offset:0\n" \
  "s_nop 0\n" \
  "v_permlane16_swap_b32 v10, v11\n" \
  "v_add_f32 v10, v10, v11\n" \
  "v_mov_b32 v11, v10\n" \
  "v_mov_b64 v[8:9], s[92:93]\n" \
  "s_nop 0\n" \
  "v_permlane32_swap_b32 v10, v11\n" \
  "v_add_f32 v10, v10, v11\n" \
  "v_rcp_f32 v10, v10\n" \
  "s_nop 0\n" \
  "v_mul_f32 v10, s84, v10\n" \
  "v_pk_mul_f32 v[20:21], v[20:21], v[10:11] op_sel_hi:[1,0]\n" \
  "v_pk_mul_f32 v[12:13], v[12:13], v[10:11] op_sel_hi:[1,0]\n" \
  "v_pk_mul_f32 v[14:15], v[14:15], v[10:11] op_sel_hi:[1,0]\n" \
  "v_cvt_pk_bf16_f32 v40, v20, v21\n" \
  "v_pk_mul_f32 v[16:17], v[16:17], v[10:11] op_sel_hi:[1,0]\n" \
  "v_cvt_pk_bf16_f32 v43, v20, v21\n" \
  "v_pk_mul_f32 v[18:19], v[18:19], v[10:11] op_sel_hi:[1,0]\n" \
  "s_waitcnt lgkmcnt(0)\n" \
  "v_mfma_f32_16x16x16_bf16 v[12:15], v[22:23], v[40:41], v[12:15]\n" \
  "v_mfma_f32_16x16x16_bf16 v[16:19], v[22:23], v[42:43], v[16:19]\n" \
  "v_mov_b64 v[10:11], s[92:93]\n" \
  "ds_read_b128 v[48:51], v6 offset:10240\n" \
  "ds_read_b128 v[52:55], v6 offset:11264\n" \
  "v_mov_b64 v[20:21], s[92:93]\n" \
  "v_mov_b64 v[22:23], s[92:93]\n" \
  "s_nop 1\n" \
  "v_cvt_pk_bf16_f32 v40, v12, v13\n" \
  "v_cvt_pk_bf16_f32 v41, v14, v15\n" \
  "v_cvt_pk_bf16_f32 v42, v16, v17\n" \
  "ds_write_b64 v4, v[40:41]\n" \
  "v_cvt_pk_bf16_f32 v43, v18, v19\n" \
  "ds_write_b64 v4, v[42:43] offset:1024\n" \
  "v_mov_b64 v[12:13], s[92:93]\n" \
  "v_mov_b64 v[14:15], s[92:93]\n" \
  "v_mov_b64 v[16:17], s[92:93]\n" \
  "v_mov_b64 v[18:19], s[92:93]\n" \
  "s_setprio 0\n" \
  "s_waitcnt lgkmcnt(0)\n" \
  "s_barrier\n" \
  "ds_read_b128 v[40:43], v1 offset:8192\n" \
  "ds_read_b128 v[44:47], v1 offset:9216\n" \
  "s_waitcnt lgkmcnt(1)\n" \
  "v_smfmac_f32_16x16x64_bf16 v[12:15], v[40:43], v[56:63], v0\n" \
  "ds_read_b128 v[56:59], v6 offset:4096\n" \
  "ds_read_b128 v[60:63], v6 offset:5120\n" \
  "v_smfmac_f32_16x16x64_bf16 v[16:19], v[40:43], v[24:31], v0\n" \
  "ds_read_b128 v[24:27], v6 offset:6144\n" \
  "ds_read_b128 v[28:31], v6 offset:7168\n" \
  "s_waitcnt lgkmcnt(4)\n" \
  "v_smfmac_f32_16x16x64_bf16 v[12:15], v[44:47], v[32:39], v0\n" \
  "ds_read_b128 v[32:35], v6 offset:12288\n" \
  "ds_read_b128 v[36:39], v6 offset:13312\n" \
  "v_smfmac_f32_16x16x64_bf16 v[16:19], v[44:47], v[48:55], v0\n" \
  "ds_read_b128 v[48:51], v6 offset:14336\n" \
  "ds_read_b128 v[52:55], v6 offset:15360\n" \
  "s_nop 5\n" \
  "v_permlane32_swap_b32 v12, v17\n" \
  "v_permlane32_swap_b32 v13, v16\n" \
  "v_permlane32_swap_b32 v14, v19\n" \
  "v_permlane32_swap_b32 v15, v18\n" \
  "v_pk_add_f32 v[12:13], v[12:13], v[16:17] op_sel:[0,1] op_sel_hi:[1,0]\n" \
  "v_pk_add_f32 v[14:15], v[14:15], v[18:19] op_sel:[0,1] op_sel_hi:[1,0]\n" \
  "buffer_atomic_add_x2 v[12:13], v5, s[40:43], s56 offen\n" \
  "buffer_atomic_add_x2 v[14:15], v5, s[40:43], s57 offen\n" \
  "s_waitcnt lgkmcnt(6)\n" \
  "v_smfmac_f32_16x16x64_bf16 v[20:23], v[40:43], v[56:63], v0\n" \
  "s_waitcnt lgkmcnt(4)\n" \
  "v_smfmac_f32_16x16x64_bf16 v[8:11], v[40:43], v[24:31], v0\n" \
  "s_waitcnt lgkmcnt(2)\n" \
  "v_smfmac_f32_16x16x64_bf16 v[20:23], v[44:47], v[32:39], v0\n" \
  "s_waitcnt lgkmcnt(0)\n" \
  "v_smfmac_f32_16x16x64_bf16 v[8:11], v[44:47], v[48:55], v0\n" \
  "s_nop 7\n" \
  "v_permlane32_swap_b32 v20, v9\n" \
  "v_permlane32_swap_b32 v21, v8\n" \
  "v_permlane32_swap_b32 v22, v11\n" \
  "v_permlane32_swap_b32 v23, v10\n" \
  "v_pk_add_f32 v[20:21], v[20:21], v[8:9] op_sel:[0,1] op_sel_hi:[1,0]\n" \
  "v_pk_add_f32 v[22:23], v[22:23], v[10:11] op_sel:[0,1] op_sel_hi:[1,0]\n" \
  "buffer_atomic_add_x2 v[20:21], v5, s[40:43], s58 offen\n" \
  "buffer_atomic_add_x2 v[22:23], v5, s[40:43], s59 offen\n" \
  "s_memrealtime s[38:39]\n" \
  "v_mbcnt_lo_u32_b32 v48, -1, 0\n" \
  "v_mbcnt_hi_u32_b32 v48, -1, v48\n" \
  "s_add_u32 s88, s74, 157760\n" \
  "v_mov_b32 v59, s88\n" \
  "v_lshl_add_u32 v52, v48, 2, v59\n" \
  "ds_read_b32 v52, v52 offset:192\n" \
  "v_lshrrev_b32 v49, 3, v48\n" \
  "v_and_b32 v50, 7, v48\n" \
  "v_lshlrev_b32 v50, 4, v50\n" \
  "v_lshl_or_b32 v44, v49, 8, v50\n" \
  "s_mov_b32 s87, 0x40000\n" \
  "v_lshrrev_b32 v49, 2, v48\n" \
  "v_lshrrev_b32 v50, 4, v48\n" \
  "v_bitop3_b32 v50, v49, v50, 3 bitop3:0x28\n" \
  "v_lshl_add_u32 v8, v50, 3, s97\n" \
  "v_and_b32 v50, 3, v48\n" \
  "v_lshl_add_u32 v8, v50, 5, v8\n" \
  "v_and_b32 v49, 1, v49\n" \
  "v_lshl_add_u32 v8, v49, 8, v8\n" \
  "v_and_b32 v49, 8, v48\n" \
  "v_lshl_add_u32 v8, v49, 4, v8\n" \
  "v_lshrrev_b32 v49, 5, v48\n" \
  "v_add_u32 v10, s99, v1\n" \
  "s_lshr_b32 s88, s99, 2\n" \
  "s_add_u32 s88, s88, s74\n" \
  "s_add_u32 s88, s88, 139264\n" \
  "v_lshl_add_u32 v7, v49, 10, s88\n" \
  "v_and_b32 v49, 31, v48\n" \
  "v_lshl_add_u32 v7, v49, 3, v7\n" \
  "v_mov_b64 v[12:13], 0\n" \
  "v_mov_b64 v[14:15], 0\n" \
  "v_mov_b64 v[16:17], 0\n" \
  "v_mov_b64 v[18:19], 0\n" \
  "v_mov_b64 v[20:21], 0\n" \
  "v_mov_b64 v[22:23], 0\n" \
  "v_mov_b32 v34, 0xc9b26ca0\n" \
  "v_mov_b32 v35, 0xc9b26c86\n" \
  "v_and_b32 v50, 3, v48\n" \
  "v_lshl_add_u32 v40, v50, 2, s98\n" \
  "v_lshrrev_b32 v50, 4, v48\n" \
  "v_mad_u32_u24 v40, v50, s95, v40\n" \
  "v_bfe_u32 v49, v48, 3, 1\n" \
  "v_lshl_add_u32 v40, v49, 6, v40\n" \
  "v_bfe_u32 v49, v48, 2, 1\n" \
  "v_lshl_add_u32 v40, v49, 7, v40\n" \
  "v_xor_b32 v41, 16, v40\n" \
  "v_xor_b32 v42, 32, v40\n" \
  "v_xor_b32 v43, 48, v40\n" \
  "s_add_i32 s80, s86, 0x35000000\n" \
  "v_mov_b32 v11, 0\n" \
  "s_mov_b32 s89, s60\n" \
  "s_bitcmp1_b32 s60, 7\n" \
  "s_cselect_b32 s88, 0x10000, 0\n" \
  "s_bitset1_b32 s88, 7\n" \
  "s_xor_b32 s56, s56, s88\n" \
  "s_xor_b32 s57, s57, s88\n" \
  "s_xor_b32 s58, s58, s88\n" \
  "s_xor_b32 s59, s59, s88\n" \
  "s_xor_b32 s60, s60, s88\n" \
  "s_sub_u32 s71, s71, 1\n" \
  "s_waitcnt lgkmcnt(0)\n" \
  "s_sub_u32 s39, s38, 150\n" \
  "v_subrev_u32 v52, s39, v52\n" \
  "v_cmp_lt_u32 vcc, 0x1000, v52\n" \
  "s_cmp_eq_u64 vcc, 0\n" \
  "s_cbranch_scc1 .LCA_OKM_%=\n" \
  ".LCA_AGN_%=:\n" \
  "buffer_load_dword v52, v5, s[0:3], s66 offen sc1\n" \
  "s_sub_u32 s87, s87, 1\n" \
  "s_cbranch_scc1 .LCA_END_%=\n" \
  "s_waitcnt vmcnt(0)\n" \
  "v_cmp_eq_u32 vcc, 0, v52\n" \
  "v_subrev_u32 v52, s39, v52\n" \
  "s_cmp_eq_u64 vcc, 0\n" \
  "s_cbranch_scc1 .LCA_OKM_%=\n" \
  "s_sleep 4\n" \
  "s_branch .LCA_AGN_%=\n" \
  ".LCA_OKM_%=:\n" \
  "s_nop 1\n" \
  "v_max_i32_dpp v52, v52, v52 row_ror:1 row_mask:0xf bank_mask:0xf\n" \
  "s_nop 1\n" \
  "v_max_i32_dpp v52, v52, v52 row_ror:2 row_mask:0xf bank_mask:0xf\n" \
  "s_nop 1\n" \
  "v_max_i32_dpp v52, v52, v52 row_ror:4 row_mask:0xf bank_mask:0xf\n" \
  "s_nop 1\n" \
  "v_max_i32_dpp v52, v52, v52 row_ror:8 row_mask:0xf bank_mask:0xf\n" \
  "s_movk_i32 vcc_lo, 150\n" \
  "v_readlane_b32 s88, v52, 0\n" \
  "s_max_i32 vcc_lo, vcc_lo, s88\n" \
  "v_readlane_b32 s88, v52, 16\n" \
  "s_max_i32 vcc_lo, vcc_lo, s88\n" \
  "v_readlane_b32 s88, v52, 32\n" \
  "s_max_i32 vcc_lo, vcc_lo, s88\n" \
  "v_readlane_b32 s88, v52, 48\n" \
  "s_max_i32 vcc_lo, vcc_lo, s88\n" \
  "s_add_u32 s66, s66, 1024\n" \
  "s_add_u32 vcc_lo, vcc_lo, s39\n" \
  "s_add_u32 vcc_lo, vcc_lo, 23\n" \
  ".LCA_TW_%=:\n" \
  "s_memrealtime s[38:39]\n" \
  "s_waitcnt lgkmcnt(0)\n" \
  "s_sub_u32 s38, s38, vcc_lo\n" \
  "s_cmp_lt_i32 s38, 0\n" \
  "s_cbranch_scc1 .LCA_TW_%=\n" \
  "buffer_load_dwordx4 v[24:27], v44, s[40:43], s89 offen sc1\n" \
  "buffer_load_dwordx4 v[28:31], v44, s[40:43], s89 offen offset:2048 sc1\n" \
  ".LCA_SEE_%=:\n" \
  "s_waitcnt vmcnt(1)\n" \
  "v_cvt_f32_i32 v24, v24\n" \
  "v_cvt_f32_i32 v25, v25\n" \
  "v_cvt_f32_i32 v26, v26\n" \
  "v_cvt_f32_i32 v27, v27\n" \
  "v_max_f32 v45, |v24|, |v26|\n" \
  "v_pk_mul_f32 v[24:25], v[24:25], s[80:81] op_sel_hi:[1,0]\n" \
  "v_pk_mul_f32 v[26:27], v[26:27], s[80:81] op_sel_hi:[1,0]\n" \
  "s_nop 0\n" \
  "v_cvt_pk_bf16_f32 v32, v24, v26\n" \
  "v_cvt_pk_bf16_f32 v33, v25, v27\n" \
  "ds_write_b32 v40, v32\n" \
  "ds_write_b32 v41, v33\n" \
  "s_waitcnt vmcnt(0)\n" \
  "v_cvt_f32_i32 v28, v28\n" \
  "v_cvt_f32_i32 v30, v30\n" \
  "v_cvt_f32_i32 v29, v29\n" \
  "v_max3_f32 v45, v45, |v28|, |v30|\n" \
  "v_cvt_f32_i32 v31, v31\n" \
  "v_cmp_lt_f32 vcc, s91, v45\n" \
  "v_pk_mul_f32 v[28:29], v[28:29], s[80:81] op_sel_hi:[1,0]\n" \
  "v_pk_mul_f32 v[30:31], v[30:31], s[80:81] op_sel_hi:[1,0]\n" \
  "s_cmp_eq_u64 vcc, 0\n" \
  "s_cbranch_scc0 .LCA_POLL_%=\n" \
  "v_cvt_pk_bf16_f32 v36, v28, v30\n" \
  "v_cvt_pk_bf16_f32 v37, v29, v31\n" \
  "ds_write_b32 v42, v36\n" \
  "ds_write_b32 v43, v37\n" \
  "v_dot2c_f32_bf16 v11, v32, v32\n" \
  "v_dot2c_f32_bf16 v11, v33, v33\n" \
  "v_dot2c_f32_bf16 v11, v36, v36\n" \
  "v_dot2c_f32_bf16 v11, v37, v37\n" \
  "s_nop 1\n" \
  "s_nop 0\n" \
  "v_cmp_lt_f32 vcc, 4.0, v11\n" \
  "s_cbranch_vccz .LCA_TOP_%=\n" \
  "s_mov_b64 exec, vcc\n" \
  "ds_max_u32 v59, v11\n" \
  "s_mov_b64 exec, -1\n" \
  "s_branch .LCA_TOP_%=\n" \
  ".LCA_POLL_%=:\n" \
  "buffer_load_dwordx4 v[24:27], v44, s[40:43], s89 offen sc1\n" \
  "buffer_load_dwordx4 v[28:31], v44, s[40:43], s89 offen offset:2048 sc1\n" \
  "s_sub_u32 s87, s87, 1\n" \
  "s_cbranch_scc1 .LCA_END_%=\n" \
  "s_branch .LCA_SEE_%=\n" \
  ".LCA_LAST_%=:\n" \
  ".LCA_END_%=:\n" \
  "s_endpgm\n"
#define CHAIN_ASM_VO \
  "s_mov_b32 s95, 272\n" \
  "s_mov_b32 s91, 0x4c7c2000\n" \
  "s_mov_b32 s85, 0xc9b26c9b\n" \
  "s_mov_b32 s92, 0xc9326c9b\n" \
  "s_mov_b32 s93, 0xc9326c9b\n" \
  "s_mov_b32 s78, 0x3e0293ee\n" \
  "s_mov_b32 s76, 0x33333333\n" \
  "s_mov_b32 s77, 0x33333333\n" \
  "s_lshl_b32 s98, s73, 10\n" \
  "s_add_u32 s98, s98, s74\n" \
  "s_add_u32 s98, s98, 131072\n" \
  "s_lshl_b32 s97, s73, 9\n" \
  "s_add_u32 s97, s97, s74\n" \
  "s_add_u32 s97, s97, 158724\n" \
  "s_lshl_b32 s99, s73, 11\n" \
  "s_add_u32 s99, s99, s74\n" \
  "s_add_u32 s99, s99, 133120\n" \
  "s_lshl_b32 s94, s73, 4\n" \
  "s_add_u32 s94, s94, s74\n" \
  "s_add_u32 s94, s94, 157760\n" \
  "v_mbcnt_lo_u32_b32 v48, -1, 0\n" \
  "v_mbcnt_hi_u32_b32 v48, -1, v48\n" \
  "s_add_u32 s88, s74, 157760\n" \
  "v_mov_b32 v59, s88\n" \
  "s_sub_u32 s88, s73, 4\n" \
  "v_lshrrev_b32 v49, 2, v48\n" \
  "v_lshrrev_b32 v50, 4, v48\n" \
  "v_bitop3_b32 v50, v49, v50, 3 bitop3:0x28\n" \
  "v_lshl_add_u32 v4, v50, 3, s97\n" \
  "v_and_b32 v50, 3, v48\n" \
  "v_lshl_add_u32 v4, v50, 5, v4\n" \
  "v_and_b32 v49, 1, v49\n" \
  "v_lshl_add_u32 v4, v49, 8, v4\n" \
  "v_and_b32 v49, 8, v48\n" \
  "v_lshl_add_u32 v4, v49, 4, v4\n" \
  "v_and_b32 v51, 15, v48\n" \
  "v_bfe_u32 v49, v48, 4, 1\n" \
  "v_lshrrev_b32 v50, 5, v48\n" \
  "s_lshl_b32 s89, s88, 10\n" \
  "s_add_u32 s89, s89, s74\n" \
  "s_add_u32 s89, s89, 158720\n" \
  "v_lshrrev_b32 v9, 3, v51\n" \
  "v_mul_u32_u24 v6, 41, v9\n" \
  "v_xor_b32 v6, v6, v51\n" \
  "v_lshl_add_u32 v6, v6, 3, s89\n" \
  "v_lshl_add_u32 v6, v49, 6, v6\n" \
  "v_lshl_add_u32 v6, v50, 9, v6\n" \
  "v_xor_b32 v9, 0x90, v6\n" \
  "v_lshlrev_b32 v8, 4, v49\n" \
  "v_lshl_add_u32 v8, v51, 5, v8\n" \
  "v_lshl_add_u32 v8, v50, 3, v8\n" \
  "s_lshl_b32 s89, s88, 8\n" \
  "s_add_u32 s89, s89, s74\n" \
  "s_add_u32 s89, s89, 139264\n" \
  "v_and_b32 v49, 31, v48\n" \
  "v_lshl_add_u32 v5, v49, 3, s89\n" \
  "v_lshl_add_u32 v5, v50, 10, v5\n" \
  "v_lshrrev_b32 v49, 2, v51\n" \
  "v_and_b32 v50, 5, v51\n" \
  "v_and_or_b32 v49, v49, 2, v50\n" \
  "v_and_b32 v50, 48, v48\n" \
  "v_lshlrev_b32 v7, 3, v50\n" \
  "v_lshl_add_u32 v7, v49, 4, v7\n" \
  "v_bitop3_b32 v49, v51, 2, 2 bitop3:0x0c\n" \
  "v_lshl_add_u32 v7, v49, 29, v7\n" \
  "v_mov_b64 v[12:13], 0\n" \
  "v_mov_b64 v[14:15], 0\n" \
  "v_mov_b64 v[16:17], 0\n" \
  "v_mov_b64 v[18:19], 0\n" \
  "v_mov_b64 v[20:21], 0\n" \
  "v_mov_b64 v[22:23], 0\n" \
  "v_lshlrev_b32 v10, 4, v48\n" \
  "s_add_u32 s78, s74, 157952\n" \
  "v_lshl_add_u32 v52, v48, 2, v59\n" \
  "ds_write_b32 v52, v12 offset:192\n" \
  "s_movk_i32 s79, 105\n" \
  "s_mov_b32 s87, 0x40000\n" \
  "v_subrev_u32 v30, 64, v59\n" \
  "v_add_u32 v17, 0x10000, v28\n" \
  "v_xor_b32 v18, 128, v28\n" \
  "v_xor_b32 v19, 128, v17\n" \
  "ds_write_b32 v30, v12 offset:0\n" \
  "ds_write_b32 v30, v12 offset:32\n" \
  "ds_write_b32 v30, v12 offset:64\n" \
  "v_xor_b32 v20, 0, v29\n" \
  "v_xor_b32 v21, 32, v29\n" \
  "v_xor_b32 v22, 64, v29\n" \
  "v_xor_b32 v23, 96, v29\n" \
  "s_waitcnt vmcnt(12) lgkmcnt(0)\n" \
  "s_barrier\n" \
  "ds_read_b64_tr_b16 v[96:97], v20 offset:0\n" \
  "ds_read_b64_tr_b16 v[98:99], v20 offset:2048\n" \
  "ds_read_b64_tr_b16 v[104:105], v21 offset:0\n" \
  "ds_read_b64_tr_b16 v[106:107], v21 offset:2048\n" \
  "ds_read_b64_tr_b16 v[112:113], v22 offset:0\n" \
  "ds_read_b64_tr_b16 v[114:115], v22 offset:2048\n" \
  "ds_read_b64_tr_b16 v[120:121], v23 offset:0\n" \
  "ds_read_b64_tr_b16 v[122:123], v23 offset:2048\n" \
  "s_lshl_b32 s88, s73, 10\n" \
  "v_add_u32 v48, s88, v1\n" \
  "ds_read_b128 v[52:55], v48 offset:0\n" \
  "s_waitcnt lgkmcnt(0)\n" \
  "v_mov_b32 v11, 0\n" \
  "v_dot2c_f32_bf16 v11, v52, v52\n" \
  "v_dot2c_f32_bf16 v11, v53, v53\n" \
  "v_dot2c_f32_bf16 v11, v54, v54\n" \
  "v_dot2c_f32_bf16 v11, v55, v55\n" \
  "s_nop 0\n" \
  "s_nop 1\n" \
  "v_max_u32_dpp v11, v11, v11 row_ror:1 row_mask:0xf bank_mask:0xf\n" \
  "s_nop 1\n" \
  "v_max_u32_dpp v11, v11, v11 row_ror:2 row_mask:0xf bank_mask:0xf\n" \
  "s_nop 1\n" \
  "v_max_u32_dpp v11, v11, v11 row_ror:4 row_mask:0xf bank_mask:0xf\n" \
  "s_nop 1\n" \
  "v_max_u32_dpp v11, v11, v11 row_ror:8 row_mask:0xf bank_mask:0xf\n" \
  "v_mov_b32 v31, v11\n" \
  "s_nop 1\n" \
  "v_permlane16_swap_b32 v11, v31\n" \
  "v_max_u32 v11, v11, v31\n" \
  "v_mov_b32 v31, v11\n" \
  "s_nop 1\n" \
  "v_permlane32_swap_b32 v11, v31\n" \
  "v_max_u32 v11, v11, v31\n" \
  "s_mov_b64 exec, 1\n" \
  "ds_max_u32 v59, v11\n" \
  "s_mov_b64 exec, -1\n" \
  "s_waitcnt vmcnt(8) lgkmcnt(0)\n" \
  "s_barrier\n" \
  "ds_read_b64_tr_b16 v[100:101], v20 offset:4096\n" \
  "ds_read_b64_tr_b16 v[102:103], v20 offset:6144\n" \
  "ds_read_b64_tr_b16 v[108:109], v21 offset:4096\n" \
  "ds_read_b64_tr_b16 v[110:111], v21 offset:6144\n" \
  "ds_read_b64_tr_b16 v[116:117], v22 offset:4096\n" \
  "ds_read_b64_tr_b16 v[118:119], v22 offset:6144\n" \
  "ds_read_b64_tr_b16 v[124:125], v23 offset:4096\n" \
  "ds_read_b64_tr_b16 v[126:127], v23 offset:6144\n" \
  "s_add_u32 m0, s19, 0\n" \
  "s_nop 0\n" \
  "global_load_lds_dwordx4 v24, s[4:5] offset:0\n" \
  "global_load_lds_dwordx4 v25, s[4:5] offset:1024\n" \
  "global_load_lds_dwordx4 v26, s[4:5] offset:2048\n" \
  "global_load_lds_dwordx4 v27, s[4:5] offset:3072\n" \
  "s_add_u32 s4, s4, 0x100000\n" \
  "s_addc_u32 s5, s5, 0\n" \
  "v_mfma_f32_16x16x32_bf16 v[64:67], v[96:99], v[96:99], 0\n" \
  "v_mfma_f32_16x16x32_bf16 v[68:71], v[104:107], v[104:107], 0\n" \
  "v_mfma_f32_16x16x32_bf16 v[72:75], v[112:115], v[112:115], 0\n" \
  "v_mfma_f32_16x16x32_bf16 v[76:79], v[120:123], v[120:123], 0\n" \
  "s_waitcnt vmcnt(8) lgkmcnt(0)\n" \
  "s_barrier\n" \
  "ds_read_b64_tr_b16 v[32:33], v20 offset:8192\n" \
  "ds_read_b64_tr_b16 v[34:35], v20 offset:10240\n" \
  "ds_read_b64_tr_b16 v[40:41], v21 offset:8192\n" \
  "ds_read_b64_tr_b16 v[42:43], v21 offset:10240\n" \
  "ds_read_b64_tr_b16 v[48:49], v22 offset:8192\n" \
  "ds_read_b64_tr_b16 v[50:51], v22 offset:10240\n" \
  "ds_read_b64_tr_b16 v[56:57], v23 offset:8192\n" \
  "ds_read_b64_tr_b16 v[58:59], v23 offset:10240\n" \
  "s_add_u32 m0, s19, 4096\n" \
  "s_nop 0\n" \
  "global_load_lds_dwordx4 v24, s[4:5] offset:0\n" \
  "global_load_lds_dwordx4 v25, s[4:5] offset:1024\n" \
  "global_load_lds_dwordx4 v26, s[4:5] offset:2048\n" \
  "global_load_lds_dwordx4 v27, s[4:5] offset:3072\n" \
  "s_add_u32 s4, s4, 0x100000\n" \
  "s_addc_u32 s5, s5, 0\n" \
  "v_mfma_f32_16x16x32_bf16 v[64:67], v[100:103], v[100:103], v[64:67]\n" \
  "v_mfma_f32_16x16x32_bf16 v[68:71], v[108:111], v[108:111], v[68:71]\n" \
  "v_mfma_f32_16x16x32_bf16 v[72:75], v[116:119], v[116:119], v[72:75]\n" \
  "v_mfma_f32_16x16x32_bf16 v[76:79], v[124:127], v[124:127], v[76:79]\n" \
  "s_waitcnt vmcnt(8) lgkmcnt(0)\n" \
  "s_barrier\n" \
  "ds_read_b64_tr_b16 v[36:37], v20 offset:12288\n" \
  "ds_read_b64_tr_b16 v[38:39], v20 offset:14336\n" \
  "ds_read_b64_tr_b16 v[44:45], v21 offset:12288\n" \
  "ds_read_b64_tr_b16 v[46:47], v21 offset:14336\n" \
  "ds_read_b64_tr_b16 v[52:53], v22 offset:12288\n" \
  "ds_read_b64_tr_b16 v[54:55], v22 offset:14336\n" \
  "ds_read_b64_tr_b16 v[60:61], v23 offset:12288\n" \
  "ds_read_b64_tr_b16 v[62:63], v23 offset:14336\n" \
  "s_add_u32 m0, s19, 8192\n" \
  "s_nop 0\n" \
  "global_load_lds_dwordx4 v24, s[4:5] offset:0\n" \
  "global_load_lds_dwordx4 v25, s[4:5] offset:1024\n" \
  "global_load_lds_dwordx4 v26, s[4:5] offset:2048\n" \
  "global_load_lds_dwordx4 v27, s[4:5] offset:3072\n" \
  "s_add_u32 s4, s4, 0x100000\n" \
  "s_addc_u32 s5, s5, 0\n" \
  "v_mfma_f32_16x16x32_bf16 v[64:67], v[32:35], v[32:35], v[64:67]\n" \
  "v_mfma_f32_16x16x32_bf16 v[68:71], v[40:43], v[40:43], v[68:71]\n" \
  "v_mfma_f32_16x16x32_bf16 v[72:75], v[48:51], v[48:51], v[72:75]\n" \
  "v_mfma_f32_16x16x32_bf16 v[76:79], v[56:59], v[56:59], v[76:79]\n" \
  "s_waitcnt vmcnt(8) lgkmcnt(0)\n" \
  "s_barrier\n" \
  "ds_read_b64_tr_b16 v[128:129], v18 offset:0\n" \
  "ds_read_b64_tr_b16 v[130:131], v18 offset:512\n" \
  "ds_read_b64_tr_b16 v[132:133], v18 offset:32768\n" \
  "ds_read_b64_tr_b16 v[134:135], v18 offset:33280\n" \
  "ds_read_b64_tr_b16 v[136:137], v19 offset:0\n" \
  "ds_read_b64_tr_b16 v[138:139], v19 offset:512\n" \
  "ds_read_b64_tr_b16 v[140:141], v19 offset:32768\n" \
  "ds_read_b64_tr_b16 v[142:143], v19 offset:33280\n" \
  "ds_read_b64_tr_b16 v[192:193], v28 offset:0\n" \
  "ds_read_b64_tr_b16 v[194:195], v28 offset:512\n" \
  "ds_read_b64_tr_b16 v[196:197], v28 offset:32768\n" \
  "ds_read_b64_tr_b16 v[198:199], v28 offset:33280\n" \
  "ds_read_b64_tr_b16 v[200:201], v17 offset:0\n" \
  "ds_read_b64_tr_b16 v[202:203], v17 offset:512\n" \
  "ds_read_b64_tr_b16 v[204:205], v17 offset:32768\n" \
  "ds_read_b64_tr_b16 v[206:207], v17 offset:33280\n" \
  "s_add_u32 m0, s19, 12288\n" \
  "s_nop 0\n" \
  "global_load_lds_dwordx4 v24, s[4:5] offset:0\n" \
  "global_load_lds_dwordx4 v25, s[4:5] offset:1024\n" \
  "global_load_lds_dwordx4 v26, s[4:5] offset:2048\n" \
  "global_load_lds_dwordx4 v27, s[4:5] offset:3072\n" \
  "s_add_u32 s4, s4, 0x100000\n" \
  "s_addc_u32 s5, s5, 0\n" \
  "v_mfma_f32_16x16x32_bf16 v[64:67], v[36:39], v[36:39], v[64:67]\n" \
  "v_mfma_f32_16x16x32_bf16 v[68:71], v[44:47], v[44:47], v[68:71]\n" \
  "v_mfma_f32_16x16x32_bf16 v[72:75], v[52:55], v[52:55], v[72:75]\n" \
  "v_mfma_f32_16x16x32_bf16 v[76:79], v[60:63], v[60:63], v[76:79]\n" \
  "s_nop 7\n" \
  "v_max3_f32 v13, v64, v65, v66\n" \
  "v_max3_f32 v13, v13, v67, v68\n" \
  "v_max3_f32 v13, v13, v69, v70\n" \
  "v_max3_f32 v13, v13, v71, v72\n" \
  "v_max3_f32 v13, v13, v73, v74\n" \
  "v_max3_f32 v13, v13, v75, v76\n" \
  "v_max3_f32 v13, v13, v77, v78\n" \
  "v_max_f32 v13, v13, v79\n" \
  "s_nop 1\n" \
  "v_max_f32_dpp v13, v13, v13 row_ror:1 row_mask:0xf bank_mask:0xf\n" \
  "s_nop 1\n" \
  "v_max_f32_dpp v13, v13, v13 row_ror:2 row_mask:0xf bank_mask:0xf\n" \
  "s_nop 1\n" \
  "v_max_f32_dpp v13, v13, v13 row_ror:4 row_mask:0xf bank_mask:0xf\n" \
  "s_nop 1\n" \
  "v_max_f32_dpp v13, v13, v13 row_ror:8 row_mask:0xf bank_mask:0xf\n" \
  "v_mov_b32 v31, v13\n" \
  "s_nop 1\n" \
  "v_permlane16_swap_b32 v13, v31\n" \
  "v_max_f32 v13, v13, v31\n" \
  "v_mov_b32 v31, v13\n" \
  "s_nop 1\n" \
  "v_permlane32_swap_b32 v13, v31\n" \
  "v_max_f32 v13, v13, v31\n" \
  "s_mov_b64 exec, 1\n" \
  "ds_max_u32 v30, v13 offset:32\n" \
  "s_mov_b64 exec, -1\n" \
  "s_waitcnt vmcnt(8) lgkmcnt(0)\n" \
  "s_barrier\n" \
  "ds_read_b64_tr_b16 v[144:145], v18 offset:4096\n" \
  "ds_read_b64_tr_b16 v[146:147], v18 offset:4608\n" \
  "ds_read_b64_tr_b16 v[148:149], v18 offset:36864\n" \
  "ds_read_b64_tr_b16 v[150:151], v18 offset:37376\n" \
  "ds_read_b64_tr_b16 v[152:153], v19 offset:4096\n" \
  "ds_read_b64_tr_b16 v[154:155], v19 offset:4608\n" \
  "ds_read_b64_tr_b16 v[156:157], v19 offset:36864\n" \
  "ds_read_b64_tr_b16 v[158:159], v19 offset:37376\n" \
  "ds_read_b64_tr_b16 v[208:209], v28 offset:4096\n" \
  "ds_read_b64_tr_b16 v[210:211], v28 offset:4608\n" \
  "ds_read_b64_tr_b16 v[212:213], v28 offset:36864\n" \
  "ds_read_b64_tr_b16 v[214:215], v28 offset:37376\n" \
  "ds_read_b64_tr_b16 v[216:217], v17 offset:4096\n" \
  "ds_read_b64_tr_b16 v[218:219], v17 offset:4608\n" \
  "ds_read_b64_tr_b16 v[220:221], v17 offset:36864\n" \
  "ds_read_b64_tr_b16 v[222:223], v17 offset:37376\n" \
  "s_add_u32 m0, s19, 0\n" \
  "s_nop 0\n" \
  "global_load_lds_dwordx4 v24, s[6:7] offset:0\n" \
  "global_load_lds_dwordx4 v25, s[6:7] offset:1024\n" \
  "global_load_lds_dwordx4 v26, s[6:7] offset:2048\n" \
  "global_load_lds_dwordx4 v27, s[6:7] offset:3072\n" \
  "s_add_u32 s6, s6, 0x100000\n" \
  "s_addc_u32 s7, s7, 0\n" \
  "v_mfma_f32_16x16x32_bf16 v[64:67], v[128:131], v[128:131], 0\n" \
  "v_mfma_f32_16x16x32_bf16 v[68:71], v[192:195], v[192:195], 0\n" \
  "v_mfma_f32_16x16x32_bf16 v[64:67], v[132:135], v[132:135], v[64:67]\n" \
  "v_mfma_f32_16x16x32_bf16 v[68:71], v[196:199], v[196:199], v[68:71]\n" \
  "v_mfma_f32_16x16x32_bf16 v[64:67], v[136:139], v[136:139], v[64:67]\n" \
  "v_mfma_f32_16x16x32_bf16 v[68:71], v[200:203], v[200:203], v[68:71]\n" \
  "v_mfma_f32_16x16x32_bf16 v[64:67], v[140:143], v[140:143], v[64:67]\n" \
  "v_mfma_f32_16x16x32_bf16 v[68:71], v[204:207], v[204:207], v[68:71]\n" \
  "s_waitcnt vmcnt(8) lgkmcnt(0)\n" \
  "s_barrier\n" \
  "ds_read_b64_tr_b16 v[160:161], v18 offset:8192\n" \
  "ds_read_b64_tr_b16 v[162:163], v18 offset:8704\n" \
  "ds_read_b64_tr_b16 v[164:165], v18 offset:40960\n" \
  "ds_read_b64_tr_b16 v[166:167], v18 offset:41472\n" \
  "ds_read_b64_tr_b16 v[168:169], v19 offset:8192\n" \
  "ds_read_b64_tr_b16 v[170:171], v19 offset:8704\n" \
  "ds_read_b64_tr_b16 v[172:173], v19 offset:40960\n" \
  "ds_read_b64_tr_b16 v[174:175], v19 offset:41472\n" \
  "ds_read_b64_tr_b16 v[224:225], v28 offset:8192\n" \
  "ds_read_b64_tr_b16 v[226:227], v28 offset:8704\n" \
  "ds_read_b64_tr_b16 v[228:229], v28 offset:40960\n" \
  "ds_read_b64_tr_b16 v[230:231], v28 offset:41472\n" \
  "ds_read_b64_tr_b16 v[232:233], v17 offset:8192\n" \
  "ds_read_b64_tr_b16 v[234:235], v17 offset:8704\n" \
  "ds_read_b64_tr_b16 v[236:237], v17 offset:40960\n" \
  "ds_read_b64_tr_b16 v[238:239], v17 offset:41472\n" \
  "s_add_u32 m0, s19, 4096\n" \
  "s_nop 0\n" \
  "global_load_lds_dwordx4 v24, s[6:7] offset:0\n" \
  "global_load_lds_dwordx4 v25, s[6:7] offset:1024\n" \
  "global_load_lds_dwordx4 v26, s[6:7] offset:2048\n" \
  "global_load_lds_dwordx4 v27, s[6:7] offset:3072\n" \
  "s_add_u32 s6, s6, 0x100000\n" \
  "s_addc_u32 s7, s7, 0\n" \
  "v_mfma_f32_16x16x32_bf16 v[64:67], v[144:147], v[144:147], v[64:67]\n" \
  "v_mfma_f32_16x16x32_bf16 v[68:71], v[208:211], v[208:211], v[68:71]\n" \
  "v_mfma_f32_16x16x32_bf16 v[64:67], v[148:151], v[148:151], v[64:67]\n" \
  "v_mfma_f32_16x16x32_bf16 v[68:71], v[212:215], v[212:215], v[68:71]\n" \
  "v_mfma_f32_16x16x32_bf16 v[64:67], v[152:155], v[152:155], v[64:67]\n" \
  "v_mfma_f32_16x16x32_bf16 v[68:71], v[216:219], v[216:219], v[68:71]\n" \
  "v_mfma_f32_16x16x32_bf16 v[64:67], v[156:159], v[156:159], v[64:67]\n" \
  "v_mfma_f32_16x16x32_bf16 v[68:71], v[220:223], v[220:223], v[68:71]\n" \
  "s_waitcnt vmcnt(8) lgkmcnt(0)\n" \
  "s_barrier\n" \
  "ds_read_b64_tr_b16 v[176:177], v18 offset:12288\n" \
  "ds_read_b64_tr_b16 v[178:179], v18 offset:12800\n" \
  "ds_read_b64_tr_b16 v[180:181], v18 offset:45056\n" \
  "ds_read_b64_tr_b16 v[182:183], v18 offset:45568\n" \
  "ds_read_b64_tr_b16 v[184:185], v19 offset:12288\n" \
  "ds_read_b64_tr_b16 v[186:187], v19 offset:12800\n" \
  "ds_read_b64_tr_b16 v[188:189], v19 offset:45056\n" \
  "ds_read_b64_tr_b16 v[190:191], v19 offset:45568\n" \
  "ds_read_b64_tr_b16 v[240:241], v28 offset:12288\n" \
  "ds_read_b64_tr_b16 v[242:243], v28 offset:12800\n" \
  "ds_read_b64_tr_b16 v[244:245], v28 offset:45056\n" \
  "ds_read_b64_tr_b16 v[246:247], v28 offset:45568\n" \
  "ds_read_b64_tr_b16 v[248:249], v17 offset:12288\n" \
  "ds_read_b64_tr_b16 v[250:251], v17 offset:12800\n" \
  "ds_read_b64_tr_b16 v[252:253], v17 offset:45056\n" \
  "ds_read_b64_tr_b16 v[254:255], v17 offset:45568\n" \
  "s_add_u32 m0, s19, 8192\n" \
  "s_nop 0\n" \
  "global_load_lds_dwordx4 v24, s[6:7] offset:0\n" \
  "global_load_lds_dwordx4 v25, s[6:7] offset:1024\n" \
  "global_load_lds_dwordx4 v26, s[6:7] offset:2048\n" \
  "global_load_lds_dwordx4 v27, s[6:7] offset:3072\n" \
  "s_add_u32 s6, s6, 0x100000\n" \
  "s_addc_u32 s7, s7, 0\n" \
  "v_mfma_f32_16x16x32_bf16 v[64:67], v[160:163], v[160:163], v[64:67]\n" \
  "v_mfma_f32_16x16x32_bf16 v[68:71], v[224:227], v[224:227], v[68:71]\n" \
  "v_mfma_f32_16x16x32_bf16 v[64:67], v[164:167], v[164:167], v[64:67]\n" \
  "v_mfma_f32_16x16x32_bf16 v[68:71], v[228:231], v[228:231], v[68:71]\n" \
  "v_mfma_f32_16x16x32_bf16 v[64:67], v[168:171], v[168:171], v[64:67]\n" \
  "v_mfma_f32_16x16x32_bf16 v[68:71], v[232:235], v[232:235], v[68:71]\n" \
  "v_mfma_f32_16x16x32_bf16 v[64:67], v[172:175], v[172:175], v[64:67]\n" \
  "v_mfma_f32_16x16x32_bf16 v[68:71], v[236:239], v[236:239], v[68:71]\n" \
  "s_waitcnt vmcnt(8) lgkmcnt(0)\n" \
  "s_barrier\n" \
  "s_add_u32 m0, s19, 12288\n" \
  "s_nop 0\n" \
  "global_load_lds_dwordx4 v24, s[6:7] offset:0\n" \
  "global_load_lds_dwordx4 v25, s[6:7] offset:1024\n" \
  "global_load_lds_dwordx4 v26, s[6:7] offset:2048\n" \
  "global_load_lds_dwordx4 v27, s[6:7] offset:3072\n" \
  "s_add_u32 s6, s6, 0x100000\n" \
  "s_addc_u32 s7, s7, 0\n" \
  "v_mfma_f32_16x16x32_bf16 v[64:67], v[176:179], v[176:179], v[64:67]\n" \
  "v_mfma_f32_16x16x32_bf16 v[68:71], v[240:243], v[240:243], v[68:71]\n" \
  "v_mfma_f32_16x16x32_bf16 v[64:67], v[180:183], v[180:183], v[64:67]\n" \
  "v_mfma_f32_16x16x32_bf16 v[68:71], v[244:247], v[244:247], v[68:71]\n" \
  "v_mfma_f32_16x16x32_bf16 v[64:67], v[184:187], v[184:187], v[64:67]\n" \
  "v_mfma_f32_16x16x32_bf16 v[68:71], v[248:251], v[248:251], v[68:71]\n" \
  "v_mfma_f32_16x16x32_bf16 v[64:67], v[188:191], v[188:191], v[64:67]\n" \
  "v_mfma_f32_16x16x32_bf16 v[68:71], v[252:255], v[252:255], v[68:71]\n" \
  "s_nop 7\n" \
  "s_mov_b32 exec_lo, 0x100001\n" \
  "s_mov_b32 exec_hi, 0x10000100\n" \
  "v_add_f32 v12, v64, v68\n" \
  "s_mov_b32 exec_lo, 0x200002\n" \
  "s_mov_b32 exec_hi, 0x20000200\n" \
  "v_add_f32 v12, v65, v69\n" \
  "s_mov_b32 exec_lo, 0x400004\n" \
  "s_mov_b32 exec_hi, 0x40000400\n" \
  "v_add_f32 v12, v66, v70\n" \
  "s_mov_b32 exec_lo, 0x800008\n" \
  "s_mov_b32 exec_hi, 0x80000800\n" \
  "v_add_f32 v12, v67, v71\n" \
  "s_mov_b64 exec, -1\n" \
  "s_nop 1\n" \
  "v_add_f32_dpp v12, v12, v12 row_ror:1 row_mask:0xf bank_mask:0xf\n" \
  "s_nop 1\n" \
  "v_add_f32_dpp v12, v12, v12 row_ror:2 row_mask:0xf bank_mask:0xf\n" \
  "s_nop 1\n" \
  "v_add_f32_dpp v12, v12, v12 row_ror:4 row_mask:0xf bank_mask:0xf\n" \
  "s_nop 1\n" \
  "v_add_f32_dpp v12, v12, v12 row_ror:8 row_mask:0xf bank_mask:0xf\n" \
  "v_mov_b32 v31, v12\n" \
  "s_nop 1\n" \
  "v_permlane16_swap_b32 v12, v31\n" \
  "v_add_f32 v12, v12, v31\n" \
  "v_mov_b32 v31, v12\n" \
  "s_nop 1\n" \
  "v_permlane32_swap_b32 v12, v31\n" \
  "v_add_f32 v12, v12, v31\n" \
  "v_lshl_add_u32 v31, s73, 2, v30\n" \
  "s_mov_b64 exec, 1\n" \
  "ds_write_b32 v31, v12\n" \
  "s_mov_b64 exec, -1\n" \
  "s_waitcnt vmcnt(8) lgkmcnt(0)\n" \
  "s_barrier\n" \
  "s_add_u32 m0, s19, 0\n" \
  "s_nop 0\n" \
  "global_load_lds_dwordx4 v24, s[8:9] offset:0\n" \
  "global_load_lds_dwordx4 v25, s[8:9] offset:1024\n" \
  "global_load_lds_dwordx4 v26, s[8:9] offset:2048\n" \
  "global_load_lds_dwordx4 v27, s[8:9] offset:3072\n" \
  "s_add_u32 s8, s8, 0x100000\n" \
  "s_addc_u32 s9, s9, 0\n" \
  "ds_read_b128 v[20:23], v30 offset:16\n" \
  "ds_read_b32 v31, v30 offset:32\n" \
  "v_mbcnt_lo_u32_b32 v16, -1, 0\n" \
  "v_mbcnt_hi_u32_b32 v16, -1, v16\n" \
  "v_lshlrev_b32 v15, 8, v16\n" \
  "s_waitcnt lgkmcnt(0)\n" \
  "v_add_f32 v20, v20, v21\n" \
  "v_add_f32 v22, v22, v23\n" \
  "v_add_f32 v20, v20, v22\n" \
  "v_mul_f32 v31, v20, v31\n" \
  "v_mul_f32 v31, 0x3f8147ae, v31\n" \
  "v_mov_b32 v12, s10\n" \
  "v_mov_b32 v13, 0xffff\n" \
  "v_bfi_b32 v13, v13, s11, v31\n" \
  "s_mov_b32 exec_hi, 0\n" \
  "s_mov_b32 exec_lo, s18\n" \
  "global_store_dwordx2 v15, v[12:13], s[16:17] sc1\n" \
  "s_mov_b64 exec, -1\n" \
  "v_lshlrev_b32 v16, 3, v16\n" \
  "s_waitcnt vmcnt(8) lgkmcnt(0)\n" \
  "s_barrier\n" \
  "s_add_u32 m0, s19, 4096\n" \
  "s_nop 0\n" \
  "global_load_lds_dwordx4 v24, s[8:9] offset:0\n" \
  "global_load_lds_dwordx4 v25, s[8:9] offset:1024\n" \
  "global_load_lds_dwordx4 v26, s[8:9] offset:2048\n" \
  "global_load_lds_dwordx4 v27, s[8:9] offset:3072\n" \
  "s_add_u32 s8, s8, 0x100000\n" \
  "s_addc_u32 s9, s9, 0\n" \
  "global_load_dwordx2 v[20:21], v16, s[12:13] sc1\n" \
  "s_waitcnt vmcnt(9) lgkmcnt(0)\n" \
  "s_barrier\n" \
  "s_add_u32 m0, s19, 8192\n" \
  "s_nop 0\n" \
  "global_load_lds_dwordx4 v24, s[8:9] offset:0\n" \
  "global_load_lds_dwordx4 v25, s[8:9] offset:1024\n" \
  "global_load_lds_dwordx4 v26, s[8:9] offset:2048\n" \
  "global_load_lds_dwordx4 v27, s[8:9] offset:3072\n" \
  "s_add_u32 s8, s8, 0x100000\n" \
  "s_addc_u32 s9, s9, 0\n" \
  "s_waitcnt vmcnt(9) lgkmcnt(0)\n" \
  "s_barrier\n" \
  "ds_read_b64_tr_b16 v[64:65], v28 offset:0\n" \
  "ds_read_b64_tr_b16 v[66:67], v28 offset:512\n" \
  "ds_read_b64_tr_b16 v[68:69], v28 offset:32768\n" \
  "ds_read_b64_tr_b16 v[70:71], v28 offset:33280\n" \
  "ds_read_b64_tr_b16 v[72:73], v17 offset:0\n" \
  "ds_read_b64_tr_b16 v[74:75], v17 offset:512\n" \
  "ds_read_b64_tr_b16 v[76:77], v17 offset:32768\n" \
  "ds_read_b64_tr_b16 v[78:79], v17 offset:33280\n" \
  "s_add_u32 m0, s19, 12288\n" \
  "s_nop 0\n" \
  "global_load_lds_dwordx4 v24, s[8:9] offset:0\n" \
  "global_load_lds_dwordx4 v25, s[8:9] offset:1024\n" \
  "global_load_lds_dwordx4 v26, s[8:9] offset:2048\n" \
  "global_load_lds_dwordx4 v27, s[8:9] offset:3072\n" \
  "s_add_u32 s8, s8, 0x100000\n" \
  "s_addc_u32 s9, s9, 0\n" \
  "s_waitcnt vmcnt(8)\n" \
  ".LCV_RD0_%=:\n" \
  "v_cmp_ne_u32 vcc, s10, v20\n" \
  "v_xor_b32 v31, s11, v21\n" \
  "v_cmp_ne_u32 s[22:23], 0, v31\n" \
  "s_or_b64 vcc, vcc, s[22:23]\n" \
  "s_cbranch_scc0 .LCV_RD1_%=\n" \
  "s_sleep 8\n" \
  "global_load_dwordx2 v[20:21], v16, s[12:13] sc1\n" \
  "s_sub_u32 s87, s87, 1\n" \
  "s_cbranch_scc1 .LCV_END_%=\n" \
  "s_waitcnt vmcnt(0)\n" \
  "s_branch .LCV_RD0_%=\n" \
  ".LCV_RD1_%=:\n" \
  "s_waitcnt vmcnt(8) lgkmcnt(0)\n" \
  "s_barrier\n" \
  "ds_write_b128 v3, v[96:99] offset:0\n" \
  "ds_write_b128 v3, v[100:103] offset:1024\n" \
  "ds_write_b128 v3, v[104:107] offset:2048\n" \
  "ds_write_b128 v3, v[108:111] offset:3072\n" \
  "ds_read_b64_tr_b16 v[80:81], v28 offset:4096\n" \
  "ds_read_b64_tr_b16 v[82:83], v28 offset:4608\n" \
  "ds_read_b64_tr_b16 v[84:85], v28 offset:36864\n" \
  "ds_read_b64_tr_b16 v[86:87], v28 offset:37376\n" \
  "ds_read_b64_tr_b16 v[88:89], v17 offset:4096\n" \
  "ds_read_b64_tr_b16 v[90:91], v17 offset:4608\n" \
  "ds_read_b64_tr_b16 v[92:93], v17 offset:36864\n" \
  "ds_read_b64_tr_b16 v[94:95], v17 offset:37376\n" \
  "v_and_b32 v16, 0xff, v16\n" \
  "global_load_dwordx2 v[22:23], v16, s[14:15] sc1\n" \
  "s_mov_b64 exec, 15\n" \
  "buffer_load_dword v13, v2, s[44:47], s68 offen sc1\n" \
  "s_mov_b64 exec, -1\n" \
  "s_waitcnt vmcnt(6) lgkmcnt(0)\n" \
  "s_barrier\n" \
  "ds_write_b128 v3, v[112:115] offset:4096\n" \
  "ds_write_b128 v3, v[116:119] offset:5120\n" \
  "ds_write_b128 v3, v[120:123] offset:6144\n" \
  "ds_write_b128 v3, v[124:127] offset:7168\n" \
  "ds_read_b64_tr_b16 v[96:97], v28 offset:8192\n" \
  "ds_read_b64_tr_b16 v[98:99], v28 offset:8704\n" \
  "ds_read_b64_tr_b16 v[100:101], v28 offset:40960\n" \
  "ds_read_b64_tr_b16 v[102:103], v28 offset:41472\n" \
  "ds_read_b64_tr_b16 v[104:105], v17 offset:8192\n" \
  "ds_read_b64_tr_b16 v[106:107], v17 offset:8704\n" \
  "ds_read_b64_tr_b16 v[108:109], v17 offset:40960\n" \
  "ds_read_b64_tr_b16 v[110:111], v17 offset:41472\n" \
  "s_waitcnt vmcnt(0)\n" \
  "v_cmp_eq_u32 vcc, 0, v13\n" \
  "s_and_b32 s90, vcc_lo, 15\n" \
  "s_cbranch_scc1 .LCV_GL0_%=\n" \
  "s_mov_b32 m0, s99\n" \
  "s_add_u32 s88, s99, 8192\n" \
  "global_load_lds_dwordx4 v10, s[62:63] sc1\n" \
  "global_load_lds_dwordx4 v10, s[62:63] offset:1024 sc1\n" \
  "s_mov_b32 m0, s88\n" \
  "s_nop 0\n" \
  "global_load_lds_dwordx4 v10, s[64:65] sc1\n" \
  "global_load_lds_dwordx4 v10, s[64:65] offset:1024 sc1\n" \
  ".LCV_GL0_%=:\n" \
  "v_cmp_ne_u32 vcc, s10, v22\n" \
  "v_xor_b32 v31, s11, v23\n" \
  "v_and_b32 v31, 0xffff, v31\n" \
  "v_cmp_ne_u32 s[22:23], 0, v31\n" \
  "s_or_b64 vcc, vcc, s[22:23]\n" \
  "s_cbranch_scc0 .LCV_GL1_%=\n" \
  "s_sleep 12\n" \
  "global_load_dwordx2 v[22:23], v16, s[14:15] sc1\n" \
  "s_sub_u32 s87, s87, 1\n" \
  "s_cbranch_scc1 .LCV_END_%=\n" \
  "s_waitcnt vmcnt(0)\n" \
  "s_branch .LCV_GL0_%=\n" \
  ".LCV_GL1_%=:\n" \
  "v_and_b32 v31, 0xffff0000, v23\n" \
  "s_nop 1\n" \
  "v_max_u32_dpp v31, v31, v31 row_ror:1 row_mask:0xf bank_mask:0xf\n" \
  "s_nop 1\n" \
  "v_max_u32_dpp v31, v31, v31 row_ror:2 row_mask:0xf bank_mask:0xf\n" \
  "s_nop 1\n" \
  "v_max_u32_dpp v31, v31, v31 row_ror:4 row_mask:0xf bank_mask:0xf\n" \
  "s_nop 1\n" \
  "v_max_u32_dpp v31, v31, v31 row_ror:8 row_mask:0xf bank_mask:0xf\n" \
  "v_mov_b32 v12, v31\n" \
  "s_nop 1\n" \
  "v_permlane16_swap_b32 v31, v12\n" \
  "v_max_u32 v31, v31, v12\n" \
  "v_mov_b32 v12, v31\n" \
  "s_nop 1\n" \
  "v_permlane32_swap_b32 v31, v12\n" \
  "v_max_u32 v31, v31, v12\n" \
  "s_mov_b64 exec, 1\n" \
  "ds_write_b32 v30, v31\n" \
  "s_mov_b64 exec, -1\n" \
  "s_waitcnt vmcnt(4) lgkmcnt(0)\n" \
  "s_barrier\n" \
  "ds_write_b128 v3, v[32:35] offset:8192\n" \
  "ds_write_b128 v3, v[36:39] offset:9216\n" \
  "ds_write_b128 v3, v[40:43] offset:10240\n" \
  "ds_write_b128 v3, v[44:47] offset:11264\n" \
  "ds_read_b64_tr_b16 v[112:113], v28 offset:12288\n" \
  "ds_read_b64_tr_b16 v[114:115], v28 offset:12800\n" \
  "ds_read_b64_tr_b16 v[116:117], v28 offset:45056\n" \
  "ds_read_b64_tr_b16 v[118:119], v28 offset:45568\n" \
  "ds_read_b64_tr_b16 v[120:121], v17 offset:12288\n" \
  "ds_read_b64_tr_b16 v[122:123], v17 offset:12800\n" \
  "ds_read_b64_tr_b16 v[124:125], v17 offset:45056\n" \
  "ds_read_b64_tr_b16 v[126:127], v17 offset:45568\n" \
  "ds_read_b32 v31, v30 offset:0\n" \
  "s_waitcnt lgkmcnt(0)\n" \
  "s_barrier\n" \
  "ds_write_b128 v3, v[48:51] offset:12288\n" \
  "ds_write_b128 v3, v[52:55] offset:13312\n" \
  "ds_write_b128 v3, v[56:59] offset:14336\n" \
  "ds_write_b128 v3, v[60:63] offset:15360\n" \
  "v_mul_f32 v31, 0x3f8e353f, v31\n" \
  "v_mul_f32 v30, 0x44870000, v31\n" \
  "v_mov_b64 v[12:13], 0\n" \
  "v_mov_b64 v[14:15], 0\n" \
  "v_mov_b64 v[16:17], 0\n" \
  "v_mov_b64 v[18:19], 0\n" \
  "v_mov_b64 v[20:21], 0\n" \
  "v_mov_b64 v[22:23], 0\n" \
  "v_readfirstlane_b32 s72, v31\n" \
  "v_readfirstlane_b32 s86, v30\n" \
  "s_lshr_b32 s86, s86, 23\n" \
  "s_addk_i32 s86, 0xff83\n" \
  "s_ashr_i32 s86, s86, 1\n" \
  "s_max_i32 s86, s86, -20\n" \
  "s_min_i32 s86, s86, 100\n" \
  "s_mov_b32 s82, s86\n" \
  "s_lshr_b32 s83, s72, 23\n" \
  "s_sub_i32 s83, s83, 243\n" \
  "s_waitcnt lgkmcnt(0)\n" \
  "s_add_u32 s88, s74, 157760\n" \
  "v_mov_b32 v59, s88\n" \
  "s_cmp_lg_u32 s61, 0\n" \
  "s_movk_i32 s76, -8000\n" \
  "s_cmovk_i32 s76, 9152\n" \
  "s_movk_i32 s77, 90\n" \
  "s_cmovk_i32 s77, 90\n" \
  "s_cselect_b32 s72, -1, 55\n" \
  "s_mov_b32 s87, 0x40000\n" \
  "s_cmp_eq_u32 s90, 0\n" \
  "s_cbranch_scc1 .LCV_W1_%=\n" \
  "s_branch .LCV_W2_%=\n" \
  ".LCV_W0_%=:\n" \
  "s_sleep 2\n" \
  "s_cmp_eq_u32 s61, 0\n" \
  "s_cbranch_scc1 .LCV_W2_%=\n" \
  "s_sleep 32\n" \
  ".LCV_W2_%=:\n" \
  "s_mov_b64 exec, 15\n" \
  "buffer_load_dword v58, v2, s[44:47], s68 offen sc1\n" \
  "s_mov_b64 exec, -1\n" \
  "s_sub_u32 s87, s87, 1\n" \
  "s_cbranch_scc1 .LCV_END_%=\n" \
  "s_waitcnt vmcnt(0)\n" \
  "v_cmp_eq_u32 vcc, 0, v58\n" \
  "s_and_b32 vcc_lo, vcc_lo, 15\n" \
  "s_cbranch_scc1 .LCV_W0_%=\n" \
  "s_mov_b32 m0, s99\n" \
  "s_add_u32 s88, s99, 8192\n" \
  "global_load_lds_dwordx4 v10, s[62:63] sc1\n" \
  "global_load_lds_dwordx4 v10, s[62:63] offset:1024 sc1\n" \
  "s_mov_b32 m0, s88\n" \
  "s_nop 0\n" \
  "global_load_lds_dwordx4 v10, s[64:65] sc1\n" \
  "global_load_lds_dwordx4 v10, s[64:65] offset:1024 sc1\n" \
  ".LCV_W1_%=:\n" \
  "s_add_u32 s62, s62, 0x80000\n" \
  "s_addc_u32 s63, s63, 0\n" \
  "s_add_u32 s64, s64, 0x40000\n" \
  "s_addc_u32 s65, s65, 0\n" \
  "s_add_u32 s68, s68, 0x1000\n" \
  "s_cmp_eq_u32 s61, 0\n" \
  "s_cbranch_scc1 .LCV_W4_%=\n" \
  "s_add_u32 s38, s67, 4992\n" \
  "v_mov_b32 v58, s38\n" \
  "buffer_load_dword v58, v58, s[0:3], 0 offen sc1\n" \
  "s_waitcnt vmcnt(0)\n" \
  "v_readfirstlane_b32 vcc_lo, v58\n" \
  "s_movk_i32 s87, 200\n" \
  "s_cmp_eq_u32 vcc_lo, 0\n" \
  "s_cbranch_scc1 .LCV_W4_%=\n" \
  "s_add_u32 vcc_lo, vcc_lo, 590\n" \
  ".LCV_W3_%=:\n" \
  "s_sleep 2\n" \
  "s_memrealtime s[38:39]\n" \
  "s_sub_u32 s87, s87, 1\n" \
  "s_cbranch_scc1 .LCV_W4_%=\n" \
  "s_waitcnt lgkmcnt(0)\n" \
  "s_sub_u32 s38, s38, vcc_lo\n" \
  "s_cmp_lt_i32 s38, 0\n" \
  "s_cbranch_scc1 .LCV_W3_%=\n" \
  ".LCV_W4_%=:\n" \
  "s_waitcnt vmcnt(0)\n" \
  ".p2align 6\n" \
  ".LCV_TOP_%=:\n" \
  "s_waitcnt lgkmcnt(0)\n" \
  "s_barrier\n" \
  "s_setprio 3\n" \
  "ds_read_b128 v[24:27], v1 offset:0\n" \
  "ds_read_b128 v[28:31], v1 offset:1024\n" \
  "ds_read_b128 v[32:35], v1 offset:2048\n" \
  "ds_read_b128 v[36:39], v1 offset:3072\n" \
  "ds_read_b128 v[40:43], v1 offset:4096\n" \
  "ds_read_b128 v[44:47], v1 offset:5120\n" \
  "ds_read_b128 v[48:51], v1 offset:6144\n" \
  "ds_read_b128 v[52:55], v1 offset:7168\n" \
  "s_memrealtime s[38:39]\n" \
  "s_waitcnt lgkmcnt(7)\n" \
  "v_smfmac_f32_16x16x64_bf16 v[12:15], v[24:27], v[64:71], v0\n" \
  "s_waitcnt lgkmcnt(6)\n" \
  "v_smfmac_f32_16x16x64_bf16 v[12:15], v[28:31], v[72:79], v0\n" \
  "s_waitcnt lgkmcnt(5)\n" \
  "v_smfmac_f32_16x16x64_bf16 v[12:15], v[32:35], v[80:87], v0\n" \
  "s_waitcnt lgkmcnt(4)\n" \
  "v_smfmac_f32_16x16x64_bf16 v[12:15], v[36:39], v[88:95], v0\n" \
  "s_waitcnt lgkmcnt(3)\n" \
  "v_smfmac_f32_16x16x64_bf16 v[12:15], v[40:43], v[96:103], v0\n" \
  "s_waitcnt lgkmcnt(2)\n" \
  "v_smfmac_f32_16x16x64_bf16 v[12:15], v[44:47], v[104:111], v0\n" \
  "s_waitcnt lgkmcnt(1)\n" \
  "v_smfmac_f32_16x16x64_bf16 v[12:15], v[48:51], v[112:119], v0\n" \
  "s_waitcnt lgkmcnt(0)\n" \
  "v_smfmac_f32_16x16x64_bf16 v[12:15], v[52:55], v[120:127], v0\n" \
  "ds_read_b32 v11, v1 offset:26688\n" \
  "s_mov_b32 s61, s38\n" \
  "s_add_u32 s38, s38, s79\n" \
  "v_or_b32 v57, 1, s38\n" \
  "v_add_u32 v56, s67, v10\n" \
  "s_add_u32 s39, s67, s76\n" \
  "s_add_u32 s38, s38, s77\n" \
  "s_bitset1_b32 s38, 0\n" \
  "v_writelane_b32 v56, s39, 1\n" \
  "v_writelane_b32 v57, s38, 1\n" \
  "s_sub_u32 s39, s71, s72\n" \
  "s_cmp_lt_u32 s39, 9\n" \
  "s_cselect_b64 exec, 19, 3\n" \
  "buffer_store_dword v57, v56, s[0:3], 0 offen sc1\n" \
  "s_mov_b64 exec, -1\n" \
  "v_permlane32_swap_b32 v12, v14\n" \
  "v_permlane32_swap_b32 v13, v15\n" \
  "v_pk_add_f32 v[12:13], v[12:13], v[14:15]\n" \
  "s_nop 0\n" \
  "v_cvt_pk_bf16_f32 v12, v12, v13\n" \
  "ds_write_b32 v4, v12\n" \
  "s_setprio 2\n" \
  "v_mov_b64 v[12:13], s[92:93]\n" \
  "v_mov_b64 v[14:15], s[92:93]\n" \
  "s_waitcnt lgkmcnt(1)\n" \
  "v_readfirstlane_b32 s86, v11\n" \
  "s_lshr_b32 s86, s86, 23\n" \
  "s_add_i32 s86, s86, s83\n" \
  "s_ashr_i32 s86, s86, 1\n" \
  "s_max_i32 s86, s86, s82\n" \
  "s_min_i32 s86, s86, 100\n" \
  "s_lshl_b32 s86, s86, 23\n" \
  "s_sub_i32 s84, 0xc8800000, s86\n" \
  "s_cmp_eq_u32 s73, 5\n" \
  "v_smfmac_f32_16x16x64_bf16 v[16:19], v[24:27], v[128:135], v0\n" \
  "v_smfmac_f32_16x16x64_bf16 v[16:19], v[28:31], v[136:143], v0\n" \
  "v_smfmac_f32_16x16x64_bf16 v[16:19], v[32:35], v[144:151], v0\n" \
  "v_smfmac_f32_16x16x64_bf16 v[16:19], v[36:39], v[152:159], v0\n" \
  "v_smfmac_f32_16x16x64_bf16 v[16:19], v[40:43], v[160:167], v0\n" \
  "v_smfmac_f32_16x16x64_bf16 v[16:19], v[44:47], v[168:175], v0\n" \
  "s_waitcnt lgkmcnt(0)\n" \
  "s_barrier\n" \
  "ds_read_b64_tr_b16 v[60:61], v6 offset:0\n" \
  "ds_read_b64_tr_b16 v[62:63], v9 offset:0\n" \
  "v_smfmac_f32_16x16x64_bf16 v[16:19], v[48:51], v[176:183], v0\n" \
  "v_smfmac_f32_16x16x64_bf16 v[16:19], v[52:55], v[184:191], v0\n" \
  "v_smfmac_f32_16x16x64_bf16 v[20:23], v[24:27], v[192:199], v0\n" \
  "v_smfmac_f32_16x16x64_bf16 v[20:23], v[28:31], v[200:207], v0\n" \
  "v_smfmac_f32_16x16x64_bf16 v[20:23], v[32:35], v[208:215], v0\n" \
  "v_smfmac_f32_16x16x64_bf16 v[20:23], v[36:39], v[216:223], v0\n" \
  "v_smfmac_f32_16x16x64_bf16 v[20:23], v[40:43], v[224:231], v0\n" \
  "v_smfmac_f32_16x16x64_bf16 v[20:23], v[44:47], v[232:239], v0\n" \
  "v_smfmac_f32_16x16x64_bf16 v[20:23], v[48:51], v[240:247], v0\n" \
  "v_smfmac_f32_16x16x64_bf16 v[20:23], v[52:55], v[248:255], v0\n" \
  "s_cmp_eq_u32 s71, 0\n" \
  "v_permlane32_swap_b32 v16, v18\n" \
  "v_permlane32_swap_b32 v17, v19\n" \
  "v_add_f32 v16, v16, v18\n" \
  "v_add_f32 v17, v17, v19\n" \
  "v_cvt_pk_bf16_f32 v16, v16, v17\n" \
  "s_waitcnt lgkmcnt(0)\n" \
  "v_max_u32 v60, 1, v60\n" \
  "v_max_u32 v61, 1, v61\n" \
  "v_max_u32 v62, 1, v62\n" \
  "v_max_u32 v63, 1, v63\n" \
  "buffer_store_dwordx4 v[60:63], v7, s[48:51], s69 offen sc1\n" \
  "v_permlane32_swap_b32 v20, v22\n" \
  "v_permlane32_swap_b32 v21, v23\n" \
  "v_pk_add_f32 v[20:21], v[20:21], v[22:23]\n" \
  "v_max_u32 v18, 1, v16\n" \
  "v_cvt_pk_bf16_f32 v17, v20, v21\n" \
  "ds_write_b64 v5, v[16:17]\n" \
  "v_max_u32 v19, 1, v17\n" \
  "s_nop 1\n" \
  "v_permlane32_swap_b32 v18, v19\n" \
  "buffer_store_dwordx2 v[18:19], v8, s[52:55], s70 offen sc1\n" \
  "ds_read_b128 v[24:27], v3 offset:0\n" \
  "ds_read_b128 v[28:31], v3 offset:1024\n" \
  "ds_read_b128 v[32:35], v3 offset:2048\n" \
  "ds_read_b128 v[36:39], v3 offset:3072\n" \
  "ds_read_b128 v[40:43], v3 offset:8192\n" \
  "ds_read_b128 v[44:47], v3 offset:9216\n" \
  "ds_read_b128 v[48:51], v3 offset:10240\n" \
  "ds_read_b128 v[52:55], v3 offset:11264\n" \
  "s_cbranch_scc1 .LCV_LAST_%=\n" \
  "s_cmp_eq_u32 s73, 5\n" \
  "s_cbranch_scc0 .LCV_NMK_%=\n" \
  "s_mov_b32 m0, s78\n" \
  "s_nop 0\n" \
  "buffer_load_dword v2, s[0:3], s66 offen sc1 lds\n" \
  ".LCV_NMK_%=:\n" \
  "s_setprio 0\n" \
  "s_waitcnt lgkmcnt(8)\n" \
  "s_barrier\n" \
  "s_add_u32 s67, s67, 1024\n" \
  "v_mov_b32 v11, 0\n" \
  "ds_write_b32 v59, v11\n" \
  "ds_read_b128 v[56:59], v3 offset:4096\n" \
  "ds_read_b128 v[60:63], v3 offset:5120\n" \
  "v_mov_b64 v[20:21], s[92:93]\n" \
  "v_mov_b64 v[22:23], s[92:93]\n" \
  "v_mov_b64 v[16:17], s[92:93]\n" \
  "v_mov_b64 v[18:19], s[92:93]\n" \
  "s_mov_b32 m0, s94\n" \
  "s_mov_b64 exec, 15\n" \
  "buffer_load_dword v2, s[44:47], s68 offen sc1 lds\n" \
  "s_mov_b64 exec, -1\n" \
  "s_setprio 2\n" \
  "s_waitcnt lgkmcnt(0)\n" \
  "s_barrier\n" \
  "ds_read_b128 v[4:7], v1 offset:8192\n" \
  "ds_read_b128 v[8:11], v1 offset:9216\n" \
  "s_waitcnt lgkmcnt(1)\n" \
  "v_smfmac_f32_16x16x64_bf16 v[12:15], v[4:7], v[24:31], v0\n" \
  "ds_read_b128 v[24:27], v3 offset:6144\n" \
  "ds_read_b128 v[28:31], v3 offset:7168\n" \
  "v_smfmac_f32_16x16x64_bf16 v[16:19], v[4:7], v[32:39], v0\n" \
  "ds_read_b128 v[32:35], v3 offset:12288\n" \
  "ds_read_b128 v[36:39], v3 offset:13312\n" \
  "s_waitcnt lgkmcnt(4)\n" \
  "v_smfmac_f32_16x16x64_bf16 v[12:15], v[8:11], v[40:47], v0\n" \
  "v_smfmac_f32_16x16x64_bf16 v[16:19], v[8:11], v[48:55], v0\n" \
  "v_smfmac_f32_16x16x64_bf16 v[20:23], v[4:7], v[56:63], v0\n" \
  "ds_read_b128 v[56:59], v3 offset:14336\n" \
  "ds_read_b128 v[60:63], v3 offset:15360\n" \
  "v_mov_b64 v[40:41], s[92:93]\n" \
  "v_mov_b64 v[42:43], s[92:93]\n" \
  "s_nop 2\n" \
  "v_permlane32_swap_b32 v12, v17\n" \
  "v_permlane32_swap_b32 v13, v16\n" \
  "v_permlane32_swap_b32 v14, v19\n" \
  "v_permlane32_swap_b32 v15, v18\n" \
  "v_pk_add_f32 v[12:13], v[12:13], v[16:17] op_sel:[0,1] op_sel_hi:[1,0]\n" \
  "v_pk_add_f32 v[14:15], v[14:15], v[18:19] op_sel:[0,1] op_sel_hi:[1,0]\n" \
  "buffer_atomic_add_x2 v[12:13], v2, s[40:43], s56 offen\n" \
  "buffer_atomic_add_x2 v[14:15], v2, s[40:43], s57 offen\n" \
  "s_waitcnt lgkmcnt(2)\n" \
  "v_smfmac_f32_16x16x64_bf16 v[20:23], v[8:11], v[32:39], v0\n" \
  "v_smfmac_f32_16x16x64_bf16 v[40:43], v[4:7], v[24:31], v0\n" \
  "s_waitcnt lgkmcnt(0)\n" \
  "v_smfmac_f32_16x16x64_bf16 v[40:43], v[8:11], v[56:63], v0\n" \
  "s_nop 7\n" \
  "v_permlane32_swap_b32 v20, v41\n" \
  "v_permlane32_swap_b32 v21, v40\n" \
  "v_permlane32_swap_b32 v22, v43\n" \
  "v_permlane32_swap_b32 v23, v42\n" \
  "v_pk_add_f32 v[20:21], v[20:21], v[40:41] op_sel:[0,1] op_sel_hi:[1,0]\n" \
  "v_pk_add_f32 v[22:23], v[22:23], v[42:43] op_sel:[0,1] op_sel_hi:[1,0]\n" \
  "buffer_atomic_add_x2 v[20:21], v2, s[40:43], s58 offen\n" \
  "buffer_atomic_add_x2 v[22:23], v2, s[40:43], s59 offen\n" \
  "s_setprio 0\n" \
  "s_memrealtime s[38:39]\n" \
  "v_mbcnt_lo_u32_b32 v48, -1, 0\n" \
  "v_mbcnt_hi_u32_b32 v48, -1, v48\n" \
  "s_add_u32 s88, s74, 157760\n" \
  "v_mov_b32 v59, s88\n" \
  "v_lshl_add_u32 v52, v48, 2, v59\n" \
  "ds_read_b32 v52, v52 offset:192\n" \
  "v_lshrrev_b32 v49, 3, v48\n" \
  "v_and_b32 v50, 7, v48\n" \
  "v_lshlrev_b32 v50, 4, v50\n" \
  "v_lshl_or_b32 v44, v49, 8, v50\n" \
  "s_mov_b32 s87, 0x40000\n" \
  "v_lshlrev_b32 v10, 4, v48\n" \
  "v_lshl_add_u32 v58, v48, 2, s94\n" \
  "s_waitcnt vmcnt(4)\n" \
  "ds_read_b32 v58, v58\n" \
  "s_waitcnt lgkmcnt(0)\n" \
  "v_cmp_eq_u32 vcc, 0, v58\n" \
  "s_and_b32 vcc_lo, vcc_lo, 15\n" \
  "s_cbranch_scc0 .LCV_GOT_%=\n" \
  "s_branch .LCV_ASK_%=\n" \
  ".LCV_LATE_%=:\n" \
  "v_cmp_eq_u32 vcc, 0, v58\n" \
  "s_and_b32 vcc_lo, vcc_lo, 15\n" \
  "s_cbranch_scc0 .LCV_GOT_%=\n" \
  "s_sleep 12\n" \
  ".LCV_ASK_%=:\n" \
  "s_mov_b64 exec, 15\n" \
  "buffer_load_dword v58, v2, s[44:47], s68 offen sc1\n" \
  "s_mov_b64 exec, -1\n" \
  "s_sub_u32 s87, s87, 1\n" \
  "s_cbranch_scc1 .LCV_END_%=\n" \
  "s_waitcnt vmcnt(0)\n" \
  "s_branch .LCV_LATE_%=\n" \
  ".LCV_GOT_%=:\n" \
  "s_mov_b32 m0, s99\n" \
  "s_add_u32 s88, s99, 8192\n" \
  "global_load_lds_dwordx4 v10, s[62:63] sc1\n" \
  "global_load_lds_dwordx4 v10, s[62:63] offset:1024 sc1\n" \
  "s_mov_b32 m0, s88\n" \
  "s_nop 0\n" \
  "global_load_lds_dwordx4 v10, s[64:65] sc1\n" \
  "global_load_lds_dwordx4 v10, s[64:65] offset:1024 sc1\n" \
  ".LCV_HAVE_%=:\n" \
  "s_waitcnt lgkmcnt(0)\n" \
  "s_sub_u32 s61, s38, s61\n" \
  "s_add_u32 s79, s79, 2\n" \
  "s_min_u32 s79, s79, s61\n" \
  "s_sub_u32 s39, s38, 150\n" \
  "v_subrev_u32 v52, s39, v52\n" \
  "v_cmp_lt_u32 vcc, 0x1000, v52\n" \
  "s_cmp_eq_u64 vcc, 0\n" \
  "s_cbranch_scc1 .LCV_OKM_%=\n" \
  ".LCV_AGN_%=:\n" \
  "buffer_load_dword v52, v2, s[0:3], s66 offen sc1\n" \
  "s_sub_u32 s87, s87, 1\n" \
  "s_cbranch_scc1 .LCV_END_%=\n" \
  "s_waitcnt vmcnt(0)\n" \
  "v_cmp_eq_u32 vcc, 0, v52\n" \
  "v_subrev_u32 v52, s39, v52\n" \
  "s_cmp_eq_u64 vcc, 0\n" \
  "s_cbranch_scc1 .LCV_OKM_%=\n" \
  "s_sleep 4\n" \
  "s_branch .LCV_AGN_%=\n" \
  ".LCV_OKM_%=:\n" \
  "s_nop 1\n" \
  "v_max_i32_dpp v52, v52, v52 row_ror:1 row_mask:0xf bank_mask:0xf\n" \
  "s_nop 1\n" \
  "v_max_i32_dpp v52, v52, v52 row_ror:2 row_mask:0xf bank_mask:0xf\n" \
  "s_nop 1\n" \
  "v_max_i32_dpp v52, v52, v52 row_ror:4 row_mask:0xf bank_mask:0xf\n" \
  "s_nop 1\n" \
  "v_max_i32_dpp v52, v52, v52 row_ror:8 row_mask:0xf bank_mask:0xf\n" \
  "s_movk_i32 vcc_lo, 150\n" \
  "v_readlane_b32 s88, v52, 0\n" \
  "s_max_i32 vcc_lo, vcc_lo, s88\n" \
  "v_readlane_b32 s88, v52, 16\n" \
  "s_max_i32 vcc_lo, vcc_lo, s88\n" \
  "v_readlane_b32 s88, v52, 32\n" \
  "s_max_i32 vcc_lo, vcc_lo, s88\n" \
  "v_readlane_b32 s88, v52, 48\n" \
  "s_max_i32 vcc_lo, vcc_lo, s88\n" \
  "s_add_u32 s66, s66, 1024\n" \
  "s_add_u32 vcc_lo, vcc_lo, s39\n" \
  "s_add_u32 vcc_lo, vcc_lo, 23\n" \
  ".LCV_TW_%=:\n" \
  "s_memrealtime s[38:39]\n" \
  "s_waitcnt lgkmcnt(0)\n" \
  "s_sub_u32 s38, s38, vcc_lo\n" \
  "s_cmp_lt_i32 s38, 0\n" \
  "s_cbranch_scc1 .LCV_TW_%=\n" \
  "buffer_load_dwordx4 v[24:27], v44, s[40:43], s60 offen sc1\n" \
  "buffer_load_dwordx4 v[28:31], v44, s[40:43], s60 offen offset:2048 sc1\n" \
  "s_add_u32 s62, s62, 0x80000\n" \
  "s_addc_u32 s63, s63, 0\n" \
  "s_add_u32 s64, s64, 0x40000\n" \
  "s_addc_u32 s65, s65, 0\n" \
  "s_add_u32 s68, s68, 0x1000\n" \
  "s_add_u32 s69, s69, 0x10000\n" \
  "s_add_u32 s70, s70, 0x10000\n" \
  "s_sub_u32 s88, s73, 4\n" \
  "v_lshrrev_b32 v49, 2, v48\n" \
  "v_lshrrev_b32 v50, 4, v48\n" \
  "v_bitop3_b32 v50, v49, v50, 3 bitop3:0x28\n" \
  "v_lshl_add_u32 v4, v50, 3, s97\n" \
  "v_and_b32 v50, 3, v48\n" \
  "v_lshl_add_u32 v4, v50, 5, v4\n" \
  "v_and_b32 v49, 1, v49\n" \
  "v_lshl_add_u32 v4, v49, 8, v4\n" \
  "v_and_b32 v49, 8, v48\n" \
  "v_lshl_add_u32 v4, v49, 4, v4\n" \
  "v_and_b32 v51, 15, v48\n" \
  "v_bfe_u32 v49, v48, 4, 1\n" \
  "v_lshrrev_b32 v50, 5, v48\n" \
  "s_lshl_b32 s89, s88, 10\n" \
  "s_add_u32 s89, s89, s74\n" \
  "s_add_u32 s89, s89, 158720\n" \
  "v_lshrrev_b32 v9, 3, v51\n" \
  "v_mul_u32_u24 v6, 41, v9\n" \
  "v_xor_b32 v6, v6, v51\n" \
  "v_lshl_add_u32 v6, v6, 3, s89\n" \
  "v_lshl_add_u32 v6, v49, 6, v6\n" \
  "v_lshl_add_u32 v6, v50, 9, v6\n" \
  "v_xor_b32 v9, 0x90, v6\n" \
  "v_lshlrev_b32 v8, 4, v49\n" \
  "v_lshl_add_u32 v8, v51, 5, v8\n" \
  "v_lshl_add_u32 v8, v50, 3, v8\n" \
  "s_lshl_b32 s89, s88, 8\n" \
  "s_add_u32 s89, s89, s74\n" \
  "s_add_u32 s89, s89, 139264\n" \
  "v_and_b32 v49, 31, v48\n" \
  "v_lshl_add_u32 v5, v49, 3, s89\n" \
  "v_lshl_add_u32 v5, v50, 10, v5\n" \
  "v_lshrrev_b32 v49, 2, v51\n" \
  "v_and_b32 v50, 5, v51\n" \
  "v_and_or_b32 v49, v49, 2, v50\n" \
  "v_and_b32 v50, 48, v48\n" \
  "v_lshlrev_b32 v7, 3, v50\n" \
  "v_lshl_add_u32 v7, v49, 4, v7\n" \
  "v_bitop3_b32 v49, v51, 2, 2 bitop3:0x0c\n" \
  "v_lshl_add_u32 v7, v49, 29, v7\n" \
  "v_mov_b64 v[12:13], 0\n" \
  "v_mov_b64 v[14:15], 0\n" \
  "v_mov_b64 v[16:17], 0\n" \
  "v_mov_b64 v[18:19], 0\n" \
  "v_mov_b64 v[20:21], 0\n" \
  "v_mov_b64 v[22:23], 0\n" \
  "v_and_b32 v50, 3, v48\n" \
  "v_lshl_add_u32 v40, v50, 2, s98\n" \
  "v_lshrrev_b32 v50, 4, v48\n" \
  "v_mad_u32_u24 v40, v50, s95, v40\n" \
  "v_bfe_u32 v49, v48, 3, 1\n" \
  "v_lshl_add_u32 v40, v49, 6, v40\n" \
  "v_bfe_u32 v49, v48, 2, 1\n" \
  "v_lshl_add_u32 v40, v49, 7, v40\n" \
  "v_xor_b32 v41, 16, v40\n" \
  "v_xor_b32 v42, 32, v40\n" \
  "v_xor_b32 v43, 48, v40\n" \
  "s_add_i32 s80, s86, 0x35000000\n" \
  "v_mov_b32 v11, 0\n" \
  "s_mov_b32 s89, s60\n" \
  "s_bitcmp1_b32 s60, 7\n" \
  "s_cselect_b32 s88, 0x10000, 0\n" \
  "s_bitset1_b32 s88, 7\n" \
  "s_xor_b32 s56, s56, s88\n" \
  "s_xor_b32 s57, s57, s88\n" \
  "s_xor_b32 s58, s58, s88\n" \
  "s_xor_b32 s59, s59, s88\n" \
  "s_xor_b32 s60, s60, s88\n" \
  "s_sub_u32 s71, s71, 1\n" \
  ".LCV_SEE_%=:\n" \
  "s_waitcnt vmcnt(1)\n" \
  "v_cvt_f32_i32 v24, v24\n" \
  "v_cvt_f32_i32 v25, v25\n" \
  "v_cvt_f32_i32 v26, v26\n" \
  "v_cvt_f32_i32 v27, v27\n" \
  "v_max_f32 v45, |v24|, |v26|\n" \
  "v_pk_mul_f32 v[24:25], v[24:25], s[80:81] op_sel_hi:[1,0]\n" \
  "v_pk_mul_f32 v[26:27], v[26:27], s[80:81] op_sel_hi:[1,0]\n" \
  "s_nop 0\n" \
  "v_cvt_pk_bf16_f32 v32, v24, v26\n" \
  "v_cvt_pk_bf16_f32 v33, v25, v27\n" \
  "ds_write_b32 v40, v32\n" \
  "ds_write_b32 v41, v33\n" \
  "s_waitcnt vmcnt(0)\n" \
  "v_cvt_f32_i32 v28, v28\n" \
  "v_cvt_f32_i32 v30, v30\n" \
  "v_cvt_f32_i32 v29, v29\n" \
  "v_max3_f32 v45, v45, |v28|, |v30|\n" \
  "v_cvt_f32_i32 v31, v31\n" \
  "v_cmp_lt_f32 vcc, s91, v45\n" \
  "v_pk_mul_f32 v[28:29], v[28:29], s[80:81] op_sel_hi:[1,0]\n" \
  "v_pk_mul_f32 v[30:31], v[30:31], s[80:81] op_sel_hi:[1,0]\n" \
  "s_cmp_eq_u64 vcc, 0\n" \
  "s_cbranch_scc0 .LCV_POLL_%=\n" \
  "v_cvt_pk_bf16_f32 v36, v28, v30\n" \
  "v_cvt_pk_bf16_f32 v37, v29, v31\n" \
  "ds_write_b32 v42, v36\n" \
  "ds_write_b32 v43, v37\n" \
  "v_dot2c_f32_bf16 v11, v32, v32\n" \
  "v_dot2c_f32_bf16 v11, v33, v33\n" \
  "v_dot2c_f32_bf16 v11, v36, v36\n" \
  "v_dot2c_f32_bf16 v11, v37, v37\n" \
  "s_nop 1\n" \
  "s_nop 0\n" \
  "v_cmp_lt_f32 vcc, 4.0, v11\n" \
  "s_cbranch_vccz .LCV_TOP_%=\n" \
  "s_mov_b64 exec, vcc\n" \
  "ds_max_u32 v59, v11\n" \
  "s_mov_b64 exec, -1\n" \
  "s_branch .LCV_TOP_%=\n" \
  ".LCV_POLL_%=:\n" \
  "buffer_load_dwordx4 v[24:27], v44, s[40:43], s89 offen sc1\n" \
  "buffer_load_dwordx4 v[28:31], v44, s[40:43], s89 offen offset:2048 sc1\n" \
  "s_sub_u32 s87, s87, 1\n" \
  "s_cbranch_scc1 .LCV_END_%=\n" \
  "s_branch .LCV_SEE_%=\n" \
  ".LCV_LAST_%=:\n" \
  "s_waitcnt vmcnt(0)\n" \
  ".LCV_END_%=:\n" \
  "s_endpgm\n"
__device__ __forceinline__ void start_kgen(const MegaArgs& A, char* smem, const int kg) {
  const int w = A.wv, l = lane_id(), tid = 64 * w + l, c = l & 15, g = l >> 4;
  KGen G;
  G.load(A, 8 * kg + w, l);
  {
    const __amdgpu_buffer_rsrc_t rz = MKRS(A.ws + OFF_Z, Z_BYTES);
#pragma unroll
    for (int i = 0; i < 4; ++i) {
      const u64 z = i == 0 && kg < (int)(2 * SZ_ACC / 8192) ? ACC_INIT : 0ull;
      ST128((v4u{(unsigned)z, (unsigned)(z >> 32), (unsigned)z, (unsigned)(z >> 32)}), rz, tid * 16, (i * 64 + kg) * 8192);
    }
  }
  const u64 tag = call_tag(A);
  DRAIN();
  __syncthreads();
  if (tid < 8) __hip_atomic_store((gu64*)(A.ws + OFF_RD) + tid * 64 + kg, tag, RLX_AGENT);
  asm volatile("" : "+v"(G.pa), "+v"(G.pb), "+v"(G.pe));
  G.setup();
  G.st = smem + w * 16640;
  const __amdgpu_buffer_rsrc_t rs = MKRS(A.ws + OFF_KF + (8 * kg + w) * 2048, 386 * 4);
  SST(1);
  unsigned tw = 0u;
#define TLB_ASK()                                                                                                      \
  if (tid < 384) {                                                                                                     \
    const unsigned o = (unsigned)OFF_XA + (unsigned)(tid * 8 + ((blockIdx.x >> 3) & 7)) * 65536u;                      \
    if (o < WS_TOTAL) asm volatile("global_load_dword %0, %1, %2" : "=v"(tw) : "v"(o), "s"(A.ws) : "memory");          \
  }
  if (TLBW == 1) TLB_ASK()
  unsigned pf[6] = {};
#define PF_ASK(M)                                                                                                                  \
  if (kg < 32) {                                                                                                                   \
    if ((M) & 1) asm volatile("global_load_dword %0, %2, %3\n\tglobal_load_dword %1, %2, %3 offset:128" : "=&v"(pf[0]), "=&v"(pf[1]) : "v"(kg * 131072 + tid * 256), "s"(A.Wo)); \
    if ((M) & 2) asm volatile("global_load_dword %0, %2, %3\n\tglobal_load_dword %1, %2, %3 offset:128" : "=&v"(pf[2]), "=&v"(pf[3]) : "v"(kg * 256 + tid * 8192), "s"(A.Wq)); \
    if ((M) & 4) asm volatile("global_load_dword %0, %2, %3\n\tglobal_load_dword %1, %2, %3 offset:128" : "=&v"(pf[4]), "=&v"(pf[5]) : "v"(kg * 256 + tid * 8192), "s"(A.Wk)); \
  }
  PF_ASK(PFA)
  G.columns(l);
  PF_ASK(PFB)
  if (TLBW == 2) TLB_ASK()
  (void)G.scale();
  const Cx zb = G.power(256.f);
  Cx b = G.C, z = G.power(2.f);
#pragma unroll 1
  for (int f = 1; f < 9;) {
    char* p = G.st + l * 8;
    const int fe = f == 1 ? 4 : 9;
#pragma unroll 1
    for (; f < fe; ++f) {
      b = cmul(b, zb);
      if (f == 2 || f == 4) z = cmul(z, z);
      p = G.rows(p, b, z, f == 1 ? 8 : f < 4 ? 4 : 2);
    }
    const f32x4 d = G.tile(l);
    const v4u r = {pkh(d.x, d.x), pkh(d.y, d.y), pkh(d.z, d.z), pkh(d.w, d.w)};
    if (f == 4) {
      ST128(r, rs, (g < 2 ? 8 * c + 4 * g : 64 * g + 4 * c) * 4, 0);
    } else {
      ST64((v2u{r.x, r.y}), rs, (256 + 64 * g + 2 * c) * 4, 0);
      ST64((v2u{r.z, r.w}), rs, (288 + 64 * g + 2 * c) * 4, 0);
    }
  }
  SST(2);
  DRAIN();
  asm volatile("" ::"v"(tw), "v"(pf[0]), "v"(pf[1]), "v"(pf[2]), "v"(pf[3]), "v"(pf[4]), "v"(pf[5]));
  if (l == 0) __hip_atomic_store((gu64*)(A.ws + OFF_KG) + kg * 16 + w, tag, RLX_AGENT);
  if (TLBW == 3) {
    TLB_ASK()
    DRAIN();
    asm volatile("" ::"v"(tw));
  }
  __syncthreads();
  SST(3);
}

constexpr int C_SM = 131072, C_SO = C_SM + 8192, C_KT = C_SO + 2048, C_X = C_KT + 16384, C_SQ = C_X + 1024;
static_assert(C_SQ + 4096 <= 163840 && C_SQ % 512 == 0, "lds");
__device__ __forceinline__ v4u mk_srd(const void* p, const unsigned bytes) {
  const unsigned long long a = (unsigned long long)p;
  return v4u{(unsigned)a, (unsigned)(a >> 32) & 0xffffu, bytes, 0x00020000u};
}
template <bool ATT>
__device__ __forceinline__ void role_chain_t(const MegaArgs& A, char* smem, const int L, const int h, const int rep) {
  const int w = A.wv;
  char* ws = A.ws;
  unsigned* FL = (unsigned*)(ws + OFF_FL);
  const int l = lane_id(), c = l & 15, g = l >> 4;
  unsigned* FQ = FL + FL_FQ + L * 64 * 32 * 32;
  const int wb = (w + ROT_H) & 7, tb = ROT_T;
  const unsigned sb = lds_base(smem);
  __builtin_amdgcn_s_waitcnt(0x0f70);
  glds16(A.mem0, (unsigned)((c & 7) * DM + 64 * w + 16 * g + 8 * (c >> 3)) * 2, sb + C_SM + w * 1024);
#pragma unroll
  for (int i = 0; i < 16; ++i)
    glds16((const char*)A.Wo + (size_t)(h * DH + 32 * (i >> 2) + 4 * w + (i & 2)) * (DM * 2),
           (unsigned)((i & 1) * 1024 + ((l ^ (((i & 1) | ((w & 3) << 1)) << 1)) << 4)), sb + w * 16384 + i * 1024);
  const u64 tag = call_tag(A);
  const char* gK = ws + OFF_QK + L * SZ_QK + ((size_t)h * 16 + 2 * w) * 1024;
  const char* gV = ws + OFF_VF + L * SZ_O + ((size_t)h * 8 + 2 * w - 8) * 1024;
  v8u pv, pw;
  {
    const unsigned voff = g * 256 + c * 8, wp = sb + w * 16384 + l * 16;
    pv[0] = (c & 8) ? 0xEEEE : 0x4444;
    pv[1] = sb + C_SM + l * 16;
    const unsigned ra = sb + C_SQ + (g >> 1) * 512 + (c ^ ((g & 1) * 18)) * 8;
    pv[2] = ATT ? ra : voff;
    pv[3] = ATT ? ra ^ 0x108 : wp;
    pv[4] = sb + ((c & 2) ? C_SQ : C_SO) + w * 256 + (((c & 5) | ((c >> 2) & 2)) + 8 * (g >> 1)) * 16 + 8 * ((g ^ (c >> 1)) & 1);
    pv[5] = voff;
    pv[6] = wp;
    pv[7] = sb + C_SO + (l >> 5) * 1024 + w * 256 + (l & 31) * 8;
    const int q = (l >> 2) & 3, p = l & 3;
    const int f = (q & 1) | ((g & 1) << 1) | ((q >> 1) << 2);
#pragma unroll
    for (int i = 0; i < 4; ++i) pw[i] = (4 * i + g) * (HD * 2) + ((c ^ (((g & 1) | (i << 1)) << 1)) << 4) - 1024 * i;
    pw[4] = sb + (g >> 1) * 16384 + (4 * (g & 1) + (q & 1) + 8 * (q >> 1)) * 256 + (w ^ f) * 32 + p * 8;
    pw[5] = sb + (4 * (g >> 1) + 2 * (q >> 1) + (g & 1)) * 16384 + (q & 1) * 1024 + p * 8 + ((4 * wb) ^ f ^ tb) * 32;
    pw[6] = pw[7] = 0u;
  }
  const size_t wo = (size_t)(16 * w) * HD + h * DH;
#define POL 2
#define PG(w) (POL == 2 ? (w) * 4096 : ((w) & 1) * 4096 + ((w) >> 1) * 16384)
  const v4u rsA = mk_srd(ws + OFF_ACC + L * SZ_ACC + (POL == 1 ? 8192 : 0), SZ_ACC - 8192);
  const v4u rsF = mk_srd(FQ, 64 * 32 * 32 * 4);
  const v4u rsMK = mk_srd(ws + OFF_MEMK + L * SZ_MEM, SZ_MEM);
  const v4u rsMV = mk_srd(ws + OFF_MEMV + L * SZ_MEM, SZ_MEM);
  const v4u pAB = (v4u{0u, 2048u, 3072u, 1024u} ^ (1536u * tb)) + PG(wb);
  const v4u rsT = mk_srd(FL + FL_FK + L * 32, 64 * 1024 - 128);
  unsigned sc[8];
  sc[0] = h * 128;
  sc[1] = (h * 4 + w - 4) * 512;
  sc[2] = (h * 4 + w - 4) * 512;
  sc[3] = NSEG - 1;
  sc[5] = w;
  sc[6] = sb;
  const char* dbg = ws;
#define CHAIN_OPS                                                                                                          \
  ::"{v[0:7]}"(pv), "{v[24:31]}"(pw), "{s[4:5]}"(A.Wv + wo), "{s[6:7]}"(A.Wq + wo), "{s[8:9]}"(A.Wk + wo), "{s[10:11]}"(tag),         \
      "{s[12:13]}"(ws + OFF_RD + (blockIdx.x & 7) * 512), "{s[14:15]}"(ws + OFF_G + (blockIdx.x & 7) * 256), "{s[16:17]}"(ws + OFF_G + h * 8), \
      "{s18}"(w == 4 && L == 0 ? 255 : 0), "{s19}"(sb + w * 16384), "{s[20:21]}"(ws + WS_TOTAL + (w == 0 ? 534 * 256 + A.vb : 610 * 256) * 8), \
      "{s[40:43]}"(rsA), "{s[44:47]}"(rsF), "{s[48:51]}"(rsMK), "{s[52:55]}"(rsMV), "{s[56:59]}"(pAB), "{s60}"(PG(w)), "{s61}"(ATT ? PG(h >> 2) + (h & 3) * 1024 : L), \
      "{s[62:63]}"(gK), "{s[64:65]}"(gV), "{s66}"(0), "{s67}"(w == 4 ? (h >> 3) * 256 + (h & 7) * 8 : 0x40000000), "{s68}"(sc[0]), "{s69}"(sc[1]), "{s70}"(sc[2]),    \
      "{s71}"(sc[3]), "{s73}"(sc[5]), "{s74}"(sc[6]), "{s[36:37]}"(dbg),                   \
      "{s[0:3]}"(rsT)                                                                                                      \
      : "memory", "vcc", "scc", "s99", "v255"
  if (ATT) asm volatile("s_mov_b64 exec, -1\ns_nop 4\n" CHAIN_ASM_ATT CHAIN_OPS);
  else asm volatile("s_mov_b64 exec, -1\ns_nop 4\n" CHAIN_ASM_VO CHAIN_OPS);
#undef CHAIN_OPS
}

__device__ __forceinline__ void role_chain(const MegaArgs& A, char* smem, const int L, const int h, const int rep) {
  if (A.wv < 4) role_chain_t<true>(A, smem, L, h, rep);
  else role_chain_t<false>(A, smem, L, h, rep);
}

constexpr int AW_SO = 0, AW_TK = 32768, AW_CNT = AW_TK + 98304;
__device__ __forceinline__ unsigned umin3(unsigned a, unsigned b, unsigned c) {
  return __builtin_elementwise_min(__builtin_elementwise_min(a, b), c);
}
#define AW_ASM_X \
  "s_mov_b32 s61, 0\n" \
  "s_mov_b32 s67, 1\n" \
  "s_mov_b32 s68, 0\n" \
  "s_mov_b32 s70, 0\n" \
  "s_mov_b32 s73, 0\n" \
  "s_mov_b32 s71, 0x3e0293ee\n" \
  "s_mov_b32 s72, 0xbe0293ee\n" \
  "s_mov_b32 s65, 49152\n" \
  "s_bitcmp0_b32 s58, 2\n" \
  "s_cbranch_scc1 .LCX_OWN_%=\n" \
  "v_swap_b32 v192, v208\n" \
  "v_swap_b32 v193, v209\n" \
  "v_swap_b32 v194, v210\n" \
  "v_swap_b32 v195, v211\n" \
  "v_swap_b32 v196, v212\n" \
  "v_swap_b32 v197, v213\n" \
  "v_swap_b32 v198, v214\n" \
  "v_swap_b32 v199, v215\n" \
  "v_swap_b32 v200, v216\n" \
  "v_swap_b32 v201, v217\n" \
  "v_swap_b32 v202, v218\n" \
  "v_swap_b32 v203, v219\n" \
  "v_swap_b32 v204, v220\n" \
  "v_swap_b32 v205, v221\n" \
  "v_swap_b32 v206, v222\n" \
  "v_swap_b32 v207, v223\n" \
  "v_swap_b32 v224, v240\n" \
  "v_swap_b32 v225, v241\n" \
  "v_swap_b32 v226, v242\n" \
  "v_swap_b32 v227, v243\n" \
  "v_swap_b32 v228, v244\n" \
  "v_swap_b32 v229, v245\n" \
  "v_swap_b32 v230, v246\n" \
  "v_swap_b32 v231, v247\n" \
  "v_swap_b32 v232, v248\n" \
  "v_swap_b32 v233, v249\n" \
  "v_swap_b32 v234, v250\n" \
  "v_swap_b32 v235, v251\n" \
  "v_swap_b32 v236, v252\n" \
  "v_swap_b32 v237, v253\n" \
  "v_swap_b32 v238, v254\n" \
  "v_swap_b32 v239, v255\n" \
  ".LCX_OWN_%=:\n" \
  "v_mov_b32 v38, s58\n" \
  "s_memrealtime s[38:39]\n" \
  "s_waitcnt lgkmcnt(0)\n" \
  "s_mov_b32 s69, s38\n" \
  "s_setprio 3\n" \
  ".p2align 6\n" \
  ".LCX_TOP_%=:\n" \
  "s_mov_b32 s62, 0x200000\n" \
  "s_cmp_ge_u32 s73, s67\n" \
  "s_cbranch_scc1 .LCX_TOK_%=\n" \
  "s_cmp_eq_u32 s61, 0\n" \
  "s_cbranch_scc1 .LCX_SPIN_%=\n" \
  ".LCX_RE_%=:\n" \
  "ds_read_b32 v37, v38\n" \
  "s_add_u32 s62, s62, 1\n" \
  "s_waitcnt lgkmcnt(0)\n" \
  "v_readfirstlane_b32 s73, v37\n" \
  "s_bfe_u32 s64, s73, 0x80008\n" \
  "s_and_b32 s73, s73, 0xff\n" \
  "s_min_u32 s73, s73, s64\n" \
  "s_cmp_ge_u32 s73, s67\n" \
  "s_cbranch_scc1 .LCX_TOK_%=\n" \
  "s_sleep 1\n" \
  "s_cmp_lt_u32 s62, 2097158\n" \
  "s_cbranch_scc1 .LCX_RE_%=\n" \
  "s_waitcnt vmcnt(0)\n" \
  "v_mov_b32 v144, s59\n" \
  "v_mov_b32 v145, 1\n" \
  "s_mov_b64 exec, 1\n" \
  "ds_add_rtn_u32 v144, v144, v145\n" \
  "s_mov_b64 exec, -1\n" \
  "s_xor_b32 s59, s59, 4\n" \
  "s_mov_b32 s61, 0\n" \
  "s_waitcnt lgkmcnt(0)\n" \
  "v_readfirstlane_b32 s63, v144\n" \
  "v_lshlrev_b32 v144, 5, v5\n" \
  "s_and_b32 s63, s63, 7\n" \
  "s_cmp_eq_u32 s63, 7\n" \
  "s_cbranch_scc0 .LCX_W1_%=\n" \
  "s_mov_b64 exec, 15\n" \
  "global_store_dword v144, v145, s[52:53] sc1\n" \
  "s_mov_b64 exec, -1\n" \
  ".LCX_W1_%=:\n" \
  "s_add_u32 s52, s52, 2048\n" \
  "s_addc_u32 s53, s53, 0\n" \
  ".LCX_SPIN_%=:\n" \
  "s_sleep 2\n" \
  "s_memrealtime s[38:39]\n" \
  "ds_read_b32 v37, v38\n" \
  "s_sub_u32 s62, s62, 1\n" \
  "s_cbranch_scc1 .LCX_END_%=\n" \
  "s_waitcnt lgkmcnt(0)\n" \
  "v_readfirstlane_b32 s73, v37\n" \
  "s_add_u32 s38, s38, 80\n" \
  "s_bfe_u32 s64, s73, 0x80008\n" \
  "s_and_b32 s73, s73, 0xff\n" \
  "s_min_u32 s73, s73, s64\n" \
  "s_cmp_ge_u32 s73, s67\n" \
  "s_cbranch_scc0 .LCX_SPIN_%=\n" \
  "s_cmp_eq_u32 s70, 0\n" \
  "s_cbranch_scc1 .LCX_TOK_%=\n" \
  "s_sub_u32 s63, s38, s69\n" \
  "s_max_i32 s63, s63, 0\n" \
  "s_add_u32 s69, s69, s63\n" \
  ".LCX_TOK_%=:\n" \
  "ds_read_b128 v[48:51], v6 offset:0\n" \
  "ds_read_b128 v[64:67], v0 offset:8192\n" \
  "ds_read_b128 v[80:83], v0 offset:12288\n" \
  "ds_read_b128 v[52:55], v6 offset:1024\n" \
  "ds_read_b128 v[68:71], v0 offset:9216\n" \
  "ds_read_b128 v[84:87], v0 offset:13312\n" \
  "ds_read_b128 v[56:59], v6 offset:2048\n" \
  "ds_read_b128 v[72:75], v0 offset:10240\n" \
  "ds_read_b128 v[88:91], v0 offset:14336\n" \
  "ds_read_b128 v[60:63], v6 offset:3072\n" \
  "ds_read_b128 v[76:79], v0 offset:11264\n" \
  "ds_read_b128 v[92:95], v0 offset:15360\n" \
  "s_waitcnt lgkmcnt(9)\n" \
  "v_mfma_f32_16x16x32_bf16 v[20:23], v[64:67], v[48:51], v[8:11]\n" \
  "v_mfma_f32_16x16x32_bf16 v[24:27], v[80:83], v[48:51], v[12:15]\n" \
  "ds_read_b128 v[96:99], v0 offset:16384\n" \
  "ds_read_b128 v[100:103], v0 offset:17408\n" \
  "s_waitcnt lgkmcnt(8)\n" \
  "v_mfma_f32_16x16x32_bf16 v[20:23], v[68:71], v[52:55], v[20:23]\n" \
  "v_mfma_f32_16x16x32_bf16 v[24:27], v[84:87], v[52:55], v[24:27]\n" \
  "ds_read_b128 v[104:107], v0 offset:18432\n" \
  "ds_read_b128 v[108:111], v0 offset:19456\n" \
  "s_waitcnt lgkmcnt(7)\n" \
  "v_mfma_f32_16x16x32_bf16 v[20:23], v[72:75], v[56:59], v[20:23]\n" \
  "v_mfma_f32_16x16x32_bf16 v[24:27], v[88:91], v[56:59], v[24:27]\n" \
  "ds_read_b128 v[112:115], v0 offset:20480\n" \
  "ds_read_b128 v[116:119], v0 offset:21504\n" \
  "s_waitcnt lgkmcnt(6)\n" \
  "v_mfma_f32_16x16x32_bf16 v[20:23], v[76:79], v[60:63], v[20:23]\n" \
  "v_mfma_f32_16x16x32_bf16 v[24:27], v[92:95], v[60:63], v[24:27]\n" \
  "ds_read_b128 v[120:123], v0 offset:22528\n" \
  "ds_read_b128 v[124:127], v0 offset:23552\n" \
  "s_add_u32 s67, s67, 1\n" \
  "v_add_u32 v0, s65, v0\n" \
  "v_add_u32 v6, s65, v6\n" \
  "s_sub_u32 s65, 0, s65\n" \
  "s_nop 1\n" \
  "v_max3_f32 v32, v20, v21, v22\n" \
  "v_max3_f32 v33, v23, v24, v25\n" \
  "v_max3_f32 v32, v32, v26, v27\n" \
  "v_max_f32 v32, v32, v33\n" \
  "v_mov_b32 v33, v32\n" \
  "s_nop 1\n" \
  "v_permlane16_swap_b32 v32, v33\n" \
  "v_max_f32 v32, v32, v33\n" \
  "v_mov_b32 v33, v32\n" \
  "s_nop 1\n" \
  "v_permlane32_swap_b32 v32, v33\n" \
  "v_max_f32 v32, v32, v33\n" \
  "v_mul_f32 v33, s72, v32\n" \
  "v_fma_f32 v20, v20, s71, v33\n" \
  "v_fma_f32 v21, v21, s71, v33\n" \
  "v_fma_f32 v22, v22, s71, v33\n" \
  "v_fma_f32 v23, v23, s71, v33\n" \
  "v_fma_f32 v24, v24, s71, v33\n" \
  "v_fma_f32 v25, v25, s71, v33\n" \
  "v_fma_f32 v26, v26, s71, v33\n" \
  "v_fma_f32 v27, v27, s71, v33\n" \
  "v_exp_f32 v20, v20\n" \
  "v_exp_f32 v21, v21\n" \
  "v_exp_f32 v22, v22\n" \
  "v_exp_f32 v23, v23\n" \
  "v_exp_f32 v24, v24\n" \
  "v_exp_f32 v25, v25\n" \
  "v_exp_f32 v26, v26\n" \
  "v_exp_f32 v27, v27\n" \
  "v_add_f32 v34, v20, v21\n" \
  "v_add_f32 v33, v22, v23\n" \
  "v_add_f32 v36, v24, v25\n" \
  "v_add_f32 v34, v34, v33\n" \
  "v_add_f32 v33, v26, v27\n" \
  "v_add_f32 v34, v34, v36\n" \
  "v_add_f32 v34, v34, v33\n" \
  "v_mov_b64 v[28:29], 0\n" \
  "v_mov_b64 v[30:31], 0\n" \
  "s_waitcnt vmcnt(0) lgkmcnt(0)\n" \
  "s_cmp_eq_u32 s61, 0\n" \
  "s_cbranch_scc1 .LCX_NP_%=\n" \
  "v_mov_b32 v144, s59\n" \
  "v_mov_b32 v145, 1\n" \
  "s_mov_b64 exec, 1\n" \
  "ds_add_rtn_u32 v144, v144, v145\n" \
  "s_mov_b64 exec, -1\n" \
  "s_xor_b32 s59, s59, 4\n" \
  "s_cmp_lg_u32 s68, 0\n" \
  "s_cbranch_scc1 .LCX_TEST_%=\n" \
  "s_mov_b32 s61, 0\n" \
  "s_waitcnt lgkmcnt(0)\n" \
  "v_readfirstlane_b32 s63, v144\n" \
  "v_lshlrev_b32 v144, 5, v5\n" \
  "s_and_b32 s63, s63, 7\n" \
  "s_cmp_eq_u32 s63, 7\n" \
  "s_cbranch_scc0 .LCX_W2_%=\n" \
  "s_mov_b64 exec, 15\n" \
  "global_store_dword v144, v145, s[52:53] sc1\n" \
  "s_mov_b64 exec, -1\n" \
  ".LCX_W2_%=:\n" \
  "s_add_u32 s52, s52, 2048\n" \
  "s_addc_u32 s53, s53, 0\n" \
  ".LCX_NP_%=:\n" \
  "s_cmp_lg_u32 s68, 0\n" \
  "s_cbranch_scc1 .LCX_TEST_%=\n" \
  ".LCX_WF_%=:\n" \
  "s_memrealtime s[38:39]\n" \
  "s_waitcnt lgkmcnt(0)\n" \
  "s_sub_u32 s63, s69, s38\n" \
  "s_cmp_le_i32 s63, 0\n" \
  "s_cbranch_scc1 .LCX_GO_%=\n" \
  "s_cmp_lt_i32 s63, 12\n" \
  "s_cbranch_scc1 .LCX_WF_%=\n" \
  "s_sleep 3\n" \
  "s_branch .LCX_WF_%=\n" \
  ".LCX_GO_%=:\n" \
  "v_mov_b32 v35, 0\n" \
  "global_load_dwordx4 v[128:131], v5, s[48:49] offset:0 sc1\n" \
  "global_load_dwordx4 v[132:135], v5, s[48:49] offset:1024 sc1\n" \
  "global_load_dwordx4 v[136:139], v3, s[48:49] offset:0 sc1\n" \
  "global_load_dwordx4 v[140:143], v3, s[48:49] offset:512 sc1\n" \
  "global_load_dword v35, v35, s[50:51] sc1\n" \
  "s_sub_u32 s62, s62, 8\n" \
  "s_cbranch_scc1 .LCX_END_%=\n" \
  "s_waitcnt vmcnt(0)\n" \
  ".LCX_TEST_%=:\n" \
  "v_smfmac_f32_16x16x64_bf16 v[28:31], v[128:131], v[48:55], v1\n" \
  "v_smfmac_f32_16x16x64_bf16 v[28:31], v[132:135], v[56:63], v1\n" \
  "v_min3_u32 v33, v128, v129, v130\n" \
  "v_min3_u32 v36, v131, v132, v133\n" \
  "v_min3_u32 v33, v33, v134, v135\n" \
  "v_min3_u32 v36, v36, v136, v137\n" \
  "v_min3_u32 v33, v33, v138, v139\n" \
  "v_min3_u32 v36, v36, v140, v141\n" \
  "v_min3_u32 v33, v33, v142, v143\n" \
  "v_min_u32 v33, v33, v36\n" \
  "v_cmp_eq_u32 vcc, 0, v33\n" \
  "v_readfirstlane_b32 s64, v35\n" \
  "s_mov_b32 s68, 0\n" \
  "s_cmp_eq_u64 vcc, 0\n" \
  "s_cbranch_scc0 .LCX_FAIL_%=\n" \
  "v_permlane32_swap_b32 v28, v30\n" \
  "v_permlane32_swap_b32 v29, v31\n" \
  "v_mov_b32 v33, s67\n" \
  "ds_write_b32 v38, v33 offset:16\n" \
  "v_add_f32 v28, v28, v30\n" \
  "v_add_f32 v29, v29, v31\n" \
  "v_max3_f32 v36, v28, v29, v32\n" \
  "v_mov_b32 v33, v36\n" \
  "s_cmp_eq_u32 s64, 0\n" \
  "s_cselect_b32 s64, s38, s64\n" \
  "v_permlane16_swap_b32 v36, v33\n" \
  "v_max_f32 v36, v36, v33\n" \
  "v_mov_b32 v33, v36\n" \
  "s_sub_u32 s63, s64, s70\n" \
  "s_mov_b32 s70, s64\n" \
  "v_permlane32_swap_b32 v36, v33\n" \
  "v_max_f32 v36, v36, v33\n" \
  "v_mul_f32 v36, s72, v36\n" \
  "v_fma_f32 v32, v32, s71, v36\n" \
  "v_fma_f32 v28, v28, s71, v36\n" \
  "v_fma_f32 v29, v29, s71, v36\n" \
  "v_exp_f32 v32, v32\n" \
  "v_exp_f32 v28, v28\n" \
  "v_exp_f32 v29, v29\n" \
  "v_fma_f32 v34, v34, v32, v28\n" \
  "v_add_f32 v34, v34, v29\n" \
  "v_mov_b32 v33, v34\n" \
  "s_min_u32 s63, s63, 250\n" \
  "s_add_u32 s69, s64, s63\n" \
  "v_permlane16_swap_b32 v34, v33\n" \
  "v_add_f32 v34, v34, v33\n" \
  "v_mov_b32 v33, v34\n" \
  "s_add_u32 s69, s69, -48\n" \
  "s_add_u32 s48, s48, 0x10000\n" \
  "s_addc_u32 s49, s49, 0\n" \
  "v_permlane32_swap_b32 v34, v33\n" \
  "v_add_f32 v34, v34, v33\n" \
  "v_rcp_f32 v34, v34\n" \
  "s_add_u32 s50, s50, 1024\n" \
  "s_addc_u32 s51, s51, 0\n" \
  "v_mul_f32 v32, v32, v34\n" \
  "v_mul_f32 v20, v20, v32\n" \
  "v_mul_f32 v21, v21, v32\n" \
  "v_mul_f32 v22, v22, v32\n" \
  "v_mul_f32 v23, v23, v32\n" \
  "v_mul_f32 v24, v24, v32\n" \
  "v_mul_f32 v25, v25, v32\n" \
  "v_mul_f32 v26, v26, v32\n" \
  "v_mul_f32 v27, v27, v32\n" \
  "v_cvt_pk_bf16_f32 v40, v20, v21\n" \
  "v_cvt_pk_bf16_f32 v41, v22, v23\n" \
  "v_cvt_pk_bf16_f32 v42, v24, v25\n" \
  "v_cvt_pk_bf16_f32 v43, v26, v27\n" \
  "v_mul_f32 v28, v28, v34\n" \
  "v_mul_f32 v29, v29, v34\n" \
  "v_mfma_f32_16x16x32_bf16 v[64:67], v[96:99], v[40:43], 0\n" \
  "v_cvt_pk_bf16_f32 v44, v28, v29\n" \
  "v_mov_b32 v45, 0\n" \
  "v_mfma_f32_16x16x32_bf16 v[68:71], v[100:103], v[40:43], 0\n" \
  "v_cvt_pk_bf16_f32 v47, v28, v29\n" \
  "v_mov_b32 v46, 0\n" \
  "v_mfma_f32_16x16x32_bf16 v[72:75], v[104:107], v[40:43], 0\n" \
  "v_permlane32_swap_b32 v44, v45\n" \
  "v_mfma_f32_16x16x32_bf16 v[76:79], v[108:111], v[40:43], 0\n" \
  "v_permlane32_swap_b32 v46, v47\n" \
  "v_mfma_f32_16x16x32_bf16 v[80:83], v[112:115], v[40:43], 0\n" \
  "v_xor_b32 v16, 4096, v7\n" \
  "v_mfma_f32_16x16x32_bf16 v[84:87], v[116:119], v[40:43], 0\n" \
  "v_xor_b32 v17, 8192, v7\n" \
  "v_mfma_f32_16x16x32_bf16 v[88:91], v[120:123], v[40:43], 0\n" \
  "v_xor_b32 v18, 12288, v7\n" \
  "v_mfma_f32_16x16x32_bf16 v[92:95], v[124:127], v[40:43], 0\n" \
  "s_cmp_eq_u32 s61, 0\n" \
  "s_cbranch_scc1 .LCX_NQ_%=\n" \
  "s_mov_b32 s61, 0\n" \
  "s_waitcnt lgkmcnt(0)\n" \
  "v_readfirstlane_b32 s63, v144\n" \
  "v_lshlrev_b32 v144, 5, v5\n" \
  "s_and_b32 s63, s63, 7\n" \
  "s_cmp_eq_u32 s63, 7\n" \
  "s_cbranch_scc0 .LCX_W3_%=\n" \
  "s_mov_b64 exec, 15\n" \
  "global_store_dword v144, v145, s[52:53] sc1\n" \
  "s_mov_b64 exec, -1\n" \
  ".LCX_W3_%=:\n" \
  "s_add_u32 s52, s52, 2048\n" \
  "s_addc_u32 s53, s53, 0\n" \
  ".LCX_NQ_%=:\n" \
  "s_memrealtime s[38:39]\n" \
  "ds_read_b32 v37, v38\n" \
  "v_mfma_f32_16x16x16_bf16 v[64:67], v[136:137], v[44:45], v[64:67]\n" \
  "v_mfma_f32_16x16x16_bf16 v[68:71], v[140:141], v[44:45], v[68:71]\n" \
  "v_mfma_f32_16x16x16_bf16 v[72:75], v[136:137], v[46:47], v[72:75]\n" \
  "v_mfma_f32_16x16x16_bf16 v[76:79], v[140:141], v[46:47], v[76:79]\n" \
  "v_mfma_f32_16x16x16_bf16 v[80:83], v[138:139], v[44:45], v[80:83]\n" \
  "v_mfma_f32_16x16x16_bf16 v[84:87], v[142:143], v[44:45], v[84:87]\n" \
  "v_mfma_f32_16x16x16_bf16 v[88:91], v[138:139], v[46:47], v[88:91]\n" \
  "v_mfma_f32_16x16x16_bf16 v[92:95], v[142:143], v[46:47], v[92:95]\n" \
  "s_nop 0\n" \
  "v_cvt_pk_bf16_f32 v160, v64, v65\n" \
  "v_cvt_pk_bf16_f32 v161, v66, v67\n" \
  "v_cvt_pk_bf16_f32 v162, v68, v69\n" \
  "v_cvt_pk_bf16_f32 v163, v70, v71\n" \
  "ds_write_b128 v7, v[160:163] offset:0\n" \
  "v_cvt_pk_bf16_f32 v164, v72, v73\n" \
  "v_cvt_pk_bf16_f32 v165, v74, v75\n" \
  "v_cvt_pk_bf16_f32 v166, v76, v77\n" \
  "v_cvt_pk_bf16_f32 v167, v78, v79\n" \
  "ds_write_b128 v7, v[164:167] offset:1024\n" \
  "v_cvt_pk_bf16_f32 v168, v80, v81\n" \
  "v_cvt_pk_bf16_f32 v169, v82, v83\n" \
  "v_cvt_pk_bf16_f32 v170, v84, v85\n" \
  "v_cvt_pk_bf16_f32 v171, v86, v87\n" \
  "ds_write_b128 v7, v[168:171] offset:2048\n" \
  "v_cvt_pk_bf16_f32 v172, v88, v89\n" \
  "v_cvt_pk_bf16_f32 v173, v90, v91\n" \
  "v_cvt_pk_bf16_f32 v174, v92, v93\n" \
  "v_cvt_pk_bf16_f32 v175, v94, v95\n" \
  "ds_write_b128 v7, v[172:175] offset:3072\n" \
  "v_xor_b32 v7, 0x4000, v7\n" \
  "v_mfma_f32_16x16x32_bf16 v[112:115], v[192:195], v[160:163], 0\n" \
  "v_mfma_f32_16x16x32_bf16 v[116:119], v[224:227], v[160:163], 0\n" \
  "s_waitcnt lgkmcnt(0)\n" \
  "s_barrier\n" \
  "ds_read_b128 v[64:67], v16 offset:0\n" \
  "ds_read_b128 v[68:71], v16 offset:1024\n" \
  "ds_read_b128 v[72:75], v16 offset:2048\n" \
  "ds_read_b128 v[76:79], v16 offset:3072\n" \
  "ds_read_b128 v[80:83], v17 offset:0\n" \
  "ds_read_b128 v[84:87], v17 offset:1024\n" \
  "ds_read_b128 v[88:91], v17 offset:2048\n" \
  "ds_read_b128 v[92:95], v17 offset:3072\n" \
  "ds_read_b128 v[96:99], v18 offset:0\n" \
  "ds_read_b128 v[100:103], v18 offset:1024\n" \
  "ds_read_b128 v[104:107], v18 offset:2048\n" \
  "ds_read_b128 v[108:111], v18 offset:3072\n" \
  "v_readfirstlane_b32 s73, v37\n" \
  "s_sub_u32 s63, s38, s69\n" \
  "s_bfe_u32 s64, s73, 0x80010\n" \
  "s_sub_u32 s64, s64, s67\n" \
  "s_or_b32 s63, s63, s64\n" \
  "s_bfe_u32 s64, s73, 0x80008\n" \
  "s_and_b32 s73, s73, 0xff\n" \
  "s_min_u32 s73, s73, s64\n" \
  "s_or_b32 s63, s63, s68\n" \
  "s_cmp_lt_i32 s63, 0\n" \
  "s_cbranch_scc1 .LCX_E4_%=\n" \
  "v_mov_b32 v35, 0\n" \
  "global_load_dwordx4 v[128:131], v5, s[48:49] offset:0 sc1\n" \
  "global_load_dwordx4 v[132:135], v5, s[48:49] offset:1024 sc1\n" \
  "global_load_dwordx4 v[136:139], v3, s[48:49] offset:0 sc1\n" \
  "global_load_dwordx4 v[140:143], v3, s[48:49] offset:512 sc1\n" \
  "global_load_dword v35, v35, s[50:51] sc1\n" \
  "v_mov_b32 v33, s67\n" \
  "ds_write_b32 v38, v33 offset:8\n" \
  "s_mov_b32 s68, -1\n" \
  ".LCX_E4_%=:\n" \
  "v_mfma_f32_16x16x32_bf16 v[112:115], v[196:199], v[164:167], v[112:115]\n" \
  "v_mfma_f32_16x16x32_bf16 v[116:119], v[228:231], v[164:167], v[116:119]\n" \
  "v_mfma_f32_16x16x32_bf16 v[112:115], v[200:203], v[168:171], v[112:115]\n" \
  "v_mfma_f32_16x16x32_bf16 v[116:119], v[232:235], v[168:171], v[116:119]\n" \
  "v_mfma_f32_16x16x32_bf16 v[112:115], v[204:207], v[172:175], v[112:115]\n" \
  "v_mfma_f32_16x16x32_bf16 v[116:119], v[236:239], v[172:175], v[116:119]\n" \
  "s_waitcnt lgkmcnt(11)\n" \
  "v_mfma_f32_16x16x32_bf16 v[120:123], v[192:195], v[64:67], 0\n" \
  "v_mfma_f32_16x16x32_bf16 v[124:127], v[224:227], v[64:67], 0\n" \
  "s_waitcnt lgkmcnt(10)\n" \
  "v_mfma_f32_16x16x32_bf16 v[120:123], v[196:199], v[68:71], v[120:123]\n" \
  "v_mfma_f32_16x16x32_bf16 v[124:127], v[228:231], v[68:71], v[124:127]\n" \
  "s_waitcnt lgkmcnt(9)\n" \
  "v_mfma_f32_16x16x32_bf16 v[120:123], v[200:203], v[72:75], v[120:123]\n" \
  "v_mfma_f32_16x16x32_bf16 v[124:127], v[232:235], v[72:75], v[124:127]\n" \
  "s_waitcnt lgkmcnt(8)\n" \
  "v_mfma_f32_16x16x32_bf16 v[120:123], v[204:207], v[76:79], v[120:123]\n" \
  "v_mfma_f32_16x16x32_bf16 v[124:127], v[236:239], v[76:79], v[124:127]\n" \
  "s_waitcnt lgkmcnt(7)\n" \
  "v_mfma_f32_16x16x32_bf16 v[112:115], v[208:211], v[80:83], v[112:115]\n" \
  "v_mfma_f32_16x16x32_bf16 v[116:119], v[240:243], v[80:83], v[116:119]\n" \
  "s_waitcnt lgkmcnt(6)\n" \
  "v_mfma_f32_16x16x32_bf16 v[112:115], v[212:215], v[84:87], v[112:115]\n" \
  "v_mfma_f32_16x16x32_bf16 v[116:119], v[244:247], v[84:87], v[116:119]\n" \
  "s_waitcnt lgkmcnt(5)\n" \
  "v_mfma_f32_16x16x32_bf16 v[112:115], v[216:219], v[88:91], v[112:115]\n" \
  "v_mfma_f32_16x16x32_bf16 v[116:119], v[248:251], v[88:91], v[116:119]\n" \
  "s_waitcnt lgkmcnt(4)\n" \
  "v_mfma_f32_16x16x32_bf16 v[112:115], v[220:223], v[92:95], v[112:115]\n" \
  "v_mfma_f32_16x16x32_bf16 v[116:119], v[252:255], v[92:95], v[116:119]\n" \
  "s_memrealtime s[38:39]\n" \
  "ds_read_b32 v37, v38\n" \
  "s_waitcnt lgkmcnt(4)\n" \
  "v_mfma_f32_16x16x32_bf16 v[120:123], v[208:211], v[96:99], v[120:123]\n" \
  "v_mfma_f32_16x16x32_bf16 v[124:127], v[240:243], v[96:99], v[124:127]\n" \
  "s_waitcnt lgkmcnt(3)\n" \
  "v_mfma_f32_16x16x32_bf16 v[120:123], v[212:215], v[100:103], v[120:123]\n" \
  "v_mfma_f32_16x16x32_bf16 v[124:127], v[244:247], v[100:103], v[124:127]\n" \
  "v_cvt_pk_bf16_f32 v20, v112, v113\n" \
  "v_cvt_pk_bf16_f32 v21, v114, v115\n" \
  "v_cvt_pk_bf16_f32 v22, v116, v117\n" \
  "v_cvt_pk_bf16_f32 v23, v118, v119\n" \
  "s_waitcnt lgkmcnt(2)\n" \
  "v_mfma_f32_16x16x32_bf16 v[120:123], v[216:219], v[104:107], v[120:123]\n" \
  "v_mfma_f32_16x16x32_bf16 v[124:127], v[248:251], v[104:107], v[124:127]\n" \
  "buffer_store_dwordx4 v[20:23], v4, s[40:43], s54 offen sc1\n" \
  "s_waitcnt lgkmcnt(0)\n" \
  "v_mfma_f32_16x16x32_bf16 v[120:123], v[220:223], v[108:111], v[120:123]\n" \
  "v_mfma_f32_16x16x32_bf16 v[124:127], v[252:255], v[108:111], v[124:127]\n" \
  "s_xor_b32 s64, s54, 16384\n" \
  "s_mov_b32 s61, 1\n" \
  "s_nop 3\n" \
  "v_cvt_pk_bf16_f32 v24, v120, v121\n" \
  "v_cvt_pk_bf16_f32 v25, v122, v123\n" \
  "v_cvt_pk_bf16_f32 v26, v124, v125\n" \
  "v_cvt_pk_bf16_f32 v27, v126, v127\n" \
  "buffer_store_dwordx4 v[24:27], v4, s[40:43], s64 offen sc1\n" \
  "v_readfirstlane_b32 s73, v37\n" \
  "s_sub_u32 s63, s38, s69\n" \
  "s_bfe_u32 s64, s73, 0x80010\n" \
  "s_sub_u32 s64, s64, s67\n" \
  "s_or_b32 s63, s63, s64\n" \
  "s_bfe_u32 s64, s73, 0x80008\n" \
  "s_and_b32 s73, s73, 0xff\n" \
  "s_min_u32 s73, s73, s64\n" \
  "s_or_b32 s63, s63, s68\n" \
  "s_cmp_lt_i32 s63, 0\n" \
  "s_cbranch_scc1 .LCX_E5_%=\n" \
  "v_mov_b32 v35, 0\n" \
  "global_load_dwordx4 v[128:131], v5, s[48:49] offset:0 sc1\n" \
  "global_load_dwordx4 v[132:135], v5, s[48:49] offset:1024 sc1\n" \
  "global_load_dwordx4 v[136:139], v3, s[48:49] offset:0 sc1\n" \
  "global_load_dwordx4 v[140:143], v3, s[48:49] offset:512 sc1\n" \
  "global_load_dword v35, v35, s[50:51] sc1\n" \
  "v_mov_b32 v33, s67\n" \
  "ds_write_b32 v38, v33 offset:8\n" \
  "s_mov_b32 s68, -1\n" \
  ".LCX_E5_%=:\n" \
  "s_add_u32 s54, s54, 0x80000\n" \
  "s_sub_u32 s60, s60, 1\n" \
  "s_cmp_lg_u32 s60, 0\n" \
  "s_cbranch_scc1 .LCX_TOP_%=\n" \
  "s_waitcnt vmcnt(0)\n" \
  "v_mov_b32 v144, s59\n" \
  "v_mov_b32 v145, 1\n" \
  "s_mov_b64 exec, 1\n" \
  "ds_add_rtn_u32 v144, v144, v145\n" \
  "s_mov_b64 exec, -1\n" \
  "s_xor_b32 s59, s59, 4\n" \
  "s_mov_b32 s61, 0\n" \
  "s_waitcnt lgkmcnt(0)\n" \
  "v_readfirstlane_b32 s63, v144\n" \
  "v_lshlrev_b32 v144, 5, v5\n" \
  "s_and_b32 s63, s63, 7\n" \
  "s_cmp_eq_u32 s63, 7\n" \
  "s_cbranch_scc0 .LCX_W6_%=\n" \
  "s_mov_b64 exec, 15\n" \
  "global_store_dword v144, v145, s[52:53] sc1\n" \
  "s_mov_b64 exec, -1\n" \
  ".LCX_W6_%=:\n" \
  "s_add_u32 s52, s52, 2048\n" \
  "s_addc_u32 s53, s53, 0\n" \
  ".LCX_END_%=:\n" \
  "s_endpgm\n" \
  ".LCX_FAIL_%=:\n" \
  "v_mov_b64 v[28:29], 0\n" \
  "v_mov_b64 v[30:31], 0\n" \
  "s_cmp_eq_u32 s61, 0\n" \
  "s_cbranch_scc1 .LCX_FAIL2_%=\n" \
  "s_mov_b32 s61, 0\n" \
  "s_waitcnt lgkmcnt(0)\n" \
  "v_readfirstlane_b32 s63, v144\n" \
  "v_lshlrev_b32 v144, 5, v5\n" \
  "s_and_b32 s63, s63, 7\n" \
  "s_cmp_eq_u32 s63, 7\n" \
  "s_cbranch_scc0 .LCX_W7_%=\n" \
  "s_mov_b64 exec, 15\n" \
  "global_store_dword v144, v145, s[52:53] sc1\n" \
  "s_mov_b64 exec, -1\n" \
  ".LCX_W7_%=:\n" \
  "s_add_u32 s52, s52, 2048\n" \
  "s_addc_u32 s53, s53, 0\n" \
  ".LCX_FAIL2_%=:\n" \
  "s_add_u32 s69, s64, -48\n" \
  "s_cmp_lg_u32 s64, 0\n" \
  "s_cbranch_scc1 .LCX_WF_%=\n" \
  "s_sleep 4\n" \
  "global_load_dword v35, v35, s[50:51] sc1\n" \
  "s_sub_u32 s62, s62, 8\n" \
  "s_cbranch_scc1 .LCX_END_%=\n" \
  "s_waitcnt vmcnt(0)\n" \
  "v_readfirstlane_b32 s64, v35\n" \
  "s_branch .LCX_FAIL2_%=\n"
#define AW_ASM_Y \
  "s_mov_b32 s72, 0\n" \
  "s_mov_b32 s67, 0\n" \
  "s_mov_b32 s68, 2\n" \
  "s_mov_b32 s69, 0\n" \
  "s_mov_b32 s65, 49152\n" \
  "s_mov_b32 s66, 16384\n" \
  "v_mov_b32 v5, s58\n" \
  "v_mov_b32 v60, v3\n" \
  "v_add_u32 v3, 4096, v2\n" \
  "s_mov_b32 s62, 0x40000\n" \
  "s_branch .LCY_ASK_%=\n" \
  ".p2align 6\n" \
  ".LCY_BAR_%=:\n" \
  "s_barrier\n" \
  "ds_read_b128 v[8:11], v0 offset:0\n" \
  "ds_read_b128 v[12:15], v0 offset:1024\n" \
  "ds_read_b128 v[16:19], v0 offset:2048\n" \
  "s_add_u32 s68, s68, 1\n" \
  "s_add_u32 s64, s54, 16384\n" \
  "s_cmp_eq_u32 s69, 0\n" \
  "s_cbranch_scc1 .LCY_NI_%=\n" \
  "s_waitcnt vmcnt(3)\n" \
  "s_cmp_lg_u32 s72, 0\n" \
  "s_cbranch_scc1 .LCY_NJ_%=\n" \
  "s_waitcnt vmcnt(0)\n" \
  ".LCY_NJ_%=:\n" \
  "v_mov_b32 v6, s55\n" \
  "s_mov_b64 exec, 1\n" \
  "ds_add_u32 v5, v6\n" \
  "s_mov_b64 exec, -1\n" \
  "s_mov_b32 s69, 0\n" \
  ".LCY_NI_%=:\n" \
  "v_cmp_eq_u32 vcc, 0, v4\n" \
  "s_cmp_eq_u64 vcc, 0\n" \
  "s_cbranch_scc0 .LCY_NS_%=\n" \
  "v_lshlrev_b32 v6, 16, s55\n" \
  "s_mov_b64 exec, 1\n" \
  "ds_add_u32 v5, v6\n" \
  "s_mov_b64 exec, -1\n" \
  "s_mov_b32 m0, s56\n" \
  "s_add_u32 s71, s56, 4096\n" \
  "global_load_lds_dwordx4 v2, s[48:49] offset:0 sc1\n" \
  "global_load_lds_dwordx4 v2, s[48:49] offset:1024 sc1\n" \
  "global_load_lds_dwordx4 v2, s[48:49] offset:2048 sc1\n" \
  "global_load_lds_dwordx4 v2, s[48:49] offset:3072 sc1\n" \
  "s_mov_b32 m0, s71\n" \
  "s_add_u32 s56, s56, s65\n" \
  "global_load_lds_dwordx4 v3, s[48:49] offset:0 sc1\n" \
  "global_load_lds_dwordx4 v3, s[48:49] offset:1024 sc1\n" \
  ".LCY_NS_%=:\n" \
  "s_cmp_eq_u32 s72, 0\n" \
  "s_cbranch_scc1 .LCY_NK_%=\n" \
  "s_waitcnt vmcnt(6)\n" \
  "s_mov_b32 s72, 0\n" \
  "s_cbranch_vccz .LCY_NL_%=\n" \
  "s_waitcnt vmcnt(0)\n" \
  ".LCY_NL_%=:\n" \
  "v_mov_b32 v6, s59\n" \
  "v_mov_b32 v7, 1\n" \
  "s_mov_b64 exec, 1\n" \
  "ds_add_rtn_u32 v6, v6, v7\n" \
  "s_mov_b64 exec, -1\n" \
  "s_xor_b32 s59, s59, 4\n" \
  "s_mov_b32 s61, 0\n" \
  "s_waitcnt lgkmcnt(0)\n" \
  "v_readfirstlane_b32 s63, v6\n" \
  "v_lshlrev_b32 v6, 5, v2\n" \
  "s_and_b32 s63, s63, 7\n" \
  "s_cmp_eq_u32 s63, 7\n" \
  "s_cbranch_scc0 .LCY_W1_%=\n" \
  "s_mov_b64 exec, 15\n" \
  "global_store_dword v6, v7, s[52:53] sc1\n" \
  "s_mov_b64 exec, -1\n" \
  ".LCY_W1_%=:\n" \
  "s_add_u32 s52, s52, 2048\n" \
  "s_addc_u32 s53, s53, 0\n" \
  ".LCY_NK_%=:\n" \
  "ds_read_b128 v[20:23], v0 offset:3072\n" \
  "s_waitcnt lgkmcnt(3)\n" \
  "v_mfma_f32_16x16x32_bf16 v[32:35], v[64:67], v[8:11], 0\n" \
  "v_mfma_f32_16x16x32_bf16 v[36:39], v[96:99], v[8:11], 0\n" \
  "v_mfma_f32_16x16x32_bf16 v[40:43], v[128:131], v[8:11], 0\n" \
  "v_mfma_f32_16x16x32_bf16 v[44:47], v[160:163], v[8:11], 0\n" \
  "v_mfma_f32_16x16x32_bf16 v[48:51], v[192:195], v[8:11], 0\n" \
  "v_mfma_f32_16x16x32_bf16 v[52:55], v[224:227], v[8:11], 0\n" \
  "ds_read_b128 v[8:11], v0 offset:8192\n" \
  "s_waitcnt lgkmcnt(3)\n" \
  "v_mfma_f32_16x16x32_bf16 v[32:35], v[68:71], v[12:15], v[32:35]\n" \
  "v_mfma_f32_16x16x32_bf16 v[36:39], v[100:103], v[12:15], v[36:39]\n" \
  "v_mfma_f32_16x16x32_bf16 v[40:43], v[132:135], v[12:15], v[40:43]\n" \
  "v_mfma_f32_16x16x32_bf16 v[44:47], v[164:167], v[12:15], v[44:47]\n" \
  "v_mfma_f32_16x16x32_bf16 v[48:51], v[196:199], v[12:15], v[48:51]\n" \
  "v_mfma_f32_16x16x32_bf16 v[52:55], v[228:231], v[12:15], v[52:55]\n" \
  "s_cbranch_vccnz .LCY_D2_%=\n" \
  "global_load_lds_dwordx4 v3, s[48:49] offset:2048 sc1\n" \
  ".LCY_D2_%=:\n" \
  "ds_read_b128 v[12:15], v0 offset:9216\n" \
  "s_waitcnt lgkmcnt(3)\n" \
  "v_mfma_f32_16x16x32_bf16 v[32:35], v[72:75], v[16:19], v[32:35]\n" \
  "v_mfma_f32_16x16x32_bf16 v[36:39], v[104:107], v[16:19], v[36:39]\n" \
  "v_mfma_f32_16x16x32_bf16 v[40:43], v[136:139], v[16:19], v[40:43]\n" \
  "v_mfma_f32_16x16x32_bf16 v[44:47], v[168:171], v[16:19], v[44:47]\n" \
  "v_mfma_f32_16x16x32_bf16 v[48:51], v[200:203], v[16:19], v[48:51]\n" \
  "v_mfma_f32_16x16x32_bf16 v[52:55], v[232:235], v[16:19], v[52:55]\n" \
  "s_cbranch_vccnz .LCY_D3_%=\n" \
  "global_load_lds_dwordx4 v3, s[48:49] offset:3072 sc1\n" \
  "s_add_u32 s48, s48, 0x80000\n" \
  "s_addc_u32 s49, s49, 0\n" \
  ".LCY_D3_%=:\n" \
  "ds_read_b128 v[16:19], v0 offset:10240\n" \
  "s_waitcnt lgkmcnt(3)\n" \
  "v_mfma_f32_16x16x32_bf16 v[32:35], v[76:79], v[20:23], v[32:35]\n" \
  "v_mfma_f32_16x16x32_bf16 v[36:39], v[108:111], v[20:23], v[36:39]\n" \
  "v_mfma_f32_16x16x32_bf16 v[40:43], v[140:143], v[20:23], v[40:43]\n" \
  "v_mfma_f32_16x16x32_bf16 v[44:47], v[172:175], v[20:23], v[44:47]\n" \
  "v_mfma_f32_16x16x32_bf16 v[48:51], v[204:207], v[20:23], v[48:51]\n" \
  "v_mfma_f32_16x16x32_bf16 v[52:55], v[236:239], v[20:23], v[52:55]\n" \
  "s_cbranch_vccnz .LCY_D4_%=\n" \
  "s_mov_b32 m0, s57\n" \
  "s_add_u32 s57, s57, s65\n" \
  "global_load_lds_dwordx4 v2, s[50:51] offset:0 sc1\n" \
  ".LCY_D4_%=:\n" \
  "ds_read_b128 v[20:23], v0 offset:11264\n" \
  "s_waitcnt lgkmcnt(3)\n" \
  "v_mfma_f32_16x16x32_bf16 v[32:35], v[80:83], v[8:11], v[32:35]\n" \
  "v_mfma_f32_16x16x32_bf16 v[36:39], v[112:115], v[8:11], v[36:39]\n" \
  "v_mfma_f32_16x16x32_bf16 v[40:43], v[144:147], v[8:11], v[40:43]\n" \
  "v_mfma_f32_16x16x32_bf16 v[44:47], v[176:179], v[8:11], v[44:47]\n" \
  "v_mfma_f32_16x16x32_bf16 v[48:51], v[208:211], v[8:11], v[48:51]\n" \
  "v_mfma_f32_16x16x32_bf16 v[52:55], v[240:243], v[8:11], v[52:55]\n" \
  "s_cbranch_vccnz .LCY_D5_%=\n" \
  "global_load_lds_dwordx4 v2, s[50:51] offset:1024 sc1\n" \
  "s_sub_u32 s65, 0, s65\n" \
  "s_add_u32 s67, s67, 4096\n" \
  ".LCY_D5_%=:\n" \
  "ds_read_b128 v[8:11], v0 offset:4096\n" \
  "s_waitcnt lgkmcnt(3)\n" \
  "v_mfma_f32_16x16x32_bf16 v[32:35], v[84:87], v[12:15], v[32:35]\n" \
  "v_mfma_f32_16x16x32_bf16 v[36:39], v[116:119], v[12:15], v[36:39]\n" \
  "v_mfma_f32_16x16x32_bf16 v[40:43], v[148:151], v[12:15], v[40:43]\n" \
  "v_mfma_f32_16x16x32_bf16 v[44:47], v[180:183], v[12:15], v[44:47]\n" \
  "v_mfma_f32_16x16x32_bf16 v[48:51], v[212:215], v[12:15], v[48:51]\n" \
  "v_mfma_f32_16x16x32_bf16 v[52:55], v[244:247], v[12:15], v[52:55]\n" \
  "s_cbranch_vccnz .LCY_D6_%=\n" \
  "global_load_lds_dwordx4 v2, s[50:51] offset:2048 sc1\n" \
  "s_sub_u32 s68, s68, 1\n" \
  "s_mov_b32 s69, 1\n" \
  ".LCY_D6_%=:\n" \
  "ds_read_b128 v[12:15], v0 offset:5120\n" \
  "s_waitcnt lgkmcnt(3)\n" \
  "v_mfma_f32_16x16x32_bf16 v[32:35], v[88:91], v[16:19], v[32:35]\n" \
  "v_mfma_f32_16x16x32_bf16 v[36:39], v[120:123], v[16:19], v[36:39]\n" \
  "v_mfma_f32_16x16x32_bf16 v[40:43], v[152:155], v[16:19], v[40:43]\n" \
  "v_mfma_f32_16x16x32_bf16 v[44:47], v[184:187], v[16:19], v[44:47]\n" \
  "v_mfma_f32_16x16x32_bf16 v[48:51], v[216:219], v[16:19], v[48:51]\n" \
  "v_mfma_f32_16x16x32_bf16 v[52:55], v[248:251], v[16:19], v[52:55]\n" \
  "s_cbranch_vccnz .LCY_D7_%=\n" \
  "global_load_lds_dwordx4 v2, s[50:51] offset:3072 sc1\n" \
  "s_add_u32 s50, s50, 0x40000\n" \
  "s_addc_u32 s51, s51, 0\n" \
  ".LCY_D7_%=:\n" \
  "ds_read_b128 v[16:19], v0 offset:6144\n" \
  "s_waitcnt lgkmcnt(3)\n" \
  "v_mfma_f32_16x16x32_bf16 v[32:35], v[92:95], v[20:23], v[32:35]\n" \
  "v_mfma_f32_16x16x32_bf16 v[36:39], v[124:127], v[20:23], v[36:39]\n" \
  "v_mfma_f32_16x16x32_bf16 v[40:43], v[156:159], v[20:23], v[40:43]\n" \
  "v_mfma_f32_16x16x32_bf16 v[44:47], v[188:191], v[20:23], v[44:47]\n" \
  "v_mfma_f32_16x16x32_bf16 v[48:51], v[220:223], v[20:23], v[48:51]\n" \
  "v_mfma_f32_16x16x32_bf16 v[52:55], v[252:255], v[20:23], v[52:55]\n" \
  "ds_read_b128 v[20:23], v0 offset:7168\n" \
  "s_waitcnt lgkmcnt(3)\n" \
  "s_nop 1\n" \
  "v_cvt_pk_bf16_f32 v24, v32, v33\n" \
  "v_cvt_pk_bf16_f32 v25, v34, v35\n" \
  "v_cvt_pk_bf16_f32 v26, v36, v37\n" \
  "v_cvt_pk_bf16_f32 v27, v38, v39\n" \
  "s_nop 0\n" \
  "v_mfma_f32_16x16x32_bf16 v[32:35], v[64:67], v[8:11], 0\n" \
  "v_mfma_f32_16x16x32_bf16 v[36:39], v[96:99], v[8:11], 0\n" \
  "buffer_store_dwordx4 v[24:27], v1, s[40:43], s54 offen offset:0 sc1\n" \
  "v_cvt_pk_bf16_f32 v28, v40, v41\n" \
  "v_cvt_pk_bf16_f32 v29, v42, v43\n" \
  "v_cvt_pk_bf16_f32 v30, v44, v45\n" \
  "v_cvt_pk_bf16_f32 v31, v46, v47\n" \
  "s_nop 0\n" \
  "v_mfma_f32_16x16x32_bf16 v[40:43], v[128:131], v[8:11], 0\n" \
  "v_mfma_f32_16x16x32_bf16 v[44:47], v[160:163], v[8:11], 0\n" \
  "buffer_store_dwordx4 v[28:31], v1, s[40:43], s54 offen offset:128 sc1\n" \
  "v_cvt_pk_bf16_f32 v56, v48, v49\n" \
  "v_cvt_pk_bf16_f32 v57, v50, v51\n" \
  "v_cvt_pk_bf16_f32 v58, v52, v53\n" \
  "v_cvt_pk_bf16_f32 v59, v54, v55\n" \
  "s_nop 0\n" \
  "v_mfma_f32_16x16x32_bf16 v[48:51], v[192:195], v[8:11], 0\n" \
  "v_mfma_f32_16x16x32_bf16 v[52:55], v[224:227], v[8:11], 0\n" \
  "buffer_store_dwordx4 v[56:59], v1, s[40:43], s54 offen offset:256 sc1\n" \
  "ds_read_b128 v[8:11], v0 offset:12288\n" \
  "s_waitcnt lgkmcnt(3)\n" \
  "v_mfma_f32_16x16x32_bf16 v[32:35], v[68:71], v[12:15], v[32:35]\n" \
  "v_mfma_f32_16x16x32_bf16 v[36:39], v[100:103], v[12:15], v[36:39]\n" \
  "v_mfma_f32_16x16x32_bf16 v[40:43], v[132:135], v[12:15], v[40:43]\n" \
  "v_mfma_f32_16x16x32_bf16 v[44:47], v[164:167], v[12:15], v[44:47]\n" \
  "v_mfma_f32_16x16x32_bf16 v[48:51], v[196:199], v[12:15], v[48:51]\n" \
  "v_mfma_f32_16x16x32_bf16 v[52:55], v[228:231], v[12:15], v[52:55]\n" \
  "ds_read_b128 v[12:15], v0 offset:13312\n" \
  "s_waitcnt lgkmcnt(3)\n" \
  "v_mfma_f32_16x16x32_bf16 v[32:35], v[72:75], v[16:19], v[32:35]\n" \
  "v_mfma_f32_16x16x32_bf16 v[36:39], v[104:107], v[16:19], v[36:39]\n" \
  "v_mfma_f32_16x16x32_bf16 v[40:43], v[136:139], v[16:19], v[40:43]\n" \
  "v_mfma_f32_16x16x32_bf16 v[44:47], v[168:171], v[16:19], v[44:47]\n" \
  "v_mfma_f32_16x16x32_bf16 v[48:51], v[200:203], v[16:19], v[48:51]\n" \
  "v_mfma_f32_16x16x32_bf16 v[52:55], v[232:235], v[16:19], v[52:55]\n" \
  "ds_read_b128 v[16:19], v0 offset:14336\n" \
  "s_waitcnt lgkmcnt(3)\n" \
  "v_mfma_f32_16x16x32_bf16 v[32:35], v[76:79], v[20:23], v[32:35]\n" \
  "v_mfma_f32_16x16x32_bf16 v[36:39], v[108:111], v[20:23], v[36:39]\n" \
  "v_mfma_f32_16x16x32_bf16 v[40:43], v[140:143], v[20:23], v[40:43]\n" \
  "v_mfma_f32_16x16x32_bf16 v[44:47], v[172:175], v[20:23], v[44:47]\n" \
  "v_mfma_f32_16x16x32_bf16 v[48:51], v[204:207], v[20:23], v[48:51]\n" \
  "v_mfma_f32_16x16x32_bf16 v[52:55], v[236:239], v[20:23], v[52:55]\n" \
  "ds_read_b32 v7, v5 offset:8\n" \
  "ds_read_b128 v[20:23], v0 offset:15360\n" \
  "s_waitcnt lgkmcnt(4)\n" \
  "v_mfma_f32_16x16x32_bf16 v[32:35], v[80:83], v[8:11], v[32:35]\n" \
  "v_mfma_f32_16x16x32_bf16 v[36:39], v[112:115], v[8:11], v[36:39]\n" \
  "v_mfma_f32_16x16x32_bf16 v[40:43], v[144:147], v[8:11], v[40:43]\n" \
  "v_mfma_f32_16x16x32_bf16 v[44:47], v[176:179], v[8:11], v[44:47]\n" \
  "v_mfma_f32_16x16x32_bf16 v[48:51], v[208:211], v[8:11], v[48:51]\n" \
  "v_mfma_f32_16x16x32_bf16 v[52:55], v[240:243], v[8:11], v[52:55]\n" \
  "s_waitcnt lgkmcnt(3)\n" \
  "v_mfma_f32_16x16x32_bf16 v[32:35], v[84:87], v[12:15], v[32:35]\n" \
  "v_mfma_f32_16x16x32_bf16 v[36:39], v[116:119], v[12:15], v[36:39]\n" \
  "v_mfma_f32_16x16x32_bf16 v[40:43], v[148:151], v[12:15], v[40:43]\n" \
  "v_mfma_f32_16x16x32_bf16 v[44:47], v[180:183], v[12:15], v[44:47]\n" \
  "v_mfma_f32_16x16x32_bf16 v[48:51], v[212:215], v[12:15], v[48:51]\n" \
  "v_mfma_f32_16x16x32_bf16 v[52:55], v[244:247], v[12:15], v[52:55]\n" \
  "s_waitcnt lgkmcnt(2)\n" \
  "v_mfma_f32_16x16x32_bf16 v[32:35], v[88:91], v[16:19], v[32:35]\n" \
  "v_mfma_f32_16x16x32_bf16 v[36:39], v[120:123], v[16:19], v[36:39]\n" \
  "v_mfma_f32_16x16x32_bf16 v[40:43], v[152:155], v[16:19], v[40:43]\n" \
  "v_mfma_f32_16x16x32_bf16 v[44:47], v[184:187], v[16:19], v[44:47]\n" \
  "v_mfma_f32_16x16x32_bf16 v[48:51], v[216:219], v[16:19], v[48:51]\n" \
  "v_mfma_f32_16x16x32_bf16 v[52:55], v[248:251], v[16:19], v[52:55]\n" \
  "s_cbranch_vccnz .LCY_NT_%=\n" \
  "v_mov_b32 v4, 0\n" \
  "buffer_load_dword v4, v60, s[44:47], s67 offen sc1\n" \
  ".LCY_NT_%=:\n" \
  "s_waitcnt lgkmcnt(0)\n" \
  "v_mfma_f32_16x16x32_bf16 v[32:35], v[92:95], v[20:23], v[32:35]\n" \
  "v_mfma_f32_16x16x32_bf16 v[36:39], v[124:127], v[20:23], v[36:39]\n" \
  "v_mfma_f32_16x16x32_bf16 v[40:43], v[156:159], v[20:23], v[40:43]\n" \
  "v_mfma_f32_16x16x32_bf16 v[44:47], v[188:191], v[20:23], v[44:47]\n" \
  "v_mfma_f32_16x16x32_bf16 v[48:51], v[220:223], v[20:23], v[48:51]\n" \
  "v_mfma_f32_16x16x32_bf16 v[52:55], v[252:255], v[20:23], v[52:55]\n" \
  "v_add_u32 v0, s66, v0\n" \
  "s_sub_u32 s66, 0, s66\n" \
  "s_add_u32 s54, s54, 0x80000\n" \
  "s_mov_b32 s62, 0x40000\n" \
  "v_readfirstlane_b32 s63, v7\n" \
  "v_cvt_pk_bf16_f32 v24, v32, v33\n" \
  "v_cvt_pk_bf16_f32 v25, v34, v35\n" \
  "v_cvt_pk_bf16_f32 v26, v36, v37\n" \
  "v_cvt_pk_bf16_f32 v27, v38, v39\n" \
  "buffer_store_dwordx4 v[24:27], v1, s[40:43], s64 offen offset:0 sc1\n" \
  "v_cvt_pk_bf16_f32 v28, v40, v41\n" \
  "v_cvt_pk_bf16_f32 v29, v42, v43\n" \
  "v_cvt_pk_bf16_f32 v30, v44, v45\n" \
  "v_cvt_pk_bf16_f32 v31, v46, v47\n" \
  "buffer_store_dwordx4 v[28:31], v1, s[40:43], s64 offen offset:128 sc1\n" \
  "v_cvt_pk_bf16_f32 v56, v48, v49\n" \
  "v_cvt_pk_bf16_f32 v57, v50, v51\n" \
  "v_cvt_pk_bf16_f32 v58, v52, v53\n" \
  "v_cvt_pk_bf16_f32 v59, v54, v55\n" \
  "buffer_store_dwordx4 v[56:59], v1, s[40:43], s64 offen offset:256 sc1\n" \
  "s_cbranch_vccnz .LCY_ND_%=\n" \
  "s_add_u32 s63, s63, s60\n" \
  "s_cmp_eq_u32 s63, 66\n" \
  "s_cselect_b32 s72, 1, 0\n" \
  "s_cbranch_scc1 .LCY_DEF_%=\n" \
  "s_waitcnt vmcnt(0)\n" \
  "v_mov_b32 v6, s55\n" \
  "s_mov_b64 exec, 1\n" \
  "ds_add_u32 v5, v6\n" \
  "s_mov_b64 exec, -1\n" \
  "s_mov_b32 s69, 0\n" \
  "s_branch .LCY_WC_%=\n" \
  ".LCY_ND_%=:\n" \
  "v_mov_b32 v4, 0\n" \
  "buffer_load_dword v4, v60, s[44:47], s67 offen sc1\n" \
  "s_waitcnt vmcnt(0)\n" \
  ".LCY_WC_%=:\n" \
  "v_mov_b32 v6, s59\n" \
  "v_mov_b32 v7, 1\n" \
  "s_mov_b64 exec, 1\n" \
  "ds_add_rtn_u32 v6, v6, v7\n" \
  "s_mov_b64 exec, -1\n" \
  "s_xor_b32 s59, s59, 4\n" \
  "s_mov_b32 s61, 0\n" \
  "s_waitcnt lgkmcnt(0)\n" \
  "v_readfirstlane_b32 s63, v6\n" \
  "v_lshlrev_b32 v6, 5, v2\n" \
  "s_and_b32 s63, s63, 7\n" \
  "s_cmp_eq_u32 s63, 7\n" \
  "s_cbranch_scc0 .LCY_W8_%=\n" \
  "s_mov_b64 exec, 15\n" \
  "global_store_dword v6, v7, s[52:53] sc1\n" \
  "s_mov_b64 exec, -1\n" \
  ".LCY_W8_%=:\n" \
  "s_add_u32 s52, s52, 2048\n" \
  "s_addc_u32 s53, s53, 0\n" \
  ".LCY_DEF_%=:\n" \
  "s_sub_u32 s60, s60, 1\n" \
  "s_cmp_eq_u32 s60, 0\n" \
  "s_cbranch_scc1 .LCY_END_%=\n" \
  ".LCY_NEED_%=:\n" \
  "s_cmp_eq_u32 s68, 0\n" \
  "s_cbranch_scc1 .LCY_BAR_%=\n" \
  "v_cmp_eq_u32 vcc, 0, v4\n" \
  "s_cmp_eq_u64 vcc, 0\n" \
  "s_cbranch_scc1 .LCY_GET_%=\n" \
  "s_cmp_ge_u32 s67, 0x40000\n" \
  "s_cbranch_scc1 .LCY_BAR_%=\n" \
  "s_sleep 4\n" \
  "ds_read_b32 v6, v5 offset:16\n" \
  "s_waitcnt lgkmcnt(0)\n" \
  "v_readfirstlane_b32 s63, v6\n" \
  "s_add_u32 s63, s63, s60\n" \
  "s_cmp_ge_u32 s63, 66\n" \
  "s_cbranch_scc1 .LCY_BAR_%=\n" \
  ".LCY_ASK_%=:\n" \
  "v_mov_b32 v4, 0\n" \
  "buffer_load_dword v4, v60, s[44:47], s67 offen sc1\n" \
  "s_sub_u32 s62, s62, 1\n" \
  "s_cbranch_scc1 .LCY_END_%=\n" \
  "s_waitcnt vmcnt(0)\n" \
  "s_branch .LCY_NEED_%=\n" \
  ".LCY_GET_%=:\n" \
  "v_lshlrev_b32 v6, 16, s55\n" \
  "s_mov_b64 exec, 1\n" \
  "ds_add_u32 v5, v6\n" \
  "s_mov_b64 exec, -1\n" \
  "s_mov_b32 m0, s56\n" \
  "s_add_u32 s71, s56, 4096\n" \
  "global_load_lds_dwordx4 v2, s[48:49] offset:0 sc1\n" \
  "global_load_lds_dwordx4 v2, s[48:49] offset:1024 sc1\n" \
  "global_load_lds_dwordx4 v2, s[48:49] offset:2048 sc1\n" \
  "global_load_lds_dwordx4 v2, s[48:49] offset:3072 sc1\n" \
  "s_mov_b32 m0, s71\n" \
  "s_add_u32 s56, s56, s65\n" \
  "global_load_lds_dwordx4 v3, s[48:49] offset:0 sc1\n" \
  "global_load_lds_dwordx4 v3, s[48:49] offset:1024 sc1\n" \
  "global_load_lds_dwordx4 v3, s[48:49] offset:2048 sc1\n" \
  "global_load_lds_dwordx4 v3, s[48:49] offset:3072 sc1\n" \
  "s_add_u32 s48, s48, 0x80000\n" \
  "s_addc_u32 s49, s49, 0\n" \
  "s_mov_b32 m0, s57\n" \
  "s_add_u32 s57, s57, s65\n" \
  "global_load_lds_dwordx4 v2, s[50:51] offset:0 sc1\n" \
  "global_load_lds_dwordx4 v2, s[50:51] offset:1024 sc1\n" \
  "s_sub_u32 s65, 0, s65\n" \
  "s_add_u32 s67, s67, 4096\n" \
  "global_load_lds_dwordx4 v2, s[50:51] offset:2048 sc1\n" \
  "s_sub_u32 s68, s68, 1\n" \
  "s_mov_b32 s69, 1\n" \
  "global_load_lds_dwordx4 v2, s[50:51] offset:3072 sc1\n" \
  "s_add_u32 s50, s50, 0x40000\n" \
  "s_addc_u32 s51, s51, 0\n" \
  "v_mov_b32 v4, 0\n" \
  "buffer_load_dword v4, v60, s[44:47], s67 offen sc1\n" \
  "s_mov_b32 s61, 0\n" \
  "s_cmp_eq_u32 s68, 0\n" \
  "s_cbranch_scc1 .LCY_BAR_%=\n" \
  "s_waitcnt vmcnt(0)\n" \
  "v_mov_b32 v6, s55\n" \
  "s_mov_b64 exec, 1\n" \
  "ds_add_u32 v5, v6\n" \
  "s_mov_b64 exec, -1\n" \
  "s_mov_b32 s69, 0\n" \
  "s_branch .LCY_NEED_%=\n" \
  ".LCY_END_%=:\n" \
  "s_endpgm\n"
template <bool AT>
__device__ __forceinline__ void role_aw_t(const MegaArgs& A, char* smem, const int L, const int a, const u64 tag) {
  const int w = A.wv;
  const int l = lane_id(), c = l & 15, g = l >> 4;
  const int qt = w & 1, hh = (w >> 1) & 1, h = 2 * a + hh;
  char* ws = A.ws;
  unsigned* FL = (unsigned*)(ws + OFF_FL);
  const unsigned sb = lds_base(smem);
  if (w == 0 && l < 8) reinterpret_cast<unsigned*>(smem + AW_CNT)[l] = 0u;
  constexpr int NP = AT ? 1 : 3;
  const int cp0 = AT ? w : 3 * w - 8;
  v8u wo[2 * NP][4];
  {
    const int q = (l >> 2) & 3, p = l & 3, f = q | ((g & 1) << 2);
    const unsigned ra = sb + (4 * g + q) * 256 + (p & 1) * 16;
#pragma unroll
    for (int r = 0; r < 2; ++r) {
#pragma unroll
      for (int k = 0; k < 2; ++k) {
        const char* base_ = (const char*)A.Wo + ((size_t)((2 * a + r) * DH + 64 * (w & 1) + 32 * k) * DM + 128 * (w >> 1)) * 2;
#pragma unroll
        for (int i = 0; i < 8; ++i) {
          const int fd = g | ((i & 1) << 2);
          glds16(base_, (unsigned)((4 * i + g) * (DM * 2) + ((c ^ (fd << 1)) << 4)), sb + w * 16384 + k * 8192 + i * 1024);
        }
      }
      SST(4 + 3 * r);
      if (r == 1) ready_wait(A, tag, ready_look(A));
      WAIT_VM(0);
      RAW_BAR();
      SST(5 + 3 * r);
#pragma unroll
      for (int k2 = 0; k2 < 2; ++k2) {
#pragma unroll
        for (int ct = 0; ct < 2 * NP; ++ct) {
          const int cp = cp0 + (ct >> 1);
          const unsigned a_ = ra + (cp >> 2) * 32768 + k2 * 16384 + ((2 * (cp & 3) + (p >> 1)) ^ f) * 32 + (ct & 1) * 8;
          const v2u r0 = trd(a_), r1 = trd(a_ + 4096), r2 = trd(a_ + 8192), r3 = trd(a_ + 12288);
          wo[ct][2 * r + k2] = v8u{r0.x, r0.y, r1.x, r1.y, r2.x, r2.y, r3.x, r3.y};
          asm volatile("" : "+v"(wo[ct][2 * r + k2]));
        }
      }
      RAW_BAR();
      SST(6 + 3 * r);
    }
  }
  WSTAMP2();
  const v4u rsW = mk_srd(ws + OFF_PW, SZ_PW);
  const int dp = (FL_FP - FL_FQ + (L * 16 + a - L * 64 * 32 - h) * 32 + 2 * hh) * 4;
  const v4u rsF = mk_srd(FL + FL_FQ + (L * 64 * 32 + h) * 32, (qt ? 0 : dp) + 64 * 4096 - 128);
  const char* p0 = AT ? ws + OFF_MEMK + L * SZ_MEM + h * 2048 : ws + OFF_QK + L * SZ_QK + ((size_t)h * 16 + 8 * qt) * 1024;
  const char* p1 = AT ? (const char*)(FL + FL_FK + L * 32 + (h >> 3) * 64 + (h & 7) * 2) : ws + OFF_VF + L * SZ_O + ((size_t)h * 8 + 4 * qt) * 1024;
  const unsigned* pFW = FL + FL_FW + (L * 64 * 16 + (a >> 2)) * 32 + (a & 3);
  const unsigned tk = sb + AW_TK + hh * 24 * 1024, voff = (c >> 1) * 2048 + (c & 1) * 64 + g * 16;
  const char* dbg = ws + WS_TOTAL + A.vb * 64 * 8;
#define AW_SOPS                                                                                                                 \
  "{s[40:43]}"(rsW), "{s[44:47]}"(rsF), "{s[48:49]}"(p0), "{s[50:51]}"(p1), "{s[52:53]}"(pFW),                                   \
      "{s54}"((L * 64 * 16 + a) * 32768 + cp0 * 128 + (AT ? qt * 16384 : 0)), "{s55}"(1 << (8 * qt)), "{s56}"(tk + qt * 8192), "{s57}"(tk + 16384 + qt * 4096),                  \
      "{s58}"(sb + AW_CNT + 8 + 4 * hh), "{s59}"(sb + AW_CNT), "{s60}"(NSEG), "{s[36:37]}"(dbg)                                  \
      : "memory", "vcc", "scc", "s80"
  if constexpr (AT) {
    const v8u pv = {tk + l * 16, (c & 8) ? 0xEEEEu : 0x4444u, 0u, (unsigned)(OFF_MEMV - OFF_MEMK) + c * 32 + (g & 1) * 16 + (g >> 1) * 1024,
                    voff, (unsigned)l * 16, tk + qt * 4096 + l * 16, sb + AW_SO + w * 4096 + l * 16};
    const unsigned neg = 0xff61b1e6u;
    v8u bt;
#pragma unroll
    for (int i = 0; i < 4; ++i) {
      const unsigned d = 4 * g + i > c ? neg : 0u;
      bt[i] = qt ? 0u : d;
      bt[4 + i] = qt ? d : neg;
    }
    asm volatile("s_mov_b64 exec, -1\ns_waitcnt vmcnt(0) lgkmcnt(0)\ns_nop 4\n" AW_ASM_X
                 ::"{v[192:199]}"(wo[0][0]), "{v[200:207]}"(wo[0][1]), "{v[208:215]}"(wo[0][2]), "{v[216:223]}"(wo[0][3]),
                 "{v[224:231]}"(wo[1][0]), "{v[232:239]}"(wo[1][1]), "{v[240:247]}"(wo[1][2]), "{v[248:255]}"(wo[1][3]),
                 "{v[0:7]}"(pv), "{v[8:15]}"(bt), AW_SOPS);
  } else {
    const v4u pv = {sb + AW_SO + l * 16, voff, (unsigned)l * 16, (l & 2) ? 16u + 8 * qt : qt ? 8u * (l & 1) : dp + 4 * (l & 1)};
    asm volatile("s_mov_b64 exec, -1\ns_waitcnt vmcnt(0) lgkmcnt(0)\ns_nop 4\n" AW_ASM_Y
                 ::"{v[64:71]}"(wo[0][0]), "{v[72:79]}"(wo[0][1]), "{v[80:87]}"(wo[0][2]), "{v[88:95]}"(wo[0][3]),
                 "{v[96:103]}"(wo[1][0]), "{v[104:111]}"(wo[1][1]), "{v[112:119]}"(wo[1][2]), "{v[120:127]}"(wo[1][3]),
                 "{v[128:135]}"(wo[2][0]), "{v[136:143]}"(wo[2][1]), "{v[144:151]}"(wo[2][2]), "{v[152:159]}"(wo[2][3]),
                 "{v[160:167]}"(wo[3][0]), "{v[168:175]}"(wo[3][1]), "{v[176:183]}"(wo[3][2]), "{v[184:191]}"(wo[3][3]),
                 "{v[192:199]}"(wo[4][0]), "{v[200:207]}"(wo[4][1]), "{v[208:215]}"(wo[4][2]), "{v[216:223]}"(wo[4][3]),
                 "{v[224:231]}"(wo[5][0]), "{v[232:239]}"(wo[5][1]), "{v[240:247]}"(wo[5][2]), "{v[248:255]}"(wo[5][3]),
                 "{v[0:3]}"(pv), AW_SOPS);
  }
#undef AW_SOPS
}
__device__ __forceinline__ void role_aw(const MegaArgs& A, char* smem, const int L, const int a, const u64 tag) {
  if (A.wv < 4) role_aw_t<true>(A, smem, L, a, tag);
  else role_aw_t<false>(A, smem, L, a, tag);
}

#define P_ASM \
  "s_mov_b32 s68, 0\n" \
  "s_mov_b32 s66, 3\n" \
  "s_mov_b32 s67, 0\n" \
  "s_mov_b32 s72, 0\n" \
  "v_mov_b32 v7, 1\n" \
  "v_mov_b32 v6, s70\n" \
  "v_mov_b32 v5, 0\n" \
  "v_add_u32 v10, 4096, v2\n" \
  ".p2align 6\n" \
  ".LCP_TOP_%=:\n" \
  "ds_read_b128 v[28:31], v1 offset:0\n" \
  "ds_read_b128 v[32:35], v1 offset:1024\n" \
  "s_add_u32 s66, s66, 3\n" \
  "s_add_u32 s67, s67, 1\n" \
  "s_mov_b32 s69, 0x400000\n" \
  "v_xor_b32 v0, 0x8000, v0\n" \
  "v_xor_b32 v3, 0x100, v3\n" \
  "v_readfirstlane_b32 s71, v5\n" \
  "s_cmp_ge_u32 s71, s66\n" \
  "s_cbranch_scc1 .LCP_GO_%=\n" \
  "s_cmp_eq_u32 s68, 1\n" \
  "s_cbranch_scc0 .LCP_X1_%=\n" \
  "s_waitcnt vmcnt(0)\n" \
  "s_mov_b64 exec, 1\n" \
  "buffer_store_dword v7, off, s[56:59], s72 sc1\n" \
  "s_mov_b64 exec, -1\n" \
  "s_add_u32 s72, s72, 0x1000\n" \
  "s_mov_b32 s68, 0\n" \
  ".LCP_X1_%=:\n" \
  ".LCP_S2_%=:\n" \
  "s_sleep 1\n" \
  "ds_read_b32 v5, v6 offset:0\n" \
  "s_sub_u32 s69, s69, 1\n" \
  "s_cbranch_scc1 .LCP_END_%=\n" \
  "s_waitcnt lgkmcnt(0)\n" \
  "v_readfirstlane_b32 s71, v5\n" \
  "s_cmp_ge_u32 s71, s66\n" \
  "s_cbranch_scc1 .LCP_GO_%=\n" \
  "s_branch .LCP_S2_%=\n" \
  ".LCP_GO_%=:\n" \
  "ds_read_b128 v[12:15], v0 offset:0\n" \
  "ds_read_b128 v[16:19], v0 offset:2048\n" \
  "ds_read_b128 v[20:23], v0 offset:4096\n" \
  "ds_read_b128 v[24:27], v0 offset:6144\n" \
  "ds_read_b128 v[36:39], v1 offset:2048\n" \
  "s_waitcnt lgkmcnt(4)\n" \
  "v_mfma_f32_16x16x32_bf16 v[48:51], v[64:67], v[12:15], 0\n" \
  "v_mfma_f32_16x16x32_bf16 v[52:55], v[128:131], v[12:15], 0\n" \
  "v_mfma_f32_16x16x32_bf16 v[56:59], v[192:195], v[12:15], 0\n" \
  "v_mfma_f32_16x16x32_bf16 v[60:63], v[28:31], v[12:15], 0\n" \
  "ds_read_b128 v[12:15], v0 offset:8192\n" \
  "ds_read_b128 v[28:31], v1 offset:3072\n" \
  "s_waitcnt lgkmcnt(5)\n" \
  "v_mfma_f32_16x16x32_bf16 v[48:51], v[68:71], v[16:19], v[48:51]\n" \
  "v_mfma_f32_16x16x32_bf16 v[52:55], v[132:135], v[16:19], v[52:55]\n" \
  "v_mfma_f32_16x16x32_bf16 v[56:59], v[196:199], v[16:19], v[56:59]\n" \
  "v_mfma_f32_16x16x32_bf16 v[60:63], v[32:35], v[16:19], v[60:63]\n" \
  "ds_read_b128 v[16:19], v0 offset:10240\n" \
  "ds_read_b128 v[32:35], v1 offset:4096\n" \
  "s_waitcnt lgkmcnt(6)\n" \
  "v_mfma_f32_16x16x32_bf16 v[48:51], v[72:75], v[20:23], v[48:51]\n" \
  "v_mfma_f32_16x16x32_bf16 v[52:55], v[136:139], v[20:23], v[52:55]\n" \
  "v_mfma_f32_16x16x32_bf16 v[56:59], v[200:203], v[20:23], v[56:59]\n" \
  "s_waitcnt lgkmcnt(4)\n" \
  "v_mfma_f32_16x16x32_bf16 v[60:63], v[36:39], v[20:23], v[60:63]\n" \
  "ds_read_b128 v[20:23], v0 offset:12288\n" \
  "ds_read_b128 v[36:39], v1 offset:5120\n" \
  "v_mfma_f32_16x16x32_bf16 v[48:51], v[76:79], v[24:27], v[48:51]\n" \
  "v_mfma_f32_16x16x32_bf16 v[52:55], v[140:143], v[24:27], v[52:55]\n" \
  "v_mfma_f32_16x16x32_bf16 v[56:59], v[204:207], v[24:27], v[56:59]\n" \
  "s_waitcnt lgkmcnt(4)\n" \
  "v_mfma_f32_16x16x32_bf16 v[60:63], v[28:31], v[24:27], v[60:63]\n" \
  "ds_read_b128 v[24:27], v0 offset:14336\n" \
  "ds_read_b128 v[28:31], v1 offset:6144\n" \
  "v_mfma_f32_16x16x32_bf16 v[48:51], v[80:83], v[12:15], v[48:51]\n" \
  "v_mfma_f32_16x16x32_bf16 v[52:55], v[144:147], v[12:15], v[52:55]\n" \
  "v_mfma_f32_16x16x32_bf16 v[56:59], v[208:211], v[12:15], v[56:59]\n" \
  "s_waitcnt lgkmcnt(4)\n" \
  "v_mfma_f32_16x16x32_bf16 v[60:63], v[32:35], v[12:15], v[60:63]\n" \
  "ds_read_b128 v[12:15], v0 offset:16384\n" \
  "ds_read_b128 v[32:35], v1 offset:7168\n" \
  "v_mfma_f32_16x16x32_bf16 v[48:51], v[84:87], v[16:19], v[48:51]\n" \
  "v_mfma_f32_16x16x32_bf16 v[52:55], v[148:151], v[16:19], v[52:55]\n" \
  "v_mfma_f32_16x16x32_bf16 v[56:59], v[212:215], v[16:19], v[56:59]\n" \
  "s_waitcnt lgkmcnt(4)\n" \
  "v_mfma_f32_16x16x32_bf16 v[60:63], v[36:39], v[16:19], v[60:63]\n" \
  "ds_read_b128 v[16:19], v0 offset:18432\n" \
  "ds_read_b128 v[36:39], v1 offset:8192\n" \
  "v_mfma_f32_16x16x32_bf16 v[48:51], v[88:91], v[20:23], v[48:51]\n" \
  "v_mfma_f32_16x16x32_bf16 v[52:55], v[152:155], v[20:23], v[52:55]\n" \
  "v_mfma_f32_16x16x32_bf16 v[56:59], v[216:219], v[20:23], v[56:59]\n" \
  "s_waitcnt lgkmcnt(4)\n" \
  "v_mfma_f32_16x16x32_bf16 v[60:63], v[28:31], v[20:23], v[60:63]\n" \
  "ds_read_b128 v[20:23], v0 offset:20480\n" \
  "ds_read_b128 v[28:31], v1 offset:9216\n" \
  "v_mfma_f32_16x16x32_bf16 v[48:51], v[92:95], v[24:27], v[48:51]\n" \
  "v_mfma_f32_16x16x32_bf16 v[52:55], v[156:159], v[24:27], v[52:55]\n" \
  "v_mfma_f32_16x16x32_bf16 v[56:59], v[220:223], v[24:27], v[56:59]\n" \
  "s_waitcnt lgkmcnt(4)\n" \
  "v_mfma_f32_16x16x32_bf16 v[60:63], v[32:35], v[24:27], v[60:63]\n" \
  "ds_read_b128 v[24:27], v0 offset:22528\n" \
  "ds_read_b128 v[32:35], v1 offset:10240\n" \
  "v_mfma_f32_16x16x32_bf16 v[48:51], v[96:99], v[12:15], v[48:51]\n" \
  "v_mfma_f32_16x16x32_bf16 v[52:55], v[160:163], v[12:15], v[52:55]\n" \
  "v_mfma_f32_16x16x32_bf16 v[56:59], v[224:227], v[12:15], v[56:59]\n" \
  "s_waitcnt lgkmcnt(4)\n" \
  "v_mfma_f32_16x16x32_bf16 v[60:63], v[36:39], v[12:15], v[60:63]\n" \
  "ds_read_b128 v[12:15], v0 offset:24576\n" \
  "ds_read_b128 v[36:39], v1 offset:11264\n" \
  "v_mfma_f32_16x16x32_bf16 v[48:51], v[100:103], v[16:19], v[48:51]\n" \
  "v_mfma_f32_16x16x32_bf16 v[52:55], v[164:167], v[16:19], v[52:55]\n" \
  "v_mfma_f32_16x16x32_bf16 v[56:59], v[228:231], v[16:19], v[56:59]\n" \
  "s_waitcnt lgkmcnt(4)\n" \
  "v_mfma_f32_16x16x32_bf16 v[60:63], v[28:31], v[16:19], v[60:63]\n" \
  "ds_read_b128 v[16:19], v0 offset:26624\n" \
  "ds_read_b128 v[28:31], v1 offset:12288\n" \
  "v_mfma_f32_16x16x32_bf16 v[48:51], v[104:107], v[20:23], v[48:51]\n" \
  "v_mfma_f32_16x16x32_bf16 v[52:55], v[168:171], v[20:23], v[52:55]\n" \
  "v_mfma_f32_16x16x32_bf16 v[56:59], v[232:235], v[20:23], v[56:59]\n" \
  "s_waitcnt lgkmcnt(4)\n" \
  "v_mfma_f32_16x16x32_bf16 v[60:63], v[32:35], v[20:23], v[60:63]\n" \
  "ds_read_b128 v[20:23], v0 offset:28672\n" \
  "ds_read_b128 v[32:35], v1 offset:13312\n" \
  "v_mfma_f32_16x16x32_bf16 v[48:51], v[108:111], v[24:27], v[48:51]\n" \
  "v_mfma_f32_16x16x32_bf16 v[52:55], v[172:175], v[24:27], v[52:55]\n" \
  "v_mfma_f32_16x16x32_bf16 v[56:59], v[236:239], v[24:27], v[56:59]\n" \
  "s_waitcnt lgkmcnt(4)\n" \
  "v_mfma_f32_16x16x32_bf16 v[60:63], v[36:39], v[24:27], v[60:63]\n" \
  "s_cmp_eq_u32 s68, 1\n" \
  "s_cbranch_scc0 .LCP_X3_%=\n" \
  "s_waitcnt vmcnt(0)\n" \
  "s_mov_b64 exec, 1\n" \
  "buffer_store_dword v7, off, s[56:59], s72 sc1\n" \
  "s_mov_b64 exec, -1\n" \
  "s_add_u32 s72, s72, 0x1000\n" \
  "s_mov_b32 s68, 0\n" \
  ".LCP_X3_%=:\n" \
  "ds_read_b32 v5, v6 offset:16\n" \
  "ds_read_b128 v[24:27], v0 offset:30720\n" \
  "ds_read_b128 v[36:39], v1 offset:14336\n" \
  "v_mfma_f32_16x16x32_bf16 v[48:51], v[112:115], v[12:15], v[48:51]\n" \
  "v_mfma_f32_16x16x32_bf16 v[52:55], v[176:179], v[12:15], v[52:55]\n" \
  "v_mfma_f32_16x16x32_bf16 v[56:59], v[240:243], v[12:15], v[56:59]\n" \
  "s_waitcnt lgkmcnt(5)\n" \
  "v_mfma_f32_16x16x32_bf16 v[60:63], v[28:31], v[12:15], v[60:63]\n" \
  "ds_read_b128 v[12:15], v0 offset:256\n" \
  "ds_read_b128 v[28:31], v1 offset:15360\n" \
  "ds_read_b128 v[40:43], v4 offset:0\n" \
  "ds_read_b128 v[44:47], v4 offset:64\n" \
  "v_mfma_f32_16x16x32_bf16 v[48:51], v[116:119], v[16:19], v[48:51]\n" \
  "v_mfma_f32_16x16x32_bf16 v[52:55], v[180:183], v[16:19], v[52:55]\n" \
  "v_mfma_f32_16x16x32_bf16 v[56:59], v[244:247], v[16:19], v[56:59]\n" \
  "s_waitcnt lgkmcnt(7)\n" \
  "v_mfma_f32_16x16x32_bf16 v[60:63], v[32:35], v[16:19], v[60:63]\n" \
  "s_waitcnt lgkmcnt(6)\n" \
  "v_readfirstlane_b32 s71, v5\n" \
  "s_cmp_ge_u32 s71, s67\n" \
  "s_cbranch_scc1 .LCP_ST_%=\n" \
  ".LCP_S4_%=:\n" \
  "s_sleep 1\n" \
  "ds_read_b32 v5, v6 offset:16\n" \
  "s_sub_u32 s69, s69, 1\n" \
  "s_cbranch_scc1 .LCP_END_%=\n" \
  "s_waitcnt lgkmcnt(0)\n" \
  "v_readfirstlane_b32 s71, v5\n" \
  "s_cmp_ge_u32 s71, s67\n" \
  "s_cbranch_scc1 .LCP_ST_%=\n" \
  "s_branch .LCP_S4_%=\n" \
  ".LCP_ST_%=:\n" \
  "ds_read_b128 v[16:19], v0 offset:2304\n" \
  "ds_read_b128 v[32:35], v4 offset:128\n" \
  "ds_read2_b32 v[8:9], v3 offset0:0 offset1:32\n" \
  "v_mfma_f32_16x16x32_bf16 v[48:51], v[120:123], v[20:23], v[48:51]\n" \
  "v_mfma_f32_16x16x32_bf16 v[52:55], v[184:187], v[20:23], v[52:55]\n" \
  "v_mfma_f32_16x16x32_bf16 v[56:59], v[248:251], v[20:23], v[56:59]\n" \
  "s_waitcnt lgkmcnt(7)\n" \
  "v_mfma_f32_16x16x32_bf16 v[60:63], v[36:39], v[20:23], v[60:63]\n" \
  "ds_read_b128 v[20:23], v0 offset:4352\n" \
  "ds_read_b128 v[36:39], v4 offset:192\n" \
  "v_mfma_f32_16x16x32_bf16 v[48:51], v[124:127], v[24:27], v[48:51]\n" \
  "v_mfma_f32_16x16x32_bf16 v[52:55], v[188:191], v[24:27], v[52:55]\n" \
  "v_mfma_f32_16x16x32_bf16 v[56:59], v[252:255], v[24:27], v[56:59]\n" \
  "s_waitcnt lgkmcnt(7)\n" \
  "v_mfma_f32_16x16x32_bf16 v[60:63], v[28:31], v[24:27], v[60:63]\n" \
  "s_waitcnt lgkmcnt(0)\n" \
  "s_nop 4\n" \
  "v_fma_f32 v48, -v8, v40, v48\n" \
  "v_fma_f32 v49, -v8, v41, v49\n" \
  "v_fma_f32 v50, -v8, v42, v50\n" \
  "v_fma_f32 v51, -v8, v43, v51\n" \
  "v_fma_f32 v52, -v8, v44, v52\n" \
  "v_fma_f32 v53, -v8, v45, v53\n" \
  "v_fma_f32 v54, -v8, v46, v54\n" \
  "v_fma_f32 v55, -v8, v47, v55\n" \
  "v_fma_f32 v56, -v8, v32, v56\n" \
  "v_fma_f32 v57, -v8, v33, v57\n" \
  "v_fma_f32 v58, -v8, v34, v58\n" \
  "v_fma_f32 v59, -v8, v35, v59\n" \
  "v_fma_f32 v60, -v8, v36, v60\n" \
  "v_fma_f32 v61, -v8, v37, v61\n" \
  "v_fma_f32 v62, -v8, v38, v62\n" \
  "v_fma_f32 v63, -v8, v39, v63\n" \
  "ds_read_b128 v[32:35], v1 offset:0\n" \
  "ds_read_b128 v[36:39], v1 offset:1024\n" \
  "v_mul_f32 v48, v48, v9\n" \
  "v_mul_f32 v49, v49, v9\n" \
  "v_mul_f32 v50, v50, v9\n" \
  "v_mul_f32 v51, v51, v9\n" \
  "v_mul_f32 v52, v52, v9\n" \
  "v_mul_f32 v53, v53, v9\n" \
  "v_mul_f32 v54, v54, v9\n" \
  "v_mul_f32 v55, v55, v9\n" \
  "v_cvt_pk_bf16_f32 v40, v48, v49\n" \
  "v_cvt_pk_bf16_f32 v41, v50, v51\n" \
  "v_cvt_pk_bf16_f32 v42, v52, v53\n" \
  "v_cvt_pk_bf16_f32 v43, v54, v55\n" \
  "buffer_store_dwordx4 v[40:43], v2, s[40:43], s65 offen offset:0 sc1\n" \
  "v_mul_f32 v56, v56, v9\n" \
  "v_mul_f32 v57, v57, v9\n" \
  "v_mul_f32 v58, v58, v9\n" \
  "v_mul_f32 v59, v59, v9\n" \
  "v_mul_f32 v60, v60, v9\n" \
  "v_mul_f32 v61, v61, v9\n" \
  "v_mul_f32 v62, v62, v9\n" \
  "v_mul_f32 v63, v63, v9\n" \
  "v_cvt_pk_bf16_f32 v44, v56, v57\n" \
  "v_cvt_pk_bf16_f32 v45, v58, v59\n" \
  "v_cvt_pk_bf16_f32 v46, v60, v61\n" \
  "v_cvt_pk_bf16_f32 v47, v62, v63\n" \
  "buffer_store_dwordx4 v[44:47], v2, s[40:43], s65 offen offset:1024 sc1\n" \
  "ds_read_b128 v[24:27], v0 offset:6400\n" \
  "ds_read_b128 v[28:31], v1 offset:2048\n" \
  "v_mfma_f32_16x16x32_bf16 v[48:51], v[64:67], v[12:15], 0\n" \
  "v_mfma_f32_16x16x32_bf16 v[52:55], v[128:131], v[12:15], 0\n" \
  "v_mfma_f32_16x16x32_bf16 v[56:59], v[192:195], v[12:15], 0\n" \
  "s_waitcnt lgkmcnt(3)\n" \
  "v_mfma_f32_16x16x32_bf16 v[60:63], v[32:35], v[12:15], 0\n" \
  "ds_read_b128 v[12:15], v0 offset:8448\n" \
  "ds_read_b128 v[32:35], v1 offset:3072\n" \
  "v_mfma_f32_16x16x32_bf16 v[48:51], v[68:71], v[16:19], v[48:51]\n" \
  "v_mfma_f32_16x16x32_bf16 v[52:55], v[132:135], v[16:19], v[52:55]\n" \
  "v_mfma_f32_16x16x32_bf16 v[56:59], v[196:199], v[16:19], v[56:59]\n" \
  "s_waitcnt lgkmcnt(4)\n" \
  "v_mfma_f32_16x16x32_bf16 v[60:63], v[36:39], v[16:19], v[60:63]\n" \
  "ds_read_b128 v[16:19], v0 offset:10496\n" \
  "ds_read_b128 v[36:39], v1 offset:4096\n" \
  "v_mfma_f32_16x16x32_bf16 v[48:51], v[72:75], v[20:23], v[48:51]\n" \
  "v_mfma_f32_16x16x32_bf16 v[52:55], v[136:139], v[20:23], v[52:55]\n" \
  "v_mfma_f32_16x16x32_bf16 v[56:59], v[200:203], v[20:23], v[56:59]\n" \
  "s_waitcnt lgkmcnt(4)\n" \
  "v_mfma_f32_16x16x32_bf16 v[60:63], v[28:31], v[20:23], v[60:63]\n" \
  "ds_read_b128 v[20:23], v0 offset:12544\n" \
  "ds_read_b128 v[28:31], v1 offset:5120\n" \
  "v_mfma_f32_16x16x32_bf16 v[48:51], v[76:79], v[24:27], v[48:51]\n" \
  "v_mfma_f32_16x16x32_bf16 v[52:55], v[140:143], v[24:27], v[52:55]\n" \
  "v_mfma_f32_16x16x32_bf16 v[56:59], v[204:207], v[24:27], v[56:59]\n" \
  "s_waitcnt lgkmcnt(4)\n" \
  "v_mfma_f32_16x16x32_bf16 v[60:63], v[32:35], v[24:27], v[60:63]\n" \
  "ds_read_b128 v[24:27], v0 offset:14592\n" \
  "ds_read_b128 v[32:35], v1 offset:6144\n" \
  "v_mfma_f32_16x16x32_bf16 v[48:51], v[80:83], v[12:15], v[48:51]\n" \
  "v_mfma_f32_16x16x32_bf16 v[52:55], v[144:147], v[12:15], v[52:55]\n" \
  "v_mfma_f32_16x16x32_bf16 v[56:59], v[208:211], v[12:15], v[56:59]\n" \
  "s_waitcnt lgkmcnt(4)\n" \
  "v_mfma_f32_16x16x32_bf16 v[60:63], v[36:39], v[12:15], v[60:63]\n" \
  "ds_read_b128 v[12:15], v0 offset:16640\n" \
  "ds_read_b128 v[36:39], v1 offset:7168\n" \
  "v_mfma_f32_16x16x32_bf16 v[48:51], v[84:87], v[16:19], v[48:51]\n" \
  "v_mfma_f32_16x16x32_bf16 v[52:55], v[148:151], v[16:19], v[52:55]\n" \
  "v_mfma_f32_16x16x32_bf16 v[56:59], v[212:215], v[16:19], v[56:59]\n" \
  "s_waitcnt lgkmcnt(4)\n" \
  "v_mfma_f32_16x16x32_bf16 v[60:63], v[28:31], v[16:19], v[60:63]\n" \
  "ds_read_b128 v[16:19], v0 offset:18688\n" \
  "ds_read_b128 v[28:31], v1 offset:8192\n" \
  "v_mfma_f32_16x16x32_bf16 v[48:51], v[88:91], v[20:23], v[48:51]\n" \
  "v_mfma_f32_16x16x32_bf16 v[52:55], v[152:155], v[20:23], v[52:55]\n" \
  "v_mfma_f32_16x16x32_bf16 v[56:59], v[216:219], v[20:23], v[56:59]\n" \
  "s_waitcnt lgkmcnt(4)\n" \
  "v_mfma_f32_16x16x32_bf16 v[60:63], v[32:35], v[20:23], v[60:63]\n" \
  "ds_read_b128 v[20:23], v0 offset:20736\n" \
  "ds_read_b128 v[32:35], v1 offset:9216\n" \
  "v_mfma_f32_16x16x32_bf16 v[48:51], v[92:95], v[24:27], v[48:51]\n" \
  "v_mfma_f32_16x16x32_bf16 v[52:55], v[156:159], v[24:27], v[52:55]\n" \
  "v_mfma_f32_16x16x32_bf16 v[56:59], v[220:223], v[24:27], v[56:59]\n" \
  "s_waitcnt lgkmcnt(4)\n" \
  "v_mfma_f32_16x16x32_bf16 v[60:63], v[36:39], v[24:27], v[60:63]\n" \
  "ds_read_b128 v[24:27], v0 offset:22784\n" \
  "ds_read_b128 v[36:39], v1 offset:10240\n" \
  "v_mfma_f32_16x16x32_bf16 v[48:51], v[96:99], v[12:15], v[48:51]\n" \
  "v_mfma_f32_16x16x32_bf16 v[52:55], v[160:163], v[12:15], v[52:55]\n" \
  "v_mfma_f32_16x16x32_bf16 v[56:59], v[224:227], v[12:15], v[56:59]\n" \
  "s_waitcnt lgkmcnt(4)\n" \
  "v_mfma_f32_16x16x32_bf16 v[60:63], v[28:31], v[12:15], v[60:63]\n" \
  "ds_read_b128 v[12:15], v0 offset:24832\n" \
  "ds_read_b128 v[28:31], v1 offset:11264\n" \
  "v_mfma_f32_16x16x32_bf16 v[48:51], v[100:103], v[16:19], v[48:51]\n" \
  "v_mfma_f32_16x16x32_bf16 v[52:55], v[164:167], v[16:19], v[52:55]\n" \
  "v_mfma_f32_16x16x32_bf16 v[56:59], v[228:231], v[16:19], v[56:59]\n" \
  "s_waitcnt lgkmcnt(4)\n" \
  "v_mfma_f32_16x16x32_bf16 v[60:63], v[32:35], v[16:19], v[60:63]\n" \
  "ds_read_b128 v[16:19], v0 offset:26880\n" \
  "ds_read_b128 v[32:35], v1 offset:12288\n" \
  "v_mfma_f32_16x16x32_bf16 v[48:51], v[104:107], v[20:23], v[48:51]\n" \
  "v_mfma_f32_16x16x32_bf16 v[52:55], v[168:171], v[20:23], v[52:55]\n" \
  "v_mfma_f32_16x16x32_bf16 v[56:59], v[232:235], v[20:23], v[56:59]\n" \
  "s_waitcnt lgkmcnt(4)\n" \
  "v_mfma_f32_16x16x32_bf16 v[60:63], v[36:39], v[20:23], v[60:63]\n" \
  "ds_read_b128 v[20:23], v0 offset:28928\n" \
  "ds_read_b128 v[36:39], v1 offset:13312\n" \
  "v_mfma_f32_16x16x32_bf16 v[48:51], v[108:111], v[24:27], v[48:51]\n" \
  "v_mfma_f32_16x16x32_bf16 v[52:55], v[172:175], v[24:27], v[52:55]\n" \
  "v_mfma_f32_16x16x32_bf16 v[56:59], v[236:239], v[24:27], v[56:59]\n" \
  "s_waitcnt lgkmcnt(4)\n" \
  "v_mfma_f32_16x16x32_bf16 v[60:63], v[28:31], v[24:27], v[60:63]\n" \
  "ds_read_b128 v[24:27], v0 offset:30976\n" \
  "ds_read_b128 v[28:31], v1 offset:14336\n" \
  "v_mfma_f32_16x16x32_bf16 v[48:51], v[112:115], v[12:15], v[48:51]\n" \
  "v_mfma_f32_16x16x32_bf16 v[52:55], v[176:179], v[12:15], v[52:55]\n" \
  "v_mfma_f32_16x16x32_bf16 v[56:59], v[240:243], v[12:15], v[56:59]\n" \
  "s_waitcnt lgkmcnt(4)\n" \
  "v_mfma_f32_16x16x32_bf16 v[60:63], v[32:35], v[12:15], v[60:63]\n" \
  "ds_read_b128 v[32:35], v1 offset:15360\n" \
  "ds_read_b128 v[40:43], v4 offset:0\n" \
  "ds_read_b128 v[44:47], v4 offset:64\n" \
  "v_mfma_f32_16x16x32_bf16 v[48:51], v[116:119], v[16:19], v[48:51]\n" \
  "v_mfma_f32_16x16x32_bf16 v[52:55], v[180:183], v[16:19], v[52:55]\n" \
  "v_mfma_f32_16x16x32_bf16 v[56:59], v[244:247], v[16:19], v[56:59]\n" \
  "s_waitcnt lgkmcnt(5)\n" \
  "v_mfma_f32_16x16x32_bf16 v[60:63], v[36:39], v[16:19], v[60:63]\n" \
  "ds_read_b128 v[36:39], v4 offset:128\n" \
  "ds_read2_b32 v[8:9], v3 offset0:16 offset1:48\n" \
  "v_mfma_f32_16x16x32_bf16 v[48:51], v[120:123], v[20:23], v[48:51]\n" \
  "v_mfma_f32_16x16x32_bf16 v[52:55], v[184:187], v[20:23], v[52:55]\n" \
  "v_mfma_f32_16x16x32_bf16 v[56:59], v[248:251], v[20:23], v[56:59]\n" \
  "s_waitcnt lgkmcnt(5)\n" \
  "v_mfma_f32_16x16x32_bf16 v[60:63], v[28:31], v[20:23], v[60:63]\n" \
  "ds_read_b128 v[28:31], v4 offset:192\n" \
  "s_mov_b64 exec, 1\n" \
  "ds_add_u32 v6, v7 offset:8\n" \
  "s_mov_b64 exec, -1\n" \
  "v_xor_b32 v6, 4, v6\n" \
  "v_mfma_f32_16x16x32_bf16 v[48:51], v[124:127], v[24:27], v[48:51]\n" \
  "v_mfma_f32_16x16x32_bf16 v[52:55], v[188:191], v[24:27], v[52:55]\n" \
  "v_mfma_f32_16x16x32_bf16 v[56:59], v[252:255], v[24:27], v[56:59]\n" \
  "s_waitcnt lgkmcnt(6)\n" \
  "v_mfma_f32_16x16x32_bf16 v[60:63], v[32:35], v[24:27], v[60:63]\n" \
  "s_waitcnt lgkmcnt(1)\n" \
  "s_nop 4\n" \
  "ds_read_b32 v5, v6 offset:0\n" \
  "v_fma_f32 v48, -v8, v40, v48\n" \
  "v_fma_f32 v49, -v8, v41, v49\n" \
  "v_fma_f32 v50, -v8, v42, v50\n" \
  "v_fma_f32 v51, -v8, v43, v51\n" \
  "v_fma_f32 v52, -v8, v44, v52\n" \
  "v_fma_f32 v53, -v8, v45, v53\n" \
  "v_fma_f32 v54, -v8, v46, v54\n" \
  "v_fma_f32 v55, -v8, v47, v55\n" \
  "v_fma_f32 v56, -v8, v36, v56\n" \
  "v_fma_f32 v57, -v8, v37, v57\n" \
  "v_fma_f32 v58, -v8, v38, v58\n" \
  "v_fma_f32 v59, -v8, v39, v59\n" \
  "v_fma_f32 v60, -v8, v28, v60\n" \
  "v_fma_f32 v61, -v8, v29, v61\n" \
  "v_fma_f32 v62, -v8, v30, v62\n" \
  "v_fma_f32 v63, -v8, v31, v63\n" \
  "v_mul_f32 v48, v48, v9\n" \
  "v_mul_f32 v49, v49, v9\n" \
  "v_mul_f32 v50, v50, v9\n" \
  "v_mul_f32 v51, v51, v9\n" \
  "v_mul_f32 v52, v52, v9\n" \
  "v_mul_f32 v53, v53, v9\n" \
  "v_mul_f32 v54, v54, v9\n" \
  "v_mul_f32 v55, v55, v9\n" \
  "v_cvt_pk_bf16_f32 v40, v48, v49\n" \
  "v_cvt_pk_bf16_f32 v41, v50, v51\n" \
  "v_cvt_pk_bf16_f32 v42, v52, v53\n" \
  "v_cvt_pk_bf16_f32 v43, v54, v55\n" \
  "buffer_store_dwordx4 v[40:43], v10, s[40:43], s65 offen offset:0 sc1\n" \
  "v_mul_f32 v56, v56, v9\n" \
  "v_mul_f32 v57, v57, v9\n" \
  "v_mul_f32 v58, v58, v9\n" \
  "v_mul_f32 v59, v59, v9\n" \
  "v_mul_f32 v60, v60, v9\n" \
  "v_mul_f32 v61, v61, v9\n" \
  "v_mul_f32 v62, v62, v9\n" \
  "v_mul_f32 v63, v63, v9\n" \
  "v_cvt_pk_bf16_f32 v44, v56, v57\n" \
  "v_cvt_pk_bf16_f32 v45, v58, v59\n" \
  "v_cvt_pk_bf16_f32 v46, v60, v61\n" \
  "v_cvt_pk_bf16_f32 v47, v62, v63\n" \
  "buffer_store_dwordx4 v[44:47], v10, s[40:43], s65 offen offset:1024 sc1\n" \
  "s_waitcnt lgkmcnt(0)\n" \
  "s_add_u32 s65, s65, 0x80000\n" \
  "s_sub_u32 s64, s64, 1\n" \
  "s_mov_b32 s68, 1\n" \
  "s_cmp_lg_u32 s64, 0\n" \
  "s_cbranch_scc1 .LCP_TOP_%=\n" \
  "s_waitcnt vmcnt(0)\n" \
  "s_mov_b64 exec, 1\n" \
  "buffer_store_dword v7, off, s[56:59], s72 sc1\n" \
  "s_mov_b64 exec, -1\n" \
  "s_add_u32 s72, s72, 0x1000\n" \
  "s_mov_b32 s68, 0\n" \
  ".LCP_END_%=:\n" \
  "s_endpgm\n"
#define V_ASM \
  "s_mov_b32 s68, 0\n" \
  "s_mov_b32 s66, 3\n" \
  "s_mov_b32 s67, 0\n" \
  "s_mov_b32 s72, 0\n" \
  "v_mov_b32 v7, 1\n" \
  "v_mov_b32 v6, s70\n" \
  "v_mov_b32 v5, 0\n" \
  "ds_read_b32 v8, v4 offset:0\n" \
  "ds_read_b32 v9, v4 offset:64\n" \
  "ds_read_b32 v10, v4 offset:128\n" \
  "ds_read_b32 v4, v4 offset:192\n" \
  "s_waitcnt lgkmcnt(0)\n" \
  ".p2align 6\n" \
  ".LCW_TOP_%=:\n" \
  "ds_read_b128 v[28:31], v1 offset:0\n" \
  "ds_read_b128 v[32:35], v1 offset:1024\n" \
  "s_add_u32 s66, s66, 3\n" \
  "s_add_u32 s67, s67, 1\n" \
  "s_mov_b32 s69, 0x400000\n" \
  "v_xor_b32 v0, 0x8000, v0\n" \
  "v_xor_b32 v3, 0x100, v3\n" \
  "v_readfirstlane_b32 s71, v5\n" \
  "s_cmp_ge_u32 s71, s66\n" \
  "s_cbranch_scc1 .LCW_GO_%=\n" \
  "s_cmp_eq_u32 s68, 1\n" \
  "s_cbranch_scc0 .LCW_X1_%=\n" \
  "s_waitcnt vmcnt(0)\n" \
  "s_mov_b64 exec, 1\n" \
  "buffer_store_dword v7, off, s[56:59], s72 sc1\n" \
  "s_mov_b64 exec, -1\n" \
  "s_add_u32 s72, s72, 0x1000\n" \
  "s_mov_b32 s68, 0\n" \
  ".LCW_X1_%=:\n" \
  ".LCW_S2_%=:\n" \
  "s_sleep 1\n" \
  "ds_read_b32 v5, v6 offset:0\n" \
  "s_sub_u32 s69, s69, 1\n" \
  "s_cbranch_scc1 .LCW_END_%=\n" \
  "s_waitcnt lgkmcnt(0)\n" \
  "v_readfirstlane_b32 s71, v5\n" \
  "s_cmp_ge_u32 s71, s66\n" \
  "s_cbranch_scc1 .LCW_GO_%=\n" \
  "s_branch .LCW_S2_%=\n" \
  ".LCW_GO_%=:\n" \
  "ds_read_b128 v[12:15], v0 offset:0\n" \
  "ds_read_b128 v[16:19], v0 offset:2048\n" \
  "ds_read_b128 v[20:23], v0 offset:4096\n" \
  "ds_read_b128 v[24:27], v0 offset:6144\n" \
  "ds_read_b128 v[36:39], v1 offset:2048\n" \
  "s_waitcnt lgkmcnt(4)\n" \
  "v_mfma_f32_16x16x32_bf16 v[42:45], v[12:15], v[64:67], 0\n" \
  "v_mfma_f32_16x16x32_bf16 v[48:51], v[12:15], v[128:131], 0\n" \
  "v_mfma_f32_16x16x32_bf16 v[54:57], v[12:15], v[192:195], 0\n" \
  "v_mfma_f32_16x16x32_bf16 v[60:63], v[12:15], v[28:31], 0\n" \
  "ds_read_b128 v[12:15], v0 offset:8192\n" \
  "ds_read_b128 v[28:31], v1 offset:3072\n" \
  "s_waitcnt lgkmcnt(5)\n" \
  "v_mfma_f32_16x16x32_bf16 v[42:45], v[16:19], v[68:71], v[42:45]\n" \
  "v_mfma_f32_16x16x32_bf16 v[48:51], v[16:19], v[132:135], v[48:51]\n" \
  "v_mfma_f32_16x16x32_bf16 v[54:57], v[16:19], v[196:199], v[54:57]\n" \
  "v_mfma_f32_16x16x32_bf16 v[60:63], v[16:19], v[32:35], v[60:63]\n" \
  "ds_read_b128 v[16:19], v0 offset:10240\n" \
  "ds_read_b128 v[32:35], v1 offset:4096\n" \
  "s_waitcnt lgkmcnt(6)\n" \
  "v_mfma_f32_16x16x32_bf16 v[42:45], v[20:23], v[72:75], v[42:45]\n" \
  "v_mfma_f32_16x16x32_bf16 v[48:51], v[20:23], v[136:139], v[48:51]\n" \
  "v_mfma_f32_16x16x32_bf16 v[54:57], v[20:23], v[200:203], v[54:57]\n" \
  "s_waitcnt lgkmcnt(4)\n" \
  "v_mfma_f32_16x16x32_bf16 v[60:63], v[20:23], v[36:39], v[60:63]\n" \
  "ds_read_b128 v[20:23], v0 offset:12288\n" \
  "ds_read_b128 v[36:39], v1 offset:5120\n" \
  "v_mfma_f32_16x16x32_bf16 v[42:45], v[24:27], v[76:79], v[42:45]\n" \
  "v_mfma_f32_16x16x32_bf16 v[48:51], v[24:27], v[140:143], v[48:51]\n" \
  "v_mfma_f32_16x16x32_bf16 v[54:57], v[24:27], v[204:207], v[54:57]\n" \
  "s_waitcnt lgkmcnt(4)\n" \
  "v_mfma_f32_16x16x32_bf16 v[60:63], v[24:27], v[28:31], v[60:63]\n" \
  "ds_read_b128 v[24:27], v0 offset:14336\n" \
  "ds_read_b128 v[28:31], v1 offset:6144\n" \
  "v_mfma_f32_16x16x32_bf16 v[42:45], v[12:15], v[80:83], v[42:45]\n" \
  "v_mfma_f32_16x16x32_bf16 v[48:51], v[12:15], v[144:147], v[48:51]\n" \
  "v_mfma_f32_16x16x32_bf16 v[54:57], v[12:15], v[208:211], v[54:57]\n" \
  "s_waitcnt lgkmcnt(4)\n" \
  "v_mfma_f32_16x16x32_bf16 v[60:63], v[12:15], v[32:35], v[60:63]\n" \
  "ds_read_b128 v[12:15], v0 offset:16384\n" \
  "ds_read_b128 v[32:35], v1 offset:7168\n" \
  "v_mfma_f32_16x16x32_bf16 v[42:45], v[16:19], v[84:87], v[42:45]\n" \
  "v_mfma_f32_16x16x32_bf16 v[48:51], v[16:19], v[148:151], v[48:51]\n" \
  "v_mfma_f32_16x16x32_bf16 v[54:57], v[16:19], v[212:215], v[54:57]\n" \
  "s_waitcnt lgkmcnt(4)\n" \
  "v_mfma_f32_16x16x32_bf16 v[60:63], v[16:19], v[36:39], v[60:63]\n" \
  "ds_read_b128 v[16:19], v0 offset:18432\n" \
  "ds_read_b128 v[36:39], v1 offset:8192\n" \
  "v_mfma_f32_16x16x32_bf16 v[42:45], v[20:23], v[88:91], v[42:45]\n" \
  "v_mfma_f32_16x16x32_bf16 v[48:51], v[20:23], v[152:155], v[48:51]\n" \
  "v_mfma_f32_16x16x32_bf16 v[54:57], v[20:23], v[216:219], v[54:57]\n" \
  "s_waitcnt lgkmcnt(4)\n" \
  "v_mfma_f32_16x16x32_bf16 v[60:63], v[20:23], v[28:31], v[60:63]\n" \
  "ds_read_b128 v[20:23], v0 offset:20480\n" \
  "ds_read_b128 v[28:31], v1 offset:9216\n" \
  "v_mfma_f32_16x16x32_bf16 v[42:45], v[24:27], v[92:95], v[42:45]\n" \
  "v_mfma_f32_16x16x32_bf16 v[48:51], v[24:27], v[156:159], v[48:51]\n" \
  "v_mfma_f32_16x16x32_bf16 v[54:57], v[24:27], v[220:223], v[54:57]\n" \
  "s_waitcnt lgkmcnt(4)\n" \
  "v_mfma_f32_16x16x32_bf16 v[60:63], v[24:27], v[32:35], v[60:63]\n" \
  "ds_read_b128 v[24:27], v0 offset:22528\n" \
  "ds_read_b128 v[32:35], v1 offset:10240\n" \
  "v_mfma_f32_16x16x32_bf16 v[42:45], v[12:15], v[96:99], v[42:45]\n" \
  "v_mfma_f32_16x16x32_bf16 v[48:51], v[12:15], v[160:163], v[48:51]\n" \
  "v_mfma_f32_16x16x32_bf16 v[54:57], v[12:15], v[224:227], v[54:57]\n" \
  "s_waitcnt lgkmcnt(4)\n" \
  "v_mfma_f32_16x16x32_bf16 v[60:63], v[12:15], v[36:39], v[60:63]\n" \
  "ds_read_b128 v[12:15], v0 offset:24576\n" \
  "ds_read_b128 v[36:39], v1 offset:11264\n" \
  "v_mfma_f32_16x16x32_bf16 v[42:45], v[16:19], v[100:103], v[42:45]\n" \
  "v_mfma_f32_16x16x32_bf16 v[48:51], v[16:19], v[164:167], v[48:51]\n" \
  "v_mfma_f32_16x16x32_bf16 v[54:57], v[16:19], v[228:231], v[54:57]\n" \
  "s_waitcnt lgkmcnt(4)\n" \
  "v_mfma_f32_16x16x32_bf16 v[60:63], v[16:19], v[28:31], v[60:63]\n" \
  "ds_read_b128 v[16:19], v0 offset:26624\n" \
  "ds_read_b128 v[28:31], v1 offset:12288\n" \
  "v_mfma_f32_16x16x32_bf16 v[42:45], v[20:23], v[104:107], v[42:45]\n" \
  "v_mfma_f32_16x16x32_bf16 v[48:51], v[20:23], v[168:171], v[48:51]\n" \
  "v_mfma_f32_16x16x32_bf16 v[54:57], v[20:23], v[232:235], v[54:57]\n" \
  "s_waitcnt lgkmcnt(4)\n" \
  "v_mfma_f32_16x16x32_bf16 v[60:63], v[20:23], v[32:35], v[60:63]\n" \
  "ds_read_b128 v[20:23], v0 offset:28672\n" \
  "ds_read_b128 v[32:35], v1 offset:13312\n" \
  "v_mfma_f32_16x16x32_bf16 v[42:45], v[24:27], v[108:111], v[42:45]\n" \
  "v_mfma_f32_16x16x32_bf16 v[48:51], v[24:27], v[172:175], v[48:51]\n" \
  "v_mfma_f32_16x16x32_bf16 v[54:57], v[24:27], v[236:239], v[54:57]\n" \
  "s_waitcnt lgkmcnt(4)\n" \
  "v_mfma_f32_16x16x32_bf16 v[60:63], v[24:27], v[36:39], v[60:63]\n" \
  "s_cmp_eq_u32 s68, 1\n" \
  "s_cbranch_scc0 .LCW_X3_%=\n" \
  "s_waitcnt vmcnt(0)\n" \
  "s_mov_b64 exec, 1\n" \
  "buffer_store_dword v7, off, s[56:59], s72 sc1\n" \
  "s_mov_b64 exec, -1\n" \
  "s_add_u32 s72, s72, 0x1000\n" \
  "s_mov_b32 s68, 0\n" \
  ".LCW_X3_%=:\n" \
  "ds_read_b32 v5, v6 offset:16\n" \
  "ds_read_b128 v[24:27], v0 offset:30720\n" \
  "ds_read_b128 v[36:39], v1 offset:14336\n" \
  "v_mfma_f32_16x16x32_bf16 v[42:45], v[12:15], v[112:115], v[42:45]\n" \
  "v_mfma_f32_16x16x32_bf16 v[48:51], v[12:15], v[176:179], v[48:51]\n" \
  "v_mfma_f32_16x16x32_bf16 v[54:57], v[12:15], v[240:243], v[54:57]\n" \
  "s_waitcnt lgkmcnt(5)\n" \
  "v_mfma_f32_16x16x32_bf16 v[60:63], v[12:15], v[28:31], v[60:63]\n" \
  "ds_read_b128 v[12:15], v0 offset:256\n" \
  "ds_read_b128 v[28:31], v1 offset:15360\n" \
  "v_mfma_f32_16x16x32_bf16 v[42:45], v[16:19], v[116:119], v[42:45]\n" \
  "v_mfma_f32_16x16x32_bf16 v[48:51], v[16:19], v[180:183], v[48:51]\n" \
  "v_mfma_f32_16x16x32_bf16 v[54:57], v[16:19], v[244:247], v[54:57]\n" \
  "s_waitcnt lgkmcnt(5)\n" \
  "v_mfma_f32_16x16x32_bf16 v[60:63], v[16:19], v[32:35], v[60:63]\n" \
  "s_waitcnt lgkmcnt(4)\n" \
  "v_readfirstlane_b32 s71, v5\n" \
  "s_cmp_ge_u32 s71, s67\n" \
  "s_cbranch_scc1 .LCW_ST_%=\n" \
  ".LCW_S4_%=:\n" \
  "s_sleep 1\n" \
  "ds_read_b32 v5, v6 offset:16\n" \
  "s_sub_u32 s69, s69, 1\n" \
  "s_cbranch_scc1 .LCW_END_%=\n" \
  "s_waitcnt lgkmcnt(0)\n" \
  "v_readfirstlane_b32 s71, v5\n" \
  "s_cmp_ge_u32 s71, s67\n" \
  "s_cbranch_scc1 .LCW_ST_%=\n" \
  "s_branch .LCW_S4_%=\n" \
  ".LCW_ST_%=:\n" \
  "ds_read_b128 v[16:19], v0 offset:2304\n" \
  "ds_read_b128 v[32:35], v3 offset:0\n" \
  "v_mfma_f32_16x16x32_bf16 v[42:45], v[20:23], v[120:123], v[42:45]\n" \
  "v_mfma_f32_16x16x32_bf16 v[48:51], v[20:23], v[184:187], v[48:51]\n" \
  "v_mfma_f32_16x16x32_bf16 v[54:57], v[20:23], v[248:251], v[54:57]\n" \
  "s_waitcnt lgkmcnt(4)\n" \
  "v_mfma_f32_16x16x32_bf16 v[60:63], v[20:23], v[36:39], v[60:63]\n" \
  "ds_read_b128 v[20:23], v0 offset:4352\n" \
  "ds_read_b128 v[36:39], v3 offset:128\n" \
  "v_mfma_f32_16x16x32_bf16 v[42:45], v[24:27], v[124:127], v[42:45]\n" \
  "v_mfma_f32_16x16x32_bf16 v[48:51], v[24:27], v[188:191], v[48:51]\n" \
  "v_mfma_f32_16x16x32_bf16 v[54:57], v[24:27], v[252:255], v[54:57]\n" \
  "s_waitcnt lgkmcnt(4)\n" \
  "v_mfma_f32_16x16x32_bf16 v[60:63], v[24:27], v[28:31], v[60:63]\n" \
  "s_waitcnt lgkmcnt(0)\n" \
  "s_nop 4\n" \
  "v_fma_f32 v42, -v32, v8, v42\n" \
  "v_fma_f32 v43, -v33, v8, v43\n" \
  "v_fma_f32 v44, -v34, v8, v44\n" \
  "v_fma_f32 v45, -v35, v8, v45\n" \
  "v_fma_f32 v48, -v32, v9, v48\n" \
  "v_fma_f32 v49, -v33, v9, v49\n" \
  "v_fma_f32 v50, -v34, v9, v50\n" \
  "v_fma_f32 v51, -v35, v9, v51\n" \
  "v_fma_f32 v54, -v32, v10, v54\n" \
  "v_fma_f32 v55, -v33, v10, v55\n" \
  "v_fma_f32 v56, -v34, v10, v56\n" \
  "v_fma_f32 v57, -v35, v10, v57\n" \
  "v_fma_f32 v60, -v32, v4, v60\n" \
  "v_fma_f32 v61, -v33, v4, v61\n" \
  "v_fma_f32 v62, -v34, v4, v62\n" \
  "v_fma_f32 v63, -v35, v4, v63\n" \
  "ds_read_b128 v[32:35], v1 offset:0\n" \
  "v_mul_f32 v42, v42, v36\n" \
  "v_mul_f32 v43, v43, v37\n" \
  "v_mul_f32 v44, v44, v38\n" \
  "v_mul_f32 v45, v45, v39\n" \
  "v_mul_f32 v48, v48, v36\n" \
  "v_mul_f32 v49, v49, v37\n" \
  "v_mul_f32 v50, v50, v38\n" \
  "v_mul_f32 v51, v51, v39\n" \
  "v_mul_f32 v54, v54, v36\n" \
  "v_mul_f32 v55, v55, v37\n" \
  "v_mul_f32 v56, v56, v38\n" \
  "v_mul_f32 v57, v57, v39\n" \
  "v_mul_f32 v60, v60, v36\n" \
  "v_mul_f32 v61, v61, v37\n" \
  "v_mul_f32 v62, v62, v38\n" \
  "v_mul_f32 v63, v63, v39\n" \
  "ds_read_b128 v[36:39], v1 offset:1024\n" \
  "v_cvt_pk_bf16_f32 v40, v42, v43\n" \
  "v_cvt_pk_bf16_f32 v41, v44, v45\n" \
  "v_cvt_pk_bf16_f32 v46, v48, v49\n" \
  "v_cvt_pk_bf16_f32 v47, v50, v51\n" \
  "v_cvt_pk_bf16_f32 v52, v54, v55\n" \
  "v_cvt_pk_bf16_f32 v53, v56, v57\n" \
  "v_cvt_pk_bf16_f32 v58, v60, v61\n" \
  "v_cvt_pk_bf16_f32 v59, v62, v63\n" \
  "ds_read_b128 v[24:27], v0 offset:6400\n" \
  "ds_read_b128 v[28:31], v1 offset:2048\n" \
  "v_mfma_f32_16x16x32_bf16 v[42:45], v[12:15], v[64:67], 0\n" \
  "v_mfma_f32_16x16x32_bf16 v[48:51], v[12:15], v[128:131], 0\n" \
  "v_mfma_f32_16x16x32_bf16 v[54:57], v[12:15], v[192:195], 0\n" \
  "s_waitcnt lgkmcnt(3)\n" \
  "v_mfma_f32_16x16x32_bf16 v[60:63], v[12:15], v[32:35], 0\n" \
  "ds_read_b128 v[12:15], v0 offset:8448\n" \
  "ds_read_b128 v[32:35], v1 offset:3072\n" \
  "v_mfma_f32_16x16x32_bf16 v[42:45], v[16:19], v[68:71], v[42:45]\n" \
  "v_mfma_f32_16x16x32_bf16 v[48:51], v[16:19], v[132:135], v[48:51]\n" \
  "v_mfma_f32_16x16x32_bf16 v[54:57], v[16:19], v[196:199], v[54:57]\n" \
  "s_waitcnt lgkmcnt(4)\n" \
  "v_mfma_f32_16x16x32_bf16 v[60:63], v[16:19], v[36:39], v[60:63]\n" \
  "ds_read_b128 v[16:19], v0 offset:10496\n" \
  "ds_read_b128 v[36:39], v1 offset:4096\n" \
  "v_mfma_f32_16x16x32_bf16 v[42:45], v[20:23], v[72:75], v[42:45]\n" \
  "v_mfma_f32_16x16x32_bf16 v[48:51], v[20:23], v[136:139], v[48:51]\n" \
  "v_mfma_f32_16x16x32_bf16 v[54:57], v[20:23], v[200:203], v[54:57]\n" \
  "s_waitcnt lgkmcnt(4)\n" \
  "v_mfma_f32_16x16x32_bf16 v[60:63], v[20:23], v[28:31], v[60:63]\n" \
  "ds_read_b128 v[20:23], v0 offset:12544\n" \
  "ds_read_b128 v[28:31], v1 offset:5120\n" \
  "v_mfma_f32_16x16x32_bf16 v[42:45], v[24:27], v[76:79], v[42:45]\n" \
  "v_mfma_f32_16x16x32_bf16 v[48:51], v[24:27], v[140:143], v[48:51]\n" \
  "v_mfma_f32_16x16x32_bf16 v[54:57], v[24:27], v[204:207], v[54:57]\n" \
  "s_waitcnt lgkmcnt(4)\n" \
  "v_mfma_f32_16x16x32_bf16 v[60:63], v[24:27], v[32:35], v[60:63]\n" \
  "ds_read_b128 v[24:27], v0 offset:14592\n" \
  "ds_read_b128 v[32:35], v1 offset:6144\n" \
  "v_mfma_f32_16x16x32_bf16 v[42:45], v[12:15], v[80:83], v[42:45]\n" \
  "v_mfma_f32_16x16x32_bf16 v[48:51], v[12:15], v[144:147], v[48:51]\n" \
  "v_mfma_f32_16x16x32_bf16 v[54:57], v[12:15], v[208:211], v[54:57]\n" \
  "s_waitcnt lgkmcnt(4)\n" \
  "v_mfma_f32_16x16x32_bf16 v[60:63], v[12:15], v[36:39], v[60:63]\n" \
  "ds_read_b128 v[12:15], v0 offset:16640\n" \
  "ds_read_b128 v[36:39], v1 offset:7168\n" \
  "v_mfma_f32_16x16x32_bf16 v[42:45], v[16:19], v[84:87], v[42:45]\n" \
  "v_mfma_f32_16x16x32_bf16 v[48:51], v[16:19], v[148:151], v[48:51]\n" \
  "v_mfma_f32_16x16x32_bf16 v[54:57], v[16:19], v[212:215], v[54:57]\n" \
  "s_waitcnt lgkmcnt(4)\n" \
  "v_mfma_f32_16x16x32_bf16 v[60:63], v[16:19], v[28:31], v[60:63]\n" \
  "ds_read_b128 v[16:19], v0 offset:18688\n" \
  "ds_read_b128 v[28:31], v1 offset:8192\n" \
  "v_mfma_f32_16x16x32_bf16 v[42:45], v[20:23], v[88:91], v[42:45]\n" \
  "v_mfma_f32_16x16x32_bf16 v[48:51], v[20:23], v[152:155], v[48:51]\n" \
  "v_mfma_f32_16x16x32_bf16 v[54:57], v[20:23], v[216:219], v[54:57]\n" \
  "s_waitcnt lgkmcnt(4)\n" \
  "v_mfma_f32_16x16x32_bf16 v[60:63], v[20:23], v[32:35], v[60:63]\n" \
  "ds_read_b128 v[20:23], v0 offset:20736\n" \
  "ds_read_b128 v[32:35], v1 offset:9216\n" \
  "v_mfma_f32_16x16x32_bf16 v[42:45], v[24:27], v[92:95], v[42:45]\n" \
  "v_mfma_f32_16x16x32_bf16 v[48:51], v[24:27], v[156:159], v[48:51]\n" \
  "v_mfma_f32_16x16x32_bf16 v[54:57], v[24:27], v[220:223], v[54:57]\n" \
  "s_waitcnt lgkmcnt(4)\n" \
  "v_mfma_f32_16x16x32_bf16 v[60:63], v[24:27], v[36:39], v[60:63]\n" \
  "ds_read_b128 v[24:27], v0 offset:22784\n" \
  "ds_read_b128 v[36:39], v1 offset:10240\n" \
  "v_mfma_f32_16x16x32_bf16 v[42:45], v[12:15], v[96:99], v[42:45]\n" \
  "v_mfma_f32_16x16x32_bf16 v[48:51], v[12:15], v[160:163], v[48:51]\n" \
  "v_mfma_f32_16x16x32_bf16 v[54:57], v[12:15], v[224:227], v[54:57]\n" \
  "s_waitcnt lgkmcnt(4)\n" \
  "v_mfma_f32_16x16x32_bf16 v[60:63], v[12:15], v[28:31], v[60:63]\n" \
  "ds_read_b128 v[12:15], v0 offset:24832\n" \
  "ds_read_b128 v[28:31], v1 offset:11264\n" \
  "v_mfma_f32_16x16x32_bf16 v[42:45], v[16:19], v[100:103], v[42:45]\n" \
  "v_mfma_f32_16x16x32_bf16 v[48:51], v[16:19], v[164:167], v[48:51]\n" \
  "v_mfma_f32_16x16x32_bf16 v[54:57], v[16:19], v[228:231], v[54:57]\n" \
  "s_waitcnt lgkmcnt(4)\n" \
  "v_mfma_f32_16x16x32_bf16 v[60:63], v[16:19], v[32:35], v[60:63]\n" \
  "ds_read_b128 v[16:19], v0 offset:26880\n" \
  "ds_read_b128 v[32:35], v1 offset:12288\n" \
  "v_mfma_f32_16x16x32_bf16 v[42:45], v[20:23], v[104:107], v[42:45]\n" \
  "v_mfma_f32_16x16x32_bf16 v[48:51], v[20:23], v[168:171], v[48:51]\n" \
  "v_mfma_f32_16x16x32_bf16 v[54:57], v[20:23], v[232:235], v[54:57]\n" \
  "s_waitcnt lgkmcnt(4)\n" \
  "v_mfma_f32_16x16x32_bf16 v[60:63], v[20:23], v[36:39], v[60:63]\n" \
  "ds_read_b128 v[20:23], v0 offset:28928\n" \
  "ds_read_b128 v[36:39], v1 offset:13312\n" \
  "v_mfma_f32_16x16x32_bf16 v[42:45], v[24:27], v[108:111], v[42:45]\n" \
  "v_mfma_f32_16x16x32_bf16 v[48:51], v[24:27], v[172:175], v[48:51]\n" \
  "v_mfma_f32_16x16x32_bf16 v[54:57], v[24:27], v[236:239], v[54:57]\n" \
  "s_waitcnt lgkmcnt(4)\n" \
  "v_mfma_f32_16x16x32_bf16 v[60:63], v[24:27], v[28:31], v[60:63]\n" \
  "ds_read_b128 v[24:27], v0 offset:30976\n" \
  "ds_read_b128 v[28:31], v1 offset:14336\n" \
  "v_mfma_f32_16x16x32_bf16 v[42:45], v[12:15], v[112:115], v[42:45]\n" \
  "v_mfma_f32_16x16x32_bf16 v[48:51], v[12:15], v[176:179], v[48:51]\n" \
  "v_mfma_f32_16x16x32_bf16 v[54:57], v[12:15], v[240:243], v[54:57]\n" \
  "s_waitcnt lgkmcnt(4)\n" \
  "v_mfma_f32_16x16x32_bf16 v[60:63], v[12:15], v[32:35], v[60:63]\n" \
  "ds_read_b128 v[32:35], v1 offset:15360\n" \
  "v_mfma_f32_16x16x32_bf16 v[42:45], v[16:19], v[116:119], v[42:45]\n" \
  "v_mfma_f32_16x16x32_bf16 v[48:51], v[16:19], v[180:183], v[48:51]\n" \
  "v_mfma_f32_16x16x32_bf16 v[54:57], v[16:19], v[244:247], v[54:57]\n" \
  "s_waitcnt lgkmcnt(3)\n" \
  "v_mfma_f32_16x16x32_bf16 v[60:63], v[16:19], v[36:39], v[60:63]\n" \
  "ds_read_b128 v[36:39], v3 offset:64\n" \
  "v_mfma_f32_16x16x32_bf16 v[42:45], v[20:23], v[120:123], v[42:45]\n" \
  "v_mfma_f32_16x16x32_bf16 v[48:51], v[20:23], v[184:187], v[48:51]\n" \
  "v_mfma_f32_16x16x32_bf16 v[54:57], v[20:23], v[248:251], v[54:57]\n" \
  "s_waitcnt lgkmcnt(2)\n" \
  "v_mfma_f32_16x16x32_bf16 v[60:63], v[20:23], v[28:31], v[60:63]\n" \
  "ds_read_b128 v[28:31], v3 offset:192\n" \
  "s_mov_b64 exec, 1\n" \
  "ds_add_u32 v6, v7 offset:8\n" \
  "s_mov_b64 exec, -1\n" \
  "v_xor_b32 v6, 4, v6\n" \
  "v_mfma_f32_16x16x32_bf16 v[42:45], v[24:27], v[124:127], v[42:45]\n" \
  "v_mfma_f32_16x16x32_bf16 v[48:51], v[24:27], v[188:191], v[48:51]\n" \
  "v_mfma_f32_16x16x32_bf16 v[54:57], v[24:27], v[252:255], v[54:57]\n" \
  "s_waitcnt lgkmcnt(3)\n" \
  "v_mfma_f32_16x16x32_bf16 v[60:63], v[24:27], v[32:35], v[60:63]\n" \
  "s_waitcnt lgkmcnt(1)\n" \
  "s_nop 4\n" \
  "ds_read_b32 v5, v6 offset:0\n" \
  "v_fma_f32 v42, -v36, v8, v42\n" \
  "v_fma_f32 v43, -v37, v8, v43\n" \
  "v_fma_f32 v44, -v38, v8, v44\n" \
  "v_fma_f32 v45, -v39, v8, v45\n" \
  "v_mul_f32 v42, v42, v28\n" \
  "v_mul_f32 v43, v43, v29\n" \
  "v_mul_f32 v44, v44, v30\n" \
  "v_mul_f32 v45, v45, v31\n" \
  "v_cvt_pk_bf16_f32 v42, v42, v43\n" \
  "v_cvt_pk_bf16_f32 v43, v44, v45\n" \
  "buffer_store_dwordx4 v[40:43], v2, s[40:43], s65 offen offset:0 sc1\n" \
  "v_fma_f32 v48, -v36, v9, v48\n" \
  "v_fma_f32 v49, -v37, v9, v49\n" \
  "v_fma_f32 v50, -v38, v9, v50\n" \
  "v_fma_f32 v51, -v39, v9, v51\n" \
  "v_mul_f32 v48, v48, v28\n" \
  "v_mul_f32 v49, v49, v29\n" \
  "v_mul_f32 v50, v50, v30\n" \
  "v_mul_f32 v51, v51, v31\n" \
  "v_cvt_pk_bf16_f32 v48, v48, v49\n" \
  "v_cvt_pk_bf16_f32 v49, v50, v51\n" \
  "buffer_store_dwordx4 v[46:49], v2, s[40:43], s65 offen offset:1024 sc1\n" \
  "v_fma_f32 v54, -v36, v10, v54\n" \
  "v_fma_f32 v55, -v37, v10, v55\n" \
  "v_fma_f32 v56, -v38, v10, v56\n" \
  "v_fma_f32 v57, -v39, v10, v57\n" \
  "v_mul_f32 v54, v54, v28\n" \
  "v_mul_f32 v55, v55, v29\n" \
  "v_mul_f32 v56, v56, v30\n" \
  "v_mul_f32 v57, v57, v31\n" \
  "v_cvt_pk_bf16_f32 v54, v54, v55\n" \
  "v_cvt_pk_bf16_f32 v55, v56, v57\n" \
  "buffer_store_dwordx4 v[52:55], v2, s[40:43], s65 offen offset:2048 sc1\n" \
  "v_fma_f32 v60, -v36, v4, v60\n" \
  "v_fma_f32 v61, -v37, v4, v61\n" \
  "v_fma_f32 v62, -v38, v4, v62\n" \
  "v_fma_f32 v63, -v39, v4, v63\n" \
  "v_mul_f32 v60, v60, v28\n" \
  "v_mul_f32 v61, v61, v29\n" \
  "v_mul_f32 v62, v62, v30\n" \
  "v_mul_f32 v63, v63, v31\n" \
  "v_cvt_pk_bf16_f32 v60, v60, v61\n" \
  "v_cvt_pk_bf16_f32 v61, v62, v63\n" \
  "buffer_store_dwordx4 v[58:61], v2, s[40:43], s65 offen offset:3072 sc1\n" \
  "s_waitcnt lgkmcnt(0)\n" \
  "s_add_u32 s65, s65, 0x40000\n" \
  "s_sub_u32 s64, s64, 1\n" \
  "s_mov_b32 s68, 1\n" \
  "s_cmp_lg_u32 s64, 0\n" \
  "s_cbranch_scc1 .LCW_TOP_%=\n" \
  "s_waitcnt vmcnt(0)\n" \
  "s_mov_b64 exec, 1\n" \
  "buffer_store_dword v7, off, s[56:59], s72 sc1\n" \
  "s_mov_b64 exec, -1\n" \
  "s_add_u32 s72, s72, 0x1000\n" \
  "s_mov_b32 s68, 0\n" \
  ".LCW_END_%=:\n" \
  "s_endpgm\n"
constexpr int RG = 2;
#define RFL(x) __builtin_amdgcn_readfirstlane(x)
#define HALF(x, i) BF8(((i) ? __builtin_shufflevector(x, x, 4, 5, 6, 7) : __builtin_shufflevector(x, x, 0, 1, 2, 3)))
constexpr int P_W = 65536, P_ST = 131072, P_SS = P_ST + 1024, P_CN = P_SS + 512;
template <bool PR>
__device__ __forceinline__ void role_proj_t(const MegaArgs& A, char* smem, const int L, const int idx, const bool kv) {
  const int w = A.wv;
  const int l = lane_id();
  const int c = l & 15, g = l >> 4;
  char* ws = A.ws;
  unsigned* FL = (unsigned*)(ws + OFF_FL);
  unsigned* TMO = FL + FL_TMO;
  const unsigned sb = lds_base(smem);
  auto cn = (volatile __attribute__((address_space(3))) unsigned*)(unsigned long)(sb + P_CN);
  u64 tag;
  {
    const int j = w & 3;
    const bool vt = kv && j >= 2;
    constexpr int NT = PR ? 3 : 1;
    v8u wq[NT][8];
    const u16* Wm = (kv ? (PR ? A.Wv : A.Wk) : A.Wq) - (PR ? 128 : 0) * HD;
    const int hm = kv ? idx : 2 * idx + PR;
    const int q = (l >> 2) & 3, p = l & 3, f = q | ((g & 1) << 2);
    unsigned ra[NT];
#pragma unroll
    for (int m = 0; m < NT; ++m) {
      const int ct = PR ? m : 3;
      ra[m] = sb + (j >> 1) * 65536 + (8 * g + q) * 256 + ((4 * (j & 1) + (vt ? ct : (ct & 2) + (p >> 1))) ^ f) * 32 + (vt ? p * 8 : (p & 1) * 16 + (ct & 1) * 8);
    }
#define W_ISSUE(n) wqkv_issue<false>(Wm + (128 * (n) - 256 * ((n) & 1)) * HD, (n) & 1, hm, w, l, sb)
#define W_STAGE(n, WAIT, NEXT)                                                                             \
  {                                                                                                        \
    WAIT;                                                                                                  \
    RAW_BAR();                                                                                             \
    _Pragma("unroll") for (int m = 0; m < NT; ++m) {                                                       \
      _Pragma("unroll") for (int t = 0; t < 2; ++t) {                                                      \
        const unsigned a_ = ra[m] + ((n) & 1) * 8192 + t * 32768;                                          \
        const v2u r0 = trd(a_), r1 = trd(a_ + 1024), r2 = trd(a_ + 16384), r3 = trd(a_ + 16384 + 1024);    \
        wq[m][2 * (n) + t] = v8u{r0.x, r0.y, r1.x, r1.y, r2.x, r2.y, r3.x, r3.y};                          \
      }                                                                                                    \
    }                                                                                                      \
    _Pragma("unroll") for (int m = 0; m < 2 * NT; ++m) asm volatile("" : "+v"(wq[m >> 1][2 * (n) + (m & 1)])); \
    RAW_BAR();                                                                                             \
    NEXT;                                                                                                  \
    _Pragma("unroll") for (int m = 0; m < 4 * NT; ++m)                                                     \
      cs[m % NT] = MFMA16(HALF(wq[m % NT][2 * (n) + m / NT / 2], (m / NT) & 1), BF8(one), cs[m % NT]);     \
    _Pragma("unroll") for (int m = 0; m < NT; ++m) asm volatile("" : "+v"(cs[m]));                         \
  }
    __builtin_amdgcn_s_waitcnt(0x0f70);
    W_ISSUE(0);
    W_ISSUE(1);
    tag = call_tag(A);
    if (w == 0 && l < 8) cn[l] = l == 1 ? 3u : 0u;
    const v4u one = {0x3f803f80u, 0x3f803f80u, 0x3f803f80u, 0x3f803f80u};
    f32x4 cs[NT] = {};
    W_STAGE(0, WAIT_VM(8), W_ISSUE(2));
    SST(4);
    W_STAGE(1, WAIT_VM(8), W_ISSUE(3));
    SST(5);
    W_STAGE(2, WAIT_VM(8), (void)0);
    SST(6);
    ready_wait(A, tag, ready_look(A));
    W_STAGE(3, WAIT_VM(0), if (!PR) {
      _Pragma("unroll") for (int ks = 0; ks < 16; ++ks)
        *reinterpret_cast<bf16x8*>(smem + P_W + j * 16384 + ks * 1024 + l * 16) = HALF(wq[0][ks >> 1], ks & 1);
    });
    SST(7);
#undef W_STAGE
#undef W_ISSUE
#pragma unroll
    for (int m = 0; m < NT; ++m) *reinterpret_cast<f32x4*>(smem + P_ST + j * 256 + (PR ? m : 3) * 64 + g * 16) = cs[m];
    RAW_BAR();
    SST(8);
    if constexpr (PR) {
      const v4u rsO = mk_srd(ws + RFL((unsigned)(vt ? OFF_VF + L * SZ_O : OFF_QK + L * SZ_QK)), RFL((unsigned)(vt ? SZ_O : SZ_QK)));
      const v4u rsP = mk_srd(FL + RFL(kv ? FL_FQ + (L * 64 * 32 + idx) * 32 + 2 * j : FL_FP + (L * 16 + idx) * 32 + j), 63 * 4096 + 4);
      const int so = RFL(vt ? idx * 8 + 4 * (j & 1) : (kv ? idx * 16 + 8 : (2 * idx + (j >> 1)) * 16) + 2 * (j & 1));
      const v8u pv = {sb + 32768 + g * 512 + c * 16, sb + P_W + j * 16384 + l * 16, (unsigned)l * 16, sb + P_SS + 256 + (vt ? g * 16 : c * 4),
                      sb + P_ST + j * 256 + (vt ? c * 4 : g * 16), 0u, 0u, 0u};
      const char* dbg = nullptr;
#define P_OPS                                                                                                                               \
  ::"{v[64:71]}"(wq[0][0]), "{v[72:79]}"(wq[0][1]), "{v[80:87]}"(wq[0][2]), "{v[88:95]}"(wq[0][3]), "{v[96:103]}"(wq[0][4]),                \
      "{v[104:111]}"(wq[0][5]), "{v[112:119]}"(wq[0][6]), "{v[120:127]}"(wq[0][7]), "{v[128:135]}"(wq[1][0]), "{v[136:143]}"(wq[1][1]),     \
      "{v[144:151]}"(wq[1][2]), "{v[152:159]}"(wq[1][3]), "{v[160:167]}"(wq[1][4]), "{v[168:175]}"(wq[1][5]), "{v[176:183]}"(wq[1][6]),     \
      "{v[184:191]}"(wq[1][7]), "{v[192:199]}"(wq[2][0]), "{v[200:207]}"(wq[2][1]), "{v[208:215]}"(wq[2][2]), "{v[216:223]}"(wq[2][3]),     \
      "{v[224:231]}"(wq[2][4]), "{v[232:239]}"(wq[2][5]), "{v[240:247]}"(wq[2][6]), "{v[248:255]}"(wq[2][7]), "{v[0:7]}"(pv),               \
      "{s[40:43]}"(rsO), "{s[56:59]}"(rsP), "{s64}"(NSEG), "{s65}"(so * 1024), "{s70}"(sb + P_CN), "{s[36:37]}"(dbg)                        \
      : "memory", "vcc", "scc", "s80", "v63"
      if (vt) asm volatile("s_mov_b64 exec, -1\ns_waitcnt vmcnt(0) lgkmcnt(0)\ns_nop 4\n" V_ASM P_OPS);
      else asm volatile("s_mov_b64 exec, -1\ns_waitcnt vmcnt(0) lgkmcnt(0)\ns_nop 4\n" P_ASM P_OPS);
#undef P_OPS
      return;
    }
  }
  __builtin_amdgcn_s_setprio(RPRIO);
  const bool h4 = kv && L;
  if (h4 || w >= 2) {
    const char* Xb = ws + (L ? OFF_X3 : OFF_XA);
    const __amdgpu_buffer_rsrc_t rsS = MKRS(ws + OFF_H + L * (SZ_X / 2), SZ_X / 2);
    const __amdgpu_buffer_rsrc_t rsM = MKRS(ws + OFF_MEMK + L * SZ_MEM, kv && w == 2 ? 3 * SZ_MEM : 0);
    const int b0 = RFL(h4 ? 3 * w : w == 2 ? 0 : HX0), b1 = RFL(h4 ? (w == 2 ? 8 : 3 * w + 3 * (w < 2)) : w == 2 ? HX0 : 8);
    const __amdgpu_buffer_rsrc_t rsF = MKRS(FL + FL_FC + L * 64 * 32 * 32, 64 * 4096);
    const int fo = ((blockIdx.x & 3) * 8 + (w == 3 ? l >> 3 : min(b0 + (l >> 3), b1 - 1))) * 128 + (l & 7) * 4;
#define FC_LOOK(s) __builtin_amdgcn_raw_buffer_load_b32(rsF, fo, (s) * 4096, AUX_SC1)
#define MEM_CLEAR(s)                                                                                                          \
  _Pragma("unroll") for (int i = 0; i < 4; ++i)                                                                               \
    ST128((v4u{0u, 0u, 0u, 0u}), rsM, l * 16 + (i & 1) * 1024, (s) * 65536 + idx * 2048 + (i >> 1) * (int)(OFF_MEMV - OFF_MEMK));
    if (kv && w == 2) { MEM_CLEAR(0) }
    unsigned tn = now10(), nf = 0u, tg = tn;
    for (int s = 0; s < NSEG; ++s) {
      for (unsigned spins = 0; !__all(nf != 0u); ++spins) {
        if (spin_timeout(spins, TMO)) return;
        if (spins) __builtin_amdgcn_s_sleep(SLP_H);
        else if (GF_H && (h4 || (GF_P && L)) && s >= GF0 && s < NSEG - 6) gate(tg);
        tg = now10() + GF_H;
        nf = FC_LOOK(s);
        asm volatile("" ::: "memory");
      }
      TS(4, s, 128);
      for (unsigned spins = 0; cn[2 + (s & 1)] < 4u * (s >> 1); ++spins) {
        if (spins > 64u * SPIN_CAP) return;
        __builtin_amdgcn_s_sleep(2);
      }
      if (L == 0 && s >= PACE0) gate(tn);
      else tn = now10();
      tn += PACE;
      TS(5, s, 128);
      for (int i = b0; i < b1; ++i) glds64c(Xb + RFL(s * 32768 + i * 4096), l * 16, RFL(sb + (s & 1) * 32768 + i * 4096));
      if (w != 3) {
        nf = L ? 0u : FC_LOOK(s + 1);
        if (kv && w == 2 && s < NSEG - 1) {
          MEM_CLEAR(s + 1)
          WAIT_VM(4);
        } else {
          WAIT_VM(0);
        }
        if (l == 0) __hip_atomic_fetch_add((__attribute__((address_space(3))) unsigned*)cn + (s & 1), h4 ? 2u : 3u, __ATOMIC_RELAXED, __HIP_MEMORY_SCOPE_WORKGROUP);
        TS(6, s, 128);
      } else {
        v4u t[16];
#pragma unroll
        for (int i = 0; i < 16; ++i) t[i] = LD128(rsS, l * 16, s * 16384 + i * 1024);
        WAIT_VM(16);
        nf = L ? 0u : FC_LOOK(s + 1);
        if (l == 0 && !h4) __hip_atomic_fetch_add((__attribute__((address_space(3))) unsigned*)cn + (s & 1), 3u, __ATOMIC_RELAXED, __HIP_MEMORY_SCOPE_WORKGROUP);
        float a = 0.f, b = 0.f, qa = 0.f, qb = 0.f;
#pragma unroll
        for (int i = 0; i < 16; ++i) {
          a = one(a + __uint_as_float(t[i].x));
          b = one(b + __uint_as_float(t[i].z));
          qa = one(qa + __uint_as_float(t[i].y));
          qb = one(qb + __uint_as_float(t[i].w));
        }
        a = xsum32(xsum16(a));
        b = xsum32(xsum16(b));
        float da = 0.f, db = 0.f;
#pragma unroll
        for (int i = 0; i < 16; ++i) {
          const float u = one(__uint_as_float(t[i].x) - a * (1.0f / 64)), v = one(__uint_as_float(t[i].z) - b * (1.0f / 64));
          da = one(da + u * u);
          db = one(db + v * v);
        }
        const float var = xsum32(swap_add16(qa + da * 0.125f, qb + db * 0.125f)) * (1.0f / DM);
        const float ri = 1.0f / bfr1(sqrtf(bfr1(bfr1(var) + 1e-5f)));
        reinterpret_cast<float*>(smem + P_SS)[(s & 1) * 64 + (g >> 1) * 32 + 2 * c + (g & 1)] = g < 2 ? bfr1(((g & 1) ? b : a) * (1.0f / DM)) : ri;
        asm volatile("" ::: "memory");
        if (l == 0) cn[4 + (s & 1)] = s + 1;
        TS(7, s, 192);
      }
    }
    return;
  }
  const int RL = !kv, rp = idx & 15, u = RFL(kv ? idx >> 4 : L);
  const __amdgpu_buffer_rsrc_t rsW = MKRS(ws + OFF_PW, SZ_PW);
  const __amdgpu_buffer_rsrc_t rsX = MKRS(ws + (RL ? OFF_X3 : OFF_XA), SZ_X);
  const __amdgpu_buffer_rsrc_t rsY = RL ? MKRS(A.out, SZ_X) : MKRS(ws + OFF_X2, SZ_X);
  const int r = 2 * rp + (g >> 1), yo = r * (DM * 2) + (l & 31) * 16 + u * 512;
  if (kv) {
    for (int s = w; s < NSEG; s += RG) ST128((v4u{0u, 0u, 0u, 0u}), rsY, yo, s * SEG * DM * 2);
    DRAIN();
    if (l < 8) set_flag(FL + FL_FZ + l * 64 + 2 * idx + w);
  }
  v4u sel;
#pragma unroll
  for (int e = 0; e < 4; ++e) sel[e] = (g >> 1) == (c >> 3) && ((c & 7) >> 1) == e ? 0x3f80u << (16 * (c & 1)) : 0u;
  unsigned tl = now10();
  for (int s = w; s < NSEG; s += RG) {
    if (GF_R) {
      gate(tl);
      const gu32* f = (gu32*)(FL + FL_FW) + ((RL * 64 + s) * 4 + (blockIdx.x & 3)) * 4 * 32 + (c >> 2) * 32 + (c & 3);
      for (unsigned spins = 0;; ++spins) {
        tl = now10() + GF_R;
        if (__all(__hip_atomic_load(f, RLX_AGENT) != 0u) || spin_timeout(spins, TMO)) break;
        __builtin_amdgcn_s_sleep(SLP_R);
      }
      asm volatile("" ::: "memory");
    } else
    grp_wait<SLP_R, 4, GATE_R>(true, nullptr, 0, FL + FL_FW + ((RL * 64 + s) * 4 + (blockIdx.x & 3)) * 4 * 32, 16, TMO, tl);
    if (s >= NSEG - 6) tl = now10();
    TS(0, s, w * 64);
    v4u p[8][2];
    const v4u xr = LD128(rsX, (l & 31) * 512 + r * 16, s * 32768 + u * 16384);
#pragma unroll
    for (int k = 0; k < 8; ++k)
#pragma unroll
      for (int b = 0; b < 2; ++b)
        p[k][b] = LD128(rsW, (g & 1) * 32768 + (c >> 2) * 128 + (g >> 1) * 64 + (c & 3) * 16 + b * 512, ((RL * 64 + s) * 16 + 2 * k) * 32768 + rp * 2048 + u * 1024);
    __builtin_amdgcn_sched_barrier(0);
    f32x4 ac[2] = {};
#pragma unroll
    for (int k = 0; k < 8; ++k)
#pragma unroll
      for (int b = 0; b < 2; ++b) ac[b] = MFMA16(BF8(sel), BF8(p[k][b]), ac[b]);
    v4u xo;
    {
      const auto x = __builtin_amdgcn_permlane16_swap(pk2s(ac[0][0], ac[0][1]), pk2s(ac[1][0], ac[1][1]), false, false);
      const auto y = __builtin_amdgcn_permlane16_swap(pk2s(ac[0][2], ac[0][3]), pk2s(ac[1][2], ac[1][3]), false, false);
      const v4u a = {x[0], y[0], x[1], y[1]};
#pragma unroll
      for (int e = 0; e < 4; ++e) {
        xo[e] = pk2s(lo16(xr[e]) + lo16(a[e]), hi16(xr[e]) + hi16(a[e]));
        if (kv) xo[e] = xo[e] ? xo[e] : 0x8000u;
      }
      ST128(xo, rsY, yo, s * SEG * DM * 2);
    }
    TS(1, s, w * 64);
    if (kv) {
      const float pv = lo16(g < 2 ? __builtin_amdgcn_readlane(xo[0], 0) : __builtin_amdgcn_readlane(xo[0], 32));
      float sm = 0.f, q0 = 0.f, q1 = 0.f;
#pragma unroll
      for (int e = 0; e < 4; ++e) {
        const float a = one(lo16(xo[e]) - pv), b = one(hi16(xo[e]) - pv);
        sm = one(sm + one(a + b));
        q0 = one(q0 + a * a);
        q1 = one(q1 + b * b);
      }
      sm = half_sum(sm);
      const float q = fmaxf(one(half_sum(q0 + q1)) - sm * sm * (1.0f / 256), 0.f);
      const u64 st = (u64)__float_as_uint(sm + pv * 256.f) | (u64)(__float_as_uint(q) | 1u) << 32;
      if ((l & 31) < 4) __hip_atomic_store((gu64*)(FL + FL_FR + ((s * 4 + (l & 3)) * 4 + (r >> 3)) * 32 + (r & 7) * 4 + 2 * u), st, RLX_AGENT);
    }
    TS(3, s, w * 64);
  }
  if (L == 1 && idx == 0 && w == 0 && l == 0) __hip_atomic_store((gu64*)(ws + OFF_EP), tag, RLX_AGENT);
}
__device__ __forceinline__ void role_proj(const MegaArgs& A, char* smem, const int L, const int idx, const bool kv) {
  if (A.wv < 4) role_proj_t<false>(A, smem, L, idx, kv);
  else role_proj_t<true>(A, smem, L, idx, kv);
}

constexpr int CUN = 2144;
__device__ __forceinline__ v2u part_stats(const v4u o) {
  float v[8];
#pragma unroll
  for (int e = 0; e < 4; ++e) { v[2 * e] = lo16(o[e]); v[2 * e + 1] = hi16(o[e]); }
  const float s = ((v[0] + v[1]) + (v[2] + v[3])) + ((v[4] + v[5]) + (v[6] + v[7])), m = s * 0.125f;
  float q0 = 0.f, q1 = 0.f;
#pragma unroll
  for (int e = 0; e < 4; ++e) {
    const float a = v[e] - m, b = v[4 + e] - m;
    q0 += a * a;
    q1 += b * b;
  }
  return v2u{__float_as_uint(s), __float_as_uint(q0 + q1)};
}
constexpr int CRN = 176;
constexpr int CTN = 272;
constexpr int CV_LDS = (8 * CKN + 8 * 2 * CUN + 64 + 2 * 32 * 8 + 16 * CRN + 64 + 8 * CTN) * 2 + 8 * 1568;
#define MFMAH(a, b, c) __builtin_amdgcn_mfma_f32_16x16x32_f16(a, b, c, 0, 0, 0)
__device__ __forceinline__ void role_conv(const MegaArgs& A, char* smem, const int cv) {
  u16* Kr = reinterpret_cast<u16*>(smem);
  u16* Uc = Kr + 8 * CKN;
  u16* Us = Uc + 8 * CUN + 63;
  u16* To = Uc + 8 * 2 * CUN + 64;
  u16* Sx = To + 32 * 8;
  u16* Rc = Sx + 32 * 8;
  u16* Rs = Rc + 8 * CRN + 63;
  u16* Ta = Rc + 16 * CRN + 64;
  const int w = A.wv;
  const int l = lane_id(), tid = 64 * w + l, c = l & 15, g = l >> 4;
  const int d = 8 * cv + w;
  char* ws = A.ws;
  unsigned* FL = (unsigned*)(ws + OFF_FL);
  unsigned* TMO = FL + FL_TMO;
  u16* kr = Kr + w * CKN;
  const u16* ka = kr + 2080 - 16 * c + 8 * g;
#define KFRAG(D0) (*reinterpret_cast<const f16x8*>(ka - (D0)))
  typedef __attribute__((address_space(3))) const unsigned* lpu;
#define BFRAG(a) __builtin_bit_cast(f16x8, (v4u{((lpu)(unsigned long)(a))[0], ((lpu)(unsigned long)(a))[1], ((lpu)(unsigned long)(a))[2], ((lpu)(unsigned long)(a))[3]}))
  f32x4 acc[8];
  v4u tx[4];
  const __amdgpu_buffer_rsrc_t rz1 = MKRS(ws + OFF_XA, SZ_X), rps = MKRS(ws + OFF_H, SZ_X);
  u16* uc = Uc + w * CUN;
  const unsigned ub = lds_base(reinterpret_cast<char*>(((c & 1) ? Us : Uc) + w * CUN + c + 8 * g + 32));
  const int t0 = 16 * c + 4 * g;
  float dsk, ysc;
#define SKIP(T) (*reinterpret_cast<const v2u*>(uc + 32 + 256 * (T) + t0))
#define TILE_OUT(dst, a, uu)                                                                   \
  {                                                                                            \
    v2u v_;                                                                                    \
    _Pragma("unroll") for (int e = 0; e < 2; ++e) {                                            \
      const unsigned yy = pk2(a[2 * e] * ysc, a[2 * e + 1] * ysc), u_ = uu[e];                 \
      const unsigned k_ = pk2(h2f((u16)u_) * dsk, h2f((u16)(u_ >> 16)) * dsk);                 \
      v_[e] = pk2(lo16(yy) + lo16(k_), hi16(yy) + hi16(k_));                                   \
    }                                                                                          \
    *reinterpret_cast<v2u*>((dst) + t0) = v_;                                                  \
  }
#define CHUNK_ST(i, src, N)                                                                    \
  {                                                                                            \
    const unsigned p_ = lds_base((char*)((src) + (c >> 2) * (N) + (tid & ~15) + 4 * (c & 3))); \
    const v2u a0_ = trd(p_), a1_ = trd(p_ + 8 * (N));                                          \
    const v4u a_ = {a0_.x, a0_.y, a1_.x, a1_.y};                                               \
    v4u o;                                                                                     \
    _Pragma("unroll") for (int e = 0; e < 4; ++e) o[e] = pk2(lo16(tx[i][e]) + lo16(a_[e]), hi16(tx[i][e]) + hi16(a_[e])); \
    ST128(o, rz1, (l >> 5) * 32768 + (l & 31) * 16, (2 * (w + 8 * (i)) * 64 + cv) * 512);      \
    ST64(part_stats(o), rps, (l >> 5) * 16384 + (l & 31) * 8, (2 * (w + 8 * (i)) * 64 + cv) * 256); \
  }
#define CHUNK_FLAG(i) set_flag(FL + FL_FC + (((2 * w + 16 * ((i) + (l >> 3)) + ((l >> 2) & 1)) * 4 + (l & 3)) * 8 + (cv >> 3)) * 32 + (cv & 7))
  {
    unsigned ta[3], k0p;
    v2u kn;
    u64 tag;
    {
      KGen G;
      G.load(A, d, l);
      const u16 pk = A.Dskip[d];
      asm volatile("" ::: "memory");
      tx[0] = *reinterpret_cast<const v4u*>(A.x0 + (size_t)tid * DM + 8 * cv);
      ask_slow(A, 16 * (w - 4) + (cv & 15), ta);
      tag = call_tag(A);
      G.setup();
      dsk = bf2f(pk) * (1.0f / 1024);
      G.st = smem + w * 16640;
      SST(0);
      G.columns(l);
      ysc = G.scale();
      const Cx z = G.power(1.f);
      G.rows(G.st + l * 8, cmul(G.C, z), z, 16);
      const f32x4 kt = G.tile(l);
      kn = v2u{pkh(kt.w, kt.z), pkh(kt.y, kt.x)};
      const float k0 = 2.0f * wave_sum(G.C.r.x + G.C.r.y);
      k0p = l == 0 ? pkh(k0, 0.f) : 0u;
    }
    SST(1);
    const __amdgpu_buffer_rsrc_t rst = MKRS(ws + OFF_GT, L_SEQ * 16);
#define U_VAL(i)                                                                               \
  {                                                                                            \
    const float mb_ = lo16(st[i]), ri_ = 1024.0f / hi16(st[i]);                                 \
    _Pragma("unroll") for (int e = 0; e < 4; ++e) {                                            \
      const unsigned t_ = pk2(lo16(tx[i][e]) - mb_, hi16(tx[i][e]) - mb_);                     \
      const unsigned q_ = pk2(lo16(t_) * ri_, hi16(t_) * ri_);                                 \
      uv[e] = pkh(lo16(q_), hi16(q_));                                                         \
    }                                                                                          \
  }
#define U_PUT(i)                                                                               \
  _Pragma("unroll") for (int e = 0; e < 4; ++e) {                                              \
    ((lph)(unsigned long)up0)[(2 * e) * CUN + 512 * (i)] = (u16)uv[e];                         \
    ((lph)(unsigned long)up0)[(2 * e + 1) * CUN + 512 * (i)] = (u16)(uv[e] >> 16);             \
    ((lph)(unsigned long)up1)[(2 * e) * CUN + 512 * (i)] = (u16)uv[e];                         \
    ((lph)(unsigned long)up1)[(2 * e + 1) * CUN + 512 * (i)] = (u16)(uv[e] >> 16);             \
  }
#define U_ROW(i) { U_VAL(i) U_PUT(i) }
    typedef __attribute__((address_space(3))) u16* lph;
    unsigned st[4], up0 = lds_base((char*)(Uc + 32 + tid)), up1 = lds_base((char*)(Us + 32 + tid));
    asm volatile("" : "+v"(up0), "+v"(up1), "+v"(kn), "+v"(k0p));
    v4u uv;
#define ST_GET(I0, I1)                                                                                                        \
  for (unsigned spins = 0; spins < SPIN_CAP; ++spins) {                                                                       \
    bool ok = true;                                                                                                           \
    v4u t_[4];                                                                                                                \
    _Pragma("unroll") for (int i = I0; i < I1; ++i) t_[i] = LD128(rst, (tid + 512 * i) * 16, 0);                              \
    _Pragma("unroll") for (int i = I0; i < I1; ++i) {                                                                         \
      ok = ok && has_tag(tag, t_[i].x, t_[i].y) && has_tag(tag, t_[i].z, t_[i].w);                                            \
      st[i] = __builtin_amdgcn_perm(t_[i].w, t_[i].y, 0x07060302);                                                            \
    }                                                                                                                         \
    if (__all(ok)) break;                                                                                                     \
    __builtin_amdgcn_s_sleep(4);                                                                                              \
    asm volatile("" ::: "memory");                                                                                            \
  }
    if (w < 4) {
      ST_GET(0, 2);
      U_VAL(0);
      asm volatile("" : "+v"(uv));
    }
#pragma unroll
    for (int i = 1; i < 4; ++i) tx[i] = *reinterpret_cast<const v4u*>(A.x0 + (size_t)(tid + 512 * i) * DM + 8 * cv);
    RAW_BAR();
    SST(2);
    if (w < 4) { U_PUT(0) }
    *reinterpret_cast<v2u*>(kr + 2076 - 16 * c - 4 * g) = kn;
    if (l < 38) reinterpret_cast<v4u*>(kr)[l < 32 ? 260 + l : l < 34 ? 194 + l : l - 34] = v4u{k0p, 0u, 0u, 0u};
    (l < 32 ? uc : Us + w * CUN - 32)[l] = 0;
    uc[32 + L_SEQ + l] = Us[w * CUN + 32 + L_SEQ + l] = 0;
    uc[32 + 256 + l] = Us[w * CUN + 32 + 256 + l] = 0;
    RAW_BAR();
    SST(11);
    {
      f32x4 a0 = {}, a1 = {};
      f16x8 bf[9], af[9];
#pragma unroll
      for (int b = 0; b < 9; ++b) {
        af[b] = KFRAG(32 - 32 * b);
        bf[b] = BFRAG(ub + 64 * (b - 1));
      }
      const v2u k0 = SKIP(0);
      __builtin_amdgcn_sched_barrier(0);
#pragma unroll
      for (int b = 0; b < 9; ++b) (b & 1 ? a1 : a0) = MFMAH(bf[b], af[b], (b & 1 ? a1 : a0));
      a0 += a1;
      TILE_OUT(Ta + w * CTN, a0, k0);
    }
    const u64 rv = ready_look(A);
    RAW_BAR();
    SST(3);
    if (w < 4) {
      CHUNK_ST(0, Ta, CTN);
      asm volatile("" : "+v"(tx[1]));
      U_ROW(1);
      DRAIN();
      ready_wait(A, tag, rv);
      if (l < 8) CHUNK_FLAG(0);
      ST_GET(2, 4);
    } else {
      ST_GET(0, 4);
      ready_wait(A, tag, rv);
      asm volatile("" : "+v"(tx[0]), "+v"(tx[1]));
      U_ROW(0);
      U_ROW(1);
    }
    ASKED(ta);
    if (tid < (16 * CRN + 64) / 8) reinterpret_cast<v4u*>(Rc)[tid] = v4u{0u, 0u, 0u, 0u};
    SST(4);
    const gu64* fk = (gu64*)(ws + OFF_KG) + cv * 16 + w;
    u64 kg = __hip_atomic_load(fk, RLX_AGENT);
    U_ROW(2);
    for (unsigned spins = 0; !__all(kg == tag) && spins < SPIN_CAP; ++spins) {
      __builtin_amdgcn_s_sleep(8);
      kg = __hip_atomic_load(fk, RLX_AGENT);
    }
    asm volatile("" : "+v"(tx[3]), "+v"(st[3])::"memory");
    SST(5);
    const unsigned* kd = reinterpret_cast<const unsigned*>(Ta + 8 * CTN) + w * 392;
    glds16c(ws + OFF_KF + d * 2048, l * 16, lds_base((char*)kd));
    if (l < 34) glds16c(ws + OFF_KF + d * 2048 + 1024, l * 16, lds_base((char*)kd) + 1024);
    U_ROW(3);
    DRAIN();
    SST(6);
    {
#define MIX(lo, hi) __builtin_amdgcn_perm(hi, lo, 0x07060100)
      const unsigned a0 = kd[384 - l], b0 = kd[383 - l], a1 = kd[320 - l], b1 = kd[319 - l];
      const unsigned a2 = kd[256 - 2 * l], m2 = kd[255 - 2 * l], b2 = kd[254 - 2 * l];
      const unsigned* k3 = kd + 124 - 4 * (l & 31);
      v4u* ko = reinterpret_cast<v4u*>(kr) + l;
      ko[4] = v4u{a0, a0, (l & 1) ? b0 : MIX(a0, b0), b0};
      ko[68] = v4u{a1, a1, (l & 1) ? b1 : MIX(a1, b1), b1};
      ko[132] = v4u{a2, MIX(a2, m2), m2, b2};
      if (l < 32) ko[196] = v4u{k3[4], MIX(k3[3], k3[2]), k3[2], MIX(k3[1], k3[0])};
#undef MIX
    }
    RAW_BAR();
    SST(7);
    {
      unsigned uq[4] = {ub, ub + 1024, ub + 2048, ub + 3072};
      f16x8 bf[8], a[4];
      const f16x8 bm = BFRAG(ub - 64);
      asm volatile("" : "+v"(uq[0]), "+v"(uq[1]), "+v"(uq[2]), "+v"(uq[3]));
#pragma unroll
      for (int j = 1; j < 8; ++j) bf[j] = BFRAG(uq[j >> 1] + 512 * (j & 1));
#pragma unroll
      for (int T = 1; T < 8; ++T) acc[T] = MFMAH(bm, KFRAG(256 * T + 32), (f32x4{}));
#pragma unroll
      for (int e = 0; e < 3; ++e) a[e] = KFRAG(256 * e);
      const u16* kq = ka - 256 * 7;
#pragma unroll 1
      for (int r = 0; r < 8; ++r) {
#pragma unroll
        for (int e = 0; e < 8; ++e) {
          a[(e + 3) & 3] = *reinterpret_cast<const f16x8*>(kq + 256 * (7 - ((e + 3) & 7)) + (e >= 5 ? 32 : 0));
          const int j = (8 - e) & 7;
          bf[j] = BFRAG(uq[j >> 1] + 512 * (j & 1) + (e ? 64 : 0));
          __builtin_amdgcn_sched_barrier(0);
#pragma unroll
          for (int T = 7; T >= (e > 1 ? e : 1); --T) acc[T] = MFMAH(bf[T - e], a[e & 3], acc[T]);
          __builtin_amdgcn_sched_barrier(0);
        }
        kq += 32;
#pragma unroll
        for (int j = 0; j < 4; ++j) uq[j] += 64;
        asm volatile("" : "+v"(uq[0]), "+v"(uq[1]), "+v"(uq[2]), "+v"(uq[3]));
      }
    }
    SST(8);
    {
      v2u k[8];
#pragma unroll
      for (int T = 1; T < 8; ++T) k[T] = SKIP(T);
#pragma unroll
      for (int T = 1; T < 8; ++T) TILE_OUT(uc + 32 + 256 * T, acc[T], k[T]);
    }
    RAW_BAR();
    SST(10);
    if (w >= 4) CHUNK_ST(0, Uc + 32, CUN);
    CHUNK_ST(1, Uc + 32 + 512, CUN);
    if (CVSP) __builtin_amdgcn_s_sleep(CVSP);
    CHUNK_ST(2, Uc + 32 + 1024, CUN);
    if (CVSP) __builtin_amdgcn_s_sleep(CVSP);
    CHUNK_ST(3, Uc + 32 + 1536, CUN);
    if (w == 1) (void)wait_n<SLP_V, 32>(FL + FL_FZ + (blockIdx.x & 7) * 64, 64, TMO);
    DRAIN();
    if (l < 32) CHUNK_FLAG(0);
    SST(9);
#undef U_ROW
#undef U_VAL
#undef U_PUT
#undef ST_GET
  }
  const unsigned rb = lds_base(reinterpret_cast<char*>(((c & 1) ? Rs : Rc) + w * CRN + c + 8 * g));
#pragma unroll
  for (int j = 0; j < 8; ++j) acc[j] = f32x4{};
  const __amdgpu_buffer_rsrc_t rx = MKRS(ws + OFF_X2, SZ_X), rz = MKRS(ws + OFF_X3, SZ_X);
  const __amdgpu_buffer_rsrc_t rf = MKRS(FL + FL_FR, 64 * 16 * 128);
  v2u st = {0u, 0u}, xx = {0u, 0u};
#define X2_LOAD(ss, far)                                                                                                      \
  {                                                                                                                      \
    xx = LD64(rx, (l >> 1) * (DM * 2) + (l & 1) * 8, (far) ? (int)SZ_X : ((ss) * SEG * DM + 8 * cv) * 2);                 \
    st = LD64(rf, l * 8, ((ss) * 4 + (blockIdx.x & 3)) * 512);                                                           \
  }
  unsigned tl = now10();
  bool late = false;
  for (int s = 0; s < NSEG; ++s) {
    const int q = s & 3, q8 = s & 7;
    if (w == 1) {
      late = __builtin_popcountll(__ballot(st.y != 0u)) >= CVLATE;
      for (unsigned spins = 0; !__all(umin3(xx.x, xx.y, st.y) != 0u); ++spins) {
        if (spin_timeout(spins, TMO)) break;
        if (GF_V && s >= GF0) {
          if (spins) __builtin_amdgcn_s_sleep(SLP_V);
          else if (s < NSEG - 6) gate(tl);
          tl = now10() + GF_V;
          X2_LOAD(s, CVT2 > 64 ? 0 : spins && __builtin_popcountll(__ballot(st.y != 0u)) < CVT2);
          continue;
        }
        if (GATE_V && spins == 0 && !__any(st.y != 0u)) gate(tl);
        if (spins && !__all(st.y != 0u)) __builtin_amdgcn_s_sleep(SLP_V);
        X2_LOAD(s, __builtin_popcountll(__ballot(st.y != 0u)) < CVTHR);
      }
      if (!GF_V || s < GF0) tl = now10() + GATE_V;
      TS(0, s, 64);
      const float s0 = __uint_as_float(st.x), s1 = dppr<0xb1>(s0), m2 = __uint_as_float(st.y);
      const float var = (m2 + dppr<0xb1>(m2) + (s0 - s1) * (s0 - s1) * (1.0f / DM)) * (1.0f / DM);
      const float mb = bfr1((s0 + s1) * (1.0f / DM)), ri = 1024.0f * __builtin_amdgcn_rcpf(bfr1(__builtin_amdgcn_sqrtf(bfr1(bfr1(var) + 1e-5f))));
      const unsigned t0 = pk2(lo16(xx.x) - mb, hi16(xx.x) - mb), t1 = pk2(lo16(xx.y) - mb, hi16(xx.y) - mb);
      const unsigned b0 = pk2(lo16(t0) * ri, hi16(t0) * ri), b1 = pk2(lo16(t1) * ri, hi16(t1) * ri);
      const unsigned u0 = pkh(lo16(b0), hi16(b0)), u1 = pkh(lo16(b1), hi16(b1));
      u16* rp = Rc + (l & 1) * 4 * CRN + 32 * q + (l >> 1);
#define R_PUT(o) { rp[o] = (u16)u0; rp[(o) + CRN] = (u16)(u0 >> 16); rp[(o) + 2 * CRN] = (u16)u1; rp[(o) + 3 * CRN] = (u16)(u1 >> 16); }
      R_PUT(0) R_PUT(8 * CRN + 63)
      if (q == 0) { R_PUT(128) R_PUT(8 * CRN + 63 + 128) }
#undef R_PUT
      *reinterpret_cast<v2u*>(Sx + l * 4) = xx;
    }
    const u16* kq = ka + 32 * q8;
    const f16x8 a0 = *reinterpret_cast<const f16x8*>(kq - 32), a1 = *reinterpret_cast<const f16x8*>(kq);
    __syncthreads();
    TS(1, s, 0);
    const f16x8 bp = BFRAG(rb + 64 * ((q + 3) & 3)), bc = BFRAG(rb + 64 * q);
    const float u0 = h2f(Rc[w * CRN + 32 * q + c]), u1 = h2f(Rc[w * CRN + 32 * q + 16 + c]);
    const float x0 = bf2f(Sx[c * 8 + w]), x1 = bf2f(Sx[(16 + c) * 8 + w]);
    acc[0] = MFMAH(a0, bp, (q8 == 0 ? acc[1] : acc[0]));
    const f32x4 tmp = MFMAH(a1, bc, acc[0]);
    const unsigned uu = pk2(u0 * dsk, u1 * dsk);
    const unsigned yy = pk2(((q & 1) ? tmp[2] : tmp[0]) * ysc, ((q & 1) ? tmp[3] : tmp[1]) * ysc);
    const unsigned aa = pk2(lo16(yy) + lo16(uu), hi16(yy) + hi16(uu)), oo = pk2(x0 + lo16(aa), x1 + hi16(aa));
    if (g == (q8 >> 1)) {
      To[c * 8 + w] = (u16)oo;
      To[(16 + c) * 8 + w] = (u16)(oo >> 16);
    }
    __syncthreads();
    if (w == 0 && l < 32) {
      const v4u o = *reinterpret_cast<const v4u*>(To + l * 8);
      ST128(o, rz, l * 16, (s * 64 + cv) * 512);
      ST64(part_stats(o), rps, l * 8, ((64 + s) * 64 + cv) * 256);
    }
    if (w == 1) {
      if (GF_V && s + 1 >= GF0) st.y = 0u;
      else X2_LOAD(s + 1, !late);
    }
    const int left = 7 - (s >> 3);
#define PUSH(J0, SH)                                                                                              \
  if (left >= (J0)) {                                                                                             \
    _Pragma("unroll") for (int j = (J0); j < (J0) + 2 && j < 8; ++j)                                              \
      acc[j] = MFMAH(*reinterpret_cast<const f16x8*>(kq - 32 - 256 * j), bp, (j + (SH) < 8 ? acc[(j + (SH)) & 7] : f32x4{})); \
  }
    TS(2, s, 0);
    if (w == 0) {
      DRAIN();
      if (l < 4) set_flag(FL + FL_FC + (((64 + s) * 4 + l) * 8 + (cv >> 3)) * 32 + (cv & 7));
    }
    TS(3, s, 0);
    if (q8 == 0) { PUSH(1, 1) PUSH(3, 1) PUSH(5, 1) PUSH(7, 1) }
    else { PUSH(1, 0) PUSH(3, 0) PUSH(5, 0) PUSH(7, 0) }
#undef PUSH
    MSTAMP();
  }
#undef KFRAG
#undef BFRAG
#undef X2_LOAD
#undef TILE_OUT
#undef SKIP
#undef CHUNK_ST
#undef CHUNK_FLAG
}

__device__ __forceinline__ v4u ln_row_packed(const v4u xr) {
  float v[8];
#pragma unroll
  for (int e = 0; e < 4; ++e) { v[2 * e] = lo16(xr[e]); v[2 * e + 1] = hi16(xr[e]); }
  float s = ((v[0] + v[1]) + (v[2] + v[3])) + ((v[4] + v[5]) + (v[6] + v[7]));
  s = wave_sum(s);
  const float mean = s * (1.0f / DM);
  float q = 0.f;
#pragma unroll
  for (int j = 0; j < 8; ++j) { float t = v[j] - mean; q += t * t; }
  q = wave_sum(q);
  const float var = q * (1.0f / DM);
  const float mb = bfr(mean);
  const float sd = bfr(sqrtf(bfr(bfr(var) + 1e-5f)));
  const float ri = 1.0f / sd;
  v4u o;
#pragma unroll
  for (int e = 0; e < 4; ++e) {
    const unsigned t = pk2(v[2 * e] - mb, v[2 * e + 1] - mb);
    const float x0 = lo16(t), x1 = hi16(t);
    float q0 = x0 * ri, q1 = x1 * ri;
    q0 = __builtin_fmaf(__builtin_fmaf(-q0, sd, x0), ri, q0);
    q1 = __builtin_fmaf(__builtin_fmaf(-q1, sd, x1), ri, q1);
    o[e] = pk2(q0, q1);
  }
  return o;
}

__device__ __forceinline__ void ln4_packed(v4u (&x)[4]) {
  f32x2_t v[16];
#pragma unroll
  for (int e = 0; e < 16; ++e) v[e] = f32x2_t{lo16(x[e >> 2][e & 3]), hi16(x[e >> 2][e & 3])};
  const f32x2_t t = (((v[0] + v[1]) + (v[2] + v[3])) + ((v[4] + v[5]) + (v[6] + v[7]))) +
                    (((v[8] + v[9]) + (v[10] + v[11])) + ((v[12] + v[13]) + (v[14] + v[15])));
  const float mean = red_mod4(t.x + t.y) * (1.0f / DM);
  const float mb = bfr1(mean);
  f32x2_t q0 = {0.f, 0.f}, q1 = {0.f, 0.f};
#pragma unroll
  for (int e = 0; e < 16; e += 2) {
    v[e] -= mb;
    v[e + 1] -= mb;
    q0 += v[e] * v[e];
    q1 += v[e + 1] * v[e + 1];
  }
  q0 += q1;
  const float dm = mean - mb;
  const float var = fmaxf(red_mod4(q0.x + q0.y) * (1.0f / DM) - dm * dm, 0.f);
  const float sd = bfr1(sqrtf(bfr1(bfr1(var) + 1e-5f)));
  const float ri = 1.0f / sd;
#pragma unroll
  for (int e = 0; e < 16; ++e) {
    const unsigned t2 = pk2(v[e].x, v[e].y);
    const f32x2_t q = f32x2_t{lo16(t2), hi16(t2)} * ri;
    x[e >> 2][e & 3] = pk2(q.x, q.y);
  }
}

constexpr int MEGA_WGS = 256;
constexpr size_t MEGA_LDS = (size_t)(32 * 4 * 64 * 8 + 8 * SMS + 15 * QS + 128 + 16 * 64 * 8 + 8 * SOS + 512) * 2;
static_assert(MEGA_LDS <= 163840, "lds");
static_assert(MEGA_LDS >= (size_t)AW_CNT + 128 && MEGA_LDS >= (size_t)P_CN + 512, "lds");
static_assert(MEGA_LDS >= (size_t)CV_LDS, "lds");
static_assert(MEGA_LDS >= (size_t)2 * 32 * SMS * 2 && MEGA_LDS >= 131072, "lds");
static_assert(MEGA_LDS >= (size_t)8 * 16640, "lds");
__device__ __forceinline__ MegaArgs role_args(const int b, const int wv) {
  auto p = (const __attribute__((address_space(4))) u64*)__builtin_amdgcn_kernarg_segment_ptr();
  asm volatile("" : "+s"(p));
#define KA(i) ((u16*)(__attribute__((address_space(1))) u16*)p[i])
  return MegaArgs{KA(0), KA(1), (char*)KA(2), KA(3), KA(4), KA(5), KA(6), KA(7), KA(8), KA(9), KA(10), KA(11), KA(12), KA(13), b, wv};
#undef KA
}
__global__ __launch_bounds__(512, 2) void mega_kernel(const u16* mem0, const u16* Dskip, char* ws, u16* out, const u16* x0,
                                                      const u16* logAre, const u16* Aim, const u16* Cre, const u16* Cim,
                                                      const u16* logdt, const u16* Wq, const u16* Wk, const u16* Wv,
                                                      const u16* Wo) {
  extern __shared__ __attribute__((aligned(16))) char smem[];
  const int b = vbid(), wv = __builtin_amdgcn_readfirstlane(threadIdx.x >> 6);
#define A role_args(b, wv)
  u64 tag = 0;
  {
    const unsigned t0 = now10();
    if ((b >= 32 && b < 64) || (b >= 200 && b < 232)) start_kgen(A, smem, b < 64 ? b - 32 : b - 168);
    else if (b >= 64 && b < 96) tag = start_stats<4>(A, b - 64);
    else if (b >= 240) (void)start_stats<8>(A, b - 224);
    const unsigned dl = (b >= 32 && b < 64) ? DL_C : (b >= 64 && b < 96) ? DL_A : b >= 240 ? DL_R2 : ((b >= 96 && b < 104) || b >= 232) ? DL_R : (b >= 200 && b < 232) ? DL_Q : 0u;
    while (now10() - t0 < dl) __builtin_amdgcn_s_sleep(16);
  }
  if ((EXP_OFF & 1) && b >= 64 && b < 80) return;
  if ((EXP_OFF & 8) && b >= 200 && b < 232) return;
  if ((EXP_OFF & 16) && b >= 32 && b < 64) return;
  if ((EXP_OFF & 32) && b >= 80 && b < 96) return;
  if (b < 64) role_chain(A, smem, b >> 5, b & 31, 0);
  else if (b < 96) role_aw(A, smem, (b >> 4) & 1, b & 15, tag);
  else if (b >= 104 && b < 168) role_conv(A, smem, b - 104);
  else {
    const bool kv = b >= 168 && b < 232;
    role_proj(A, smem, b >= (kv ? 200 : 240), kv ? (b - 168) & 31 : b & 15, kv);
  }
#undef A
}

extern "C" void kernel_launch(void* const* d_in, const int* in_sizes, int n_in, void* d_out, int out_size, void* d_ws,
                              size_t ws_size, hipStream_t stream) {
  MegaArgs A;
  A.x0 = (const u16*)d_in[0];
  A.logAre = (const u16*)d_in[1];
  A.Aim = (const u16*)d_in[2];
  A.Cre = (const u16*)d_in[3];
  A.Cim = (const u16*)d_in[4];
  A.logdt = (const u16*)d_in[5];
  A.Dskip = (const u16*)d_in[6];
  A.mem0 = (const u16*)d_in[8];
  A.Wq = (const u16*)d_in[9];
  A.Wk = (const u16*)d_in[10];
  A.Wv = (const u16*)d_in[11];
  A.Wo = (const u16*)d_in[12];
  A.ws = (char*)d_ws;
  A.out = (u16*)d_out;

  (void)hipFuncSetAttribute((const void*)mega_kernel, hipFuncAttributeMaxDynamicSharedMemorySize, (int)MEGA_LDS);
  mega_kernel<<<MEGA_WGS, 512, MEGA_LDS, stream>>>(A.mem0, A.Dskip, A.ws, A.out, A.x0, A.logAre, A.Aim, A.Cre, A.Cim, A.logdt,
                                                   A.Wq, A.Wk, A.Wv, A.Wo);
}
```

```cpp
#include <hip/hip_runtime.h>
#include <hip/hip_bf16.h>
#include <stdint.h>

constexpr int L_SEQ = 2048;
constexpr int DM    = 512;
constexpr int NST   = 128;
constexpr int NH    = 32;
constexpr int DH    = 128;
constexpr int HD    = NH * DH;
constexpr int MM    = 8;
constexpr int SEG   = 32;
constexpr int NSEG  = L_SEQ / SEG;
constexpr int LK    = MM + SEG;

typedef unsigned short u16;
using bf16x8 = __attribute__((ext_vector_type(8))) short;
using f32x4  = __attribute__((ext_vector_type(4))) float;
using f32x16 = __attribute__((ext_vector_type(16))) float;

static __device__ __forceinline__ u16 f2bf(float f) {
  unsigned u = __float_as_uint(f);
  u += 0x7fffu + ((u >> 16) & 1u);
  return (u16)(u >> 16);
}
static __device__ __forceinline__ float bf2f(u16 h) { return __uint_as_float(((unsigned)h) << 16); }
static __device__ __forceinline__ float bfr(float f) { return bf2f(f2bf(f)); }

constexpr size_t SZ_X      = (size_t)L_SEQ * DM * 2;
constexpr size_t OFF_KF    = 0;
constexpr size_t OFF_UT    = OFF_KF + (size_t)DM * L_SEQ * 4;
constexpr size_t OFF_YT    = OFF_UT + SZ_X;
constexpr size_t OFF_XA    = OFF_YT + SZ_X;
constexpr size_t OFF_H     = OFF_XA + SZ_X;
constexpr size_t OFF_X2    = OFF_H + SZ_X;
constexpr size_t OFF_U2    = OFF_X2 + SZ_X;
constexpr size_t OFF_X3    = OFF_U2 + SZ_X;
constexpr size_t SZ_QK     = (size_t)L_SEQ * 2 * HD * 2;
constexpr size_t OFF_QK    = OFF_X3 + SZ_X;
constexpr size_t SZ_O      = (size_t)L_SEQ * HD * 2;
constexpr size_t OFF_VF    = OFF_QK + 2 * SZ_QK;
constexpr size_t SZ_PW     = (size_t)2 * NSEG * 16 * SEG * DM * 2;
constexpr size_t OFF_PW    = OFF_VF + 2 * SZ_O;
constexpr size_t SZ_MEM    = (size_t)NSEG * MM * HD * 2;
constexpr size_t OFF_MEMK  = OFF_PW + SZ_PW;
constexpr size_t OFF_MEMV  = OFF_MEMK + 2 * SZ_MEM;
constexpr size_t OFF_GT    = OFF_MEMV + 2 * SZ_MEM;
constexpr size_t OFF_EP    = OFF_GT + 65536;
constexpr size_t OFF_RD    = OFF_EP + 4096;
constexpr size_t OFF_KG    = OFF_RD + 4096;
constexpr size_t OFF_G     = OFF_KG + 8192;
constexpr size_t SZ_ACC    = (size_t)2 * 65536;
constexpr size_t OFF_Z     = OFF_GT + (size_t)128 * DM * 4;
constexpr size_t OFF_ACC   = OFF_Z;
constexpr size_t OFF_FL    = OFF_ACC + 2 * SZ_ACC;
constexpr int FL_FK  = 0;
constexpr int FL_FW  = FL_FK + 64 * 8 * 32;
constexpr int FL_FR  = FL_FW + 2 * 64 * 16 * 32;
constexpr int FL_FC  = FL_FR + 64 * 16 * 32;
constexpr int FL_FQ  = FL_FC + 2 * 64 * 32 * 32;
constexpr int FL_FP  = FL_FQ + 2 * 64 * 32 * 32;
constexpr int FL_FZ  = FL_FP + 64 * 32 * 32;
constexpr int FL_TMO = FL_FZ + 16 * 32;
constexpr int FL_END = FL_TMO + 64;
constexpr size_t Z_BYTES   = OFF_FL + (size_t)FL_END * 4 - OFF_Z;
constexpr size_t WS_TOTAL  = OFF_Z + Z_BYTES;
static_assert(Z_BYTES % 16 == 0 && OFF_Z % 256 == 0 && Z_BYTES <= 4 * 64 * 8192 && OFF_G + 2048 <= OFF_Z, "zero region");
static_assert(WS_TOTAL + (4 << 20) <= (size_t)256 * 1024 * 1024, "workspace too large");

static_assert(WS_TOTAL - OFF_XA <= (size_t)384 * 8 * 65536, "translation warm-up");
constexpr int CKN = 2336;
static_assert((size_t)DM * 2 * CKN * 2 <= (size_t)DM * L_SEQ * 4 + SZ_X, "K buffer");
__device__ __forceinline__ float pos_of_index(int p) {
  if (p < 256) return (float)p;
  if (p < 384) return (float)(256 + 2 * (p - 256));
  if (p < 512) return (float)(512 + 4 * (p - 384));
  if (p < 640) return (float)(1024 + 8 * (p - 512));
  return 2048.0f;
}
__device__ __forceinline__ int index_of_l(int l) {
  float pf = bfr((float)l);
  int p = (int)pf;
  if (p < 256) return p;
  if (p < 512) return 256 + ((p - 256) >> 1);
  if (p < 1024) return 384 + ((p - 512) >> 2);
  if (p < 2048) return 512 + ((p - 1024) >> 3);
  return 640;
}

template <int CTRL> __device__ __forceinline__ float dppr(float x) {
  return __builtin_bit_cast(float, __builtin_amdgcn_update_dpp(0, __builtin_bit_cast(int, x), CTRL, 0xf, 0xf, false));
}
__device__ __forceinline__ float wave_sum(float v) {
  v += dppr<0x121>(v);
  v += dppr<0x122>(v);
  v += dppr<0x124>(v);
  v += dppr<0x128>(v);
  {
    auto r = __builtin_amdgcn_permlane16_swap(__float_as_uint(v), __float_as_uint(v), false, false);
    v = __uint_as_float(r[0]) + __uint_as_float(r[1]);
  }
  {
    auto r = __builtin_amdgcn_permlane32_swap(__float_as_uint(v), __float_as_uint(v), false, false);
    v = __uint_as_float(r[0]) + __uint_as_float(r[1]);
  }
  return v;
}
__device__ __forceinline__ void ln_row8(float (&v)[8]) {
  float s = 0.f;
#pragma unroll
  for (int j = 0; j < 8; ++j) s += v[j];
  s = wave_sum(s);
  const float mean = s * (1.0f / DM);
  float q = 0.f;
#pragma unroll
  for (int j = 0; j < 8; ++j) { float t = v[j] - mean; q += t * t; }
  q = wave_sum(q);
  const float var = q * (1.0f / DM);
  const float mb = bfr(mean);
  const float sd = bfr(sqrtf(bfr(bfr(var) + 1e-5f)));
#pragma unroll
  for (int j = 0; j < 8; ++j) v[j] = bfr(bfr(v[j] - mb) / sd);
}

#define RLX_AGENT __ATOMIC_RELAXED, __HIP_MEMORY_SCOPE_AGENT
typedef __attribute__((address_space(1))) unsigned gu32;
typedef unsigned v4u __attribute__((ext_vector_type(4)));
constexpr int AUX_SC1 = 16;
constexpr unsigned SPIN_CAP = 1u << 18;

__device__ __forceinline__ bool wait_flags(const unsigned* flags, int lane, int nfl, unsigned want, unsigned* tmo) {
  for (unsigned spins = 0;; ++spins) {
    unsigned v = want;
    if (lane < nfl) v = __hip_atomic_load((gu32*)flags + lane * 32, RLX_AGENT);
    if (__all(v >= want)) break;
    if (spins > SPIN_CAP) {
      if (lane == 0) __hip_atomic_store((gu32*)tmo, 1u, RLX_AGENT);
      return false;
    }
  }
  __builtin_amdgcn_fence(__ATOMIC_ACQUIRE, "wavefront");
  return true;
}

#define STAMP(i)
#define STAMP_DUMP()
#define WSTAMP(i)
template <int CTRL> __device__ __forceinline__ float dppf(float old, float x) {
  return __builtin_bit_cast(float, __builtin_amdgcn_update_dpp(__builtin_bit_cast(int, old), __builtin_bit_cast(int, x), CTRL, 0xf, 0xf, false));
}
__device__ __forceinline__ float wave_max_dpp(float x) {
  const float ninf = -3e38f;
  x = fmaxf(x, dppf<0x111>(ninf, x));
  x = fmaxf(x, dppf<0x112>(ninf, x));
  x = fmaxf(x, dppf<0x114>(ninf, x));
  x = fmaxf(x, dppf<0x118>(ninf, x));
  x = fmaxf(x, dppf<0x142>(ninf, x));
  x = fmaxf(x, dppf<0x143>(ninf, x));
  return __builtin_bit_cast(float, __builtin_amdgcn_readlane(__builtin_bit_cast(int, x), 63));
}
__device__ __forceinline__ float wave_sum_dpp(float x) {
  x += dppf<0x111>(0.f, x);
  x += dppf<0x112>(0.f, x);
  x += dppf<0x114>(0.f, x);
  x += dppf<0x118>(0.f, x);
  x += dppf<0x142>(0.f, x);
  x += dppf<0x143>(0.f, x);
  return __builtin_bit_cast(float, __builtin_amdgcn_readlane(__builtin_bit_cast(int, x), 63));
}
constexpr int SMS = 528;
constexpr int QS = 144;
constexpr int VS = 80;
constexpr int SOS = 144;
#define VAR_NREP 4
constexpr int NREP = VAR_NREP;
__device__ __forceinline__ float xsum16(float x) {
  auto r = __builtin_amdgcn_permlane16_swap(__float_as_uint(x), __float_as_uint(x), false, false);
  return __uint_as_float(r[0]) + __uint_as_float(r[1]);
}
__device__ __forceinline__ float xsum32(float x) {
  auto r = __builtin_amdgcn_permlane32_swap(__float_as_uint(x), __float_as_uint(x), false, false);
  return __uint_as_float(r[0]) + __uint_as_float(r[1]);
}
__device__ __forceinline__ float xmax16(float x) {
  auto r = __builtin_amdgcn_permlane16_swap(__float_as_uint(x), __float_as_uint(x), false, false);
  return fmaxf(__uint_as_float(r[0]), __uint_as_float(r[1]));
}
__device__ __forceinline__ float xmax32(float x) {
  auto r = __builtin_amdgcn_permlane32_swap(__float_as_uint(x), __float_as_uint(x), false, false);
  return fmaxf(__uint_as_float(r[0]), __uint_as_float(r[1]));
}
__device__ __forceinline__ float red_mod4(float x) {
  x += dppf<0x124>(0.f, x);
  x += dppf<0x128>(0.f, x);
  return xsum32(xsum16(x));
}
typedef __bf16 bf16x2_t __attribute__((ext_vector_type(2)));
typedef float f32x2_t __attribute__((ext_vector_type(2)));
__device__ __forceinline__ unsigned pk2(float a, float b) {
  f32x2_t v = {a, b};
  bf16x2_t r = __builtin_convertvector(v, bf16x2_t);
  return __builtin_bit_cast(unsigned, r);
}
typedef _Float16 f16x2_t __attribute__((ext_vector_type(2)));
typedef _Float16 f16x8 __attribute__((ext_vector_type(8)));
__device__ __forceinline__ unsigned pkh(float a, float b) {
  f32x2_t v = {a, b};
  return __builtin_bit_cast(unsigned, __builtin_convertvector(v, f16x2_t));
}
__device__ __forceinline__ float h2f(u16 h) { return (float)__builtin_bit_cast(_Float16, h); }
__device__ __forceinline__ float one(float x) {
  asm("" : "+v"(x));
  return x;
}
__device__ __forceinline__ unsigned pk2s(float a, float b) { return pk2(one(a), one(b)); }
__device__ __forceinline__ float dot2sq(const unsigned x, const float acc) {
  return __builtin_amdgcn_fdot2_f32_bf16(__builtin_bit_cast(bf16x2_t, x), __builtin_bit_cast(bf16x2_t, x), acc, false);
}
__device__ __forceinline__ float lo16(unsigned u) { return __uint_as_float(u << 16); }
__device__ __forceinline__ float hi16(unsigned u) { return __uint_as_float(u & 0xffff0000u); }
__device__ __forceinline__ float bfr1(float x) { return lo16(pk2(x, x)); }
__device__ __forceinline__ float half_sum(float v) {
  v += dppr<0x121>(v);
  v += dppr<0x122>(v);
  v += dppr<0x124>(v);
  v += dppr<0x128>(v);
  return xsum16(v);
}
__device__ __forceinline__ void scale_pair(float a, float b, float scale, float& oa, float& ob) {
  unsigned u = pk2(a, b);
  unsigned v = pk2s(lo16(u) * scale, hi16(u) * scale);
  oa = lo16(v);
  ob = hi16(v);
}

#define XMAP 2
__device__ __forceinline__ int vbid() {
  const int b = blockIdx.x, x = b & 7, t = b >> 3;
  if (XMAP == 0) {
    if (b < 64) return (b & 32) + 4 * x + (t & 3);
    if (b < 96) return (b & ~15) + 2 * x + (t & 1);
    if (b >= 104 && b < 168) return 104 + 8 * x + t - 13;
    if (b >= 168 && b < 232) return 168 + ((b - 168) & 32) + 4 * x + ((t - 21) & 3);
    return b;
  }
  if (XMAP == 3) {
    const int y = x & 3;
    if (t >= 16 && t < 24) return 104 + 16 * y + 2 * (x & 4) + t - 16;
    if (x < 4) return t < 16 ? 4 * (t & 8) + 8 * x + (t & 7) : 64 + 4 * (t & 4) + 4 * y + (t & 3);
    if (t >= 24) return (t >= 28 ? 240 : y < 2 ? 96 : 224) + 4 * y + (t & 3);
    return 168 + 4 * (t & 8) + 8 * y + (t & 7);
  }
  if (XMAP == 4) {
    const int y = x & 3;
    if (x < 4) return t < 16 ? 4 * (t & 8) + 8 * x + (t & 7) : t < 28 ? 104 + 16 * y + t - 16 : 64 + 4 * y + (t & 3);
    if (t >= 24) return (t >= 28 ? 240 : y < 2 ? 96 : 224) + 4 * y + (t & 3);
    if (t < 16) return 168 + 4 * (t & 8) + 8 * y + (t & 7);
    return t < 20 ? 104 + 16 * y + t - 4 : 80 + 4 * y + (t & 3);
  }
  if (XMAP == 2 || XMAP >= 5) {
    if (XMAP == 5 && x < 4 && t < 16) return 16 * x + t;
    if (XMAP == 6 && x < 4 && t < 16) return 32 * (x & 1) + 8 * (x & 2) + t;
    if (XMAP == 7 && x < 4) return (x & 1) ? 104 + 16 * (x & 2) + t : 16 * x + t;
    if (x < 4) return t < 16 ? 4 * (t & 8) + 8 * x + (t & 7) : 104 + 16 * x + t - 16;
    const int y = x - 4;
    if (t >= 24) return (t >= 28 ? 240 : y < 2 ? 96 : 224) + 4 * y + (t & 3);
    if (t < 16) return 168 + 4 * (t & 8) + 8 * y + (t & 7);
    return 64 + 4 * (t & 4) + 4 * y + (t & 3);
  }
  if (x < 2) return 2 * (t & 16) + 16 * x + (t & 15);
  const int i = 16 * (x - 2) + (t & 15);
  if (t < 16) {
    const int k = (i * 171) >> 9, r = i - 3 * k;
    return r == 0 ? 168 + k : r == 1 ? 200 + k : 64 + 16 * (k & 1) + (k >> 1);
  }
  const int g = i >> 3, e = i & 7;
  if (g == 3) return 96 + e;
  if (g == 7) return 232 + e;
  if (g == 9) return 240 + e;
  if (g == 11) return 248 + e;
  return 104 + 8 * (g - (g > 3) - (g > 7) - (g > 9)) + e;
}
#define MSTAMP()
#define WSTAMP2()
#define SST(k)
#define SSX(k, c)
#define SSW(k, v)
#define TS(k, ss, t)
#define LS(k, ss)
#define LS_DUMP()
struct MegaArgs {
  const u16* mem0;
  const u16* Dskip;
  char* ws;
  u16* out;
  const u16* x0;
  const u16 *logAre, *Aim, *Cre, *Cim, *logdt;
  const u16 *Wq, *Wk, *Wv, *Wo;
  int vb, wv;
};

__device__ __forceinline__ int lane_id() {
  int l;
  asm volatile("v_mbcnt_lo_u32_b32 %0, -1, 0\n\tv_mbcnt_hi_u32_b32 %0, -1, %0" : "=v"(l));
  __builtin_assume(l >= 0 && l < 64);
  return l;
}
__device__ __forceinline__ bool spin_timeout(unsigned spins, unsigned* tmo) {
  if (spins > SPIN_CAP) {
    if (lane_id() == 0) __hip_atomic_store((gu32*)tmo, 1u, RLX_AGENT);
    return true;
  }
  return false;
}
#define ROT_H (4 * (h & 1))
#define ROT_T (h & 2)
#define C2G 420
#define CVSP 40
#define PFA 0
#define PFB 0
#define WO1 1
#define AWH 0
#define PEEK0 1
#define SLP0 2
#define GF_R 400
#define GF_V 0
#define PP 0
#define EL 1
#define IPF 1
#define XL 0
#define MC3 2
#define XZD 300
#define XZP 4
#define NFW 1
#define TP 1
#define TPS (4 * TP + 4)
#define MCE 1
#define H4S 0
#define GF_H 150
#define CVT2 48
#define GF_P 0
#define GF0 8
#define SLP_H 12
#define HX0 6
#define CVSTEP 2
#define CVLATE 64
#define PACE 203
#define PACE0 6
#define RPRIO 3
#define DL_R2 1400
#define PNL 8
#define GSL 12
#define AWDIR 1
#define AWAT 0
#define AWRP 0
#define PEPS 4
#define FGATE 0
#define DL_C 0
#define DL_A 800
#define DL_R 0
#define DL_Q 0
#define KC0 0
#define CPRIO 1
#define P1PRIO 3
#define QLEAD 2
#define POLL_SLEEP 12
#define SLP_C POLL_SLEEP
#define SLP_A POLL_SLEEP
#define SLP_W POLL_SLEEP
#define SLP_R POLL_SLEEP
#define SLP_V POLL_SLEEP
#define SLP_Q POLL_SLEEP
#define SLP_M 4
#define AWPRIO 2
#define SLP_S 8
#define SPOS 4
#define CVPRE 12
#define QPD 3
#define QPRI 2
#define TLBW 3
#define QHV 0
#define QCHK 0
#define LEADN 99
#define GATE_V 170
#define GATE_R 380
#define GATE_N 380
#define AWAHEAD 1
#define GATE_M 150
#define PRI_A 3
#define PRI_WA 2
#define PRI_WB 1
#define AWSKIP 3
#define PRI_0 3
#define AWDEF 0
#define SLP_F 2
#define EXP_OFF 0
#define AWSPEC 1
#define CVTHR 48
#define NR_C 8
#define NR_R 8
#define NR_W 8
#define NR_N 2
#define QAHEAD 0
#define NS0 4
#define SLP_N POLL_SLEEP
template <int SLP, int WPL>
__device__ __forceinline__ bool wait_n(const unsigned* f, int n, unsigned* tmo) {
  const int l = lane_id();
  for (unsigned spins = 0;; ++spins) {
    unsigned v = 1u;
    if (l < n) v = __hip_atomic_load((gu32*)f + (l / WPL) * 32 + l % WPL, RLX_AGENT);
    if (__all(v != 0u)) break;
    if (spin_timeout(spins, tmo)) return false;
    __builtin_amdgcn_s_sleep(SLP);
  }
  asm volatile("" ::: "memory");
  return true;
}
__device__ __forceinline__ unsigned peek(const unsigned* base, const unsigned off) {
  unsigned r;
  asm volatile("global_atomic_or %0, %1, %2, %3 sc0\n\ts_waitcnt vmcnt(0)" : "=&v"(r) : "v"(off), "v"(0u), "s"(base) : "memory");
  return r;
}
__device__ __forceinline__ unsigned now10() { return (unsigned)__builtin_amdgcn_s_memrealtime(); }
__device__ __forceinline__ void gate(const unsigned t) {
#pragma unroll 1
  for (int i = 0; i < 64; ++i) {
    const int d = (int)(t - now10());
    if (d <= 0) break;
    if (d > 100) __builtin_amdgcn_s_sleep(32);
    else if (d > 25) __builtin_amdgcn_s_sleep(8);
    else __builtin_amdgcn_s_sleep(2);
  }
}
template <int SLP, int WPL, int GT>
__device__ __forceinline__ void grp_wait(const bool leader, volatile unsigned* sw, const unsigned val, const unsigned* f, int n,
                                         unsigned* tmo, unsigned& tl) {
  if (leader) {
    const int l = lane_id();
    for (unsigned spins = 0;; ++spins) {
      unsigned v = 1u;
      if (l < n) v = __hip_atomic_load((gu32*)f + (l / WPL) * 32 + l % WPL, RLX_AGENT);
      if (__all(v != 0u) || spin_timeout(spins, tmo)) break;
      if (GT && spins == 0) gate(tl);
      else __builtin_amdgcn_s_sleep(SLP);
    }
    if (GT) tl = now10() + GT;
    if (sw && l == 0) *sw = val;
  } else {
    for (unsigned spins = 0; *sw < val; ++spins) {
      if (spins > 64u * SPIN_CAP) break;
      if (FGATE && GT && spins == 0) gate(tl);
      __builtin_amdgcn_s_sleep(SLP_F);
    }
    if (FGATE && GT) tl = now10() + GT;
  }
  asm volatile("" ::: "memory");
}
template <int SLP, int WPL>
__device__ __forceinline__ bool wg_wait(const unsigned* f, int n, unsigned* tmo) {
  if (threadIdx.x < 64) (void)wait_n<SLP, WPL>(f, n, tmo);
  __syncthreads();
  asm volatile("" ::: "memory");
  return true;
}
__device__ __forceinline__ void set_flag(unsigned* f) { __hip_atomic_store((gu32*)f, 1u, RLX_AGENT); }
#define DRAIN() asm volatile("s_waitcnt vmcnt(0)" ::: "memory")
#define MKRS(ptr, bytes) __builtin_amdgcn_make_buffer_rsrc((void*)(ptr), 0, (int)(bytes), 0x00020000)
typedef unsigned v2u __attribute__((ext_vector_type(2)));
#define LD128(rs, voff, soff) __builtin_amdgcn_raw_buffer_load_b128(rs, voff, soff, AUX_SC1)
#define LD64(rs, voff, soff) __builtin_amdgcn_raw_buffer_load_b64(rs, voff, soff, AUX_SC1)
#define ST128(v, rs, voff, soff)                                              \
  do {                                                                        \
    const v4u st_d_ = (v);                                                    \
    __builtin_amdgcn_raw_buffer_store_b128(st_d_, rs, voff, soff, AUX_SC1);   \
    asm volatile("s_nop 1" ::"v"(st_d_) : "memory");                          \
  } while (0)
#define ST64(v, rs, voff, soff) __builtin_amdgcn_raw_buffer_store_b64(v, rs, voff, soff, AUX_SC1)
#define BF8(x) __builtin_bit_cast(bf16x8, x)
#define MFMA16(a, b, c) __builtin_amdgcn_mfma_f32_16x16x32_bf16(a, b, c, 0, 0, 0)
typedef short bf16x4 __attribute__((ext_vector_type(4)));
#define MFMA16K(a, b, c) \
  __builtin_amdgcn_mfma_f32_16x16x16bf16_1k(__builtin_bit_cast(bf16x4, a), __builtin_bit_cast(bf16x4, b), c, 0, 0, 0)
__device__ __forceinline__ float fmax_nn(float a, float b) { return __builtin_elementwise_maximum(a, b); }
typedef __bf16 bfv8 __attribute__((ext_vector_type(8)));
typedef __bf16 bfv16 __attribute__((ext_vector_type(16)));
typedef unsigned v8u __attribute__((ext_vector_type(8)));
#define SMF(a, b, c, idx) \
  __builtin_amdgcn_smfmac_f32_16x16x64_bf16(__builtin_bit_cast(bfv8, a), __builtin_bit_cast(bfv16, b), c, idx, 0, 0)
__device__ __forceinline__ float swap_add32(float a, float b) {
  auto r = __builtin_amdgcn_permlane32_swap(__float_as_uint(a), __float_as_uint(b), false, false);
  return __uint_as_float(r[0]) + __uint_as_float(r[1]);
}
__device__ __forceinline__ float swap_add16(float a, float b) {
  auto r = __builtin_amdgcn_permlane16_swap(__float_as_uint(a), __float_as_uint(b), false, false);
  return __uint_as_float(r[0]) + __uint_as_float(r[1]);
}
__device__ __forceinline__ v4u smf_fix(const v4u d) {
  auto x = __builtin_amdgcn_permlane16_swap(d.x, d.z, false, false);
  auto y = __builtin_amdgcn_permlane16_swap(d.y, d.w, false, false);
  return v4u{x[0], x[1], y[0], y[1]};
}
__device__ __forceinline__ v8u smf_bfrag(const u16* p) {
  const v2u a = *reinterpret_cast<const v2u*>(p), b = *reinterpret_cast<const v2u*>(p + 8);
  const v2u c = *reinterpret_cast<const v2u*>(p + 32), d = *reinterpret_cast<const v2u*>(p + 40);
  return v8u{a.x, b.x, a.y, b.y, c.x, d.x, c.y, d.y};
}

#define VAR_PPA 0
#define VAR_PPB 0
#define VAR_THR 0
#define VAR_THR2 0
#define CV_LEAD 100
#define VAR_EARLY 1
#define VAR_DRAIN 1
#define VAR_D1 0
#define VAR_D2 0
#define VAR_DR2 1
#define VAR_S1 0
#define VAR_S2 0
typedef __attribute__((address_space(1))) unsigned long long gu64;
typedef __attribute__((address_space(1))) char gch;
__device__ __forceinline__ int cvt_rpi(float x) {
  int r;
  asm("v_cvt_rpi_i32_f32 %0, %1" : "=v"(r) : "v"(x));
  return r;
}
typedef unsigned long long u64;
__device__ __forceinline__ u64 call_tag(const MegaArgs& A) {
  u64 t = *(const __attribute__((address_space(4))) u64*)(A.ws + OFF_EP) * 6364136223846793005ull + 1442695040888963407ull;
  asm volatile("" : "+s"(t)::"memory");
  return t;
}
__device__ __forceinline__ u64 tagged(const u64 tag, const unsigned v) { return (tag & 0xffffffffffffull) | (u64)(v >> 16) << 48; }
__device__ __forceinline__ bool has_tag(const u64 tag, const unsigned lo, const unsigned hi) {
  return lo == (unsigned)tag && (hi & 0xffffu) == ((unsigned)(tag >> 32) & 0xffffu);
}
__device__ __forceinline__ u64 ready_look(const MegaArgs& A) {
  return __hip_atomic_load((gu64*)(A.ws + OFF_RD) + (blockIdx.x & 7) * 64 + lane_id(), RLX_AGENT);
}
#define RD_ASK(v) \
  asm volatile("global_load_dwordx2 %0, %1, %2 sc1" : "=v"(v) : "v"(lane_id() * 8), "s"(A.ws + OFF_RD + (blockIdx.x & 7) * 512) : "memory")
__device__ __forceinline__ void ready_wait(const MegaArgs& A, const u64 tag, u64 v) {
  for (unsigned spins = 0; !__all(v == tag) && spins < SPIN_CAP; ++spins) {
    __builtin_amdgcn_s_sleep(8);
    v = ready_look(A);
  }
  asm volatile("" ::: "memory");
}
typedef short v4s __attribute__((ext_vector_type(4)));
typedef __attribute__((address_space(3))) v4s* lp4s;
__device__ __forceinline__ void glds16(const void* sbase, const unsigned voff, const unsigned lds) {
  unsigned keep;
  asm volatile("s_nop 4\n\ts_mov_b32 %0, m0\n\ts_mov_b32 m0, %3\n\ts_nop 0\n\tglobal_load_lds_dwordx4 %1, %2\n\ts_mov_b32 m0, %0"
               : "=&s"(keep)
               : "v"(voff), "s"(sbase), "s"(lds)
               : "memory");
}
__device__ __forceinline__ void glds16c(const void* sbase, const unsigned voff, const unsigned lds) {
  unsigned keep;
  asm volatile("s_nop 4\n\ts_mov_b32 %0, m0\n\ts_mov_b32 m0, %3\n\ts_nop 0\n\tglobal_load_lds_dwordx4 %1, %2 sc1\n\ts_mov_b32 m0, %0"
               : "=&s"(keep)
               : "v"(voff), "s"(sbase), "s"(lds)
               : "memory");
}
__device__ __forceinline__ void glds64c(const void* sbase, const unsigned voff, const unsigned lds) {
  unsigned keep;
  asm volatile("s_nop 4\n\ts_mov_b32 %0, m0\n\ts_mov_b32 m0, %3\n\ts_nop 0\n\tglobal_load_lds_dwordx4 %1, %2 sc1\n\t"
               "global_load_lds_dwordx4 %1, %2 offset:1024 sc1\n\tglobal_load_lds_dwordx4 %1, %2 offset:2048 sc1\n\t"
               "global_load_lds_dwordx4 %1, %2 offset:3072 sc1\n\ts_mov_b32 m0, %0"
               : "=&s"(keep)
               : "v"(voff), "s"(sbase), "s"(lds)
               : "memory");
}
__device__ __forceinline__ v2u trd(const unsigned a) {
  return __builtin_bit_cast(v2u, __builtin_amdgcn_ds_read_tr16_b64_v4i16((lp4s)(unsigned long)a));
}
__device__ __forceinline__ unsigned lds_base(char* smem) { return (unsigned)(unsigned long)(__attribute__((address_space(3))) char*)smem; }
#define RAW_BAR()                                           \
  do {                                                      \
    asm volatile("s_waitcnt lgkmcnt(0)" ::: "memory");      \
    __builtin_amdgcn_s_barrier();                           \
    asm volatile("" ::: "memory");                          \
  } while (0)
#define WAIT_VM(n) asm volatile("s_waitcnt vmcnt(" #n ")" ::: "memory")
template <bool SP>
__device__ __forceinline__ void wqkv_issue(const u16* Wm, const int hf, const int h, const int w, const int l, const unsigned sb) {
  const char* base = (const char*)Wm + (size_t)(256 * hf + 32 * w) * (HD * 2) + h * (DH * 2);
#pragma unroll
  for (int i = 0; i < 8; ++i) {
    const int r = l >> 4;
    const int f = SP ? ((r & 1) | ((i & 1) << 1) | (((i >> 1) & 1) << 2)) : (r | (((i >> 1) & 1) << 2));
    glds16(base, (unsigned)((4 * i + r) * (HD * 2) + (((l & 15) ^ (f << 1)) << 4)), sb + (w >> 2) * 65536 + hf * 32768 + (w & 3) * 8192 + i * 1024);
  }
}

__device__ __forceinline__ unsigned row_stats(const v4u raw) {
  float v[8];
#pragma unroll
  for (int e = 0; e < 4; ++e) { v[2 * e] = lo16(raw[e]); v[2 * e + 1] = hi16(raw[e]); }
  float s = 0.f;
#pragma unroll
  for (int e = 0; e < 8; ++e) s += v[e];
  const float mean = wave_sum(s) * (1.0f / DM);
  float q = 0.f;
#pragma unroll
  for (int e = 0; e < 8; ++e) { const float t = v[e] - mean; q += t * t; }
  const float var = wave_sum(q) * (1.0f / DM);
  const float sd = bfr(sqrtf(bfr(bfr(var) + 1e-5f)));
  return (unsigned)f2bf(mean) | ((unsigned)f2bf(sd) << 16);
}
__device__ __forceinline__ void ask_slow(const MegaArgs& A, const int k, unsigned (&t)[3]) {
  const int l = lane_id(), x = blockIdx.x & 7;
  const char *wq = (const char*)A.Wq, *wv = (const char*)A.Wv;
  asm("" : "+s"(wq), "+s"(wv));
#pragma unroll
  for (int m = 0; m < 3; ++m) {
    const char* W = m == 1 ? (const char*)A.Wk : ((m == 0) == (x < 4)) ? wv : wq;
    const int a = (int)((unsigned long)W >> 8) + 8 * (x & 3), h = k < 32 ? ((1 - a) & 3) + 4 * (k & 1) : (31 - a) & 31;
    const int r = k < 32 ? k >> 1 : k - 32;
    t[m] = __builtin_amdgcn_raw_buffer_load_b32(MKRS(W, (unsigned)k < 48u && h < 8 ? DM * HD * 2 : 0), (l >> 1) * (HD * 2) + (l & 1) * 128,
                                                (32 * (r & 15)) * (HD * 2) + (8 * (x & 3) + (h & 7)) * 256, 0);
  }
}
#define ASKED(t) asm volatile("" ::"v"(t[0]), "v"(t[1]), "v"(t[2]))
template <int N>
__device__ __forceinline__ u64 start_stats(const MegaArgs& A, const int idx) {
  const int w = A.wv, l = lane_id();
  const int r0 = N * (8 * idx + w);
  unsigned ta[3], tb[3];
  v4u raw[N];
#pragma unroll
  for (int i = 0; i < N; ++i) raw[i] = *reinterpret_cast<const v4u*>(A.x0 + (size_t)(r0 + i) * DM + l * 8);
  asm volatile("" ::: "memory");
  ask_slow(A, 16 * w + (idx & 3) + (idx >> 4) * N, ta);
  if (N > 4) ask_slow(A, 16 * w + (idx & 3) + 12, tb);
  unsigned tc[2] = {};
  if (N == 4 && AWH > 0) tc[0] = *reinterpret_cast<const unsigned*>(A.Wo + (size_t)idx * 65536 + 32768 + (64 * w + l) * 64);
  if (N == 4 && AWH > 1 && w >= 4) tc[1] = *reinterpret_cast<const unsigned*>(A.Wo + (size_t)idx * 65536 + (64 * w + l) * 64);
  const u64 tag = call_tag(A);
  unsigned stw = 0u;
#pragma unroll
  for (int i = 0; i < N; ++i) {
    const unsigned t = row_stats(raw[i]);
    if ((l >> 1) == i) stw = t;
  }
  if (l < 2 * N) __hip_atomic_store((gu64*)(A.ws + OFF_GT) + 2 * r0 + l, tagged(tag, (l & 1) ? stw : stw << 16), RLX_AGENT);
  SST(0);
  ASKED(ta);
  if (N > 4) ASKED(tb);
  asm volatile("" ::"v"(tc[0]), "v"(tc[1]));
  return tag;
}
#define MFMAH(a, b, c) __builtin_amdgcn_mfma_f32_16x16x32_f16(a, b, c, 0, 0, 0)
struct Cx {
  f32x2_t r, i;
};
__device__ __forceinline__ Cx cmul(const Cx a, const Cx b) { return Cx{a.r * b.r - a.i * b.i, a.r * b.i + a.i * b.r}; }
struct KGen {
  unsigned pa, pb, pc, pd;
  u16 pe;
  f32x2_t ar, ai;
  Cx C;
  char* st;
  v4u bh[8], bl[8];
  __device__ __forceinline__ void load(const MegaArgs& A, const int d, const int l) {
    pa = *reinterpret_cast<const unsigned*>(A.logAre + d * NST + 2 * l);
    pb = *reinterpret_cast<const unsigned*>(A.Aim + d * NST + 2 * l);
    pc = *reinterpret_cast<const unsigned*>(A.Cre + d * NST + 2 * l);
    pd = *reinterpret_cast<const unsigned*>(A.Cim + d * NST + 2 * l);
    pe = A.logdt[d];
  }
  __device__ __forceinline__ void setup() {
    const float dt = bfr(expf(bf2f(pe)));
    ar = f32x2_t{-bfr(expf(lo16(pa))) * dt, -bfr(expf(hi16(pa))) * dt};
    ai = f32x2_t{lo16(pb) * dt, hi16(pb) * dt};
    C = Cx{f32x2_t{lo16(pc) * dt, hi16(pc) * dt}, f32x2_t{lo16(pd) * dt, hi16(pd) * dt}};
  }
  __device__ __forceinline__ float scale() {
    const float b = 2.0f * wave_sum((fabsf(C.r.x) + fabsf(C.r.y)) + (fabsf(C.i.x) + fabsf(C.i.y)));
    const unsigned eb = max(__float_as_uint(b) >> 23, 25u);
    const float ks = __uint_as_float((268u - eb) << 23);
    C.r *= ks;
    C.i *= ks;
    return __uint_as_float((eb - 24u) << 23);
  }
  __device__ __forceinline__ Cx power(const float p) const {
    Cx z;
#pragma unroll
    for (int e = 0; e < 2; ++e) {
      const float x = ai[e] * p;
      const float f = __builtin_fmaf(x, 6.4206382e-9f, __builtin_fmaf(x, 0.15915494f, -__builtin_rintf(x * 0.15915494f)));
      const float m = __expf(ar[e] * p);
      z.r[e] = m * __builtin_amdgcn_cosf(f);
      z.i[e] = m * __builtin_amdgcn_sinf(f);
    }
    return z;
  }
  static __device__ __forceinline__ unsigned rest(const f32x2_t y, const unsigned h) {
    unsigned r;
    asm("v_fma_mixlo_f16 %0, %1, -1.0, %2 op_sel_hi:[1,0,0]" : "=v"(r) : "v"(h), "v"(y.x));
    asm("v_fma_mixhi_f16 %0, %1, -1.0, %2 op_sel:[1,0,0] op_sel_hi:[1,0,0]" : "+v"(r) : "v"(h), "v"(y.y));
    return r;
  }
  __device__ __forceinline__ char* rows(char* p, Cx& y, const Cx s, const int n) const {
#pragma unroll 1
    for (int i = 0; i < n; i += 2) {
#pragma unroll
      for (int k = 0; k < 2; ++k) {
        const unsigned h0 = pkh(y.r.x, y.r.y), h1 = pkh(y.i.x, y.i.y);
        *reinterpret_cast<v2u*>(p + 1040 * k) = v2u{h0, h1};
        *reinterpret_cast<v2u*>(p + 1040 * k + 512) = v2u{rest(y.r, h0), rest(y.i, h1)};
        y = cmul(y, s);
      }
      p += 2080;
    }
    return p;
  }
  __device__ __forceinline__ void get(v4u (&h)[8], v4u (&o)[8], const int l) const {
    const char* p = st + (l & 15) * 1040 + (l >> 4) * 16;
    asm volatile("" ::: "memory");
#pragma unroll
    for (int k = 0; k < 8; ++k) {
      h[k] = *reinterpret_cast<const v4u*>(p + 64 * k);
      o[k] = *reinterpret_cast<const v4u*>(p + 512 + 64 * k);
    }
    asm volatile("" ::: "memory");
  }
  Cx cy, cs;
  __device__ __forceinline__ void columns(const int l, const int j0, const int j1) {
    if (j0 == 0) {
      cs = power(16.f);
      cs.i = -cs.i;
      cy = Cx{f32x2_t{16384.f, 16384.f}, f32x2_t{0.f, 0.f}};
    }
    rows(st + l * 8 + 1040 * j0, cy, cs, j1 - j0);
  }
  template <bool COL>
  __device__ __forceinline__ f32x4 tile(const int l) const {
    v4u h[8], o[8];
    get(h, o, l);
    f32x4 d = {}, e = {}, f = {};
#define H8(x) __builtin_bit_cast(f16x8, x)
#define KMM(a, b, d) (COL ? MFMAH(H8(b), H8(a), d) : MFMAH(H8(a), H8(b), d))
#pragma unroll
    for (int k = 0; k < 8; ++k) {
      d = KMM(h[k], bh[k], d);
      e = KMM(o[k], bh[k], e);
      f = KMM(h[k], bl[k], f);
    }
#undef KMM
#undef H8
    return (d + (e + f)) * (1.0f / 8192);
  }
};
#define ACC_INIT 0xc9b26c86c9b26ca0ull
#define CHAIN_ASM_ATT \
  "s_mov_b32 s95, 272\n" \
  "s_mov_b32 s91, 0x4c7c2000\n" \
  "s_mov_b32 s85, 0xc9b26c9b\n" \
  "s_mov_b32 s92, 0xc9326c9b\n" \
  "s_mov_b32 s93, 0xc9326c9b\n" \
  "s_mov_b32 s78, 0x3e0293ee\n" \
  "s_mov_b32 s76, 0x33333333\n" \
  "s_mov_b32 s77, 0x33333333\n" \
  "s_lshl_b32 s98, s73, 10\n" \
  "s_add_u32 s98, s98, s74\n" \
  "s_add_u32 s98, s98, 131072\n" \
  "s_lshl_b32 s97, s73, 9\n" \
  "s_add_u32 s97, s97, s74\n" \
  "s_add_u32 s97, s97, 158720\n" \
  "s_lshl_b32 s99, s73, 10\n" \
  "v_mbcnt_lo_u32_b32 v48, -1, 0\n" \
  "v_mbcnt_hi_u32_b32 v48, -1, v48\n" \
  "s_add_u32 s88, s74, 157760\n" \
  "v_mov_b32 v59, s88\n" \
  "v_lshrrev_b32 v49, 2, v48\n" \
  "v_lshrrev_b32 v50, 4, v48\n" \
  "v_bitop3_b32 v50, v49, v50, 3 bitop3:0x28\n" \
  "v_lshl_add_u32 v8, v50, 3, s97\n" \
  "v_and_b32 v50, 3, v48\n" \
  "v_lshl_add_u32 v8, v50, 5, v8\n" \
  "v_and_b32 v49, 1, v49\n" \
  "v_lshl_add_u32 v8, v49, 8, v8\n" \
  "v_and_b32 v49, 8, v48\n" \
  "v_lshl_add_u32 v8, v49, 4, v8\n" \
  "v_lshrrev_b32 v49, 5, v48\n" \
  "v_add_u32 v10, s99, v1\n" \
  "s_lshr_b32 s88, s99, 2\n" \
  "s_add_u32 s88, s88, s74\n" \
  "s_add_u32 s88, s88, 139264\n" \
  "v_lshl_add_u32 v7, v49, 10, s88\n" \
  "v_and_b32 v49, 31, v48\n" \
  "v_lshl_add_u32 v7, v49, 3, v7\n" \
  "v_mov_b64 v[12:13], 0\n" \
  "v_mov_b64 v[14:15], 0\n" \
  "v_mov_b64 v[16:17], 0\n" \
  "v_mov_b64 v[18:19], 0\n" \
  "v_mov_b64 v[20:21], 0\n" \
  "v_mov_b64 v[22:23], 0\n" \
  "v_mov_b32 v34, 0xc9b26ca0\n" \
  "v_mov_b32 v35, 0xc9b26c86\n" \
  "s_mov_b32 s87, 0x40000\n" \
  "v_subrev_u32 v30, 64, v59\n" \
  "v_add_u32 v17, 0x10000, v28\n" \
  "v_xor_b32 v18, 128, v28\n" \
  "v_xor_b32 v19, 128, v17\n" \
  "ds_write_b32 v30, v12 offset:0\n" \
  "ds_write_b32 v30, v12 offset:32\n" \
  "ds_write_b32 v30, v12 offset:64\n" \
  "v_xor_b32 v20, 0, v29\n" \
  "v_xor_b32 v21, 32, v29\n" \
  "v_xor_b32 v22, 64, v29\n" \
  "v_xor_b32 v23, 96, v29\n" \
  "s_waitcnt vmcnt(12) lgkmcnt(0)\n" \
  "s_barrier\n" \
  "ds_read_b64_tr_b16 v[160:161], v20 offset:0\n" \
  "ds_read_b64_tr_b16 v[162:163], v20 offset:2048\n" \
  "ds_read_b64_tr_b16 v[168:169], v21 offset:0\n" \
  "ds_read_b64_tr_b16 v[170:171], v21 offset:2048\n" \
  "ds_read_b64_tr_b16 v[176:177], v22 offset:0\n" \
  "ds_read_b64_tr_b16 v[178:179], v22 offset:2048\n" \
  "ds_read_b64_tr_b16 v[184:185], v23 offset:0\n" \
  "ds_read_b64_tr_b16 v[186:187], v23 offset:2048\n" \
  "s_lshl_b32 s88, s73, 10\n" \
  "v_add_u32 v48, s88, v1\n" \
  "ds_read_b128 v[52:55], v48 offset:0\n" \
  "s_waitcnt lgkmcnt(0)\n" \
  "v_mov_b32 v11, 0\n" \
  "v_dot2c_f32_bf16 v11, v52, v52\n" \
  "v_dot2c_f32_bf16 v11, v53, v53\n" \
  "v_dot2c_f32_bf16 v11, v54, v54\n" \
  "v_dot2c_f32_bf16 v11, v55, v55\n" \
  "s_nop 0\n" \
  "s_nop 1\n" \
  "v_max_u32_dpp v11, v11, v11 row_ror:1 row_mask:0xf bank_mask:0xf\n" \
  "s_nop 1\n" \
  "v_max_u32_dpp v11, v11, v11 row_ror:2 row_mask:0xf bank_mask:0xf\n" \
  "s_nop 1\n" \
  "v_max_u32_dpp v11, v11, v11 row_ror:4 row_mask:0xf bank_mask:0xf\n" \
  "s_nop 1\n" \
  "v_max_u32_dpp v11, v11, v11 row_ror:8 row_mask:0xf bank_mask:0xf\n" \
  "v_mov_b32 v31, v11\n" \
  "s_nop 1\n" \
  "v_permlane16_swap_b32 v11, v31\n" \
  "v_max_u32 v11, v11, v31\n" \
  "v_mov_b32 v31, v11\n" \
  "s_nop 1\n" \
  "v_permlane32_swap_b32 v11, v31\n" \
  "v_max_u32 v11, v11, v31\n" \
  "s_mov_b64 exec, 1\n" \
  "ds_max_u32 v59, v11\n" \
  "s_mov_b64 exec, -1\n" \
  "s_waitcnt vmcnt(8) lgkmcnt(0)\n" \
  "s_barrier\n" \
  "ds_read_b64_tr_b16 v[164:165], v20 offset:4096\n" \
  "ds_read_b64_tr_b16 v[166:167], v20 offset:6144\n" \
  "ds_read_b64_tr_b16 v[172:173], v21 offset:4096\n" \
  "ds_read_b64_tr_b16 v[174:175], v21 offset:6144\n" \
  "ds_read_b64_tr_b16 v[180:181], v22 offset:4096\n" \
  "ds_read_b64_tr_b16 v[182:183], v22 offset:6144\n" \
  "ds_read_b64_tr_b16 v[188:189], v23 offset:4096\n" \
  "ds_read_b64_tr_b16 v[190:191], v23 offset:6144\n" \
  "s_add_u32 m0, s19, 0\n" \
  "s_nop 0\n" \
  "global_load_lds_dwordx4 v24, s[4:5] offset:0\n" \
  "global_load_lds_dwordx4 v25, s[4:5] offset:1024\n" \
  "global_load_lds_dwordx4 v26, s[4:5] offset:2048\n" \
  "global_load_lds_dwordx4 v27, s[4:5] offset:3072\n" \
  "s_add_u32 s4, s4, 0x100000\n" \
  "s_addc_u32 s5, s5, 0\n" \
  "v_mfma_f32_16x16x32_bf16 v[64:67], v[160:163], v[160:163], 0\n" \
  "v_mfma_f32_16x16x32_bf16 v[68:71], v[168:171], v[168:171], 0\n" \
  "v_mfma_f32_16x16x32_bf16 v[72:75], v[176:179], v[176:179], 0\n" \
  "v_mfma_f32_16x16x32_bf16 v[76:79], v[184:187], v[184:187], 0\n" \
  "s_waitcnt vmcnt(8) lgkmcnt(0)\n" \
  "s_barrier\n" \
  "ds_read_b64_tr_b16 v[32:33], v20 offset:8192\n" \
  "ds_read_b64_tr_b16 v[34:35], v20 offset:10240\n" \
  "ds_read_b64_tr_b16 v[40:41], v21 offset:8192\n" \
  "ds_read_b64_tr_b16 v[42:43], v21 offset:10240\n" \
  "ds_read_b64_tr_b16 v[48:49], v22 offset:8192\n" \
  "ds_read_b64_tr_b16 v[50:51], v22 offset:10240\n" \
  "ds_read_b64_tr_b16 v[56:57], v23 offset:8192\n" \
  "ds_read_b64_tr_b16 v[58:59], v23 offset:10240\n" \
  "s_add_u32 m0, s19, 4096\n" \
  "s_nop 0\n" \
  "global_load_lds_dwordx4 v24, s[4:5] offset:0\n" \
  "global_load_lds_dwordx4 v25, s[4:5] offset:1024\n" \
  "global_load_lds_dwordx4 v26, s[4:5] offset:2048\n" \
  "global_load_lds_dwordx4 v27, s[4:5] offset:3072\n" \
  "s_add_u32 s4, s4, 0x100000\n" \
  "s_addc_u32 s5, s5, 0\n" \
  "v_mfma_f32_16x16x32_bf16 v[64:67], v[164:167], v[164:167], v[64:67]\n" \
  "v_mfma_f32_16x16x32_bf16 v[68:71], v[172:175], v[172:175], v[68:71]\n" \
  "v_mfma_f32_16x16x32_bf16 v[72:75], v[180:183], v[180:183], v[72:75]\n" \
  "v_mfma_f32_16x16x32_bf16 v[76:79], v[188:191], v[188:191], v[76:79]\n" \
  "s_waitcnt vmcnt(8) lgkmcnt(0)\n" \
  "s_barrier\n" \
  "ds_read_b64_tr_b16 v[36:37], v20 offset:12288\n" \
  "ds_read_b64_tr_b16 v[38:39], v20 offset:14336\n" \
  "ds_read_b64_tr_b16 v[44:45], v21 offset:12288\n" \
  "ds_read_b64_tr_b16 v[46:47], v21 offset:14336\n" \
  "ds_read_b64_tr_b16 v[52:53], v22 offset:12288\n" \
  "ds_read_b64_tr_b16 v[54:55], v22 offset:14336\n" \
  "ds_read_b64_tr_b16 v[60:61], v23 offset:12288\n" \
  "ds_read_b64_tr_b16 v[62:63], v23 offset:14336\n" \
  "s_add_u32 m0, s19, 8192\n" \
  "s_nop 0\n" \
  "global_load_lds_dwordx4 v24, s[4:5] offset:0\n" \
  "global_load_lds_dwordx4 v25, s[4:5] offset:1024\n" \
  "global_load_lds_dwordx4 v26, s[4:5] offset:2048\n" \
  "global_load_lds_dwordx4 v27, s[4:5] offset:3072\n" \
  "s_add_u32 s4, s4, 0x100000\n" \
  "s_addc_u32 s5, s5, 0\n" \
  "v_mfma_f32_16x16x32_bf16 v[64:67], v[32:35], v[32:35], v[64:67]\n" \
  "v_mfma_f32_16x16x32_bf16 v[68:71], v[40:43], v[40:43], v[68:71]\n" \
  "v_mfma_f32_16x16x32_bf16 v[72:75], v[48:51], v[48:51], v[72:75]\n" \
  "v_mfma_f32_16x16x32_bf16 v[76:79], v[56:59], v[56:59], v[76:79]\n" \
  "s_waitcnt vmcnt(8) lgkmcnt(0)\n" \
  "s_barrier\n" \
  "s_add_u32 m0, s19, 12288\n" \
  "s_nop 0\n" \
  "global_load_lds_dwordx4 v24, s[4:5] offset:0\n" \
  "global_load_lds_dwordx4 v25, s[4:5] offset:1024\n" \
  "global_load_lds_dwordx4 v26, s[4:5] offset:2048\n" \
  "global_load_lds_dwordx4 v27, s[4:5] offset:3072\n" \
  "s_add_u32 s4, s4, 0x100000\n" \
  "s_addc_u32 s5, s5, 0\n" \
  "v_mfma_f32_16x16x32_bf16 v[64:67], v[36:39], v[36:39], v[64:67]\n" \
  "v_mfma_f32_16x16x32_bf16 v[68:71], v[44:47], v[44:47], v[68:71]\n" \
  "v_mfma_f32_16x16x32_bf16 v[72:75], v[52:55], v[52:55], v[72:75]\n" \
  "v_mfma_f32_16x16x32_bf16 v[76:79], v[60:63], v[60:63], v[76:79]\n" \
  "s_nop 7\n" \
  "v_max3_f32 v13, v64, v65, v66\n" \
  "v_max3_f32 v13, v13, v67, v68\n" \
  "v_max3_f32 v13, v13, v69, v70\n" \
  "v_max3_f32 v13, v13, v71, v72\n" \
  "v_max3_f32 v13, v13, v73, v74\n" \
  "v_max3_f32 v13, v13, v75, v76\n" \
  "v_max3_f32 v13, v13, v77, v78\n" \
  "v_max_f32 v13, v13, v79\n" \
  "s_nop 1\n" \
  "v_max_f32_dpp v13, v13, v13 row_ror:1 row_mask:0xf bank_mask:0xf\n" \
  "s_nop 1\n" \
  "v_max_f32_dpp v13, v13, v13 row_ror:2 row_mask:0xf bank_mask:0xf\n" \
  "s_nop 1\n" \
  "v_max_f32_dpp v13, v13, v13 row_ror:4 row_mask:0xf bank_mask:0xf\n" \
  "s_nop 1\n" \
  "v_max_f32_dpp v13, v13, v13 row_ror:8 row_mask:0xf bank_mask:0xf\n" \
  "v_mov_b32 v31, v13\n" \
  "s_nop 1\n" \
  "v_permlane16_swap_b32 v13, v31\n" \
  "v_max_f32 v13, v13, v31\n" \
  "v_mov_b32 v31, v13\n" \
  "s_nop 1\n" \
  "v_permlane32_swap_b32 v13, v31\n" \
  "v_max_f32 v13, v13, v31\n" \
  "s_mov_b64 exec, 1\n" \
  "ds_max_u32 v30, v13 offset:32\n" \
  "s_mov_b64 exec, -1\n" \
  "s_waitcnt vmcnt(8) lgkmcnt(0)\n" \
  "s_barrier\n" \
  "s_add_u32 m0, s19, 0\n" \
  "s_nop 0\n" \
  "global_load_lds_dwordx4 v24, s[6:7] offset:0\n" \
  "global_load_lds_dwordx4 v25, s[6:7] offset:1024\n" \
  "global_load_lds_dwordx4 v26, s[6:7] offset:2048\n" \
  "global_load_lds_dwordx4 v27, s[6:7] offset:3072\n" \
  "s_add_u32 s6, s6, 0x100000\n" \
  "s_addc_u32 s7, s7, 0\n" \
  "s_waitcnt vmcnt(8) lgkmcnt(0)\n" \
  "s_barrier\n" \
  "s_add_u32 m0, s19, 4096\n" \
  "s_nop 0\n" \
  "global_load_lds_dwordx4 v24, s[6:7] offset:0\n" \
  "global_load_lds_dwordx4 v25, s[6:7] offset:1024\n" \
  "global_load_lds_dwordx4 v26, s[6:7] offset:2048\n" \
  "global_load_lds_dwordx4 v27, s[6:7] offset:3072\n" \
  "s_add_u32 s6, s6, 0x100000\n" \
  "s_addc_u32 s7, s7, 0\n" \
  "s_waitcnt vmcnt(8) lgkmcnt(0)\n" \
  "s_barrier\n" \
  "s_add_u32 m0, s19, 8192\n" \
  "s_nop 0\n" \
  "global_load_lds_dwordx4 v24, s[6:7] offset:0\n" \
  "global_load_lds_dwordx4 v25, s[6:7] offset:1024\n" \
  "global_load_lds_dwordx4 v26, s[6:7] offset:2048\n" \
  "global_load_lds_dwordx4 v27, s[6:7] offset:3072\n" \
  "s_add_u32 s6, s6, 0x100000\n" \
  "s_addc_u32 s7, s7, 0\n" \
  "s_waitcnt vmcnt(8) lgkmcnt(0)\n" \
  "s_barrier\n" \
  "ds_read_b64_tr_b16 v[64:65], v28 offset:0\n" \
  "ds_read_b64_tr_b16 v[66:67], v28 offset:512\n" \
  "ds_read_b64_tr_b16 v[68:69], v28 offset:32768\n" \
  "ds_read_b64_tr_b16 v[70:71], v28 offset:33280\n" \
  "ds_read_b64_tr_b16 v[72:73], v17 offset:0\n" \
  "ds_read_b64_tr_b16 v[74:75], v17 offset:512\n" \
  "ds_read_b64_tr_b16 v[76:77], v17 offset:32768\n" \
  "ds_read_b64_tr_b16 v[78:79], v17 offset:33280\n" \
  "ds_read_b64_tr_b16 v[192:193], v18 offset:0\n" \
  "ds_read_b64_tr_b16 v[194:195], v18 offset:512\n" \
  "ds_read_b64_tr_b16 v[196:197], v18 offset:32768\n" \
  "ds_read_b64_tr_b16 v[198:199], v18 offset:33280\n" \
  "ds_read_b64_tr_b16 v[200:201], v19 offset:0\n" \
  "ds_read_b64_tr_b16 v[202:203], v19 offset:512\n" \
  "ds_read_b64_tr_b16 v[204:205], v19 offset:32768\n" \
  "ds_read_b64_tr_b16 v[206:207], v19 offset:33280\n" \
  "s_add_u32 m0, s19, 12288\n" \
  "s_nop 0\n" \
  "global_load_lds_dwordx4 v24, s[6:7] offset:0\n" \
  "global_load_lds_dwordx4 v25, s[6:7] offset:1024\n" \
  "global_load_lds_dwordx4 v26, s[6:7] offset:2048\n" \
  "global_load_lds_dwordx4 v27, s[6:7] offset:3072\n" \
  "s_add_u32 s6, s6, 0x100000\n" \
  "s_addc_u32 s7, s7, 0\n" \
  "s_waitcnt vmcnt(8) lgkmcnt(0)\n" \
  "s_barrier\n" \
  "ds_read_b64_tr_b16 v[80:81], v28 offset:4096\n" \
  "ds_read_b64_tr_b16 v[82:83], v28 offset:4608\n" \
  "ds_read_b64_tr_b16 v[84:85], v28 offset:36864\n" \
  "ds_read_b64_tr_b16 v[86:87], v28 offset:37376\n" \
  "ds_read_b64_tr_b16 v[88:89], v17 offset:4096\n" \
  "ds_read_b64_tr_b16 v[90:91], v17 offset:4608\n" \
  "ds_read_b64_tr_b16 v[92:93], v17 offset:36864\n" \
  "ds_read_b64_tr_b16 v[94:95], v17 offset:37376\n" \
  "ds_read_b64_tr_b16 v[208:209], v18 offset:4096\n" \
  "ds_read_b64_tr_b16 v[210:211], v18 offset:4608\n" \
  "ds_read_b64_tr_b16 v[212:213], v18 offset:36864\n" \
  "ds_read_b64_tr_b16 v[214:215], v18 offset:37376\n" \
  "ds_read_b64_tr_b16 v[216:217], v19 offset:4096\n" \
  "ds_read_b64_tr_b16 v[218:219], v19 offset:4608\n" \
  "ds_read_b64_tr_b16 v[220:221], v19 offset:36864\n" \
  "ds_read_b64_tr_b16 v[222:223], v19 offset:37376\n" \
  "s_add_u32 m0, s19, 0\n" \
  "s_nop 0\n" \
  "global_load_lds_dwordx4 v24, s[8:9] offset:0\n" \
  "global_load_lds_dwordx4 v25, s[8:9] offset:1024\n" \
  "global_load_lds_dwordx4 v26, s[8:9] offset:2048\n" \
  "global_load_lds_dwordx4 v27, s[8:9] offset:3072\n" \
  "s_add_u32 s8, s8, 0x100000\n" \
  "s_addc_u32 s9, s9, 0\n" \
  "s_waitcnt vmcnt(8) lgkmcnt(0)\n" \
  "s_barrier\n" \
  "ds_read_b64_tr_b16 v[96:97], v28 offset:8192\n" \
  "ds_read_b64_tr_b16 v[98:99], v28 offset:8704\n" \
  "ds_read_b64_tr_b16 v[100:101], v28 offset:40960\n" \
  "ds_read_b64_tr_b16 v[102:103], v28 offset:41472\n" \
  "ds_read_b64_tr_b16 v[104:105], v17 offset:8192\n" \
  "ds_read_b64_tr_b16 v[106:107], v17 offset:8704\n" \
  "ds_read_b64_tr_b16 v[108:109], v17 offset:40960\n" \
  "ds_read_b64_tr_b16 v[110:111], v17 offset:41472\n" \
  "ds_read_b64_tr_b16 v[224:225], v18 offset:8192\n" \
  "ds_read_b64_tr_b16 v[226:227], v18 offset:8704\n" \
  "ds_read_b64_tr_b16 v[228:229], v18 offset:40960\n" \
  "ds_read_b64_tr_b16 v[230:231], v18 offset:41472\n" \
  "ds_read_b64_tr_b16 v[232:233], v19 offset:8192\n" \
  "ds_read_b64_tr_b16 v[234:235], v19 offset:8704\n" \
  "ds_read_b64_tr_b16 v[236:237], v19 offset:40960\n" \
  "ds_read_b64_tr_b16 v[238:239], v19 offset:41472\n" \
  "s_add_u32 m0, s19, 4096\n" \
  "s_nop 0\n" \
  "global_load_lds_dwordx4 v24, s[8:9] offset:0\n" \
  "global_load_lds_dwordx4 v25, s[8:9] offset:1024\n" \
  "global_load_lds_dwordx4 v26, s[8:9] offset:2048\n" \
  "global_load_lds_dwordx4 v27, s[8:9] offset:3072\n" \
  "s_add_u32 s8, s8, 0x100000\n" \
  "s_addc_u32 s9, s9, 0\n" \
  "s_waitcnt vmcnt(8) lgkmcnt(0)\n" \
  "s_barrier\n" \
  "ds_read_b64_tr_b16 v[112:113], v28 offset:12288\n" \
  "ds_read_b64_tr_b16 v[114:115], v28 offset:12800\n" \
  "ds_read_b64_tr_b16 v[116:117], v28 offset:45056\n" \
  "ds_read_b64_tr_b16 v[118:119], v28 offset:45568\n" \
  "ds_read_b64_tr_b16 v[120:121], v17 offset:12288\n" \
  "ds_read_b64_tr_b16 v[122:123], v17 offset:12800\n" \
  "ds_read_b64_tr_b16 v[124:125], v17 offset:45056\n" \
  "ds_read_b64_tr_b16 v[126:127], v17 offset:45568\n" \
  "ds_read_b64_tr_b16 v[240:241], v18 offset:12288\n" \
  "ds_read_b64_tr_b16 v[242:243], v18 offset:12800\n" \
  "ds_read_b64_tr_b16 v[244:245], v18 offset:45056\n" \
  "ds_read_b64_tr_b16 v[246:247], v18 offset:45568\n" \
  "ds_read_b64_tr_b16 v[248:249], v19 offset:12288\n" \
  "ds_read_b64_tr_b16 v[250:251], v19 offset:12800\n" \
  "ds_read_b64_tr_b16 v[252:253], v19 offset:45056\n" \
  "ds_read_b64_tr_b16 v[254:255], v19 offset:45568\n" \
  "s_add_u32 m0, s19, 8192\n" \
  "s_nop 0\n" \
  "global_load_lds_dwordx4 v24, s[8:9] offset:0\n" \
  "global_load_lds_dwordx4 v25, s[8:9] offset:1024\n" \
  "global_load_lds_dwordx4 v26, s[8:9] offset:2048\n" \
  "global_load_lds_dwordx4 v27, s[8:9] offset:3072\n" \
  "s_add_u32 s8, s8, 0x100000\n" \
  "s_addc_u32 s9, s9, 0\n" \
  "s_waitcnt vmcnt(8) lgkmcnt(0)\n" \
  "s_barrier\n" \
  "ds_read_b64_tr_b16 v[128:129], v28 offset:0\n" \
  "ds_read_b64_tr_b16 v[130:131], v28 offset:512\n" \
  "ds_read_b64_tr_b16 v[132:133], v28 offset:32768\n" \
  "ds_read_b64_tr_b16 v[134:135], v28 offset:33280\n" \
  "ds_read_b64_tr_b16 v[136:137], v17 offset:0\n" \
  "ds_read_b64_tr_b16 v[138:139], v17 offset:512\n" \
  "ds_read_b64_tr_b16 v[140:141], v17 offset:32768\n" \
  "ds_read_b64_tr_b16 v[142:143], v17 offset:33280\n" \
  "s_add_u32 m0, s19, 12288\n" \
  "s_nop 0\n" \
  "global_load_lds_dwordx4 v24, s[8:9] offset:0\n" \
  "global_load_lds_dwordx4 v25, s[8:9] offset:1024\n" \
  "global_load_lds_dwordx4 v26, s[8:9] offset:2048\n" \
  "global_load_lds_dwordx4 v27, s[8:9] offset:3072\n" \
  "s_add_u32 s8, s8, 0x100000\n" \
  "s_addc_u32 s9, s9, 0\n" \
  "s_waitcnt vmcnt(8) lgkmcnt(0)\n" \
  "s_barrier\n" \
  "ds_write_b128 v6, v[160:163] offset:0\n" \
  "ds_write_b128 v6, v[164:167] offset:1024\n" \
  "ds_write_b128 v6, v[168:171] offset:2048\n" \
  "ds_write_b128 v6, v[172:175] offset:3072\n" \
  "ds_read_b64_tr_b16 v[144:145], v28 offset:4096\n" \
  "ds_read_b64_tr_b16 v[146:147], v28 offset:4608\n" \
  "ds_read_b64_tr_b16 v[148:149], v28 offset:36864\n" \
  "ds_read_b64_tr_b16 v[150:151], v28 offset:37376\n" \
  "ds_read_b64_tr_b16 v[152:153], v17 offset:4096\n" \
  "ds_read_b64_tr_b16 v[154:155], v17 offset:4608\n" \
  "ds_read_b64_tr_b16 v[156:157], v17 offset:36864\n" \
  "ds_read_b64_tr_b16 v[158:159], v17 offset:37376\n" \
  "s_waitcnt vmcnt(4) lgkmcnt(0)\n" \
  "s_barrier\n" \
  "ds_write_b128 v6, v[176:179] offset:4096\n" \
  "ds_write_b128 v6, v[180:183] offset:5120\n" \
  "ds_write_b128 v6, v[184:187] offset:6144\n" \
  "ds_write_b128 v6, v[188:191] offset:7168\n" \
  "ds_read_b64_tr_b16 v[160:161], v28 offset:8192\n" \
  "ds_read_b64_tr_b16 v[162:163], v28 offset:8704\n" \
  "ds_read_b64_tr_b16 v[164:165], v28 offset:40960\n" \
  "ds_read_b64_tr_b16 v[166:167], v28 offset:41472\n" \
  "ds_read_b64_tr_b16 v[168:169], v17 offset:8192\n" \
  "ds_read_b64_tr_b16 v[170:171], v17 offset:8704\n" \
  "ds_read_b64_tr_b16 v[172:173], v17 offset:40960\n" \
  "ds_read_b64_tr_b16 v[174:175], v17 offset:41472\n" \
  "s_waitcnt vmcnt(0) lgkmcnt(0)\n" \
  "s_barrier\n" \
  "ds_write_b128 v6, v[32:35] offset:8192\n" \
  "ds_write_b128 v6, v[36:39] offset:9216\n" \
  "ds_write_b128 v6, v[40:43] offset:10240\n" \
  "ds_write_b128 v6, v[44:47] offset:11264\n" \
  "ds_read_b64_tr_b16 v[176:177], v28 offset:12288\n" \
  "ds_read_b64_tr_b16 v[178:179], v28 offset:12800\n" \
  "ds_read_b64_tr_b16 v[180:181], v28 offset:45056\n" \
  "ds_read_b64_tr_b16 v[182:183], v28 offset:45568\n" \
  "ds_read_b64_tr_b16 v[184:185], v17 offset:12288\n" \
  "ds_read_b64_tr_b16 v[186:187], v17 offset:12800\n" \
  "ds_read_b64_tr_b16 v[188:189], v17 offset:45056\n" \
  "ds_read_b64_tr_b16 v[190:191], v17 offset:45568\n" \
  "ds_read_b32 v31, v30 offset:0\n" \
  "s_waitcnt lgkmcnt(0)\n" \
  "s_barrier\n" \
  "ds_write_b128 v6, v[48:51] offset:12288\n" \
  "ds_write_b128 v6, v[52:55] offset:13312\n" \
  "ds_write_b128 v6, v[56:59] offset:14336\n" \
  "ds_write_b128 v6, v[60:63] offset:15360\n" \
  "v_mul_f32 v31, 0x3f8e353f, v31\n" \
  "v_mul_f32 v30, 0x44870000, v31\n" \
  "v_mov_b64 v[12:13], 0\n" \
  "v_mov_b64 v[14:15], 0\n" \
  "v_mov_b64 v[16:17], 0\n" \
  "v_mov_b64 v[18:19], 0\n" \
  "v_mov_b64 v[20:21], 0\n" \
  "v_mov_b64 v[22:23], 0\n" \
  "v_readfirstlane_b32 s72, v31\n" \
  "v_readfirstlane_b32 s86, v30\n" \
  "s_lshr_b32 s86, s86, 23\n" \
  "s_addk_i32 s86, 0xff83\n" \
  "s_ashr_i32 s86, s86, 1\n" \
  "s_max_i32 s86, s86, -20\n" \
  "s_min_i32 s86, s86, 100\n" \
  "s_mov_b32 s82, s86\n" \
  "s_lshr_b32 s83, s72, 23\n" \
  "s_sub_i32 s83, s83, 243\n" \
  "s_waitcnt lgkmcnt(0)\n" \
  "s_add_u32 s88, s74, 157760\n" \
  "v_mov_b32 v59, s88\n" \
  "v_mov_b32 v34, 0xc9b26ca0\n" \
  "v_mov_b32 v35, 0xc9b26c86\n" \
  ".p2align 6\n" \
  "s_nop 0\n" \
  "s_nop 0\n" \
  "s_nop 0\n" \
  ".LCA_TOP_%=:\n" \
  "s_waitcnt lgkmcnt(0)\n" \
  "s_barrier\n" \
  "ds_read_b128 v[40:43], v1 offset:0\n" \
  "ds_read_b128 v[44:47], v1 offset:1024\n" \
  "ds_read_b128 v[48:51], v1 offset:2048\n" \
  "ds_read_b128 v[52:55], v1 offset:3072\n" \
  "ds_read_b128 v[56:59], v1 offset:4096\n" \
  "ds_read_b128 v[60:63], v1 offset:5120\n" \
  "s_cmp_lg_u32 s73, 0\n" \
  "s_cbranch_scc1 .LCA_NOZ_%=\n" \
  "s_xor_b32 s88, s60, 0x10000\n" \
  "s_add_u32 s88, s88, s61\n" \
  "buffer_store_dwordx2 v[34:35], v5, s[40:43], s88 offen sc1\n" \
  ".LCA_NOZ_%=:\n" \
  "s_waitcnt lgkmcnt(5)\n" \
  "v_smfmac_f32_16x16x64_bf16 v[20:23], v[40:43], v[192:199], v0\n" \
  "v_smfmac_f32_16x16x64_bf16 v[12:15], v[40:43], v[64:71], v0\n" \
  "v_smfmac_f32_16x16x64_bf16 v[16:19], v[40:43], v[128:135], v0\n" \
  "ds_read_b128 v[40:43], v1 offset:6144\n" \
  "s_waitcnt lgkmcnt(5)\n" \
  "v_smfmac_f32_16x16x64_bf16 v[20:23], v[44:47], v[200:207], v0\n" \
  "v_smfmac_f32_16x16x64_bf16 v[12:15], v[44:47], v[72:79], v0\n" \
  "v_smfmac_f32_16x16x64_bf16 v[16:19], v[44:47], v[136:143], v0\n" \
  "ds_read_b128 v[44:47], v1 offset:7168\n" \
  "s_waitcnt lgkmcnt(5)\n" \
  "v_smfmac_f32_16x16x64_bf16 v[20:23], v[48:51], v[208:215], v0\n" \
  "v_smfmac_f32_16x16x64_bf16 v[12:15], v[48:51], v[80:87], v0\n" \
  "v_smfmac_f32_16x16x64_bf16 v[16:19], v[48:51], v[144:151], v0\n" \
  "ds_read_b128 v[24:27], v1 offset:10240\n" \
  "ds_read_b128 v[28:31], v1 offset:14336\n" \
  "s_waitcnt lgkmcnt(6)\n" \
  "v_smfmac_f32_16x16x64_bf16 v[20:23], v[52:55], v[216:223], v0\n" \
  "v_smfmac_f32_16x16x64_bf16 v[12:15], v[52:55], v[88:95], v0\n" \
  "v_smfmac_f32_16x16x64_bf16 v[16:19], v[52:55], v[152:159], v0\n" \
  "ds_read_b128 v[32:35], v1 offset:11264\n" \
  "ds_read_b128 v[36:39], v1 offset:15360\n" \
  "s_waitcnt lgkmcnt(7)\n" \
  "v_smfmac_f32_16x16x64_bf16 v[20:23], v[56:59], v[224:231], v0\n" \
  "v_smfmac_f32_16x16x64_bf16 v[12:15], v[56:59], v[96:103], v0\n" \
  "v_smfmac_f32_16x16x64_bf16 v[16:19], v[56:59], v[160:167], v0\n" \
  "ds_read_b128 v[56:59], v1 offset:12288\n" \
  "s_waitcnt lgkmcnt(7)\n" \
  "v_smfmac_f32_16x16x64_bf16 v[20:23], v[60:63], v[232:239], v0\n" \
  "v_smfmac_f32_16x16x64_bf16 v[12:15], v[60:63], v[104:111], v0\n" \
  "v_smfmac_f32_16x16x64_bf16 v[16:19], v[60:63], v[168:175], v0\n" \
  "ds_read_b128 v[60:63], v1 offset:16384\n" \
  "s_waitcnt lgkmcnt(7)\n" \
  "v_smfmac_f32_16x16x64_bf16 v[20:23], v[40:43], v[240:247], v0\n" \
  "v_smfmac_f32_16x16x64_bf16 v[12:15], v[40:43], v[112:119], v0\n" \
  "v_smfmac_f32_16x16x64_bf16 v[16:19], v[40:43], v[176:183], v0\n" \
  "s_waitcnt lgkmcnt(6)\n" \
  "v_smfmac_f32_16x16x64_bf16 v[20:23], v[44:47], v[248:255], v0\n" \
  "v_smfmac_f32_16x16x64_bf16 v[12:15], v[44:47], v[120:127], v0\n" \
  "v_smfmac_f32_16x16x64_bf16 v[16:19], v[44:47], v[184:191], v0\n" \
  "s_cmp_eq_u32 s71, 0\n" \
  "v_permlane32_swap_b32 v20, v22\n" \
  "v_permlane32_swap_b32 v21, v23\n" \
  "v_pk_add_f32 v[20:21], v[20:21], v[22:23]\n" \
  "s_mov_b64 vcc, s[76:77]\n" \
  "v_permlane32_swap_b32 v12, v14\n" \
  "v_permlane32_swap_b32 v13, v15\n" \
  "v_cvt_pk_bf16_f32 v20, v20, v21\n" \
  "v_pk_add_f32 v[12:13], v[12:13], v[14:15]\n" \
  "v_permlane32_swap_b32 v16, v18\n" \
  "v_permlane32_swap_b32 v17, v19\n" \
  "ds_write_b32 v8, v20 offset:2048\n" \
  "v_cvt_pk_bf16_f32 v12, v12, v13\n" \
  "v_pk_add_f32 v[16:17], v[16:17], v[18:19]\n" \
  "s_nop 0\n" \
  "v_cvt_pk_bf16_f32 v13, v16, v17\n" \
  "ds_write_b64 v8, v[12:13]\n" \
  "s_setprio 1\n" \
  "s_waitcnt lgkmcnt(0)\n" \
  "s_barrier\n" \
  "s_cbranch_scc1 .LCA_LAST_%=\n" \
  "ds_read_b64_tr_b16 v[40:41], v2 offset:0\n" \
  "ds_read_b64_tr_b16 v[42:43], v3 offset:0\n" \
  "ds_read_b64_tr_b16 v[44:45], v2 offset:1024\n" \
  "ds_read_b64_tr_b16 v[46:47], v3 offset:1024\n" \
  "ds_read_b64_tr_b16 v[48:49], v2 offset:2048\n" \
  "ds_read_b64_tr_b16 v[50:51], v3 offset:2048\n" \
  "ds_read_b64_tr_b16 v[52:53], v2 offset:3072\n" \
  "ds_read_b64_tr_b16 v[54:55], v3 offset:3072\n" \
  "ds_read_b32 v11, v1 offset:26688\n" \
  "s_waitcnt lgkmcnt(7)\n" \
  "v_mfma_f32_16x16x32_bf16 v[12:15], v[24:27], v[40:43], 0\n" \
  "v_mfma_f32_16x16x32_bf16 v[16:19], v[28:31], v[40:43], 0\n" \
  "ds_read_b128 v[24:27], v1 offset:13312\n" \
  "ds_read_b128 v[28:31], v1 offset:17408\n" \
  "v_mfma_f32_16x16x32_bf16 v[20:23], v[40:43], v[40:43], 0\n" \
  "s_waitcnt lgkmcnt(7)\n" \
  "v_mfma_f32_16x16x32_bf16 v[12:15], v[32:35], v[44:47], v[12:15]\n" \
  "v_mfma_f32_16x16x32_bf16 v[16:19], v[36:39], v[44:47], v[16:19]\n" \
  "v_mfma_f32_16x16x32_bf16 v[20:23], v[44:47], v[44:47], v[20:23]\n" \
  "s_waitcnt lgkmcnt(2)\n" \
  "v_mfma_f32_16x16x32_bf16 v[12:15], v[56:59], v[48:51], v[12:15]\n" \
  "v_readfirstlane_b32 s86, v11\n" \
  "v_mfma_f32_16x16x32_bf16 v[16:19], v[60:63], v[48:51], v[16:19]\n" \
  "ds_read_b128 v[32:35], v6 offset:8192\n" \
  "ds_read_b128 v[36:39], v6 offset:9216\n" \
  "v_mfma_f32_16x16x32_bf16 v[20:23], v[48:51], v[48:51], v[20:23]\n" \
  "s_lshr_b32 s86, s86, 23\n" \
  "s_add_i32 s86, s86, s83\n" \
  "s_ashr_i32 s86, s86, 1\n" \
  "ds_read_b128 v[48:51], v10 offset:18432\n" \
  "v_mfma_f32_16x16x32_bf16 v[20:23], v[52:55], v[52:55], v[20:23]\n" \
  "s_max_i32 s86, s86, s82\n" \
  "s_min_i32 s86, s86, 100\n" \
  "s_lshl_b32 s86, s86, 23\n" \
  "s_sub_i32 s84, 0xc8800000, s86\n" \
  "s_waitcnt lgkmcnt(3)\n" \
  "v_mfma_f32_16x16x32_bf16 v[12:15], v[24:27], v[52:55], v[12:15]\n" \
  "v_mfma_f32_16x16x32_bf16 v[16:19], v[28:31], v[52:55], v[16:19]\n" \
  "ds_read_b128 v[52:55], v10 offset:22528\n" \
  "ds_read_b128 v[56:59], v6 offset:0\n" \
  "ds_read_b128 v[60:63], v6 offset:1024\n" \
  "ds_read_b128 v[24:27], v6 offset:2048\n" \
  "ds_read_b128 v[28:31], v6 offset:3072\n" \
  "v_cndmask_b32_dpp v20, v23, v22, vcc quad_perm:[2,3,0,1] row_mask:0xf bank_mask:0xf\n" \
  "v_mov_b32 v41, 0\n" \
  "v_mov_b32 v42, 0\n" \
  "v_cndmask_b32_dpp v12, v16, v12, vcc quad_perm:[2,3,0,1] row_mask:0xf bank_mask:0xf\n" \
  "v_cndmask_b32_dpp v13, v17, v13, vcc quad_perm:[2,3,0,1] row_mask:0xf bank_mask:0xf\n" \
  "v_cndmask_b32_dpp v14, v18, v14, vcc quad_perm:[2,3,0,1] row_mask:0xf bank_mask:0xf\n" \
  "v_cndmask_b32_dpp v15, v19, v15, vcc quad_perm:[2,3,0,1] row_mask:0xf bank_mask:0xf\n" \
  "v_maximum3_f32 v16, v20, v12, v13\n" \
  "v_maximum3_f32 v16, v16, v14, v15\n" \
  "s_nop 1\n" \
  "v_max_f32_dpp v16, v16, v16 quad_perm:[2,3,0,1] row_mask:0xf bank_mask:0xf\n" \
  "v_mov_b32 v17, v16\n" \
  "s_nop 1\n" \
  "v_permlane16_swap_b32 v16, v17\n" \
  "v_max_f32 v16, v16, v17\n" \
  "v_mov_b32 v17, v16\n" \
  "s_nop 1\n" \
  "v_permlane32_swap_b32 v16, v17\n" \
  "v_max_f32 v16, v16, v17\n" \
  "v_mul_f32 v16, s78, v16\n" \
  "v_pk_fma_f32 v[12:13], v[12:13], s[78:79], v[16:17] op_sel_hi:[1,0,0] neg_lo:[0,0,1] neg_hi:[0,0,1]\n" \
  "v_pk_fma_f32 v[14:15], v[14:15], s[78:79], v[16:17] op_sel_hi:[1,0,0] neg_lo:[0,0,1] neg_hi:[0,0,1]\n" \
  "v_fma_f32 v20, v20, s78, -v16\n" \
  "v_exp_f32 v12, v12\n" \
  "v_exp_f32 v13, v13\n" \
  "v_exp_f32 v14, v14\n" \
  "v_exp_f32 v15, v15\n" \
  "v_exp_f32 v20, v20\n" \
  "s_waitcnt lgkmcnt(4)\n" \
  "v_cvt_pk_bf16_f32 v44, v12, v13\n" \
  "v_cvt_pk_bf16_f32 v45, v14, v15\n" \
  "v_pk_add_f32 v[8:9], v[12:13], v[14:15]\n" \
  "v_mov_b32_dpp v46, v44 quad_perm:[2,3,0,1] row_mask:0xf bank_mask:0xf\n" \
  "v_mov_b32_dpp v47, v45 quad_perm:[2,3,0,1] row_mask:0xf bank_mask:0xf\n" \
  "v_add_f32 v10, v8, v20\n" \
  "v_add_f32 v10, v10, v9\n" \
  "v_mfma_f32_16x16x32_bf16 v[12:15], v[48:51], v[44:47], 0\n" \
  "v_mov_b32_dpp v21, v20 quad_perm:[2,3,0,1] row_mask:0xf bank_mask:0xf\n" \
  "v_add_f32_dpp v10, v10, v10 quad_perm:[2,3,0,1] row_mask:0xf bank_mask:0xf\n" \
  "v_mov_b32 v11, v10\n" \
  "v_mfma_f32_16x16x32_bf16 v[16:19], v[52:55], v[44:47], 0\n" \
  "s_setprio 3\n" \
  "s_barrier\n" \
  "ds_read_b64 v[22:23], v7 offset:0\n" \
  "s_nop 0\n" \
  "v_permlane16_swap_b32 v10, v11\n" \
  "v_add_f32 v10, v10, v11\n" \
  "v_mov_b32 v11, v10\n" \
  "v_mov_b64 v[8:9], s[92:93]\n" \
  "s_nop 0\n" \
  "v_permlane32_swap_b32 v10, v11\n" \
  "v_add_f32 v10, v10, v11\n" \
  "v_rcp_f32 v10, v10\n" \
  "s_nop 0\n" \
  "v_mul_f32 v10, s84, v10\n" \
  "v_pk_mul_f32 v[20:21], v[20:21], v[10:11] op_sel_hi:[1,0]\n" \
  "v_pk_mul_f32 v[12:13], v[12:13], v[10:11] op_sel_hi:[1,0]\n" \
  "v_pk_mul_f32 v[14:15], v[14:15], v[10:11] op_sel_hi:[1,0]\n" \
  "v_cvt_pk_bf16_f32 v40, v20, v21\n" \
  "v_pk_mul_f32 v[16:17], v[16:17], v[10:11] op_sel_hi:[1,0]\n" \
  "v_cvt_pk_bf16_f32 v43, v20, v21\n" \
  "v_pk_mul_f32 v[18:19], v[18:19], v[10:11] op_sel_hi:[1,0]\n" \
  "s_waitcnt lgkmcnt(0)\n" \
  "v_mfma_f32_16x16x16_bf16 v[12:15], v[22:23], v[40:41], v[12:15]\n" \
  "v_mfma_f32_16x16x16_bf16 v[16:19], v[22:23], v[42:43], v[16:19]\n" \
  "v_mov_b64 v[10:11], s[92:93]\n" \
  "ds_read_b128 v[48:51], v6 offset:10240\n" \
  "ds_read_b128 v[52:55], v6 offset:11264\n" \
  "v_mov_b64 v[20:21], s[92:93]\n" \
  "v_mov_b64 v[22:23], s[92:93]\n" \
  "s_nop 1\n" \
  "v_cvt_pk_bf16_f32 v40, v12, v13\n" \
  "v_cvt_pk_bf16_f32 v41, v14, v15\n" \
  "v_cvt_pk_bf16_f32 v42, v16, v17\n" \
  "ds_write_b64 v4, v[40:41]\n" \
  "v_cvt_pk_bf16_f32 v43, v18, v19\n" \
  "ds_write_b64 v4, v[42:43] offset:1024\n" \
  "v_mov_b64 v[12:13], s[92:93]\n" \
  "v_mov_b64 v[14:15], s[92:93]\n" \
  "v_mov_b64 v[16:17], s[92:93]\n" \
  "v_mov_b64 v[18:19], s[92:93]\n" \
  "s_setprio 0\n" \
  "s_waitcnt lgkmcnt(0)\n" \
  "s_barrier\n" \
  "ds_read_b128 v[40:43], v1 offset:8192\n" \
  "ds_read_b128 v[44:47], v1 offset:9216\n" \
  "s_waitcnt lgkmcnt(1)\n" \
  "v_smfmac_f32_16x16x64_bf16 v[12:15], v[40:43], v[56:63], v0\n" \
  "ds_read_b128 v[56:59], v6 offset:4096\n" \
  "ds_read_b128 v[60:63], v6 offset:5120\n" \
  "v_smfmac_f32_16x16x64_bf16 v[16:19], v[40:43], v[24:31], v0\n" \
  "ds_read_b128 v[24:27], v6 offset:6144\n" \
  "ds_read_b128 v[28:31], v6 offset:7168\n" \
  "s_waitcnt lgkmcnt(4)\n" \
  "v_smfmac_f32_16x16x64_bf16 v[12:15], v[44:47], v[32:39], v0\n" \
  "ds_read_b128 v[32:35], v6 offset:12288\n" \
  "ds_read_b128 v[36:39], v6 offset:13312\n" \
  "v_smfmac_f32_16x16x64_bf16 v[16:19], v[44:47], v[48:55], v0\n" \
  "ds_read_b128 v[48:51], v6 offset:14336\n" \
  "ds_read_b128 v[52:55], v6 offset:15360\n" \
  "s_nop 5\n" \
  "v_permlane32_swap_b32 v12, v17\n" \
  "v_permlane32_swap_b32 v13, v16\n" \
  "v_permlane32_swap_b32 v14, v19\n" \
  "v_permlane32_swap_b32 v15, v18\n" \
  "v_pk_add_f32 v[12:13], v[12:13], v[16:17] op_sel:[0,1] op_sel_hi:[1,0]\n" \
  "v_pk_add_f32 v[14:15], v[14:15], v[18:19] op_sel:[0,1] op_sel_hi:[1,0]\n" \
  "buffer_atomic_add_x2 v[12:13], v5, s[40:43], s56 offen\n" \
  "buffer_atomic_add_x2 v[14:15], v5, s[40:43], s57 offen\n" \
  "s_waitcnt lgkmcnt(6)\n" \
  "v_smfmac_f32_16x16x64_bf16 v[20:23], v[40:43], v[56:63], v0\n" \
  "s_waitcnt lgkmcnt(4)\n" \
  "v_smfmac_f32_16x16x64_bf16 v[8:11], v[40:43], v[24:31], v0\n" \
  "s_waitcnt lgkmcnt(2)\n" \
  "v_smfmac_f32_16x16x64_bf16 v[20:23], v[44:47], v[32:39], v0\n" \
  "s_waitcnt lgkmcnt(0)\n" \
  "v_smfmac_f32_16x16x64_bf16 v[8:11], v[44:47], v[48:55], v0\n" \
  "s_nop 7\n" \
  "v_permlane32_swap_b32 v20, v9\n" \
  "v_permlane32_swap_b32 v21, v8\n" \
  "v_permlane32_swap_b32 v22, v11\n" \
  "v_permlane32_swap_b32 v23, v10\n" \
  "v_pk_add_f32 v[20:21], v[20:21], v[8:9] op_sel:[0,1] op_sel_hi:[1,0]\n" \
  "v_pk_add_f32 v[22:23], v[22:23], v[10:11] op_sel:[0,1] op_sel_hi:[1,0]\n" \
  "buffer_atomic_add_x2 v[20:21], v5, s[40:43], s58 offen\n" \
  "buffer_atomic_add_x2 v[22:23], v5, s[40:43], s59 offen\n" \
  "s_memrealtime s[38:39]\n" \
  "v_mbcnt_lo_u32_b32 v48, -1, 0\n" \
  "v_mbcnt_hi_u32_b32 v48, -1, v48\n" \
  "s_add_u32 s88, s74, 157760\n" \
  "v_mov_b32 v59, s88\n" \
  "v_lshl_add_u32 v52, v48, 2, v59\n" \
  "ds_read_b32 v52, v52 offset:192\n" \
  "v_lshrrev_b32 v49, 3, v48\n" \
  "v_and_b32 v50, 7, v48\n" \
  "v_lshlrev_b32 v50, 4, v50\n" \
  "v_lshl_or_b32 v44, v49, 8, v50\n" \
  "s_mov_b32 s87, 0x40000\n" \
  "v_lshrrev_b32 v49, 2, v48\n" \
  "v_lshrrev_b32 v50, 4, v48\n" \
  "v_bitop3_b32 v50, v49, v50, 3 bitop3:0x28\n" \
  "v_lshl_add_u32 v8, v50, 3, s97\n" \
  "v_and_b32 v50, 3, v48\n" \
  "v_lshl_add_u32 v8, v50, 5, v8\n" \
  "v_and_b32 v49, 1, v49\n" \
  "v_lshl_add_u32 v8, v49, 8, v8\n" \
  "v_and_b32 v49, 8, v48\n" \
  "v_lshl_add_u32 v8, v49, 4, v8\n" \
  "v_lshrrev_b32 v49, 5, v48\n" \
  "v_add_u32 v10, s99, v1\n" \
  "s_lshr_b32 s88, s99, 2\n" \
  "s_add_u32 s88, s88, s74\n" \
  "s_add_u32 s88, s88, 139264\n" \
  "v_lshl_add_u32 v7, v49, 10, s88\n" \
  "v_and_b32 v49, 31, v48\n" \
  "v_lshl_add_u32 v7, v49, 3, v7\n" \
  "v_mov_b64 v[12:13], 0\n" \
  "v_mov_b64 v[14:15], 0\n" \
  "v_mov_b64 v[16:17], 0\n" \
  "v_mov_b64 v[18:19], 0\n" \
  "v_mov_b64 v[20:21], 0\n" \
  "v_mov_b64 v[22:23], 0\n" \
  "v_mov_b32 v34, 0xc9b26ca0\n" \
  "v_mov_b32 v35, 0xc9b26c86\n" \
  "v_and_b32 v50, 3, v48\n" \
  "v_lshl_add_u32 v40, v50, 2, s98\n" \
  "v_lshrrev_b32 v50, 4, v48\n" \
  "v_mad_u32_u24 v40, v50, s95, v40\n" \
  "v_bfe_u32 v49, v48, 3, 1\n" \
  "v_lshl_add_u32 v40, v49, 6, v40\n" \
  "v_bfe_u32 v49, v48, 2, 1\n" \
  "v_lshl_add_u32 v40, v49, 7, v40\n" \
  "v_xor_b32 v41, 16, v40\n" \
  "v_xor_b32 v42, 32, v40\n" \
  "v_xor_b32 v43, 48, v40\n" \
  "s_add_i32 s80, s86, 0x35000000\n" \
  "v_mov_b32 v11, 0\n" \
  "s_mov_b32 s89, s60\n" \
  "s_bitcmp1_b32 s60, 7\n" \
  "s_cselect_b32 s88, 0x10000, 0\n" \
  "s_bitset1_b32 s88, 7\n" \
  "s_xor_b32 s56, s56, s88\n" \
  "s_xor_b32 s57, s57, s88\n" \
  "s_xor_b32 s58, s58, s88\n" \
  "s_xor_b32 s59, s59, s88\n" \
  "s_xor_b32 s60, s60, s88\n" \
  "s_sub_u32 s71, s71, 1\n" \
  "s_waitcnt lgkmcnt(0)\n" \
  "s_sub_u32 s39, s38, 150\n" \
  "v_subrev_u32 v52, s39, v52\n" \
  "v_cmp_lt_u32 vcc, 0x1000, v52\n" \
  "s_cmp_eq_u64 vcc, 0\n" \
  "s_cbranch_scc1 .LCA_OKM_%=\n" \
  ".LCA_AGN_%=:\n" \
  "buffer_load_dword v52, v5, s[0:3], s66 offen sc1\n" \
  "s_sub_u32 s87, s87, 1\n" \
  "s_cbranch_scc1 .LCA_END_%=\n" \
  "s_waitcnt vmcnt(0)\n" \
  "v_cmp_eq_u32 vcc, 0, v52\n" \
  "v_subrev_u32 v52, s39, v52\n" \
  "s_cmp_eq_u64 vcc, 0\n" \
  "s_cbranch_scc1 .LCA_OKM_%=\n" \
  "s_sleep 4\n" \
  "s_branch .LCA_AGN_%=\n" \
  ".LCA_OKM_%=:\n" \
  "s_nop 1\n" \
  "v_max_i32_dpp v52, v52, v52 row_ror:1 row_mask:0xf bank_mask:0xf\n" \
  "s_nop 1\n" \
  "v_max_i32_dpp v52, v52, v52 row_ror:2 row_mask:0xf bank_mask:0xf\n" \
  "s_nop 1\n" \
  "v_max_i32_dpp v52, v52, v52 row_ror:4 row_mask:0xf bank_mask:0xf\n" \
  "s_nop 1\n" \
  "v_max_i32_dpp v52, v52, v52 row_ror:8 row_mask:0xf bank_mask:0xf\n" \
  "s_movk_i32 vcc_lo, 150\n" \
  "v_readlane_b32 s88, v52, 0\n" \
  "s_max_i32 vcc_lo, vcc_lo, s88\n" \
  "v_readlane_b32 s88, v52, 16\n" \
  "s_max_i32 vcc_lo, vcc_lo, s88\n" \
  "v_readlane_b32 s88, v52, 32\n" \
  "s_max_i32 vcc_lo, vcc_lo, s88\n" \
  "v_readlane_b32 s88, v52, 48\n" \
  "s_max_i32 vcc_lo, vcc_lo, s88\n" \
  "s_add_u32 s66, s66, 1024\n" \
  "s_add_u32 vcc_lo, vcc_lo, s39\n" \
  "s_add_u32 vcc_lo, vcc_lo, 23\n" \
  ".LCA_TW_%=:\n" \
  "s_memrealtime s[38:39]\n" \
  "s_waitcnt lgkmcnt(0)\n" \
  "s_sub_u32 s38, s38, vcc_lo\n" \
  "s_cmp_lt_i32 s38, 0\n" \
  "s_cbranch_scc1 .LCA_TW_%=\n" \
  "buffer_load_dwordx4 v[24:27], v44, s[40:43], s89 offen sc1\n" \
  "buffer_load_dwordx4 v[28:31], v44, s[40:43], s89 offen offset:2048 sc1\n" \
  ".LCA_SEE_%=:\n" \
  "s_waitcnt vmcnt(1)\n" \
  "v_cvt_f32_i32 v24, v24\n" \
  "v_cvt_f32_i32 v25, v25\n" \
  "v_cvt_f32_i32 v26, v26\n" \
  "v_cvt_f32_i32 v27, v27\n" \
  "v_max_f32 v45, |v24|, |v26|\n" \
  "v_pk_mul_f32 v[24:25], v[24:25], s[80:81] op_sel_hi:[1,0]\n" \
  "v_pk_mul_f32 v[26:27], v[26:27], s[80:81] op_sel_hi:[1,0]\n" \
  "s_nop 0\n" \
  "v_cvt_pk_bf16_f32 v32, v24, v26\n" \
  "v_cvt_pk_bf16_f32 v33, v25, v27\n" \
  "ds_write_b32 v40, v32\n" \
  "ds_write_b32 v41, v33\n" \
  "s_waitcnt vmcnt(0)\n" \
  "v_cvt_f32_i32 v28, v28\n" \
  "v_cvt_f32_i32 v30, v30\n" \
  "v_cvt_f32_i32 v29, v29\n" \
  "v_max3_f32 v45, v45, |v28|, |v30|\n" \
  "v_cvt_f32_i32 v31, v31\n" \
  "v_cmp_lt_f32 vcc, s91, v45\n" \
  "v_pk_mul_f32 v[28:29], v[28:29], s[80:81] op_sel_hi:[1,0]\n" \
  "v_pk_mul_f32 v[30:31], v[30:31], s[80:81] op_sel_hi:[1,0]\n" \
  "s_cmp_eq_u64 vcc, 0\n" \
  "s_cbranch_scc0 .LCA_POLL_%=\n" \
  "v_cvt_pk_bf16_f32 v36, v28, v30\n" \
  "v_cvt_pk_bf16_f32 v37, v29, v31\n" \
  "ds_write_b32 v42, v36\n" \
  "ds_write_b32 v43, v37\n" \
  "v_dot2c_f32_bf16 v11, v32, v32\n" \
  "v_dot2c_f32_bf16 v11, v33, v33\n" \
  "v_dot2c_f32_bf16 v11, v36, v36\n" \
  "v_dot2c_f32_bf16 v11, v37, v37\n" \
  "s_nop 1\n" \
  "s_nop 0\n" \
  "v_cmp_lt_f32 vcc, 4.0, v11\n" \
  "s_cbranch_vccz .LCA_TOP_%=\n" \
  "s_mov_b64 exec, vcc\n" \
  "ds_max_u32 v59, v11\n" \
  "s_mov_b64 exec, -1\n" \
  "s_branch .LCA_TOP_%=\n" \
  ".LCA_POLL_%=:\n" \
  "buffer_load_dwordx4 v[24:27], v44, s[40:43], s89 offen sc1\n" \
  "buffer_load_dwordx4 v[28:31], v44, s[40:43], s89 offen offset:2048 sc1\n" \
  "s_sub_u32 s87, s87, 1\n" \
  "s_cbranch_scc1 .LCA_END_%=\n" \
  "s_branch .LCA_SEE_%=\n" \
  ".LCA_LAST_%=:\n" \
  ".LCA_END_%=:\n" \
  "s_endpgm\n"
#define CHAIN_ASM_VO \
  "s_mov_b32 s95, 272\n" \
  "s_mov_b32 s91, 0x4c7c2000\n" \
  "s_mov_b32 s85, 0xc9b26c9b\n" \
  "s_mov_b32 s92, 0xc9326c9b\n" \
  "s_mov_b32 s93, 0xc9326c9b\n" \
  "s_mov_b32 s78, 0x3e0293ee\n" \
  "s_mov_b32 s76, 0x33333333\n" \
  "s_mov_b32 s77, 0x33333333\n" \
  "s_lshl_b32 s98, s73, 10\n" \
  "s_add_u32 s98, s98, s74\n" \
  "s_add_u32 s98, s98, 131072\n" \
  "s_lshl_b32 s97, s73, 9\n" \
  "s_add_u32 s97, s97, s74\n" \
  "s_add_u32 s97, s97, 158724\n" \
  "s_lshl_b32 s99, s73, 11\n" \
  "s_add_u32 s99, s99, s74\n" \
  "s_add_u32 s99, s99, 133120\n" \
  "s_lshl_b32 s94, s73, 4\n" \
  "s_add_u32 s94, s94, s74\n" \
  "s_add_u32 s94, s94, 157760\n" \
  "v_mbcnt_lo_u32_b32 v48, -1, 0\n" \
  "v_mbcnt_hi_u32_b32 v48, -1, v48\n" \
  "s_add_u32 s88, s74, 157760\n" \
  "v_mov_b32 v59, s88\n" \
  "s_sub_u32 s88, s73, 4\n" \
  "v_lshrrev_b32 v49, 2, v48\n" \
  "v_lshrrev_b32 v50, 4, v48\n" \
  "v_bitop3_b32 v50, v49, v50, 3 bitop3:0x28\n" \
  "v_lshl_add_u32 v4, v50, 3, s97\n" \
  "v_and_b32 v50, 3, v48\n" \
  "v_lshl_add_u32 v4, v50, 5, v4\n" \
  "v_and_b32 v49, 1, v49\n" \
  "v_lshl_add_u32 v4, v49, 8, v4\n" \
  "v_and_b32 v49, 8, v48\n" \
  "v_lshl_add_u32 v4, v49, 4, v4\n" \
  "v_and_b32 v51, 15, v48\n" \
  "v_bfe_u32 v49, v48, 4, 1\n" \
  "v_lshrrev_b32 v50, 5, v48\n" \
  "s_lshl_b32 s89, s88, 10\n" \
  "s_add_u32 s89, s89, s74\n" \
  "s_add_u32 s89, s89, 158720\n" \
  "v_lshrrev_b32 v9, 3, v51\n" \
  "v_mul_u32_u24 v6, 41, v9\n" \
  "v_xor_b32 v6, v6, v51\n" \
  "v_lshl_add_u32 v6, v6, 3, s89\n" \
  "v_lshl_add_u32 v6, v49, 6, v6\n" \
  "v_lshl_add_u32 v6, v50, 9, v6\n" \
  "v_xor_b32 v9, 0x90, v6\n" \
  "v_lshlrev_b32 v8, 4, v49\n" \
  "v_lshl_add_u32 v8, v51, 5, v8\n" \
  "v_lshl_add_u32 v8, v50, 3, v8\n" \
  "s_lshl_b32 s89, s88, 8\n" \
  "s_add_u32 s89, s89, s74\n" \
  "s_add_u32 s89, s89, 139264\n" \
  "v_and_b32 v49, 31, v48\n" \
  "v_lshl_add_u32 v5, v49, 3, s89\n" \
  "v_lshl_add_u32 v5, v50, 10, v5\n" \
  "v_lshrrev_b32 v49, 2, v51\n" \
  "v_and_b32 v50, 5, v51\n" \
  "v_and_or_b32 v49, v49, 2, v50\n" \
  "v_and_b32 v50, 48, v48\n" \
  "v_lshlrev_b32 v7, 3, v50\n" \
  "v_lshl_add_u32 v7, v49, 4, v7\n" \
  "v_bitop3_b32 v49, v51, 2, 2 bitop3:0x0c\n" \
  "v_lshl_add_u32 v7, v49, 29, v7\n" \
  "v_mov_b64 v[12:13], 0\n" \
  "v_mov_b64 v[14:15], 0\n" \
  "v_mov_b64 v[16:17], 0\n" \
  "v_mov_b64 v[18:19], 0\n" \
  "v_mov_b64 v[20:21], 0\n" \
  "v_mov_b64 v[22:23], 0\n" \
  "v_lshlrev_b32 v10, 4, v48\n" \
  "s_add_u32 s78, s74, 157952\n" \
  "v_lshl_add_u32 v52, v48, 2, v59\n" \
  "ds_write_b32 v52, v12 offset:192\n" \
  "s_movk_i32 s79, 105\n" \
  "s_movk_i32 s96, 25\n" \
  "s_mov_b32 s87, 0x40000\n" \
  "v_subrev_u32 v30, 64, v59\n" \
  "v_add_u32 v17, 0x10000, v28\n" \
  "v_xor_b32 v18, 128, v28\n" \
  "v_xor_b32 v19, 128, v17\n" \
  "ds_write_b32 v30, v12 offset:0\n" \
  "ds_write_b32 v30, v12 offset:32\n" \
  "ds_write_b32 v30, v12 offset:64\n" \
  "v_xor_b32 v20, 0, v29\n" \
  "v_xor_b32 v21, 32, v29\n" \
  "v_xor_b32 v22, 64, v29\n" \
  "v_xor_b32 v23, 96, v29\n" \
  "s_waitcnt vmcnt(12) lgkmcnt(0)\n" \
  "s_barrier\n" \
  "ds_read_b64_tr_b16 v[96:97], v20 offset:0\n" \
  "ds_read_b64_tr_b16 v[98:99], v20 offset:2048\n" \
  "ds_read_b64_tr_b16 v[104:105], v21 offset:0\n" \
  "ds_read_b64_tr_b16 v[106:107], v21 offset:2048\n" \
  "ds_read_b64_tr_b16 v[112:113], v22 offset:0\n" \
  "ds_read_b64_tr_b16 v[114:115], v22 offset:2048\n" \
  "ds_read_b64_tr_b16 v[120:121], v23 offset:0\n" \
  "ds_read_b64_tr_b16 v[122:123], v23 offset:2048\n" \
  "s_lshl_b32 s88, s73, 10\n" \
  "v_add_u32 v48, s88, v1\n" \
  "ds_read_b128 v[52:55], v48 offset:0\n" \
  "s_waitcnt lgkmcnt(0)\n" \
  "v_mov_b32 v11, 0\n" \
  "v_dot2c_f32_bf16 v11, v52, v52\n" \
  "v_dot2c_f32_bf16 v11, v53, v53\n" \
  "v_dot2c_f32_bf16 v11, v54, v54\n" \
  "v_dot2c_f32_bf16 v11, v55, v55\n" \
  "s_nop 0\n" \
  "s_nop 1\n" \
  "v_max_u32_dpp v11, v11, v11 row_ror:1 row_mask:0xf bank_mask:0xf\n" \
  "s_nop 1\n" \
  "v_max_u32_dpp v11, v11, v11 row_ror:2 row_mask:0xf bank_mask:0xf\n" \
  "s_nop 1\n" \
  "v_max_u32_dpp v11, v11, v11 row_ror:4 row_mask:0xf bank_mask:0xf\n" \
  "s_nop 1\n" \
  "v_max_u32_dpp v11, v11, v11 row_ror:8 row_mask:0xf bank_mask:0xf\n" \
  "v_mov_b32 v31, v11\n" \
  "s_nop 1\n" \
  "v_permlane16_swap_b32 v11, v31\n" \
  "v_max_u32 v11, v11, v31\n" \
  "v_mov_b32 v31, v11\n" \
  "s_nop 1\n" \
  "v_permlane32_swap_b32 v11, v31\n" \
  "v_max_u32 v11, v11, v31\n" \
  "s_mov_b64 exec, 1\n" \
  "ds_max_u32 v59, v11\n" \
  "s_mov_b64 exec, -1\n" \
  "s_waitcnt vmcnt(8) lgkmcnt(0)\n" \
  "s_barrier\n" \
  "ds_read_b64_tr_b16 v[100:101], v20 offset:4096\n" \
  "ds_read_b64_tr_b16 v[102:103], v20 offset:6144\n" \
  "ds_read_b64_tr_b16 v[108:109], v21 offset:4096\n" \
  "ds_read_b64_tr_b16 v[110:111], v21 offset:6144\n" \
  "ds_read_b64_tr_b16 v[116:117], v22 offset:4096\n" \
  "ds_read_b64_tr_b16 v[118:119], v22 offset:6144\n" \
  "ds_read_b64_tr_b16 v[124:125], v23 offset:4096\n" \
  "ds_read_b64_tr_b16 v[126:127], v23 offset:6144\n" \
  "s_add_u32 m0, s19, 0\n" \
  "s_nop 0\n" \
  "global_load_lds_dwordx4 v24, s[4:5] offset:0\n" \
  "global_load_lds_dwordx4 v25, s[4:5] offset:1024\n" \
  "global_load_lds_dwordx4 v26, s[4:5] offset:2048\n" \
  "global_load_lds_dwordx4 v27, s[4:5] offset:3072\n" \
  "s_add_u32 s4, s4, 0x100000\n" \
  "s_addc_u32 s5, s5, 0\n" \
  "v_mfma_f32_16x16x32_bf16 v[64:67], v[96:99], v[96:99], 0\n" \
  "v_mfma_f32_16x16x32_bf16 v[68:71], v[104:107], v[104:107], 0\n" \
  "v_mfma_f32_16x16x32_bf16 v[72:75], v[112:115], v[112:115], 0\n" \
  "v_mfma_f32_16x16x32_bf16 v[76:79], v[120:123], v[120:123], 0\n" \
  "s_waitcnt vmcnt(8) lgkmcnt(0)\n" \
  "s_barrier\n" \
  "ds_read_b64_tr_b16 v[32:33], v20 offset:8192\n" \
  "ds_read_b64_tr_b16 v[34:35], v20 offset:10240\n" \
  "ds_read_b64_tr_b16 v[40:41], v21 offset:8192\n" \
  "ds_read_b64_tr_b16 v[42:43], v21 offset:10240\n" \
  "ds_read_b64_tr_b16 v[48:49], v22 offset:8192\n" \
  "ds_read_b64_tr_b16 v[50:51], v22 offset:10240\n" \
  "ds_read_b64_tr_b16 v[56:57], v23 offset:8192\n" \
  "ds_read_b64_tr_b16 v[58:59], v23 offset:10240\n" \
  "s_add_u32 m0, s19, 4096\n" \
  "s_nop 0\n" \
  "global_load_lds_dwordx4 v24, s[4:5] offset:0\n" \
  "global_load_lds_dwordx4 v25, s[4:5] offset:1024\n" \
  "global_load_lds_dwordx4 v26, s[4:5] offset:2048\n" \
  "global_load_lds_dwordx4 v27, s[4:5] offset:3072\n" \
  "s_add_u32 s4, s4, 0x100000\n" \
  "s_addc_u32 s5, s5, 0\n" \
  "v_mfma_f32_16x16x32_bf16 v[64:67], v[100:103], v[100:103], v[64:67]\n" \
  "v_mfma_f32_16x16x32_bf16 v[68:71], v[108:111], v[108:111], v[68:71]\n" \
  "v_mfma_f32_16x16x32_bf16 v[72:75], v[116:119], v[116:119], v[72:75]\n" \
  "v_mfma_f32_16x16x32_bf16 v[76:79], v[124:127], v[124:127], v[76:79]\n" \
  "s_waitcnt vmcnt(8) lgkmcnt(0)\n" \
  "s_barrier\n" \
  "ds_read_b64_tr_b16 v[36:37], v20 offset:12288\n" \
  "ds_read_b64_tr_b16 v[38:39], v20 offset:14336\n" \
  "ds_read_b64_tr_b16 v[44:45], v21 offset:12288\n" \
  "ds_read_b64_tr_b16 v[46:47], v21 offset:14336\n" \
  "ds_read_b64_tr_b16 v[52:53], v22 offset:12288\n" \
  "ds_read_b64_tr_b16 v[54:55], v22 offset:14336\n" \
  "ds_read_b64_tr_b16 v[60:61], v23 offset:12288\n" \
  "ds_read_b64_tr_b16 v[62:63], v23 offset:14336\n" \
  "s_add_u32 m0, s19, 8192\n" \
  "s_nop 0\n" \
  "global_load_lds_dwordx4 v24, s[4:5] offset:0\n" \
  "global_load_lds_dwordx4 v25, s[4:5] offset:1024\n" \
  "global_load_lds_dwordx4 v26, s[4:5] offset:2048\n" \
  "global_load_lds_dwordx4 v27, s[4:5] offset:3072\n" \
  "s_add_u32 s4, s4, 0x100000\n" \
  "s_addc_u32 s5, s5, 0\n" \
  "v_mfma_f32_16x16x32_bf16 v[64:67], v[32:35], v[32:35], v[64:67]\n" \
  "v_mfma_f32_16x16x32_bf16 v[68:71], v[40:43], v[40:43], v[68:71]\n" \
  "v_mfma_f32_16x16x32_bf16 v[72:75], v[48:51], v[48:51], v[72:75]\n" \
  "v_mfma_f32_16x16x32_bf16 v[76:79], v[56:59], v[56:59], v[76:79]\n" \
  "s_waitcnt vmcnt(8) lgkmcnt(0)\n" \
  "s_barrier\n" \
  "ds_read_b64_tr_b16 v[128:129], v18 offset:0\n" \
  "ds_read_b64_tr_b16 v[130:131], v18 offset:512\n" \
  "ds_read_b64_tr_b16 v[132:133], v18 offset:32768\n" \
  "ds_read_b64_tr_b16 v[134:135], v18 offset:33280\n" \
  "ds_read_b64_tr_b16 v[136:137], v19 offset:0\n" \
  "ds_read_b64_tr_b16 v[138:139], v19 offset:512\n" \
  "ds_read_b64_tr_b16 v[140:141], v19 offset:32768\n" \
  "ds_read_b64_tr_b16 v[142:143], v19 offset:33280\n" \
  "ds_read_b64_tr_b16 v[192:193], v28 offset:0\n" \
  "ds_read_b64_tr_b16 v[194:195], v28 offset:512\n" \
  "ds_read_b64_tr_b16 v[196:197], v28 offset:32768\n" \
  "ds_read_b64_tr_b16 v[198:199], v28 offset:33280\n" \
  "ds_read_b64_tr_b16 v[200:201], v17 offset:0\n" \
  "ds_read_b64_tr_b16 v[202:203], v17 offset:512\n" \
  "ds_read_b64_tr_b16 v[204:205], v17 offset:32768\n" \
  "ds_read_b64_tr_b16 v[206:207], v17 offset:33280\n" \
  "s_add_u32 m0, s19, 12288\n" \
  "s_nop 0\n" \
  "global_load_lds_dwordx4 v24, s[4:5] offset:0\n" \
  "global_load_lds_dwordx4 v25, s[4:5] offset:1024\n" \
  "global_load_lds_dwordx4 v26, s[4:5] offset:2048\n" \
  "global_load_lds_dwordx4 v27, s[4:5] offset:3072\n" \
  "s_add_u32 s4, s4, 0x100000\n" \
  "s_addc_u32 s5, s5, 0\n" \
  "v_mfma_f32_16x16x32_bf16 v[64:67], v[36:39], v[36:39], v[64:67]\n" \
  "v_mfma_f32_16x16x32_bf16 v[68:71], v[44:47], v[44:47], v[68:71]\n" \
  "v_mfma_f32_16x16x32_bf16 v[72:75], v[52:55], v[52:55], v[72:75]\n" \
  "v_mfma_f32_16x16x32_bf16 v[76:79], v[60:63], v[60:63], v[76:79]\n" \
  "s_nop 7\n" \
  "v_max3_f32 v13, v64, v65, v66\n" \
  "v_max3_f32 v13, v13, v67, v68\n" \
  "v_max3_f32 v13, v13, v69, v70\n" \
  "v_max3_f32 v13, v13, v71, v72\n" \
  "v_max3_f32 v13, v13, v73, v74\n" \
  "v_max3_f32 v13, v13, v75, v76\n" \
  "v_max3_f32 v13, v13, v77, v78\n" \
  "v_max_f32 v13, v13, v79\n" \
  "s_nop 1\n" \
  "v_max_f32_dpp v13, v13, v13 row_ror:1 row_mask:0xf bank_mask:0xf\n" \
  "s_nop 1\n" \
  "v_max_f32_dpp v13, v13, v13 row_ror:2 row_mask:0xf bank_mask:0xf\n" \
  "s_nop 1\n" \
  "v_max_f32_dpp v13, v13, v13 row_ror:4 row_mask:0xf bank_mask:0xf\n" \
  "s_nop 1\n" \
  "v_max_f32_dpp v13, v13, v13 row_ror:8 row_mask:0xf bank_mask:0xf\n" \
  "v_mov_b32 v31, v13\n" \
  "s_nop 1\n" \
  "v_permlane16_swap_b32 v13, v31\n" \
  "v_max_f32 v13, v13, v31\n" \
  "v_mov_b32 v31, v13\n" \
  "s_nop 1\n" \
  "v_permlane32_swap_b32 v13, v31\n" \
  "v_max_f32 v13, v13, v31\n" \
  "s_mov_b64 exec, 1\n" \
  "ds_max_u32 v30, v13 offset:32\n" \
  "s_mov_b64 exec, -1\n" \
  "s_waitcnt vmcnt(8) lgkmcnt(0)\n" \
  "s_barrier\n" \
  "ds_read_b64_tr_b16 v[144:145], v18 offset:4096\n" \
  "ds_read_b64_tr_b16 v[146:147], v18 offset:4608\n" \
  "ds_read_b64_tr_b16 v[148:149], v18 offset:36864\n" \
  "ds_read_b64_tr_b16 v[150:151], v18 offset:37376\n" \
  "ds_read_b64_tr_b16 v[152:153], v19 offset:4096\n" \
  "ds_read_b64_tr_b16 v[154:155], v19 offset:4608\n" \
  "ds_read_b64_tr_b16 v[156:157], v19 offset:36864\n" \
  "ds_read_b64_tr_b16 v[158:159], v19 offset:37376\n" \
  "ds_read_b64_tr_b16 v[208:209], v28 offset:4096\n" \
  "ds_read_b64_tr_b16 v[210:211], v28 offset:4608\n" \
  "ds_read_b64_tr_b16 v[212:213], v28 offset:36864\n" \
  "ds_read_b64_tr_b16 v[214:215], v28 offset:37376\n" \
  "ds_read_b64_tr_b16 v[216:217], v17 offset:4096\n" \
  "ds_read_b64_tr_b16 v[218:219], v17 offset:4608\n" \
  "ds_read_b64_tr_b16 v[220:221], v17 offset:36864\n" \
  "ds_read_b64_tr_b16 v[222:223], v17 offset:37376\n" \
  "s_add_u32 m0, s19, 0\n" \
  "s_nop 0\n" \
  "global_load_lds_dwordx4 v24, s[6:7] offset:0\n" \
  "global_load_lds_dwordx4 v25, s[6:7] offset:1024\n" \
  "global_load_lds_dwordx4 v26, s[6:7] offset:2048\n" \
  "global_load_lds_dwordx4 v27, s[6:7] offset:3072\n" \
  "s_add_u32 s6, s6, 0x100000\n" \
  "s_addc_u32 s7, s7, 0\n" \
  "v_mfma_f32_16x16x32_bf16 v[64:67], v[128:131], v[128:131], 0\n" \
  "v_mfma_f32_16x16x32_bf16 v[68:71], v[192:195], v[192:195], 0\n" \
  "v_mfma_f32_16x16x32_bf16 v[64:67], v[132:135], v[132:135], v[64:67]\n" \
  "v_mfma_f32_16x16x32_bf16 v[68:71], v[196:199], v[196:199], v[68:71]\n" \
  "v_mfma_f32_16x16x32_bf16 v[64:67], v[136:139], v[136:139], v[64:67]\n" \
  "v_mfma_f32_16x16x32_bf16 v[68:71], v[200:203], v[200:203], v[68:71]\n" \
  "v_mfma_f32_16x16x32_bf16 v[64:67], v[140:143], v[140:143], v[64:67]\n" \
  "v_mfma_f32_16x16x32_bf16 v[68:71], v[204:207], v[204:207], v[68:71]\n" \
  "s_waitcnt vmcnt(8) lgkmcnt(0)\n" \
  "s_barrier\n" \
  "ds_read_b64_tr_b16 v[160:161], v18 offset:8192\n" \
  "ds_read_b64_tr_b16 v[162:163], v18 offset:8704\n" \
  "ds_read_b64_tr_b16 v[164:165], v18 offset:40960\n" \
  "ds_read_b64_tr_b16 v[166:167], v18 offset:41472\n" \
  "ds_read_b64_tr_b16 v[168:169], v19 offset:8192\n" \
  "ds_read_b64_tr_b16 v[170:171], v19 offset:8704\n" \
  "ds_read_b64_tr_b16 v[172:173], v19 offset:40960\n" \
  "ds_read_b64_tr_b16 v[174:175], v19 offset:41472\n" \
  "ds_read_b64_tr_b16 v[224:225], v28 offset:8192\n" \
  "ds_read_b64_tr_b16 v[226:227], v28 offset:8704\n" \
  "ds_read_b64_tr_b16 v[228:229], v28 offset:40960\n" \
  "ds_read_b64_tr_b16 v[230:231], v28 offset:41472\n" \
  "ds_read_b64_tr_b16 v[232:233], v17 offset:8192\n" \
  "ds_read_b64_tr_b16 v[234:235], v17 offset:8704\n" \
  "ds_read_b64_tr_b16 v[236:237], v17 offset:40960\n" \
  "ds_read_b64_tr_b16 v[238:239], v17 offset:41472\n" \
  "s_add_u32 m0, s19, 4096\n" \
  "s_nop 0\n" \
  "global_load_lds_dwordx4 v24, s[6:7] offset:0\n" \
  "global_load_lds_dwordx4 v25, s[6:7] offset:1024\n" \
  "global_load_lds_dwordx4 v26, s[6:7] offset:2048\n" \
  "global_load_lds_dwordx4 v27, s[6:7] offset:3072\n" \
  "s_add_u32 s6, s6, 0x100000\n" \
  "s_addc_u32 s7, s7, 0\n" \
  "v_mfma_f32_16x16x32_bf16 v[64:67], v[144:147], v[144:147], v[64:67]\n" \
  "v_mfma_f32_16x16x32_bf16 v[68:71], v[208:211], v[208:211], v[68:71]\n" \
  "v_mfma_f32_16x16x32_bf16 v[64:67], v[148:151], v[148:151], v[64:67]\n" \
  "v_mfma_f32_16x16x32_bf16 v[68:71], v[212:215], v[212:215], v[68:71]\n" \
  "v_mfma_f32_16x16x32_bf16 v[64:67], v[152:155], v[152:155], v[64:67]\n" \
  "v_mfma_f32_16x16x32_bf16 v[68:71], v[216:219], v[216:219], v[68:71]\n" \
  "v_mfma_f32_16x16x32_bf16 v[64:67], v[156:159], v[156:159], v[64:67]\n" \
  "v_mfma_f32_16x16x32_bf16 v[68:71], v[220:223], v[220:223], v[68:71]\n" \
  "s_waitcnt vmcnt(8) lgkmcnt(0)\n" \
  "s_barrier\n" \
  "ds_read_b64_tr_b16 v[176:177], v18 offset:12288\n" \
  "ds_read_b64_tr_b16 v[178:179], v18 offset:12800\n" \
  "ds_read_b64_tr_b16 v[180:181], v18 offset:45056\n" \
  "ds_read_b64_tr_b16 v[182:183], v18 offset:45568\n" \
  "ds_read_b64_tr_b16 v[184:185], v19 offset:12288\n" \
  "ds_read_b64_tr_b16 v[186:187], v19 offset:12800\n" \
  "ds_read_b64_tr_b16 v[188:189], v19 offset:45056\n" \
  "ds_read_b64_tr_b16 v[190:191], v19 offset:45568\n" \
  "ds_read_b64_tr_b16 v[240:241], v28 offset:12288\n" \
  "ds_read_b64_tr_b16 v[242:243], v28 offset:12800\n" \
  "ds_read_b64_tr_b16 v[244:245], v28 offset:45056\n" \
  "ds_read_b64_tr_b16 v[246:247], v28 offset:45568\n" \
  "ds_read_b64_tr_b16 v[248:249], v17 offset:12288\n" \
  "ds_read_b64_tr_b16 v[250:251], v17 offset:12800\n" \
  "ds_read_b64_tr_b16 v[252:253], v17 offset:45056\n" \
  "ds_read_b64_tr_b16 v[254:255], v17 offset:45568\n" \
  "s_add_u32 m0, s19, 8192\n" \
  "s_nop 0\n" \
  "global_load_lds_dwordx4 v24, s[6:7] offset:0\n" \
  "global_load_lds_dwordx4 v25, s[6:7] offset:1024\n" \
  "global_load_lds_dwordx4 v26, s[6:7] offset:2048\n" \
  "global_load_lds_dwordx4 v27, s[6:7] offset:3072\n" \
  "s_add_u32 s6, s6, 0x100000\n" \
  "s_addc_u32 s7, s7, 0\n" \
  "v_mfma_f32_16x16x32_bf16 v[64:67], v[160:163], v[160:163], v[64:67]\n" \
  "v_mfma_f32_16x16x32_bf16 v[68:71], v[224:227], v[224:227], v[68:71]\n" \
  "v_mfma_f32_16x16x32_bf16 v[64:67], v[164:167], v[164:167], v[64:67]\n" \
  "v_mfma_f32_16x16x32_bf16 v[68:71], v[228:231], v[228:231], v[68:71]\n" \
  "v_mfma_f32_16x16x32_bf16 v[64:67], v[168:171], v[168:171], v[64:67]\n" \
  "v_mfma_f32_16x16x32_bf16 v[68:71], v[232:235], v[232:235], v[68:71]\n" \
  "v_mfma_f32_16x16x32_bf16 v[64:67], v[172:175], v[172:175], v[64:67]\n" \
  "v_mfma_f32_16x16x32_bf16 v[68:71], v[236:239], v[236:239], v[68:71]\n" \
  "s_waitcnt vmcnt(8) lgkmcnt(0)\n" \
  "s_barrier\n" \
  "s_add_u32 m0, s19, 12288\n" \
  "s_nop 0\n" \
  "global_load_lds_dwordx4 v24, s[6:7] offset:0\n" \
  "global_load_lds_dwordx4 v25, s[6:7] offset:1024\n" \
  "global_load_lds_dwordx4 v26, s[6:7] offset:2048\n" \
  "global_load_lds_dwordx4 v27, s[6:7] offset:3072\n" \
  "s_add_u32 s6, s6, 0x100000\n" \
  "s_addc_u32 s7, s7, 0\n" \
  "v_mfma_f32_16x16x32_bf16 v[64:67], v[176:179], v[176:179], v[64:67]\n" \
  "v_mfma_f32_16x16x32_bf16 v[68:71], v[240:243], v[240:243], v[68:71]\n" \
  "v_mfma_f32_16x16x32_bf16 v[64:67], v[180:183], v[180:183], v[64:67]\n" \
  "v_mfma_f32_16x16x32_bf16 v[68:71], v[244:247], v[244:247], v[68:71]\n" \
  "v_mfma_f32_16x16x32_bf16 v[64:67], v[184:187], v[184:187], v[64:67]\n" \
  "v_mfma_f32_16x16x32_bf16 v[68:71], v[248:251], v[248:251], v[68:71]\n" \
  "v_mfma_f32_16x16x32_bf16 v[64:67], v[188:191], v[188:191], v[64:67]\n" \
  "v_mfma_f32_16x16x32_bf16 v[68:71], v[252:255], v[252:255], v[68:71]\n" \
  "s_nop 7\n" \
  "s_mov_b32 exec_lo, 0x100001\n" \
  "s_mov_b32 exec_hi, 0x10000100\n" \
  "v_add_f32 v12, v64, v68\n" \
  "s_mov_b32 exec_lo, 0x200002\n" \
  "s_mov_b32 exec_hi, 0x20000200\n" \
  "v_add_f32 v12, v65, v69\n" \
  "s_mov_b32 exec_lo, 0x400004\n" \
  "s_mov_b32 exec_hi, 0x40000400\n" \
  "v_add_f32 v12, v66, v70\n" \
  "s_mov_b32 exec_lo, 0x800008\n" \
  "s_mov_b32 exec_hi, 0x80000800\n" \
  "v_add_f32 v12, v67, v71\n" \
  "s_mov_b64 exec, -1\n" \
  "s_nop 1\n" \
  "v_add_f32_dpp v12, v12, v12 row_ror:1 row_mask:0xf bank_mask:0xf\n" \
  "s_nop 1\n" \
  "v_add_f32_dpp v12, v12, v12 row_ror:2 row_mask:0xf bank_mask:0xf\n" \
  "s_nop 1\n" \
  "v_add_f32_dpp v12, v12, v12 row_ror:4 row_mask:0xf bank_mask:0xf\n" \
  "s_nop 1\n" \
  "v_add_f32_dpp v12, v12, v12 row_ror:8 row_mask:0xf bank_mask:0xf\n" \
  "v_mov_b32 v31, v12\n" \
  "s_nop 1\n" \
  "v_permlane16_swap_b32 v12, v31\n" \
  "v_add_f32 v12, v12, v31\n" \
  "v_mov_b32 v31, v12\n" \
  "s_nop 1\n" \
  "v_permlane32_swap_b32 v12, v31\n" \
  "v_add_f32 v12, v12, v31\n" \
  "v_lshl_add_u32 v31, s73, 2, v30\n" \
  "s_mov_b64 exec, 1\n" \
  "ds_write_b32 v31, v12\n" \
  "s_mov_b64 exec, -1\n" \
  "s_waitcnt vmcnt(8) lgkmcnt(0)\n" \
  "s_barrier\n" \
  "s_add_u32 m0, s19, 0\n" \
  "s_nop 0\n" \
  "global_load_lds_dwordx4 v24, s[8:9] offset:0\n" \
  "global_load_lds_dwordx4 v25, s[8:9] offset:1024\n" \
  "global_load_lds_dwordx4 v26, s[8:9] offset:2048\n" \
  "global_load_lds_dwordx4 v27, s[8:9] offset:3072\n" \
  "s_add_u32 s8, s8, 0x100000\n" \
  "s_addc_u32 s9, s9, 0\n" \
  "ds_read_b128 v[20:23], v30 offset:16\n" \
  "ds_read_b32 v31, v30 offset:32\n" \
  "v_mbcnt_lo_u32_b32 v16, -1, 0\n" \
  "v_mbcnt_hi_u32_b32 v16, -1, v16\n" \
  "v_lshlrev_b32 v15, 8, v16\n" \
  "s_waitcnt lgkmcnt(0)\n" \
  "v_add_f32 v20, v20, v21\n" \
  "v_add_f32 v22, v22, v23\n" \
  "v_add_f32 v20, v20, v22\n" \
  "v_mul_f32 v31, v20, v31\n" \
  "v_mul_f32 v31, 0x3f8147ae, v31\n" \
  "v_mov_b32 v12, s10\n" \
  "v_mov_b32 v13, 0xffff\n" \
  "v_bfi_b32 v13, v13, s11, v31\n" \
  "s_mov_b32 exec_hi, 0\n" \
  "s_mov_b32 exec_lo, s18\n" \
  "global_store_dwordx2 v15, v[12:13], s[16:17] sc1\n" \
  "s_mov_b64 exec, -1\n" \
  "v_lshlrev_b32 v16, 3, v16\n" \
  "s_waitcnt vmcnt(8) lgkmcnt(0)\n" \
  "s_barrier\n" \
  "s_add_u32 m0, s19, 4096\n" \
  "s_nop 0\n" \
  "global_load_lds_dwordx4 v24, s[8:9] offset:0\n" \
  "global_load_lds_dwordx4 v25, s[8:9] offset:1024\n" \
  "global_load_lds_dwordx4 v26, s[8:9] offset:2048\n" \
  "global_load_lds_dwordx4 v27, s[8:9] offset:3072\n" \
  "s_add_u32 s8, s8, 0x100000\n" \
  "s_addc_u32 s9, s9, 0\n" \
  "global_load_dwordx2 v[20:21], v16, s[12:13] sc1\n" \
  "s_waitcnt vmcnt(9) lgkmcnt(0)\n" \
  "s_barrier\n" \
  "s_add_u32 m0, s19, 8192\n" \
  "s_nop 0\n" \
  "global_load_lds_dwordx4 v24, s[8:9] offset:0\n" \
  "global_load_lds_dwordx4 v25, s[8:9] offset:1024\n" \
  "global_load_lds_dwordx4 v26, s[8:9] offset:2048\n" \
  "global_load_lds_dwordx4 v27, s[8:9] offset:3072\n" \
  "s_add_u32 s8, s8, 0x100000\n" \
  "s_addc_u32 s9, s9, 0\n" \
  "s_waitcnt vmcnt(9) lgkmcnt(0)\n" \
  "s_barrier\n" \
  "ds_read_b64_tr_b16 v[64:65], v28 offset:0\n" \
  "ds_read_b64_tr_b16 v[66:67], v28 offset:512\n" \
  "ds_read_b64_tr_b16 v[68:69], v28 offset:32768\n" \
  "ds_read_b64_tr_b16 v[70:71], v28 offset:33280\n" \
  "ds_read_b64_tr_b16 v[72:73], v17 offset:0\n" \
  "ds_read_b64_tr_b16 v[74:75], v17 offset:512\n" \
  "ds_read_b64_tr_b16 v[76:77], v17 offset:32768\n" \
  "ds_read_b64_tr_b16 v[78:79], v17 offset:33280\n" \
  "s_add_u32 m0, s19, 12288\n" \
  "s_nop 0\n" \
  "global_load_lds_dwordx4 v24, s[8:9] offset:0\n" \
  "global_load_lds_dwordx4 v25, s[8:9] offset:1024\n" \
  "global_load_lds_dwordx4 v26, s[8:9] offset:2048\n" \
  "global_load_lds_dwordx4 v27, s[8:9] offset:3072\n" \
  "s_add_u32 s8, s8, 0x100000\n" \
  "s_addc_u32 s9, s9, 0\n" \
  "s_waitcnt vmcnt(8)\n" \
  ".LCV_RD0_%=:\n" \
  "v_cmp_ne_u32 vcc, s10, v20\n" \
  "v_xor_b32 v31, s11, v21\n" \
  "v_cmp_ne_u32 s[22:23], 0, v31\n" \
  "s_or_b64 vcc, vcc, s[22:23]\n" \
  "s_cbranch_scc0 .LCV_RD1_%=\n" \
  "s_sleep 8\n" \
  "global_load_dwordx2 v[20:21], v16, s[12:13] sc1\n" \
  "s_sub_u32 s87, s87, 1\n" \
  "s_cbranch_scc1 .LCV_END_%=\n" \
  "s_waitcnt vmcnt(0)\n" \
  "s_branch .LCV_RD0_%=\n" \
  ".LCV_RD1_%=:\n" \
  "s_waitcnt vmcnt(8) lgkmcnt(0)\n" \
  "s_barrier\n" \
  "ds_write_b128 v3, v[96:99] offset:0\n" \
  "ds_write_b128 v3, v[100:103] offset:1024\n" \
  "ds_write_b128 v3, v[104:107] offset:2048\n" \
  "ds_write_b128 v3, v[108:111] offset:3072\n" \
  "ds_read_b64_tr_b16 v[80:81], v28 offset:4096\n" \
  "ds_read_b64_tr_b16 v[82:83], v28 offset:4608\n" \
  "ds_read_b64_tr_b16 v[84:85], v28 offset:36864\n" \
  "ds_read_b64_tr_b16 v[86:87], v28 offset:37376\n" \
  "ds_read_b64_tr_b16 v[88:89], v17 offset:4096\n" \
  "ds_read_b64_tr_b16 v[90:91], v17 offset:4608\n" \
  "ds_read_b64_tr_b16 v[92:93], v17 offset:36864\n" \
  "ds_read_b64_tr_b16 v[94:95], v17 offset:37376\n" \
  "v_and_b32 v16, 0xff, v16\n" \
  "global_load_dwordx2 v[22:23], v16, s[14:15] sc1\n" \
  "s_mov_b64 exec, 15\n" \
  "buffer_load_dword v13, v2, s[44:47], s68 offen sc1\n" \
  "s_mov_b64 exec, -1\n" \
  "s_waitcnt vmcnt(6) lgkmcnt(0)\n" \
  "s_barrier\n" \
  "ds_write_b128 v3, v[112:115] offset:4096\n" \
  "ds_write_b128 v3, v[116:119] offset:5120\n" \
  "ds_write_b128 v3, v[120:123] offset:6144\n" \
  "ds_write_b128 v3, v[124:127] offset:7168\n" \
  "ds_read_b64_tr_b16 v[96:97], v28 offset:8192\n" \
  "ds_read_b64_tr_b16 v[98:99], v28 offset:8704\n" \
  "ds_read_b64_tr_b16 v[100:101], v28 offset:40960\n" \
  "ds_read_b64_tr_b16 v[102:103], v28 offset:41472\n" \
  "ds_read_b64_tr_b16 v[104:105], v17 offset:8192\n" \
  "ds_read_b64_tr_b16 v[106:107], v17 offset:8704\n" \
  "ds_read_b64_tr_b16 v[108:109], v17 offset:40960\n" \
  "ds_read_b64_tr_b16 v[110:111], v17 offset:41472\n" \
  "s_waitcnt vmcnt(0)\n" \
  "v_cmp_eq_u32 vcc, 0, v13\n" \
  "s_and_b32 s90, vcc_lo, 15\n" \
  "s_cbranch_scc1 .LCV_GL0_%=\n" \
  "s_mov_b32 m0, s99\n" \
  "s_add_u32 s88, s99, 8192\n" \
  "global_load_lds_dwordx4 v10, s[62:63] sc1\n" \
  "global_load_lds_dwordx4 v10, s[62:63] offset:1024 sc1\n" \
  "s_mov_b32 m0, s88\n" \
  "s_nop 0\n" \
  "global_load_lds_dwordx4 v10, s[64:65] sc1\n" \
  "global_load_lds_dwordx4 v10, s[64:65] offset:1024 sc1\n" \
  ".LCV_GL0_%=:\n" \
  "v_cmp_ne_u32 vcc, s10, v22\n" \
  "v_xor_b32 v31, s11, v23\n" \
  "v_and_b32 v31, 0xffff, v31\n" \
  "v_cmp_ne_u32 s[22:23], 0, v31\n" \
  "s_or_b64 vcc, vcc, s[22:23]\n" \
  "s_cbranch_scc0 .LCV_GL1_%=\n" \
  "s_sleep 12\n" \
  "global_load_dwordx2 v[22:23], v16, s[14:15] sc1\n" \
  "s_sub_u32 s87, s87, 1\n" \
  "s_cbranch_scc1 .LCV_END_%=\n" \
  "s_waitcnt vmcnt(0)\n" \
  "s_branch .LCV_GL0_%=\n" \
  ".LCV_GL1_%=:\n" \
  "v_and_b32 v31, 0xffff0000, v23\n" \
  "s_nop 1\n" \
  "v_max_u32_dpp v31, v31, v31 row_ror:1 row_mask:0xf bank_mask:0xf\n" \
  "s_nop 1\n" \
  "v_max_u32_dpp v31, v31, v31 row_ror:2 row_mask:0xf bank_mask:0xf\n" \
  "s_nop 1\n" \
  "v_max_u32_dpp v31, v31, v31 row_ror:4 row_mask:0xf bank_mask:0xf\n" \
  "s_nop 1\n" \
  "v_max_u32_dpp v31, v31, v31 row_ror:8 row_mask:0xf bank_mask:0xf\n" \
  "v_mov_b32 v12, v31\n" \
  "s_nop 1\n" \
  "v_permlane16_swap_b32 v31, v12\n" \
  "v_max_u32 v31, v31, v12\n" \
  "v_mov_b32 v12, v31\n" \
  "s_nop 1\n" \
  "v_permlane32_swap_b32 v31, v12\n" \
  "v_max_u32 v31, v31, v12\n" \
  "s_mov_b64 exec, 1\n" \
  "ds_write_b32 v30, v31\n" \
  "s_mov_b64 exec, -1\n" \
  "s_waitcnt vmcnt(4) lgkmcnt(0)\n" \
  "s_barrier\n" \
  "ds_write_b128 v3, v[32:35] offset:8192\n" \
  "ds_write_b128 v3, v[36:39] offset:9216\n" \
  "ds_write_b128 v3, v[40:43] offset:10240\n" \
  "ds_write_b128 v3, v[44:47] offset:11264\n" \
  "ds_read_b64_tr_b16 v[112:113], v28 offset:12288\n" \
  "ds_read_b64_tr_b16 v[114:115], v28 offset:12800\n" \
  "ds_read_b64_tr_b16 v[116:117], v28 offset:45056\n" \
  "ds_read_b64_tr_b16 v[118:119], v28 offset:45568\n" \
  "ds_read_b64_tr_b16 v[120:121], v17 offset:12288\n" \
  "ds_read_b64_tr_b16 v[122:123], v17 offset:12800\n" \
  "ds_read_b64_tr_b16 v[124:125], v17 offset:45056\n" \
  "ds_read_b64_tr_b16 v[126:127], v17 offset:45568\n" \
  "ds_read_b32 v31, v30 offset:0\n" \
  "s_waitcnt lgkmcnt(0)\n" \
  "s_barrier\n" \
  "ds_write_b128 v3, v[48:51] offset:12288\n" \
  "ds_write_b128 v3, v[52:55] offset:13312\n" \
  "ds_write_b128 v3, v[56:59] offset:14336\n" \
  "ds_write_b128 v3, v[60:63] offset:15360\n" \
  "v_mul_f32 v31, 0x3f8e353f, v31\n" \
  "v_mul_f32 v30, 0x44870000, v31\n" \
  "v_mov_b64 v[12:13], 0\n" \
  "v_mov_b64 v[14:15], 0\n" \
  "v_mov_b64 v[16:17], 0\n" \
  "v_mov_b64 v[18:19], 0\n" \
  "v_mov_b64 v[20:21], 0\n" \
  "v_mov_b64 v[22:23], 0\n" \
  "v_readfirstlane_b32 s72, v31\n" \
  "v_readfirstlane_b32 s86, v30\n" \
  "s_lshr_b32 s86, s86, 23\n" \
  "s_addk_i32 s86, 0xff83\n" \
  "s_ashr_i32 s86, s86, 1\n" \
  "s_max_i32 s86, s86, -20\n" \
  "s_min_i32 s86, s86, 100\n" \
  "s_mov_b32 s82, s86\n" \
  "s_lshr_b32 s83, s72, 23\n" \
  "s_sub_i32 s83, s83, 243\n" \
  "s_waitcnt lgkmcnt(0)\n" \
  "s_add_u32 s88, s74, 157760\n" \
  "v_mov_b32 v59, s88\n" \
  "s_cmp_lg_u32 s61, 0\n" \
  "s_movk_i32 s76, -8000\n" \
  "s_cmovk_i32 s76, 9152\n" \
  "s_movk_i32 s77, 90\n" \
  "s_cmovk_i32 s77, 90\n" \
  "s_cselect_b32 s72, -1, 55\n" \
  "s_mov_b32 s87, 0x40000\n" \
  "s_cmp_eq_u32 s90, 0\n" \
  "s_cbranch_scc1 .LCV_W1_%=\n" \
  "s_branch .LCV_W2_%=\n" \
  ".LCV_W0_%=:\n" \
  "s_sleep 2\n" \
  "s_cmp_eq_u32 s61, 0\n" \
  "s_cbranch_scc1 .LCV_W2_%=\n" \
  "s_sleep 32\n" \
  ".LCV_W2_%=:\n" \
  "s_mov_b64 exec, 15\n" \
  "buffer_load_dword v58, v2, s[44:47], s68 offen sc1\n" \
  "s_mov_b64 exec, -1\n" \
  "s_sub_u32 s87, s87, 1\n" \
  "s_cbranch_scc1 .LCV_END_%=\n" \
  "s_waitcnt vmcnt(0)\n" \
  "v_cmp_eq_u32 vcc, 0, v58\n" \
  "s_and_b32 vcc_lo, vcc_lo, 15\n" \
  "s_cbranch_scc1 .LCV_W0_%=\n" \
  "s_mov_b32 m0, s99\n" \
  "s_add_u32 s88, s99, 8192\n" \
  "global_load_lds_dwordx4 v10, s[62:63] sc1\n" \
  "global_load_lds_dwordx4 v10, s[62:63] offset:1024 sc1\n" \
  "s_mov_b32 m0, s88\n" \
  "s_nop 0\n" \
  "global_load_lds_dwordx4 v10, s[64:65] sc1\n" \
  "global_load_lds_dwordx4 v10, s[64:65] offset:1024 sc1\n" \
  ".LCV_W1_%=:\n" \
  "s_add_u32 s62, s62, 0x80000\n" \
  "s_addc_u32 s63, s63, 0\n" \
  "s_add_u32 s64, s64, 0x40000\n" \
  "s_addc_u32 s65, s65, 0\n" \
  "s_add_u32 s68, s68, 0x1000\n" \
  "s_cmp_eq_u32 s61, 0\n" \
  "s_cbranch_scc1 .LCV_W4_%=\n" \
  "s_add_u32 s38, s67, 4992\n" \
  "v_mov_b32 v58, s38\n" \
  "buffer_load_dword v58, v58, s[0:3], 0 offen sc1\n" \
  "s_waitcnt vmcnt(0)\n" \
  "v_readfirstlane_b32 vcc_lo, v58\n" \
  "s_movk_i32 s87, 200\n" \
  "s_cmp_eq_u32 vcc_lo, 0\n" \
  "s_cbranch_scc1 .LCV_W4_%=\n" \
  "s_add_u32 vcc_lo, vcc_lo, 590\n" \
  ".LCV_W3_%=:\n" \
  "s_sleep 2\n" \
  "s_memrealtime s[38:39]\n" \
  "s_sub_u32 s87, s87, 1\n" \
  "s_cbranch_scc1 .LCV_W4_%=\n" \
  "s_waitcnt lgkmcnt(0)\n" \
  "s_sub_u32 s38, s38, vcc_lo\n" \
  "s_cmp_lt_i32 s38, 0\n" \
  "s_cbranch_scc1 .LCV_W3_%=\n" \
  ".LCV_W4_%=:\n" \
  "s_waitcnt vmcnt(0)\n" \
  ".p2align 6\n" \
  ".LCV_TOP_%=:\n" \
  "s_waitcnt lgkmcnt(0)\n" \
  "s_barrier\n" \
  "s_setprio 3\n" \
  "ds_read_b128 v[24:27], v1 offset:0\n" \
  "ds_read_b128 v[28:31], v1 offset:1024\n" \
  "ds_read_b128 v[32:35], v1 offset:2048\n" \
  "ds_read_b128 v[36:39], v1 offset:3072\n" \
  "ds_read_b128 v[40:43], v1 offset:4096\n" \
  "ds_read_b128 v[44:47], v1 offset:5120\n" \
  "ds_read_b128 v[48:51], v1 offset:6144\n" \
  "ds_read_b128 v[52:55], v1 offset:7168\n" \
  "s_memrealtime s[38:39]\n" \
  "s_waitcnt lgkmcnt(7)\n" \
  "v_smfmac_f32_16x16x64_bf16 v[12:15], v[24:27], v[64:71], v0\n" \
  "s_waitcnt lgkmcnt(6)\n" \
  "v_smfmac_f32_16x16x64_bf16 v[12:15], v[28:31], v[72:79], v0\n" \
  "s_waitcnt lgkmcnt(5)\n" \
  "v_smfmac_f32_16x16x64_bf16 v[12:15], v[32:35], v[80:87], v0\n" \
  "s_waitcnt lgkmcnt(4)\n" \
  "v_smfmac_f32_16x16x64_bf16 v[12:15], v[36:39], v[88:95], v0\n" \
  "s_waitcnt lgkmcnt(3)\n" \
  "v_smfmac_f32_16x16x64_bf16 v[12:15], v[40:43], v[96:103], v0\n" \
  "s_waitcnt lgkmcnt(2)\n" \
  "v_smfmac_f32_16x16x64_bf16 v[12:15], v[44:47], v[104:111], v0\n" \
  "s_waitcnt lgkmcnt(1)\n" \
  "v_smfmac_f32_16x16x64_bf16 v[12:15], v[48:51], v[112:119], v0\n" \
  "s_waitcnt lgkmcnt(0)\n" \
  "v_smfmac_f32_16x16x64_bf16 v[12:15], v[52:55], v[120:127], v0\n" \
  "ds_read_b32 v11, v1 offset:26688\n" \
  "s_mov_b32 s61, s38\n" \
  "s_add_u32 s38, s38, s79\n" \
  "s_add_u32 s38, s38, s96\n" \
  "s_mov_b32 s96, 0\n" \
  "v_or_b32 v57, 1, s38\n" \
  "v_add_u32 v56, s67, v10\n" \
  "s_add_u32 s39, s67, s76\n" \
  "s_add_u32 s38, s38, s77\n" \
  "s_bitset1_b32 s38, 0\n" \
  "v_writelane_b32 v56, s39, 1\n" \
  "v_writelane_b32 v57, s38, 1\n" \
  "s_sub_u32 s39, s71, s72\n" \
  "s_cmp_lt_u32 s39, 9\n" \
  "s_cselect_b64 exec, 19, 3\n" \
  "buffer_store_dword v57, v56, s[0:3], 0 offen sc1\n" \
  "s_mov_b64 exec, -1\n" \
  "v_permlane32_swap_b32 v12, v14\n" \
  "v_permlane32_swap_b32 v13, v15\n" \
  "v_pk_add_f32 v[12:13], v[12:13], v[14:15]\n" \
  "s_nop 0\n" \
  "v_cvt_pk_bf16_f32 v12, v12, v13\n" \
  "ds_write_b32 v4, v12\n" \
  "s_setprio 2\n" \
  "v_mov_b64 v[12:13], s[92:93]\n" \
  "v_mov_b64 v[14:15], s[92:93]\n" \
  "s_waitcnt lgkmcnt(1)\n" \
  "v_readfirstlane_b32 s86, v11\n" \
  "s_lshr_b32 s86, s86, 23\n" \
  "s_add_i32 s86, s86, s83\n" \
  "s_ashr_i32 s86, s86, 1\n" \
  "s_max_i32 s86, s86, s82\n" \
  "s_min_i32 s86, s86, 100\n" \
  "s_lshl_b32 s86, s86, 23\n" \
  "s_sub_i32 s84, 0xc8800000, s86\n" \
  "s_cmp_eq_u32 s73, 5\n" \
  "v_smfmac_f32_16x16x64_bf16 v[16:19], v[24:27], v[128:135], v0\n" \
  "v_smfmac_f32_16x16x64_bf16 v[16:19], v[28:31], v[136:143], v0\n" \
  "v_smfmac_f32_16x16x64_bf16 v[16:19], v[32:35], v[144:151], v0\n" \
  "v_smfmac_f32_16x16x64_bf16 v[16:19], v[36:39], v[152:159], v0\n" \
  "v_smfmac_f32_16x16x64_bf16 v[16:19], v[40:43], v[160:167], v0\n" \
  "v_smfmac_f32_16x16x64_bf16 v[16:19], v[44:47], v[168:175], v0\n" \
  "s_waitcnt lgkmcnt(0)\n" \
  "s_barrier\n" \
  "ds_read_b64_tr_b16 v[60:61], v6 offset:0\n" \
  "ds_read_b64_tr_b16 v[62:63], v9 offset:0\n" \
  "v_smfmac_f32_16x16x64_bf16 v[16:19], v[48:51], v[176:183], v0\n" \
  "v_smfmac_f32_16x16x64_bf16 v[16:19], v[52:55], v[184:191], v0\n" \
  "v_smfmac_f32_16x16x64_bf16 v[20:23], v[24:27], v[192:199], v0\n" \
  "v_smfmac_f32_16x16x64_bf16 v[20:23], v[28:31], v[200:207], v0\n" \
  "v_smfmac_f32_16x16x64_bf16 v[20:23], v[32:35], v[208:215], v0\n" \
  "v_smfmac_f32_16x16x64_bf16 v[20:23], v[36:39], v[216:223], v0\n" \
  "v_smfmac_f32_16x16x64_bf16 v[20:23], v[40:43], v[224:231], v0\n" \
  "v_smfmac_f32_16x16x64_bf16 v[20:23], v[44:47], v[232:239], v0\n" \
  "v_smfmac_f32_16x16x64_bf16 v[20:23], v[48:51], v[240:247], v0\n" \
  "v_smfmac_f32_16x16x64_bf16 v[20:23], v[52:55], v[248:255], v0\n" \
  "s_cmp_eq_u32 s71, 0\n" \
  "v_permlane32_swap_b32 v16, v18\n" \
  "v_permlane32_swap_b32 v17, v19\n" \
  "v_add_f32 v16, v16, v18\n" \
  "v_add_f32 v17, v17, v19\n" \
  "v_cvt_pk_bf16_f32 v16, v16, v17\n" \
  "s_waitcnt lgkmcnt(0)\n" \
  "v_max_u32 v60, 1, v60\n" \
  "v_max_u32 v61, 1, v61\n" \
  "v_max_u32 v62, 1, v62\n" \
  "v_max_u32 v63, 1, v63\n" \
  "buffer_store_dwordx4 v[60:63], v7, s[48:51], s69 offen sc1\n" \
  "v_permlane32_swap_b32 v20, v22\n" \
  "v_permlane32_swap_b32 v21, v23\n" \
  "v_pk_add_f32 v[20:21], v[20:21], v[22:23]\n" \
  "v_max_u32 v18, 1, v16\n" \
  "v_cvt_pk_bf16_f32 v17, v20, v21\n" \
  "ds_write_b64 v5, v[16:17]\n" \
  "v_max_u32 v19, 1, v17\n" \
  "s_nop 1\n" \
  "v_permlane32_swap_b32 v18, v19\n" \
  "buffer_store_dwordx2 v[18:19], v8, s[52:55], s70 offen sc1\n" \
  "ds_read_b128 v[24:27], v3 offset:0\n" \
  "ds_read_b128 v[28:31], v3 offset:1024\n" \
  "ds_read_b128 v[32:35], v3 offset:2048\n" \
  "ds_read_b128 v[36:39], v3 offset:3072\n" \
  "ds_read_b128 v[40:43], v3 offset:8192\n" \
  "ds_read_b128 v[44:47], v3 offset:9216\n" \
  "ds_read_b128 v[48:51], v3 offset:10240\n" \
  "ds_read_b128 v[52:55], v3 offset:11264\n" \
  "s_cbranch_scc1 .LCV_LAST_%=\n" \
  "s_cmp_eq_u32 s73, 5\n" \
  "s_cbranch_scc0 .LCV_NMK_%=\n" \
  "s_mov_b32 m0, s78\n" \
  "s_nop 0\n" \
  "buffer_load_dword v2, s[0:3], s66 offen sc1 lds\n" \
  ".LCV_NMK_%=:\n" \
  "s_setprio 0\n" \
  "s_waitcnt lgkmcnt(8)\n" \
  "s_barrier\n" \
  "s_add_u32 s67, s67, 1024\n" \
  "v_mov_b32 v11, 0\n" \
  "ds_write_b32 v59, v11\n" \
  "ds_read_b128 v[56:59], v3 offset:4096\n" \
  "ds_read_b128 v[60:63], v3 offset:5120\n" \
  "v_mov_b64 v[20:21], s[92:93]\n" \
  "v_mov_b64 v[22:23], s[92:93]\n" \
  "v_mov_b64 v[16:17], s[92:93]\n" \
  "v_mov_b64 v[18:19], s[92:93]\n" \
  "s_mov_b32 m0, s94\n" \
  "s_mov_b64 exec, 15\n" \
  "buffer_load_dword v2, s[44:47], s68 offen sc1 lds\n" \
  "s_mov_b64 exec, -1\n" \
  "s_setprio 2\n" \
  "s_waitcnt lgkmcnt(0)\n" \
  "s_barrier\n" \
  "ds_read_b128 v[4:7], v1 offset:8192\n" \
  "ds_read_b128 v[8:11], v1 offset:9216\n" \
  "s_waitcnt lgkmcnt(1)\n" \
  "v_smfmac_f32_16x16x64_bf16 v[12:15], v[4:7], v[24:31], v0\n" \
  "ds_read_b128 v[24:27], v3 offset:6144\n" \
  "ds_read_b128 v[28:31], v3 offset:7168\n" \
  "v_smfmac_f32_16x16x64_bf16 v[16:19], v[4:7], v[32:39], v0\n" \
  "ds_read_b128 v[32:35], v3 offset:12288\n" \
  "ds_read_b128 v[36:39], v3 offset:13312\n" \
  "s_waitcnt lgkmcnt(4)\n" \
  "v_smfmac_f32_16x16x64_bf16 v[12:15], v[8:11], v[40:47], v0\n" \
  "v_smfmac_f32_16x16x64_bf16 v[16:19], v[8:11], v[48:55], v0\n" \
  "v_smfmac_f32_16x16x64_bf16 v[20:23], v[4:7], v[56:63], v0\n" \
  "ds_read_b128 v[56:59], v3 offset:14336\n" \
  "ds_read_b128 v[60:63], v3 offset:15360\n" \
  "v_mov_b64 v[40:41], s[92:93]\n" \
  "v_mov_b64 v[42:43], s[92:93]\n" \
  "s_nop 2\n" \
  "v_permlane32_swap_b32 v12, v17\n" \
  "v_permlane32_swap_b32 v13, v16\n" \
  "v_permlane32_swap_b32 v14, v19\n" \
  "v_permlane32_swap_b32 v15, v18\n" \
  "v_pk_add_f32 v[12:13], v[12:13], v[16:17] op_sel:[0,1] op_sel_hi:[1,0]\n" \
  "v_pk_add_f32 v[14:15], v[14:15], v[18:19] op_sel:[0,1] op_sel_hi:[1,0]\n" \
  "buffer_atomic_add_x2 v[12:13], v2, s[40:43], s56 offen\n" \
  "buffer_atomic_add_x2 v[14:15], v2, s[40:43], s57 offen\n" \
  "s_waitcnt lgkmcnt(2)\n" \
  "v_smfmac_f32_16x16x64_bf16 v[20:23], v[8:11], v[32:39], v0\n" \
  "v_smfmac_f32_16x16x64_bf16 v[40:43], v[4:7], v[24:31], v0\n" \
  "s_waitcnt lgkmcnt(0)\n" \
  "v_smfmac_f32_16x16x64_bf16 v[40:43], v[8:11], v[56:63], v0\n" \
  "s_nop 7\n" \
  "v_permlane32_swap_b32 v20, v41\n" \
  "v_permlane32_swap_b32 v21, v40\n" \
  "v_permlane32_swap_b32 v22, v43\n" \
  "v_permlane32_swap_b32 v23, v42\n" \
  "v_pk_add_f32 v[20:21], v[20:21], v[40:41] op_sel:[0,1] op_sel_hi:[1,0]\n" \
  "v_pk_add_f32 v[22:23], v[22:23], v[42:43] op_sel:[0,1] op_sel_hi:[1,0]\n" \
  "buffer_atomic_add_x2 v[20:21], v2, s[40:43], s58 offen\n" \
  "buffer_atomic_add_x2 v[22:23], v2, s[40:43], s59 offen\n" \
  "s_setprio 0\n" \
  "s_memrealtime s[38:39]\n" \
  "v_mbcnt_lo_u32_b32 v48, -1, 0\n" \
  "v_mbcnt_hi_u32_b32 v48, -1, v48\n" \
  "s_add_u32 s88, s74, 157760\n" \
  "v_mov_b32 v59, s88\n" \
  "v_lshl_add_u32 v52, v48, 2, v59\n" \
  "ds_read_b32 v52, v52 offset:192\n" \
  "v_lshrrev_b32 v49, 3, v48\n" \
  "v_and_b32 v50, 7, v48\n" \
  "v_lshlrev_b32 v50, 4, v50\n" \
  "v_lshl_or_b32 v44, v49, 8, v50\n" \
  "s_mov_b32 s87, 0x40000\n" \
  "v_lshlrev_b32 v10, 4, v48\n" \
  "v_lshl_add_u32 v58, v48, 2, s94\n" \
  "s_waitcnt vmcnt(4)\n" \
  "ds_read_b32 v58, v58\n" \
  "s_waitcnt lgkmcnt(0)\n" \
  "v_cmp_eq_u32 vcc, 0, v58\n" \
  "s_and_b32 vcc_lo, vcc_lo, 15\n" \
  "s_cbranch_scc0 .LCV_GOT_%=\n" \
  "s_branch .LCV_ASK_%=\n" \
  ".LCV_LATE_%=:\n" \
  "v_cmp_eq_u32 vcc, 0, v58\n" \
  "s_and_b32 vcc_lo, vcc_lo, 15\n" \
  "s_cbranch_scc0 .LCV_GOT_%=\n" \
  "s_sleep 12\n" \
  ".LCV_ASK_%=:\n" \
  "s_mov_b64 exec, 15\n" \
  "buffer_load_dword v58, v2, s[44:47], s68 offen sc1\n" \
  "s_mov_b64 exec, -1\n" \
  "s_sub_u32 s87, s87, 1\n" \
  "s_cbranch_scc1 .LCV_END_%=\n" \
  "s_waitcnt vmcnt(0)\n" \
  "s_branch .LCV_LATE_%=\n" \
  ".LCV_GOT_%=:\n" \
  "s_mov_b32 m0, s99\n" \
  "s_add_u32 s88, s99, 8192\n" \
  "global_load_lds_dwordx4 v10, s[62:63] sc1\n" \
  "global_load_lds_dwordx4 v10, s[62:63] offset:1024 sc1\n" \
  "s_mov_b32 m0, s88\n" \
  "s_nop 0\n" \
  "global_load_lds_dwordx4 v10, s[64:65] sc1\n" \
  "global_load_lds_dwordx4 v10, s[64:65] offset:1024 sc1\n" \
  ".LCV_HAVE_%=:\n" \
  "s_waitcnt lgkmcnt(0)\n" \
  "s_sub_u32 s61, s38, s61\n" \
  "s_add_u32 s79, s79, 2\n" \
  "s_min_u32 s79, s79, s61\n" \
  "s_sub_u32 s39, s38, 150\n" \
  "v_subrev_u32 v52, s39, v52\n" \
  "v_cmp_lt_u32 vcc, 0x1000, v52\n" \
  "s_cmp_eq_u64 vcc, 0\n" \
  "s_cbranch_scc1 .LCV_OKM_%=\n" \
  ".LCV_AGN_%=:\n" \
  "buffer_load_dword v52, v2, s[0:3], s66 offen sc1\n" \
  "s_sub_u32 s87, s87, 1\n" \
  "s_cbranch_scc1 .LCV_END_%=\n" \
  "s_waitcnt vmcnt(0)\n" \
  "v_cmp_eq_u32 vcc, 0, v52\n" \
  "v_subrev_u32 v52, s39, v52\n" \
  "s_cmp_eq_u64 vcc, 0\n" \
  "s_cbranch_scc1 .LCV_OKM_%=\n" \
  "s_sleep 4\n" \
  "s_branch .LCV_AGN_%=\n" \
  ".LCV_OKM_%=:\n" \
  "s_nop 1\n" \
  "v_max_i32_dpp v52, v52, v52 row_ror:1 row_mask:0xf bank_mask:0xf\n" \
  "s_nop 1\n" \
  "v_max_i32_dpp v52, v52, v52 row_ror:2 row_mask:0xf bank_mask:0xf\n" \
  "s_nop 1\n" \
  "v_max_i32_dpp v52, v52, v52 row_ror:4 row_mask:0xf bank_mask:0xf\n" \
  "s_nop 1\n" \
  "v_max_i32_dpp v52, v52, v52 row_ror:8 row_mask:0xf bank_mask:0xf\n" \
  "s_movk_i32 vcc_lo, 150\n" \
  "v_readlane_b32 s88, v52, 0\n" \
  "s_max_i32 vcc_lo, vcc_lo, s88\n" \
  "v_readlane_b32 s88, v52, 16\n" \
  "s_max_i32 vcc_lo, vcc_lo, s88\n" \
  "v_readlane_b32 s88, v52, 32\n" \
  "s_max_i32 vcc_lo, vcc_lo, s88\n" \
  "v_readlane_b32 s88, v52, 48\n" \
  "s_max_i32 vcc_lo, vcc_lo, s88\n" \
  "s_add_u32 s66, s66, 1024\n" \
  "s_add_u32 vcc_lo, vcc_lo, s39\n" \
  "s_add_u32 vcc_lo, vcc_lo, 23\n" \
  ".LCV_TW_%=:\n" \
  "s_memrealtime s[38:39]\n" \
  "s_waitcnt lgkmcnt(0)\n" \
  "s_sub_u32 s38, s38, vcc_lo\n" \
  "s_cmp_lt_i32 s38, 0\n" \
  "s_cbranch_scc1 .LCV_TW_%=\n" \
  "buffer_load_dwordx4 v[24:27], v44, s[40:43], s60 offen sc1\n" \
  "buffer_load_dwordx4 v[28:31], v44, s[40:43], s60 offen offset:2048 sc1\n" \
  "s_add_u32 s62, s62, 0x80000\n" \
  "s_addc_u32 s63, s63, 0\n" \
  "s_add_u32 s64, s64, 0x40000\n" \
  "s_addc_u32 s65, s65, 0\n" \
  "s_add_u32 s68, s68, 0x1000\n" \
  "s_add_u32 s69, s69, 0x10000\n" \
  "s_add_u32 s70, s70, 0x10000\n" \
  "s_sub_u32 s88, s73, 4\n" \
  "v_lshrrev_b32 v49, 2, v48\n" \
  "v_lshrrev_b32 v50, 4, v48\n" \
  "v_bitop3_b32 v50, v49, v50, 3 bitop3:0x28\n" \
  "v_lshl_add_u32 v4, v50, 3, s97\n" \
  "v_and_b32 v50, 3, v48\n" \
  "v_lshl_add_u32 v4, v50, 5, v4\n" \
  "v_and_b32 v49, 1, v49\n" \
  "v_lshl_add_u32 v4, v49, 8, v4\n" \
  "v_and_b32 v49, 8, v48\n" \
  "v_lshl_add_u32 v4, v49, 4, v4\n" \
  "v_and_b32 v51, 15, v48\n" \
  "v_bfe_u32 v49, v48, 4, 1\n" \
  "v_lshrrev_b32 v50, 5, v48\n" \
  "s_lshl_b32 s89, s88, 10\n" \
  "s_add_u32 s89, s89, s74\n" \
  "s_add_u32 s89, s89, 158720\n" \
  "v_lshrrev_b32 v9, 3, v51\n" \
  "v_mul_u32_u24 v6, 41, v9\n" \
  "v_xor_b32 v6, v6, v51\n" \
  "v_lshl_add_u32 v6, v6, 3, s89\n" \
  "v_lshl_add_u32 v6, v49, 6, v6\n" \
  "v_lshl_add_u32 v6, v50, 9, v6\n" \
  "v_xor_b32 v9, 0x90, v6\n" \
  "v_lshlrev_b32 v8, 4, v49\n" \
  "v_lshl_add_u32 v8, v51, 5, v8\n" \
  "v_lshl_add_u32 v8, v50, 3, v8\n" \
  "s_lshl_b32 s89, s88, 8\n" \
  "s_add_u32 s89, s89, s74\n" \
  "s_add_u32 s89, s89, 139264\n" \
  "v_and_b32 v49, 31, v48\n" \
  "v_lshl_add_u32 v5, v49, 3, s89\n" \
  "v_lshl_add_u32 v5, v50, 10, v5\n" \
  "v_lshrrev_b32 v49, 2, v51\n" \
  "v_and_b32 v50, 5, v51\n" \
  "v_and_or_b32 v49, v49, 2, v50\n" \
  "v_and_b32 v50, 48, v48\n" \
  "v_lshlrev_b32 v7, 3, v50\n" \
  "v_lshl_add_u32 v7, v49, 4, v7\n" \
  "v_bitop3_b32 v49, v51, 2, 2 bitop3:0x0c\n" \
  "v_lshl_add_u32 v7, v49, 29, v7\n" \
  "v_mov_b64 v[12:13], 0\n" \
  "v_mov_b64 v[14:15], 0\n" \
  "v_mov_b64 v[16:17], 0\n" \
  "v_mov_b64 v[18:19], 0\n" \
  "v_mov_b64 v[20:21], 0\n" \
  "v_mov_b64 v[22:23], 0\n" \
  "v_and_b32 v50, 3, v48\n" \
  "v_lshl_add_u32 v40, v50, 2, s98\n" \
  "v_lshrrev_b32 v50, 4, v48\n" \
  "v_mad_u32_u24 v40, v50, s95, v40\n" \
  "v_bfe_u32 v49, v48, 3, 1\n" \
  "v_lshl_add_u32 v40, v49, 6, v40\n" \
  "v_bfe_u32 v49, v48, 2, 1\n" \
  "v_lshl_add_u32 v40, v49, 7, v40\n" \
  "v_xor_b32 v41, 16, v40\n" \
  "v_xor_b32 v42, 32, v40\n" \
  "v_xor_b32 v43, 48, v40\n" \
  "s_add_i32 s80, s86, 0x35000000\n" \
  "v_mov_b32 v11, 0\n" \
  "s_mov_b32 s89, s60\n" \
  "s_bitcmp1_b32 s60, 7\n" \
  "s_cselect_b32 s88, 0x10000, 0\n" \
  "s_bitset1_b32 s88, 7\n" \
  "s_xor_b32 s56, s56, s88\n" \
  "s_xor_b32 s57, s57, s88\n" \
  "s_xor_b32 s58, s58, s88\n" \
  "s_xor_b32 s59, s59, s88\n" \
  "s_xor_b32 s60, s60, s88\n" \
  "s_sub_u32 s71, s71, 1\n" \
  ".LCV_SEE_%=:\n" \
  "s_waitcnt vmcnt(1)\n" \
  "v_cvt_f32_i32 v24, v24\n" \
  "v_cvt_f32_i32 v25, v25\n" \
  "v_cvt_f32_i32 v26, v26\n" \
  "v_cvt_f32_i32 v27, v27\n" \
  "v_max_f32 v45, |v24|, |v26|\n" \
  "v_pk_mul_f32 v[24:25], v[24:25], s[80:81] op_sel_hi:[1,0]\n" \
  "v_pk_mul_f32 v[26:27], v[26:27], s[80:81] op_sel_hi:[1,0]\n" \
  "s_nop 0\n" \
  "v_cvt_pk_bf16_f32 v32, v24, v26\n" \
  "v_cvt_pk_bf16_f32 v33, v25, v27\n" \
  "ds_write_b32 v40, v32\n" \
  "ds_write_b32 v41, v33\n" \
  "s_waitcnt vmcnt(0)\n" \
  "v_cvt_f32_i32 v28, v28\n" \
  "v_cvt_f32_i32 v30, v30\n" \
  "v_cvt_f32_i32 v29, v29\n" \
  "v_max3_f32 v45, v45, |v28|, |v30|\n" \
  "v_cvt_f32_i32 v31, v31\n" \
  "v_cmp_lt_f32 vcc, s91, v45\n" \
  "v_pk_mul_f32 v[28:29], v[28:29], s[80:81] op_sel_hi:[1,0]\n" \
  "v_pk_mul_f32 v[30:31], v[30:31], s[80:81] op_sel_hi:[1,0]\n" \
  "s_cmp_eq_u64 vcc, 0\n" \
  "s_cbranch_scc0 .LCV_POLL_%=\n" \
  "v_cvt_pk_bf16_f32 v36, v28, v30\n" \
  "v_cvt_pk_bf16_f32 v37, v29, v31\n" \
  "ds_write_b32 v42, v36\n" \
  "ds_write_b32 v43, v37\n" \
  "v_dot2c_f32_bf16 v11, v32, v32\n" \
  "v_dot2c_f32_bf16 v11, v33, v33\n" \
  "v_dot2c_f32_bf16 v11, v36, v36\n" \
  "v_dot2c_f32_bf16 v11, v37, v37\n" \
  "s_nop 1\n" \
  "s_nop 0\n" \
  "v_cmp_lt_f32 vcc, 4.0, v11\n" \
  "s_cbranch_vccz .LCV_TOP_%=\n" \
  "s_mov_b64 exec, vcc\n" \
  "ds_max_u32 v59, v11\n" \
  "s_mov_b64 exec, -1\n" \
  "s_branch .LCV_TOP_%=\n" \
  ".LCV_POLL_%=:\n" \
  "buffer_load_dwordx4 v[24:27], v44, s[40:43], s89 offen sc1\n" \
  "buffer_load_dwordx4 v[28:31], v44, s[40:43], s89 offen offset:2048 sc1\n" \
  "s_sub_u32 s87, s87, 1\n" \
  "s_cbranch_scc1 .LCV_END_%=\n" \
  "s_branch .LCV_SEE_%=\n" \
  ".LCV_LAST_%=:\n" \
  "s_waitcnt vmcnt(0)\n" \
  ".LCV_END_%=:\n" \
  "s_endpgm\n"
__device__ __forceinline__ void start_kgen(const MegaArgs& A, char* smem, const int kg) {
  const int w = A.wv, l = lane_id(), tid = 64 * w + l, c = l & 15, g = l >> 4;
  KGen G;
  v4u raw[2] = {};
#pragma unroll
  for (int i = 0; i < TP; ++i)
    if (kg < 32) raw[i] = *reinterpret_cast<const v4u*>(A.x0 + (size_t)(TP * (8 * (kg & 7) + w) + i) * DM + l * 8);
  G.load(A, 8 * kg + w, l);
  {
    const __amdgpu_buffer_rsrc_t rz = MKRS(A.ws + OFF_Z, Z_BYTES);
#pragma unroll
    for (int i = 0; i < 4; ++i) {
      const u64 z = i == 0 && kg < (int)(2 * SZ_ACC / 8192) ? ACC_INIT : 0ull;
      ST128((v4u{(unsigned)z, (unsigned)(z >> 32), (unsigned)z, (unsigned)(z >> 32)}), rz, tid * 16, (i * 64 + kg) * 8192);
    }
  }
  const u64 tag = call_tag(A);
  if (TP && kg < 32) {
    unsigned t = row_stats(raw[0]);
    if (TP > 1) {
      const unsigned t1 = row_stats(raw[1]);
      t = l < 2 ? t : t1;
    }
    if (l < 2 * TP) __hip_atomic_store((gu64*)(A.ws + OFF_GT) + 2 * (L_SEQ + TP * (8 * kg + w)) + l, tagged(tag, (l & 1) ? t : t << 16), RLX_AGENT);
  }
  DRAIN();
  __syncthreads();
  if (tid < 8) __hip_atomic_store((gu64*)(A.ws + OFF_RD) + tid * 64 + kg, tag, RLX_AGENT);
  asm volatile("" : "+v"(G.pa), "+v"(G.pb), "+v"(G.pe));
  G.setup();
  G.st = smem + w * 16640;
  const __amdgpu_buffer_rsrc_t rs = MKRS(A.ws + OFF_KF + (8 * kg + w) * 2048, 386 * 4);
  SST(1);
  unsigned tw = 0u;
#define TLB_ASK()                                                                                                      \
  if (tid < 384) {                                                                                                     \
    const unsigned o = (unsigned)OFF_XA + (unsigned)(tid * 8 + ((blockIdx.x >> 3) & 7)) * 65536u;                      \
    if (o < WS_TOTAL) asm volatile("global_load_dword %0, %1, %2" : "=v"(tw) : "v"(o), "s"(A.ws) : "memory");          \
  }
#define X2_CLEAR() \
  _Pragma("unroll") for (int i = 0; i < 4; ++i) ST128((v4u{0u, 0u, 0u, 0u}), MKRS(A.ws + OFF_X2, SZ_X), tid * 16, (4 * kg + i) * 8192);
  if (XZP == 1) { X2_CLEAR() }
  unsigned tp = 0u;
  if (IPF) {
    const char* pc;
    unsigned n;
    asm volatile("s_getpc_b64 %0\n.LKG_PC:\n\ts_mov_b32 %1, .LKG_LAST-.LKG_PC" : "=s"(pc), "=s"(n));
    const unsigned o = (unsigned)(tid * 8 + ((blockIdx.x >> 3) & 7)) * 128u;
    if ((int)o < (int)n) asm volatile("global_load_dword %0, %1, %2" : "=v"(tp) : "v"(o), "s"(pc) : "memory");
  }
  if (TLBW == 1) TLB_ASK()
  unsigned pf[6] = {};
#define PF_ASK(M)                                                                                                                  \
  if (kg < 32) {                                                                                                                   \
    if ((M) & 1) asm volatile("global_load_dword %0, %2, %3\n\tglobal_load_dword %1, %2, %3 offset:128" : "=&v"(pf[0]), "=&v"(pf[1]) : "v"(kg * 131072 + tid * 256), "s"(A.Wo)); \
    if ((M) & 2) asm volatile("global_load_dword %0, %2, %3\n\tglobal_load_dword %1, %2, %3 offset:128" : "=&v"(pf[2]), "=&v"(pf[3]) : "v"(kg * 256 + tid * 8192), "s"(A.Wq)); \
    if ((M) & 4) asm volatile("global_load_dword %0, %2, %3\n\tglobal_load_dword %1, %2, %3 offset:128" : "=&v"(pf[4]), "=&v"(pf[5]) : "v"(kg * 256 + tid * 8192), "s"(A.Wk)); \
  }
  PF_ASK(PFA)
  G.columns(l, 0, 16);
  G.get(G.bh, G.bl, l);
  if (XZP == 2) { X2_CLEAR() }
  PF_ASK(PFB)
  if (TLBW == 2) TLB_ASK()
  (void)G.scale();
  const Cx zb = G.power(256.f);
  Cx b = G.C, z = G.power(2.f);
#pragma unroll 1
  for (int f = 1; f < 9;) {
    char* p = G.st + l * 8;
    const int fe = f == 1 ? 4 : 9;
#pragma unroll 1
    for (; f < fe; ++f) {
      b = cmul(b, zb);
      if (f == 2 || f == 4) z = cmul(z, z);
      Cx y = b;
      p = G.rows(p, y, z, f == 1 ? 8 : f < 4 ? 4 : 2);
    }
    const f32x4 d = G.tile<false>(l);
    const v4u r = {pkh(d.x, d.x), pkh(d.y, d.y), pkh(d.z, d.z), pkh(d.w, d.w)};
    if (f == 4) {
      ST128(r, rs, (g < 2 ? 8 * c + 4 * g : 64 * g + 4 * c) * 4, 0);
    } else {
      ST64((v2u{r.x, r.y}), rs, (256 + 64 * g + 2 * c) * 4, 0);
      ST64((v2u{r.z, r.w}), rs, (288 + 64 * g + 2 * c) * 4, 0);
    }
  }
  SST(2);
  DRAIN();
  asm volatile("" ::"v"(tp), "v"(tw), "v"(pf[0]), "v"(pf[1]), "v"(pf[2]), "v"(pf[3]), "v"(pf[4]), "v"(pf[5]));
  if (l == 0) __hip_atomic_store((gu64*)(A.ws + OFF_KG) + kg * 16 + w, tag, RLX_AGENT);
  if (XZP == 3) { X2_CLEAR() }
  const u64 rv = (XZP & 3) && w == 0 ? ready_look(A) : 0;
  if (TLBW == 3) {
    TLB_ASK()
    DRAIN();
    asm volatile("" ::"v"(tw));
  }
  DRAIN();
  __syncthreads();
  if ((XZP & 3) && w == 0) {
    ready_wait(A, tag, rv);
    if (l < 8) set_flag((unsigned*)(A.ws + OFF_FL) + FL_FZ + l * 64 + kg);
  }
  SST(3);
}

constexpr int C_SM = 131072, C_SO = C_SM + 8192, C_KT = C_SO + 2048, C_X = C_KT + 16384, C_SQ = C_X + 1024;
static_assert(C_SQ + 4096 <= 163840 && C_SQ % 512 == 0, "lds");
__device__ __forceinline__ v4u mk_srd(const void* p, const unsigned bytes) {
  const unsigned long long a = (unsigned long long)p;
  return v4u{(unsigned)a, (unsigned)(a >> 32) & 0xffffu, bytes, 0x00020000u};
}
template <bool ATT>
__device__ __forceinline__ void role_chain_t(const MegaArgs& A, char* smem, const int L, const int h, const int rep) {
  const int w = A.wv;
  char* ws = A.ws;
  unsigned* FL = (unsigned*)(ws + OFF_FL);
  const int l = lane_id(), c = l & 15, g = l >> 4;
  unsigned* FQ = FL + FL_FQ + L * 64 * 32 * 32;
  const int wb = (w + ROT_H) & 7, tb = ROT_T;
  const unsigned sb = lds_base(smem);
  __builtin_amdgcn_s_waitcnt(0x0f70);
  glds16(A.mem0, (unsigned)((c & 7) * DM + 64 * w + 16 * g + 8 * (c >> 3)) * 2, sb + C_SM + w * 1024);
#pragma unroll
  for (int i = 0; i < 16; ++i)
    glds16((const char*)A.Wo + (size_t)(h * DH + 32 * (i >> 2) + 4 * w + (i & 2)) * (DM * 2),
           (unsigned)((i & 1) * 1024 + ((l ^ (((i & 1) | ((w & 3) << 1)) << 1)) << 4)), sb + w * 16384 + i * 1024);
  const u64 tag = call_tag(A);
  const char* gK = ws + OFF_QK + L * SZ_QK + ((size_t)h * 16 + 2 * w) * 1024;
  const char* gV = ws + OFF_VF + L * SZ_O + ((size_t)h * 8 + 2 * w - 8) * 1024;
  v8u pv, pw;
  {
    const unsigned voff = g * 256 + c * 8, wp = sb + w * 16384 + l * 16;
    pv[0] = (c & 8) ? 0xEEEE : 0x4444;
    pv[1] = sb + C_SM + l * 16;
    const unsigned ra = sb + C_SQ + (g >> 1) * 512 + (c ^ ((g & 1) * 18)) * 8;
    pv[2] = ATT ? ra : voff;
    pv[3] = ATT ? ra ^ 0x108 : wp;
    pv[4] = sb + ((c & 2) ? C_SQ : C_SO) + w * 256 + (((c & 5) | ((c >> 2) & 2)) + 8 * (g >> 1)) * 16 + 8 * ((g ^ (c >> 1)) & 1);
    pv[5] = voff;
    pv[6] = wp;
    pv[7] = sb + C_SO + (l >> 5) * 1024 + w * 256 + (l & 31) * 8;
    const int q = (l >> 2) & 3, p = l & 3;
    const int f = (q & 1) | ((g & 1) << 1) | ((q >> 1) << 2);
#pragma unroll
    for (int i = 0; i < 4; ++i) pw[i] = (4 * i + g) * (HD * 2) + ((c ^ (((g & 1) | (i << 1)) << 1)) << 4) - 1024 * i;
    pw[4] = sb + (g >> 1) * 16384 + (4 * (g & 1) + (q & 1) + 8 * (q >> 1)) * 256 + (w ^ f) * 32 + p * 8;
    pw[5] = sb + (4 * (g >> 1) + 2 * (q >> 1) + (g & 1)) * 16384 + (q & 1) * 1024 + p * 8 + ((4 * wb) ^ f ^ tb) * 32;
    pw[6] = pw[7] = 0u;
  }
  const size_t wo = (size_t)(16 * w) * HD + h * DH;
#define POL 2
#define PG(w) (POL == 2 ? (w) * 4096 : ((w) & 1) * 4096 + ((w) >> 1) * 16384)
  const v4u rsA = mk_srd(ws + OFF_ACC + L * SZ_ACC + (POL == 1 ? 8192 : 0), SZ_ACC - 8192);
  const v4u rsF = mk_srd(FQ, 64 * 32 * 32 * 4);
  const v4u rsMK = mk_srd(ws + OFF_MEMK + L * SZ_MEM, SZ_MEM);
  const v4u rsMV = mk_srd(ws + OFF_MEMV + L * SZ_MEM, SZ_MEM);
  const v4u pAB = (v4u{0u, 2048u, 3072u, 1024u} ^ (1536u * tb)) + PG(wb);
  const v4u rsT = mk_srd(FL + FL_FK + L * 32, 64 * 1024 - 128);
  unsigned sc[8];
  sc[0] = h * 128;
  sc[1] = (h * 4 + w - 4) * 512;
  sc[2] = (h * 4 + w - 4) * 512;
  sc[3] = NSEG - 1;
  sc[5] = w;
  sc[6] = sb;
  const char* dbg = ws;
#define CHAIN_OPS                                                                                                          \
  ::"{v[0:7]}"(pv), "{v[24:31]}"(pw), "{s[4:5]}"(A.Wv + wo), "{s[6:7]}"(A.Wq + wo), "{s[8:9]}"(A.Wk + wo), "{s[10:11]}"(tag),         \
      "{s[12:13]}"(ws + OFF_RD + (blockIdx.x & 7) * 512), "{s[14:15]}"(ws + OFF_G + (blockIdx.x & 7) * 256), "{s[16:17]}"(ws + OFF_G + h * 8), \
      "{s18}"(w == 4 && L == 0 ? 255 : 0), "{s19}"(sb + w * 16384), "{s[20:21]}"(ws + WS_TOTAL + (w == 0 ? 534 * 256 + A.vb : 610 * 256) * 8), \
      "{s[40:43]}"(rsA), "{s[44:47]}"(rsF), "{s[48:51]}"(rsMK), "{s[52:55]}"(rsMV), "{s[56:59]}"(pAB), "{s60}"(PG(w)), "{s61}"(ATT ? PG(h >> 2) + (h & 3) * 1024 : L), \
      "{s[62:63]}"(gK), "{s[64:65]}"(gV), "{s66}"(0), "{s67}"(w == 4 ? (h >> 3) * 256 + (h & 7) * 8 : 0x40000000), "{s68}"(sc[0]), "{s69}"(sc[1]), "{s70}"(sc[2]),    \
      "{s71}"(sc[3]), "{s73}"(sc[5]), "{s74}"(sc[6]), "{s[36:37]}"(dbg),                   \
      "{s[0:3]}"(rsT)                                                                                                      \
      : "memory", "vcc", "scc", "s99", "v255"
  if (ATT) asm volatile("s_mov_b64 exec, -1\ns_nop 4\n" CHAIN_ASM_ATT CHAIN_OPS);
  else asm volatile("s_mov_b64 exec, -1\ns_nop 4\n" CHAIN_ASM_VO CHAIN_OPS);
#undef CHAIN_OPS
}

__device__ __forceinline__ void role_chain(const MegaArgs& A, char* smem, const int L, const int h, const int rep) {
  if (A.wv < 4) role_chain_t<true>(A, smem, L, h, rep);
  else role_chain_t<false>(A, smem, L, h, rep);
}

constexpr int AW_SO = 0, AW_TK = 32768, AW_CNT = AW_TK + 98304;
__device__ __forceinline__ unsigned umin3(unsigned a, unsigned b, unsigned c) {
  return __builtin_elementwise_min(__builtin_elementwise_min(a, b), c);
}
#define AW_ASM_X \
  "s_mov_b32 s61, 0\n" \
  "s_mov_b32 s67, 1\n" \
  "s_mov_b32 s68, 0\n" \
  "s_mov_b32 s70, 0\n" \
  "s_mov_b32 s73, 0\n" \
  "s_mov_b32 s71, 0x3e0293ee\n" \
  "s_mov_b32 s72, 0xbe0293ee\n" \
  "s_mov_b32 s65, 49152\n" \
  "s_bitcmp0_b32 s58, 2\n" \
  "s_cbranch_scc1 .LCX_OWN_%=\n" \
  "v_swap_b32 v192, v208\n" \
  "v_swap_b32 v193, v209\n" \
  "v_swap_b32 v194, v210\n" \
  "v_swap_b32 v195, v211\n" \
  "v_swap_b32 v196, v212\n" \
  "v_swap_b32 v197, v213\n" \
  "v_swap_b32 v198, v214\n" \
  "v_swap_b32 v199, v215\n" \
  "v_swap_b32 v200, v216\n" \
  "v_swap_b32 v201, v217\n" \
  "v_swap_b32 v202, v218\n" \
  "v_swap_b32 v203, v219\n" \
  "v_swap_b32 v204, v220\n" \
  "v_swap_b32 v205, v221\n" \
  "v_swap_b32 v206, v222\n" \
  "v_swap_b32 v207, v223\n" \
  "v_swap_b32 v224, v240\n" \
  "v_swap_b32 v225, v241\n" \
  "v_swap_b32 v226, v242\n" \
  "v_swap_b32 v227, v243\n" \
  "v_swap_b32 v228, v244\n" \
  "v_swap_b32 v229, v245\n" \
  "v_swap_b32 v230, v246\n" \
  "v_swap_b32 v231, v247\n" \
  "v_swap_b32 v232, v248\n" \
  "v_swap_b32 v233, v249\n" \
  "v_swap_b32 v234, v250\n" \
  "v_swap_b32 v235, v251\n" \
  "v_swap_b32 v236, v252\n" \
  "v_swap_b32 v237, v253\n" \
  "v_swap_b32 v238, v254\n" \
  "v_swap_b32 v239, v255\n" \
  ".LCX_OWN_%=:\n" \
  "v_mov_b32 v38, s58\n" \
  "s_memrealtime s[38:39]\n" \
  "s_waitcnt lgkmcnt(0)\n" \
  "s_mov_b32 s69, s38\n" \
  "s_setprio 3\n" \
  ".p2align 6\n" \
  ".LCX_TOP_%=:\n" \
  "s_mov_b32 s62, 0x200000\n" \
  "s_cmp_ge_u32 s73, s67\n" \
  "s_cbranch_scc1 .LCX_TOK_%=\n" \
  "s_cmp_eq_u32 s61, 0\n" \
  "s_cbranch_scc1 .LCX_SPIN_%=\n" \
  ".LCX_RE_%=:\n" \
  "ds_read_b32 v37, v38\n" \
  "s_add_u32 s62, s62, 1\n" \
  "s_waitcnt lgkmcnt(0)\n" \
  "v_readfirstlane_b32 s73, v37\n" \
  "s_bfe_u32 s64, s73, 0x80008\n" \
  "s_and_b32 s73, s73, 0xff\n" \
  "s_min_u32 s73, s73, s64\n" \
  "s_cmp_ge_u32 s73, s67\n" \
  "s_cbranch_scc1 .LCX_TOK_%=\n" \
  "s_sleep 1\n" \
  "s_cmp_lt_u32 s62, 2097158\n" \
  "s_cbranch_scc1 .LCX_RE_%=\n" \
  "s_waitcnt vmcnt(0)\n" \
  "v_mov_b32 v144, s59\n" \
  "v_mov_b32 v145, 1\n" \
  "s_mov_b64 exec, 1\n" \
  "ds_add_rtn_u32 v144, v144, v145\n" \
  "s_mov_b64 exec, -1\n" \
  "s_xor_b32 s59, s59, 4\n" \
  "s_mov_b32 s61, 0\n" \
  "s_waitcnt lgkmcnt(0)\n" \
  "v_readfirstlane_b32 s63, v144\n" \
  "v_lshlrev_b32 v144, 5, v5\n" \
  "s_and_b32 s63, s63, 7\n" \
  "s_cmp_eq_u32 s63, 7\n" \
  "s_cbranch_scc0 .LCX_W1_%=\n" \
  "s_mov_b64 exec, 15\n" \
  "global_store_dword v144, v145, s[52:53] sc1\n" \
  "s_mov_b64 exec, -1\n" \
  ".LCX_W1_%=:\n" \
  "s_add_u32 s52, s52, 2048\n" \
  "s_addc_u32 s53, s53, 0\n" \
  ".LCX_SPIN_%=:\n" \
  "s_sleep 2\n" \
  "s_memrealtime s[38:39]\n" \
  "ds_read_b32 v37, v38\n" \
  "s_sub_u32 s62, s62, 1\n" \
  "s_cbranch_scc1 .LCX_END_%=\n" \
  "s_waitcnt lgkmcnt(0)\n" \
  "v_readfirstlane_b32 s73, v37\n" \
  "s_add_u32 s38, s38, 80\n" \
  "s_bfe_u32 s64, s73, 0x80008\n" \
  "s_and_b32 s73, s73, 0xff\n" \
  "s_min_u32 s73, s73, s64\n" \
  "s_cmp_ge_u32 s73, s67\n" \
  "s_cbranch_scc0 .LCX_SPIN_%=\n" \
  "s_cmp_eq_u32 s70, 0\n" \
  "s_cbranch_scc1 .LCX_TOK_%=\n" \
  "s_sub_u32 s63, s38, s69\n" \
  "s_max_i32 s63, s63, 0\n" \
  "s_add_u32 s69, s69, s63\n" \
  ".LCX_TOK_%=:\n" \
  "ds_read_b128 v[48:51], v6 offset:0\n" \
  "ds_read_b128 v[64:67], v0 offset:8192\n" \
  "ds_read_b128 v[80:83], v0 offset:12288\n" \
  "ds_read_b128 v[52:55], v6 offset:1024\n" \
  "ds_read_b128 v[68:71], v0 offset:9216\n" \
  "ds_read_b128 v[84:87], v0 offset:13312\n" \
  "ds_read_b128 v[56:59], v6 offset:2048\n" \
  "ds_read_b128 v[72:75], v0 offset:10240\n" \
  "ds_read_b128 v[88:91], v0 offset:14336\n" \
  "ds_read_b128 v[60:63], v6 offset:3072\n" \
  "ds_read_b128 v[76:79], v0 offset:11264\n" \
  "ds_read_b128 v[92:95], v0 offset:15360\n" \
  "s_waitcnt lgkmcnt(9)\n" \
  "v_mfma_f32_16x16x32_bf16 v[20:23], v[64:67], v[48:51], v[8:11]\n" \
  "v_mfma_f32_16x16x32_bf16 v[24:27], v[80:83], v[48:51], v[12:15]\n" \
  "ds_read_b128 v[96:99], v0 offset:16384\n" \
  "ds_read_b128 v[100:103], v0 offset:17408\n" \
  "s_waitcnt lgkmcnt(8)\n" \
  "v_mfma_f32_16x16x32_bf16 v[20:23], v[68:71], v[52:55], v[20:23]\n" \
  "v_mfma_f32_16x16x32_bf16 v[24:27], v[84:87], v[52:55], v[24:27]\n" \
  "ds_read_b128 v[104:107], v0 offset:18432\n" \
  "ds_read_b128 v[108:111], v0 offset:19456\n" \
  "s_waitcnt lgkmcnt(7)\n" \
  "v_mfma_f32_16x16x32_bf16 v[20:23], v[72:75], v[56:59], v[20:23]\n" \
  "v_mfma_f32_16x16x32_bf16 v[24:27], v[88:91], v[56:59], v[24:27]\n" \
  "ds_read_b128 v[112:115], v0 offset:20480\n" \
  "ds_read_b128 v[116:119], v0 offset:21504\n" \
  "s_waitcnt lgkmcnt(6)\n" \
  "v_mfma_f32_16x16x32_bf16 v[20:23], v[76:79], v[60:63], v[20:23]\n" \
  "v_mfma_f32_16x16x32_bf16 v[24:27], v[92:95], v[60:63], v[24:27]\n" \
  "ds_read_b128 v[120:123], v0 offset:22528\n" \
  "ds_read_b128 v[124:127], v0 offset:23552\n" \
  "s_add_u32 s67, s67, 1\n" \
  "v_add_u32 v0, s65, v0\n" \
  "v_add_u32 v6, s65, v6\n" \
  "s_sub_u32 s65, 0, s65\n" \
  "s_nop 1\n" \
  "v_max3_f32 v32, v20, v21, v22\n" \
  "v_max3_f32 v33, v23, v24, v25\n" \
  "v_max3_f32 v32, v32, v26, v27\n" \
  "v_max_f32 v32, v32, v33\n" \
  "v_mov_b32 v33, v32\n" \
  "s_nop 1\n" \
  "v_permlane16_swap_b32 v32, v33\n" \
  "v_max_f32 v32, v32, v33\n" \
  "v_mov_b32 v33, v32\n" \
  "s_nop 1\n" \
  "v_permlane32_swap_b32 v32, v33\n" \
  "v_max_f32 v32, v32, v33\n" \
  "v_mul_f32 v33, s72, v32\n" \
  "v_fma_f32 v20, v20, s71, v33\n" \
  "v_fma_f32 v21, v21, s71, v33\n" \
  "v_fma_f32 v22, v22, s71, v33\n" \
  "v_fma_f32 v23, v23, s71, v33\n" \
  "v_fma_f32 v24, v24, s71, v33\n" \
  "v_fma_f32 v25, v25, s71, v33\n" \
  "v_fma_f32 v26, v26, s71, v33\n" \
  "v_fma_f32 v27, v27, s71, v33\n" \
  "v_exp_f32 v20, v20\n" \
  "v_exp_f32 v21, v21\n" \
  "v_exp_f32 v22, v22\n" \
  "v_exp_f32 v23, v23\n" \
  "v_exp_f32 v24, v24\n" \
  "v_exp_f32 v25, v25\n" \
  "v_exp_f32 v26, v26\n" \
  "v_exp_f32 v27, v27\n" \
  "v_add_f32 v34, v20, v21\n" \
  "v_add_f32 v33, v22, v23\n" \
  "v_add_f32 v36, v24, v25\n" \
  "v_add_f32 v34, v34, v33\n" \
  "v_add_f32 v33, v26, v27\n" \
  "v_add_f32 v34, v34, v36\n" \
  "v_add_f32 v34, v34, v33\n" \
  "v_mov_b64 v[28:29], 0\n" \
  "v_mov_b64 v[30:31], 0\n" \
  "s_waitcnt vmcnt(0) lgkmcnt(0)\n" \
  "s_cmp_eq_u32 s61, 0\n" \
  "s_cbranch_scc1 .LCX_NP_%=\n" \
  "v_mov_b32 v144, s59\n" \
  "v_mov_b32 v145, 1\n" \
  "s_mov_b64 exec, 1\n" \
  "ds_add_rtn_u32 v144, v144, v145\n" \
  "s_mov_b64 exec, -1\n" \
  "s_xor_b32 s59, s59, 4\n" \
  "s_cmp_lg_u32 s68, 0\n" \
  "s_cbranch_scc1 .LCX_TEST_%=\n" \
  "s_mov_b32 s61, 0\n" \
  "s_waitcnt lgkmcnt(0)\n" \
  "v_readfirstlane_b32 s63, v144\n" \
  "v_lshlrev_b32 v144, 5, v5\n" \
  "s_and_b32 s63, s63, 7\n" \
  "s_cmp_eq_u32 s63, 7\n" \
  "s_cbranch_scc0 .LCX_W2_%=\n" \
  "s_mov_b64 exec, 15\n" \
  "global_store_dword v144, v145, s[52:53] sc1\n" \
  "s_mov_b64 exec, -1\n" \
  ".LCX_W2_%=:\n" \
  "s_add_u32 s52, s52, 2048\n" \
  "s_addc_u32 s53, s53, 0\n" \
  ".LCX_NP_%=:\n" \
  "s_cmp_lg_u32 s68, 0\n" \
  "s_cbranch_scc1 .LCX_TEST_%=\n" \
  ".LCX_WF_%=:\n" \
  "s_memrealtime s[38:39]\n" \
  "s_waitcnt lgkmcnt(0)\n" \
  "s_sub_u32 s63, s69, s38\n" \
  "s_cmp_le_i32 s63, 0\n" \
  "s_cbranch_scc1 .LCX_GO_%=\n" \
  "s_cmp_lt_i32 s63, 12\n" \
  "s_cbranch_scc1 .LCX_WF_%=\n" \
  "s_sleep 3\n" \
  "s_branch .LCX_WF_%=\n" \
  ".LCX_GO_%=:\n" \
  "v_mov_b32 v35, 0\n" \
  "global_load_dwordx4 v[128:131], v5, s[48:49] offset:0 sc1\n" \
  "global_load_dwordx4 v[132:135], v5, s[48:49] offset:1024 sc1\n" \
  "global_load_dwordx4 v[136:139], v3, s[48:49] offset:0 sc1\n" \
  "global_load_dwordx4 v[140:143], v3, s[48:49] offset:512 sc1\n" \
  "global_load_dword v35, v35, s[50:51] sc1\n" \
  "s_sub_u32 s62, s62, 8\n" \
  "s_cbranch_scc1 .LCX_END_%=\n" \
  "s_waitcnt vmcnt(0)\n" \
  ".LCX_TEST_%=:\n" \
  "v_smfmac_f32_16x16x64_bf16 v[28:31], v[128:131], v[48:55], v1\n" \
  "v_smfmac_f32_16x16x64_bf16 v[28:31], v[132:135], v[56:63], v1\n" \
  "v_min3_u32 v33, v128, v129, v130\n" \
  "v_min3_u32 v36, v131, v132, v133\n" \
  "v_min3_u32 v33, v33, v134, v135\n" \
  "v_min3_u32 v36, v36, v136, v137\n" \
  "v_min3_u32 v33, v33, v138, v139\n" \
  "v_min3_u32 v36, v36, v140, v141\n" \
  "v_min3_u32 v33, v33, v142, v143\n" \
  "v_min_u32 v33, v33, v36\n" \
  "v_cmp_eq_u32 vcc, 0, v33\n" \
  "v_readfirstlane_b32 s64, v35\n" \
  "s_mov_b32 s68, 0\n" \
  "s_cmp_eq_u64 vcc, 0\n" \
  "s_cbranch_scc0 .LCX_FAIL_%=\n" \
  "v_permlane32_swap_b32 v28, v30\n" \
  "v_permlane32_swap_b32 v29, v31\n" \
  "v_mov_b32 v33, s67\n" \
  "ds_write_b32 v38, v33 offset:16\n" \
  "v_add_f32 v28, v28, v30\n" \
  "v_add_f32 v29, v29, v31\n" \
  "v_max3_f32 v36, v28, v29, v32\n" \
  "v_mov_b32 v33, v36\n" \
  "s_cmp_eq_u32 s64, 0\n" \
  "s_cselect_b32 s64, s38, s64\n" \
  "v_permlane16_swap_b32 v36, v33\n" \
  "v_max_f32 v36, v36, v33\n" \
  "v_mov_b32 v33, v36\n" \
  "s_sub_u32 s63, s64, s70\n" \
  "s_mov_b32 s70, s64\n" \
  "v_permlane32_swap_b32 v36, v33\n" \
  "v_max_f32 v36, v36, v33\n" \
  "v_mul_f32 v36, s72, v36\n" \
  "v_fma_f32 v32, v32, s71, v36\n" \
  "v_fma_f32 v28, v28, s71, v36\n" \
  "v_fma_f32 v29, v29, s71, v36\n" \
  "v_exp_f32 v32, v32\n" \
  "v_exp_f32 v28, v28\n" \
  "v_exp_f32 v29, v29\n" \
  "v_fma_f32 v34, v34, v32, v28\n" \
  "v_add_f32 v34, v34, v29\n" \
  "v_mov_b32 v33, v34\n" \
  "s_min_u32 s63, s63, 250\n" \
  "s_add_u32 s69, s64, s63\n" \
  "v_permlane16_swap_b32 v34, v33\n" \
  "v_add_f32 v34, v34, v33\n" \
  "v_mov_b32 v33, v34\n" \
  "s_add_u32 s69, s69, -48\n" \
  "s_add_u32 s48, s48, 0x10000\n" \
  "s_addc_u32 s49, s49, 0\n" \
  "v_permlane32_swap_b32 v34, v33\n" \
  "v_add_f32 v34, v34, v33\n" \
  "v_rcp_f32 v34, v34\n" \
  "s_add_u32 s50, s50, 1024\n" \
  "s_addc_u32 s51, s51, 0\n" \
  "v_mul_f32 v32, v32, v34\n" \
  "v_mul_f32 v20, v20, v32\n" \
  "v_mul_f32 v21, v21, v32\n" \
  "v_mul_f32 v22, v22, v32\n" \
  "v_mul_f32 v23, v23, v32\n" \
  "v_mul_f32 v24, v24, v32\n" \
  "v_mul_f32 v25, v25, v32\n" \
  "v_mul_f32 v26, v26, v32\n" \
  "v_mul_f32 v27, v27, v32\n" \
  "v_cvt_pk_bf16_f32 v40, v20, v21\n" \
  "v_cvt_pk_bf16_f32 v41, v22, v23\n" \
  "v_cvt_pk_bf16_f32 v42, v24, v25\n" \
  "v_cvt_pk_bf16_f32 v43, v26, v27\n" \
  "v_mul_f32 v28, v28, v34\n" \
  "v_mul_f32 v29, v29, v34\n" \
  "v_mfma_f32_16x16x32_bf16 v[64:67], v[96:99], v[40:43], 0\n" \
  "v_cvt_pk_bf16_f32 v44, v28, v29\n" \
  "v_mov_b32 v45, 0\n" \
  "v_mfma_f32_16x16x32_bf16 v[68:71], v[100:103], v[40:43], 0\n" \
  "v_cvt_pk_bf16_f32 v47, v28, v29\n" \
  "v_mov_b32 v46, 0\n" \
  "v_mfma_f32_16x16x32_bf16 v[72:75], v[104:107], v[40:43], 0\n" \
  "v_permlane32_swap_b32 v44, v45\n" \
  "v_mfma_f32_16x16x32_bf16 v[76:79], v[108:111], v[40:43], 0\n" \
  "v_permlane32_swap_b32 v46, v47\n" \
  "v_mfma_f32_16x16x32_bf16 v[80:83], v[112:115], v[40:43], 0\n" \
  "v_xor_b32 v16, 4096, v7\n" \
  "v_mfma_f32_16x16x32_bf16 v[84:87], v[116:119], v[40:43], 0\n" \
  "v_xor_b32 v17, 8192, v7\n" \
  "v_mfma_f32_16x16x32_bf16 v[88:91], v[120:123], v[40:43], 0\n" \
  "v_xor_b32 v18, 12288, v7\n" \
  "v_mfma_f32_16x16x32_bf16 v[92:95], v[124:127], v[40:43], 0\n" \
  "s_cmp_eq_u32 s61, 0\n" \
  "s_cbranch_scc1 .LCX_NQ_%=\n" \
  "s_mov_b32 s61, 0\n" \
  "s_waitcnt lgkmcnt(0)\n" \
  "v_readfirstlane_b32 s63, v144\n" \
  "v_lshlrev_b32 v144, 5, v5\n" \
  "s_and_b32 s63, s63, 7\n" \
  "s_cmp_eq_u32 s63, 7\n" \
  "s_cbranch_scc0 .LCX_W3_%=\n" \
  "s_mov_b64 exec, 15\n" \
  "global_store_dword v144, v145, s[52:53] sc1\n" \
  "s_mov_b64 exec, -1\n" \
  ".LCX_W3_%=:\n" \
  "s_add_u32 s52, s52, 2048\n" \
  "s_addc_u32 s53, s53, 0\n" \
  ".LCX_NQ_%=:\n" \
  "s_memrealtime s[38:39]\n" \
  "ds_read_b32 v37, v38\n" \
  "v_mfma_f32_16x16x16_bf16 v[64:67], v[136:137], v[44:45], v[64:67]\n" \
  "v_mfma_f32_16x16x16_bf16 v[68:71], v[140:141], v[44:45], v[68:71]\n" \
  "v_mfma_f32_16x16x16_bf16 v[72:75], v[136:137], v[46:47], v[72:75]\n" \
  "v_mfma_f32_16x16x16_bf16 v[76:79], v[140:141], v[46:47], v[76:79]\n" \
  "v_mfma_f32_16x16x16_bf16 v[80:83], v[138:139], v[44:45], v[80:83]\n" \
  "v_mfma_f32_16x16x16_bf16 v[84:87], v[142:143], v[44:45], v[84:87]\n" \
  "v_mfma_f32_16x16x16_bf16 v[88:91], v[138:139], v[46:47], v[88:91]\n" \
  "v_mfma_f32_16x16x16_bf16 v[92:95], v[142:143], v[46:47], v[92:95]\n" \
  "s_nop 0\n" \
  "v_cvt_pk_bf16_f32 v160, v64, v65\n" \
  "v_cvt_pk_bf16_f32 v161, v66, v67\n" \
  "v_cvt_pk_bf16_f32 v162, v68, v69\n" \
  "v_cvt_pk_bf16_f32 v163, v70, v71\n" \
  "ds_write_b128 v7, v[160:163] offset:0\n" \
  "v_cvt_pk_bf16_f32 v164, v72, v73\n" \
  "v_cvt_pk_bf16_f32 v165, v74, v75\n" \
  "v_cvt_pk_bf16_f32 v166, v76, v77\n" \
  "v_cvt_pk_bf16_f32 v167, v78, v79\n" \
  "ds_write_b128 v7, v[164:167] offset:1024\n" \
  "v_cvt_pk_bf16_f32 v168, v80, v81\n" \
  "v_cvt_pk_bf16_f32 v169, v82, v83\n" \
  "v_cvt_pk_bf16_f32 v170, v84, v85\n" \
  "v_cvt_pk_bf16_f32 v171, v86, v87\n" \
  "ds_write_b128 v7, v[168:171] offset:2048\n" \
  "v_cvt_pk_bf16_f32 v172, v88, v89\n" \
  "v_cvt_pk_bf16_f32 v173, v90, v91\n" \
  "v_cvt_pk_bf16_f32 v174, v92, v93\n" \
  "v_cvt_pk_bf16_f32 v175, v94, v95\n" \
  "ds_write_b128 v7, v[172:175] offset:3072\n" \
  "v_xor_b32 v7, 0x4000, v7\n" \
  "v_mfma_f32_16x16x32_bf16 v[112:115], v[192:195], v[160:163], 0\n" \
  "v_mfma_f32_16x16x32_bf16 v[116:119], v[224:227], v[160:163], 0\n" \
  "s_waitcnt lgkmcnt(0)\n" \
  "s_barrier\n" \
  "ds_read_b128 v[64:67], v16 offset:0\n" \
  "ds_read_b128 v[68:71], v16 offset:1024\n" \
  "ds_read_b128 v[72:75], v16 offset:2048\n" \
  "ds_read_b128 v[76:79], v16 offset:3072\n" \
  "ds_read_b128 v[80:83], v17 offset:0\n" \
  "ds_read_b128 v[84:87], v17 offset:1024\n" \
  "ds_read_b128 v[88:91], v17 offset:2048\n" \
  "ds_read_b128 v[92:95], v17 offset:3072\n" \
  "ds_read_b128 v[96:99], v18 offset:0\n" \
  "ds_read_b128 v[100:103], v18 offset:1024\n" \
  "ds_read_b128 v[104:107], v18 offset:2048\n" \
  "ds_read_b128 v[108:111], v18 offset:3072\n" \
  "v_readfirstlane_b32 s73, v37\n" \
  "s_sub_u32 s63, s38, s69\n" \
  "s_bfe_u32 s64, s73, 0x80010\n" \
  "s_sub_u32 s64, s64, s67\n" \
  "s_or_b32 s63, s63, s64\n" \
  "s_bfe_u32 s64, s73, 0x80008\n" \
  "s_and_b32 s73, s73, 0xff\n" \
  "s_min_u32 s73, s73, s64\n" \
  "s_or_b32 s63, s63, s68\n" \
  "s_cmp_lt_i32 s63, 0\n" \
  "s_cbranch_scc1 .LCX_E4_%=\n" \
  "v_mov_b32 v35, 0\n" \
  "global_load_dwordx4 v[128:131], v5, s[48:49] offset:0 sc1\n" \
  "global_load_dwordx4 v[132:135], v5, s[48:49] offset:1024 sc1\n" \
  "global_load_dwordx4 v[136:139], v3, s[48:49] offset:0 sc1\n" \
  "global_load_dwordx4 v[140:143], v3, s[48:49] offset:512 sc1\n" \
  "global_load_dword v35, v35, s[50:51] sc1\n" \
  "v_mov_b32 v33, s67\n" \
  "ds_write_b32 v38, v33 offset:8\n" \
  "s_mov_b32 s68, -1\n" \
  ".LCX_E4_%=:\n" \
  "v_mfma_f32_16x16x32_bf16 v[112:115], v[196:199], v[164:167], v[112:115]\n" \
  "v_mfma_f32_16x16x32_bf16 v[116:119], v[228:231], v[164:167], v[116:119]\n" \
  "v_mfma_f32_16x16x32_bf16 v[112:115], v[200:203], v[168:171], v[112:115]\n" \
  "v_mfma_f32_16x16x32_bf16 v[116:119], v[232:235], v[168:171], v[116:119]\n" \
  "v_mfma_f32_16x16x32_bf16 v[112:115], v[204:207], v[172:175], v[112:115]\n" \
  "v_mfma_f32_16x16x32_bf16 v[116:119], v[236:239], v[172:175], v[116:119]\n" \
  "s_waitcnt lgkmcnt(11)\n" \
  "v_mfma_f32_16x16x32_bf16 v[120:123], v[192:195], v[64:67], 0\n" \
  "v_mfma_f32_16x16x32_bf16 v[124:127], v[224:227], v[64:67], 0\n" \
  "s_waitcnt lgkmcnt(10)\n" \
  "v_mfma_f32_16x16x32_bf16 v[120:123], v[196:199], v[68:71], v[120:123]\n" \
  "v_mfma_f32_16x16x32_bf16 v[124:127], v[228:231], v[68:71], v[124:127]\n" \
  "s_waitcnt lgkmcnt(9)\n" \
  "v_mfma_f32_16x16x32_bf16 v[120:123], v[200:203], v[72:75], v[120:123]\n" \
  "v_mfma_f32_16x16x32_bf16 v[124:127], v[232:235], v[72:75], v[124:127]\n" \
  "s_waitcnt lgkmcnt(8)\n" \
  "v_mfma_f32_16x16x32_bf16 v[120:123], v[204:207], v[76:79], v[120:123]\n" \
  "v_mfma_f32_16x16x32_bf16 v[124:127], v[236:239], v[76:79], v[124:127]\n" \
  "s_waitcnt lgkmcnt(7)\n" \
  "v_mfma_f32_16x16x32_bf16 v[112:115], v[208:211], v[80:83], v[112:115]\n" \
  "v_mfma_f32_16x16x32_bf16 v[116:119], v[240:243], v[80:83], v[116:119]\n" \
  "s_waitcnt lgkmcnt(6)\n" \
  "v_mfma_f32_16x16x32_bf16 v[112:115], v[212:215], v[84:87], v[112:115]\n" \
  "v_mfma_f32_16x16x32_bf16 v[116:119], v[244:247], v[84:87], v[116:119]\n" \
  "s_waitcnt lgkmcnt(5)\n" \
  "v_mfma_f32_16x16x32_bf16 v[112:115], v[216:219], v[88:91], v[112:115]\n" \
  "v_mfma_f32_16x16x32_bf16 v[116:119], v[248:251], v[88:91], v[116:119]\n" \
  "s_waitcnt lgkmcnt(4)\n" \
  "v_mfma_f32_16x16x32_bf16 v[112:115], v[220:223], v[92:95], v[112:115]\n" \
  "v_mfma_f32_16x16x32_bf16 v[116:119], v[252:255], v[92:95], v[116:119]\n" \
  "s_memrealtime s[38:39]\n" \
  "ds_read_b32 v37, v38\n" \
  "s_waitcnt lgkmcnt(4)\n" \
  "v_mfma_f32_16x16x32_bf16 v[120:123], v[208:211], v[96:99], v[120:123]\n" \
  "v_mfma_f32_16x16x32_bf16 v[124:127], v[240:243], v[96:99], v[124:127]\n" \
  "s_waitcnt lgkmcnt(3)\n" \
  "v_mfma_f32_16x16x32_bf16 v[120:123], v[212:215], v[100:103], v[120:123]\n" \
  "v_mfma_f32_16x16x32_bf16 v[124:127], v[244:247], v[100:103], v[124:127]\n" \
  "v_cvt_pk_bf16_f32 v20, v112, v113\n" \
  "v_cvt_pk_bf16_f32 v21, v114, v115\n" \
  "v_cvt_pk_bf16_f32 v22, v116, v117\n" \
  "v_cvt_pk_bf16_f32 v23, v118, v119\n" \
  "s_waitcnt lgkmcnt(2)\n" \
  "v_mfma_f32_16x16x32_bf16 v[120:123], v[216:219], v[104:107], v[120:123]\n" \
  "v_mfma_f32_16x16x32_bf16 v[124:127], v[248:251], v[104:107], v[124:127]\n" \
  "buffer_store_dwordx4 v[20:23], v4, s[40:43], s54 offen sc1\n" \
  "s_waitcnt lgkmcnt(0)\n" \
  "v_mfma_f32_16x16x32_bf16 v[120:123], v[220:223], v[108:111], v[120:123]\n" \
  "v_mfma_f32_16x16x32_bf16 v[124:127], v[252:255], v[108:111], v[124:127]\n" \
  "s_xor_b32 s64, s54, 16384\n" \
  "s_mov_b32 s61, 1\n" \
  "s_nop 3\n" \
  "v_cvt_pk_bf16_f32 v24, v120, v121\n" \
  "v_cvt_pk_bf16_f32 v25, v122, v123\n" \
  "v_cvt_pk_bf16_f32 v26, v124, v125\n" \
  "v_cvt_pk_bf16_f32 v27, v126, v127\n" \
  "buffer_store_dwordx4 v[24:27], v4, s[40:43], s64 offen sc1\n" \
  "v_readfirstlane_b32 s73, v37\n" \
  "s_sub_u32 s63, s38, s69\n" \
  "s_bfe_u32 s64, s73, 0x80010\n" \
  "s_sub_u32 s64, s64, s67\n" \
  "s_or_b32 s63, s63, s64\n" \
  "s_bfe_u32 s64, s73, 0x80008\n" \
  "s_and_b32 s73, s73, 0xff\n" \
  "s_min_u32 s73, s73, s64\n" \
  "s_or_b32 s63, s63, s68\n" \
  "s_cmp_lt_i32 s63, 0\n" \
  "s_cbranch_scc1 .LCX_E5_%=\n" \
  "v_mov_b32 v35, 0\n" \
  "global_load_dwordx4 v[128:131], v5, s[48:49] offset:0 sc1\n" \
  "global_load_dwordx4 v[132:135], v5, s[48:49] offset:1024 sc1\n" \
  "global_load_dwordx4 v[136:139], v3, s[48:49] offset:0 sc1\n" \
  "global_load_dwordx4 v[140:143], v3, s[48:49] offset:512 sc1\n" \
  "global_load_dword v35, v35, s[50:51] sc1\n" \
  "v_mov_b32 v33, s67\n" \
  "ds_write_b32 v38, v33 offset:8\n" \
  "s_mov_b32 s68, -1\n" \
  ".LCX_E5_%=:\n" \
  "s_add_u32 s54, s54, 0x80000\n" \
  "s_sub_u32 s60, s60, 1\n" \
  "s_cmp_lg_u32 s60, 0\n" \
  "s_cbranch_scc1 .LCX_TOP_%=\n" \
  "s_waitcnt vmcnt(0)\n" \
  "v_mov_b32 v144, s59\n" \
  "v_mov_b32 v145, 1\n" \
  "s_mov_b64 exec, 1\n" \
  "ds_add_rtn_u32 v144, v144, v145\n" \
  "s_mov_b64 exec, -1\n" \
  "s_xor_b32 s59, s59, 4\n" \
  "s_mov_b32 s61, 0\n" \
  "s_waitcnt lgkmcnt(0)\n" \
  "v_readfirstlane_b32 s63, v144\n" \
  "v_lshlrev_b32 v144, 5, v5\n" \
  "s_and_b32 s63, s63, 7\n" \
  "s_cmp_eq_u32 s63, 7\n" \
  "s_cbranch_scc0 .LCX_W6_%=\n" \
  "s_mov_b64 exec, 15\n" \
  "global_store_dword v144, v145, s[52:53] sc1\n" \
  "s_mov_b64 exec, -1\n" \
  ".LCX_W6_%=:\n" \
  "s_add_u32 s52, s52, 2048\n" \
  "s_addc_u32 s53, s53, 0\n" \
  ".LCX_END_%=:\n" \
  "s_endpgm\n" \
  ".LCX_FAIL_%=:\n" \
  "v_mov_b64 v[28:29], 0\n" \
  "v_mov_b64 v[30:31], 0\n" \
  "s_cmp_eq_u32 s61, 0\n" \
  "s_cbranch_scc1 .LCX_FAIL2_%=\n" \
  "s_mov_b32 s61, 0\n" \
  "s_waitcnt lgkmcnt(0)\n" \
  "v_readfirstlane_b32 s63, v144\n" \
  "v_lshlrev_b32 v144, 5, v5\n" \
  "s_and_b32 s63, s63, 7\n" \
  "s_cmp_eq_u32 s63, 7\n" \
  "s_cbranch_scc0 .LCX_W7_%=\n" \
  "s_mov_b64 exec, 15\n" \
  "global_store_dword v144, v145, s[52:53] sc1\n" \
  "s_mov_b64 exec, -1\n" \
  ".LCX_W7_%=:\n" \
  "s_add_u32 s52, s52, 2048\n" \
  "s_addc_u32 s53, s53, 0\n" \
  ".LCX_FAIL2_%=:\n" \
  "s_add_u32 s69, s64, -48\n" \
  "s_cmp_lg_u32 s64, 0\n" \
  "s_cbranch_scc1 .LCX_WF_%=\n" \
  "s_sleep 4\n" \
  "global_load_dword v35, v35, s[50:51] sc1\n" \
  "s_sub_u32 s62, s62, 8\n" \
  "s_cbranch_scc1 .LCX_END_%=\n" \
  "s_waitcnt vmcnt(0)\n" \
  "v_readfirstlane_b32 s64, v35\n" \
  "s_branch .LCX_FAIL2_%=\n"
#define AW_ASM_Y \
  "s_mov_b32 s72, 0\n" \
  "s_mov_b32 s67, 0\n" \
  "s_mov_b32 s68, 2\n" \
  "s_mov_b32 s69, 0\n" \
  "s_mov_b32 s65, 49152\n" \
  "s_mov_b32 s66, 16384\n" \
  "v_mov_b32 v5, s58\n" \
  "v_mov_b32 v60, v3\n" \
  "v_add_u32 v3, 4096, v2\n" \
  "s_mov_b32 s62, 0x40000\n" \
  "s_branch .LCY_ASK_%=\n" \
  ".p2align 6\n" \
  ".LCY_BAR_%=:\n" \
  "s_barrier\n" \
  "ds_read_b128 v[8:11], v0 offset:0\n" \
  "ds_read_b128 v[12:15], v0 offset:1024\n" \
  "ds_read_b128 v[16:19], v0 offset:2048\n" \
  "s_add_u32 s68, s68, 1\n" \
  "s_add_u32 s64, s54, 16384\n" \
  "s_cmp_eq_u32 s69, 0\n" \
  "s_cbranch_scc1 .LCY_NI_%=\n" \
  "s_waitcnt vmcnt(3)\n" \
  "s_cmp_lg_u32 s72, 0\n" \
  "s_cbranch_scc1 .LCY_NJ_%=\n" \
  "s_waitcnt vmcnt(0)\n" \
  ".LCY_NJ_%=:\n" \
  "v_mov_b32 v6, s55\n" \
  "s_mov_b64 exec, 1\n" \
  "ds_add_u32 v5, v6\n" \
  "s_mov_b64 exec, -1\n" \
  "s_mov_b32 s69, 0\n" \
  ".LCY_NI_%=:\n" \
  "v_cmp_eq_u32 vcc, 0, v4\n" \
  "s_cmp_eq_u64 vcc, 0\n" \
  "s_cbranch_scc0 .LCY_NS_%=\n" \
  "v_lshlrev_b32 v6, 16, s55\n" \
  "s_mov_b64 exec, 1\n" \
  "ds_add_u32 v5, v6\n" \
  "s_mov_b64 exec, -1\n" \
  "s_mov_b32 m0, s56\n" \
  "s_add_u32 s71, s56, 4096\n" \
  "global_load_lds_dwordx4 v2, s[48:49] offset:0 sc1\n" \
  "global_load_lds_dwordx4 v2, s[48:49] offset:1024 sc1\n" \
  "global_load_lds_dwordx4 v2, s[48:49] offset:2048 sc1\n" \
  "global_load_lds_dwordx4 v2, s[48:49] offset:3072 sc1\n" \
  "s_mov_b32 m0, s71\n" \
  "s_add_u32 s56, s56, s65\n" \
  "global_load_lds_dwordx4 v3, s[48:49] offset:0 sc1\n" \
  "global_load_lds_dwordx4 v3, s[48:49] offset:1024 sc1\n" \
  ".LCY_NS_%=:\n" \
  "s_cmp_eq_u32 s72, 0\n" \
  "s_cbranch_scc1 .LCY_NK_%=\n" \
  "s_waitcnt vmcnt(6)\n" \
  "s_mov_b32 s72, 0\n" \
  "s_cbranch_vccz .LCY_NL_%=\n" \
  "s_waitcnt vmcnt(0)\n" \
  ".LCY_NL_%=:\n" \
  "v_mov_b32 v6, s59\n" \
  "v_mov_b32 v7, 1\n" \
  "s_mov_b64 exec, 1\n" \
  "ds_add_rtn_u32 v6, v6, v7\n" \
  "s_mov_b64 exec, -1\n" \
  "s_xor_b32 s59, s59, 4\n" \
  "s_mov_b32 s61, 0\n" \
  "s_waitcnt lgkmcnt(0)\n" \
  "v_readfirstlane_b32 s63, v6\n" \
  "v_lshlrev_b32 v6, 5, v2\n" \
  "s_and_b32 s63, s63, 7\n" \
  "s_cmp_eq_u32 s63, 7\n" \
  "s_cbranch_scc0 .LCY_W1_%=\n" \
  "s_mov_b64 exec, 15\n" \
  "global_store_dword v6, v7, s[52:53] sc1\n" \
  "s_mov_b64 exec, -1\n" \
  ".LCY_W1_%=:\n" \
  "s_add_u32 s52, s52, 2048\n" \
  "s_addc_u32 s53, s53, 0\n" \
  ".LCY_NK_%=:\n" \
  "ds_read_b128 v[20:23], v0 offset:3072\n" \
  "s_waitcnt lgkmcnt(3)\n" \
  "v_mfma_f32_16x16x32_bf16 v[32:35], v[64:67], v[8:11], 0\n" \
  "v_mfma_f32_16x16x32_bf16 v[36:39], v[96:99], v[8:11], 0\n" \
  "v_mfma_f32_16x16x32_bf16 v[40:43], v[128:131], v[8:11], 0\n" \
  "v_mfma_f32_16x16x32_bf16 v[44:47], v[160:163], v[8:11], 0\n" \
  "v_mfma_f32_16x16x32_bf16 v[48:51], v[192:195], v[8:11], 0\n" \
  "v_mfma_f32_16x16x32_bf16 v[52:55], v[224:227], v[8:11], 0\n" \
  "ds_read_b128 v[8:11], v0 offset:8192\n" \
  "s_waitcnt lgkmcnt(3)\n" \
  "v_mfma_f32_16x16x32_bf16 v[32:35], v[68:71], v[12:15], v[32:35]\n" \
  "v_mfma_f32_16x16x32_bf16 v[36:39], v[100:103], v[12:15], v[36:39]\n" \
  "v_mfma_f32_16x16x32_bf16 v[40:43], v[132:135], v[12:15], v[40:43]\n" \
  "v_mfma_f32_16x16x32_bf16 v[44:47], v[164:167], v[12:15], v[44:47]\n" \
  "v_mfma_f32_16x16x32_bf16 v[48:51], v[196:199], v[12:15], v[48:51]\n" \
  "v_mfma_f32_16x16x32_bf16 v[52:55], v[228:231], v[12:15], v[52:55]\n" \
  "s_cbranch_vccnz .LCY_D2_%=\n" \
  "global_load_lds_dwordx4 v3, s[48:49] offset:2048 sc1\n" \
  ".LCY_D2_%=:\n" \
  "ds_read_b128 v[12:15], v0 offset:9216\n" \
  "s_waitcnt lgkmcnt(3)\n" \
  "v_mfma_f32_16x16x32_bf16 v[32:35], v[72:75], v[16:19], v[32:35]\n" \
  "v_mfma_f32_16x16x32_bf16 v[36:39], v[104:107], v[16:19], v[36:39]\n" \
  "v_mfma_f32_16x16x32_bf16 v[40:43], v[136:139], v[16:19], v[40:43]\n" \
  "v_mfma_f32_16x16x32_bf16 v[44:47], v[168:171], v[16:19], v[44:47]\n" \
  "v_mfma_f32_16x16x32_bf16 v[48:51], v[200:203], v[16:19], v[48:51]\n" \
  "v_mfma_f32_16x16x32_bf16 v[52:55], v[232:235], v[16:19], v[52:55]\n" \
  "s_cbranch_vccnz .LCY_D3_%=\n" \
  "global_load_lds_dwordx4 v3, s[48:49] offset:3072 sc1\n" \
  "s_add_u32 s48, s48, 0x80000\n" \
  "s_addc_u32 s49, s49, 0\n" \
  ".LCY_D3_%=:\n" \
  "ds_read_b128 v[16:19], v0 offset:10240\n" \
  "s_waitcnt lgkmcnt(3)\n" \
  "v_mfma_f32_16x16x32_bf16 v[32:35], v[76:79], v[20:23], v[32:35]\n" \
  "v_mfma_f32_16x16x32_bf16 v[36:39], v[108:111], v[20:23], v[36:39]\n" \
  "v_mfma_f32_16x16x32_bf16 v[40:43], v[140:143], v[20:23], v[40:43]\n" \
  "v_mfma_f32_16x16x32_bf16 v[44:47], v[172:175], v[20:23], v[44:47]\n" \
  "v_mfma_f32_16x16x32_bf16 v[48:51], v[204:207], v[20:23], v[48:51]\n" \
  "v_mfma_f32_16x16x32_bf16 v[52:55], v[236:239], v[20:23], v[52:55]\n" \
  "s_cbranch_vccnz .LCY_D4_%=\n" \
  "s_mov_b32 m0, s57\n" \
  "s_add_u32 s57, s57, s65\n" \
  "global_load_lds_dwordx4 v2, s[50:51] offset:0 sc1\n" \
  ".LCY_D4_%=:\n" \
  "ds_read_b128 v[20:23], v0 offset:11264\n" \
  "s_waitcnt lgkmcnt(3)\n" \
  "v_mfma_f32_16x16x32_bf16 v[32:35], v[80:83], v[8:11], v[32:35]\n" \
  "v_mfma_f32_16x16x32_bf16 v[36:39], v[112:115], v[8:11], v[36:39]\n" \
  "v_mfma_f32_16x16x32_bf16 v[40:43], v[144:147], v[8:11], v[40:43]\n" \
  "v_mfma_f32_16x16x32_bf16 v[44:47], v[176:179], v[8:11], v[44:47]\n" \
  "v_mfma_f32_16x16x32_bf16 v[48:51], v[208:211], v[8:11], v[48:51]\n" \
  "v_mfma_f32_16x16x32_bf16 v[52:55], v[240:243], v[8:11], v[52:55]\n" \
  "s_cbranch_vccnz .LCY_D5_%=\n" \
  "global_load_lds_dwordx4 v2, s[50:51] offset:1024 sc1\n" \
  "s_sub_u32 s65, 0, s65\n" \
  "s_add_u32 s67, s67, 4096\n" \
  ".LCY_D5_%=:\n" \
  "ds_read_b128 v[8:11], v0 offset:4096\n" \
  "s_waitcnt lgkmcnt(3)\n" \
  "v_mfma_f32_16x16x32_bf16 v[32:35], v[84:87], v[12:15], v[32:35]\n" \
  "v_mfma_f32_16x16x32_bf16 v[36:39], v[116:119], v[12:15], v[36:39]\n" \
  "v_mfma_f32_16x16x32_bf16 v[40:43], v[148:151], v[12:15], v[40:43]\n" \
  "v_mfma_f32_16x16x32_bf16 v[44:47], v[180:183], v[12:15], v[44:47]\n" \
  "v_mfma_f32_16x16x32_bf16 v[48:51], v[212:215], v[12:15], v[48:51]\n" \
  "v_mfma_f32_16x16x32_bf16 v[52:55], v[244:247], v[12:15], v[52:55]\n" \
  "s_cbranch_vccnz .LCY_D6_%=\n" \
  "global_load_lds_dwordx4 v2, s[50:51] offset:2048 sc1\n" \
  "s_sub_u32 s68, s68, 1\n" \
  "s_mov_b32 s69, 1\n" \
  ".LCY_D6_%=:\n" \
  "ds_read_b128 v[12:15], v0 offset:5120\n" \
  "s_waitcnt lgkmcnt(3)\n" \
  "v_mfma_f32_16x16x32_bf16 v[32:35], v[88:91], v[16:19], v[32:35]\n" \
  "v_mfma_f32_16x16x32_bf16 v[36:39], v[120:123], v[16:19], v[36:39]\n" \
  "v_mfma_f32_16x16x32_bf16 v[40:43], v[152:155], v[16:19], v[40:43]\n" \
  "v_mfma_f32_16x16x32_bf16 v[44:47], v[184:187], v[16:19], v[44:47]\n" \
  "v_mfma_f32_16x16x32_bf16 v[48:51], v[216:219], v[16:19], v[48:51]\n" \
  "v_mfma_f32_16x16x32_bf16 v[52:55], v[248:251], v[16:19], v[52:55]\n" \
  "s_cbranch_vccnz .LCY_D7_%=\n" \
  "global_load_lds_dwordx4 v2, s[50:51] offset:3072 sc1\n" \
  "s_add_u32 s50, s50, 0x40000\n" \
  "s_addc_u32 s51, s51, 0\n" \
  ".LCY_D7_%=:\n" \
  "ds_read_b128 v[16:19], v0 offset:6144\n" \
  "s_waitcnt lgkmcnt(3)\n" \
  "v_mfma_f32_16x16x32_bf16 v[32:35], v[92:95], v[20:23], v[32:35]\n" \
  "v_mfma_f32_16x16x32_bf16 v[36:39], v[124:127], v[20:23], v[36:39]\n" \
  "v_mfma_f32_16x16x32_bf16 v[40:43], v[156:159], v[20:23], v[40:43]\n" \
  "v_mfma_f32_16x16x32_bf16 v[44:47], v[188:191], v[20:23], v[44:47]\n" \
  "v_mfma_f32_16x16x32_bf16 v[48:51], v[220:223], v[20:23], v[48:51]\n" \
  "v_mfma_f32_16x16x32_bf16 v[52:55], v[252:255], v[20:23], v[52:55]\n" \
  "ds_read_b128 v[20:23], v0 offset:7168\n" \
  "s_waitcnt lgkmcnt(3)\n" \
  "s_nop 1\n" \
  "v_cvt_pk_bf16_f32 v24, v32, v33\n" \
  "v_cvt_pk_bf16_f32 v25, v34, v35\n" \
  "v_cvt_pk_bf16_f32 v26, v36, v37\n" \
  "v_cvt_pk_bf16_f32 v27, v38, v39\n" \
  "s_nop 0\n" \
  "v_mfma_f32_16x16x32_bf16 v[32:35], v[64:67], v[8:11], 0\n" \
  "v_mfma_f32_16x16x32_bf16 v[36:39], v[96:99], v[8:11], 0\n" \
  "buffer_store_dwordx4 v[24:27], v1, s[40:43], s54 offen offset:0 sc1\n" \
  "v_cvt_pk_bf16_f32 v28, v40, v41\n" \
  "v_cvt_pk_bf16_f32 v29, v42, v43\n" \
  "v_cvt_pk_bf16_f32 v30, v44, v45\n" \
  "v_cvt_pk_bf16_f32 v31, v46, v47\n" \
  "s_nop 0\n" \
  "v_mfma_f32_16x16x32_bf16 v[40:43], v[128:131], v[8:11], 0\n" \
  "v_mfma_f32_16x16x32_bf16 v[44:47], v[160:163], v[8:11], 0\n" \
  "buffer_store_dwordx4 v[28:31], v1, s[40:43], s54 offen offset:128 sc1\n" \
  "v_cvt_pk_bf16_f32 v56, v48, v49\n" \
  "v_cvt_pk_bf16_f32 v57, v50, v51\n" \
  "v_cvt_pk_bf16_f32 v58, v52, v53\n" \
  "v_cvt_pk_bf16_f32 v59, v54, v55\n" \
  "s_nop 0\n" \
  "v_mfma_f32_16x16x32_bf16 v[48:51], v[192:195], v[8:11], 0\n" \
  "v_mfma_f32_16x16x32_bf16 v[52:55], v[224:227], v[8:11], 0\n" \
  "buffer_store_dwordx4 v[56:59], v1, s[40:43], s54 offen offset:256 sc1\n" \
  "ds_read_b128 v[8:11], v0 offset:12288\n" \
  "s_waitcnt lgkmcnt(3)\n" \
  "v_mfma_f32_16x16x32_bf16 v[32:35], v[68:71], v[12:15], v[32:35]\n" \
  "v_mfma_f32_16x16x32_bf16 v[36:39], v[100:103], v[12:15], v[36:39]\n" \
  "v_mfma_f32_16x16x32_bf16 v[40:43], v[132:135], v[12:15], v[40:43]\n" \
  "v_mfma_f32_16x16x32_bf16 v[44:47], v[164:167], v[12:15], v[44:47]\n" \
  "v_mfma_f32_16x16x32_bf16 v[48:51], v[196:199], v[12:15], v[48:51]\n" \
  "v_mfma_f32_16x16x32_bf16 v[52:55], v[228:231], v[12:15], v[52:55]\n" \
  "ds_read_b128 v[12:15], v0 offset:13312\n" \
  "s_waitcnt lgkmcnt(3)\n" \
  "v_mfma_f32_16x16x32_bf16 v[32:35], v[72:75], v[16:19], v[32:35]\n" \
  "v_mfma_f32_16x16x32_bf16 v[36:39], v[104:107], v[16:19], v[36:39]\n" \
  "v_mfma_f32_16x16x32_bf16 v[40:43], v[136:139], v[16:19], v[40:43]\n" \
  "v_mfma_f32_16x16x32_bf16 v[44:47], v[168:171], v[16:19], v[44:47]\n" \
  "v_mfma_f32_16x16x32_bf16 v[48:51], v[200:203], v[16:19], v[48:51]\n" \
  "v_mfma_f32_16x16x32_bf16 v[52:55], v[232:235], v[16:19], v[52:55]\n" \
  "ds_read_b128 v[16:19], v0 offset:14336\n" \
  "s_waitcnt lgkmcnt(3)\n" \
  "v_mfma_f32_16x16x32_bf16 v[32:35], v[76:79], v[20:23], v[32:35]\n" \
  "v_mfma_f32_16x16x32_bf16 v[36:39], v[108:111], v[20:23], v[36:39]\n" \
  "v_mfma_f32_16x16x32_bf16 v[40:43], v[140:143], v[20:23], v[40:43]\n" \
  "v_mfma_f32_16x16x32_bf16 v[44:47], v[172:175], v[20:23], v[44:47]\n" \
  "v_mfma_f32_16x16x32_bf16 v[48:51], v[204:207], v[20:23], v[48:51]\n" \
  "v_mfma_f32_16x16x32_bf16 v[52:55], v[236:239], v[20:23], v[52:55]\n" \
  "ds_read_b32 v7, v5 offset:8\n" \
  "ds_read_b128 v[20:23], v0 offset:15360\n" \
  "s_waitcnt lgkmcnt(4)\n" \
  "v_mfma_f32_16x16x32_bf16 v[32:35], v[80:83], v[8:11], v[32:35]\n" \
  "v_mfma_f32_16x16x32_bf16 v[36:39], v[112:115], v[8:11], v[36:39]\n" \
  "v_mfma_f32_16x16x32_bf16 v[40:43], v[144:147], v[8:11], v[40:43]\n" \
  "v_mfma_f32_16x16x32_bf16 v[44:47], v[176:179], v[8:11], v[44:47]\n" \
  "v_mfma_f32_16x16x32_bf16 v[48:51], v[208:211], v[8:11], v[48:51]\n" \
  "v_mfma_f32_16x16x32_bf16 v[52:55], v[240:243], v[8:11], v[52:55]\n" \
  "s_waitcnt lgkmcnt(3)\n" \
  "v_mfma_f32_16x16x32_bf16 v[32:35], v[84:87], v[12:15], v[32:35]\n" \
  "v_mfma_f32_16x16x32_bf16 v[36:39], v[116:119], v[12:15], v[36:39]\n" \
  "v_mfma_f32_16x16x32_bf16 v[40:43], v[148:151], v[12:15], v[40:43]\n" \
  "v_mfma_f32_16x16x32_bf16 v[44:47], v[180:183], v[12:15], v[44:47]\n" \
  "v_mfma_f32_16x16x32_bf16 v[48:51], v[212:215], v[12:15], v[48:51]\n" \
  "v_mfma_f32_16x16x32_bf16 v[52:55], v[244:247], v[12:15], v[52:55]\n" \
  "s_waitcnt lgkmcnt(2)\n" \
  "v_mfma_f32_16x16x32_bf16 v[32:35], v[88:91], v[16:19], v[32:35]\n" \
  "v_mfma_f32_16x16x32_bf16 v[36:39], v[120:123], v[16:19], v[36:39]\n" \
  "v_mfma_f32_16x16x32_bf16 v[40:43], v[152:155], v[16:19], v[40:43]\n" \
  "v_mfma_f32_16x16x32_bf16 v[44:47], v[184:187], v[16:19], v[44:47]\n" \
  "v_mfma_f32_16x16x32_bf16 v[48:51], v[216:219], v[16:19], v[48:51]\n" \
  "v_mfma_f32_16x16x32_bf16 v[52:55], v[248:251], v[16:19], v[52:55]\n" \
  "s_cbranch_vccnz .LCY_NT_%=\n" \
  "v_mov_b32 v4, 0\n" \
  "buffer_load_dword v4, v60, s[44:47], s67 offen sc1\n" \
  ".LCY_NT_%=:\n" \
  "s_waitcnt lgkmcnt(0)\n" \
  "v_mfma_f32_16x16x32_bf16 v[32:35], v[92:95], v[20:23], v[32:35]\n" \
  "v_mfma_f32_16x16x32_bf16 v[36:39], v[124:127], v[20:23], v[36:39]\n" \
  "v_mfma_f32_16x16x32_bf16 v[40:43], v[156:159], v[20:23], v[40:43]\n" \
  "v_mfma_f32_16x16x32_bf16 v[44:47], v[188:191], v[20:23], v[44:47]\n" \
  "v_mfma_f32_16x16x32_bf16 v[48:51], v[220:223], v[20:23], v[48:51]\n" \
  "v_mfma_f32_16x16x32_bf16 v[52:55], v[252:255], v[20:23], v[52:55]\n" \
  "v_add_u32 v0, s66, v0\n" \
  "s_sub_u32 s66, 0, s66\n" \
  "s_add_u32 s54, s54, 0x80000\n" \
  "s_mov_b32 s62, 0x40000\n" \
  "v_readfirstlane_b32 s63, v7\n" \
  "v_cvt_pk_bf16_f32 v24, v32, v33\n" \
  "v_cvt_pk_bf16_f32 v25, v34, v35\n" \
  "v_cvt_pk_bf16_f32 v26, v36, v37\n" \
  "v_cvt_pk_bf16_f32 v27, v38, v39\n" \
  "buffer_store_dwordx4 v[24:27], v1, s[40:43], s64 offen offset:0 sc1\n" \
  "v_cvt_pk_bf16_f32 v28, v40, v41\n" \
  "v_cvt_pk_bf16_f32 v29, v42, v43\n" \
  "v_cvt_pk_bf16_f32 v30, v44, v45\n" \
  "v_cvt_pk_bf16_f32 v31, v46, v47\n" \
  "buffer_store_dwordx4 v[28:31], v1, s[40:43], s64 offen offset:128 sc1\n" \
  "v_cvt_pk_bf16_f32 v56, v48, v49\n" \
  "v_cvt_pk_bf16_f32 v57, v50, v51\n" \
  "v_cvt_pk_bf16_f32 v58, v52, v53\n" \
  "v_cvt_pk_bf16_f32 v59, v54, v55\n" \
  "buffer_store_dwordx4 v[56:59], v1, s[40:43], s64 offen offset:256 sc1\n" \
  "s_cbranch_vccnz .LCY_ND_%=\n" \
  "s_add_u32 s63, s63, s60\n" \
  "s_cmp_eq_u32 s63, 66\n" \
  "s_cselect_b32 s72, 1, 0\n" \
  "s_cbranch_scc1 .LCY_DEF_%=\n" \
  "s_waitcnt vmcnt(0)\n" \
  "v_mov_b32 v6, s55\n" \
  "s_mov_b64 exec, 1\n" \
  "ds_add_u32 v5, v6\n" \
  "s_mov_b64 exec, -1\n" \
  "s_mov_b32 s69, 0\n" \
  "s_branch .LCY_WC_%=\n" \
  ".LCY_ND_%=:\n" \
  "v_mov_b32 v4, 0\n" \
  "buffer_load_dword v4, v60, s[44:47], s67 offen sc1\n" \
  "s_waitcnt vmcnt(0)\n" \
  ".LCY_WC_%=:\n" \
  "v_mov_b32 v6, s59\n" \
  "v_mov_b32 v7, 1\n" \
  "s_mov_b64 exec, 1\n" \
  "ds_add_rtn_u32 v6, v6, v7\n" \
  "s_mov_b64 exec, -1\n" \
  "s_xor_b32 s59, s59, 4\n" \
  "s_mov_b32 s61, 0\n" \
  "s_waitcnt lgkmcnt(0)\n" \
  "v_readfirstlane_b32 s63, v6\n" \
  "v_lshlrev_b32 v6, 5, v2\n" \
  "s_and_b32 s63, s63, 7\n" \
  "s_cmp_eq_u32 s63, 7\n" \
  "s_cbranch_scc0 .LCY_W8_%=\n" \
  "s_mov_b64 exec, 15\n" \
  "global_store_dword v6, v7, s[52:53] sc1\n" \
  "s_mov_b64 exec, -1\n" \
  ".LCY_W8_%=:\n" \
  "s_add_u32 s52, s52, 2048\n" \
  "s_addc_u32 s53, s53, 0\n" \
  ".LCY_DEF_%=:\n" \
  "s_sub_u32 s60, s60, 1\n" \
  "s_cmp_eq_u32 s60, 0\n" \
  "s_cbranch_scc1 .LCY_END_%=\n" \
  ".LCY_NEED_%=:\n" \
  "s_cmp_eq_u32 s68, 0\n" \
  "s_cbranch_scc1 .LCY_BAR_%=\n" \
  "v_cmp_eq_u32 vcc, 0, v4\n" \
  "s_cmp_eq_u64 vcc, 0\n" \
  "s_cbranch_scc1 .LCY_GET_%=\n" \
  "s_cmp_ge_u32 s67, 0x40000\n" \
  "s_cbranch_scc1 .LCY_BAR_%=\n" \
  "s_sleep 4\n" \
  "ds_read_b32 v6, v5 offset:16\n" \
  "s_waitcnt lgkmcnt(0)\n" \
  "v_readfirstlane_b32 s63, v6\n" \
  "s_add_u32 s63, s63, s60\n" \
  "s_cmp_ge_u32 s63, 66\n" \
  "s_cbranch_scc1 .LCY_BAR_%=\n" \
  ".LCY_ASK_%=:\n" \
  "v_mov_b32 v4, 0\n" \
  "buffer_load_dword v4, v60, s[44:47], s67 offen sc1\n" \
  "s_sub_u32 s62, s62, 1\n" \
  "s_cbranch_scc1 .LCY_END_%=\n" \
  "s_waitcnt vmcnt(0)\n" \
  "s_branch .LCY_NEED_%=\n" \
  ".LCY_GET_%=:\n" \
  "v_lshlrev_b32 v6, 16, s55\n" \
  "s_mov_b64 exec, 1\n" \
  "ds_add_u32 v5, v6\n" \
  "s_mov_b64 exec, -1\n" \
  "s_mov_b32 m0, s56\n" \
  "s_add_u32 s71, s56, 4096\n" \
  "global_load_lds_dwordx4 v2, s[48:49] offset:0 sc1\n" \
  "global_load_lds_dwordx4 v2, s[48:49] offset:1024 sc1\n" \
  "global_load_lds_dwordx4 v2, s[48:49] offset:2048 sc1\n" \
  "global_load_lds_dwordx4 v2, s[48:49] offset:3072 sc1\n" \
  "s_mov_b32 m0, s71\n" \
  "s_add_u32 s56, s56, s65\n" \
  "global_load_lds_dwordx4 v3, s[48:49] offset:0 sc1\n" \
  "global_load_lds_dwordx4 v3, s[48:49] offset:1024 sc1\n" \
  "global_load_lds_dwordx4 v3, s[48:49] offset:2048 sc1\n" \
  "global_load_lds_dwordx4 v3, s[48:49] offset:3072 sc1\n" \
  "s_add_u32 s48, s48, 0x80000\n" \
  "s_addc_u32 s49, s49, 0\n" \
  "s_mov_b32 m0, s57\n" \
  "s_add_u32 s57, s57, s65\n" \
  "global_load_lds_dwordx4 v2, s[50:51] offset:0 sc1\n" \
  "global_load_lds_dwordx4 v2, s[50:51] offset:1024 sc1\n" \
  "s_sub_u32 s65, 0, s65\n" \
  "s_add_u32 s67, s67, 4096\n" \
  "global_load_lds_dwordx4 v2, s[50:51] offset:2048 sc1\n" \
  "s_sub_u32 s68, s68, 1\n" \
  "s_mov_b32 s69, 1\n" \
  "global_load_lds_dwordx4 v2, s[50:51] offset:3072 sc1\n" \
  "s_add_u32 s50, s50, 0x40000\n" \
  "s_addc_u32 s51, s51, 0\n" \
  "v_mov_b32 v4, 0\n" \
  "buffer_load_dword v4, v60, s[44:47], s67 offen sc1\n" \
  "s_mov_b32 s61, 0\n" \
  "s_cmp_eq_u32 s68, 0\n" \
  "s_cbranch_scc1 .LCY_BAR_%=\n" \
  "s_waitcnt vmcnt(0)\n" \
  "v_mov_b32 v6, s55\n" \
  "s_mov_b64 exec, 1\n" \
  "ds_add_u32 v5, v6\n" \
  "s_mov_b64 exec, -1\n" \
  "s_mov_b32 s69, 0\n" \
  "s_branch .LCY_NEED_%=\n" \
  ".LCY_END_%=:\n" \
  "s_endpgm\n"
template <bool AT>
__device__ __forceinline__ void role_aw_t(const MegaArgs& A, char* smem, const int L, const int a, const u64 tag) {
  const int w = A.wv;
  const int l = lane_id(), c = l & 15, g = l >> 4;
  const int qt = w & 1, hh = (w >> 1) & 1, h = 2 * a + hh;
  char* ws = A.ws;
  unsigned* FL = (unsigned*)(ws + OFF_FL);
  const unsigned sb = lds_base(smem);
  if (w == 0 && l < 8) reinterpret_cast<unsigned*>(smem + AW_CNT)[l] = 0u;
  constexpr int NP = AT ? 1 : 3;
  const int cp0 = AT ? w : 3 * w - 8;
  v8u wo[2 * NP][4];
  {
    const int q = (l >> 2) & 3, p = l & 3, f = q | ((g & 1) << 2);
    const unsigned ra = sb + (4 * g + q) * 256 + (p & 1) * 16;
#pragma unroll
    for (int r = 0; r < 2; ++r) {
#pragma unroll
      for (int k = 0; k < 2; ++k) {
        const char* base_ = (const char*)A.Wo + ((size_t)((2 * a + r) * DH + 64 * (w & 1) + 32 * k) * DM + 128 * (w >> 1)) * 2;
#pragma unroll
        for (int i = 0; i < 8; ++i) {
          const int fd = g | ((i & 1) << 2);
          glds16(base_, (unsigned)((4 * i + g) * (DM * 2) + ((c ^ (fd << 1)) << 4)), sb + w * 16384 + k * 8192 + i * 1024);
        }
      }
      SST(4 + 3 * r);
      if (r == 1) ready_wait(A, tag, ready_look(A));
      WAIT_VM(0);
      RAW_BAR();
      SST(5 + 3 * r);
#pragma unroll
      for (int k2 = 0; k2 < 2; ++k2) {
#pragma unroll
        for (int ct = 0; ct < 2 * NP; ++ct) {
          const int cp = cp0 + (ct >> 1);
          const unsigned a_ = ra + (cp >> 2) * 32768 + k2 * 16384 + ((2 * (cp & 3) + (p >> 1)) ^ f) * 32 + (ct & 1) * 8;
          const v2u r0 = trd(a_), r1 = trd(a_ + 4096), r2 = trd(a_ + 8192), r3 = trd(a_ + 12288);
          wo[ct][2 * r + k2] = v8u{r0.x, r0.y, r1.x, r1.y, r2.x, r2.y, r3.x, r3.y};
          asm volatile("" : "+v"(wo[ct][2 * r + k2]));
        }
      }
      RAW_BAR();
      SST(6 + 3 * r);
    }
  }
  WSTAMP2();
  const v4u rsW = mk_srd(ws + OFF_PW, SZ_PW);
  const int dp = (FL_FP - FL_FQ + (L * 16 + a - L * 64 * 32 - h) * 32 + 2 * hh) * 4;
  const v4u rsF = mk_srd(FL + FL_FQ + (L * 64 * 32 + h) * 32, (qt ? 0 : dp) + 64 * 4096 - 128);
  const char* p0 = AT ? ws + OFF_MEMK + L * SZ_MEM + h * 2048 : ws + OFF_QK + L * SZ_QK + ((size_t)h * 16 + 8 * qt) * 1024;
  const char* p1 = AT ? (const char*)(FL + FL_FK + L * 32 + (h >> 3) * 64 + (h & 7) * 2) : ws + OFF_VF + L * SZ_O + ((size_t)h * 8 + 4 * qt) * 1024;
  const unsigned* pFW = FL + FL_FW + (L * 64 * 16 + (a >> 2)) * 32 + (a & 3);
  const unsigned tk = sb + AW_TK + hh * 24 * 1024, voff = (c >> 1) * 2048 + (c & 1) * 64 + g * 16;
  const char* dbg = ws + WS_TOTAL + A.vb * 64 * 8;
#define AW_SOPS                                                                                                                 \
  "{s[40:43]}"(rsW), "{s[44:47]}"(rsF), "{s[48:49]}"(p0), "{s[50:51]}"(p1), "{s[52:53]}"(pFW),                                   \
      "{s54}"((L * 64 * 16 + a) * 32768 + cp0 * 128 + (AT ? qt * 16384 : 0)), "{s55}"(1 << (8 * qt)), "{s56}"(tk + qt * 8192), "{s57}"(tk + 16384 + qt * 4096),                  \
      "{s58}"(sb + AW_CNT + 8 + 4 * hh), "{s59}"(sb + AW_CNT), "{s60}"(NSEG), "{s[36:37]}"(dbg)                                  \
      : "memory", "vcc", "scc", "s80"
  if constexpr (AT) {
    const v8u pv = {tk + l * 16, (c & 8) ? 0xEEEEu : 0x4444u, 0u, (unsigned)(OFF_MEMV - OFF_MEMK) + c * 32 + (g & 1) * 16 + (g >> 1) * 1024,
                    voff, (unsigned)l * 16, tk + qt * 4096 + l * 16, sb + AW_SO + w * 4096 + l * 16};
    const unsigned neg = 0xff61b1e6u;
    v8u bt;
#pragma unroll
    for (int i = 0; i < 4; ++i) {
      const unsigned d = 4 * g + i > c ? neg : 0u;
      bt[i] = qt ? 0u : d;
      bt[4 + i] = qt ? d : neg;
    }
    asm volatile("s_mov_b64 exec, -1\ns_waitcnt vmcnt(0) lgkmcnt(0)\ns_nop 4\n" AW_ASM_X ".LKG_LAST:\n"
                 ::"{v[192:199]}"(wo[0][0]), "{v[200:207]}"(wo[0][1]), "{v[208:215]}"(wo[0][2]), "{v[216:223]}"(wo[0][3]),
                 "{v[224:231]}"(wo[1][0]), "{v[232:239]}"(wo[1][1]), "{v[240:247]}"(wo[1][2]), "{v[248:255]}"(wo[1][3]),
                 "{v[0:7]}"(pv), "{v[8:15]}"(bt), AW_SOPS);
  } else {
    const v4u pv = {sb + AW_SO + l * 16, voff, (unsigned)l * 16, (l & 2) ? 16u + 8 * qt : qt ? 8u * (l & 1) : dp + 4 * (l & 1)};
    asm volatile("s_mov_b64 exec, -1\ns_waitcnt vmcnt(0) lgkmcnt(0)\ns_nop 4\n" AW_ASM_Y
                 ::"{v[64:71]}"(wo[0][0]), "{v[72:79]}"(wo[0][1]), "{v[80:87]}"(wo[0][2]), "{v[88:95]}"(wo[0][3]),
                 "{v[96:103]}"(wo[1][0]), "{v[104:111]}"(wo[1][1]), "{v[112:119]}"(wo[1][2]), "{v[120:127]}"(wo[1][3]),
                 "{v[128:135]}"(wo[2][0]), "{v[136:143]}"(wo[2][1]), "{v[144:151]}"(wo[2][2]), "{v[152:159]}"(wo[2][3]),
                 "{v[160:167]}"(wo[3][0]), "{v[168:175]}"(wo[3][1]), "{v[176:183]}"(wo[3][2]), "{v[184:191]}"(wo[3][3]),
                 "{v[192:199]}"(wo[4][0]), "{v[200:207]}"(wo[4][1]), "{v[208:215]}"(wo[4][2]), "{v[216:223]}"(wo[4][3]),
                 "{v[224:231]}"(wo[5][0]), "{v[232:239]}"(wo[5][1]), "{v[240:247]}"(wo[5][2]), "{v[248:255]}"(wo[5][3]),
                 "{v[0:3]}"(pv), AW_SOPS);
  }
#undef AW_SOPS
}
__device__ __forceinline__ void role_aw(const MegaArgs& A, char* smem, const int L, const int a, const u64 tag) {
  if (A.wv < 4) role_aw_t<true>(A, smem, L, a, tag);
  else role_aw_t<false>(A, smem, L, a, tag);
}

#define P_ASM \
  "s_mov_b32 s68, 0\n" \
  "s_mov_b32 s66, 3\n" \
  "s_mov_b32 s67, 2\n" \
  "s_mov_b32 s72, 0\n" \
  "v_mov_b32 v7, 1\n" \
  "v_mov_b32 v6, s70\n" \
  "v_mov_b32 v5, 0\n" \
  "v_add_u32 v10, 4096, v2\n" \
  ".p2align 6\n" \
  ".LCP_TOP_%=:\n" \
  "ds_read_b128 v[28:31], v1 offset:0\n" \
  "ds_read_b128 v[32:35], v1 offset:1024\n" \
  "s_add_u32 s66, s66, 3\n" \
  "s_add_u32 s67, s67, 2\n" \
  "s_mov_b32 s69, 0x400000\n" \
  "v_xor_b32 v0, 0x8000, v0\n" \
  "v_xor_b32 v3, 0x100, v3\n" \
  "v_readfirstlane_b32 s71, v5\n" \
  "s_cmp_ge_u32 s71, s66\n" \
  "s_cbranch_scc1 .LCP_GO_%=\n" \
  "s_cmp_eq_u32 s68, 1\n" \
  "s_cbranch_scc0 .LCP_X1_%=\n" \
  "s_waitcnt vmcnt(0)\n" \
  "s_mov_b64 exec, 1\n" \
  "buffer_store_dword v7, off, s[56:59], s72 sc1\n" \
  "s_mov_b64 exec, -1\n" \
  "s_add_u32 s72, s72, 0x1000\n" \
  "s_mov_b32 s68, 0\n" \
  ".LCP_X1_%=:\n" \
  ".LCP_S2_%=:\n" \
  "s_sleep 1\n" \
  "ds_read_b32 v5, v6 offset:0\n" \
  "s_sub_u32 s69, s69, 1\n" \
  "s_cbranch_scc1 .LCP_END_%=\n" \
  "s_waitcnt lgkmcnt(0)\n" \
  "v_readfirstlane_b32 s71, v5\n" \
  "s_cmp_ge_u32 s71, s66\n" \
  "s_cbranch_scc1 .LCP_GO_%=\n" \
  "s_branch .LCP_S2_%=\n" \
  ".LCP_GO_%=:\n" \
  "ds_read_b128 v[12:15], v0 offset:0\n" \
  "ds_read_b128 v[16:19], v0 offset:2048\n" \
  "ds_read_b128 v[20:23], v0 offset:4096\n" \
  "ds_read_b128 v[24:27], v0 offset:6144\n" \
  "ds_read_b128 v[36:39], v1 offset:2048\n" \
  "s_waitcnt lgkmcnt(4)\n" \
  "v_mfma_f32_16x16x32_bf16 v[48:51], v[64:67], v[12:15], 0\n" \
  "v_mfma_f32_16x16x32_bf16 v[52:55], v[128:131], v[12:15], 0\n" \
  "v_mfma_f32_16x16x32_bf16 v[56:59], v[192:195], v[12:15], 0\n" \
  "v_mfma_f32_16x16x32_bf16 v[60:63], v[28:31], v[12:15], 0\n" \
  "ds_read_b128 v[12:15], v0 offset:8192\n" \
  "ds_read_b128 v[28:31], v1 offset:3072\n" \
  "s_waitcnt lgkmcnt(5)\n" \
  "v_mfma_f32_16x16x32_bf16 v[48:51], v[68:71], v[16:19], v[48:51]\n" \
  "v_mfma_f32_16x16x32_bf16 v[52:55], v[132:135], v[16:19], v[52:55]\n" \
  "v_mfma_f32_16x16x32_bf16 v[56:59], v[196:199], v[16:19], v[56:59]\n" \
  "v_mfma_f32_16x16x32_bf16 v[60:63], v[32:35], v[16:19], v[60:63]\n" \
  "ds_read_b128 v[16:19], v0 offset:10240\n" \
  "ds_read_b128 v[32:35], v1 offset:4096\n" \
  "s_waitcnt lgkmcnt(6)\n" \
  "v_mfma_f32_16x16x32_bf16 v[48:51], v[72:75], v[20:23], v[48:51]\n" \
  "v_mfma_f32_16x16x32_bf16 v[52:55], v[136:139], v[20:23], v[52:55]\n" \
  "v_mfma_f32_16x16x32_bf16 v[56:59], v[200:203], v[20:23], v[56:59]\n" \
  "s_waitcnt lgkmcnt(4)\n" \
  "v_mfma_f32_16x16x32_bf16 v[60:63], v[36:39], v[20:23], v[60:63]\n" \
  "ds_read_b128 v[20:23], v0 offset:12288\n" \
  "ds_read_b128 v[36:39], v1 offset:5120\n" \
  "v_mfma_f32_16x16x32_bf16 v[48:51], v[76:79], v[24:27], v[48:51]\n" \
  "v_mfma_f32_16x16x32_bf16 v[52:55], v[140:143], v[24:27], v[52:55]\n" \
  "v_mfma_f32_16x16x32_bf16 v[56:59], v[204:207], v[24:27], v[56:59]\n" \
  "s_waitcnt lgkmcnt(4)\n" \
  "v_mfma_f32_16x16x32_bf16 v[60:63], v[28:31], v[24:27], v[60:63]\n" \
  "ds_read_b128 v[24:27], v0 offset:14336\n" \
  "ds_read_b128 v[28:31], v1 offset:6144\n" \
  "v_mfma_f32_16x16x32_bf16 v[48:51], v[80:83], v[12:15], v[48:51]\n" \
  "v_mfma_f32_16x16x32_bf16 v[52:55], v[144:147], v[12:15], v[52:55]\n" \
  "v_mfma_f32_16x16x32_bf16 v[56:59], v[208:211], v[12:15], v[56:59]\n" \
  "s_waitcnt lgkmcnt(4)\n" \
  "v_mfma_f32_16x16x32_bf16 v[60:63], v[32:35], v[12:15], v[60:63]\n" \
  "ds_read_b128 v[12:15], v0 offset:16384\n" \
  "ds_read_b128 v[32:35], v1 offset:7168\n" \
  "v_mfma_f32_16x16x32_bf16 v[48:51], v[84:87], v[16:19], v[48:51]\n" \
  "v_mfma_f32_16x16x32_bf16 v[52:55], v[148:151], v[16:19], v[52:55]\n" \
  "v_mfma_f32_16x16x32_bf16 v[56:59], v[212:215], v[16:19], v[56:59]\n" \
  "s_waitcnt lgkmcnt(4)\n" \
  "v_mfma_f32_16x16x32_bf16 v[60:63], v[36:39], v[16:19], v[60:63]\n" \
  "ds_read_b128 v[16:19], v0 offset:18432\n" \
  "ds_read_b128 v[36:39], v1 offset:8192\n" \
  "v_mfma_f32_16x16x32_bf16 v[48:51], v[88:91], v[20:23], v[48:51]\n" \
  "v_mfma_f32_16x16x32_bf16 v[52:55], v[152:155], v[20:23], v[52:55]\n" \
  "v_mfma_f32_16x16x32_bf16 v[56:59], v[216:219], v[20:23], v[56:59]\n" \
  "s_waitcnt lgkmcnt(4)\n" \
  "v_mfma_f32_16x16x32_bf16 v[60:63], v[28:31], v[20:23], v[60:63]\n" \
  "ds_read_b128 v[20:23], v0 offset:20480\n" \
  "ds_read_b128 v[28:31], v1 offset:9216\n" \
  "v_mfma_f32_16x16x32_bf16 v[48:51], v[92:95], v[24:27], v[48:51]\n" \
  "v_mfma_f32_16x16x32_bf16 v[52:55], v[156:159], v[24:27], v[52:55]\n" \
  "v_mfma_f32_16x16x32_bf16 v[56:59], v[220:223], v[24:27], v[56:59]\n" \
  "s_waitcnt lgkmcnt(4)\n" \
  "v_mfma_f32_16x16x32_bf16 v[60:63], v[32:35], v[24:27], v[60:63]\n" \
  "ds_read_b128 v[24:27], v0 offset:22528\n" \
  "ds_read_b128 v[32:35], v1 offset:10240\n" \
  "v_mfma_f32_16x16x32_bf16 v[48:51], v[96:99], v[12:15], v[48:51]\n" \
  "v_mfma_f32_16x16x32_bf16 v[52:55], v[160:163], v[12:15], v[52:55]\n" \
  "v_mfma_f32_16x16x32_bf16 v[56:59], v[224:227], v[12:15], v[56:59]\n" \
  "s_waitcnt lgkmcnt(4)\n" \
  "v_mfma_f32_16x16x32_bf16 v[60:63], v[36:39], v[12:15], v[60:63]\n" \
  "ds_read_b128 v[12:15], v0 offset:24576\n" \
  "ds_read_b128 v[36:39], v1 offset:11264\n" \
  "v_mfma_f32_16x16x32_bf16 v[48:51], v[100:103], v[16:19], v[48:51]\n" \
  "v_mfma_f32_16x16x32_bf16 v[52:55], v[164:167], v[16:19], v[52:55]\n" \
  "v_mfma_f32_16x16x32_bf16 v[56:59], v[228:231], v[16:19], v[56:59]\n" \
  "s_waitcnt lgkmcnt(4)\n" \
  "v_mfma_f32_16x16x32_bf16 v[60:63], v[28:31], v[16:19], v[60:63]\n" \
  "ds_read_b128 v[16:19], v0 offset:26624\n" \
  "ds_read_b128 v[28:31], v1 offset:12288\n" \
  "v_mfma_f32_16x16x32_bf16 v[48:51], v[104:107], v[20:23], v[48:51]\n" \
  "v_mfma_f32_16x16x32_bf16 v[52:55], v[168:171], v[20:23], v[52:55]\n" \
  "v_mfma_f32_16x16x32_bf16 v[56:59], v[232:235], v[20:23], v[56:59]\n" \
  "s_waitcnt lgkmcnt(4)\n" \
  "v_mfma_f32_16x16x32_bf16 v[60:63], v[32:35], v[20:23], v[60:63]\n" \
  "ds_read_b128 v[20:23], v0 offset:28672\n" \
  "ds_read_b128 v[32:35], v1 offset:13312\n" \
  "v_mfma_f32_16x16x32_bf16 v[48:51], v[108:111], v[24:27], v[48:51]\n" \
  "v_mfma_f32_16x16x32_bf16 v[52:55], v[172:175], v[24:27], v[52:55]\n" \
  "v_mfma_f32_16x16x32_bf16 v[56:59], v[236:239], v[24:27], v[56:59]\n" \
  "s_waitcnt lgkmcnt(4)\n" \
  "v_mfma_f32_16x16x32_bf16 v[60:63], v[36:39], v[24:27], v[60:63]\n" \
  "s_cmp_eq_u32 s68, 1\n" \
  "s_cbranch_scc0 .LCP_X3_%=\n" \
  "s_waitcnt vmcnt(0)\n" \
  "s_mov_b64 exec, 1\n" \
  "buffer_store_dword v7, off, s[56:59], s72 sc1\n" \
  "s_mov_b64 exec, -1\n" \
  "s_add_u32 s72, s72, 0x1000\n" \
  "s_mov_b32 s68, 0\n" \
  ".LCP_X3_%=:\n" \
  "ds_read_b32 v5, v6 offset:16\n" \
  "ds_read_b128 v[24:27], v0 offset:30720\n" \
  "ds_read_b128 v[36:39], v1 offset:14336\n" \
  "v_mfma_f32_16x16x32_bf16 v[48:51], v[112:115], v[12:15], v[48:51]\n" \
  "v_mfma_f32_16x16x32_bf16 v[52:55], v[176:179], v[12:15], v[52:55]\n" \
  "v_mfma_f32_16x16x32_bf16 v[56:59], v[240:243], v[12:15], v[56:59]\n" \
  "s_waitcnt lgkmcnt(5)\n" \
  "v_mfma_f32_16x16x32_bf16 v[60:63], v[28:31], v[12:15], v[60:63]\n" \
  "ds_read_b128 v[12:15], v0 offset:256\n" \
  "ds_read_b128 v[28:31], v1 offset:15360\n" \
  "ds_read_b128 v[40:43], v4 offset:0\n" \
  "ds_read_b128 v[44:47], v4 offset:64\n" \
  "v_mfma_f32_16x16x32_bf16 v[48:51], v[116:119], v[16:19], v[48:51]\n" \
  "v_mfma_f32_16x16x32_bf16 v[52:55], v[180:183], v[16:19], v[52:55]\n" \
  "v_mfma_f32_16x16x32_bf16 v[56:59], v[244:247], v[16:19], v[56:59]\n" \
  "s_waitcnt lgkmcnt(7)\n" \
  "v_mfma_f32_16x16x32_bf16 v[60:63], v[32:35], v[16:19], v[60:63]\n" \
  "s_waitcnt lgkmcnt(6)\n" \
  "v_readfirstlane_b32 s71, v5\n" \
  "s_cmp_ge_u32 s71, s67\n" \
  "s_cbranch_scc1 .LCP_ST_%=\n" \
  ".LCP_S4_%=:\n" \
  "s_sleep 1\n" \
  "ds_read_b32 v5, v6 offset:16\n" \
  "s_sub_u32 s69, s69, 1\n" \
  "s_cbranch_scc1 .LCP_END_%=\n" \
  "s_waitcnt lgkmcnt(0)\n" \
  "v_readfirstlane_b32 s71, v5\n" \
  "s_cmp_ge_u32 s71, s67\n" \
  "s_cbranch_scc1 .LCP_ST_%=\n" \
  "s_branch .LCP_S4_%=\n" \
  ".LCP_ST_%=:\n" \
  "ds_read_b128 v[16:19], v0 offset:2304\n" \
  "ds_read_b128 v[32:35], v4 offset:128\n" \
  "ds_read2_b32 v[8:9], v3 offset0:0 offset1:32\n" \
  "v_mfma_f32_16x16x32_bf16 v[48:51], v[120:123], v[20:23], v[48:51]\n" \
  "v_mfma_f32_16x16x32_bf16 v[52:55], v[184:187], v[20:23], v[52:55]\n" \
  "v_mfma_f32_16x16x32_bf16 v[56:59], v[248:251], v[20:23], v[56:59]\n" \
  "s_waitcnt lgkmcnt(7)\n" \
  "v_mfma_f32_16x16x32_bf16 v[60:63], v[36:39], v[20:23], v[60:63]\n" \
  "ds_read_b128 v[20:23], v0 offset:4352\n" \
  "ds_read_b128 v[36:39], v4 offset:192\n" \
  "v_mfma_f32_16x16x32_bf16 v[48:51], v[124:127], v[24:27], v[48:51]\n" \
  "v_mfma_f32_16x16x32_bf16 v[52:55], v[188:191], v[24:27], v[52:55]\n" \
  "v_mfma_f32_16x16x32_bf16 v[56:59], v[252:255], v[24:27], v[56:59]\n" \
  "s_waitcnt lgkmcnt(7)\n" \
  "v_mfma_f32_16x16x32_bf16 v[60:63], v[28:31], v[24:27], v[60:63]\n" \
  "s_waitcnt lgkmcnt(0)\n" \
  "s_nop 4\n" \
  "v_fma_f32 v48, -v8, v40, v48\n" \
  "v_fma_f32 v49, -v8, v41, v49\n" \
  "v_fma_f32 v50, -v8, v42, v50\n" \
  "v_fma_f32 v51, -v8, v43, v51\n" \
  "v_fma_f32 v52, -v8, v44, v52\n" \
  "v_fma_f32 v53, -v8, v45, v53\n" \
  "v_fma_f32 v54, -v8, v46, v54\n" \
  "v_fma_f32 v55, -v8, v47, v55\n" \
  "v_fma_f32 v56, -v8, v32, v56\n" \
  "v_fma_f32 v57, -v8, v33, v57\n" \
  "v_fma_f32 v58, -v8, v34, v58\n" \
  "v_fma_f32 v59, -v8, v35, v59\n" \
  "v_fma_f32 v60, -v8, v36, v60\n" \
  "v_fma_f32 v61, -v8, v37, v61\n" \
  "v_fma_f32 v62, -v8, v38, v62\n" \
  "v_fma_f32 v63, -v8, v39, v63\n" \
  "ds_read_b128 v[32:35], v1 offset:0\n" \
  "ds_read_b128 v[36:39], v1 offset:1024\n" \
  "v_mul_f32 v48, v48, v9\n" \
  "v_mul_f32 v49, v49, v9\n" \
  "v_mul_f32 v50, v50, v9\n" \
  "v_mul_f32 v51, v51, v9\n" \
  "v_mul_f32 v52, v52, v9\n" \
  "v_mul_f32 v53, v53, v9\n" \
  "v_mul_f32 v54, v54, v9\n" \
  "v_mul_f32 v55, v55, v9\n" \
  "v_cvt_pk_bf16_f32 v40, v48, v49\n" \
  "v_cvt_pk_bf16_f32 v41, v50, v51\n" \
  "v_cvt_pk_bf16_f32 v42, v52, v53\n" \
  "v_cvt_pk_bf16_f32 v43, v54, v55\n" \
  "buffer_store_dwordx4 v[40:43], v2, s[40:43], s65 offen offset:0 sc1\n" \
  "v_mul_f32 v56, v56, v9\n" \
  "v_mul_f32 v57, v57, v9\n" \
  "v_mul_f32 v58, v58, v9\n" \
  "v_mul_f32 v59, v59, v9\n" \
  "v_mul_f32 v60, v60, v9\n" \
  "v_mul_f32 v61, v61, v9\n" \
  "v_mul_f32 v62, v62, v9\n" \
  "v_mul_f32 v63, v63, v9\n" \
  "v_cvt_pk_bf16_f32 v44, v56, v57\n" \
  "v_cvt_pk_bf16_f32 v45, v58, v59\n" \
  "v_cvt_pk_bf16_f32 v46, v60, v61\n" \
  "v_cvt_pk_bf16_f32 v47, v62, v63\n" \
  "buffer_store_dwordx4 v[44:47], v2, s[40:43], s65 offen offset:1024 sc1\n" \
  "ds_read_b128 v[24:27], v0 offset:6400\n" \
  "ds_read_b128 v[28:31], v1 offset:2048\n" \
  "v_mfma_f32_16x16x32_bf16 v[48:51], v[64:67], v[12:15], 0\n" \
  "v_mfma_f32_16x16x32_bf16 v[52:55], v[128:131], v[12:15], 0\n" \
  "v_mfma_f32_16x16x32_bf16 v[56:59], v[192:195], v[12:15], 0\n" \
  "s_waitcnt lgkmcnt(3)\n" \
  "v_mfma_f32_16x16x32_bf16 v[60:63], v[32:35], v[12:15], 0\n" \
  "ds_read_b128 v[12:15], v0 offset:8448\n" \
  "ds_read_b128 v[32:35], v1 offset:3072\n" \
  "v_mfma_f32_16x16x32_bf16 v[48:51], v[68:71], v[16:19], v[48:51]\n" \
  "v_mfma_f32_16x16x32_bf16 v[52:55], v[132:135], v[16:19], v[52:55]\n" \
  "v_mfma_f32_16x16x32_bf16 v[56:59], v[196:199], v[16:19], v[56:59]\n" \
  "s_waitcnt lgkmcnt(4)\n" \
  "v_mfma_f32_16x16x32_bf16 v[60:63], v[36:39], v[16:19], v[60:63]\n" \
  "ds_read_b128 v[16:19], v0 offset:10496\n" \
  "ds_read_b128 v[36:39], v1 offset:4096\n" \
  "v_mfma_f32_16x16x32_bf16 v[48:51], v[72:75], v[20:23], v[48:51]\n" \
  "v_mfma_f32_16x16x32_bf16 v[52:55], v[136:139], v[20:23], v[52:55]\n" \
  "v_mfma_f32_16x16x32_bf16 v[56:59], v[200:203], v[20:23], v[56:59]\n" \
  "s_waitcnt lgkmcnt(4)\n" \
  "v_mfma_f32_16x16x32_bf16 v[60:63], v[28:31], v[20:23], v[60:63]\n" \
  "ds_read_b128 v[20:23], v0 offset:12544\n" \
  "ds_read_b128 v[28:31], v1 offset:5120\n" \
  "v_mfma_f32_16x16x32_bf16 v[48:51], v[76:79], v[24:27], v[48:51]\n" \
  "v_mfma_f32_16x16x32_bf16 v[52:55], v[140:143], v[24:27], v[52:55]\n" \
  "v_mfma_f32_16x16x32_bf16 v[56:59], v[204:207], v[24:27], v[56:59]\n" \
  "s_waitcnt lgkmcnt(4)\n" \
  "v_mfma_f32_16x16x32_bf16 v[60:63], v[32:35], v[24:27], v[60:63]\n" \
  "ds_read_b128 v[24:27], v0 offset:14592\n" \
  "ds_read_b128 v[32:35], v1 offset:6144\n" \
  "v_mfma_f32_16x16x32_bf16 v[48:51], v[80:83], v[12:15], v[48:51]\n" \
  "v_mfma_f32_16x16x32_bf16 v[52:55], v[144:147], v[12:15], v[52:55]\n" \
  "v_mfma_f32_16x16x32_bf16 v[56:59], v[208:211], v[12:15], v[56:59]\n" \
  "s_waitcnt lgkmcnt(4)\n" \
  "v_mfma_f32_16x16x32_bf16 v[60:63], v[36:39], v[12:15], v[60:63]\n" \
  "ds_read_b128 v[12:15], v0 offset:16640\n" \
  "ds_read_b128 v[36:39], v1 offset:7168\n" \
  "v_mfma_f32_16x16x32_bf16 v[48:51], v[84:87], v[16:19], v[48:51]\n" \
  "v_mfma_f32_16x16x32_bf16 v[52:55], v[148:151], v[16:19], v[52:55]\n" \
  "v_mfma_f32_16x16x32_bf16 v[56:59], v[212:215], v[16:19], v[56:59]\n" \
  "s_waitcnt lgkmcnt(4)\n" \
  "v_mfma_f32_16x16x32_bf16 v[60:63], v[28:31], v[16:19], v[60:63]\n" \
  "ds_read_b128 v[16:19], v0 offset:18688\n" \
  "ds_read_b128 v[28:31], v1 offset:8192\n" \
  "v_mfma_f32_16x16x32_bf16 v[48:51], v[88:91], v[20:23], v[48:51]\n" \
  "v_mfma_f32_16x16x32_bf16 v[52:55], v[152:155], v[20:23], v[52:55]\n" \
  "v_mfma_f32_16x16x32_bf16 v[56:59], v[216:219], v[20:23], v[56:59]\n" \
  "s_waitcnt lgkmcnt(4)\n" \
  "v_mfma_f32_16x16x32_bf16 v[60:63], v[32:35], v[20:23], v[60:63]\n" \
  "ds_read_b128 v[20:23], v0 offset:20736\n" \
  "ds_read_b128 v[32:35], v1 offset:9216\n" \
  "v_mfma_f32_16x16x32_bf16 v[48:51], v[92:95], v[24:27], v[48:51]\n" \
  "v_mfma_f32_16x16x32_bf16 v[52:55], v[156:159], v[24:27], v[52:55]\n" \
  "v_mfma_f32_16x16x32_bf16 v[56:59], v[220:223], v[24:27], v[56:59]\n" \
  "s_waitcnt lgkmcnt(4)\n" \
  "v_mfma_f32_16x16x32_bf16 v[60:63], v[36:39], v[24:27], v[60:63]\n" \
  "ds_read_b128 v[24:27], v0 offset:22784\n" \
  "ds_read_b128 v[36:39], v1 offset:10240\n" \
  "v_mfma_f32_16x16x32_bf16 v[48:51], v[96:99], v[12:15], v[48:51]\n" \
  "v_mfma_f32_16x16x32_bf16 v[52:55], v[160:163], v[12:15], v[52:55]\n" \
  "v_mfma_f32_16x16x32_bf16 v[56:59], v[224:227], v[12:15], v[56:59]\n" \
  "s_waitcnt lgkmcnt(4)\n" \
  "v_mfma_f32_16x16x32_bf16 v[60:63], v[28:31], v[12:15], v[60:63]\n" \
  "ds_read_b128 v[12:15], v0 offset:24832\n" \
  "ds_read_b128 v[28:31], v1 offset:11264\n" \
  "v_mfma_f32_16x16x32_bf16 v[48:51], v[100:103], v[16:19], v[48:51]\n" \
  "v_mfma_f32_16x16x32_bf16 v[52:55], v[164:167], v[16:19], v[52:55]\n" \
  "v_mfma_f32_16x16x32_bf16 v[56:59], v[228:231], v[16:19], v[56:59]\n" \
  "s_waitcnt lgkmcnt(4)\n" \
  "v_mfma_f32_16x16x32_bf16 v[60:63], v[32:35], v[16:19], v[60:63]\n" \
  "ds_read_b128 v[16:19], v0 offset:26880\n" \
  "ds_read_b128 v[32:35], v1 offset:12288\n" \
  "v_mfma_f32_16x16x32_bf16 v[48:51], v[104:107], v[20:23], v[48:51]\n" \
  "v_mfma_f32_16x16x32_bf16 v[52:55], v[168:171], v[20:23], v[52:55]\n" \
  "v_mfma_f32_16x16x32_bf16 v[56:59], v[232:235], v[20:23], v[56:59]\n" \
  "s_waitcnt lgkmcnt(4)\n" \
  "v_mfma_f32_16x16x32_bf16 v[60:63], v[36:39], v[20:23], v[60:63]\n" \
  "ds_read_b128 v[20:23], v0 offset:28928\n" \
  "ds_read_b128 v[36:39], v1 offset:13312\n" \
  "v_mfma_f32_16x16x32_bf16 v[48:51], v[108:111], v[24:27], v[48:51]\n" \
  "v_mfma_f32_16x16x32_bf16 v[52:55], v[172:175], v[24:27], v[52:55]\n" \
  "v_mfma_f32_16x16x32_bf16 v[56:59], v[236:239], v[24:27], v[56:59]\n" \
  "s_waitcnt lgkmcnt(4)\n" \
  "v_mfma_f32_16x16x32_bf16 v[60:63], v[28:31], v[24:27], v[60:63]\n" \
  "ds_read_b128 v[24:27], v0 offset:30976\n" \
  "ds_read_b128 v[28:31], v1 offset:14336\n" \
  "v_mfma_f32_16x16x32_bf16 v[48:51], v[112:115], v[12:15], v[48:51]\n" \
  "v_mfma_f32_16x16x32_bf16 v[52:55], v[176:179], v[12:15], v[52:55]\n" \
  "v_mfma_f32_16x16x32_bf16 v[56:59], v[240:243], v[12:15], v[56:59]\n" \
  "s_waitcnt lgkmcnt(4)\n" \
  "v_mfma_f32_16x16x32_bf16 v[60:63], v[32:35], v[12:15], v[60:63]\n" \
  "ds_read_b128 v[32:35], v1 offset:15360\n" \
  "ds_read_b128 v[40:43], v4 offset:0\n" \
  "ds_read_b128 v[44:47], v4 offset:64\n" \
  "v_mfma_f32_16x16x32_bf16 v[48:51], v[116:119], v[16:19], v[48:51]\n" \
  "v_mfma_f32_16x16x32_bf16 v[52:55], v[180:183], v[16:19], v[52:55]\n" \
  "v_mfma_f32_16x16x32_bf16 v[56:59], v[244:247], v[16:19], v[56:59]\n" \
  "s_waitcnt lgkmcnt(5)\n" \
  "v_mfma_f32_16x16x32_bf16 v[60:63], v[36:39], v[16:19], v[60:63]\n" \
  "ds_read_b128 v[36:39], v4 offset:128\n" \
  "ds_read2_b32 v[8:9], v3 offset0:16 offset1:48\n" \
  "v_mfma_f32_16x16x32_bf16 v[48:51], v[120:123], v[20:23], v[48:51]\n" \
  "v_mfma_f32_16x16x32_bf16 v[52:55], v[184:187], v[20:23], v[52:55]\n" \
  "v_mfma_f32_16x16x32_bf16 v[56:59], v[248:251], v[20:23], v[56:59]\n" \
  "s_waitcnt lgkmcnt(5)\n" \
  "v_mfma_f32_16x16x32_bf16 v[60:63], v[28:31], v[20:23], v[60:63]\n" \
  "ds_read_b128 v[28:31], v4 offset:192\n" \
  "s_mov_b64 exec, 1\n" \
  "ds_add_u32 v6, v7 offset:8\n" \
  "s_mov_b64 exec, -1\n" \
  "v_xor_b32 v6, 4, v6\n" \
  "v_mfma_f32_16x16x32_bf16 v[48:51], v[124:127], v[24:27], v[48:51]\n" \
  "v_mfma_f32_16x16x32_bf16 v[52:55], v[188:191], v[24:27], v[52:55]\n" \
  "v_mfma_f32_16x16x32_bf16 v[56:59], v[252:255], v[24:27], v[56:59]\n" \
  "s_waitcnt lgkmcnt(6)\n" \
  "v_mfma_f32_16x16x32_bf16 v[60:63], v[32:35], v[24:27], v[60:63]\n" \
  "s_waitcnt lgkmcnt(1)\n" \
  "s_nop 4\n" \
  "ds_read_b32 v5, v6 offset:0\n" \
  "v_fma_f32 v48, -v8, v40, v48\n" \
  "v_fma_f32 v49, -v8, v41, v49\n" \
  "v_fma_f32 v50, -v8, v42, v50\n" \
  "v_fma_f32 v51, -v8, v43, v51\n" \
  "v_fma_f32 v52, -v8, v44, v52\n" \
  "v_fma_f32 v53, -v8, v45, v53\n" \
  "v_fma_f32 v54, -v8, v46, v54\n" \
  "v_fma_f32 v55, -v8, v47, v55\n" \
  "v_fma_f32 v56, -v8, v36, v56\n" \
  "v_fma_f32 v57, -v8, v37, v57\n" \
  "v_fma_f32 v58, -v8, v38, v58\n" \
  "v_fma_f32 v59, -v8, v39, v59\n" \
  "v_fma_f32 v60, -v8, v28, v60\n" \
  "v_fma_f32 v61, -v8, v29, v61\n" \
  "v_fma_f32 v62, -v8, v30, v62\n" \
  "v_fma_f32 v63, -v8, v31, v63\n" \
  "v_mul_f32 v48, v48, v9\n" \
  "v_mul_f32 v49, v49, v9\n" \
  "v_mul_f32 v50, v50, v9\n" \
  "v_mul_f32 v51, v51, v9\n" \
  "v_mul_f32 v52, v52, v9\n" \
  "v_mul_f32 v53, v53, v9\n" \
  "v_mul_f32 v54, v54, v9\n" \
  "v_mul_f32 v55, v55, v9\n" \
  "v_cvt_pk_bf16_f32 v40, v48, v49\n" \
  "v_cvt_pk_bf16_f32 v41, v50, v51\n" \
  "v_cvt_pk_bf16_f32 v42, v52, v53\n" \
  "v_cvt_pk_bf16_f32 v43, v54, v55\n" \
  "buffer_store_dwordx4 v[40:43], v10, s[40:43], s65 offen offset:0 sc1\n" \
  "v_mul_f32 v56, v56, v9\n" \
  "v_mul_f32 v57, v57, v9\n" \
  "v_mul_f32 v58, v58, v9\n" \
  "v_mul_f32 v59, v59, v9\n" \
  "v_mul_f32 v60, v60, v9\n" \
  "v_mul_f32 v61, v61, v9\n" \
  "v_mul_f32 v62, v62, v9\n" \
  "v_mul_f32 v63, v63, v9\n" \
  "v_cvt_pk_bf16_f32 v44, v56, v57\n" \
  "v_cvt_pk_bf16_f32 v45, v58, v59\n" \
  "v_cvt_pk_bf16_f32 v46, v60, v61\n" \
  "v_cvt_pk_bf16_f32 v47, v62, v63\n" \
  "buffer_store_dwordx4 v[44:47], v10, s[40:43], s65 offen offset:1024 sc1\n" \
  "s_waitcnt lgkmcnt(0)\n" \
  "s_add_u32 s65, s65, 0x80000\n" \
  "s_sub_u32 s64, s64, 1\n" \
  "s_mov_b32 s68, 1\n" \
  "s_cmp_lg_u32 s64, 0\n" \
  "s_cbranch_scc1 .LCP_TOP_%=\n" \
  "s_waitcnt vmcnt(0)\n" \
  "s_mov_b64 exec, 1\n" \
  "buffer_store_dword v7, off, s[56:59], s72 sc1\n" \
  "s_mov_b64 exec, -1\n" \
  "s_add_u32 s72, s72, 0x1000\n" \
  "s_mov_b32 s68, 0\n" \
  ".LCP_END_%=:\n" \
  "s_endpgm\n"
#define V_ASM \
  "s_mov_b32 s68, 0\n" \
  "s_mov_b32 s66, 3\n" \
  "s_mov_b32 s67, 2\n" \
  "s_mov_b32 s72, 0\n" \
  "v_mov_b32 v7, 1\n" \
  "v_mov_b32 v6, s70\n" \
  "v_mov_b32 v5, 0\n" \
  "ds_read_b32 v8, v4 offset:0\n" \
  "ds_read_b32 v9, v4 offset:64\n" \
  "ds_read_b32 v10, v4 offset:128\n" \
  "ds_read_b32 v4, v4 offset:192\n" \
  "s_waitcnt lgkmcnt(0)\n" \
  ".p2align 6\n" \
  ".LCW_TOP_%=:\n" \
  "ds_read_b128 v[28:31], v1 offset:0\n" \
  "ds_read_b128 v[32:35], v1 offset:1024\n" \
  "s_add_u32 s66, s66, 3\n" \
  "s_add_u32 s67, s67, 2\n" \
  "s_mov_b32 s69, 0x400000\n" \
  "v_xor_b32 v0, 0x8000, v0\n" \
  "v_xor_b32 v3, 0x100, v3\n" \
  "v_readfirstlane_b32 s71, v5\n" \
  "s_cmp_ge_u32 s71, s66\n" \
  "s_cbranch_scc1 .LCW_GO_%=\n" \
  "s_cmp_eq_u32 s68, 1\n" \
  "s_cbranch_scc0 .LCW_X1_%=\n" \
  "s_waitcnt vmcnt(0)\n" \
  "s_mov_b64 exec, 1\n" \
  "buffer_store_dword v7, off, s[56:59], s72 sc1\n" \
  "s_mov_b64 exec, -1\n" \
  "s_add_u32 s72, s72, 0x1000\n" \
  "s_mov_b32 s68, 0\n" \
  ".LCW_X1_%=:\n" \
  ".LCW_S2_%=:\n" \
  "s_sleep 1\n" \
  "ds_read_b32 v5, v6 offset:0\n" \
  "s_sub_u32 s69, s69, 1\n" \
  "s_cbranch_scc1 .LCW_END_%=\n" \
  "s_waitcnt lgkmcnt(0)\n" \
  "v_readfirstlane_b32 s71, v5\n" \
  "s_cmp_ge_u32 s71, s66\n" \
  "s_cbranch_scc1 .LCW_GO_%=\n" \
  "s_branch .LCW_S2_%=\n" \
  ".LCW_GO_%=:\n" \
  "ds_read_b128 v[12:15], v0 offset:0\n" \
  "ds_read_b128 v[16:19], v0 offset:2048\n" \
  "ds_read_b128 v[20:23], v0 offset:4096\n" \
  "ds_read_b128 v[24:27], v0 offset:6144\n" \
  "ds_read_b128 v[36:39], v1 offset:2048\n" \
  "s_waitcnt lgkmcnt(4)\n" \
  "v_mfma_f32_16x16x32_bf16 v[42:45], v[12:15], v[64:67], 0\n" \
  "v_mfma_f32_16x16x32_bf16 v[48:51], v[12:15], v[128:131], 0\n" \
  "v_mfma_f32_16x16x32_bf16 v[54:57], v[12:15], v[192:195], 0\n" \
  "v_mfma_f32_16x16x32_bf16 v[60:63], v[12:15], v[28:31], 0\n" \
  "ds_read_b128 v[12:15], v0 offset:8192\n" \
  "ds_read_b128 v[28:31], v1 offset:3072\n" \
  "s_waitcnt lgkmcnt(5)\n" \
  "v_mfma_f32_16x16x32_bf16 v[42:45], v[16:19], v[68:71], v[42:45]\n" \
  "v_mfma_f32_16x16x32_bf16 v[48:51], v[16:19], v[132:135], v[48:51]\n" \
  "v_mfma_f32_16x16x32_bf16 v[54:57], v[16:19], v[196:199], v[54:57]\n" \
  "v_mfma_f32_16x16x32_bf16 v[60:63], v[16:19], v[32:35], v[60:63]\n" \
  "ds_read_b128 v[16:19], v0 offset:10240\n" \
  "ds_read_b128 v[32:35], v1 offset:4096\n" \
  "s_waitcnt lgkmcnt(6)\n" \
  "v_mfma_f32_16x16x32_bf16 v[42:45], v[20:23], v[72:75], v[42:45]\n" \
  "v_mfma_f32_16x16x32_bf16 v[48:51], v[20:23], v[136:139], v[48:51]\n" \
  "v_mfma_f32_16x16x32_bf16 v[54:57], v[20:23], v[200:203], v[54:57]\n" \
  "s_waitcnt lgkmcnt(4)\n" \
  "v_mfma_f32_16x16x32_bf16 v[60:63], v[20:23], v[36:39], v[60:63]\n" \
  "ds_read_b128 v[20:23], v0 offset:12288\n" \
  "ds_read_b128 v[36:39], v1 offset:5120\n" \
  "v_mfma_f32_16x16x32_bf16 v[42:45], v[24:27], v[76:79], v[42:45]\n" \
  "v_mfma_f32_16x16x32_bf16 v[48:51], v[24:27], v[140:143], v[48:51]\n" \
  "v_mfma_f32_16x16x32_bf16 v[54:57], v[24:27], v[204:207], v[54:57]\n" \
  "s_waitcnt lgkmcnt(4)\n" \
  "v_mfma_f32_16x16x32_bf16 v[60:63], v[24:27], v[28:31], v[60:63]\n" \
  "ds_read_b128 v[24:27], v0 offset:14336\n" \
  "ds_read_b128 v[28:31], v1 offset:6144\n" \
  "v_mfma_f32_16x16x32_bf16 v[42:45], v[12:15], v[80:83], v[42:45]\n" \
  "v_mfma_f32_16x16x32_bf16 v[48:51], v[12:15], v[144:147], v[48:51]\n" \
  "v_mfma_f32_16x16x32_bf16 v[54:57], v[12:15], v[208:211], v[54:57]\n" \
  "s_waitcnt lgkmcnt(4)\n" \
  "v_mfma_f32_16x16x32_bf16 v[60:63], v[12:15], v[32:35], v[60:63]\n" \
  "ds_read_b128 v[12:15], v0 offset:16384\n" \
  "ds_read_b128 v[32:35], v1 offset:7168\n" \
  "v_mfma_f32_16x16x32_bf16 v[42:45], v[16:19], v[84:87], v[42:45]\n" \
  "v_mfma_f32_16x16x32_bf16 v[48:51], v[16:19], v[148:151], v[48:51]\n" \
  "v_mfma_f32_16x16x32_bf16 v[54:57], v[16:19], v[212:215], v[54:57]\n" \
  "s_waitcnt lgkmcnt(4)\n" \
  "v_mfma_f32_16x16x32_bf16 v[60:63], v[16:19], v[36:39], v[60:63]\n" \
  "ds_read_b128 v[16:19], v0 offset:18432\n" \
  "ds_read_b128 v[36:39], v1 offset:8192\n" \
  "v_mfma_f32_16x16x32_bf16 v[42:45], v[20:23], v[88:91], v[42:45]\n" \
  "v_mfma_f32_16x16x32_bf16 v[48:51], v[20:23], v[152:155], v[48:51]\n" \
  "v_mfma_f32_16x16x32_bf16 v[54:57], v[20:23], v[216:219], v[54:57]\n" \
  "s_waitcnt lgkmcnt(4)\n" \
  "v_mfma_f32_16x16x32_bf16 v[60:63], v[20:23], v[28:31], v[60:63]\n" \
  "ds_read_b128 v[20:23], v0 offset:20480\n" \
  "ds_read_b128 v[28:31], v1 offset:9216\n" \
  "v_mfma_f32_16x16x32_bf16 v[42:45], v[24:27], v[92:95], v[42:45]\n" \
  "v_mfma_f32_16x16x32_bf16 v[48:51], v[24:27], v[156:159], v[48:51]\n" \
  "v_mfma_f32_16x16x32_bf16 v[54:57], v[24:27], v[220:223], v[54:57]\n" \
  "s_waitcnt lgkmcnt(4)\n" \
  "v_mfma_f32_16x16x32_bf16 v[60:63], v[24:27], v[32:35], v[60:63]\n" \
  "ds_read_b128 v[24:27], v0 offset:22528\n" \
  "ds_read_b128 v[32:35], v1 offset:10240\n" \
  "v_mfma_f32_16x16x32_bf16 v[42:45], v[12:15], v[96:99], v[42:45]\n" \
  "v_mfma_f32_16x16x32_bf16 v[48:51], v[12:15], v[160:163], v[48:51]\n" \
  "v_mfma_f32_16x16x32_bf16 v[54:57], v[12:15], v[224:227], v[54:57]\n" \
  "s_waitcnt lgkmcnt(4)\n" \
  "v_mfma_f32_16x16x32_bf16 v[60:63], v[12:15], v[36:39], v[60:63]\n" \
  "ds_read_b128 v[12:15], v0 offset:24576\n" \
  "ds_read_b128 v[36:39], v1 offset:11264\n" \
  "v_mfma_f32_16x16x32_bf16 v[42:45], v[16:19], v[100:103], v[42:45]\n" \
  "v_mfma_f32_16x16x32_bf16 v[48:51], v[16:19], v[164:167], v[48:51]\n" \
  "v_mfma_f32_16x16x32_bf16 v[54:57], v[16:19], v[228:231], v[54:57]\n" \
  "s_waitcnt lgkmcnt(4)\n" \
  "v_mfma_f32_16x16x32_bf16 v[60:63], v[16:19], v[28:31], v[60:63]\n" \
  "ds_read_b128 v[16:19], v0 offset:26624\n" \
  "ds_read_b128 v[28:31], v1 offset:12288\n" \
  "v_mfma_f32_16x16x32_bf16 v[42:45], v[20:23], v[104:107], v[42:45]\n" \
  "v_mfma_f32_16x16x32_bf16 v[48:51], v[20:23], v[168:171], v[48:51]\n" \
  "v_mfma_f32_16x16x32_bf16 v[54:57], v[20:23], v[232:235], v[54:57]\n" \
  "s_waitcnt lgkmcnt(4)\n" \
  "v_mfma_f32_16x16x32_bf16 v[60:63], v[20:23], v[32:35], v[60:63]\n" \
  "ds_read_b128 v[20:23], v0 offset:28672\n" \
  "ds_read_b128 v[32:35], v1 offset:13312\n" \
  "v_mfma_f32_16x16x32_bf16 v[42:45], v[24:27], v[108:111], v[42:45]\n" \
  "v_mfma_f32_16x16x32_bf16 v[48:51], v[24:27], v[172:175], v[48:51]\n" \
  "v_mfma_f32_16x16x32_bf16 v[54:57], v[24:27], v[236:239], v[54:57]\n" \
  "s_waitcnt lgkmcnt(4)\n" \
  "v_mfma_f32_16x16x32_bf16 v[60:63], v[24:27], v[36:39], v[60:63]\n" \
  "s_cmp_eq_u32 s68, 1\n" \
  "s_cbranch_scc0 .LCW_X3_%=\n" \
  "s_waitcnt vmcnt(0)\n" \
  "s_mov_b64 exec, 1\n" \
  "buffer_store_dword v7, off, s[56:59], s72 sc1\n" \
  "s_mov_b64 exec, -1\n" \
  "s_add_u32 s72, s72, 0x1000\n" \
  "s_mov_b32 s68, 0\n" \
  ".LCW_X3_%=:\n" \
  "ds_read_b32 v5, v6 offset:16\n" \
  "ds_read_b128 v[24:27], v0 offset:30720\n" \
  "ds_read_b128 v[36:39], v1 offset:14336\n" \
  "v_mfma_f32_16x16x32_bf16 v[42:45], v[12:15], v[112:115], v[42:45]\n" \
  "v_mfma_f32_16x16x32_bf16 v[48:51], v[12:15], v[176:179], v[48:51]\n" \
  "v_mfma_f32_16x16x32_bf16 v[54:57], v[12:15], v[240:243], v[54:57]\n" \
  "s_waitcnt lgkmcnt(5)\n" \
  "v_mfma_f32_16x16x32_bf16 v[60:63], v[12:15], v[28:31], v[60:63]\n" \
  "ds_read_b128 v[12:15], v0 offset:256\n" \
  "ds_read_b128 v[28:31], v1 offset:15360\n" \
  "v_mfma_f32_16x16x32_bf16 v[42:45], v[16:19], v[116:119], v[42:45]\n" \
  "v_mfma_f32_16x16x32_bf16 v[48:51], v[16:19], v[180:183], v[48:51]\n" \
  "v_mfma_f32_16x16x32_bf16 v[54:57], v[16:19], v[244:247], v[54:57]\n" \
  "s_waitcnt lgkmcnt(5)\n" \
  "v_mfma_f32_16x16x32_bf16 v[60:63], v[16:19], v[32:35], v[60:63]\n" \
  "s_waitcnt lgkmcnt(4)\n" \
  "v_readfirstlane_b32 s71, v5\n" \
  "s_cmp_ge_u32 s71, s67\n" \
  "s_cbranch_scc1 .LCW_ST_%=\n" \
  ".LCW_S4_%=:\n" \
  "s_sleep 1\n" \
  "ds_read_b32 v5, v6 offset:16\n" \
  "s_sub_u32 s69, s69, 1\n" \
  "s_cbranch_scc1 .LCW_END_%=\n" \
  "s_waitcnt lgkmcnt(0)\n" \
  "v_readfirstlane_b32 s71, v5\n" \
  "s_cmp_ge_u32 s71, s67\n" \
  "s_cbranch_scc1 .LCW_ST_%=\n" \
  "s_branch .LCW_S4_%=\n" \
  ".LCW_ST_%=:\n" \
  "ds_read_b128 v[16:19], v0 offset:2304\n" \
  "ds_read_b128 v[32:35], v3 offset:0\n" \
  "v_mfma_f32_16x16x32_bf16 v[42:45], v[20:23], v[120:123], v[42:45]\n" \
  "v_mfma_f32_16x16x32_bf16 v[48:51], v[20:23], v[184:187], v[48:51]\n" \
  "v_mfma_f32_16x16x32_bf16 v[54:57], v[20:23], v[248:251], v[54:57]\n" \
  "s_waitcnt lgkmcnt(4)\n" \
  "v_mfma_f32_16x16x32_bf16 v[60:63], v[20:23], v[36:39], v[60:63]\n" \
  "ds_read_b128 v[20:23], v0 offset:4352\n" \
  "ds_read_b128 v[36:39], v3 offset:128\n" \
  "v_mfma_f32_16x16x32_bf16 v[42:45], v[24:27], v[124:127], v[42:45]\n" \
  "v_mfma_f32_16x16x32_bf16 v[48:51], v[24:27], v[188:191], v[48:51]\n" \
  "v_mfma_f32_16x16x32_bf16 v[54:57], v[24:27], v[252:255], v[54:57]\n" \
  "s_waitcnt lgkmcnt(4)\n" \
  "v_mfma_f32_16x16x32_bf16 v[60:63], v[24:27], v[28:31], v[60:63]\n" \
  "s_waitcnt lgkmcnt(0)\n" \
  "s_nop 4\n" \
  "v_fma_f32 v42, -v32, v8, v42\n" \
  "v_fma_f32 v43, -v33, v8, v43\n" \
  "v_fma_f32 v44, -v34, v8, v44\n" \
  "v_fma_f32 v45, -v35, v8, v45\n" \
  "v_fma_f32 v48, -v32, v9, v48\n" \
  "v_fma_f32 v49, -v33, v9, v49\n" \
  "v_fma_f32 v50, -v34, v9, v50\n" \
  "v_fma_f32 v51, -v35, v9, v51\n" \
  "v_fma_f32 v54, -v32, v10, v54\n" \
  "v_fma_f32 v55, -v33, v10, v55\n" \
  "v_fma_f32 v56, -v34, v10, v56\n" \
  "v_fma_f32 v57, -v35, v10, v57\n" \
  "v_fma_f32 v60, -v32, v4, v60\n" \
  "v_fma_f32 v61, -v33, v4, v61\n" \
  "v_fma_f32 v62, -v34, v4, v62\n" \
  "v_fma_f32 v63, -v35, v4, v63\n" \
  "ds_read_b128 v[32:35], v1 offset:0\n" \
  "v_mul_f32 v42, v42, v36\n" \
  "v_mul_f32 v43, v43, v37\n" \
  "v_mul_f32 v44, v44, v38\n" \
  "v_mul_f32 v45, v45, v39\n" \
  "v_mul_f32 v48, v48, v36\n" \
  "v_mul_f32 v49, v49, v37\n" \
  "v_mul_f32 v50, v50, v38\n" \
  "v_mul_f32 v51, v51, v39\n" \
  "v_mul_f32 v54, v54, v36\n" \
  "v_mul_f32 v55, v55, v37\n" \
  "v_mul_f32 v56, v56, v38\n" \
  "v_mul_f32 v57, v57, v39\n" \
  "v_mul_f32 v60, v60, v36\n" \
  "v_mul_f32 v61, v61, v37\n" \
  "v_mul_f32 v62, v62, v38\n" \
  "v_mul_f32 v63, v63, v39\n" \
  "ds_read_b128 v[36:39], v1 offset:1024\n" \
  "v_cvt_pk_bf16_f32 v40, v42, v43\n" \
  "v_cvt_pk_bf16_f32 v41, v44, v45\n" \
  "v_cvt_pk_bf16_f32 v46, v48, v49\n" \
  "v_cvt_pk_bf16_f32 v47, v50, v51\n" \
  "v_cvt_pk_bf16_f32 v52, v54, v55\n" \
  "v_cvt_pk_bf16_f32 v53, v56, v57\n" \
  "v_cvt_pk_bf16_f32 v58, v60, v61\n" \
  "v_cvt_pk_bf16_f32 v59, v62, v63\n" \
  "ds_read_b128 v[24:27], v0 offset:6400\n" \
  "ds_read_b128 v[28:31], v1 offset:2048\n" \
  "v_mfma_f32_16x16x32_bf16 v[42:45], v[12:15], v[64:67], 0\n" \
  "v_mfma_f32_16x16x32_bf16 v[48:51], v[12:15], v[128:131], 0\n" \
  "v_mfma_f32_16x16x32_bf16 v[54:57], v[12:15], v[192:195], 0\n" \
  "s_waitcnt lgkmcnt(3)\n" \
  "v_mfma_f32_16x16x32_bf16 v[60:63], v[12:15], v[32:35], 0\n" \
  "ds_read_b128 v[12:15], v0 offset:8448\n" \
  "ds_read_b128 v[32:35], v1 offset:3072\n" \
  "v_mfma_f32_16x16x32_bf16 v[42:45], v[16:19], v[68:71], v[42:45]\n" \
  "v_mfma_f32_16x16x32_bf16 v[48:51], v[16:19], v[132:135], v[48:51]\n" \
  "v_mfma_f32_16x16x32_bf16 v[54:57], v[16:19], v[196:199], v[54:57]\n" \
  "s_waitcnt lgkmcnt(4)\n" \
  "v_mfma_f32_16x16x32_bf16 v[60:63], v[16:19], v[36:39], v[60:63]\n" \
  "ds_read_b128 v[16:19], v0 offset:10496\n" \
  "ds_read_b128 v[36:39], v1 offset:4096\n" \
  "v_mfma_f32_16x16x32_bf16 v[42:45], v[20:23], v[72:75], v[42:45]\n" \
  "v_mfma_f32_16x16x32_bf16 v[48:51], v[20:23], v[136:139], v[48:51]\n" \
  "v_mfma_f32_16x16x32_bf16 v[54:57], v[20:23], v[200:203], v[54:57]\n" \
  "s_waitcnt lgkmcnt(4)\n" \
  "v_mfma_f32_16x16x32_bf16 v[60:63], v[20:23], v[28:31], v[60:63]\n" \
  "ds_read_b128 v[20:23], v0 offset:12544\n" \
  "ds_read_b128 v[28:31], v1 offset:5120\n" \
  "v_mfma_f32_16x16x32_bf16 v[42:45], v[24:27], v[76:79], v[42:45]\n" \
  "v_mfma_f32_16x16x32_bf16 v[48:51], v[24:27], v[140:143], v[48:51]\n" \
  "v_mfma_f32_16x16x32_bf16 v[54:57], v[24:27], v[204:207], v[54:57]\n" \
  "s_waitcnt lgkmcnt(4)\n" \
  "v_mfma_f32_16x16x32_bf16 v[60:63], v[24:27], v[32:35], v[60:63]\n" \
  "ds_read_b128 v[24:27], v0 offset:14592\n" \
  "ds_read_b128 v[32:35], v1 offset:6144\n" \
  "v_mfma_f32_16x16x32_bf16 v[42:45], v[12:15], v[80:83], v[42:45]\n" \
  "v_mfma_f32_16x16x32_bf16 v[48:51], v[12:15], v[144:147], v[48:51]\n" \
  "v_mfma_f32_16x16x32_bf16 v[54:57], v[12:15], v[208:211], v[54:57]\n" \
  "s_waitcnt lgkmcnt(4)\n" \
  "v_mfma_f32_16x16x32_bf16 v[60:63], v[12:15], v[36:39], v[60:63]\n" \
  "ds_read_b128 v[12:15], v0 offset:16640\n" \
  "ds_read_b128 v[36:39], v1 offset:7168\n" \
  "v_mfma_f32_16x16x32_bf16 v[42:45], v[16:19], v[84:87], v[42:45]\n" \
  "v_mfma_f32_16x16x32_bf16 v[48:51], v[16:19], v[148:151], v[48:51]\n" \
  "v_mfma_f32_16x16x32_bf16 v[54:57], v[16:19], v[212:215], v[54:57]\n" \
  "s_waitcnt lgkmcnt(4)\n" \
  "v_mfma_f32_16x16x32_bf16 v[60:63], v[16:19], v[28:31], v[60:63]\n" \
  "ds_read_b128 v[16:19], v0 offset:18688\n" \
  "ds_read_b128 v[28:31], v1 offset:8192\n" \
  "v_mfma_f32_16x16x32_bf16 v[42:45], v[20:23], v[88:91], v[42:45]\n" \
  "v_mfma_f32_16x16x32_bf16 v[48:51], v[20:23], v[152:155], v[48:51]\n" \
  "v_mfma_f32_16x16x32_bf16 v[54:57], v[20:23], v[216:219], v[54:57]\n" \
  "s_waitcnt lgkmcnt(4)\n" \
  "v_mfma_f32_16x16x32_bf16 v[60:63], v[20:23], v[32:35], v[60:63]\n" \
  "ds_read_b128 v[20:23], v0 offset:20736\n" \
  "ds_read_b128 v[32:35], v1 offset:9216\n" \
  "v_mfma_f32_16x16x32_bf16 v[42:45], v[24:27], v[92:95], v[42:45]\n" \
  "v_mfma_f32_16x16x32_bf16 v[48:51], v[24:27], v[156:159], v[48:51]\n" \
  "v_mfma_f32_16x16x32_bf16 v[54:57], v[24:27], v[220:223], v[54:57]\n" \
  "s_waitcnt lgkmcnt(4)\n" \
  "v_mfma_f32_16x16x32_bf16 v[60:63], v[24:27], v[36:39], v[60:63]\n" \
  "ds_read_b128 v[24:27], v0 offset:22784\n" \
  "ds_read_b128 v[36:39], v1 offset:10240\n" \
  "v_mfma_f32_16x16x32_bf16 v[42:45], v[12:15], v[96:99], v[42:45]\n" \
  "v_mfma_f32_16x16x32_bf16 v[48:51], v[12:15], v[160:163], v[48:51]\n" \
  "v_mfma_f32_16x16x32_bf16 v[54:57], v[12:15], v[224:227], v[54:57]\n" \
  "s_waitcnt lgkmcnt(4)\n" \
  "v_mfma_f32_16x16x32_bf16 v[60:63], v[12:15], v[28:31], v[60:63]\n" \
  "ds_read_b128 v[12:15], v0 offset:24832\n" \
  "ds_read_b128 v[28:31], v1 offset:11264\n" \
  "v_mfma_f32_16x16x32_bf16 v[42:45], v[16:19], v[100:103], v[42:45]\n" \
  "v_mfma_f32_16x16x32_bf16 v[48:51], v[16:19], v[164:167], v[48:51]\n" \
  "v_mfma_f32_16x16x32_bf16 v[54:57], v[16:19], v[228:231], v[54:57]\n" \
  "s_waitcnt lgkmcnt(4)\n" \
  "v_mfma_f32_16x16x32_bf16 v[60:63], v[16:19], v[32:35], v[60:63]\n" \
  "ds_read_b128 v[16:19], v0 offset:26880\n" \
  "ds_read_b128 v[32:35], v1 offset:12288\n" \
  "v_mfma_f32_16x16x32_bf16 v[42:45], v[20:23], v[104:107], v[42:45]\n" \
  "v_mfma_f32_16x16x32_bf16 v[48:51], v[20:23], v[168:171], v[48:51]\n" \
  "v_mfma_f32_16x16x32_bf16 v[54:57], v[20:23], v[232:235], v[54:57]\n" \
  "s_waitcnt lgkmcnt(4)\n" \
  "v_mfma_f32_16x16x32_bf16 v[60:63], v[20:23], v[36:39], v[60:63]\n" \
  "ds_read_b128 v[20:23], v0 offset:28928\n" \
  "ds_read_b128 v[36:39], v1 offset:13312\n" \
  "v_mfma_f32_16x16x32_bf16 v[42:45], v[24:27], v[108:111], v[42:45]\n" \
  "v_mfma_f32_16x16x32_bf16 v[48:51], v[24:27], v[172:175], v[48:51]\n" \
  "v_mfma_f32_16x16x32_bf16 v[54:57], v[24:27], v[236:239], v[54:57]\n" \
  "s_waitcnt lgkmcnt(4)\n" \
  "v_mfma_f32_16x16x32_bf16 v[60:63], v[24:27], v[28:31], v[60:63]\n" \
  "ds_read_b128 v[24:27], v0 offset:30976\n" \
  "ds_read_b128 v[28:31], v1 offset:14336\n" \
  "v_mfma_f32_16x16x32_bf16 v[42:45], v[12:15], v[112:115], v[42:45]\n" \
  "v_mfma_f32_16x16x32_bf16 v[48:51], v[12:15], v[176:179], v[48:51]\n" \
  "v_mfma_f32_16x16x32_bf16 v[54:57], v[12:15], v[240:243], v[54:57]\n" \
  "s_waitcnt lgkmcnt(4)\n" \
  "v_mfma_f32_16x16x32_bf16 v[60:63], v[12:15], v[32:35], v[60:63]\n" \
  "ds_read_b128 v[32:35], v1 offset:15360\n" \
  "v_mfma_f32_16x16x32_bf16 v[42:45], v[16:19], v[116:119], v[42:45]\n" \
  "v_mfma_f32_16x16x32_bf16 v[48:51], v[16:19], v[180:183], v[48:51]\n" \
  "v_mfma_f32_16x16x32_bf16 v[54:57], v[16:19], v[244:247], v[54:57]\n" \
  "s_waitcnt lgkmcnt(3)\n" \
  "v_mfma_f32_16x16x32_bf16 v[60:63], v[16:19], v[36:39], v[60:63]\n" \
  "ds_read_b128 v[36:39], v3 offset:64\n" \
  "v_mfma_f32_16x16x32_bf16 v[42:45], v[20:23], v[120:123], v[42:45]\n" \
  "v_mfma_f32_16x16x32_bf16 v[48:51], v[20:23], v[184:187], v[48:51]\n" \
  "v_mfma_f32_16x16x32_bf16 v[54:57], v[20:23], v[248:251], v[54:57]\n" \
  "s_waitcnt lgkmcnt(2)\n" \
  "v_mfma_f32_16x16x32_bf16 v[60:63], v[20:23], v[28:31], v[60:63]\n" \
  "ds_read_b128 v[28:31], v3 offset:192\n" \
  "s_mov_b64 exec, 1\n" \
  "ds_add_u32 v6, v7 offset:8\n" \
  "s_mov_b64 exec, -1\n" \
  "v_xor_b32 v6, 4, v6\n" \
  "v_mfma_f32_16x16x32_bf16 v[42:45], v[24:27], v[124:127], v[42:45]\n" \
  "v_mfma_f32_16x16x32_bf16 v[48:51], v[24:27], v[188:191], v[48:51]\n" \
  "v_mfma_f32_16x16x32_bf16 v[54:57], v[24:27], v[252:255], v[54:57]\n" \
  "s_waitcnt lgkmcnt(3)\n" \
  "v_mfma_f32_16x16x32_bf16 v[60:63], v[24:27], v[32:35], v[60:63]\n" \
  "s_waitcnt lgkmcnt(1)\n" \
  "s_nop 4\n" \
  "ds_read_b32 v5, v6 offset:0\n" \
  "v_fma_f32 v42, -v36, v8, v42\n" \
  "v_fma_f32 v43, -v37, v8, v43\n" \
  "v_fma_f32 v44, -v38, v8, v44\n" \
  "v_fma_f32 v45, -v39, v8, v45\n" \
  "v_mul_f32 v42, v42, v28\n" \
  "v_mul_f32 v43, v43, v29\n" \
  "v_mul_f32 v44, v44, v30\n" \
  "v_mul_f32 v45, v45, v31\n" \
  "v_cvt_pk_bf16_f32 v42, v42, v43\n" \
  "v_cvt_pk_bf16_f32 v43, v44, v45\n" \
  "buffer_store_dwordx4 v[40:43], v2, s[40:43], s65 offen offset:0 sc1\n" \
  "v_fma_f32 v48, -v36, v9, v48\n" \
  "v_fma_f32 v49, -v37, v9, v49\n" \
  "v_fma_f32 v50, -v38, v9, v50\n" \
  "v_fma_f32 v51, -v39, v9, v51\n" \
  "v_mul_f32 v48, v48, v28\n" \
  "v_mul_f32 v49, v49, v29\n" \
  "v_mul_f32 v50, v50, v30\n" \
  "v_mul_f32 v51, v51, v31\n" \
  "v_cvt_pk_bf16_f32 v48, v48, v49\n" \
  "v_cvt_pk_bf16_f32 v49, v50, v51\n" \
  "buffer_store_dwordx4 v[46:49], v2, s[40:43], s65 offen offset:1024 sc1\n" \
  "v_fma_f32 v54, -v36, v10, v54\n" \
  "v_fma_f32 v55, -v37, v10, v55\n" \
  "v_fma_f32 v56, -v38, v10, v56\n" \
  "v_fma_f32 v57, -v39, v10, v57\n" \
  "v_mul_f32 v54, v54, v28\n" \
  "v_mul_f32 v55, v55, v29\n" \
  "v_mul_f32 v56, v56, v30\n" \
  "v_mul_f32 v57, v57, v31\n" \
  "v_cvt_pk_bf16_f32 v54, v54, v55\n" \
  "v_cvt_pk_bf16_f32 v55, v56, v57\n" \
  "buffer_store_dwordx4 v[52:55], v2, s[40:43], s65 offen offset:2048 sc1\n" \
  "v_fma_f32 v60, -v36, v4, v60\n" \
  "v_fma_f32 v61, -v37, v4, v61\n" \
  "v_fma_f32 v62, -v38, v4, v62\n" \
  "v_fma_f32 v63, -v39, v4, v63\n" \
  "v_mul_f32 v60, v60, v28\n" \
  "v_mul_f32 v61, v61, v29\n" \
  "v_mul_f32 v62, v62, v30\n" \
  "v_mul_f32 v63, v63, v31\n" \
  "v_cvt_pk_bf16_f32 v60, v60, v61\n" \
  "v_cvt_pk_bf16_f32 v61, v62, v63\n" \
  "buffer_store_dwordx4 v[58:61], v2, s[40:43], s65 offen offset:3072 sc1\n" \
  "s_waitcnt lgkmcnt(0)\n" \
  "s_add_u32 s65, s65, 0x40000\n" \
  "s_sub_u32 s64, s64, 1\n" \
  "s_mov_b32 s68, 1\n" \
  "s_cmp_lg_u32 s64, 0\n" \
  "s_cbranch_scc1 .LCW_TOP_%=\n" \
  "s_waitcnt vmcnt(0)\n" \
  "s_mov_b64 exec, 1\n" \
  "buffer_store_dword v7, off, s[56:59], s72 sc1\n" \
  "s_mov_b64 exec, -1\n" \
  "s_add_u32 s72, s72, 0x1000\n" \
  "s_mov_b32 s68, 0\n" \
  ".LCW_END_%=:\n" \
  "s_endpgm\n"
constexpr int RG = 2;
#define RFL(x) __builtin_amdgcn_readfirstlane(x)
#define HALF(x, i) BF8(((i) ? __builtin_shufflevector(x, x, 4, 5, 6, 7) : __builtin_shufflevector(x, x, 0, 1, 2, 3)))
constexpr int P_W = 65536, P_ST = 131072, P_SS = P_ST + 1024, P_CN = P_SS + 512;
template <bool PR>
__device__ __forceinline__ void role_proj_t(const MegaArgs& A, char* smem, const int L, const int idx, const bool kv) {
  const int w = A.wv;
  const int l = lane_id();
  const int c = l & 15, g = l >> 4;
  char* ws = A.ws;
  unsigned* FL = (unsigned*)(ws + OFF_FL);
  unsigned* TMO = FL + FL_TMO;
  const unsigned sb = lds_base(smem);
  auto cn = (volatile __attribute__((address_space(3))) unsigned*)(unsigned long)(sb + P_CN);
  u64 tag;
  const bool h4 = kv && L;
  const __amdgpu_buffer_rsrc_t rsM = MKRS(ws + OFF_MEMK + L * SZ_MEM, kv && w == 2 ? (4 - L) * SZ_MEM : 0);
  const bool mc = kv && w == 2 && (MCE < 2 || !L);
  constexpr int NMC = MCE == 2 ? 8 : 4;
  const int b0 = RFL(h4 ? (H4S ? 2 : 3) * w : w == 2 ? 0 : HX0),
            b1 = RFL(h4 ? (H4S ? 2 * w + 2 : w == 2 ? 8 : 3 * w + 3 * (w < 2)) : w == 2 ? HX0 : 8);
  const __amdgpu_buffer_rsrc_t rsF = MKRS(FL + FL_FC + L * 64 * 32 * 32, 64 * 4096);
  const int fo = ((blockIdx.x & 3) * 8 + ((H4S && h4) || w == 3 ? l >> 3 : min(b0 + (l >> 3), b1 - 1))) * 128 + (l & 7) * 4;
#define FC_LOOK(s) __builtin_amdgcn_raw_buffer_load_b32(rsF, fo, (s) * 4096, AUX_SC1)
#define MEM_CLEAR(s)                                                                                                          \
  _Pragma("unroll") for (int i = 0; i < NMC; ++i)                                                                             \
    ST128((v4u{0u, 0u, 0u, 0u}), rsM, l * 16 + (i & 1) * 1024, (s) * 65536 + idx * 2048 + (i >> 1) * (int)SZ_MEM * (MCE == 2 ? 1 : 2));
  const char* Xb = ws + (L ? OFF_X3 : OFF_XA);
  const __amdgpu_buffer_rsrc_t rsS = MKRS(ws + OFF_H + L * (SZ_X / 2), SZ_X / 2);
  unsigned nf = 0u;
  bool early = false;
  v4u t0[16];
#define X_ASK(s) \
  for (int i = b0; i < b1; ++i) glds64c(Xb + RFL((s) * 32768 + i * 4096), l * 16, RFL(sb + ((s) & 1) * 32768 + i * 4096));
#define S_ASK(s, t) _Pragma("unroll") for (int i = 0; i < 16; ++i) t[i] = LD128(rsS, l * 16, (s) * 16384 + i * 1024);
  {
    const int j = w & 3;
    const bool vt = kv && j >= 2;
    constexpr int NT = PR ? 3 : 1;
    v8u wq[NT][8];
    const u16* Wm = (kv ? (PR ? A.Wv : A.Wk) : A.Wq) - (PR ? 128 : 0) * HD;
    const int hm = kv ? idx : 2 * idx + PR;
    const int q = (l >> 2) & 3, p = l & 3, f = q | ((g & 1) << 2);
    unsigned ra[NT];
#pragma unroll
    for (int m = 0; m < NT; ++m) {
      const int ct = PR ? m : 3;
      ra[m] = sb + (j >> 1) * 65536 + (8 * g + q) * 256 + ((4 * (j & 1) + (vt ? ct : (ct & 2) + (p >> 1))) ^ f) * 32 + (vt ? p * 8 : (p & 1) * 16 + (ct & 1) * 8);
    }
#define W_ISSUE(n) wqkv_issue<false>(Wm + (128 * (n) - 256 * ((n) & 1)) * HD, (n) & 1, hm, w, l, sb)
#define W_STAGE(n, WAIT, NEXT)                                                                             \
  {                                                                                                        \
    WAIT;                                                                                                  \
    if (n == 3 && EL == 2 && !PR && L == 0 && w >= 2) nf = FC_LOOK(0);                                     \
    if (n == 3 && EL == 1 && !PR && L == 0 && w >= 2) {                                                    \
      early = __all(nf != 0u);                                                                             \
      if (early) {                                                                                         \
        X_ASK(0)                                                                                           \
        if (w == 3) { S_ASK(0, t0) }                                                                       \
      }                                                                                                    \
      nf = FC_LOOK(early);                                                                                 \
    }                                                                                                      \
    RAW_BAR();                                                                                             \
    if (n == 3 && EL == 3 && !PR && L == 0 && w >= 2) nf = FC_LOOK(0);                                     \
    _Pragma("unroll") for (int m = 0; m < NT; ++m) {                                                       \
      _Pragma("unroll") for (int t = 0; t < 2; ++t) {                                                      \
        const unsigned a_ = ra[m] + ((n) & 1) * 32768 + t * 16384;                                         \
        const v2u r0 = trd(a_), r1 = trd(a_ + 1024), r2 = trd(a_ + 8192), r3 = trd(a_ + 8192 + 1024);      \
        wq[m][2 * (n) + t] = v8u{r0.x, r0.y, r1.x, r1.y, r2.x, r2.y, r3.x, r3.y};                          \
      }                                                                                                    \
    }                                                                                                      \
    _Pragma("unroll") for (int m = 0; m < 2 * NT; ++m) asm volatile("" : "+v"(wq[m >> 1][2 * (n) + (m & 1)])); \
    RAW_BAR();                                                                                             \
    NEXT;                                                                                                  \
    _Pragma("unroll") for (int m = 0; m < 4 * NT; ++m)                                                     \
      cs[m % NT] = MFMA16(HALF(wq[m % NT][2 * (n) + m / NT / 2], (m / NT) & 1), BF8(one), cs[m % NT]);     \
    _Pragma("unroll") for (int m = 0; m < NT; ++m) asm volatile("" : "+v"(cs[m]));                         \
  }
    __builtin_amdgcn_s_waitcnt(0x0f70);
    if (EL && !MC3 && mc) { MEM_CLEAR(0) }
    W_ISSUE(0);
    W_ISSUE(1);
    tag = call_tag(A);
    if (w == 0 && l < 8) cn[l] = l == 1 ? 3u : l == 5 ? 2u : 0u;
    const v4u one = {0x3f803f80u, 0x3f803f80u, 0x3f803f80u, 0x3f803f80u};
    f32x4 cs[NT] = {};
    u64 rv = 0;
    W_STAGE(0, WAIT_VM(8), W_ISSUE(2); if (EL) RD_ASK(rv));
    SST(4);
    W_STAGE(1, if (EL) WAIT_VM(9); else WAIT_VM(8), W_ISSUE(3); if (EL != 0 && MC3 != 0 && mc) { MEM_CLEAR(0) if (MC3 == 2) { MEM_CLEAR(1) } });
    SST(5);
    W_STAGE(2, if (EL != 0 && MC3 == 2 && mc) WAIT_VM(16); else if (EL != 0 && MC3 != 0 && mc) WAIT_VM(12); else WAIT_VM(8), (void)0);
    SST(6);
    asm volatile("" : "+v"(rv));
    if (!EL) ready_wait(A, tag, ready_look(A));
    for (unsigned spins = 0; EL && !__all(rv == tag) && spins < SPIN_CAP; ++spins) {
      __builtin_amdgcn_s_sleep(8);
      RD_ASK(rv);
      asm volatile("s_waitcnt vmcnt(0)" : "+v"(rv)::"memory");
    }
    if (EL == 1 && !PR && L == 0 && w >= 2) nf = FC_LOOK(0);
    W_STAGE(3, if (EL >= 2 && MC3 == 2 && mc) WAIT_VM(8); else if (EL >= 2 && MC3 == 1 && mc) WAIT_VM(4); else WAIT_VM(0), if (!PR) {
      _Pragma("unroll") for (int ks = 0; ks < 16; ++ks)
        *reinterpret_cast<bf16x8*>(smem + P_W + j * 16384 + ks * 1024 + l * 16) = HALF(wq[0][ks >> 1], ks & 1);
    });
    SST(7);
#undef W_STAGE
#undef W_ISSUE
#pragma unroll
    for (int m = 0; m < NT; ++m) *reinterpret_cast<f32x4*>(smem + P_ST + j * 256 + (PR ? m : 3) * 64 + g * 16) = cs[m];
    RAW_BAR();
    SST(8);
    if constexpr (PR) {
      const v4u rsO = mk_srd(ws + RFL((unsigned)(vt ? OFF_VF + L * SZ_O : OFF_QK + L * SZ_QK)), RFL((unsigned)(vt ? SZ_O : SZ_QK)));
      const v4u rsP = mk_srd(FL + RFL(kv ? FL_FQ + (L * 64 * 32 + idx) * 32 + 2 * j : FL_FP + (L * 16 + idx) * 32 + j), 63 * 4096 + 4);
      const int so = RFL(vt ? idx * 8 + 4 * (j & 1) : (kv ? idx * 16 + 8 : (2 * idx + (j >> 1)) * 16) + 2 * (j & 1));
      const v8u pv = {sb + 32768 + g * 512 + c * 16, sb + P_W + j * 16384 + l * 16, (unsigned)l * 16, sb + P_SS + 256 + (vt ? g * 16 : c * 4),
                      sb + P_ST + j * 256 + (vt ? c * 4 : g * 16), 0u, 0u, 0u};
      const char* dbg = nullptr;
#define P_OPS                                                                                                                               \
  ::"{v[64:71]}"(wq[0][0]), "{v[72:79]}"(wq[0][1]), "{v[80:87]}"(wq[0][2]), "{v[88:95]}"(wq[0][3]), "{v[96:103]}"(wq[0][4]),                \
      "{v[104:111]}"(wq[0][5]), "{v[112:119]}"(wq[0][6]), "{v[120:127]}"(wq[0][7]), "{v[128:135]}"(wq[1][0]), "{v[136:143]}"(wq[1][1]),     \
      "{v[144:151]}"(wq[1][2]), "{v[152:159]}"(wq[1][3]), "{v[160:167]}"(wq[1][4]), "{v[168:175]}"(wq[1][5]), "{v[176:183]}"(wq[1][6]),     \
      "{v[184:191]}"(wq[1][7]), "{v[192:199]}"(wq[2][0]), "{v[200:207]}"(wq[2][1]), "{v[208:215]}"(wq[2][2]), "{v[216:223]}"(wq[2][3]),     \
      "{v[224:231]}"(wq[2][4]), "{v[232:239]}"(wq[2][5]), "{v[240:247]}"(wq[2][6]), "{v[248:255]}"(wq[2][7]), "{v[0:7]}"(pv),               \
      "{s[40:43]}"(rsO), "{s[56:59]}"(rsP), "{s64}"(NSEG), "{s65}"(so * 1024), "{s70}"(sb + P_CN), "{s[36:37]}"(dbg)                        \
      : "memory", "vcc", "scc", "s80", "v63"
      if (vt) asm volatile("s_mov_b64 exec, -1\ns_waitcnt vmcnt(0) lgkmcnt(0)\ns_nop 4\n" V_ASM P_OPS);
      else asm volatile("s_mov_b64 exec, -1\ns_waitcnt vmcnt(0) lgkmcnt(0)\ns_nop 4\n" P_ASM P_OPS);
#undef P_OPS
      return;
    }
  }
  __builtin_amdgcn_s_setprio(RPRIO);
  if (h4 || w >= 2) {
    if (!EL && mc) { MEM_CLEAR(0) }
    auto stats = [&](const v4u (&t)[16], const int s) {
      float a = 0.f, b = 0.f, qa = 0.f, qb = 0.f;
#pragma unroll
      for (int i = 0; i < 16; ++i) {
        a = one(a + __uint_as_float(t[i].x));
        b = one(b + __uint_as_float(t[i].z));
        qa = one(qa + __uint_as_float(t[i].y));
        qb = one(qb + __uint_as_float(t[i].w));
      }
      a = xsum32(xsum16(a));
      b = xsum32(xsum16(b));
      float da = 0.f, db = 0.f;
#pragma unroll
      for (int i = 0; i < 16; ++i) {
        const float u = one(__uint_as_float(t[i].x) - a * (1.0f / 64)), v = one(__uint_as_float(t[i].z) - b * (1.0f / 64));
        da = one(da + u * u);
        db = one(db + v * v);
      }
      const float var = xsum32(swap_add16(qa + da * 0.125f, qb + db * 0.125f)) * (1.0f / DM);
      const float ri = 1.0f / bfr1(sqrtf(bfr1(bfr1(var) + 1e-5f)));
      reinterpret_cast<float*>(smem + P_SS)[(s & 1) * 64 + (g >> 1) * 32 + 2 * c + (g & 1)] = g < 2 ? bfr1(((g & 1) ? b : a) * (1.0f / DM)) : ri;
      asm volatile("" ::: "memory");
      if (l == 0) cn[4 + (s & 1)] = 2 * s + 4;
    };
    static_assert(EL != 1 || MC3 == 2, "clear");
    if (early) {
      if (w == 3) WAIT_VM(17);
      else WAIT_VM(1);
      if (l == 0) __hip_atomic_fetch_add((__attribute__((address_space(3))) unsigned*)cn, 3u, __ATOMIC_RELAXED, __HIP_MEMORY_SCOPE_WORKGROUP);
      SSX(10, w == 2);
      SSX(9, w == 2);
      if (w == 3) stats(t0, 0);
      SSX(11, w == 3);
    }
    unsigned tn = now10(), tg = tn;
    for (int s = early; s < NSEG; ++s) {
      for (unsigned spins = 0; !__all(nf != 0u); ++spins) {
        if (spin_timeout(spins, TMO)) return;
        if (spins) __builtin_amdgcn_s_sleep(SLP_H);
        else if (GF_H && (h4 || (GF_P && L)) && s >= GF0 && s < NSEG - 6) gate(tg);
        tg = now10() + GF_H;
        nf = FC_LOOK(s);
        asm volatile("" ::: "memory");
      }
      if (NFW) asm volatile("" ::"v"(nf));
      TS(4, s, 128);
      for (unsigned spins = 0; cn[2 + (s & 1)] < 4u * (s >> 1); ++spins) {
        if (spins > 64u * SPIN_CAP) return;
        __builtin_amdgcn_s_sleep(2);
      }
      if (L == 0 && s >= PACE0) gate(tn);
      else tn = now10();
      tn += PACE;
      TS(5, s, 128);
      X_ASK(s)
      if (H4S && h4) {
        v4u t[4];
#pragma unroll
        for (int i = 0; i < 4; ++i) t[i] = LD128(rsS, (l >> 2) * 256 + (l & 3) * 16 + w * 64, s * 16384 + i * 4096);
        nf = 0u;
        if (MCE == 1 && w == 2 && s < NSEG - 1) {
          MEM_CLEAR(s + 1)
          WAIT_VM(8);
        } else {
          WAIT_VM(4);
        }
        if (l == 0) __hip_atomic_fetch_add((__attribute__((address_space(3))) unsigned*)cn + (s & 1), w < 2 ? 2u : 1u, __ATOMIC_RELAXED, __HIP_MEMORY_SCOPE_WORKGROUP);
        TS(6, s, 128);
#define F(x) __uint_as_float(x)
        const float a = red_mod4(one(F(t[0].x) + F(t[1].x)) + one(F(t[2].x) + F(t[3].x)));
        const float b = red_mod4(one(F(t[0].z) + F(t[1].z)) + one(F(t[2].z) + F(t[3].z)));
        float da = 0.f, db = 0.f;
#pragma unroll
        for (int i = 0; i < 4; ++i) {
          const float u = one(F(t[i].x) - a * (1.0f / 64)), v = one(F(t[i].z) - b * (1.0f / 64));
          da = one(da + u * u);
          db = one(db + v * v);
        }
        const float qa = one(F(t[0].y) + F(t[1].y)) + one(F(t[2].y) + F(t[3].y)), qb = one(F(t[0].w) + F(t[1].w)) + one(F(t[2].w) + F(t[3].w));
#undef F
        float var = xsum32(swap_add16(one(qa + da * 0.125f), one(qb + db * 0.125f)));
        var += dppr<0x124>(var);
        var += dppr<0x128>(var);
        const float ri = 1.0f / bfr1(sqrtf(bfr1(bfr1(var * (1.0f / DM)) + 1e-5f)));
        if (c < 4) reinterpret_cast<float*>(smem + P_SS)[(s & 1) * 64 + (g >> 1) * 32 + 8 * w + 2 * c + (g & 1)] = g < 2 ? bfr1(((g & 1) ? b : a) * (1.0f / DM)) : ri;
        asm volatile("" ::: "memory");
        if (l == 0) __hip_atomic_fetch_add((__attribute__((address_space(3))) unsigned*)cn + 4 + (s & 1), 1u, __ATOMIC_RELAXED, __HIP_MEMORY_SCOPE_WORKGROUP);
        TS(7, s, 192);
        if (!MCE && w == 2 && s < NSEG - 1) { MEM_CLEAR(s + 1) }
      } else if (w != 3) {
        nf = L ? 0u : FC_LOOK(s + 1);
        if (mc && s < NSEG - 1 && (EL == 0 || MC3 != 2 || s)) {
          MEM_CLEAR(s + 1)
          if (NMC == 8) WAIT_VM(8);
          else WAIT_VM(4);
        } else {
          WAIT_VM(0);
        }
        if (l == 0) __hip_atomic_fetch_add((__attribute__((address_space(3))) unsigned*)cn + (s & 1), h4 ? 2u : 3u, __ATOMIC_RELAXED, __HIP_MEMORY_SCOPE_WORKGROUP);
        TS(6, s, 128);
        SSX(10, s == 0 && w == 2);
      } else {
        v4u t[16];
        S_ASK(s, t)
        WAIT_VM(16);
        nf = L ? 0u : FC_LOOK(s + 1);
        if (l == 0 && !h4) __hip_atomic_fetch_add((__attribute__((address_space(3))) unsigned*)cn + (s & 1), 3u, __ATOMIC_RELAXED, __HIP_MEMORY_SCOPE_WORKGROUP);
        stats(t, s);
        SSX(11, s == 0);
        TS(7, s, 192);
      }
    }
    return;
  }
  const int RL = !kv, rp = idx & 15, u = RFL(kv ? idx >> 4 : L);
  const __amdgpu_buffer_rsrc_t rsW = MKRS(ws + OFF_PW, SZ_PW);
  const __amdgpu_buffer_rsrc_t rsX = MKRS(ws + (RL ? OFF_X3 : OFF_XA), SZ_X);
  const __amdgpu_buffer_rsrc_t rsY = RL ? MKRS(A.out, SZ_X) : MKRS(ws + OFF_X2, SZ_X);
  const int r = 2 * rp + (g >> 1), yo = r * (DM * 2) + (l & 31) * 16 + u * 512;
  if (kv) {
    if (XZD) gate(now10() + XZD);
    if (XZP & 3) {
      if (!wait_n<SLP_V, 32>(FL + FL_FZ + (blockIdx.x & 7) * 64, 64, TMO)) return;
    } else {
      for (int s = w; s < (XZP ? 2 * RG : NSEG); s += RG) ST128((v4u{0u, 0u, 0u, 0u}), rsY, yo, s * SEG * DM * 2);
      DRAIN();
      if (l < 8) set_flag(FL + FL_FZ + l * 64 + 2 * idx + w);
    }
  }
  v4u sel;
#pragma unroll
  for (int e = 0; e < 4; ++e) sel[e] = (g >> 1) == (c >> 3) && ((c & 7) >> 1) == e ? 0x3f80u << (16 * (c & 1)) : 0u;
  unsigned tl = now10();
  for (int s = w; s < NSEG; s += RG) {
    if (GF_R) {
      gate(tl);
      const gu32* f = (gu32*)(FL + FL_FW) + ((RL * 64 + s) * 4 + (blockIdx.x & 3)) * 4 * 32 + (c >> 2) * 32 + (c & 3);
      for (unsigned spins = 0;; ++spins) {
        tl = now10() + GF_R;
        if (__all(__hip_atomic_load(f, RLX_AGENT) != 0u) || spin_timeout(spins, TMO)) break;
        __builtin_amdgcn_s_sleep(SLP_R);
      }
      asm volatile("" ::: "memory");
    } else
    grp_wait<SLP_R, 4, GATE_R>(true, nullptr, 0, FL + FL_FW + ((RL * 64 + s) * 4 + (blockIdx.x & 3)) * 4 * 32, 16, TMO, tl);
    if (s >= NSEG - 6) tl = now10();
    DRAIN();
    TS(0, s, w * 64);
    v4u p[8][2];
    const v4u xr = LD128(rsX, (l & 31) * 512 + r * 16, s * 32768 + u * 16384);
#pragma unroll
    for (int k = 0; k < 8; ++k)
#pragma unroll
      for (int b = 0; b < 2; ++b)
        p[k][b] = LD128(rsW, (g & 1) * 32768 + (c >> 2) * 128 + (g >> 1) * 64 + (c & 3) * 16 + b * 512, ((RL * 64 + s) * 16 + 2 * k) * 32768 + rp * 2048 + u * 1024);
    __builtin_amdgcn_sched_barrier(0);
    f32x4 ac[2] = {};
#pragma unroll
    for (int k = 0; k < 8; ++k)
#pragma unroll
      for (int b = 0; b < 2; ++b) ac[b] = MFMA16(BF8(sel), BF8(p[k][b]), ac[b]);
    v4u xo;
    {
      const auto x = __builtin_amdgcn_permlane16_swap(pk2s(ac[0][0], ac[0][1]), pk2s(ac[1][0], ac[1][1]), false, false);
      const auto y = __builtin_amdgcn_permlane16_swap(pk2s(ac[0][2], ac[0][3]), pk2s(ac[1][2], ac[1][3]), false, false);
      const v4u a = {x[0], y[0], x[1], y[1]};
#pragma unroll
      for (int e = 0; e < 4; ++e) {
        xo[e] = pk2s(lo16(xr[e]) + lo16(a[e]), hi16(xr[e]) + hi16(a[e]));
        if (kv) xo[e] = xo[e] ? xo[e] : 0x8000u;
      }
      ST128(xo, rsY, yo, s * SEG * DM * 2);
    }
    TS(1, s, w * 64);
    if (kv) {
      const float pv = lo16(g < 2 ? __builtin_amdgcn_readlane(xo[0], 0) : __builtin_amdgcn_readlane(xo[0], 32));
      float sm = 0.f, q0 = 0.f, q1 = 0.f;
#pragma unroll
      for (int e = 0; e < 4; ++e) {
        const float a = one(lo16(xo[e]) - pv), b = one(hi16(xo[e]) - pv);
        sm = one(sm + one(a + b));
        q0 = one(q0 + a * a);
        q1 = one(q1 + b * b);
      }
      sm = half_sum(sm);
      const float q = fmaxf(one(half_sum(q0 + q1)) - sm * sm * (1.0f / 256), 0.f);
      const u64 st = (u64)__float_as_uint(sm + pv * 256.f) | (u64)(__float_as_uint(q) | 1u) << 32;
      if ((l & 31) < 4) __hip_atomic_store((gu64*)(FL + FL_FR + ((s * 4 + (l & 3)) * 4 + (r >> 3)) * 32 + (r & 7) * 4 + 2 * u), st, RLX_AGENT);
      if (XZP == 4 && s + 2 * RG < NSEG) ST128((v4u{0u, 0u, 0u, 0u}), rsY, yo, (s + 2 * RG) * SEG * DM * 2);
    }
    TS(3, s, w * 64);
  }
  if (L == 1 && idx == 0 && w == 0 && l == 0) __hip_atomic_store((gu64*)(ws + OFF_EP), tag, RLX_AGENT);
}
__device__ __forceinline__ void role_proj(const MegaArgs& A, char* smem, const int L, const int idx, const bool kv) {
  if (A.wv < 4) role_proj_t<false>(A, smem, L, idx, kv);
  else role_proj_t<true>(A, smem, L, idx, kv);
}

constexpr int CUN = 2144;
__device__ __forceinline__ v2u part_stats(const v4u o) {
  float v[8];
#pragma unroll
  for (int e = 0; e < 4; ++e) { v[2 * e] = lo16(o[e]); v[2 * e + 1] = hi16(o[e]); }
  const float s = ((v[0] + v[1]) + (v[2] + v[3])) + ((v[4] + v[5]) + (v[6] + v[7])), m = s * 0.125f;
  float q0 = 0.f, q1 = 0.f;
#pragma unroll
  for (int e = 0; e < 4; ++e) {
    const float a = v[e] - m, b = v[4 + e] - m;
    q0 += a * a;
    q1 += b * b;
  }
  return v2u{__float_as_uint(s), __float_as_uint(q0 + q1)};
}
constexpr int CRN = 176;
constexpr int CTN = 272;
constexpr int CV_LDS = (8 * CKN + 8 * 2 * CUN + 64 + 2 * 32 * 8 + 16 * CRN + 64 + 8 * CTN) * 2 + 8 * 1568;
__device__ __forceinline__ void role_conv(const MegaArgs& A, char* smem, const int cv) {
  u16* Kr = reinterpret_cast<u16*>(smem);
  u16* Uc = Kr + 8 * CKN;
  u16* Us = Uc + 8 * CUN + 63;
  u16* To = Uc + 8 * 2 * CUN + 64;
  u16* Sx = To + 32 * 8;
  u16* Rc = Sx + 32 * 8;
  u16* Rs = Rc + 8 * CRN + 63;
  u16* Ta = Rc + 16 * CRN + 64;
  const int w = A.wv;
  const int l = lane_id(), tid = 64 * w + l, c = l & 15, g = l >> 4;
  const int d = 8 * cv + w;
  char* ws = A.ws;
  unsigned* FL = (unsigned*)(ws + OFF_FL);
  unsigned* TMO = FL + FL_TMO;
  u16* kr = Kr + w * CKN;
  const u16* ka = kr + 2080 - 16 * c + 8 * g;
#define KFRAG(D0) (*reinterpret_cast<const f16x8*>(ka - (D0)))
  typedef __attribute__((address_space(3))) const unsigned* lpu;
#define BFRAG(a) __builtin_bit_cast(f16x8, (v4u{((lpu)(unsigned long)(a))[0], ((lpu)(unsigned long)(a))[1], ((lpu)(unsigned long)(a))[2], ((lpu)(unsigned long)(a))[3]}))
  f32x4 acc[8];
  v4u tx[4];
  const __amdgpu_buffer_rsrc_t rz1 = MKRS(ws + OFF_XA, SZ_X), rps = MKRS(ws + OFF_H, SZ_X);
  u16* uc = Uc + w * CUN;
  const unsigned ub = lds_base(reinterpret_cast<char*>(((c & 1) ? Us : Uc) + w * CUN + c + 8 * g + 32));
  const int t0 = 16 * c + 4 * g;
  float dsk, ysc;
#define SKIP(T) (*reinterpret_cast<const v2u*>(uc + 32 + 256 * (T) + t0))
#define TILE_OUT(dst, a, uu)                                                                   \
  {                                                                                            \
    v2u v_;                                                                                    \
    _Pragma("unroll") for (int e = 0; e < 2; ++e) {                                            \
      const unsigned yy = pk2(a[2 * e] * ysc, a[2 * e + 1] * ysc), u_ = uu[e];                 \
      const unsigned k_ = pk2(h2f((u16)u_) * dsk, h2f((u16)(u_ >> 16)) * dsk);                 \
      v_[e] = pk2(lo16(yy) + lo16(k_), hi16(yy) + hi16(k_));                                   \
    }                                                                                          \
    *reinterpret_cast<v2u*>((dst) + t0) = v_;                                                  \
  }
#define CHUNK_ST(i, src, N) CHUNK_STX(i, tx[i], src, N)
#define CHUNK_STX(i, X, src, N)                                                                \
  {                                                                                            \
    const unsigned p_ = lds_base((char*)((src) + (c >> 2) * (N) + (tid & ~15) + 4 * (c & 3))); \
    const v2u a0_ = trd(p_), a1_ = trd(p_ + 8 * (N));                                          \
    const v4u a_ = {a0_.x, a0_.y, a1_.x, a1_.y};                                               \
    v4u o;                                                                                     \
    _Pragma("unroll") for (int e = 0; e < 4; ++e) o[e] = pk2(lo16(X[e]) + lo16(a_[e]), hi16(X[e]) + hi16(a_[e])); \
    ST128(o, rz1, (l >> 5) * 32768 + (l & 31) * 16, (2 * (w + 8 * (i)) * 64 + cv) * 512);      \
    ST64(part_stats(o), rps, (l >> 5) * 16384 + (l & 31) * 8, (2 * (w + 8 * (i)) * 64 + cv) * 256); \
  }
#define CHUNK_FLAG(i) set_flag(FL + FL_FC + (((2 * w + 16 * ((i) + (l >> 3)) + ((l >> 2) & 1)) * 4 + (l & 3)) * 8 + (cv >> 3)) * 32 + (cv & 7))
  {
#define U_VAL(i)                                                                               \
  {                                                                                            \
    const float mb_ = lo16(st[i]), ri_ = 1024.0f / hi16(st[i]);                                 \
    _Pragma("unroll") for (int e = 0; e < 4; ++e) {                                            \
      const unsigned t_ = pk2(lo16(tx[i][e]) - mb_, hi16(tx[i][e]) - mb_);                     \
      const unsigned q_ = pk2(lo16(t_) * ri_, hi16(t_) * ri_);                                 \
      uv[e] = pkh(lo16(q_), hi16(q_));                                                         \
    }                                                                                          \
  }
    unsigned ta[3], k0p, st[4];
    v4u uv;
    v2u kn;
    u64 tag;
    const __amdgpu_buffer_rsrc_t rst = MKRS(ws + OFF_GT, (L_SEQ + 512) * 16);
    {
      KGen G;
      G.load(A, d, l);
      const u16 pk = A.Dskip[d];
      asm volatile("" ::: "memory");
      if (XL == 1) ask_slow(A, 16 * (w - 4) + (cv & 15), ta);
      if (XL) asm volatile("" : "+v"(G.pa), "+v"(G.pb), "+v"(G.pc), "+v"(G.pd), "+v"(G.pe)::"memory");
      tx[0] = *reinterpret_cast<const v4u*>(A.x0 + (size_t)tid * DM + 8 * cv);
      if (XL != 1) ask_slow(A, 16 * (w - 4) + (cv & 15), ta);
      tag = call_tag(A);
      G.setup();
      dsk = bf2f(pk) * (1.0f / 1024);
      G.st = smem + w * 16640;
      SST(0);
      ysc = G.scale();
      const Cx z = G.power(1.f);
      Cx y = cmul(G.C, z);
      G.rows(G.st + l * 8, y, z, 16);
      G.get(G.bh, G.bl, l);
      const float k0 = 2.0f * wave_sum(G.C.r.x + G.C.r.y);
      k0p = l == 0 ? pkh(k0, 0.f) : 0u;
      if (TP) {
        constexpr int R = 64 * TP, U0 = 2 * R + 32, U1 = 3 * R + 111, KN = 4 * R + 192;
        u16* k1 = reinterpret_cast<u16*>(smem + 8 * 16640) + w * KN;
        u16* t1 = reinterpret_cast<u16*>(smem + 8 * 16640) + 8 * KN;
        v4u s1 = {};
        u64 rv = 0;
        if (w < TP) {
          s1 = LD128(rst, tid * 16, (L_SEQ + R * (cv >> 4)) * 16);
          rv = ready_look(A);
        }
        if (l < R / 8 + 4) reinterpret_cast<v4u*>(k1)[l < R / 8 ? l + R / 8 + 4 : l - R / 8] = v4u{k0p, 0u, 0u, 0u};
        k1[U0 + (l < 32 ? l : R + l)] = k1[U1 + (l < 32 ? l : R + l)] = 0;
        G.columns(l, 0, R / 16);
        const f32x4 kt = G.tile<true>(l);
        if (c < R / 16) *reinterpret_cast<v2u*>(k1 + R + 28 - 16 * c - 4 * g) = v2u{pkh(kt.w, kt.z), pkh(kt.y, kt.x)};
        if (w < TP) {
          for (unsigned spins = 0; !__all(has_tag(tag, s1.x, s1.y) && has_tag(tag, s1.z, s1.w)) && spins < SPIN_CAP; ++spins) {
            __builtin_amdgcn_s_sleep(4);
            s1 = LD128(rst, tid * 16, (L_SEQ + R * (cv >> 4)) * 16);
          }
          st[0] = __builtin_amdgcn_perm(s1.w, s1.y, 0x07060302);
          U_VAL(0);
          u16* up = reinterpret_cast<u16*>(smem + 8 * 16640) + 32 + tid;
#pragma unroll
          for (int e = 0; e < 8; ++e) up[e * KN + U0] = up[e * KN + U1] = (u16)(uv[e >> 1] >> (16 * (e & 1)));
        }
        RAW_BAR();
        const unsigned u1 = lds_base(reinterpret_cast<char*>(k1 + ((c & 1) ? U1 : U0) + c + 8 * g));
        f32x4 a = {};
#pragma unroll
        for (int b = 0; b <= R / 32; ++b)
          a = MFMAH(BFRAG(u1 + 64 * b), *reinterpret_cast<const f16x8*>(k1 + R - 16 * (c & (R / 16 - 1)) + 8 * g + 32 * b), a);
        if (c < R / 16) {
          const v2u sk = *reinterpret_cast<const v2u*>(k1 + U0 + 32 + t0);
          TILE_OUT(t1 + w * (R + 16), a, sk);
        }
        RAW_BAR();
        SST(1);
        if (w < TP) CHUNK_ST(0, t1, R + 16);
        G.columns(l, R / 16, TPS);
        if (w < TP) {
          DRAIN();
          ready_wait(A, tag, rv);
          if (l < 8) CHUNK_FLAG(0);
        }
        SST(6);
        G.columns(l, TPS, 16);
      } else {
        G.columns(l, 0, 16);
      }
      const f32x4 kt = G.tile<true>(l);
      kn = v2u{pkh(kt.w, kt.z), pkh(kt.y, kt.x)};
    }
#define U_PUT(i)                                                                               \
  _Pragma("unroll") for (int e = 0; e < 4; ++e) {                                              \
    ((lph)(unsigned long)up0)[(2 * e) * CUN + 512 * (i)] = (u16)uv[e];                         \
    ((lph)(unsigned long)up0)[(2 * e + 1) * CUN + 512 * (i)] = (u16)(uv[e] >> 16);             \
    ((lph)(unsigned long)up1)[(2 * e) * CUN + 512 * (i)] = (u16)uv[e];                         \
    ((lph)(unsigned long)up1)[(2 * e + 1) * CUN + 512 * (i)] = (u16)(uv[e] >> 16);             \
  }
#define U_ROW(i) { U_VAL(i) U_PUT(i) }
    typedef __attribute__((address_space(3))) u16* lph;
    unsigned up0 = lds_base((char*)(Uc + 32 + tid)), up1 = lds_base((char*)(Us + 32 + tid));
    asm volatile("" : "+v"(up0), "+v"(up1), "+v"(kn), "+v"(k0p));
#define ST_GET(I0, I1)                                                                                                        \
  for (unsigned spins = 0; spins < SPIN_CAP; ++spins) {                                                                       \
    bool ok = true;                                                                                                           \
    v4u t_[4];                                                                                                                \
    _Pragma("unroll") for (int i = I0; i < I1; ++i) t_[i] = LD128(rst, (tid + 512 * i) * 16, 0);                              \
    _Pragma("unroll") for (int i = I0; i < I1; ++i) {                                                                         \
      ok = ok && has_tag(tag, t_[i].x, t_[i].y) && has_tag(tag, t_[i].z, t_[i].w);                                            \
      st[i] = __builtin_amdgcn_perm(t_[i].w, t_[i].y, 0x07060302);                                                            \
    }                                                                                                                         \
    if (__all(ok)) break;                                                                                                     \
    __builtin_amdgcn_s_sleep(4);                                                                                              \
    asm volatile("" ::: "memory");                                                                                            \
  }
    if (w < 4) {
      ST_GET(0, 2);
      U_VAL(0);
      asm volatile("" : "+v"(uv));
    }
#pragma unroll
    for (int i = 1; i < 4; ++i) tx[i] = *reinterpret_cast<const v4u*>(A.x0 + (size_t)(tid + 512 * i) * DM + 8 * cv);
    RAW_BAR();
    SST(2);
    if (w < 4) { U_PUT(0) }
    *reinterpret_cast<v2u*>(kr + 2076 - 16 * c - 4 * g) = kn;
    if (l < 38) reinterpret_cast<v4u*>(kr)[l < 32 ? 260 + l : l < 34 ? 194 + l : l - 34] = v4u{k0p, 0u, 0u, 0u};
    (l < 32 ? uc : Us + w * CUN - 32)[l] = 0;
    uc[32 + L_SEQ + l] = Us[w * CUN + 32 + L_SEQ + l] = 0;
    uc[32 + 256 + l] = Us[w * CUN + 32 + 256 + l] = 0;
    RAW_BAR();
    SST(11);
    {
      f32x4 a0 = {}, a1 = {};
      f16x8 bf[9], af[9];
#pragma unroll
      for (int b = 0; b < 9; ++b) {
        af[b] = KFRAG(32 - 32 * b);
        bf[b] = BFRAG(ub + 64 * (b - 1));
      }
      const v2u k0 = SKIP(0);
      __builtin_amdgcn_sched_barrier(0);
#pragma unroll
      for (int b = 0; b < 9; ++b) (b & 1 ? a1 : a0) = MFMAH(bf[b], af[b], (b & 1 ? a1 : a0));
      a0 += a1;
      TILE_OUT(Ta + w * CTN, a0, k0);
    }
    const u64 rv = ready_look(A);
    RAW_BAR();
    SST(3);
    if (w < 4) {
      if (w >= TP) CHUNK_ST(0, Ta, CTN);
      asm volatile("" : "+v"(tx[1]));
      U_ROW(1);
      DRAIN();
      ready_wait(A, tag, rv);
      if (l < 8 && w >= TP) CHUNK_FLAG(0);
      ST_GET(2, 4);
    } else {
      ST_GET(0, 4);
      ready_wait(A, tag, rv);
      asm volatile("" : "+v"(tx[0]), "+v"(tx[1]));
      U_ROW(0);
      U_ROW(1);
    }
    ASKED(ta);
    if (tid < (16 * CRN + 64) / 8) reinterpret_cast<v4u*>(Rc)[tid] = v4u{0u, 0u, 0u, 0u};
    SST(4);
    const gu64* fk = (gu64*)(ws + OFF_KG) + cv * 16 + w;
    u64 kg = __hip_atomic_load(fk, RLX_AGENT);
    U_ROW(2);
    for (unsigned spins = 0; !__all(kg == tag) && spins < SPIN_CAP; ++spins) {
      __builtin_amdgcn_s_sleep(8);
      kg = __hip_atomic_load(fk, RLX_AGENT);
    }
    asm volatile("" : "+v"(tx[3]), "+v"(st[3])::"memory");
    SST(5);
    const unsigned* kd = reinterpret_cast<const unsigned*>(Ta + 8 * CTN) + w * 392;
    glds16c(ws + OFF_KF + d * 2048, l * 16, lds_base((char*)kd));
    if (l < 34) glds16c(ws + OFF_KF + d * 2048 + 1024, l * 16, lds_base((char*)kd) + 1024);
    U_ROW(3);
    DRAIN();
    {
#define MIX(lo, hi) __builtin_amdgcn_perm(hi, lo, 0x07060100)
      const unsigned a0 = kd[384 - l], b0 = kd[383 - l], a1 = kd[320 - l], b1 = kd[319 - l];
      const unsigned a2 = kd[256 - 2 * l], m2 = kd[255 - 2 * l], b2 = kd[254 - 2 * l];
      const unsigned* k3 = kd + 124 - 4 * (l & 31);
      v4u* ko = reinterpret_cast<v4u*>(kr) + l;
      ko[4] = v4u{a0, a0, (l & 1) ? b0 : MIX(a0, b0), b0};
      ko[68] = v4u{a1, a1, (l & 1) ? b1 : MIX(a1, b1), b1};
      ko[132] = v4u{a2, MIX(a2, m2), m2, b2};
      if (l < 32) ko[196] = v4u{k3[4], MIX(k3[3], k3[2]), k3[2], MIX(k3[1], k3[0])};
#undef MIX
    }
    RAW_BAR();
    SST(7);
    {
      unsigned uq[4] = {ub, ub + 1024, ub + 2048, ub + 3072};
      f16x8 bf[8], a[4];
      const f16x8 bm = BFRAG(ub - 64);
      asm volatile("" : "+v"(uq[0]), "+v"(uq[1]), "+v"(uq[2]), "+v"(uq[3]));
#pragma unroll
      for (int j = 1; j < 8; ++j) bf[j] = BFRAG(uq[j >> 1] + 512 * (j & 1));
#pragma unroll
      for (int T = 1; T < 8; ++T) acc[T] = MFMAH(bm, KFRAG(256 * T + 32), (f32x4{}));
#pragma unroll
      for (int e = 0; e < 3; ++e) a[e] = KFRAG(256 * e);
      const u16* kq = ka - 256 * 7;
#pragma unroll 1
      for (int r = 0; r < 8; ++r) {
#pragma unroll
        for (int e = 0; e < 8; ++e) {
          a[(e + 3) & 3] = *reinterpret_cast<const f16x8*>(kq + 256 * (7 - ((e + 3) & 7)) + (e >= 5 ? 32 : 0));
          const int j = (8 - e) & 7;
          bf[j] = BFRAG(uq[j >> 1] + 512 * (j & 1) + (e ? 64 : 0));
          __builtin_amdgcn_sched_barrier(0);
#pragma unroll
          for (int T = 7; T >= (e > 1 ? e : 1); --T) acc[T] = MFMAH(bf[T - e], a[e & 3], acc[T]);
          __builtin_amdgcn_sched_barrier(0);
        }
        kq += 32;
#pragma unroll
        for (int j = 0; j < 4; ++j) uq[j] += 64;
        asm volatile("" : "+v"(uq[0]), "+v"(uq[1]), "+v"(uq[2]), "+v"(uq[3]));
      }
    }
    SST(8);
    {
      v2u k[8];
#pragma unroll
      for (int T = 1; T < 8; ++T) k[T] = SKIP(T);
#pragma unroll
      for (int T = 1; T < 8; ++T) TILE_OUT(uc + 32 + 256 * T, acc[T], k[T]);
    }
    RAW_BAR();
    SST(10);
    if (w >= 4) CHUNK_ST(0, Uc + 32, CUN);
    CHUNK_ST(1, Uc + 32 + 512, CUN);
    if (PP < 0) {
      if (CVSP) __builtin_amdgcn_s_sleep(CVSP);
      CHUNK_ST(2, Uc + 32 + 1024, CUN);
      if (CVSP) __builtin_amdgcn_s_sleep(CVSP);
      CHUNK_ST(3, Uc + 32 + 1536, CUN);
    }
    if (w == 1) (void)wait_n<SLP_V, 32>(FL + FL_FZ + (blockIdx.x & 7) * 64, 64, TMO);
    DRAIN();
    if (l < (PP < 0 ? 32 : 16)) CHUNK_FLAG(0);
    SST(9);
#undef U_ROW
#undef U_VAL
#undef U_PUT
#undef ST_GET
  }
  const unsigned rb = lds_base(reinterpret_cast<char*>(((c & 1) ? Rs : Rc) + w * CRN + c + 8 * g));
#pragma unroll
  for (int j = 0; j < 8; ++j) acc[j] = f32x4{};
  const __amdgpu_buffer_rsrc_t rx = MKRS(ws + OFF_X2, SZ_X), rz = MKRS(ws + OFF_X3, SZ_X);
  const __amdgpu_buffer_rsrc_t rf = MKRS(FL + FL_FR, 64 * 16 * 128);
  v2u st = {0u, 0u}, xx = {0u, 0u};
#define X2_LOAD(ss, far)                                                                                                      \
  {                                                                                                                      \
    xx = LD64(rx, (l >> 1) * (DM * 2) + (l & 1) * 8, (far) ? (int)SZ_X : ((ss) * SEG * DM + 8 * cv) * 2);                 \
    st = LD64(rf, l * 8, ((ss) * 4 + (blockIdx.x & 3)) * 512);                                                           \
  }
  unsigned tl = now10();
  bool late = false;
  for (int s = 0; s < NSEG; ++s) {
    const int q = s & 3, q8 = s & 7;
    if (w == 1) {
      late = __builtin_popcountll(__ballot(st.y != 0u)) >= CVLATE;
      for (unsigned spins = 0; !__all(umin3(xx.x, xx.y, st.y) != 0u); ++spins) {
        if (spin_timeout(spins, TMO)) break;
        if (GF_V && s >= GF0) {
          if (spins) __builtin_amdgcn_s_sleep(SLP_V);
          else if (s < NSEG - 6) gate(tl);
          tl = now10() + GF_V;
          X2_LOAD(s, CVT2 > 64 ? 0 : spins && __builtin_popcountll(__ballot(st.y != 0u)) < CVT2);
          continue;
        }
        if (GATE_V && spins == 0 && !__any(st.y != 0u)) gate(tl);
        if (spins && !__all(st.y != 0u)) __builtin_amdgcn_s_sleep(SLP_V);
        X2_LOAD(s, __builtin_popcountll(__ballot(st.y != 0u)) < CVTHR);
      }
      if (!GF_V || s < GF0) tl = now10() + GATE_V;
      TS(0, s, 64);
      const float s0 = __uint_as_float(st.x), s1 = dppr<0xb1>(s0), m2 = __uint_as_float(st.y);
      const float var = (m2 + dppr<0xb1>(m2) + (s0 - s1) * (s0 - s1) * (1.0f / DM)) * (1.0f / DM);
      const float mb = bfr1((s0 + s1) * (1.0f / DM)), ri = 1024.0f * __builtin_amdgcn_rcpf(bfr1(__builtin_amdgcn_sqrtf(bfr1(bfr1(var) + 1e-5f))));
      const unsigned t0 = pk2(lo16(xx.x) - mb, hi16(xx.x) - mb), t1 = pk2(lo16(xx.y) - mb, hi16(xx.y) - mb);
      const unsigned b0 = pk2(lo16(t0) * ri, hi16(t0) * ri), b1 = pk2(lo16(t1) * ri, hi16(t1) * ri);
      const unsigned u0 = pkh(lo16(b0), hi16(b0)), u1 = pkh(lo16(b1), hi16(b1));
      u16* rp = Rc + (l & 1) * 4 * CRN + 32 * q + (l >> 1);
#define R_PUT(o) { rp[o] = (u16)u0; rp[(o) + CRN] = (u16)(u0 >> 16); rp[(o) + 2 * CRN] = (u16)u1; rp[(o) + 3 * CRN] = (u16)(u1 >> 16); }
      R_PUT(0) R_PUT(8 * CRN + 63)
      if (q == 0) { R_PUT(128) R_PUT(8 * CRN + 63 + 128) }
#undef R_PUT
      *reinterpret_cast<v2u*>(Sx + l * 4) = xx;
    }
    const u16* kq = ka + 32 * q8;
    const f16x8 a0 = *reinterpret_cast<const f16x8*>(kq - 32), a1 = *reinterpret_cast<const f16x8*>(kq);
    __syncthreads();
    TS(1, s, 0);
    const f16x8 bp = BFRAG(rb + 64 * ((q + 3) & 3)), bc = BFRAG(rb + 64 * q);
    const float u0 = h2f(Rc[w * CRN + 32 * q + c]), u1 = h2f(Rc[w * CRN + 32 * q + 16 + c]);
    const float x0 = bf2f(Sx[c * 8 + w]), x1 = bf2f(Sx[(16 + c) * 8 + w]);
    acc[0] = MFMAH(a0, bp, (q8 == 0 ? acc[1] : acc[0]));
    const f32x4 tmp = MFMAH(a1, bc, acc[0]);
    const unsigned uu = pk2(u0 * dsk, u1 * dsk);
    const unsigned yy = pk2(((q & 1) ? tmp[2] : tmp[0]) * ysc, ((q & 1) ? tmp[3] : tmp[1]) * ysc);
    const unsigned aa = pk2(lo16(yy) + lo16(uu), hi16(yy) + hi16(uu)), oo = pk2(x0 + lo16(aa), x1 + hi16(aa));
    if (g == (q8 >> 1)) {
      To[c * 8 + w] = (u16)oo;
      To[(16 + c) * 8 + w] = (u16)(oo >> 16);
    }
    __syncthreads();
    if (w == 0 && l < 32) {
      const v4u o = *reinterpret_cast<const v4u*>(To + l * 8);
      ST128(o, rz, l * 16, (s * 64 + cv) * 512);
      ST64(part_stats(o), rps, l * 8, ((64 + s) * 64 + cv) * 256);
    }
    if (w == 1) {
      if (GF_V && s + 1 >= GF0) st.y = 0u;
      else X2_LOAD(s + 1, !late);
    }
    const int left = 7 - (s >> 3);
#define PUSH(J0, SH)                                                                                              \
  if (left >= (J0)) {                                                                                             \
    _Pragma("unroll") for (int j = (J0); j < (J0) + 2 && j < 8; ++j)                                              \
      acc[j] = MFMAH(*reinterpret_cast<const f16x8*>(kq - 32 - 256 * j), bp, (j + (SH) < 8 ? acc[(j + (SH)) & 7] : f32x4{})); \
  }
    TS(2, s, 0);
    if (w == 0) {
      DRAIN();
      if (l < 4) set_flag(FL + FL_FC + (((64 + s) * 4 + l) * 8 + (cv >> 3)) * 32 + (cv & 7));
    }
    TS(3, s, 0);
    if (q8 == 0) { PUSH(1, 1) PUSH(3, 1) PUSH(5, 1) PUSH(7, 1) }
    else { PUSH(1, 0) PUSH(3, 0) PUSH(5, 0) PUSH(7, 0) }
#undef PUSH
    if (PP >= 0 && (unsigned)(s - PP) < 3u) {
      const int i = 2 + s - PP;
      if (i > 2) {
        DRAIN();
        if (l < 8) CHUNK_FLAG(i - 1);
        tx[2] = tx[3];
      }
      if (i < 4) CHUNK_STX(i, tx[2], Uc + 32 + 512 * i, CUN);
    }
    MSTAMP();
  }
#undef KFRAG
#undef BFRAG
#undef X2_LOAD
#undef TILE_OUT
#undef SKIP
#undef CHUNK_ST
#undef CHUNK_STX
#undef CHUNK_FLAG
}

__device__ __forceinline__ v4u ln_row_packed(const v4u xr) {
  float v[8];
#pragma unroll
  for (int e = 0; e < 4; ++e) { v[2 * e] = lo16(xr[e]); v[2 * e + 1] = hi16(xr[e]); }
  float s = ((v[0] + v[1]) + (v[2] + v[3])) + ((v[4] + v[5]) + (v[6] + v[7]));
  s = wave_sum(s);
  const float mean = s * (1.0f / DM);
  float q = 0.f;
#pragma unroll
  for (int j = 0; j < 8; ++j) { float t = v[j] - mean; q += t * t; }
  q = wave_sum(q);
  const float var = q * (1.0f / DM);
  const float mb = bfr(mean);
  const float sd = bfr(sqrtf(bfr(bfr(var) + 1e-5f)));
  const float ri = 1.0f / sd;
  v4u o;
#pragma unroll
  for (int e = 0; e < 4; ++e) {
    const unsigned t = pk2(v[2 * e] - mb, v[2 * e + 1] - mb);
    const float x0 = lo16(t), x1 = hi16(t);
    float q0 = x0 * ri, q1 = x1 * ri;
    q0 = __builtin_fmaf(__builtin_fmaf(-q0, sd, x0), ri, q0);
    q1 = __builtin_fmaf(__builtin_fmaf(-q1, sd, x1), ri, q1);
    o[e] = pk2(q0, q1);
  }
  return o;
}

__device__ __forceinline__ void ln4_packed(v4u (&x)[4]) {
  f32x2_t v[16];
#pragma unroll
  for (int e = 0; e < 16; ++e) v[e] = f32x2_t{lo16(x[e >> 2][e & 3]), hi16(x[e >> 2][e & 3])};
  const f32x2_t t = (((v[0] + v[1]) + (v[2] + v[3])) + ((v[4] + v[5]) + (v[6] + v[7]))) +
                    (((v[8] + v[9]) + (v[10] + v[11])) + ((v[12] + v[13]) + (v[14] + v[15])));
  const float mean = red_mod4(t.x + t.y) * (1.0f / DM);
  const float mb = bfr1(mean);
  f32x2_t q0 = {0.f, 0.f}, q1 = {0.f, 0.f};
#pragma unroll
  for (int e = 0; e < 16; e += 2) {
    v[e] -= mb;
    v[e + 1] -= mb;
    q0 += v[e] * v[e];
    q1 += v[e + 1] * v[e + 1];
  }
  q0 += q1;
  const float dm = mean - mb;
  const float var = fmaxf(red_mod4(q0.x + q0.y) * (1.0f / DM) - dm * dm, 0.f);
  const float sd = bfr1(sqrtf(bfr1(bfr1(var) + 1e-5f)));
  const float ri = 1.0f / sd;
#pragma unroll
  for (int e = 0; e < 16; ++e) {
    const unsigned t2 = pk2(v[e].x, v[e].y);
    const f32x2_t q = f32x2_t{lo16(t2), hi16(t2)} * ri;
    x[e >> 2][e & 3] = pk2(q.x, q.y);
  }
}

constexpr int MEGA_WGS = 256;
constexpr size_t MEGA_LDS = (size_t)(32 * 4 * 64 * 8 + 8 * SMS + 15 * QS + 128 + 16 * 64 * 8 + 8 * SOS + 512) * 2;
static_assert(MEGA_LDS <= 163840, "lds");
static_assert(MEGA_LDS >= (size_t)AW_CNT + 128 && MEGA_LDS >= (size_t)P_CN + 512, "lds");
static_assert(MEGA_LDS >= (size_t)CV_LDS, "lds");
static_assert(MEGA_LDS >= (size_t)2 * 32 * SMS * 2 && MEGA_LDS >= 131072, "lds");
static_assert(MEGA_LDS >= (size_t)8 * (16640 + 2 * (704 + 144)), "lds");
__device__ __forceinline__ MegaArgs role_args(const int b, const int wv) {
  auto p = (const __attribute__((address_space(4))) u64*)__builtin_amdgcn_kernarg_segment_ptr();
  asm volatile("" : "+s"(p));
#define KA(i) ((u16*)(__attribute__((address_space(1))) u16*)p[i])
  return MegaArgs{KA(0), KA(1), (char*)KA(2), KA(3), KA(4), KA(5), KA(6), KA(7), KA(8), KA(9), KA(10), KA(11), KA(12), KA(13), b, wv};
#undef KA
}
__global__ __launch_bounds__(512, 2) void mega_kernel(const u16* mem0, const u16* Dskip, char* ws, u16* out, const u16* x0,
                                                      const u16* logAre, const u16* Aim, const u16* Cre, const u16* Cim,
                                                      const u16* logdt, const u16* Wq, const u16* Wk, const u16* Wv,
                                                      const u16* Wo) {
  extern __shared__ __attribute__((aligned(16))) char smem[];
  const int b = vbid(), wv = __builtin_amdgcn_readfirstlane(threadIdx.x >> 6);
#define A role_args(b, wv)
  u64 tag = 0;
  {
    const unsigned t0 = now10();
    if ((b >= 32 && b < 64) || (b >= 200 && b < 232)) start_kgen(A, smem, b < 64 ? b - 32 : b - 168);
    else if (b >= 64 && b < 96) tag = start_stats<4>(A, b - 64);
    else if (b >= 240) (void)start_stats<8>(A, b - 224);
    const unsigned dl = (b >= 32 && b < 64) ? DL_C : (b >= 64 && b < 96) ? DL_A : b >= 240 ? DL_R2 : ((b >= 96 && b < 104) || b >= 232) ? DL_R : (b >= 200 && b < 232) ? DL_Q : 0u;
    while (now10() - t0 < dl) __builtin_amdgcn_s_sleep(16);
  }
  if ((EXP_OFF & 1) && b >= 64 && b < 80) return;
  if ((EXP_OFF & 8) && b >= 200 && b < 232) return;
  if ((EXP_OFF & 16) && b >= 32 && b < 64) return;
  if ((EXP_OFF & 32) && b >= 80 && b < 96) return;
  if (b < 64) role_chain(A, smem, b >> 5, b & 31, 0);
  else if (b < 96) role_aw(A, smem, (b >> 4) & 1, b & 15, tag);
  else if (b >= 104 && b < 168) role_conv(A, smem, b - 104);
  else {
    const bool kv = b >= 168 && b < 232;
    role_proj(A, smem, b >= (kv ? 200 : 240), kv ? (b - 168) & 31 : b & 15, kv);
  }
#undef A
}

extern "C" void kernel_launch(void* const* d_in, const int* in_sizes, int n_in, void* d_out, int out_size, void* d_ws,
                              size_t ws_size, hipStream_t stream) {
  MegaArgs A;
  A.x0 = (const u16*)d_in[0];
  A.logAre = (const u16*)d_in[1];
  A.Aim = (const u16*)d_in[2];
  A.Cre = (const u16*)d_in[3];
  A.Cim = (const u16*)d_in[4];
  A.logdt = (const u16*)d_in[5];
  A.Dskip = (const u16*)d_in[6];
  A.mem0 = (const u16*)d_in[8];
  A.Wq = (const u16*)d_in[9];
  A.Wk = (const u16*)d_in[10];
  A.Wv = (const u16*)d_in[11];
  A.Wo = (const u16*)d_in[12];
  A.ws = (char*)d_ws;
  A.out = (u16*)d_out;

  (void)hipFuncSetAttribute((const void*)mega_kernel, hipFuncAttributeMaxDynamicSharedMemorySize, (int)MEGA_LDS);
  mega_kernel<<<MEGA_WGS, 512, MEGA_LDS, stream>>>(A.mem0, A.Dskip, A.ws, A.out, A.x0, A.logAre, A.Aim, A.Cre, A.Cim, A.logdt,
                                                   A.Wq, A.Wk, A.Wv, A.Wo);
}
```

```cpp
#include <hip/hip_runtime.h>
#include <hip/hip_bf16.h>
#include <stdint.h>

constexpr int L_SEQ = 2048;
constexpr int DM    = 512;
constexpr int NST   = 128;
constexpr int NH    = 32;
constexpr int DH    = 128;
constexpr int HD    = NH * DH;
constexpr int MM    = 8;
constexpr int SEG   = 32;
constexpr int NSEG  = L_SEQ / SEG;
constexpr int LK    = MM + SEG;

typedef unsigned short u16;
using bf16x8 = __attribute__((ext_vector_type(8))) short;
using f32x4  = __attribute__((ext_vector_type(4))) float;
using f32x16 = __attribute__((ext_vector_type(16))) float;

static __device__ __forceinline__ u16 f2bf(float f) {
  unsigned u = __float_as_uint(f);
  u += 0x7fffu + ((u >> 16) & 1u);
  return (u16)(u >> 16);
}
static __device__ __forceinline__ float bf2f(u16 h) { return __uint_as_float(((unsigned)h) << 16); }
static __device__ __forceinline__ float bfr(float f) { return bf2f(f2bf(f)); }

constexpr size_t SZ_X      = (size_t)L_SEQ * DM * 2;
constexpr size_t OFF_KF    = 0;
constexpr size_t OFF_UT    = OFF_KF + (size_t)DM * L_SEQ * 4;
constexpr size_t OFF_YT    = OFF_UT + SZ_X;
constexpr size_t OFF_XA    = OFF_YT + SZ_X;
constexpr size_t OFF_H     = OFF_XA + SZ_X;
constexpr size_t OFF_X2    = OFF_H + SZ_X;
constexpr size_t OFF_U2    = OFF_X2 + SZ_X;
constexpr size_t OFF_X3    = OFF_U2 + SZ_X;
constexpr size_t SZ_QK     = (size_t)L_SEQ * 2 * HD * 2;
constexpr size_t OFF_QK    = OFF_X3 + SZ_X;
constexpr size_t SZ_O      = (size_t)L_SEQ * HD * 2;
constexpr size_t OFF_VF    = OFF_QK + 2 * SZ_QK;
constexpr size_t SZ_PW     = (size_t)2 * NSEG * 16 * SEG * DM * 2;
constexpr size_t OFF_PW    = OFF_VF + 2 * SZ_O;
constexpr size_t SZ_MEM    = (size_t)NSEG * MM * HD * 2;
constexpr size_t OFF_MEMK  = OFF_PW + SZ_PW;
constexpr size_t OFF_MEMV  = OFF_MEMK + 2 * SZ_MEM;
constexpr size_t OFF_GT    = OFF_MEMV + 2 * SZ_MEM;
constexpr size_t OFF_EP    = OFF_GT + 65536;
constexpr size_t OFF_RD    = OFF_EP + 4096;
constexpr size_t OFF_KG    = OFF_RD + 4096;
constexpr size_t OFF_G     = OFF_KG + 8192;
constexpr size_t SZ_ACC    = (size_t)2 * 65536;
constexpr size_t OFF_Z     = OFF_GT + (size_t)128 * DM * 4;
constexpr size_t OFF_ACC   = OFF_Z;
constexpr size_t OFF_FL    = OFF_ACC + 2 * SZ_ACC;
constexpr int FL_FK  = 0;
constexpr int FL_FW  = FL_FK + 64 * 8 * 32;
constexpr int FL_FR  = FL_FW + 2 * 64 * 16 * 32;
constexpr int FL_FC  = FL_FR + 64 * 16 * 32;
constexpr int FL_FQ  = FL_FC + 2 * 64 * 32 * 32;
constexpr int FL_FP  = FL_FQ + 2 * 64 * 32 * 32;
constexpr int FL_FZ  = FL_FP + 64 * 32 * 32;
constexpr int FL_TMO = FL_FZ + 16 * 32;
constexpr int FL_END = FL_TMO + 64;
constexpr size_t Z_BYTES   = OFF_FL + (size_t)FL_END * 4 - OFF_Z;
constexpr size_t WS_TOTAL  = OFF_Z + Z_BYTES;
static_assert(Z_BYTES % 16 == 0 && OFF_Z % 256 == 0 && Z_BYTES <= 4 * 64 * 8192 && OFF_G + 2048 <= OFF_Z, "zero region");
static_assert(WS_TOTAL + (4 << 20) <= (size_t)256 * 1024 * 1024, "workspace too large");

static_assert(WS_TOTAL - OFF_XA <= (size_t)384 * 8 * 65536, "translation warm-up");
constexpr int CKN = 2336;
static_assert((size_t)DM * 2 * CKN * 2 <= (size_t)DM * L_SEQ * 4 + SZ_X, "K buffer");
__device__ __forceinline__ float pos_of_index(int p) {
  if (p < 256) return (float)p;
  if (p < 384) return (float)(256 + 2 * (p - 256));
  if (p < 512) return (float)(512 + 4 * (p - 384));
  if (p < 640) return (float)(1024 + 8 * (p - 512));
  return 2048.0f;
}
__device__ __forceinline__ int index_of_l(int l) {
  float pf = bfr((float)l);
  int p = (int)pf;
  if (p < 256) return p;
  if (p < 512) return 256 + ((p - 256) >> 1);
  if (p < 1024) return 384 + ((p - 512) >> 2);
  if (p < 2048) return 512 + ((p - 1024) >> 3);
  return 640;
}

template <int CTRL> __device__ __forceinline__ float dppr(float x) {
  return __builtin_bit_cast(float, __builtin_amdgcn_update_dpp(0, __builtin_bit_cast(int, x), CTRL, 0xf, 0xf, false));
}
__device__ __forceinline__ float wave_sum(float v) {
  v += dppr<0x121>(v);
  v += dppr<0x122>(v);
  v += dppr<0x124>(v);
  v += dppr<0x128>(v);
  {
    auto r = __builtin_amdgcn_permlane16_swap(__float_as_uint(v), __float_as_uint(v), false, false);
    v = __uint_as_float(r[0]) + __uint_as_float(r[1]);
  }
  {
    auto r = __builtin_amdgcn_permlane32_swap(__float_as_uint(v), __float_as_uint(v), false, false);
    v = __uint_as_float(r[0]) + __uint_as_float(r[1]);
  }
  return v;
}
__device__ __forceinline__ void ln_row8(float (&v)[8]) {
  float s = 0.f;
#pragma unroll
  for (int j = 0; j < 8; ++j) s += v[j];
  s = wave_sum(s);
  const float mean = s * (1.0f / DM);
  float q = 0.f;
#pragma unroll
  for (int j = 0; j < 8; ++j) { float t = v[j] - mean; q += t * t; }
  q = wave_sum(q);
  const float var = q * (1.0f / DM);
  const float mb = bfr(mean);
  const float sd = bfr(sqrtf(bfr(bfr(var) + 1e-5f)));
#pragma unroll
  for (int j = 0; j < 8; ++j) v[j] = bfr(bfr(v[j] - mb) / sd);
}

#define RLX_AGENT __ATOMIC_RELAXED, __HIP_MEMORY_SCOPE_AGENT
typedef __attribute__((address_space(1))) unsigned gu32;
typedef unsigned v4u __attribute__((ext_vector_type(4)));
constexpr int AUX_SC1 = 16;
constexpr unsigned SPIN_CAP = 1u << 18;

__device__ __forceinline__ bool wait_flags(const unsigned* flags, int lane, int nfl, unsigned want, unsigned* tmo) {
  for (unsigned spins = 0;; ++spins) {
    unsigned v = want;
    if (lane < nfl) v = __hip_atomic_load((gu32*)flags + lane * 32, RLX_AGENT);
    if (__all(v >= want)) break;
    if (spins > SPIN_CAP) {
      if (lane == 0) __hip_atomic_store((gu32*)tmo, 1u, RLX_AGENT);
      return false;
    }
  }
  __builtin_amdgcn_fence(__ATOMIC_ACQUIRE, "wavefront");
  return true;
}

#define STAMP(i)
#define STAMP_DUMP()
#define WSTAMP(i)
template <int CTRL> __device__ __forceinline__ float dppf(float old, float x) {
  return __builtin_bit_cast(float, __builtin_amdgcn_update_dpp(__builtin_bit_cast(int, old), __builtin_bit_cast(int, x), CTRL, 0xf, 0xf, false));
}
__device__ __forceinline__ float wave_max_dpp(float x) {
  const float ninf = -3e38f;
  x = fmaxf(x, dppf<0x111>(ninf, x));
  x = fmaxf(x, dppf<0x112>(ninf, x));
  x = fmaxf(x, dppf<0x114>(ninf, x));
  x = fmaxf(x, dppf<0x118>(ninf, x));
  x = fmaxf(x, dppf<0x142>(ninf, x));
  x = fmaxf(x, dppf<0x143>(ninf, x));
  return __builtin_bit_cast(float, __builtin_amdgcn_readlane(__builtin_bit_cast(int, x), 63));
}
__device__ __forceinline__ float wave_sum_dpp(float x) {
  x += dppf<0x111>(0.f, x);
  x += dppf<0x112>(0.f, x);
  x += dppf<0x114>(0.f, x);
  x += dppf<0x118>(0.f, x);
  x += dppf<0x142>(0.f, x);
  x += dppf<0x143>(0.f, x);
  return __builtin_bit_cast(float, __builtin_amdgcn_readlane(__builtin_bit_cast(int, x), 63));
}
constexpr int SMS = 528;
constexpr int QS = 144;
constexpr int VS = 80;
constexpr int SOS = 144;
#define VAR_NREP 4
constexpr int NREP = VAR_NREP;
__device__ __forceinline__ float xsum16(float x) {
  auto r = __builtin_amdgcn_permlane16_swap(__float_as_uint(x), __float_as_uint(x), false, false);
  return __uint_as_float(r[0]) + __uint_as_float(r[1]);
}
__device__ __forceinline__ float xsum32(float x) {
  auto r = __builtin_amdgcn_permlane32_swap(__float_as_uint(x), __float_as_uint(x), false, false);
  return __uint_as_float(r[0]) + __uint_as_float(r[1]);
}
__device__ __forceinline__ float xmax16(float x) {
  auto r = __builtin_amdgcn_permlane16_swap(__float_as_uint(x), __float_as_uint(x), false, false);
  return fmaxf(__uint_as_float(r[0]), __uint_as_float(r[1]));
}
__device__ __forceinline__ float xmax32(float x) {
  auto r = __builtin_amdgcn_permlane32_swap(__float_as_uint(x), __float_as_uint(x), false, false);
  return fmaxf(__uint_as_float(r[0]), __uint_as_float(r[1]));
}
__device__ __forceinline__ float red_mod4(float x) {
  x += dppf<0x124>(0.f, x);
  x += dppf<0x128>(0.f, x);
  return xsum32(xsum16(x));
}
typedef __bf16 bf16x2_t __attribute__((ext_vector_type(2)));
typedef float f32x2_t __attribute__((ext_vector_type(2)));
__device__ __forceinline__ unsigned pk2(float a, float b) {
  f32x2_t v = {a, b};
  bf16x2_t r = __builtin_convertvector(v, bf16x2_t);
  return __builtin_bit_cast(unsigned, r);
}
typedef _Float16 f16x2_t __attribute__((ext_vector_type(2)));
typedef _Float16 f16x8 __attribute__((ext_vector_type(8)));
__device__ __forceinline__ unsigned pkh(float a, float b) {
  f32x2_t v = {a, b};
  return __builtin_bit_cast(unsigned, __builtin_convertvector(v, f16x2_t));
}
__device__ __forceinline__ float h2f(u16 h) { return (float)__builtin_bit_cast(_Float16, h); }
__device__ __forceinline__ float one(float x) {
  asm("" : "+v"(x));
  return x;
}
__device__ __forceinline__ unsigned pk2s(float a, float b) { return pk2(one(a), one(b)); }
__device__ __forceinline__ float dot2sq(const unsigned x, const float acc) {
  return __builtin_amdgcn_fdot2_f32_bf16(__builtin_bit_cast(bf16x2_t, x), __builtin_bit_cast(bf16x2_t, x), acc, false);
}
__device__ __forceinline__ float lo16(unsigned u) { return __uint_as_float(u << 16); }
__device__ __forceinline__ float hi16(unsigned u) { return __uint_as_float(u & 0xffff0000u); }
__device__ __forceinline__ float bfr1(float x) { return lo16(pk2(x, x)); }
__device__ __forceinline__ float half_sum(float v) {
  v += dppr<0x121>(v);
  v += dppr<0x122>(v);
  v += dppr<0x124>(v);
  v += dppr<0x128>(v);
  return xsum16(v);
}
__device__ __forceinline__ void scale_pair(float a, float b, float scale, float& oa, float& ob) {
  unsigned u = pk2(a, b);
  unsigned v = pk2s(lo16(u) * scale, hi16(u) * scale);
  oa = lo16(v);
  ob = hi16(v);
}

#define XMAP 2
__device__ __forceinline__ int vbid() {
  const int b = blockIdx.x, x = b & 7, t = b >> 3;
  if (XMAP == 0) {
    if (b < 64) return (b & 32) + 4 * x + (t & 3);
    if (b < 96) return (b & ~15) + 2 * x + (t & 1);
    if (b >= 104 && b < 168) return 104 + 8 * x + t - 13;
    if (b >= 168 && b < 232) return 168 + ((b - 168) & 32) + 4 * x + ((t - 21) & 3);
    return b;
  }
  if (XMAP == 3) {
    const int y = x & 3;
    if (t >= 16 && t < 24) return 104 + 16 * y + 2 * (x & 4) + t - 16;
    if (x < 4) return t < 16 ? 4 * (t & 8) + 8 * x + (t & 7) : 64 + 4 * (t & 4) + 4 * y + (t & 3);
    if (t >= 24) return (t >= 28 ? 240 : y < 2 ? 96 : 224) + 4 * y + (t & 3);
    return 168 + 4 * (t & 8) + 8 * y + (t & 7);
  }
  if (XMAP == 4) {
    const int y = x & 3;
    if (x < 4) return t < 16 ? 4 * (t & 8) + 8 * x + (t & 7) : t < 28 ? 104 + 16 * y + t - 16 : 64 + 4 * y + (t & 3);
    if (t >= 24) return (t >= 28 ? 240 : y < 2 ? 96 : 224) + 4 * y + (t & 3);
    if (t < 16) return 168 + 4 * (t & 8) + 8 * y + (t & 7);
    return t < 20 ? 104 + 16 * y + t - 4 : 80 + 4 * y + (t & 3);
  }
  if (XMAP == 2 || XMAP >= 5) {
    if (XMAP == 5 && x < 4 && t < 16) return 16 * x + t;
    if (XMAP == 6 && x < 4 && t < 16) return 32 * (x & 1) + 8 * (x & 2) + t;
    if (XMAP == 7 && x < 4) return (x & 1) ? 104 + 16 * (x & 2) + t : 16 * x + t;
    if (x < 4) return t < 16 ? 4 * (t & 8) + 8 * x + (t & 7) : 104 + 16 * x + t - 16;
    const int y = x - 4;
    if (t >= 24) return (t >= 28 ? 240 : y < 2 ? 96 : 224) + 4 * y + (t & 3);
    if (t < 16) return 168 + 4 * (t & 8) + 8 * y + (t & 7);
    return 64 + 4 * (t & 4) + 4 * y + (t & 3);
  }
  if (x < 2) return 2 * (t & 16) + 16 * x + (t & 15);
  const int i = 16 * (x - 2) + (t & 15);
  if (t < 16) {
    const int k = (i * 171) >> 9, r = i - 3 * k;
    return r == 0 ? 168 + k : r == 1 ? 200 + k : 64 + 16 * (k & 1) + (k >> 1);
  }
  const int g = i >> 3, e = i & 7;
  if (g == 3) return 96 + e;
  if (g == 7) return 232 + e;
  if (g == 9) return 240 + e;
  if (g == 11) return 248 + e;
  return 104 + 8 * (g - (g > 3) - (g > 7) - (g > 9)) + e;
}
#define MSTAMP()
#define WSTAMP2()
#define SST(k)
#define SSX(k, c)
#define SSW(k, v)
#define TS(k, ss, t)
#define LS(k, ss)
#define LS_DUMP()
struct MegaArgs {
  const u16* mem0;
  const u16* Dskip;
  char* ws;
  u16* out;
  const u16* x0;
  const u16 *logAre, *Aim, *Cre, *Cim, *logdt;
  const u16 *Wq, *Wk, *Wv, *Wo;
  int vb, wv;
};

__device__ __forceinline__ int lane_id() {
  int l;
  asm volatile("v_mbcnt_lo_u32_b32 %0, -1, 0\n\tv_mbcnt_hi_u32_b32 %0, -1, %0" : "=v"(l));
  __builtin_assume(l >= 0 && l < 64);
  return l;
}
__device__ __forceinline__ bool spin_timeout(unsigned spins, unsigned* tmo) {
  if (spins > SPIN_CAP) {
    if (lane_id() == 0) __hip_atomic_store((gu32*)tmo, 1u, RLX_AGENT);
    return true;
  }
  return false;
}
#define ROT_H (4 * (h & 1))
#define ROT_T (h & 2)
#define C2G 420
#define CVSP 40
#define PFA 0
#define PFB 0
#define WO1 1
#define AWH 0
#define PEEK0 1
#define SLP0 2
#define GF_R 400
#define GF_V 0
#define PP 0
#define EL 1
#define IPF 2
#define XL 0
#define MC3 2
#define XZD 300
#define XZP 4
#define NFW 1
#define IPS 128u
#define TP 1
#define TPS (4 * TP + 4)
#define MCE 1
#define H4S 0
#define GF_H 150
#define CVT2 48
#define GF_P 0
#define GF0 8
#define SLP_H 12
#define HX0 6
#define CVSTEP 2
#define CVLATE 64
#define PACE 203
#define PACE0 6
#define RPRIO 3
#define DL_R2 1400
#define PNL 8
#define GSL 12
#define AWDIR 1
#define AWAT 0
#define AWRP 0
#define PEPS 4
#define FGATE 0
#define DL_C 0
#define DL_A 800
#define DL_R 0
#define DL_Q 0
#define KC0 0
#define CPRIO 1
#define P1PRIO 3
#define QLEAD 2
#define POLL_SLEEP 12
#define SLP_C POLL_SLEEP
#define SLP_A POLL_SLEEP
#define SLP_W POLL_SLEEP
#define SLP_R POLL_SLEEP
#define SLP_V POLL_SLEEP
#define SLP_Q POLL_SLEEP
#define SLP_M 4
#define AWPRIO 2
#define SLP_S 8
#define SPOS 4
#define CVPRE 12
#define QPD 3
#define QPRI 2
#define TLBW 3
#define QHV 0
#define QCHK 0
#define LEADN 99
#define GATE_V 170
#define GATE_R 380
#define GATE_N 380
#define AWAHEAD 1
#define GATE_M 150
#define PRI_A 3
#define PRI_WA 2
#define PRI_WB 1
#define AWSKIP 3
#define PRI_0 3
#define AWDEF 0
#define SLP_F 2
#define EXP_OFF 0
#define AWSPEC 1
#define CVTHR 48
#define NR_C 8
#define NR_R 8
#define NR_W 8
#define NR_N 2
#define QAHEAD 0
#define NS0 4
#define SLP_N POLL_SLEEP
template <int SLP, int WPL>
__device__ __forceinline__ bool wait_n(const unsigned* f, int n, unsigned* tmo) {
  const int l = lane_id();
  for (unsigned spins = 0;; ++spins) {
    unsigned v = 1u;
    if (l < n) v = __hip_atomic_load((gu32*)f + (l / WPL) * 32 + l % WPL, RLX_AGENT);
    if (__all(v != 0u)) break;
    if (spin_timeout(spins, tmo)) return false;
    __builtin_amdgcn_s_sleep(SLP);
  }
  asm volatile("" ::: "memory");
  return true;
}
__device__ __forceinline__ unsigned peek(const unsigned* base, const unsigned off) {
  unsigned r;
  asm volatile("global_atomic_or %0, %1, %2, %3 sc0\n\ts_waitcnt vmcnt(0)" : "=&v"(r) : "v"(off), "v"(0u), "s"(base) : "memory");
  return r;
}
__device__ __forceinline__ unsigned now10() { return (unsigned)__builtin_amdgcn_s_memrealtime(); }
__device__ __forceinline__ void gate(const unsigned t) {
#pragma unroll 1
  for (int i = 0; i < 64; ++i) {
    const int d = (int)(t - now10());
    if (d <= 0) break;
    if (d > 100) __builtin_amdgcn_s_sleep(32);
    else if (d > 25) __builtin_amdgcn_s_sleep(8);
    else __builtin_amdgcn_s_sleep(2);
  }
}
template <int SLP, int WPL, int GT>
__device__ __forceinline__ void grp_wait(const bool leader, volatile unsigned* sw, const unsigned val, const unsigned* f, int n,
                                         unsigned* tmo, unsigned& tl) {
  if (leader) {
    const int l = lane_id();
    for (unsigned spins = 0;; ++spins) {
      unsigned v = 1u;
      if (l < n) v = __hip_atomic_load((gu32*)f + (l / WPL) * 32 + l % WPL, RLX_AGENT);
      if (__all(v != 0u) || spin_timeout(spins, tmo)) break;
      if (GT && spins == 0) gate(tl);
      else __builtin_amdgcn_s_sleep(SLP);
    }
    if (GT) tl = now10() + GT;
    if (sw && l == 0) *sw = val;
  } else {
    for (unsigned spins = 0; *sw < val; ++spins) {
      if (spins > 64u * SPIN_CAP) break;
      if (FGATE && GT && spins == 0) gate(tl);
      __builtin_amdgcn_s_sleep(SLP_F);
    }
    if (FGATE && GT) tl = now10() + GT;
  }
  asm volatile("" ::: "memory");
}
template <int SLP, int WPL>
__device__ __forceinline__ bool wg_wait(const unsigned* f, int n, unsigned* tmo) {
  if (threadIdx.x < 64) (void)wait_n<SLP, WPL>(f, n, tmo);
  __syncthreads();
  asm volatile("" ::: "memory");
  return true;
}
__device__ __forceinline__ void set_flag(unsigned* f) { __hip_atomic_store((gu32*)f, 1u, RLX_AGENT); }
#define DRAIN() asm volatile("s_waitcnt vmcnt(0)" ::: "memory")
#define MKRS(ptr, bytes) __builtin_amdgcn_make_buffer_rsrc((void*)(ptr), 0, (int)(bytes), 0x00020000)
typedef unsigned v2u __attribute__((ext_vector_type(2)));
#define LD128(rs, voff, soff) __builtin_amdgcn_raw_buffer_load_b128(rs, voff, soff, AUX_SC1)
#define LD64(rs, voff, soff) __builtin_amdgcn_raw_buffer_load_b64(rs, voff, soff, AUX_SC1)
#define ST128(v, rs, voff, soff)                                              \
  do {                                                                        \
    const v4u st_d_ = (v);                                                    \
    __builtin_amdgcn_raw_buffer_store_b128(st_d_, rs, voff, soff, AUX_SC1);   \
    asm volatile("s_nop 1" ::"v"(st_d_) : "memory");                          \
  } while (0)
#define ST64(v, rs, voff, soff) __builtin_amdgcn_raw_buffer_store_b64(v, rs, voff, soff, AUX_SC1)
#define BF8(x) __builtin_bit_cast(bf16x8, x)
#define MFMA16(a, b, c) __builtin_amdgcn_mfma_f32_16x16x32_bf16(a, b, c, 0, 0, 0)
typedef short bf16x4 __attribute__((ext_vector_type(4)));
#define MFMA16K(a, b, c) \
  __builtin_amdgcn_mfma_f32_16x16x16bf16_1k(__builtin_bit_cast(bf16x4, a), __builtin_bit_cast(bf16x4, b), c, 0, 0, 0)
__device__ __forceinline__ float fmax_nn(float a, float b) { return __builtin_elementwise_maximum(a, b); }
typedef __bf16 bfv8 __attribute__((ext_vector_type(8)));
typedef __bf16 bfv16 __attribute__((ext_vector_type(16)));
typedef unsigned v8u __attribute__((ext_vector_type(8)));
#define SMF(a, b, c, idx) \
  __builtin_amdgcn_smfmac_f32_16x16x64_bf16(__builtin_bit_cast(bfv8, a), __builtin_bit_cast(bfv16, b), c, idx, 0, 0)
__device__ __forceinline__ float swap_add32(float a, float b) {
  auto r = __builtin_amdgcn_permlane32_swap(__float_as_uint(a), __float_as_uint(b), false, false);
  return __uint_as_float(r[0]) + __uint_as_float(r[1]);
}
__device__ __forceinline__ float swap_add16(float a, float b) {
  auto r = __builtin_amdgcn_permlane16_swap(__float_as_uint(a), __float_as_uint(b), false, false);
  return __uint_as_float(r[0]) + __uint_as_float(r[1]);
}
__device__ __forceinline__ v4u smf_fix(const v4u d) {
  auto x = __builtin_amdgcn_permlane16_swap(d.x, d.z, false, false);
  auto y = __builtin_amdgcn_permlane16_swap(d.y, d.w, false, false);
  return v4u{x[0], x[1], y[0], y[1]};
}
__device__ __forceinline__ v8u smf_bfrag(const u16* p) {
  const v2u a = *reinterpret_cast<const v2u*>(p), b = *reinterpret_cast<const v2u*>(p + 8);
  const v2u c = *reinterpret_cast<const v2u*>(p + 32), d = *reinterpret_cast<const v2u*>(p + 40);
  return v8u{a.x, b.x, a.y, b.y, c.x, d.x, c.y, d.y};
}

#define VAR_PPA 0
#define VAR_PPB 0
#define VAR_THR 0
#define VAR_THR2 0
#define CV_LEAD 100
#define VAR_EARLY 1
#define VAR_DRAIN 1
#define VAR_D1 0
#define VAR_D2 0
#define VAR_DR2 1
#define VAR_S1 0
#define VAR_S2 0
typedef __attribute__((address_space(1))) unsigned long long gu64;
typedef __attribute__((address_space(1))) char gch;
__device__ __forceinline__ int cvt_rpi(float x) {
  int r;
  asm("v_cvt_rpi_i32_f32 %0, %1" : "=v"(r) : "v"(x));
  return r;
}
typedef unsigned long long u64;
__device__ __forceinline__ u64 call_tag(const MegaArgs& A) {
  u64 t = *(const __attribute__((address_space(4))) u64*)(A.ws + OFF_EP) * 6364136223846793005ull + 1442695040888963407ull;
  asm volatile("" : "+s"(t)::"memory");
  return t;
}
__device__ __forceinline__ u64 tagged(const u64 tag, const unsigned v) { return (tag & 0xffffffffffffull) | (u64)(v >> 16) << 48; }
__device__ __forceinline__ bool has_tag(const u64 tag, const unsigned lo, const unsigned hi) {
  return lo == (unsigned)tag && (hi & 0xffffu) == ((unsigned)(tag >> 32) & 0xffffu);
}
__device__ __forceinline__ u64 ready_look(const MegaArgs& A) {
  return __hip_atomic_load((gu64*)(A.ws + OFF_RD) + (blockIdx.x & 7) * 64 + lane_id(), RLX_AGENT);
}
#define RD_ASK(v) \
  asm volatile("global_load_dwordx2 %0, %1, %2 sc1" : "=v"(v) : "v"(lane_id() * 8), "s"(A.ws + OFF_RD + (blockIdx.x & 7) * 512) : "memory")
__device__ __forceinline__ void ready_wait(const MegaArgs& A, const u64 tag, u64 v) {
  for (unsigned spins = 0; !__all(v == tag) && spins < SPIN_CAP; ++spins) {
    __builtin_amdgcn_s_sleep(8);
    v = ready_look(A);
  }
  asm volatile("" ::: "memory");
}
typedef short v4s __attribute__((ext_vector_type(4)));
typedef __attribute__((address_space(3))) v4s* lp4s;
__device__ __forceinline__ void glds16(const void* sbase, const unsigned voff, const unsigned lds) {
  unsigned keep;
  asm volatile("s_nop 4\n\ts_mov_b32 %0, m0\n\ts_mov_b32 m0, %3\n\ts_nop 0\n\tglobal_load_lds_dwordx4 %1, %2\n\ts_mov_b32 m0, %0"
               : "=&s"(keep)
               : "v"(voff), "s"(sbase), "s"(lds)
               : "memory");
}
__device__ __forceinline__ void glds16c(const void* sbase, const unsigned voff, const unsigned lds) {
  unsigned keep;
  asm volatile("s_nop 4\n\ts_mov_b32 %0, m0\n\ts_mov_b32 m0, %3\n\ts_nop 0\n\tglobal_load_lds_dwordx4 %1, %2 sc1\n\ts_mov_b32 m0, %0"
               : "=&s"(keep)
               : "v"(voff), "s"(sbase), "s"(lds)
               : "memory");
}
__device__ __forceinline__ void glds64c(const void* sbase, const unsigned voff, const unsigned lds) {
  unsigned keep;
  asm volatile("s_nop 4\n\ts_mov_b32 %0, m0\n\ts_mov_b32 m0, %3\n\ts_nop 0\n\tglobal_load_lds_dwordx4 %1, %2 sc1\n\t"
               "global_load_lds_dwordx4 %1, %2 offset:1024 sc1\n\tglobal_load_lds_dwordx4 %1, %2 offset:2048 sc1\n\t"
               "global_load_lds_dwordx4 %1, %2 offset:3072 sc1\n\ts_mov_b32 m0, %0"
               : "=&s"(keep)
               : "v"(voff), "s"(sbase), "s"(lds)
               : "memory");
}
__device__ __forceinline__ v2u trd(const unsigned a) {
  return __builtin_bit_cast(v2u, __builtin_amdgcn_ds_read_tr16_b64_v4i16((lp4s)(unsigned long)a));
}
__device__ __forceinline__ unsigned lds_base(char* smem) { return (unsigned)(unsigned long)(__attribute__((address_space(3))) char*)smem; }
#define RAW_BAR()                                           \
  do {                                                      \
    asm volatile("s_waitcnt lgkmcnt(0)" ::: "memory");      \
    __builtin_amdgcn_s_barrier();                           \
    asm volatile("" ::: "memory");                          \
  } while (0)
#define WAIT_VM(n) asm volatile("s_waitcnt vmcnt(" #n ")" ::: "memory")
template <bool SP>
__device__ __forceinline__ void wqkv_issue(const u16* Wm, const int hf, const int h, const int w, const int l, const unsigned sb) {
  const char* base = (const char*)Wm + (size_t)(256 * hf + 32 * w) * (HD * 2) + h * (DH * 2);
#pragma unroll
  for (int i = 0; i < 8; ++i) {
    const int r = l >> 4;
    const int f = SP ? ((r & 1) | ((i & 1) << 1) | (((i >> 1) & 1) << 2)) : (r | (((i >> 1) & 1) << 2));
    glds16(base, (unsigned)((4 * i + r) * (HD * 2) + (((l & 15) ^ (f << 1)) << 4)), sb + (w >> 2) * 65536 + hf * 32768 + (w & 3) * 8192 + i * 1024);
  }
}

__device__ __forceinline__ unsigned row_stats(const v4u raw) {
  float v[8];
#pragma unroll
  for (int e = 0; e < 4; ++e) { v[2 * e] = lo16(raw[e]); v[2 * e + 1] = hi16(raw[e]); }
  float s = 0.f;
#pragma unroll
  for (int e = 0; e < 8; ++e) s += v[e];
  const float mean = wave_sum(s) * (1.0f / DM);
  float q = 0.f;
#pragma unroll
  for (int e = 0; e < 8; ++e) { const float t = v[e] - mean; q += t * t; }
  const float var = wave_sum(q) * (1.0f / DM);
  const float sd = bfr(sqrtf(bfr(bfr(var) + 1e-5f)));
  return (unsigned)f2bf(mean) | ((unsigned)f2bf(sd) << 16);
}
__device__ __forceinline__ void ask_slow(const MegaArgs& A, const int k, unsigned (&t)[3]) {
  const int l = lane_id(), x = blockIdx.x & 7;
  const char *wq = (const char*)A.Wq, *wv = (const char*)A.Wv;
  asm("" : "+s"(wq), "+s"(wv));
#pragma unroll
  for (int m = 0; m < 3; ++m) {
    const char* W = m == 1 ? (const char*)A.Wk : ((m == 0) == (x < 4)) ? wv : wq;
    const int a = (int)((unsigned long)W >> 8) + 8 * (x & 3), h = k < 32 ? ((1 - a) & 3) + 4 * (k & 1) : (31 - a) & 31;
    const int r = k < 32 ? k >> 1 : k - 32;
    t[m] = __builtin_amdgcn_raw_buffer_load_b32(MKRS(W, (unsigned)k < 48u && h < 8 ? DM * HD * 2 : 0), (l >> 1) * (HD * 2) + (l & 1) * 128,
                                                (32 * (r & 15)) * (HD * 2) + (8 * (x & 3) + (h & 7)) * 256, 0);
  }
}
#define ASKED(t) asm volatile("" ::"v"(t[0]), "v"(t[1]), "v"(t[2]))
template <int N>
__device__ __forceinline__ u64 start_stats(const MegaArgs& A, const int idx) {
  const int w = A.wv, l = lane_id();
  const int r0 = N * (8 * idx + w);
  unsigned ta[3], tb[3];
  v4u raw[N];
#pragma unroll
  for (int i = 0; i < N; ++i) raw[i] = *reinterpret_cast<const v4u*>(A.x0 + (size_t)(r0 + i) * DM + l * 8);
  asm volatile("" ::: "memory");
  ask_slow(A, 16 * w + (idx & 3) + (idx >> 4) * N, ta);
  if (N > 4) ask_slow(A, 16 * w + (idx & 3) + 12, tb);
  unsigned tc[2] = {};
  if (N == 4 && AWH > 0) tc[0] = *reinterpret_cast<const unsigned*>(A.Wo + (size_t)idx * 65536 + 32768 + (64 * w + l) * 64);
  if (N == 4 && AWH > 1 && w >= 4) tc[1] = *reinterpret_cast<const unsigned*>(A.Wo + (size_t)idx * 65536 + (64 * w + l) * 64);
  const u64 tag = call_tag(A);
  unsigned stw = 0u;
#pragma unroll
  for (int i = 0; i < N; ++i) {
    const unsigned t = row_stats(raw[i]);
    if ((l >> 1) == i) stw = t;
  }
  if (l < 2 * N) __hip_atomic_store((gu64*)(A.ws + OFF_GT) + 2 * r0 + l, tagged(tag, (l & 1) ? stw : stw << 16), RLX_AGENT);
  SST(0);
  ASKED(ta);
  if (N > 4) ASKED(tb);
  asm volatile("" ::"v"(tc[0]), "v"(tc[1]));
  return tag;
}
#define MFMAH(a, b, c) __builtin_amdgcn_mfma_f32_16x16x32_f16(a, b, c, 0, 0, 0)
struct Cx {
  f32x2_t r, i;
};
__device__ __forceinline__ Cx cmul(const Cx a, const Cx b) { return Cx{a.r * b.r - a.i * b.i, a.r * b.i + a.i * b.r}; }
struct KGen {
  unsigned pa, pb, pc, pd;
  u16 pe;
  f32x2_t ar, ai;
  Cx C;
  char* st;
  v4u bh[8], bl[8];
  __device__ __forceinline__ void load(const MegaArgs& A, const int d, const int l) {
    pa = *reinterpret_cast<const unsigned*>(A.logAre + d * NST + 2 * l);
    pb = *reinterpret_cast<const unsigned*>(A.Aim + d * NST + 2 * l);
    pc = *reinterpret_cast<const unsigned*>(A.Cre + d * NST + 2 * l);
    pd = *reinterpret_cast<const unsigned*>(A.Cim + d * NST + 2 * l);
    pe = A.logdt[d];
  }
  __device__ __forceinline__ void setup() {
    const float dt = bfr(expf(bf2f(pe)));
    ar = f32x2_t{-bfr(expf(lo16(pa))) * dt, -bfr(expf(hi16(pa))) * dt};
    ai = f32x2_t{lo16(pb) * dt, hi16(pb) * dt};
    C = Cx{f32x2_t{lo16(pc) * dt, hi16(pc) * dt}, f32x2_t{lo16(pd) * dt, hi16(pd) * dt}};
  }
  __device__ __forceinline__ float scale() {
    const float b = 2.0f * wave_sum((fabsf(C.r.x) + fabsf(C.r.y)) + (fabsf(C.i.x) + fabsf(C.i.y)));
    const unsigned eb = max(__float_as_uint(b) >> 23, 25u);
    const float ks = __uint_as_float((268u - eb) << 23);
    C.r *= ks;
    C.i *= ks;
    return __uint_as_float((eb - 24u) << 23);
  }
  __device__ __forceinline__ Cx power(const float p) const {
    Cx z;
#pragma unroll
    for (int e = 0; e < 2; ++e) {
      const float x = ai[e] * p;
      const float f = __builtin_fmaf(x, 6.4206382e-9f, __builtin_fmaf(x, 0.15915494f, -__builtin_rintf(x * 0.15915494f)));
      const float m = __expf(ar[e] * p);
      z.r[e] = m * __builtin_amdgcn_cosf(f);
      z.i[e] = m * __builtin_amdgcn_sinf(f);
    }
    return z;
  }
  static __device__ __forceinline__ unsigned rest(const f32x2_t y, const unsigned h) {
    unsigned r;
    asm("v_fma_mixlo_f16 %0, %1, -1.0, %2 op_sel_hi:[1,0,0]" : "=v"(r) : "v"(h), "v"(y.x));
    asm("v_fma_mixhi_f16 %0, %1, -1.0, %2 op_sel:[1,0,0] op_sel_hi:[1,0,0]" : "+v"(r) : "v"(h), "v"(y.y));
    return r;
  }
  __device__ __forceinline__ char* rows(char* p, Cx& y, const Cx s, const int n) const {
#pragma unroll 1
    for (int i = 0; i < n; i += 2) {
#pragma unroll
      for (int k = 0; k < 2; ++k) {
        const unsigned h0 = pkh(y.r.x, y.r.y), h1 = pkh(y.i.x, y.i.y);
        *reinterpret_cast<v2u*>(p + 1040 * k) = v2u{h0, h1};
        *reinterpret_cast<v2u*>(p + 1040 * k + 512) = v2u{rest(y.r, h0), rest(y.i, h1)};
        y = cmul(y, s);
      }
      p += 2080;
    }
    return p;
  }
  __device__ __forceinline__ void get(v4u (&h)[8], v4u (&o)[8], const int l) const {
    const char* p = st + (l & 15) * 1040 + (l >> 4) * 16;
    asm volatile("" ::: "memory");
#pragma unroll
    for (int k = 0; k < 8; ++k) {
      h[k] = *reinterpret_cast<const v4u*>(p + 64 * k);
      o[k] = *reinterpret_cast<const v4u*>(p + 512 + 64 * k);
    }
    asm volatile("" ::: "memory");
  }
  Cx cy, cs;
  __device__ __forceinline__ void columns(const int l, const int j0, const int j1) {
    if (j0 == 0) {
      cs = power(16.f);
      cs.i = -cs.i;
      cy = Cx{f32x2_t{16384.f, 16384.f}, f32x2_t{0.f, 0.f}};
    }
    rows(st + l * 8 + 1040 * j0, cy, cs, j1 - j0);
  }
  template <bool COL>
  __device__ __forceinline__ f32x4 tile(const int l) const {
    v4u h[8], o[8];
    get(h, o, l);
    f32x4 d = {}, e = {}, f = {};
#define H8(x) __builtin_bit_cast(f16x8, x)
#define KMM(a, b, d) (COL ? MFMAH(H8(b), H8(a), d) : MFMAH(H8(a), H8(b), d))
#pragma unroll
    for (int k = 0; k < 8; ++k) {
      d = KMM(h[k], bh[k], d);
      e = KMM(o[k], bh[k], e);
      f = KMM(h[k], bl[k], f);
    }
#undef KMM
#undef H8
    return (d + (e + f)) * (1.0f / 8192);
  }
};
#define ACC_INIT 0xc9b26c86c9b26ca0ull
#define CHAIN_ASM_ATT \
  "s_mov_b32 s95, 272\n" \
  "s_mov_b32 s91, 0x4c7c2000\n" \
  "s_mov_b32 s85, 0xc9b26c9b\n" \
  "s_mov_b32 s92, 0xc9326c9b\n" \
  "s_mov_b32 s93, 0xc9326c9b\n" \
  "s_mov_b32 s78, 0x3e0293ee\n" \
  "s_mov_b32 s76, 0x33333333\n" \
  "s_mov_b32 s77, 0x33333333\n" \
  "s_lshl_b32 s98, s73, 10\n" \
  "s_add_u32 s98, s98, s74\n" \
  "s_add_u32 s98, s98, 131072\n" \
  "s_lshl_b32 s97, s73, 9\n" \
  "s_add_u32 s97, s97, s74\n" \
  "s_add_u32 s97, s97, 158720\n" \
  "s_lshl_b32 s99, s73, 10\n" \
  "v_mbcnt_lo_u32_b32 v48, -1, 0\n" \
  "v_mbcnt_hi_u32_b32 v48, -1, v48\n" \
  "s_add_u32 s88, s74, 157760\n" \
  "v_mov_b32 v59, s88\n" \
  "v_lshrrev_b32 v49, 2, v48\n" \
  "v_lshrrev_b32 v50, 4, v48\n" \
  "v_bitop3_b32 v50, v49, v50, 3 bitop3:0x28\n" \
  "v_lshl_add_u32 v8, v50, 3, s97\n" \
  "v_and_b32 v50, 3, v48\n" \
  "v_lshl_add_u32 v8, v50, 5, v8\n" \
  "v_and_b32 v49, 1, v49\n" \
  "v_lshl_add_u32 v8, v49, 8, v8\n" \
  "v_and_b32 v49, 8, v48\n" \
  "v_lshl_add_u32 v8, v49, 4, v8\n" \
  "v_lshrrev_b32 v49, 5, v48\n" \
  "v_add_u32 v10, s99, v1\n" \
  "s_lshr_b32 s88, s99, 2\n" \
  "s_add_u32 s88, s88, s74\n" \
  "s_add_u32 s88, s88, 139264\n" \
  "v_lshl_add_u32 v7, v49, 10, s88\n" \
  "v_and_b32 v49, 31, v48\n" \
  "v_lshl_add_u32 v7, v49, 3, v7\n" \
  "v_mov_b64 v[12:13], 0\n" \
  "v_mov_b64 v[14:15], 0\n" \
  "v_mov_b64 v[16:17], 0\n" \
  "v_mov_b64 v[18:19], 0\n" \
  "v_mov_b64 v[20:21], 0\n" \
  "v_mov_b64 v[22:23], 0\n" \
  "v_mov_b32 v34, 0xc9b26ca0\n" \
  "v_mov_b32 v35, 0xc9b26c86\n" \
  "s_mov_b32 s87, 0x40000\n" \
  "v_subrev_u32 v30, 64, v59\n" \
  "v_add_u32 v17, 0x10000, v28\n" \
  "v_xor_b32 v18, 128, v28\n" \
  "v_xor_b32 v19, 128, v17\n" \
  "ds_write_b32 v30, v12 offset:0\n" \
  "ds_write_b32 v30, v12 offset:32\n" \
  "ds_write_b32 v30, v12 offset:64\n" \
  "v_xor_b32 v20, 0, v29\n" \
  "v_xor_b32 v21, 32, v29\n" \
  "v_xor_b32 v22, 64, v29\n" \
  "v_xor_b32 v23, 96, v29\n" \
  "s_waitcnt vmcnt(12) lgkmcnt(0)\n" \
  "s_barrier\n" \
  "ds_read_b64_tr_b16 v[160:161], v20 offset:0\n" \
  "ds_read_b64_tr_b16 v[162:163], v20 offset:2048\n" \
  "ds_read_b64_tr_b16 v[168:169], v21 offset:0\n" \
  "ds_read_b64_tr_b16 v[170:171], v21 offset:2048\n" \
  "ds_read_b64_tr_b16 v[176:177], v22 offset:0\n" \
  "ds_read_b64_tr_b16 v[178:179], v22 offset:2048\n" \
  "ds_read_b64_tr_b16 v[184:185], v23 offset:0\n" \
  "ds_read_b64_tr_b16 v[186:187], v23 offset:2048\n" \
  "s_lshl_b32 s88, s73, 10\n" \
  "v_add_u32 v48, s88, v1\n" \
  "ds_read_b128 v[52:55], v48 offset:0\n" \
  "s_waitcnt lgkmcnt(0)\n" \
  "v_mov_b32 v11, 0\n" \
  "v_dot2c_f32_bf16 v11, v52, v52\n" \
  "v_dot2c_f32_bf16 v11, v53, v53\n" \
  "v_dot2c_f32_bf16 v11, v54, v54\n" \
  "v_dot2c_f32_bf16 v11, v55, v55\n" \
  "s_nop 0\n" \
  "s_nop 1\n" \
  "v_max_u32_dpp v11, v11, v11 row_ror:1 row_mask:0xf bank_mask:0xf\n" \
  "s_nop 1\n" \
  "v_max_u32_dpp v11, v11, v11 row_ror:2 row_mask:0xf bank_mask:0xf\n" \
  "s_nop 1\n" \
  "v_max_u32_dpp v11, v11, v11 row_ror:4 row_mask:0xf bank_mask:0xf\n" \
  "s_nop 1\n" \
  "v_max_u32_dpp v11, v11, v11 row_ror:8 row_mask:0xf bank_mask:0xf\n" \
  "v_mov_b32 v31, v11\n" \
  "s_nop 1\n" \
  "v_permlane16_swap_b32 v11, v31\n" \
  "v_max_u32 v11, v11, v31\n" \
  "v_mov_b32 v31, v11\n" \
  "s_nop 1\n" \
  "v_permlane32_swap_b32 v11, v31\n" \
  "v_max_u32 v11, v11, v31\n" \
  "s_mov_b64 exec, 1\n" \
  "ds_max_u32 v59, v11\n" \
  "s_mov_b64 exec, -1\n" \
  "s_waitcnt vmcnt(8) lgkmcnt(0)\n" \
  "s_barrier\n" \
  "ds_read_b64_tr_b16 v[164:165], v20 offset:4096\n" \
  "ds_read_b64_tr_b16 v[166:167], v20 offset:6144\n" \
  "ds_read_b64_tr_b16 v[172:173], v21 offset:4096\n" \
  "ds_read_b64_tr_b16 v[174:175], v21 offset:6144\n" \
  "ds_read_b64_tr_b16 v[180:181], v22 offset:4096\n" \
  "ds_read_b64_tr_b16 v[182:183], v22 offset:6144\n" \
  "ds_read_b64_tr_b16 v[188:189], v23 offset:4096\n" \
  "ds_read_b64_tr_b16 v[190:191], v23 offset:6144\n" \
  "s_add_u32 m0, s19, 0\n" \
  "s_nop 0\n" \
  "global_load_lds_dwordx4 v24, s[4:5] offset:0\n" \
  "global_load_lds_dwordx4 v25, s[4:5] offset:1024\n" \
  "global_load_lds_dwordx4 v26, s[4:5] offset:2048\n" \
  "global_load_lds_dwordx4 v27, s[4:5] offset:3072\n" \
  "s_add_u32 s4, s4, 0x100000\n" \
  "s_addc_u32 s5, s5, 0\n" \
  "v_mfma_f32_16x16x32_bf16 v[64:67], v[160:163], v[160:163], 0\n" \
  "v_mfma_f32_16x16x32_bf16 v[68:71], v[168:171], v[168:171], 0\n" \
  "v_mfma_f32_16x16x32_bf16 v[72:75], v[176:179], v[176:179], 0\n" \
  "v_mfma_f32_16x16x32_bf16 v[76:79], v[184:187], v[184:187], 0\n" \
  "s_waitcnt vmcnt(8) lgkmcnt(0)\n" \
  "s_barrier\n" \
  "ds_read_b64_tr_b16 v[32:33], v20 offset:8192\n" \
  "ds_read_b64_tr_b16 v[34:35], v20 offset:10240\n" \
  "ds_read_b64_tr_b16 v[40:41], v21 offset:8192\n" \
  "ds_read_b64_tr_b16 v[42:43], v21 offset:10240\n" \
  "ds_read_b64_tr_b16 v[48:49], v22 offset:8192\n" \
  "ds_read_b64_tr_b16 v[50:51], v22 offset:10240\n" \
  "ds_read_b64_tr_b16 v[56:57], v23 offset:8192\n" \
  "ds_read_b64_tr_b16 v[58:59], v23 offset:10240\n" \
  "s_add_u32 m0, s19, 4096\n" \
  "s_nop 0\n" \
  "global_load_lds_dwordx4 v24, s[4:5] offset:0\n" \
  "global_load_lds_dwordx4 v25, s[4:5] offset:1024\n" \
  "global_load_lds_dwordx4 v26, s[4:5] offset:2048\n" \
  "global_load_lds_dwordx4 v27, s[4:5] offset:3072\n" \
  "s_add_u32 s4, s4, 0x100000\n" \
  "s_addc_u32 s5, s5, 0\n" \
  "v_mfma_f32_16x16x32_bf16 v[64:67], v[164:167], v[164:167], v[64:67]\n" \
  "v_mfma_f32_16x16x32_bf16 v[68:71], v[172:175], v[172:175], v[68:71]\n" \
  "v_mfma_f32_16x16x32_bf16 v[72:75], v[180:183], v[180:183], v[72:75]\n" \
  "v_mfma_f32_16x16x32_bf16 v[76:79], v[188:191], v[188:191], v[76:79]\n" \
  "s_waitcnt vmcnt(8) lgkmcnt(0)\n" \
  "s_barrier\n" \
  "ds_read_b64_tr_b16 v[36:37], v20 offset:12288\n" \
  "ds_read_b64_tr_b16 v[38:39], v20 offset:14336\n" \
  "ds_read_b64_tr_b16 v[44:45], v21 offset:12288\n" \
  "ds_read_b64_tr_b16 v[46:47], v21 offset:14336\n" \
  "ds_read_b64_tr_b16 v[52:53], v22 offset:12288\n" \
  "ds_read_b64_tr_b16 v[54:55], v22 offset:14336\n" \
  "ds_read_b64_tr_b16 v[60:61], v23 offset:12288\n" \
  "ds_read_b64_tr_b16 v[62:63], v23 offset:14336\n" \
  "s_add_u32 m0, s19, 8192\n" \
  "s_nop 0\n" \
  "global_load_lds_dwordx4 v24, s[4:5] offset:0\n" \
  "global_load_lds_dwordx4 v25, s[4:5] offset:1024\n" \
  "global_load_lds_dwordx4 v26, s[4:5] offset:2048\n" \
  "global_load_lds_dwordx4 v27, s[4:5] offset:3072\n" \
  "s_add_u32 s4, s4, 0x100000\n" \
  "s_addc_u32 s5, s5, 0\n" \
  "v_mfma_f32_16x16x32_bf16 v[64:67], v[32:35], v[32:35], v[64:67]\n" \
  "v_mfma_f32_16x16x32_bf16 v[68:71], v[40:43], v[40:43], v[68:71]\n" \
  "v_mfma_f32_16x16x32_bf16 v[72:75], v[48:51], v[48:51], v[72:75]\n" \
  "v_mfma_f32_16x16x32_bf16 v[76:79], v[56:59], v[56:59], v[76:79]\n" \
  "s_waitcnt vmcnt(8) lgkmcnt(0)\n" \
  "s_barrier\n" \
  "s_add_u32 m0, s19, 12288\n" \
  "s_nop 0\n" \
  "global_load_lds_dwordx4 v24, s[4:5] offset:0\n" \
  "global_load_lds_dwordx4 v25, s[4:5] offset:1024\n" \
  "global_load_lds_dwordx4 v26, s[4:5] offset:2048\n" \
  "global_load_lds_dwordx4 v27, s[4:5] offset:3072\n" \
  "s_add_u32 s4, s4, 0x100000\n" \
  "s_addc_u32 s5, s5, 0\n" \
  "v_mfma_f32_16x16x32_bf16 v[64:67], v[36:39], v[36:39], v[64:67]\n" \
  "v_mfma_f32_16x16x32_bf16 v[68:71], v[44:47], v[44:47], v[68:71]\n" \
  "v_mfma_f32_16x16x32_bf16 v[72:75], v[52:55], v[52:55], v[72:75]\n" \
  "v_mfma_f32_16x16x32_bf16 v[76:79], v[60:63], v[60:63], v[76:79]\n" \
  "s_nop 7\n" \
  "v_max3_f32 v13, v64, v65, v66\n" \
  "v_max3_f32 v13, v13, v67, v68\n" \
  "v_max3_f32 v13, v13, v69, v70\n" \
  "v_max3_f32 v13, v13, v71, v72\n" \
  "v_max3_f32 v13, v13, v73, v74\n" \
  "v_max3_f32 v13, v13, v75, v76\n" \
  "v_max3_f32 v13, v13, v77, v78\n" \
  "v_max_f32 v13, v13, v79\n" \
  "s_nop 1\n" \
  "v_max_f32_dpp v13, v13, v13 row_ror:1 row_mask:0xf bank_mask:0xf\n" \
  "s_nop 1\n" \
  "v_max_f32_dpp v13, v13, v13 row_ror:2 row_mask:0xf bank_mask:0xf\n" \
  "s_nop 1\n" \
  "v_max_f32_dpp v13, v13, v13 row_ror:4 row_mask:0xf bank_mask:0xf\n" \
  "s_nop 1\n" \
  "v_max_f32_dpp v13, v13, v13 row_ror:8 row_mask:0xf bank_mask:0xf\n" \
  "v_mov_b32 v31, v13\n" \
  "s_nop 1\n" \
  "v_permlane16_swap_b32 v13, v31\n" \
  "v_max_f32 v13, v13, v31\n" \
  "v_mov_b32 v31, v13\n" \
  "s_nop 1\n" \
  "v_permlane32_swap_b32 v13, v31\n" \
  "v_max_f32 v13, v13, v31\n" \
  "s_mov_b64 exec, 1\n" \
  "ds_max_u32 v30, v13 offset:32\n" \
  "s_mov_b64 exec, -1\n" \
  "s_waitcnt vmcnt(8) lgkmcnt(0)\n" \
  "s_barrier\n" \
  "s_add_u32 m0, s19, 0\n" \
  "s_nop 0\n" \
  "global_load_lds_dwordx4 v24, s[6:7] offset:0\n" \
  "global_load_lds_dwordx4 v25, s[6:7] offset:1024\n" \
  "global_load_lds_dwordx4 v26, s[6:7] offset:2048\n" \
  "global_load_lds_dwordx4 v27, s[6:7] offset:3072\n" \
  "s_add_u32 s6, s6, 0x100000\n" \
  "s_addc_u32 s7, s7, 0\n" \
  "s_waitcnt vmcnt(8) lgkmcnt(0)\n" \
  "s_barrier\n" \
  "s_add_u32 m0, s19, 4096\n" \
  "s_nop 0\n" \
  "global_load_lds_dwordx4 v24, s[6:7] offset:0\n" \
  "global_load_lds_dwordx4 v25, s[6:7] offset:1024\n" \
  "global_load_lds_dwordx4 v26, s[6:7] offset:2048\n" \
  "global_load_lds_dwordx4 v27, s[6:7] offset:3072\n" \
  "s_add_u32 s6, s6, 0x100000\n" \
  "s_addc_u32 s7, s7, 0\n" \
  "s_waitcnt vmcnt(8) lgkmcnt(0)\n" \
  "s_barrier\n" \
  "s_add_u32 m0, s19, 8192\n" \
  "s_nop 0\n" \
  "global_load_lds_dwordx4 v24, s[6:7] offset:0\n" \
  "global_load_lds_dwordx4 v25, s[6:7] offset:1024\n" \
  "global_load_lds_dwordx4 v26, s[6:7] offset:2048\n" \
  "global_load_lds_dwordx4 v27, s[6:7] offset:3072\n" \
  "s_add_u32 s6, s6, 0x100000\n" \
  "s_addc_u32 s7, s7, 0\n" \
  "s_waitcnt vmcnt(8) lgkmcnt(0)\n" \
  "s_barrier\n" \
  "ds_read_b64_tr_b16 v[64:65], v28 offset:0\n" \
  "ds_read_b64_tr_b16 v[66:67], v28 offset:512\n" \
  "ds_read_b64_tr_b16 v[68:69], v28 offset:32768\n" \
  "ds_read_b64_tr_b16 v[70:71], v28 offset:33280\n" \
  "ds_read_b64_tr_b16 v[72:73], v17 offset:0\n" \
  "ds_read_b64_tr_b16 v[74:75], v17 offset:512\n" \
  "ds_read_b64_tr_b16 v[76:77], v17 offset:32768\n" \
  "ds_read_b64_tr_b16 v[78:79], v17 offset:33280\n" \
  "ds_read_b64_tr_b16 v[192:193], v18 offset:0\n" \
  "ds_read_b64_tr_b16 v[194:195], v18 offset:512\n" \
  "ds_read_b64_tr_b16 v[196:197], v18 offset:32768\n" \
  "ds_read_b64_tr_b16 v[198:199], v18 offset:33280\n" \
  "ds_read_b64_tr_b16 v[200:201], v19 offset:0\n" \
  "ds_read_b64_tr_b16 v[202:203], v19 offset:512\n" \
  "ds_read_b64_tr_b16 v[204:205], v19 offset:32768\n" \
  "ds_read_b64_tr_b16 v[206:207], v19 offset:33280\n" \
  "s_add_u32 m0, s19, 12288\n" \
  "s_nop 0\n" \
  "global_load_lds_dwordx4 v24, s[6:7] offset:0\n" \
  "global_load_lds_dwordx4 v25, s[6:7] offset:1024\n" \
  "global_load_lds_dwordx4 v26, s[6:7] offset:2048\n" \
  "global_load_lds_dwordx4 v27, s[6:7] offset:3072\n" \
  "s_add_u32 s6, s6, 0x100000\n" \
  "s_addc_u32 s7, s7, 0\n" \
  "s_waitcnt vmcnt(8) lgkmcnt(0)\n" \
  "s_barrier\n" \
  "ds_read_b64_tr_b16 v[80:81], v28 offset:4096\n" \
  "ds_read_b64_tr_b16 v[82:83], v28 offset:4608\n" \
  "ds_read_b64_tr_b16 v[84:85], v28 offset:36864\n" \
  "ds_read_b64_tr_b16 v[86:87], v28 offset:37376\n" \
  "ds_read_b64_tr_b16 v[88:89], v17 offset:4096\n" \
  "ds_read_b64_tr_b16 v[90:91], v17 offset:4608\n" \
  "ds_read_b64_tr_b16 v[92:93], v17 offset:36864\n" \
  "ds_read_b64_tr_b16 v[94:95], v17 offset:37376\n" \
  "ds_read_b64_tr_b16 v[208:209], v18 offset:4096\n" \
  "ds_read_b64_tr_b16 v[210:211], v18 offset:4608\n" \
  "ds_read_b64_tr_b16 v[212:213], v18 offset:36864\n" \
  "ds_read_b64_tr_b16 v[214:215], v18 offset:37376\n" \
  "ds_read_b64_tr_b16 v[216:217], v19 offset:4096\n" \
  "ds_read_b64_tr_b16 v[218:219], v19 offset:4608\n" \
  "ds_read_b64_tr_b16 v[220:221], v19 offset:36864\n" \
  "ds_read_b64_tr_b16 v[222:223], v19 offset:37376\n" \
  "s_add_u32 m0, s19, 0\n" \
  "s_nop 0\n" \
  "global_load_lds_dwordx4 v24, s[8:9] offset:0\n" \
  "global_load_lds_dwordx4 v25, s[8:9] offset:1024\n" \
  "global_load_lds_dwordx4 v26, s[8:9] offset:2048\n" \
  "global_load_lds_dwordx4 v27, s[8:9] offset:3072\n" \
  "s_add_u32 s8, s8, 0x100000\n" \
  "s_addc_u32 s9, s9, 0\n" \
  "s_waitcnt vmcnt(8) lgkmcnt(0)\n" \
  "s_barrier\n" \
  "ds_read_b64_tr_b16 v[96:97], v28 offset:8192\n" \
  "ds_read_b64_tr_b16 v[98:99], v28 offset:8704\n" \
  "ds_read_b64_tr_b16 v[100:101], v28 offset:40960\n" \
  "ds_read_b64_tr_b16 v[102:103], v28 offset:41472\n" \
  "ds_read_b64_tr_b16 v[104:105], v17 offset:8192\n" \
  "ds_read_b64_tr_b16 v[106:107], v17 offset:8704\n" \
  "ds_read_b64_tr_b16 v[108:109], v17 offset:40960\n" \
  "ds_read_b64_tr_b16 v[110:111], v17 offset:41472\n" \
  "ds_read_b64_tr_b16 v[224:225], v18 offset:8192\n" \
  "ds_read_b64_tr_b16 v[226:227], v18 offset:8704\n" \
  "ds_read_b64_tr_b16 v[228:229], v18 offset:40960\n" \
  "ds_read_b64_tr_b16 v[230:231], v18 offset:41472\n" \
  "ds_read_b64_tr_b16 v[232:233], v19 offset:8192\n" \
  "ds_read_b64_tr_b16 v[234:235], v19 offset:8704\n" \
  "ds_read_b64_tr_b16 v[236:237], v19 offset:40960\n" \
  "ds_read_b64_tr_b16 v[238:239], v19 offset:41472\n" \
  "s_add_u32 m0, s19, 4096\n" \
  "s_nop 0\n" \
  "global_load_lds_dwordx4 v24, s[8:9] offset:0\n" \
  "global_load_lds_dwordx4 v25, s[8:9] offset:1024\n" \
  "global_load_lds_dwordx4 v26, s[8:9] offset:2048\n" \
  "global_load_lds_dwordx4 v27, s[8:9] offset:3072\n" \
  "s_add_u32 s8, s8, 0x100000\n" \
  "s_addc_u32 s9, s9, 0\n" \
  "s_waitcnt vmcnt(8) lgkmcnt(0)\n" \
  "s_barrier\n" \
  "ds_read_b64_tr_b16 v[112:113], v28 offset:12288\n" \
  "ds_read_b64_tr_b16 v[114:115], v28 offset:12800\n" \
  "ds_read_b64_tr_b16 v[116:117], v28 offset:45056\n" \
  "ds_read_b64_tr_b16 v[118:119], v28 offset:45568\n" \
  "ds_read_b64_tr_b16 v[120:121], v17 offset:12288\n" \
  "ds_read_b64_tr_b16 v[122:123], v17 offset:12800\n" \
  "ds_read_b64_tr_b16 v[124:125], v17 offset:45056\n" \
  "ds_read_b64_tr_b16 v[126:127], v17 offset:45568\n" \
  "ds_read_b64_tr_b16 v[240:241], v18 offset:12288\n" \
  "ds_read_b64_tr_b16 v[242:243], v18 offset:12800\n" \
  "ds_read_b64_tr_b16 v[244:245], v18 offset:45056\n" \
  "ds_read_b64_tr_b16 v[246:247], v18 offset:45568\n" \
  "ds_read_b64_tr_b16 v[248:249], v19 offset:12288\n" \
  "ds_read_b64_tr_b16 v[250:251], v19 offset:12800\n" \
  "ds_read_b64_tr_b16 v[252:253], v19 offset:45056\n" \
  "ds_read_b64_tr_b16 v[254:255], v19 offset:45568\n" \
  "s_add_u32 m0, s19, 8192\n" \
  "s_nop 0\n" \
  "global_load_lds_dwordx4 v24, s[8:9] offset:0\n" \
  "global_load_lds_dwordx4 v25, s[8:9] offset:1024\n" \
  "global_load_lds_dwordx4 v26, s[8:9] offset:2048\n" \
  "global_load_lds_dwordx4 v27, s[8:9] offset:3072\n" \
  "s_add_u32 s8, s8, 0x100000\n" \
  "s_addc_u32 s9, s9, 0\n" \
  "s_waitcnt vmcnt(8) lgkmcnt(0)\n" \
  "s_barrier\n" \
  "ds_read_b64_tr_b16 v[128:129], v28 offset:0\n" \
  "ds_read_b64_tr_b16 v[130:131], v28 offset:512\n" \
  "ds_read_b64_tr_b16 v[132:133], v28 offset:32768\n" \
  "ds_read_b64_tr_b16 v[134:135], v28 offset:33280\n" \
  "ds_read_b64_tr_b16 v[136:137], v17 offset:0\n" \
  "ds_read_b64_tr_b16 v[138:139], v17 offset:512\n" \
  "ds_read_b64_tr_b16 v[140:141], v17 offset:32768\n" \
  "ds_read_b64_tr_b16 v[142:143], v17 offset:33280\n" \
  "s_add_u32 m0, s19, 12288\n" \
  "s_nop 0\n" \
  "global_load_lds_dwordx4 v24, s[8:9] offset:0\n" \
  "global_load_lds_dwordx4 v25, s[8:9] offset:1024\n" \
  "global_load_lds_dwordx4 v26, s[8:9] offset:2048\n" \
  "global_load_lds_dwordx4 v27, s[8:9] offset:3072\n" \
  "s_add_u32 s8, s8, 0x100000\n" \
  "s_addc_u32 s9, s9, 0\n" \
  "s_waitcnt vmcnt(8) lgkmcnt(0)\n" \
  "s_barrier\n" \
  "ds_write_b128 v6, v[160:163] offset:0\n" \
  "ds_write_b128 v6, v[164:167] offset:1024\n" \
  "ds_write_b128 v6, v[168:171] offset:2048\n" \
  "ds_write_b128 v6, v[172:175] offset:3072\n" \
  "ds_read_b64_tr_b16 v[144:145], v28 offset:4096\n" \
  "ds_read_b64_tr_b16 v[146:147], v28 offset:4608\n" \
  "ds_read_b64_tr_b16 v[148:149], v28 offset:36864\n" \
  "ds_read_b64_tr_b16 v[150:151], v28 offset:37376\n" \
  "ds_read_b64_tr_b16 v[152:153], v17 offset:4096\n" \
  "ds_read_b64_tr_b16 v[154:155], v17 offset:4608\n" \
  "ds_read_b64_tr_b16 v[156:157], v17 offset:36864\n" \
  "ds_read_b64_tr_b16 v[158:159], v17 offset:37376\n" \
  "s_waitcnt vmcnt(4) lgkmcnt(0)\n" \
  "s_barrier\n" \
  "ds_write_b128 v6, v[176:179] offset:4096\n" \
  "ds_write_b128 v6, v[180:183] offset:5120\n" \
  "ds_write_b128 v6, v[184:187] offset:6144\n" \
  "ds_write_b128 v6, v[188:191] offset:7168\n" \
  "ds_read_b64_tr_b16 v[160:161], v28 offset:8192\n" \
  "ds_read_b64_tr_b16 v[162:163], v28 offset:8704\n" \
  "ds_read_b64_tr_b16 v[164:165], v28 offset:40960\n" \
  "ds_read_b64_tr_b16 v[166:167], v28 offset:41472\n" \
  "ds_read_b64_tr_b16 v[168:169], v17 offset:8192\n" \
  "ds_read_b64_tr_b16 v[170:171], v17 offset:8704\n" \
  "ds_read_b64_tr_b16 v[172:173], v17 offset:40960\n" \
  "ds_read_b64_tr_b16 v[174:175], v17 offset:41472\n" \
  "s_waitcnt vmcnt(0) lgkmcnt(0)\n" \
  "s_barrier\n" \
  "ds_write_b128 v6, v[32:35] offset:8192\n" \
  "ds_write_b128 v6, v[36:39] offset:9216\n" \
  "ds_write_b128 v6, v[40:43] offset:10240\n" \
  "ds_write_b128 v6, v[44:47] offset:11264\n" \
  "ds_read_b64_tr_b16 v[176:177], v28 offset:12288\n" \
  "ds_read_b64_tr_b16 v[178:179], v28 offset:12800\n" \
  "ds_read_b64_tr_b16 v[180:181], v28 offset:45056\n" \
  "ds_read_b64_tr_b16 v[182:183], v28 offset:45568\n" \
  "ds_read_b64_tr_b16 v[184:185], v17 offset:12288\n" \
  "ds_read_b64_tr_b16 v[186:187], v17 offset:12800\n" \
  "ds_read_b64_tr_b16 v[188:189], v17 offset:45056\n" \
  "ds_read_b64_tr_b16 v[190:191], v17 offset:45568\n" \
  "ds_read_b32 v31, v30 offset:0\n" \
  "s_waitcnt lgkmcnt(0)\n" \
  "s_barrier\n" \
  "ds_write_b128 v6, v[48:51] offset:12288\n" \
  "ds_write_b128 v6, v[52:55] offset:13312\n" \
  "ds_write_b128 v6, v[56:59] offset:14336\n" \
  "ds_write_b128 v6, v[60:63] offset:15360\n" \
  "v_mul_f32 v31, 0x3f8e353f, v31\n" \
  "v_mul_f32 v30, 0x44870000, v31\n" \
  "v_mov_b64 v[12:13], 0\n" \
  "v_mov_b64 v[14:15], 0\n" \
  "v_mov_b64 v[16:17], 0\n" \
  "v_mov_b64 v[18:19], 0\n" \
  "v_mov_b64 v[20:21], 0\n" \
  "v_mov_b64 v[22:23], 0\n" \
  "v_readfirstlane_b32 s72, v31\n" \
  "v_readfirstlane_b32 s86, v30\n" \
  "s_lshr_b32 s86, s86, 23\n" \
  "s_addk_i32 s86, 0xff83\n" \
  "s_ashr_i32 s86, s86, 1\n" \
  "s_max_i32 s86, s86, -20\n" \
  "s_min_i32 s86, s86, 100\n" \
  "s_mov_b32 s82, s86\n" \
  "s_lshr_b32 s83, s72, 23\n" \
  "s_sub_i32 s83, s83, 243\n" \
  "s_waitcnt lgkmcnt(0)\n" \
  "s_add_u32 s88, s74, 157760\n" \
  "v_mov_b32 v59, s88\n" \
  "v_mov_b32 v34, 0xc9b26ca0\n" \
  "v_mov_b32 v35, 0xc9b26c86\n" \
  ".p2align 6\n" \
  "s_nop 0\n" \
  "s_nop 0\n" \
  "s_nop 0\n" \
  ".LCA_TOP_%=:\n" \
  "s_waitcnt lgkmcnt(0)\n" \
  "s_barrier\n" \
  "ds_read_b128 v[40:43], v1 offset:0\n" \
  "ds_read_b128 v[44:47], v1 offset:1024\n" \
  "ds_read_b128 v[48:51], v1 offset:2048\n" \
  "ds_read_b128 v[52:55], v1 offset:3072\n" \
  "ds_read_b128 v[56:59], v1 offset:4096\n" \
  "ds_read_b128 v[60:63], v1 offset:5120\n" \
  "s_cmp_lg_u32 s73, 0\n" \
  "s_cbranch_scc1 .LCA_NOZ_%=\n" \
  "s_xor_b32 s88, s60, 0x10000\n" \
  "s_add_u32 s88, s88, s61\n" \
  "buffer_store_dwordx2 v[34:35], v5, s[40:43], s88 offen sc1\n" \
  ".LCA_NOZ_%=:\n" \
  "s_waitcnt lgkmcnt(5)\n" \
  "v_smfmac_f32_16x16x64_bf16 v[20:23], v[40:43], v[192:199], v0\n" \
  "v_smfmac_f32_16x16x64_bf16 v[12:15], v[40:43], v[64:71], v0\n" \
  "v_smfmac_f32_16x16x64_bf16 v[16:19], v[40:43], v[128:135], v0\n" \
  "ds_read_b128 v[40:43], v1 offset:6144\n" \
  "s_waitcnt lgkmcnt(5)\n" \
  "v_smfmac_f32_16x16x64_bf16 v[20:23], v[44:47], v[200:207], v0\n" \
  "v_smfmac_f32_16x16x64_bf16 v[12:15], v[44:47], v[72:79], v0\n" \
  "v_smfmac_f32_16x16x64_bf16 v[16:19], v[44:47], v[136:143], v0\n" \
  "ds_read_b128 v[44:47], v1 offset:7168\n" \
  "s_waitcnt lgkmcnt(5)\n" \
  "v_smfmac_f32_16x16x64_bf16 v[20:23], v[48:51], v[208:215], v0\n" \
  "v_smfmac_f32_16x16x64_bf16 v[12:15], v[48:51], v[80:87], v0\n" \
  "v_smfmac_f32_16x16x64_bf16 v[16:19], v[48:51], v[144:151], v0\n" \
  "ds_read_b128 v[24:27], v1 offset:10240\n" \
  "ds_read_b128 v[28:31], v1 offset:14336\n" \
  "s_waitcnt lgkmcnt(6)\n" \
  "v_smfmac_f32_16x16x64_bf16 v[20:23], v[52:55], v[216:223], v0\n" \
  "v_smfmac_f32_16x16x64_bf16 v[12:15], v[52:55], v[88:95], v0\n" \
  "v_smfmac_f32_16x16x64_bf16 v[16:19], v[52:55], v[152:159], v0\n" \
  "ds_read_b128 v[32:35], v1 offset:11264\n" \
  "ds_read_b128 v[36:39], v1 offset:15360\n" \
  "s_waitcnt lgkmcnt(7)\n" \
  "v_smfmac_f32_16x16x64_bf16 v[20:23], v[56:59], v[224:231], v0\n" \
  "v_smfmac_f32_16x16x64_bf16 v[12:15], v[56:59], v[96:103], v0\n" \
  "v_smfmac_f32_16x16x64_bf16 v[16:19], v[56:59], v[160:167], v0\n" \
  "ds_read_b128 v[56:59], v1 offset:12288\n" \
  "s_waitcnt lgkmcnt(7)\n" \
  "v_smfmac_f32_16x16x64_bf16 v[20:23], v[60:63], v[232:239], v0\n" \
  "v_smfmac_f32_16x16x64_bf16 v[12:15], v[60:63], v[104:111], v0\n" \
  "v_smfmac_f32_16x16x64_bf16 v[16:19], v[60:63], v[168:175], v0\n" \
  "ds_read_b128 v[60:63], v1 offset:16384\n" \
  "s_waitcnt lgkmcnt(7)\n" \
  "v_smfmac_f32_16x16x64_bf16 v[20:23], v[40:43], v[240:247], v0\n" \
  "v_smfmac_f32_16x16x64_bf16 v[12:15], v[40:43], v[112:119], v0\n" \
  "v_smfmac_f32_16x16x64_bf16 v[16:19], v[40:43], v[176:183], v0\n" \
  "s_waitcnt lgkmcnt(6)\n" \
  "v_smfmac_f32_16x16x64_bf16 v[20:23], v[44:47], v[248:255], v0\n" \
  "v_smfmac_f32_16x16x64_bf16 v[12:15], v[44:47], v[120:127], v0\n" \
  "v_smfmac_f32_16x16x64_bf16 v[16:19], v[44:47], v[184:191], v0\n" \
  "s_cmp_eq_u32 s71, 0\n" \
  "v_permlane32_swap_b32 v20, v22\n" \
  "v_permlane32_swap_b32 v21, v23\n" \
  "v_pk_add_f32 v[20:21], v[20:21], v[22:23]\n" \
  "s_mov_b64 vcc, s[76:77]\n" \
  "v_permlane32_swap_b32 v12, v14\n" \
  "v_permlane32_swap_b32 v13, v15\n" \
  "v_cvt_pk_bf16_f32 v20, v20, v21\n" \
  "v_pk_add_f32 v[12:13], v[12:13], v[14:15]\n" \
  "v_permlane32_swap_b32 v16, v18\n" \
  "v_permlane32_swap_b32 v17, v19\n" \
  "ds_write_b32 v8, v20 offset:2048\n" \
  "v_cvt_pk_bf16_f32 v12, v12, v13\n" \
  "v_pk_add_f32 v[16:17], v[16:17], v[18:19]\n" \
  "s_nop 0\n" \
  "v_cvt_pk_bf16_f32 v13, v16, v17\n" \
  "ds_write_b64 v8, v[12:13]\n" \
  "s_setprio 1\n" \
  "s_waitcnt lgkmcnt(0)\n" \
  "s_barrier\n" \
  "s_cbranch_scc1 .LCA_LAST_%=\n" \
  "ds_read_b64_tr_b16 v[40:41], v2 offset:0\n" \
  "ds_read_b64_tr_b16 v[42:43], v3 offset:0\n" \
  "ds_read_b64_tr_b16 v[44:45], v2 offset:1024\n" \
  "ds_read_b64_tr_b16 v[46:47], v3 offset:1024\n" \
  "ds_read_b64_tr_b16 v[48:49], v2 offset:2048\n" \
  "ds_read_b64_tr_b16 v[50:51], v3 offset:2048\n" \
  "ds_read_b64_tr_b16 v[52:53], v2 offset:3072\n" \
  "ds_read_b64_tr_b16 v[54:55], v3 offset:3072\n" \
  "ds_read_b32 v11, v1 offset:26688\n" \
  "s_waitcnt lgkmcnt(7)\n" \
  "v_mfma_f32_16x16x32_bf16 v[12:15], v[24:27], v[40:43], 0\n" \
  "v_mfma_f32_16x16x32_bf16 v[16:19], v[28:31], v[40:43], 0\n" \
  "ds_read_b128 v[24:27], v1 offset:13312\n" \
  "ds_read_b128 v[28:31], v1 offset:17408\n" \
  "v_mfma_f32_16x16x32_bf16 v[20:23], v[40:43], v[40:43], 0\n" \
  "s_waitcnt lgkmcnt(7)\n" \
  "v_mfma_f32_16x16x32_bf16 v[12:15], v[32:35], v[44:47], v[12:15]\n" \
  "v_mfma_f32_16x16x32_bf16 v[16:19], v[36:39], v[44:47], v[16:19]\n" \
  "v_mfma_f32_16x16x32_bf16 v[20:23], v[44:47], v[44:47], v[20:23]\n" \
  "s_waitcnt lgkmcnt(2)\n" \
  "v_mfma_f32_16x16x32_bf16 v[12:15], v[56:59], v[48:51], v[12:15]\n" \
  "v_readfirstlane_b32 s86, v11\n" \
  "v_mfma_f32_16x16x32_bf16 v[16:19], v[60:63], v[48:51], v[16:19]\n" \
  "ds_read_b128 v[32:35], v6 offset:8192\n" \
  "ds_read_b128 v[36:39], v6 offset:9216\n" \
  "v_mfma_f32_16x16x32_bf16 v[20:23], v[48:51], v[48:51], v[20:23]\n" \
  "s_lshr_b32 s86, s86, 23\n" \
  "s_add_i32 s86, s86, s83\n" \
  "s_ashr_i32 s86, s86, 1\n" \
  "ds_read_b128 v[48:51], v10 offset:18432\n" \
  "v_mfma_f32_16x16x32_bf16 v[20:23], v[52:55], v[52:55], v[20:23]\n" \
  "s_max_i32 s86, s86, s82\n" \
  "s_min_i32 s86, s86, 100\n" \
  "s_lshl_b32 s86, s86, 23\n" \
  "s_sub_i32 s84, 0xc8800000, s86\n" \
  "s_waitcnt lgkmcnt(3)\n" \
  "v_mfma_f32_16x16x32_bf16 v[12:15], v[24:27], v[52:55], v[12:15]\n" \
  "v_mfma_f32_16x16x32_bf16 v[16:19], v[28:31], v[52:55], v[16:19]\n" \
  "ds_read_b128 v[52:55], v10 offset:22528\n" \
  "ds_read_b128 v[56:59], v6 offset:0\n" \
  "ds_read_b128 v[60:63], v6 offset:1024\n" \
  "ds_read_b128 v[24:27], v6 offset:2048\n" \
  "ds_read_b128 v[28:31], v6 offset:3072\n" \
  "v_cndmask_b32_dpp v20, v23, v22, vcc quad_perm:[2,3,0,1] row_mask:0xf bank_mask:0xf\n" \
  "v_mov_b32 v41, 0\n" \
  "v_mov_b32 v42, 0\n" \
  "v_cndmask_b32_dpp v12, v16, v12, vcc quad_perm:[2,3,0,1] row_mask:0xf bank_mask:0xf\n" \
  "v_cndmask_b32_dpp v13, v17, v13, vcc quad_perm:[2,3,0,1] row_mask:0xf bank_mask:0xf\n" \
  "v_cndmask_b32_dpp v14, v18, v14, vcc quad_perm:[2,3,0,1] row_mask:0xf bank_mask:0xf\n" \
  "v_cndmask_b32_dpp v15, v19, v15, vcc quad_perm:[2,3,0,1] row_mask:0xf bank_mask:0xf\n" \
  "v_maximum3_f32 v16, v20, v12, v13\n" \
  "v_maximum3_f32 v16, v16, v14, v15\n" \
  "s_nop 1\n" \
  "v_max_f32_dpp v16, v16, v16 quad_perm:[2,3,0,1] row_mask:0xf bank_mask:0xf\n" \
  "v_mov_b32 v17, v16\n" \
  "s_nop 1\n" \
  "v_permlane16_swap_b32 v16, v17\n" \
  "v_max_f32 v16, v16, v17\n" \
  "v_mov_b32 v17, v16\n" \
  "s_nop 1\n" \
  "v_permlane32_swap_b32 v16, v17\n" \
  "v_max_f32 v16, v16, v17\n" \
  "v_mul_f32 v16, s78, v16\n" \
  "v_pk_fma_f32 v[12:13], v[12:13], s[78:79], v[16:17] op_sel_hi:[1,0,0] neg_lo:[0,0,1] neg_hi:[0,0,1]\n" \
  "v_pk_fma_f32 v[14:15], v[14:15], s[78:79], v[16:17] op_sel_hi:[1,0,0] neg_lo:[0,0,1] neg_hi:[0,0,1]\n" \
  "v_fma_f32 v20, v20, s78, -v16\n" \
  "v_exp_f32 v12, v12\n" \
  "v_exp_f32 v13, v13\n" \
  "v_exp_f32 v14, v14\n" \
  "v_exp_f32 v15, v15\n" \
  "v_exp_f32 v20, v20\n" \
  "s_waitcnt lgkmcnt(4)\n" \
  "v_cvt_pk_bf16_f32 v44, v12, v13\n" \
  "v_cvt_pk_bf16_f32 v45, v14, v15\n" \
  "v_pk_add_f32 v[8:9], v[12:13], v[14:15]\n" \
  "v_mov_b32_dpp v46, v44 quad_perm:[2,3,0,1] row_mask:0xf bank_mask:0xf\n" \
  "v_mov_b32_dpp v47, v45 quad_perm:[2,3,0,1] row_mask:0xf bank_mask:0xf\n" \
  "v_add_f32 v10, v8, v20\n" \
  "v_add_f32 v10, v10, v9\n" \
  "v_mfma_f32_16x16x32_bf16 v[12:15], v[48:51], v[44:47], 0\n" \
  "v_mov_b32_dpp v21, v20 quad_perm:[2,3,0,1] row_mask:0xf bank_mask:0xf\n" \
  "v_add_f32_dpp v10, v10, v10 quad_perm:[2,3,0,1] row_mask:0xf bank_mask:0xf\n" \
  "v_mov_b32 v11, v10\n" \
  "v_mfma_f32_16x16x32_bf16 v[16:19], v[52:55], v[44:47], 0\n" \
  "s_setprio 3\n" \
  "s_barrier\n" \
  "ds_read_b64 v[22:23], v7 offset:0\n" \
  "s_nop 0\n" \
  "v_permlane16_swap_b32 v10, v11\n" \
  "v_add_f32 v10, v10, v11\n" \
  "v_mov_b32 v11, v10\n" \
  "v_mov_b64 v[8:9], s[92:93]\n" \
  "s_nop 0\n" \
  "v_permlane32_swap_b32 v10, v11\n" \
  "v_add_f32 v10, v10, v11\n" \
  "v_rcp_f32 v10, v10\n" \
  "s_nop 0\n" \
  "v_mul_f32 v10, s84, v10\n" \
  "v_pk_mul_f32 v[20:21], v[20:21], v[10:11] op_sel_hi:[1,0]\n" \
  "v_pk_mul_f32 v[12:13], v[12:13], v[10:11] op_sel_hi:[1,0]\n" \
  "v_pk_mul_f32 v[14:15], v[14:15], v[10:11] op_sel_hi:[1,0]\n" \
  "v_cvt_pk_bf16_f32 v40, v20, v21\n" \
  "v_pk_mul_f32 v[16:17], v[16:17], v[10:11] op_sel_hi:[1,0]\n" \
  "v_cvt_pk_bf16_f32 v43, v20, v21\n" \
  "v_pk_mul_f32 v[18:19], v[18:19], v[10:11] op_sel_hi:[1,0]\n" \
  "s_waitcnt lgkmcnt(0)\n" \
  "v_mfma_f32_16x16x16_bf16 v[12:15], v[22:23], v[40:41], v[12:15]\n" \
  "v_mfma_f32_16x16x16_bf16 v[16:19], v[22:23], v[42:43], v[16:19]\n" \
  "v_mov_b64 v[10:11], s[92:93]\n" \
  "ds_read_b128 v[48:51], v6 offset:10240\n" \
  "ds_read_b128 v[52:55], v6 offset:11264\n" \
  "v_mov_b64 v[20:21], s[92:93]\n" \
  "v_mov_b64 v[22:23], s[92:93]\n" \
  "s_nop 1\n" \
  "v_cvt_pk_bf16_f32 v40, v12, v13\n" \
  "v_cvt_pk_bf16_f32 v41, v14, v15\n" \
  "v_cvt_pk_bf16_f32 v42, v16, v17\n" \
  "ds_write_b64 v4, v[40:41]\n" \
  "v_cvt_pk_bf16_f32 v43, v18, v19\n" \
  "ds_write_b64 v4, v[42:43] offset:1024\n" \
  "v_mov_b64 v[12:13], s[92:93]\n" \
  "v_mov_b64 v[14:15], s[92:93]\n" \
  "v_mov_b64 v[16:17], s[92:93]\n" \
  "v_mov_b64 v[18:19], s[92:93]\n" \
  "s_setprio 0\n" \
  "s_waitcnt lgkmcnt(0)\n" \
  "s_barrier\n" \
  "ds_read_b128 v[40:43], v1 offset:8192\n" \
  "ds_read_b128 v[44:47], v1 offset:9216\n" \
  "s_waitcnt lgkmcnt(1)\n" \
  "v_smfmac_f32_16x16x64_bf16 v[12:15], v[40:43], v[56:63], v0\n" \
  "ds_read_b128 v[56:59], v6 offset:4096\n" \
  "ds_read_b128 v[60:63], v6 offset:5120\n" \
  "v_smfmac_f32_16x16x64_bf16 v[16:19], v[40:43], v[24:31], v0\n" \
  "ds_read_b128 v[24:27], v6 offset:6144\n" \
  "ds_read_b128 v[28:31], v6 offset:7168\n" \
  "s_waitcnt lgkmcnt(4)\n" \
  "v_smfmac_f32_16x16x64_bf16 v[12:15], v[44:47], v[32:39], v0\n" \
  "ds_read_b128 v[32:35], v6 offset:12288\n" \
  "ds_read_b128 v[36:39], v6 offset:13312\n" \
  "v_smfmac_f32_16x16x64_bf16 v[16:19], v[44:47], v[48:55], v0\n" \
  "ds_read_b128 v[48:51], v6 offset:14336\n" \
  "ds_read_b128 v[52:55], v6 offset:15360\n" \
  "s_nop 5\n" \
  "v_permlane32_swap_b32 v12, v17\n" \
  "v_permlane32_swap_b32 v13, v16\n" \
  "v_permlane32_swap_b32 v14, v19\n" \
  "v_permlane32_swap_b32 v15, v18\n" \
  "v_pk_add_f32 v[12:13], v[12:13], v[16:17] op_sel:[0,1] op_sel_hi:[1,0]\n" \
  "v_pk_add_f32 v[14:15], v[14:15], v[18:19] op_sel:[0,1] op_sel_hi:[1,0]\n" \
  "buffer_atomic_add_x2 v[12:13], v5, s[40:43], s56 offen\n" \
  "buffer_atomic_add_x2 v[14:15], v5, s[40:43], s57 offen\n" \
  "s_waitcnt lgkmcnt(6)\n" \
  "v_smfmac_f32_16x16x64_bf16 v[20:23], v[40:43], v[56:63], v0\n" \
  "s_waitcnt lgkmcnt(4)\n" \
  "v_smfmac_f32_16x16x64_bf16 v[8:11], v[40:43], v[24:31], v0\n" \
  "s_waitcnt lgkmcnt(2)\n" \
  "v_smfmac_f32_16x16x64_bf16 v[20:23], v[44:47], v[32:39], v0\n" \
  "s_waitcnt lgkmcnt(0)\n" \
  "v_smfmac_f32_16x16x64_bf16 v[8:11], v[44:47], v[48:55], v0\n" \
  "s_nop 7\n" \
  "v_permlane32_swap_b32 v20, v9\n" \
  "v_permlane32_swap_b32 v21, v8\n" \
  "v_permlane32_swap_b32 v22, v11\n" \
  "v_permlane32_swap_b32 v23, v10\n" \
  "v_pk_add_f32 v[20:21], v[20:21], v[8:9] op_sel:[0,1] op_sel_hi:[1,0]\n" \
  "v_pk_add_f32 v[22:23], v[22:23], v[10:11] op_sel:[0,1] op_sel_hi:[1,0]\n" \
  "buffer_atomic_add_x2 v[20:21], v5, s[40:43], s58 offen\n" \
  "buffer_atomic_add_x2 v[22:23], v5, s[40:43], s59 offen\n" \
  "s_memrealtime s[38:39]\n" \
  "v_mbcnt_lo_u32_b32 v48, -1, 0\n" \
  "v_mbcnt_hi_u32_b32 v48, -1, v48\n" \
  "s_add_u32 s88, s74, 157760\n" \
  "v_mov_b32 v59, s88\n" \
  "v_lshl_add_u32 v52, v48, 2, v59\n" \
  "ds_read_b32 v52, v52 offset:192\n" \
  "v_lshrrev_b32 v49, 3, v48\n" \
  "v_and_b32 v50, 7, v48\n" \
  "v_lshlrev_b32 v50, 4, v50\n" \
  "v_lshl_or_b32 v44, v49, 8, v50\n" \
  "s_mov_b32 s87, 0x40000\n" \
  "v_lshrrev_b32 v49, 2, v48\n" \
  "v_lshrrev_b32 v50, 4, v48\n" \
  "v_bitop3_b32 v50, v49, v50, 3 bitop3:0x28\n" \
  "v_lshl_add_u32 v8, v50, 3, s97\n" \
  "v_and_b32 v50, 3, v48\n" \
  "v_lshl_add_u32 v8, v50, 5, v8\n" \
  "v_and_b32 v49, 1, v49\n" \
  "v_lshl_add_u32 v8, v49, 8, v8\n" \
  "v_and_b32 v49, 8, v48\n" \
  "v_lshl_add_u32 v8, v49, 4, v8\n" \
  "v_lshrrev_b32 v49, 5, v48\n" \
  "v_add_u32 v10, s99, v1\n" \
  "s_lshr_b32 s88, s99, 2\n" \
  "s_add_u32 s88, s88, s74\n" \
  "s_add_u32 s88, s88, 139264\n" \
  "v_lshl_add_u32 v7, v49, 10, s88\n" \
  "v_and_b32 v49, 31, v48\n" \
  "v_lshl_add_u32 v7, v49, 3, v7\n" \
  "v_mov_b64 v[12:13], 0\n" \
  "v_mov_b64 v[14:15], 0\n" \
  "v_mov_b64 v[16:17], 0\n" \
  "v_mov_b64 v[18:19], 0\n" \
  "v_mov_b64 v[20:21], 0\n" \
  "v_mov_b64 v[22:23], 0\n" \
  "v_mov_b32 v34, 0xc9b26ca0\n" \
  "v_mov_b32 v35, 0xc9b26c86\n" \
  "v_and_b32 v50, 3, v48\n" \
  "v_lshl_add_u32 v40, v50, 2, s98\n" \
  "v_lshrrev_b32 v50, 4, v48\n" \
  "v_mad_u32_u24 v40, v50, s95, v40\n" \
  "v_bfe_u32 v49, v48, 3, 1\n" \
  "v_lshl_add_u32 v40, v49, 6, v40\n" \
  "v_bfe_u32 v49, v48, 2, 1\n" \
  "v_lshl_add_u32 v40, v49, 7, v40\n" \
  "v_xor_b32 v41, 16, v40\n" \
  "v_xor_b32 v42, 32, v40\n" \
  "v_xor_b32 v43, 48, v40\n" \
  "s_add_i32 s80, s86, 0x35000000\n" \
  "v_mov_b32 v11, 0\n" \
  "s_mov_b32 s89, s60\n" \
  "s_bitcmp1_b32 s60, 7\n" \
  "s_cselect_b32 s88, 0x10000, 0\n" \
  "s_bitset1_b32 s88, 7\n" \
  "s_xor_b32 s56, s56, s88\n" \
  "s_xor_b32 s57, s57, s88\n" \
  "s_xor_b32 s58, s58, s88\n" \
  "s_xor_b32 s59, s59, s88\n" \
  "s_xor_b32 s60, s60, s88\n" \
  "s_sub_u32 s71, s71, 1\n" \
  "s_waitcnt lgkmcnt(0)\n" \
  "s_sub_u32 s39, s38, 150\n" \
  "v_subrev_u32 v52, s39, v52\n" \
  "v_cmp_lt_u32 vcc, 0x1000, v52\n" \
  "s_cmp_eq_u64 vcc, 0\n" \
  "s_cbranch_scc1 .LCA_OKM_%=\n" \
  ".LCA_AGN_%=:\n" \
  "buffer_load_dword v52, v5, s[0:3], s66 offen sc1\n" \
  "s_sub_u32 s87, s87, 1\n" \
  "s_cbranch_scc1 .LCA_END_%=\n" \
  "s_waitcnt vmcnt(0)\n" \
  "v_cmp_eq_u32 vcc, 0, v52\n" \
  "v_subrev_u32 v52, s39, v52\n" \
  "s_cmp_eq_u64 vcc, 0\n" \
  "s_cbranch_scc1 .LCA_OKM_%=\n" \
  "s_sleep 4\n" \
  "s_branch .LCA_AGN_%=\n" \
  ".LCA_OKM_%=:\n" \
  "s_nop 1\n" \
  "v_max_i32_dpp v52, v52, v52 row_ror:1 row_mask:0xf bank_mask:0xf\n" \
  "s_nop 1\n" \
  "v_max_i32_dpp v52, v52, v52 row_ror:2 row_mask:0xf bank_mask:0xf\n" \
  "s_nop 1\n" \
  "v_max_i32_dpp v52, v52, v52 row_ror:4 row_mask:0xf bank_mask:0xf\n" \
  "s_nop 1\n" \
  "v_max_i32_dpp v52, v52, v52 row_ror:8 row_mask:0xf bank_mask:0xf\n" \
  "s_movk_i32 vcc_lo, 150\n" \
  "v_readlane_b32 s88, v52, 0\n" \
  "s_max_i32 vcc_lo, vcc_lo, s88\n" \
  "v_readlane_b32 s88, v52, 16\n" \
  "s_max_i32 vcc_lo, vcc_lo, s88\n" \
  "v_readlane_b32 s88, v52, 32\n" \
  "s_max_i32 vcc_lo, vcc_lo, s88\n" \
  "v_readlane_b32 s88, v52, 48\n" \
  "s_max_i32 vcc_lo, vcc_lo, s88\n" \
  "s_add_u32 s66, s66, 1024\n" \
  "s_add_u32 vcc_lo, vcc_lo, s39\n" \
  "s_add_u32 vcc_lo, vcc_lo, 23\n" \
  ".LCA_TW_%=:\n" \
  "s_memrealtime s[38:39]\n" \
  "s_waitcnt lgkmcnt(0)\n" \
  "s_sub_u32 s38, s38, vcc_lo\n" \
  "s_cmp_lt_i32 s38, 0\n" \
  "s_cbranch_scc1 .LCA_TW_%=\n" \
  "buffer_load_dwordx4 v[24:27], v44, s[40:43], s89 offen sc1\n" \
  "buffer_load_dwordx4 v[28:31], v44, s[40:43], s89 offen offset:2048 sc1\n" \
  ".LCA_SEE_%=:\n" \
  "s_waitcnt vmcnt(1)\n" \
  "v_cvt_f32_i32 v24, v24\n" \
  "v_cvt_f32_i32 v25, v25\n" \
  "v_cvt_f32_i32 v26, v26\n" \
  "v_cvt_f32_i32 v27, v27\n" \
  "v_max_f32 v45, |v24|, |v26|\n" \
  "v_pk_mul_f32 v[24:25], v[24:25], s[80:81] op_sel_hi:[1,0]\n" \
  "v_pk_mul_f32 v[26:27], v[26:27], s[80:81] op_sel_hi:[1,0]\n" \
  "s_nop 0\n" \
  "v_cvt_pk_bf16_f32 v32, v24, v26\n" \
  "v_cvt_pk_bf16_f32 v33, v25, v27\n" \
  "ds_write_b32 v40, v32\n" \
  "ds_write_b32 v41, v33\n" \
  "s_waitcnt vmcnt(0)\n" \
  "v_cvt_f32_i32 v28, v28\n" \
  "v_cvt_f32_i32 v30, v30\n" \
  "v_cvt_f32_i32 v29, v29\n" \
  "v_max3_f32 v45, v45, |v28|, |v30|\n" \
  "v_cvt_f32_i32 v31, v31\n" \
  "v_cmp_lt_f32 vcc, s91, v45\n" \
  "v_pk_mul_f32 v[28:29], v[28:29], s[80:81] op_sel_hi:[1,0]\n" \
  "v_pk_mul_f32 v[30:31], v[30:31], s[80:81] op_sel_hi:[1,0]\n" \
  "s_cmp_eq_u64 vcc, 0\n" \
  "s_cbranch_scc0 .LCA_POLL_%=\n" \
  "v_cvt_pk_bf16_f32 v36, v28, v30\n" \
  "v_cvt_pk_bf16_f32 v37, v29, v31\n" \
  "ds_write_b32 v42, v36\n" \
  "ds_write_b32 v43, v37\n" \
  "v_dot2c_f32_bf16 v11, v32, v32\n" \
  "v_dot2c_f32_bf16 v11, v33, v33\n" \
  "v_dot2c_f32_bf16 v11, v36, v36\n" \
  "v_dot2c_f32_bf16 v11, v37, v37\n" \
  "s_nop 1\n" \
  "s_nop 0\n" \
  "v_cmp_lt_f32 vcc, 4.0, v11\n" \
  "s_cbranch_vccz .LCA_TOP_%=\n" \
  "s_mov_b64 exec, vcc\n" \
  "ds_max_u32 v59, v11\n" \
  "s_mov_b64 exec, -1\n" \
  "s_branch .LCA_TOP_%=\n" \
  ".LCA_POLL_%=:\n" \
  "buffer_load_dwordx4 v[24:27], v44, s[40:43], s89 offen sc1\n" \
  "buffer_load_dwordx4 v[28:31], v44, s[40:43], s89 offen offset:2048 sc1\n" \
  "s_sub_u32 s87, s87, 1\n" \
  "s_cbranch_scc1 .LCA_END_%=\n" \
  "s_branch .LCA_SEE_%=\n" \
  ".LCA_LAST_%=:\n" \
  ".LCA_END_%=:\n" \
  "s_endpgm\n"
#define CHAIN_ASM_VO \
  "s_mov_b32 s95, 272\n" \
  "s_mov_b32 s91, 0x4c7c2000\n" \
  "s_mov_b32 s85, 0xc9b26c9b\n" \
  "s_mov_b32 s92, 0xc9326c9b\n" \
  "s_mov_b32 s93, 0xc9326c9b\n" \
  "s_mov_b32 s78, 0x3e0293ee\n" \
  "s_mov_b32 s76, 0x33333333\n" \
  "s_mov_b32 s77, 0x33333333\n" \
  "s_lshl_b32 s98, s73, 10\n" \
  "s_add_u32 s98, s98, s74\n" \
  "s_add_u32 s98, s98, 131072\n" \
  "s_lshl_b32 s97, s73, 9\n" \
  "s_add_u32 s97, s97, s74\n" \
  "s_add_u32 s97, s97, 158724\n" \
  "s_lshl_b32 s99, s73, 11\n" \
  "s_add_u32 s99, s99, s74\n" \
  "s_add_u32 s99, s99, 133120\n" \
  "s_lshl_b32 s94, s73, 4\n" \
  "s_add_u32 s94, s94, s74\n" \
  "s_add_u32 s94, s94, 157760\n" \
  "v_mbcnt_lo_u32_b32 v48, -1, 0\n" \
  "v_mbcnt_hi_u32_b32 v48, -1, v48\n" \
  "s_add_u32 s88, s74, 157760\n" \
  "v_mov_b32 v59, s88\n" \
  "s_sub_u32 s88, s73, 4\n" \
  "v_lshrrev_b32 v49, 2, v48\n" \
  "v_lshrrev_b32 v50, 4, v48\n" \
  "v_bitop3_b32 v50, v49, v50, 3 bitop3:0x28\n" \
  "v_lshl_add_u32 v4, v50, 3, s97\n" \
  "v_and_b32 v50, 3, v48\n" \
  "v_lshl_add_u32 v4, v50, 5, v4\n" \
  "v_and_b32 v49, 1, v49\n" \
  "v_lshl_add_u32 v4, v49, 8, v4\n" \
  "v_and_b32 v49, 8, v48\n" \
  "v_lshl_add_u32 v4, v49, 4, v4\n" \
  "v_and_b32 v51, 15, v48\n" \
  "v_bfe_u32 v49, v48, 4, 1\n" \
  "v_lshrrev_b32 v50, 5, v48\n" \
  "s_lshl_b32 s89, s88, 10\n" \
  "s_add_u32 s89, s89, s74\n" \
  "s_add_u32 s89, s89, 158720\n" \
  "v_lshrrev_b32 v9, 3, v51\n" \
  "v_mul_u32_u24 v6, 41, v9\n" \
  "v_xor_b32 v6, v6, v51\n" \
  "v_lshl_add_u32 v6, v6, 3, s89\n" \
  "v_lshl_add_u32 v6, v49, 6, v6\n" \
  "v_lshl_add_u32 v6, v50, 9, v6\n" \
  "v_xor_b32 v9, 0x90, v6\n" \
  "v_lshlrev_b32 v8, 4, v49\n" \
  "v_lshl_add_u32 v8, v51, 5, v8\n" \
  "v_lshl_add_u32 v8, v50, 3, v8\n" \
  "s_lshl_b32 s89, s88, 8\n" \
  "s_add_u32 s89, s89, s74\n" \
  "s_add_u32 s89, s89, 139264\n" \
  "v_and_b32 v49, 31, v48\n" \
  "v_lshl_add_u32 v5, v49, 3, s89\n" \
  "v_lshl_add_u32 v5, v50, 10, v5\n" \
  "v_lshrrev_b32 v49, 2, v51\n" \
  "v_and_b32 v50, 5, v51\n" \
  "v_and_or_b32 v49, v49, 2, v50\n" \
  "v_and_b32 v50, 48, v48\n" \
  "v_lshlrev_b32 v7, 3, v50\n" \
  "v_lshl_add_u32 v7, v49, 4, v7\n" \
  "v_bitop3_b32 v49, v51, 2, 2 bitop3:0x0c\n" \
  "v_lshl_add_u32 v7, v49, 29, v7\n" \
  "v_mov_b64 v[12:13], 0\n" \
  "v_mov_b64 v[14:15], 0\n" \
  "v_mov_b64 v[16:17], 0\n" \
  "v_mov_b64 v[18:19], 0\n" \
  "v_mov_b64 v[20:21], 0\n" \
  "v_mov_b64 v[22:23], 0\n" \
  "v_lshlrev_b32 v10, 4, v48\n" \
  "s_add_u32 s78, s74, 157952\n" \
  "v_lshl_add_u32 v52, v48, 2, v59\n" \
  "ds_write_b32 v52, v12 offset:192\n" \
  "s_movk_i32 s79, 105\n" \
  "s_movk_i32 s96, 45\n" \
  "s_mov_b32 s87, 0x40000\n" \
  "v_subrev_u32 v30, 64, v59\n" \
  "v_add_u32 v17, 0x10000, v28\n" \
  "v_xor_b32 v18, 128, v28\n" \
  "v_xor_b32 v19, 128, v17\n" \
  "ds_write_b32 v30, v12 offset:0\n" \
  "ds_write_b32 v30, v12 offset:32\n" \
  "ds_write_b32 v30, v12 offset:64\n" \
  "v_xor_b32 v20, 0, v29\n" \
  "v_xor_b32 v21, 32, v29\n" \
  "v_xor_b32 v22, 64, v29\n" \
  "v_xor_b32 v23, 96, v29\n" \
  "s_waitcnt vmcnt(12) lgkmcnt(0)\n" \
  "s_barrier\n" \
  "ds_read_b64_tr_b16 v[96:97], v20 offset:0\n" \
  "ds_read_b64_tr_b16 v[98:99], v20 offset:2048\n" \
  "ds_read_b64_tr_b16 v[104:105], v21 offset:0\n" \
  "ds_read_b64_tr_b16 v[106:107], v21 offset:2048\n" \
  "ds_read_b64_tr_b16 v[112:113], v22 offset:0\n" \
  "ds_read_b64_tr_b16 v[114:115], v22 offset:2048\n" \
  "ds_read_b64_tr_b16 v[120:121], v23 offset:0\n" \
  "ds_read_b64_tr_b16 v[122:123], v23 offset:2048\n" \
  "s_lshl_b32 s88, s73, 10\n" \
  "v_add_u32 v48, s88, v1\n" \
  "ds_read_b128 v[52:55], v48 offset:0\n" \
  "s_waitcnt lgkmcnt(0)\n" \
  "v_mov_b32 v11, 0\n" \
  "v_dot2c_f32_bf16 v11, v52, v52\n" \
  "v_dot2c_f32_bf16 v11, v53, v53\n" \
  "v_dot2c_f32_bf16 v11, v54, v54\n" \
  "v_dot2c_f32_bf16 v11, v55, v55\n" \
  "s_nop 0\n" \
  "s_nop 1\n" \
  "v_max_u32_dpp v11, v11, v11 row_ror:1 row_mask:0xf bank_mask:0xf\n" \
  "s_nop 1\n" \
  "v_max_u32_dpp v11, v11, v11 row_ror:2 row_mask:0xf bank_mask:0xf\n" \
  "s_nop 1\n" \
  "v_max_u32_dpp v11, v11, v11 row_ror:4 row_mask:0xf bank_mask:0xf\n" \
  "s_nop 1\n" \
  "v_max_u32_dpp v11, v11, v11 row_ror:8 row_mask:0xf bank_mask:0xf\n" \
  "v_mov_b32 v31, v11\n" \
  "s_nop 1\n" \
  "v_permlane16_swap_b32 v11, v31\n" \
  "v_max_u32 v11, v11, v31\n" \
  "v_mov_b32 v31, v11\n" \
  "s_nop 1\n" \
  "v_permlane32_swap_b32 v11, v31\n" \
  "v_max_u32 v11, v11, v31\n" \
  "s_mov_b64 exec, 1\n" \
  "ds_max_u32 v59, v11\n" \
  "s_mov_b64 exec, -1\n" \
  "s_waitcnt vmcnt(8) lgkmcnt(0)\n" \
  "s_barrier\n" \
  "ds_read_b64_tr_b16 v[100:101], v20 offset:4096\n" \
  "ds_read_b64_tr_b16 v[102:103], v20 offset:6144\n" \
  "ds_read_b64_tr_b16 v[108:109], v21 offset:4096\n" \
  "ds_read_b64_tr_b16 v[110:111], v21 offset:6144\n" \
  "ds_read_b64_tr_b16 v[116:117], v22 offset:4096\n" \
  "ds_read_b64_tr_b16 v[118:119], v22 offset:6144\n" \
  "ds_read_b64_tr_b16 v[124:125], v23 offset:4096\n" \
  "ds_read_b64_tr_b16 v[126:127], v23 offset:6144\n" \
  "s_add_u32 m0, s19, 0\n" \
  "s_nop 0\n" \
  "global_load_lds_dwordx4 v24, s[4:5] offset:0\n" \
  "global_load_lds_dwordx4 v25, s[4:5] offset:1024\n" \
  "global_load_lds_dwordx4 v26, s[4:5] offset:2048\n" \
  "global_load_lds_dwordx4 v27, s[4:5] offset:3072\n" \
  "s_add_u32 s4, s4, 0x100000\n" \
  "s_addc_u32 s5, s5, 0\n" \
  "v_mfma_f32_16x16x32_bf16 v[64:67], v[96:99], v[96:99], 0\n" \
  "v_mfma_f32_16x16x32_bf16 v[68:71], v[104:107], v[104:107], 0\n" \
  "v_mfma_f32_16x16x32_bf16 v[72:75], v[112:115], v[112:115], 0\n" \
  "v_mfma_f32_16x16x32_bf16 v[76:79], v[120:123], v[120:123], 0\n" \
  "s_waitcnt vmcnt(8) lgkmcnt(0)\n" \
  "s_barrier\n" \
  "ds_read_b64_tr_b16 v[32:33], v20 offset:8192\n" \
  "ds_read_b64_tr_b16 v[34:35], v20 offset:10240\n" \
  "ds_read_b64_tr_b16 v[40:41], v21 offset:8192\n" \
  "ds_read_b64_tr_b16 v[42:43], v21 offset:10240\n" \
  "ds_read_b64_tr_b16 v[48:49], v22 offset:8192\n" \
  "ds_read_b64_tr_b16 v[50:51], v22 offset:10240\n" \
  "ds_read_b64_tr_b16 v[56:57], v23 offset:8192\n" \
  "ds_read_b64_tr_b16 v[58:59], v23 offset:10240\n" \
  "s_add_u32 m0, s19, 4096\n" \
  "s_nop 0\n" \
  "global_load_lds_dwordx4 v24, s[4:5] offset:0\n" \
  "global_load_lds_dwordx4 v25, s[4:5] offset:1024\n" \
  "global_load_lds_dwordx4 v26, s[4:5] offset:2048\n" \
  "global_load_lds_dwordx4 v27, s[4:5] offset:3072\n" \
  "s_add_u32 s4, s4, 0x100000\n" \
  "s_addc_u32 s5, s5, 0\n" \
  "v_mfma_f32_16x16x32_bf16 v[64:67], v[100:103], v[100:103], v[64:67]\n" \
  "v_mfma_f32_16x16x32_bf16 v[68:71], v[108:111], v[108:111], v[68:71]\n" \
  "v_mfma_f32_16x16x32_bf16 v[72:75], v[116:119], v[116:119], v[72:75]\n" \
  "v_mfma_f32_16x16x32_bf16 v[76:79], v[124:127], v[124:127], v[76:79]\n" \
  "s_waitcnt vmcnt(8) lgkmcnt(0)\n" \
  "s_barrier\n" \
  "ds_read_b64_tr_b16 v[36:37], v20 offset:12288\n" \
  "ds_read_b64_tr_b16 v[38:39], v20 offset:14336\n" \
  "ds_read_b64_tr_b16 v[44:45], v21 offset:12288\n" \
  "ds_read_b64_tr_b16 v[46:47], v21 offset:14336\n" \
  "ds_read_b64_tr_b16 v[52:53], v22 offset:12288\n" \
  "ds_read_b64_tr_b16 v[54:55], v22 offset:14336\n" \
  "ds_read_b64_tr_b16 v[60:61], v23 offset:12288\n" \
  "ds_read_b64_tr_b16 v[62:63], v23 offset:14336\n" \
  "s_add_u32 m0, s19, 8192\n" \
  "s_nop 0\n" \
  "global_load_lds_dwordx4 v24, s[4:5] offset:0\n" \
  "global_load_lds_dwordx4 v25, s[4:5] offset:1024\n" \
  "global_load_lds_dwordx4 v26, s[4:5] offset:2048\n" \
  "global_load_lds_dwordx4 v27, s[4:5] offset:3072\n" \
  "s_add_u32 s4, s4, 0x100000\n" \
  "s_addc_u32 s5, s5, 0\n" \
  "v_mfma_f32_16x16x32_bf16 v[64:67], v[32:35], v[32:35], v[64:67]\n" \
  "v_mfma_f32_16x16x32_bf16 v[68:71], v[40:43], v[40:43], v[68:71]\n" \
  "v_mfma_f32_16x16x32_bf16 v[72:75], v[48:51], v[48:51], v[72:75]\n" \
  "v_mfma_f32_16x16x32_bf16 v[76:79], v[56:59], v[56:59], v[76:79]\n" \
  "s_waitcnt vmcnt(8) lgkmcnt(0)\n" \
  "s_barrier\n" \
  "ds_read_b64_tr_b16 v[128:129], v18 offset:0\n" \
  "ds_read_b64_tr_b16 v[130:131], v18 offset:512\n" \
  "ds_read_b64_tr_b16 v[132:133], v18 offset:32768\n" \
  "ds_read_b64_tr_b16 v[134:135], v18 offset:33280\n" \
  "ds_read_b64_tr_b16 v[136:137], v19 offset:0\n" \
  "ds_read_b64_tr_b16 v[138:139], v19 offset:512\n" \
  "ds_read_b64_tr_b16 v[140:141], v19 offset:32768\n" \
  "ds_read_b64_tr_b16 v[142:143], v19 offset:33280\n" \
  "ds_read_b64_tr_b16 v[192:193], v28 offset:0\n" \
  "ds_read_b64_tr_b16 v[194:195], v28 offset:512\n" \
  "ds_read_b64_tr_b16 v[196:197], v28 offset:32768\n" \
  "ds_read_b64_tr_b16 v[198:199], v28 offset:33280\n" \
  "ds_read_b64_tr_b16 v[200:201], v17 offset:0\n" \
  "ds_read_b64_tr_b16 v[202:203], v17 offset:512\n" \
  "ds_read_b64_tr_b16 v[204:205], v17 offset:32768\n" \
  "ds_read_b64_tr_b16 v[206:207], v17 offset:33280\n" \
  "s_add_u32 m0, s19, 12288\n" \
  "s_nop 0\n" \
  "global_load_lds_dwordx4 v24, s[4:5] offset:0\n" \
  "global_load_lds_dwordx4 v25, s[4:5] offset:1024\n" \
  "global_load_lds_dwordx4 v26, s[4:5] offset:2048\n" \
  "global_load_lds_dwordx4 v27, s[4:5] offset:3072\n" \
  "s_add_u32 s4, s4, 0x100000\n" \
  "s_addc_u32 s5, s5, 0\n" \
  "v_mfma_f32_16x16x32_bf16 v[64:67], v[36:39], v[36:39], v[64:67]\n" \
  "v_mfma_f32_16x16x32_bf16 v[68:71], v[44:47], v[44:47], v[68:71]\n" \
  "v_mfma_f32_16x16x32_bf16 v[72:75], v[52:55], v[52:55], v[72:75]\n" \
  "v_mfma_f32_16x16x32_bf16 v[76:79], v[60:63], v[60:63], v[76:79]\n" \
  "s_nop 7\n" \
  "v_max3_f32 v13, v64, v65, v66\n" \
  "v_max3_f32 v13, v13, v67, v68\n" \
  "v_max3_f32 v13, v13, v69, v70\n" \
  "v_max3_f32 v13, v13, v71, v72\n" \
  "v_max3_f32 v13, v13, v73, v74\n" \
  "v_max3_f32 v13, v13, v75, v76\n" \
  "v_max3_f32 v13, v13, v77, v78\n" \
  "v_max_f32 v13, v13, v79\n" \
  "s_nop 1\n" \
  "v_max_f32_dpp v13, v13, v13 row_ror:1 row_mask:0xf bank_mask:0xf\n" \
  "s_nop 1\n" \
  "v_max_f32_dpp v13, v13, v13 row_ror:2 row_mask:0xf bank_mask:0xf\n" \
  "s_nop 1\n" \
  "v_max_f32_dpp v13, v13, v13 row_ror:4 row_mask:0xf bank_mask:0xf\n" \
  "s_nop 1\n" \
  "v_max_f32_dpp v13, v13, v13 row_ror:8 row_mask:0xf bank_mask:0xf\n" \
  "v_mov_b32 v31, v13\n" \
  "s_nop 1\n" \
  "v_permlane16_swap_b32 v13, v31\n" \
  "v_max_f32 v13, v13, v31\n" \
  "v_mov_b32 v31, v13\n" \
  "s_nop 1\n" \
  "v_permlane32_swap_b32 v13, v31\n" \
  "v_max_f32 v13, v13, v31\n" \
  "s_mov_b64 exec, 1\n" \
  "ds_max_u32 v30, v13 offset:32\n" \
  "s_mov_b64 exec, -1\n" \
  "s_waitcnt vmcnt(8) lgkmcnt(0)\n" \
  "s_barrier\n" \
  "ds_read_b64_tr_b16 v[144:145], v18 offset:4096\n" \
  "ds_read_b64_tr_b16 v[146:147], v18 offset:4608\n" \
  "ds_read_b64_tr_b16 v[148:149], v18 offset:36864\n" \
  "ds_read_b64_tr_b16 v[150:151], v18 offset:37376\n" \
  "ds_read_b64_tr_b16 v[152:153], v19 offset:4096\n" \
  "ds_read_b64_tr_b16 v[154:155], v19 offset:4608\n" \
  "ds_read_b64_tr_b16 v[156:157], v19 offset:36864\n" \
  "ds_read_b64_tr_b16 v[158:159], v19 offset:37376\n" \
  "ds_read_b64_tr_b16 v[208:209], v28 offset:4096\n" \
  "ds_read_b64_tr_b16 v[210:211], v28 offset:4608\n" \
  "ds_read_b64_tr_b16 v[212:213], v28 offset:36864\n" \
  "ds_read_b64_tr_b16 v[214:215], v28 offset:37376\n" \
  "ds_read_b64_tr_b16 v[216:217], v17 offset:4096\n" \
  "ds_read_b64_tr_b16 v[218:219], v17 offset:4608\n" \
  "ds_read_b64_tr_b16 v[220:221], v17 offset:36864\n" \
  "ds_read_b64_tr_b16 v[222:223], v17 offset:37376\n" \
  "s_add_u32 m0, s19, 0\n" \
  "s_nop 0\n" \
  "global_load_lds_dwordx4 v24, s[6:7] offset:0\n" \
  "global_load_lds_dwordx4 v25, s[6:7] offset:1024\n" \
  "global_load_lds_dwordx4 v26, s[6:7] offset:2048\n" \
  "global_load_lds_dwordx4 v27, s[6:7] offset:3072\n" \
  "s_add_u32 s6, s6, 0x100000\n" \
  "s_addc_u32 s7, s7, 0\n" \
  "v_mfma_f32_16x16x32_bf16 v[64:67], v[128:131], v[128:131], 0\n" \
  "v_mfma_f32_16x16x32_bf16 v[68:71], v[192:195], v[192:195], 0\n" \
  "v_mfma_f32_16x16x32_bf16 v[64:67], v[132:135], v[132:135], v[64:67]\n" \
  "v_mfma_f32_16x16x32_bf16 v[68:71], v[196:199], v[196:199], v[68:71]\n" \
  "v_mfma_f32_16x16x32_bf16 v[64:67], v[136:139], v[136:139], v[64:67]\n" \
  "v_mfma_f32_16x16x32_bf16 v[68:71], v[200:203], v[200:203], v[68:71]\n" \
  "v_mfma_f32_16x16x32_bf16 v[64:67], v[140:143], v[140:143], v[64:67]\n" \
  "v_mfma_f32_16x16x32_bf16 v[68:71], v[204:207], v[204:207], v[68:71]\n" \
  "s_waitcnt vmcnt(8) lgkmcnt(0)\n" \
  "s_barrier\n" \
  "ds_read_b64_tr_b16 v[160:161], v18 offset:8192\n" \
  "ds_read_b64_tr_b16 v[162:163], v18 offset:8704\n" \
  "ds_read_b64_tr_b16 v[164:165], v18 offset:40960\n" \
  "ds_read_b64_tr_b16 v[166:167], v18 offset:41472\n" \
  "ds_read_b64_tr_b16 v[168:169], v19 offset:8192\n" \
  "ds_read_b64_tr_b16 v[170:171], v19 offset:8704\n" \
  "ds_read_b64_tr_b16 v[172:173], v19 offset:40960\n" \
  "ds_read_b64_tr_b16 v[174:175], v19 offset:41472\n" \
  "ds_read_b64_tr_b16 v[224:225], v28 offset:8192\n" \
  "ds_read_b64_tr_b16 v[226:227], v28 offset:8704\n" \
  "ds_read_b64_tr_b16 v[228:229], v28 offset:40960\n" \
  "ds_read_b64_tr_b16 v[230:231], v28 offset:41472\n" \
  "ds_read_b64_tr_b16 v[232:233], v17 offset:8192\n" \
  "ds_read_b64_tr_b16 v[234:235], v17 offset:8704\n" \
  "ds_read_b64_tr_b16 v[236:237], v17 offset:40960\n" \
  "ds_read_b64_tr_b16 v[238:239], v17 offset:41472\n" \
  "s_add_u32 m0, s19, 4096\n" \
  "s_nop 0\n" \
  "global_load_lds_dwordx4 v24, s[6:7] offset:0\n" \
  "global_load_lds_dwordx4 v25, s[6:7] offset:1024\n" \
  "global_load_lds_dwordx4 v26, s[6:7] offset:2048\n" \
  "global_load_lds_dwordx4 v27, s[6:7] offset:3072\n" \
  "s_add_u32 s6, s6, 0x100000\n" \
  "s_addc_u32 s7, s7, 0\n" \
  "v_mfma_f32_16x16x32_bf16 v[64:67], v[144:147], v[144:147], v[64:67]\n" \
  "v_mfma_f32_16x16x32_bf16 v[68:71], v[208:211], v[208:211], v[68:71]\n" \
  "v_mfma_f32_16x16x32_bf16 v[64:67], v[148:151], v[148:151], v[64:67]\n" \
  "v_mfma_f32_16x16x32_bf16 v[68:71], v[212:215], v[212:215], v[68:71]\n" \
  "v_mfma_f32_16x16x32_bf16 v[64:67], v[152:155], v[152:155], v[64:67]\n" \
  "v_mfma_f32_16x16x32_bf16 v[68:71], v[216:219], v[216:219], v[68:71]\n" \
  "v_mfma_f32_16x16x32_bf16 v[64:67], v[156:159], v[156:159], v[64:67]\n" \
  "v_mfma_f32_16x16x32_bf16 v[68:71], v[220:223], v[220:223], v[68:71]\n" \
  "s_waitcnt vmcnt(8) lgkmcnt(0)\n" \
  "s_barrier\n" \
  "ds_read_b64_tr_b16 v[176:177], v18 offset:12288\n" \
  "ds_read_b64_tr_b16 v[178:179], v18 offset:12800\n" \
  "ds_read_b64_tr_b16 v[180:181], v18 offset:45056\n" \
  "ds_read_b64_tr_b16 v[182:183], v18 offset:45568\n" \
  "ds_read_b64_tr_b16 v[184:185], v19 offset:12288\n" \
  "ds_read_b64_tr_b16 v[186:187], v19 offset:12800\n" \
  "ds_read_b64_tr_b16 v[188:189], v19 offset:45056\n" \
  "ds_read_b64_tr_b16 v[190:191], v19 offset:45568\n" \
  "ds_read_b64_tr_b16 v[240:241], v28 offset:12288\n" \
  "ds_read_b64_tr_b16 v[242:243], v28 offset:12800\n" \
  "ds_read_b64_tr_b16 v[244:245], v28 offset:45056\n" \
  "ds_read_b64_tr_b16 v[246:247], v28 offset:45568\n" \
  "ds_read_b64_tr_b16 v[248:249], v17 offset:12288\n" \
  "ds_read_b64_tr_b16 v[250:251], v17 offset:12800\n" \
  "ds_read_b64_tr_b16 v[252:253], v17 offset:45056\n" \
  "ds_read_b64_tr_b16 v[254:255], v17 offset:45568\n" \
  "s_add_u32 m0, s19, 8192\n" \
  "s_nop 0\n" \
  "global_load_lds_dwordx4 v24, s[6:7] offset:0\n" \
  "global_load_lds_dwordx4 v25, s[6:7] offset:1024\n" \
  "global_load_lds_dwordx4 v26, s[6:7] offset:2048\n" \
  "global_load_lds_dwordx4 v27, s[6:7] offset:3072\n" \
  "s_add_u32 s6, s6, 0x100000\n" \
  "s_addc_u32 s7, s7, 0\n" \
  "v_mfma_f32_16x16x32_bf16 v[64:67], v[160:163], v[160:163], v[64:67]\n" \
  "v_mfma_f32_16x16x32_bf16 v[68:71], v[224:227], v[224:227], v[68:71]\n" \
  "v_mfma_f32_16x16x32_bf16 v[64:67], v[164:167], v[164:167], v[64:67]\n" \
  "v_mfma_f32_16x16x32_bf16 v[68:71], v[228:231], v[228:231], v[68:71]\n" \
  "v_mfma_f32_16x16x32_bf16 v[64:67], v[168:171], v[168:171], v[64:67]\n" \
  "v_mfma_f32_16x16x32_bf16 v[68:71], v[232:235], v[232:235], v[68:71]\n" \
  "v_mfma_f32_16x16x32_bf16 v[64:67], v[172:175], v[172:175], v[64:67]\n" \
  "v_mfma_f32_16x16x32_bf16 v[68:71], v[236:239], v[236:239], v[68:71]\n" \
  "s_waitcnt vmcnt(8) lgkmcnt(0)\n" \
  "s_barrier\n" \
  "s_add_u32 m0, s19, 12288\n" \
  "s_nop 0\n" \
  "global_load_lds_dwordx4 v24, s[6:7] offset:0\n" \
  "global_load_lds_dwordx4 v25, s[6:7] offset:1024\n" \
  "global_load_lds_dwordx4 v26, s[6:7] offset:2048\n" \
  "global_load_lds_dwordx4 v27, s[6:7] offset:3072\n" \
  "s_add_u32 s6, s6, 0x100000\n" \
  "s_addc_u32 s7, s7, 0\n" \
  "v_mfma_f32_16x16x32_bf16 v[64:67], v[176:179], v[176:179], v[64:67]\n" \
  "v_mfma_f32_16x16x32_bf16 v[68:71], v[240:243], v[240:243], v[68:71]\n" \
  "v_mfma_f32_16x16x32_bf16 v[64:67], v[180:183], v[180:183], v[64:67]\n" \
  "v_mfma_f32_16x16x32_bf16 v[68:71], v[244:247], v[244:247], v[68:71]\n" \
  "v_mfma_f32_16x16x32_bf16 v[64:67], v[184:187], v[184:187], v[64:67]\n" \
  "v_mfma_f32_16x16x32_bf16 v[68:71], v[248:251], v[248:251], v[68:71]\n" \
  "v_mfma_f32_16x16x32_bf16 v[64:67], v[188:191], v[188:191], v[64:67]\n" \
  "v_mfma_f32_16x16x32_bf16 v[68:71], v[252:255], v[252:255], v[68:71]\n" \
  "s_nop 7\n" \
  "s_mov_b32 exec_lo, 0x100001\n" \
  "s_mov_b32 exec_hi, 0x10000100\n" \
  "v_add_f32 v12, v64, v68\n" \
  "s_mov_b32 exec_lo, 0x200002\n" \
  "s_mov_b32 exec_hi, 0x20000200\n" \
  "v_add_f32 v12, v65, v69\n" \
  "s_mov_b32 exec_lo, 0x400004\n" \
  "s_mov_b32 exec_hi, 0x40000400\n" \
  "v_add_f32 v12, v66, v70\n" \
  "s_mov_b32 exec_lo, 0x800008\n" \
  "s_mov_b32 exec_hi, 0x80000800\n" \
  "v_add_f32 v12, v67, v71\n" \
  "s_mov_b64 exec, -1\n" \
  "s_nop 1\n" \
  "v_add_f32_dpp v12, v12, v12 row_ror:1 row_mask:0xf bank_mask:0xf\n" \
  "s_nop 1\n" \
  "v_add_f32_dpp v12, v12, v12 row_ror:2 row_mask:0xf bank_mask:0xf\n" \
  "s_nop 1\n" \
  "v_add_f32_dpp v12, v12, v12 row_ror:4 row_mask:0xf bank_mask:0xf\n" \
  "s_nop 1\n" \
  "v_add_f32_dpp v12, v12, v12 row_ror:8 row_mask:0xf bank_mask:0xf\n" \
  "v_mov_b32 v31, v12\n" \
  "s_nop 1\n" \
  "v_permlane16_swap_b32 v12, v31\n" \
  "v_add_f32 v12, v12, v31\n" \
  "v_mov_b32 v31, v12\n" \
  "s_nop 1\n" \
  "v_permlane32_swap_b32 v12, v31\n" \
  "v_add_f32 v12, v12, v31\n" \
  "v_lshl_add_u32 v31, s73, 2, v30\n" \
  "s_mov_b64 exec, 1\n" \
  "ds_write_b32 v31, v12\n" \
  "s_mov_b64 exec, -1\n" \
  "s_waitcnt vmcnt(8) lgkmcnt(0)\n" \
  "s_barrier\n" \
  "s_add_u32 m0, s19, 0\n" \
  "s_nop 0\n" \
  "global_load_lds_dwordx4 v24, s[8:9] offset:0\n" \
  "global_load_lds_dwordx4 v25, s[8:9] offset:1024\n" \
  "global_load_lds_dwordx4 v26, s[8:9] offset:2048\n" \
  "global_load_lds_dwordx4 v27, s[8:9] offset:3072\n" \
  "s_add_u32 s8, s8, 0x100000\n" \
  "s_addc_u32 s9, s9, 0\n" \
  "ds_read_b128 v[20:23], v30 offset:16\n" \
  "ds_read_b32 v31, v30 offset:32\n" \
  "v_mbcnt_lo_u32_b32 v16, -1, 0\n" \
  "v_mbcnt_hi_u32_b32 v16, -1, v16\n" \
  "v_lshlrev_b32 v15, 8, v16\n" \
  "s_waitcnt lgkmcnt(0)\n" \
  "v_add_f32 v20, v20, v21\n" \
  "v_add_f32 v22, v22, v23\n" \
  "v_add_f32 v20, v20, v22\n" \
  "v_mul_f32 v31, v20, v31\n" \
  "v_mul_f32 v31, 0x3f8147ae, v31\n" \
  "v_mov_b32 v12, s10\n" \
  "v_mov_b32 v13, 0xffff\n" \
  "v_bfi_b32 v13, v13, s11, v31\n" \
  "s_mov_b32 exec_hi, 0\n" \
  "s_mov_b32 exec_lo, s18\n" \
  "global_store_dwordx2 v15, v[12:13], s[16:17] sc1\n" \
  "s_mov_b64 exec, -1\n" \
  "v_lshlrev_b32 v16, 3, v16\n" \
  "s_waitcnt vmcnt(8) lgkmcnt(0)\n" \
  "s_barrier\n" \
  "s_add_u32 m0, s19, 4096\n" \
  "s_nop 0\n" \
  "global_load_lds_dwordx4 v24, s[8:9] offset:0\n" \
  "global_load_lds_dwordx4 v25, s[8:9] offset:1024\n" \
  "global_load_lds_dwordx4 v26, s[8:9] offset:2048\n" \
  "global_load_lds_dwordx4 v27, s[8:9] offset:3072\n" \
  "s_add_u32 s8, s8, 0x100000\n" \
  "s_addc_u32 s9, s9, 0\n" \
  "global_load_dwordx2 v[20:21], v16, s[12:13] sc1\n" \
  "s_waitcnt vmcnt(9) lgkmcnt(0)\n" \
  "s_barrier\n" \
  "s_add_u32 m0, s19, 8192\n" \
  "s_nop 0\n" \
  "global_load_lds_dwordx4 v24, s[8:9] offset:0\n" \
  "global_load_lds_dwordx4 v25, s[8:9] offset:1024\n" \
  "global_load_lds_dwordx4 v26, s[8:9] offset:2048\n" \
  "global_load_lds_dwordx4 v27, s[8:9] offset:3072\n" \
  "s_add_u32 s8, s8, 0x100000\n" \
  "s_addc_u32 s9, s9, 0\n" \
  "s_waitcnt vmcnt(9) lgkmcnt(0)\n" \
  "s_barrier\n" \
  "ds_read_b64_tr_b16 v[64:65], v28 offset:0\n" \
  "ds_read_b64_tr_b16 v[66:67], v28 offset:512\n" \
  "ds_read_b64_tr_b16 v[68:69], v28 offset:32768\n" \
  "ds_read_b64_tr_b16 v[70:71], v28 offset:33280\n" \
  "ds_read_b64_tr_b16 v[72:73], v17 offset:0\n" \
  "ds_read_b64_tr_b16 v[74:75], v17 offset:512\n" \
  "ds_read_b64_tr_b16 v[76:77], v17 offset:32768\n" \
  "ds_read_b64_tr_b16 v[78:79], v17 offset:33280\n" \
  "s_add_u32 m0, s19, 12288\n" \
  "s_nop 0\n" \
  "global_load_lds_dwordx4 v24, s[8:9] offset:0\n" \
  "global_load_lds_dwordx4 v25, s[8:9] offset:1024\n" \
  "global_load_lds_dwordx4 v26, s[8:9] offset:2048\n" \
  "global_load_lds_dwordx4 v27, s[8:9] offset:3072\n" \
  "s_add_u32 s8, s8, 0x100000\n" \
  "s_addc_u32 s9, s9, 0\n" \
  "s_waitcnt vmcnt(8)\n" \
  ".LCV_RD0_%=:\n" \
  "v_cmp_ne_u32 vcc, s10, v20\n" \
  "v_xor_b32 v31, s11, v21\n" \
  "v_cmp_ne_u32 s[22:23], 0, v31\n" \
  "s_or_b64 vcc, vcc, s[22:23]\n" \
  "s_cbranch_scc0 .LCV_RD1_%=\n" \
  "s_sleep 8\n" \
  "global_load_dwordx2 v[20:21], v16, s[12:13] sc1\n" \
  "s_sub_u32 s87, s87, 1\n" \
  "s_cbranch_scc1 .LCV_END_%=\n" \
  "s_waitcnt vmcnt(0)\n" \
  "s_branch .LCV_RD0_%=\n" \
  ".LCV_RD1_%=:\n" \
  "s_waitcnt vmcnt(8) lgkmcnt(0)\n" \
  "s_barrier\n" \
  "ds_write_b128 v3, v[96:99] offset:0\n" \
  "ds_write_b128 v3, v[100:103] offset:1024\n" \
  "ds_write_b128 v3, v[104:107] offset:2048\n" \
  "ds_write_b128 v3, v[108:111] offset:3072\n" \
  "ds_read_b64_tr_b16 v[80:81], v28 offset:4096\n" \
  "ds_read_b64_tr_b16 v[82:83], v28 offset:4608\n" \
  "ds_read_b64_tr_b16 v[84:85], v28 offset:36864\n" \
  "ds_read_b64_tr_b16 v[86:87], v28 offset:37376\n" \
  "ds_read_b64_tr_b16 v[88:89], v17 offset:4096\n" \
  "ds_read_b64_tr_b16 v[90:91], v17 offset:4608\n" \
  "ds_read_b64_tr_b16 v[92:93], v17 offset:36864\n" \
  "ds_read_b64_tr_b16 v[94:95], v17 offset:37376\n" \
  "v_and_b32 v16, 0xff, v16\n" \
  "global_load_dwordx2 v[22:23], v16, s[14:15] sc1\n" \
  "s_mov_b64 exec, 15\n" \
  "buffer_load_dword v13, v2, s[44:47], s68 offen sc1\n" \
  "s_mov_b64 exec, -1\n" \
  "s_waitcnt vmcnt(6) lgkmcnt(0)\n" \
  "s_barrier\n" \
  "ds_write_b128 v3, v[112:115] offset:4096\n" \
  "ds_write_b128 v3, v[116:119] offset:5120\n" \
  "ds_write_b128 v3, v[120:123] offset:6144\n" \
  "ds_write_b128 v3, v[124:127] offset:7168\n" \
  "ds_read_b64_tr_b16 v[96:97], v28 offset:8192\n" \
  "ds_read_b64_tr_b16 v[98:99], v28 offset:8704\n" \
  "ds_read_b64_tr_b16 v[100:101], v28 offset:40960\n" \
  "ds_read_b64_tr_b16 v[102:103], v28 offset:41472\n" \
  "ds_read_b64_tr_b16 v[104:105], v17 offset:8192\n" \
  "ds_read_b64_tr_b16 v[106:107], v17 offset:8704\n" \
  "ds_read_b64_tr_b16 v[108:109], v17 offset:40960\n" \
  "ds_read_b64_tr_b16 v[110:111], v17 offset:41472\n" \
  "s_waitcnt vmcnt(0)\n" \
  "v_cmp_eq_u32 vcc, 0, v13\n" \
  "s_and_b32 s90, vcc_lo, 15\n" \
  "s_cbranch_scc1 .LCV_GL0_%=\n" \
  "s_mov_b32 m0, s99\n" \
  "s_add_u32 s88, s99, 8192\n" \
  "global_load_lds_dwordx4 v10, s[62:63] sc1\n" \
  "global_load_lds_dwordx4 v10, s[62:63] offset:1024 sc1\n" \
  "s_mov_b32 m0, s88\n" \
  "s_nop 0\n" \
  "global_load_lds_dwordx4 v10, s[64:65] sc1\n" \
  "global_load_lds_dwordx4 v10, s[64:65] offset:1024 sc1\n" \
  ".LCV_GL0_%=:\n" \
  "v_cmp_ne_u32 vcc, s10, v22\n" \
  "v_xor_b32 v31, s11, v23\n" \
  "v_and_b32 v31, 0xffff, v31\n" \
  "v_cmp_ne_u32 s[22:23], 0, v31\n" \
  "s_or_b64 vcc, vcc, s[22:23]\n" \
  "s_cbranch_scc0 .LCV_GL1_%=\n" \
  "s_sleep 12\n" \
  "global_load_dwordx2 v[22:23], v16, s[14:15] sc1\n" \
  "s_sub_u32 s87, s87, 1\n" \
  "s_cbranch_scc1 .LCV_END_%=\n" \
  "s_waitcnt vmcnt(0)\n" \
  "s_branch .LCV_GL0_%=\n" \
  ".LCV_GL1_%=:\n" \
  "v_and_b32 v31, 0xffff0000, v23\n" \
  "s_nop 1\n" \
  "v_max_u32_dpp v31, v31, v31 row_ror:1 row_mask:0xf bank_mask:0xf\n" \
  "s_nop 1\n" \
  "v_max_u32_dpp v31, v31, v31 row_ror:2 row_mask:0xf bank_mask:0xf\n" \
  "s_nop 1\n" \
  "v_max_u32_dpp v31, v31, v31 row_ror:4 row_mask:0xf bank_mask:0xf\n" \
  "s_nop 1\n" \
  "v_max_u32_dpp v31, v31, v31 row_ror:8 row_mask:0xf bank_mask:0xf\n" \
  "v_mov_b32 v12, v31\n" \
  "s_nop 1\n" \
  "v_permlane16_swap_b32 v31, v12\n" \
  "v_max_u32 v31, v31, v12\n" \
  "v_mov_b32 v12, v31\n" \
  "s_nop 1\n" \
  "v_permlane32_swap_b32 v31, v12\n" \
  "v_max_u32 v31, v31, v12\n" \
  "s_mov_b64 exec, 1\n" \
  "ds_write_b32 v30, v31\n" \
  "s_mov_b64 exec, -1\n" \
  "s_waitcnt vmcnt(4) lgkmcnt(0)\n" \
  "s_barrier\n" \
  "ds_write_b128 v3, v[32:35] offset:8192\n" \
  "ds_write_b128 v3, v[36:39] offset:9216\n" \
  "ds_write_b128 v3, v[40:43] offset:10240\n" \
  "ds_write_b128 v3, v[44:47] offset:11264\n" \
  "ds_read_b64_tr_b16 v[112:113], v28 offset:12288\n" \
  "ds_read_b64_tr_b16 v[114:115], v28 offset:12800\n" \
  "ds_read_b64_tr_b16 v[116:117], v28 offset:45056\n" \
  "ds_read_b64_tr_b16 v[118:119], v28 offset:45568\n" \
  "ds_read_b64_tr_b16 v[120:121], v17 offset:12288\n" \
  "ds_read_b64_tr_b16 v[122:123], v17 offset:12800\n" \
  "ds_read_b64_tr_b16 v[124:125], v17 offset:45056\n" \
  "ds_read_b64_tr_b16 v[126:127], v17 offset:45568\n" \
  "ds_read_b32 v31, v30 offset:0\n" \
  "s_waitcnt lgkmcnt(0)\n" \
  "s_barrier\n" \
  "ds_write_b128 v3, v[48:51] offset:12288\n" \
  "ds_write_b128 v3, v[52:55] offset:13312\n" \
  "ds_write_b128 v3, v[56:59] offset:14336\n" \
  "ds_write_b128 v3, v[60:63] offset:15360\n" \
  "v_mul_f32 v31, 0x3f8e353f, v31\n" \
  "v_mul_f32 v30, 0x44870000, v31\n" \
  "v_mov_b64 v[12:13], 0\n" \
  "v_mov_b64 v[14:15], 0\n" \
  "v_mov_b64 v[16:17], 0\n" \
  "v_mov_b64 v[18:19], 0\n" \
  "v_mov_b64 v[20:21], 0\n" \
  "v_mov_b64 v[22:23], 0\n" \
  "v_readfirstlane_b32 s72, v31\n" \
  "v_readfirstlane_b32 s86, v30\n" \
  "s_lshr_b32 s86, s86, 23\n" \
  "s_addk_i32 s86, 0xff83\n" \
  "s_ashr_i32 s86, s86, 1\n" \
  "s_max_i32 s86, s86, -20\n" \
  "s_min_i32 s86, s86, 100\n" \
  "s_mov_b32 s82, s86\n" \
  "s_lshr_b32 s83, s72, 23\n" \
  "s_sub_i32 s83, s83, 243\n" \
  "s_waitcnt lgkmcnt(0)\n" \
  "s_add_u32 s88, s74, 157760\n" \
  "v_mov_b32 v59, s88\n" \
  "s_cmp_lg_u32 s61, 0\n" \
  "s_movk_i32 s76, -8000\n" \
  "s_cmovk_i32 s76, 9152\n" \
  "s_movk_i32 s77, 90\n" \
  "s_cmovk_i32 s77, 90\n" \
  "s_cselect_b32 s72, -1, 55\n" \
  "s_mov_b32 s87, 0x40000\n" \
  "s_cmp_eq_u32 s90, 0\n" \
  "s_cbranch_scc1 .LCV_W1_%=\n" \
  "s_branch .LCV_W2_%=\n" \
  ".LCV_W0_%=:\n" \
  "s_sleep 2\n" \
  "s_cmp_eq_u32 s61, 0\n" \
  "s_cbranch_scc1 .LCV_W2_%=\n" \
  "s_sleep 32\n" \
  ".LCV_W2_%=:\n" \
  "s_mov_b64 exec, 15\n" \
  "buffer_load_dword v58, v2, s[44:47], s68 offen sc1\n" \
  "s_mov_b64 exec, -1\n" \
  "s_sub_u32 s87, s87, 1\n" \
  "s_cbranch_scc1 .LCV_END_%=\n" \
  "s_waitcnt vmcnt(0)\n" \
  "v_cmp_eq_u32 vcc, 0, v58\n" \
  "s_and_b32 vcc_lo, vcc_lo, 15\n" \
  "s_cbranch_scc1 .LCV_W0_%=\n" \
  "s_mov_b32 m0, s99\n" \
  "s_add_u32 s88, s99, 8192\n" \
  "global_load_lds_dwordx4 v10, s[62:63] sc1\n" \
  "global_load_lds_dwordx4 v10, s[62:63] offset:1024 sc1\n" \
  "s_mov_b32 m0, s88\n" \
  "s_nop 0\n" \
  "global_load_lds_dwordx4 v10, s[64:65] sc1\n" \
  "global_load_lds_dwordx4 v10, s[64:65] offset:1024 sc1\n" \
  ".LCV_W1_%=:\n" \
  "s_add_u32 s62, s62, 0x80000\n" \
  "s_addc_u32 s63, s63, 0\n" \
  "s_add_u32 s64, s64, 0x40000\n" \
  "s_addc_u32 s65, s65, 0\n" \
  "s_add_u32 s68, s68, 0x1000\n" \
  "s_cmp_eq_u32 s61, 0\n" \
  "s_cbranch_scc1 .LCV_W4_%=\n" \
  "s_add_u32 s38, s67, 4992\n" \
  "v_mov_b32 v58, s38\n" \
  "buffer_load_dword v58, v58, s[0:3], 0 offen sc1\n" \
  "s_waitcnt vmcnt(0)\n" \
  "v_readfirstlane_b32 vcc_lo, v58\n" \
  "s_movk_i32 s87, 200\n" \
  "s_cmp_eq_u32 vcc_lo, 0\n" \
  "s_cbranch_scc1 .LCV_W4_%=\n" \
  "s_add_u32 vcc_lo, vcc_lo, 590\n" \
  ".LCV_W3_%=:\n" \
  "s_sleep 2\n" \
  "s_memrealtime s[38:39]\n" \
  "s_sub_u32 s87, s87, 1\n" \
  "s_cbranch_scc1 .LCV_W4_%=\n" \
  "s_waitcnt lgkmcnt(0)\n" \
  "s_sub_u32 s38, s38, vcc_lo\n" \
  "s_cmp_lt_i32 s38, 0\n" \
  "s_cbranch_scc1 .LCV_W3_%=\n" \
  ".LCV_W4_%=:\n" \
  "s_waitcnt vmcnt(0)\n" \
  ".p2align 6\n" \
  ".LCV_TOP_%=:\n" \
  "s_waitcnt lgkmcnt(0)\n" \
  "s_barrier\n" \
  "s_setprio 3\n" \
  "ds_read_b128 v[24:27], v1 offset:0\n" \
  "ds_read_b128 v[28:31], v1 offset:1024\n" \
  "ds_read_b128 v[32:35], v1 offset:2048\n" \
  "ds_read_b128 v[36:39], v1 offset:3072\n" \
  "ds_read_b128 v[40:43], v1 offset:4096\n" \
  "ds_read_b128 v[44:47], v1 offset:5120\n" \
  "ds_read_b128 v[48:51], v1 offset:6144\n" \
  "ds_read_b128 v[52:55], v1 offset:7168\n" \
  "s_memrealtime s[38:39]\n" \
  "s_waitcnt lgkmcnt(7)\n" \
  "v_smfmac_f32_16x16x64_bf16 v[12:15], v[24:27], v[64:71], v0\n" \
  "s_waitcnt lgkmcnt(6)\n" \
  "v_smfmac_f32_16x16x64_bf16 v[12:15], v[28:31], v[72:79], v0\n" \
  "s_waitcnt lgkmcnt(5)\n" \
  "v_smfmac_f32_16x16x64_bf16 v[12:15], v[32:35], v[80:87], v0\n" \
  "s_waitcnt lgkmcnt(4)\n" \
  "v_smfmac_f32_16x16x64_bf16 v[12:15], v[36:39], v[88:95], v0\n" \
  "s_waitcnt lgkmcnt(3)\n" \
  "v_smfmac_f32_16x16x64_bf16 v[12:15], v[40:43], v[96:103], v0\n" \
  "s_waitcnt lgkmcnt(2)\n" \
  "v_smfmac_f32_16x16x64_bf16 v[12:15], v[44:47], v[104:111], v0\n" \
  "s_waitcnt lgkmcnt(1)\n" \
  "v_smfmac_f32_16x16x64_bf16 v[12:15], v[48:51], v[112:119], v0\n" \
  "s_waitcnt lgkmcnt(0)\n" \
  "v_smfmac_f32_16x16x64_bf16 v[12:15], v[52:55], v[120:127], v0\n" \
  "ds_read_b32 v11, v1 offset:26688\n" \
  "s_mov_b32 s61, s38\n" \
  "s_add_u32 s38, s38, s79\n" \
  "s_add_u32 s38, s38, s96\n" \
  "s_mov_b32 s96, 0\n" \
  "v_or_b32 v57, 1, s38\n" \
  "v_add_u32 v56, s67, v10\n" \
  "s_add_u32 s39, s67, s76\n" \
  "s_add_u32 s38, s38, s77\n" \
  "s_bitset1_b32 s38, 0\n" \
  "v_writelane_b32 v56, s39, 1\n" \
  "v_writelane_b32 v57, s38, 1\n" \
  "s_sub_u32 s39, s71, s72\n" \
  "s_cmp_lt_u32 s39, 9\n" \
  "s_cselect_b64 exec, 19, 3\n" \
  "buffer_store_dword v57, v56, s[0:3], 0 offen sc1\n" \
  "s_mov_b64 exec, -1\n" \
  "v_permlane32_swap_b32 v12, v14\n" \
  "v_permlane32_swap_b32 v13, v15\n" \
  "v_pk_add_f32 v[12:13], v[12:13], v[14:15]\n" \
  "s_nop 0\n" \
  "v_cvt_pk_bf16_f32 v12, v12, v13\n" \
  "ds_write_b32 v4, v12\n" \
  "s_setprio 2\n" \
  "v_mov_b64 v[12:13], s[92:93]\n" \
  "v_mov_b64 v[14:15], s[92:93]\n" \
  "s_waitcnt lgkmcnt(1)\n" \
  "v_readfirstlane_b32 s86, v11\n" \
  "s_lshr_b32 s86, s86, 23\n" \
  "s_add_i32 s86, s86, s83\n" \
  "s_ashr_i32 s86, s86, 1\n" \
  "s_max_i32 s86, s86, s82\n" \
  "s_min_i32 s86, s86, 100\n" \
  "s_lshl_b32 s86, s86, 23\n" \
  "s_sub_i32 s84, 0xc8800000, s86\n" \
  "s_cmp_eq_u32 s73, 5\n" \
  "v_smfmac_f32_16x16x64_bf16 v[16:19], v[24:27], v[128:135], v0\n" \
  "v_smfmac_f32_16x16x64_bf16 v[16:19], v[28:31], v[136:143], v0\n" \
  "v_smfmac_f32_16x16x64_bf16 v[16:19], v[32:35], v[144:151], v0\n" \
  "v_smfmac_f32_16x16x64_bf16 v[16:19], v[36:39], v[152:159], v0\n" \
  "v_smfmac_f32_16x16x64_bf16 v[16:19], v[40:43], v[160:167], v0\n" \
  "v_smfmac_f32_16x16x64_bf16 v[16:19], v[44:47], v[168:175], v0\n" \
  "s_waitcnt lgkmcnt(0)\n" \
  "s_barrier\n" \
  "ds_read_b64_tr_b16 v[60:61], v6 offset:0\n" \
  "ds_read_b64_tr_b16 v[62:63], v9 offset:0\n" \
  "v_smfmac_f32_16x16x64_bf16 v[16:19], v[48:51], v[176:183], v0\n" \
  "v_smfmac_f32_16x16x64_bf16 v[16:19], v[52:55], v[184:191], v0\n" \
  "v_smfmac_f32_16x16x64_bf16 v[20:23], v[24:27], v[192:199], v0\n" \
  "v_smfmac_f32_16x16x64_bf16 v[20:23], v[28:31], v[200:207], v0\n" \
  "v_smfmac_f32_16x16x64_bf16 v[20:23], v[32:35], v[208:215], v0\n" \
  "v_smfmac_f32_16x16x64_bf16 v[20:23], v[36:39], v[216:223], v0\n" \
  "v_smfmac_f32_16x16x64_bf16 v[20:23], v[40:43], v[224:231], v0\n" \
  "v_smfmac_f32_16x16x64_bf16 v[20:23], v[44:47], v[232:239], v0\n" \
  "v_smfmac_f32_16x16x64_bf16 v[20:23], v[48:51], v[240:247], v0\n" \
  "v_smfmac_f32_16x16x64_bf16 v[20:23], v[52:55], v[248:255], v0\n" \
  "s_cmp_eq_u32 s71, 0\n" \
  "v_permlane32_swap_b32 v16, v18\n" \
  "v_permlane32_swap_b32 v17, v19\n" \
  "v_add_f32 v16, v16, v18\n" \
  "v_add_f32 v17, v17, v19\n" \
  "v_cvt_pk_bf16_f32 v16, v16, v17\n" \
  "s_waitcnt lgkmcnt(0)\n" \
  "v_max_u32 v60, 1, v60\n" \
  "v_max_u32 v61, 1, v61\n" \
  "v_max_u32 v62, 1, v62\n" \
  "v_max_u32 v63, 1, v63\n" \
  "buffer_store_dwordx4 v[60:63], v7, s[48:51], s69 offen sc1\n" \
  "v_permlane32_swap_b32 v20, v22\n" \
  "v_permlane32_swap_b32 v21, v23\n" \
  "v_pk_add_f32 v[20:21], v[20:21], v[22:23]\n" \
  "v_max_u32 v18, 1, v16\n" \
  "v_cvt_pk_bf16_f32 v17, v20, v21\n" \
  "ds_write_b64 v5, v[16:17]\n" \
  "v_max_u32 v19, 1, v17\n" \
  "s_nop 1\n" \
  "v_permlane32_swap_b32 v18, v19\n" \
  "buffer_store_dwordx2 v[18:19], v8, s[52:55], s70 offen sc1\n" \
  "ds_read_b128 v[24:27], v3 offset:0\n" \
  "ds_read_b128 v[28:31], v3 offset:1024\n" \
  "ds_read_b128 v[32:35], v3 offset:2048\n" \
  "ds_read_b128 v[36:39], v3 offset:3072\n" \
  "ds_read_b128 v[40:43], v3 offset:8192\n" \
  "ds_read_b128 v[44:47], v3 offset:9216\n" \
  "ds_read_b128 v[48:51], v3 offset:10240\n" \
  "ds_read_b128 v[52:55], v3 offset:11264\n" \
  "s_cbranch_scc1 .LCV_LAST_%=\n" \
  "s_cmp_eq_u32 s73, 5\n" \
  "s_cbranch_scc0 .LCV_NMK_%=\n" \
  "s_mov_b32 m0, s78\n" \
  "s_nop 0\n" \
  "buffer_load_dword v2, s[0:3], s66 offen sc1 lds\n" \
  ".LCV_NMK_%=:\n" \
  "s_setprio 0\n" \
  "s_waitcnt lgkmcnt(8)\n" \
  "s_barrier\n" \
  "s_add_u32 s67, s67, 1024\n" \
  "v_mov_b32 v11, 0\n" \
  "ds_write_b32 v59, v11\n" \
  "ds_read_b128 v[56:59], v3 offset:4096\n" \
  "ds_read_b128 v[60:63], v3 offset:5120\n" \
  "v_mov_b64 v[20:21], s[92:93]\n" \
  "v_mov_b64 v[22:23], s[92:93]\n" \
  "v_mov_b64 v[16:17], s[92:93]\n" \
  "v_mov_b64 v[18:19], s[92:93]\n" \
  "s_mov_b32 m0, s94\n" \
  "s_mov_b64 exec, 15\n" \
  "buffer_load_dword v2, s[44:47], s68 offen sc1 lds\n" \
  "s_mov_b64 exec, -1\n" \
  "s_setprio 2\n" \
  "s_waitcnt lgkmcnt(0)\n" \
  "s_barrier\n" \
  "ds_read_b128 v[4:7], v1 offset:8192\n" \
  "ds_read_b128 v[8:11], v1 offset:9216\n" \
  "s_waitcnt lgkmcnt(1)\n" \
  "v_smfmac_f32_16x16x64_bf16 v[12:15], v[4:7], v[24:31], v0\n" \
  "ds_read_b128 v[24:27], v3 offset:6144\n" \
  "ds_read_b128 v[28:31], v3 offset:7168\n" \
  "v_smfmac_f32_16x16x64_bf16 v[16:19], v[4:7], v[32:39], v0\n" \
  "ds_read_b128 v[32:35], v3 offset:12288\n" \
  "ds_read_b128 v[36:39], v3 offset:13312\n" \
  "s_waitcnt lgkmcnt(4)\n" \
  "v_smfmac_f32_16x16x64_bf16 v[12:15], v[8:11], v[40:47], v0\n" \
  "v_smfmac_f32_16x16x64_bf16 v[16:19], v[8:11], v[48:55], v0\n" \
  "v_smfmac_f32_16x16x64_bf16 v[20:23], v[4:7], v[56:63], v0\n" \
  "ds_read_b128 v[56:59], v3 offset:14336\n" \
  "ds_read_b128 v[60:63], v3 offset:15360\n" \
  "v_mov_b64 v[40:41], s[92:93]\n" \
  "v_mov_b64 v[42:43], s[92:93]\n" \
  "s_nop 2\n" \
  "v_permlane32_swap_b32 v12, v17\n" \
  "v_permlane32_swap_b32 v13, v16\n" \
  "v_permlane32_swap_b32 v14, v19\n" \
  "v_permlane32_swap_b32 v15, v18\n" \
  "v_pk_add_f32 v[12:13], v[12:13], v[16:17] op_sel:[0,1] op_sel_hi:[1,0]\n" \
  "v_pk_add_f32 v[14:15], v[14:15], v[18:19] op_sel:[0,1] op_sel_hi:[1,0]\n" \
  "buffer_atomic_add_x2 v[12:13], v2, s[40:43], s56 offen\n" \
  "buffer_atomic_add_x2 v[14:15], v2, s[40:43], s57 offen\n" \
  "s_waitcnt lgkmcnt(2)\n" \
  "v_smfmac_f32_16x16x64_bf16 v[20:23], v[8:11], v[32:39], v0\n" \
  "v_smfmac_f32_16x16x64_bf16 v[40:43], v[4:7], v[24:31], v0\n" \
  "s_waitcnt lgkmcnt(0)\n" \
  "v_smfmac_f32_16x16x64_bf16 v[40:43], v[8:11], v[56:63], v0\n" \
  "s_nop 7\n" \
  "v_permlane32_swap_b32 v20, v41\n" \
  "v_permlane32_swap_b32 v21, v40\n" \
  "v_permlane32_swap_b32 v22, v43\n" \
  "v_permlane32_swap_b32 v23, v42\n" \
  "v_pk_add_f32 v[20:21], v[20:21], v[40:41] op_sel:[0,1] op_sel_hi:[1,0]\n" \
  "v_pk_add_f32 v[22:23], v[22:23], v[42:43] op_sel:[0,1] op_sel_hi:[1,0]\n" \
  "buffer_atomic_add_x2 v[20:21], v2, s[40:43], s58 offen\n" \
  "buffer_atomic_add_x2 v[22:23], v2, s[40:43], s59 offen\n" \
  "s_setprio 0\n" \
  "s_memrealtime s[38:39]\n" \
  "v_mbcnt_lo_u32_b32 v48, -1, 0\n" \
  "v_mbcnt_hi_u32_b32 v48, -1, v48\n" \
  "s_add_u32 s88, s74, 157760\n" \
  "v_mov_b32 v59, s88\n" \
  "v_lshl_add_u32 v52, v48, 2, v59\n" \
  "ds_read_b32 v52, v52 offset:192\n" \
  "v_lshrrev_b32 v49, 3, v48\n" \
  "v_and_b32 v50, 7, v48\n" \
  "v_lshlrev_b32 v50, 4, v50\n" \
  "v_lshl_or_b32 v44, v49, 8, v50\n" \
  "s_mov_b32 s87, 0x40000\n" \
  "v_lshlrev_b32 v10, 4, v48\n" \
  "v_lshl_add_u32 v58, v48, 2, s94\n" \
  "s_waitcnt vmcnt(4)\n" \
  "ds_read_b32 v58, v58\n" \
  "s_waitcnt lgkmcnt(0)\n" \
  "v_cmp_eq_u32 vcc, 0, v58\n" \
  "s_and_b32 vcc_lo, vcc_lo, 15\n" \
  "s_cbranch_scc0 .LCV_GOT_%=\n" \
  "s_branch .LCV_ASK_%=\n" \
  ".LCV_LATE_%=:\n" \
  "v_cmp_eq_u32 vcc, 0, v58\n" \
  "s_and_b32 vcc_lo, vcc_lo, 15\n" \
  "s_cbranch_scc0 .LCV_GOT_%=\n" \
  "s_sleep 12\n" \
  ".LCV_ASK_%=:\n" \
  "s_mov_b64 exec, 15\n" \
  "buffer_load_dword v58, v2, s[44:47], s68 offen sc1\n" \
  "s_mov_b64 exec, -1\n" \
  "s_sub_u32 s87, s87, 1\n" \
  "s_cbranch_scc1 .LCV_END_%=\n" \
  "s_waitcnt vmcnt(0)\n" \
  "s_branch .LCV_LATE_%=\n" \
  ".LCV_GOT_%=:\n" \
  "s_mov_b32 m0, s99\n" \
  "s_add_u32 s88, s99, 8192\n" \
  "global_load_lds_dwordx4 v10, s[62:63] sc1\n" \
  "global_load_lds_dwordx4 v10, s[62:63] offset:1024 sc1\n" \
  "s_mov_b32 m0, s88\n" \
  "s_nop 0\n" \
  "global_load_lds_dwordx4 v10, s[64:65] sc1\n" \
  "global_load_lds_dwordx4 v10, s[64:65] offset:1024 sc1\n" \
  ".LCV_HAVE_%=:\n" \
  "s_waitcnt lgkmcnt(0)\n" \
  "s_sub_u32 s61, s38, s61\n" \
  "s_add_u32 s79, s79, 2\n" \
  "s_min_u32 s79, s79, s61\n" \
  "s_sub_u32 s39, s38, 150\n" \
  "v_subrev_u32 v52, s39, v52\n" \
  "v_cmp_lt_u32 vcc, 0x1000, v52\n" \
  "s_cmp_eq_u64 vcc, 0\n" \
  "s_cbranch_scc1 .LCV_OKM_%=\n" \
  ".LCV_AGN_%=:\n" \
  "buffer_load_dword v52, v2, s[0:3], s66 offen sc1\n" \
  "s_sub_u32 s87, s87, 1\n" \
  "s_cbranch_scc1 .LCV_END_%=\n" \
  "s_waitcnt vmcnt(0)\n" \
  "v_cmp_eq_u32 vcc, 0, v52\n" \
  "v_subrev_u32 v52, s39, v52\n" \
  "s_cmp_eq_u64 vcc, 0\n" \
  "s_cbranch_scc1 .LCV_OKM_%=\n" \
  "s_sleep 4\n" \
  "s_branch .LCV_AGN_%=\n" \
  ".LCV_OKM_%=:\n" \
  "s_nop 1\n" \
  "v_max_i32_dpp v52, v52, v52 row_ror:1 row_mask:0xf bank_mask:0xf\n" \
  "s_nop 1\n" \
  "v_max_i32_dpp v52, v52, v52 row_ror:2 row_mask:0xf bank_mask:0xf\n" \
  "s_nop 1\n" \
  "v_max_i32_dpp v52, v52, v52 row_ror:4 row_mask:0xf bank_mask:0xf\n" \
  "s_nop 1\n" \
  "v_max_i32_dpp v52, v52, v52 row_ror:8 row_mask:0xf bank_mask:0xf\n" \
  "s_movk_i32 vcc_lo, 150\n" \
  "v_readlane_b32 s88, v52, 0\n" \
  "s_max_i32 vcc_lo, vcc_lo, s88\n" \
  "v_readlane_b32 s88, v52, 16\n" \
  "s_max_i32 vcc_lo, vcc_lo, s88\n" \
  "v_readlane_b32 s88, v52, 32\n" \
  "s_max_i32 vcc_lo, vcc_lo, s88\n" \
  "v_readlane_b32 s88, v52, 48\n" \
  "s_max_i32 vcc_lo, vcc_lo, s88\n" \
  "s_add_u32 s66, s66, 1024\n" \
  "s_add_u32 vcc_lo, vcc_lo, s39\n" \
  "s_add_u32 vcc_lo, vcc_lo, 23\n" \
  ".LCV_TW_%=:\n" \
  "s_memrealtime s[38:39]\n" \
  "s_waitcnt lgkmcnt(0)\n" \
  "s_sub_u32 s38, s38, vcc_lo\n" \
  "s_cmp_lt_i32 s38, 0\n" \
  "s_cbranch_scc1 .LCV_TW_%=\n" \
  "buffer_load_dwordx4 v[24:27], v44, s[40:43], s60 offen sc1\n" \
  "buffer_load_dwordx4 v[28:31], v44, s[40:43], s60 offen offset:2048 sc1\n" \
  "s_add_u32 s62, s62, 0x80000\n" \
  "s_addc_u32 s63, s63, 0\n" \
  "s_add_u32 s64, s64, 0x40000\n" \
  "s_addc_u32 s65, s65, 0\n" \
  "s_add_u32 s68, s68, 0x1000\n" \
  "s_add_u32 s69, s69, 0x10000\n" \
  "s_add_u32 s70, s70, 0x10000\n" \
  "s_sub_u32 s88, s73, 4\n" \
  "v_lshrrev_b32 v49, 2, v48\n" \
  "v_lshrrev_b32 v50, 4, v48\n" \
  "v_bitop3_b32 v50, v49, v50, 3 bitop3:0x28\n" \
  "v_lshl_add_u32 v4, v50, 3, s97\n" \
  "v_and_b32 v50, 3, v48\n" \
  "v_lshl_add_u32 v4, v50, 5, v4\n" \
  "v_and_b32 v49, 1, v49\n" \
  "v_lshl_add_u32 v4, v49, 8, v4\n" \
  "v_and_b32 v49, 8, v48\n" \
  "v_lshl_add_u32 v4, v49, 4, v4\n" \
  "v_and_b32 v51, 15, v48\n" \
  "v_bfe_u32 v49, v48, 4, 1\n" \
  "v_lshrrev_b32 v50, 5, v48\n" \
  "s_lshl_b32 s89, s88, 10\n" \
  "s_add_u32 s89, s89, s74\n" \
  "s_add_u32 s89, s89, 158720\n" \
  "v_lshrrev_b32 v9, 3, v51\n" \
  "v_mul_u32_u24 v6, 41, v9\n" \
  "v_xor_b32 v6, v6, v51\n" \
  "v_lshl_add_u32 v6, v6, 3, s89\n" \
  "v_lshl_add_u32 v6, v49, 6, v6\n" \
  "v_lshl_add_u32 v6, v50, 9, v6\n" \
  "v_xor_b32 v9, 0x90, v6\n" \
  "v_lshlrev_b32 v8, 4, v49\n" \
  "v_lshl_add_u32 v8, v51, 5, v8\n" \
  "v_lshl_add_u32 v8, v50, 3, v8\n" \
  "s_lshl_b32 s89, s88, 8\n" \
  "s_add_u32 s89, s89, s74\n" \
  "s_add_u32 s89, s89, 139264\n" \
  "v_and_b32 v49, 31, v48\n" \
  "v_lshl_add_u32 v5, v49, 3, s89\n" \
  "v_lshl_add_u32 v5, v50, 10, v5\n" \
  "v_lshrrev_b32 v49, 2, v51\n" \
  "v_and_b32 v50, 5, v51\n" \
  "v_and_or_b32 v49, v49, 2, v50\n" \
  "v_and_b32 v50, 48, v48\n" \
  "v_lshlrev_b32 v7, 3, v50\n" \
  "v_lshl_add_u32 v7, v49, 4, v7\n" \
  "v_bitop3_b32 v49, v51, 2, 2 bitop3:0x0c\n" \
  "v_lshl_add_u32 v7, v49, 29, v7\n" \
  "v_mov_b64 v[12:13], 0\n" \
  "v_mov_b64 v[14:15], 0\n" \
  "v_mov_b64 v[16:17], 0\n" \
  "v_mov_b64 v[18:19], 0\n" \
  "v_mov_b64 v[20:21], 0\n" \
  "v_mov_b64 v[22:23], 0\n" \
  "v_and_b32 v50, 3, v48\n" \
  "v_lshl_add_u32 v40, v50, 2, s98\n" \
  "v_lshrrev_b32 v50, 4, v48\n" \
  "v_mad_u32_u24 v40, v50, s95, v40\n" \
  "v_bfe_u32 v49, v48, 3, 1\n" \
  "v_lshl_add_u32 v40, v49, 6, v40\n" \
  "v_bfe_u32 v49, v48, 2, 1\n" \
  "v_lshl_add_u32 v40, v49, 7, v40\n" \
  "v_xor_b32 v41, 16, v40\n" \
  "v_xor_b32 v42, 32, v40\n" \
  "v_xor_b32 v43, 48, v40\n" \
  "s_add_i32 s80, s86, 0x35000000\n" \
  "v_mov_b32 v11, 0\n" \
  "s_mov_b32 s89, s60\n" \
  "s_bitcmp1_b32 s60, 7\n" \
  "s_cselect_b32 s88, 0x10000, 0\n" \
  "s_bitset1_b32 s88, 7\n" \
  "s_xor_b32 s56, s56, s88\n" \
  "s_xor_b32 s57, s57, s88\n" \
  "s_xor_b32 s58, s58, s88\n" \
  "s_xor_b32 s59, s59, s88\n" \
  "s_xor_b32 s60, s60, s88\n" \
  "s_sub_u32 s71, s71, 1\n" \
  ".LCV_SEE_%=:\n" \
  "s_waitcnt vmcnt(1)\n" \
  "v_cvt_f32_i32 v24, v24\n" \
  "v_cvt_f32_i32 v25, v25\n" \
  "v_cvt_f32_i32 v26, v26\n" \
  "v_cvt_f32_i32 v27, v27\n" \
  "v_max_f32 v45, |v24|, |v26|\n" \
  "v_pk_mul_f32 v[24:25], v[24:25], s[80:81] op_sel_hi:[1,0]\n" \
  "v_pk_mul_f32 v[26:27], v[26:27], s[80:81] op_sel_hi:[1,0]\n" \
  "s_nop 0\n" \
  "v_cvt_pk_bf16_f32 v32, v24, v26\n" \
  "v_cvt_pk_bf16_f32 v33, v25, v27\n" \
  "ds_write_b32 v40, v32\n" \
  "ds_write_b32 v41, v33\n" \
  "s_waitcnt vmcnt(0)\n" \
  "v_cvt_f32_i32 v28, v28\n" \
  "v_cvt_f32_i32 v30, v30\n" \
  "v_cvt_f32_i32 v29, v29\n" \
  "v_max3_f32 v45, v45, |v28|, |v30|\n" \
  "v_cvt_f32_i32 v31, v31\n" \
  "v_cmp_lt_f32 vcc, s91, v45\n" \
  "v_pk_mul_f32 v[28:29], v[28:29], s[80:81] op_sel_hi:[1,0]\n" \
  "v_pk_mul_f32 v[30:31], v[30:31], s[80:81] op_sel_hi:[1,0]\n" \
  "s_cmp_eq_u64 vcc, 0\n" \
  "s_cbranch_scc0 .LCV_POLL_%=\n" \
  "v_cvt_pk_bf16_f32 v36, v28, v30\n" \
  "v_cvt_pk_bf16_f32 v37, v29, v31\n" \
  "ds_write_b32 v42, v36\n" \
  "ds_write_b32 v43, v37\n" \
  "v_dot2c_f32_bf16 v11, v32, v32\n" \
  "v_dot2c_f32_bf16 v11, v33, v33\n" \
  "v_dot2c_f32_bf16 v11, v36, v36\n" \
  "v_dot2c_f32_bf16 v11, v37, v37\n" \
  "s_nop 1\n" \
  "s_nop 0\n" \
  "v_cmp_lt_f32 vcc, 4.0, v11\n" \
  "s_cbranch_vccz .LCV_TOP_%=\n" \
  "s_mov_b64 exec, vcc\n" \
  "ds_max_u32 v59, v11\n" \
  "s_mov_b64 exec, -1\n" \
  "s_branch .LCV_TOP_%=\n" \
  ".LCV_POLL_%=:\n" \
  "buffer_load_dwordx4 v[24:27], v44, s[40:43], s89 offen sc1\n" \
  "buffer_load_dwordx4 v[28:31], v44, s[40:43], s89 offen offset:2048 sc1\n" \
  "s_sub_u32 s87, s87, 1\n" \
  "s_cbranch_scc1 .LCV_END_%=\n" \
  "s_branch .LCV_SEE_%=\n" \
  ".LCV_LAST_%=:\n" \
  "s_waitcnt vmcnt(0)\n" \
  ".LCV_END_%=:\n" \
  "s_endpgm\n"
__device__ __forceinline__ void start_kgen(const MegaArgs& A, char* smem, const int kg) {
  const int w = A.wv, l = lane_id(), tid = 64 * w + l, c = l & 15, g = l >> 4;
  KGen G;
  unsigned tp = 0u;
#define TEXT_ASK()                                                                                                     \
  {                                                                                                                    \
    const char* pc;                                                                                                    \
    unsigned n;                                                                                                        \
    asm volatile("s_getpc_b64 %0\n.LKG_PC:\n\ts_mov_b32 %1, .LKG_LAST-.LKG_PC" : "=s"(pc), "=s"(n));                    \
    const unsigned o = (unsigned)(tid * 8 + ((blockIdx.x >> 3) & 7)) * IPS;                                            \
    if ((int)o < (int)n) asm volatile("global_load_dword %0, %1, %2" : "=v"(tp) : "v"(o), "s"(pc) : "memory");         \
  }
  if (IPF == 2) TEXT_ASK()
  v4u raw[2] = {};
#pragma unroll
  for (int i = 0; i < TP; ++i)
    if (kg < 32) raw[i] = *reinterpret_cast<const v4u*>(A.x0 + (size_t)(TP * (8 * (kg & 7) + w) + i) * DM + l * 8);
  G.load(A, 8 * kg + w, l);
  {
    const __amdgpu_buffer_rsrc_t rz = MKRS(A.ws + OFF_Z, Z_BYTES);
#pragma unroll
    for (int i = 0; i < 4; ++i) {
      const u64 z = i == 0 && kg < (int)(2 * SZ_ACC / 8192) ? ACC_INIT : 0ull;
      ST128((v4u{(unsigned)z, (unsigned)(z >> 32), (unsigned)z, (unsigned)(z >> 32)}), rz, tid * 16, (i * 64 + kg) * 8192);
    }
  }
  const u64 tag = call_tag(A);
  if (TP && kg < 32) {
    unsigned t = row_stats(raw[0]);
    if (TP > 1) {
      const unsigned t1 = row_stats(raw[1]);
      t = l < 2 ? t : t1;
    }
    if (l < 2 * TP) __hip_atomic_store((gu64*)(A.ws + OFF_GT) + 2 * (L_SEQ + TP * (8 * kg + w)) + l, tagged(tag, (l & 1) ? t : t << 16), RLX_AGENT);
  }
  DRAIN();
  __syncthreads();
  if (tid < 8) __hip_atomic_store((gu64*)(A.ws + OFF_RD) + tid * 64 + kg, tag, RLX_AGENT);
  asm volatile("" : "+v"(G.pa), "+v"(G.pb), "+v"(G.pe));
  G.setup();
  G.st = smem + w * 16640;
  const __amdgpu_buffer_rsrc_t rs = MKRS(A.ws + OFF_KF + (8 * kg + w) * 2048, 386 * 4);
  SST(1);
  unsigned tw = 0u;
#define TLB_ASK()                                                                                                      \
  if (tid < 384) {                                                                                                     \
    const unsigned o = (unsigned)OFF_XA + (unsigned)(tid * 8 + ((blockIdx.x >> 3) & 7)) * 65536u;                      \
    if (o < WS_TOTAL) asm volatile("global_load_dword %0, %1, %2" : "=v"(tw) : "v"(o), "s"(A.ws) : "memory");          \
  }
#define X2_CLEAR() \
  _Pragma("unroll") for (int i = 0; i < 4; ++i) ST128((v4u{0u, 0u, 0u, 0u}), MKRS(A.ws + OFF_X2, SZ_X), tid * 16, (4 * kg + i) * 8192);
  if (XZP == 1) { X2_CLEAR() }
  if (IPF == 1) TEXT_ASK()
  if (TLBW == 1) TLB_ASK()
  unsigned pf[6] = {};
#define PF_ASK(M)                                                                                                                  \
  if (kg < 32) {                                                                                                                   \
    if ((M) & 1) asm volatile("global_load_dword %0, %2, %3\n\tglobal_load_dword %1, %2, %3 offset:128" : "=&v"(pf[0]), "=&v"(pf[1]) : "v"(kg * 131072 + tid * 256), "s"(A.Wo)); \
    if ((M) & 2) asm volatile("global_load_dword %0, %2, %3\n\tglobal_load_dword %1, %2, %3 offset:128" : "=&v"(pf[2]), "=&v"(pf[3]) : "v"(kg * 256 + tid * 8192), "s"(A.Wq)); \
    if ((M) & 4) asm volatile("global_load_dword %0, %2, %3\n\tglobal_load_dword %1, %2, %3 offset:128" : "=&v"(pf[4]), "=&v"(pf[5]) : "v"(kg * 256 + tid * 8192), "s"(A.Wk)); \
  }
  PF_ASK(PFA)
  G.columns(l, 0, 16);
  G.get(G.bh, G.bl, l);
  if (XZP == 2) { X2_CLEAR() }
  PF_ASK(PFB)
  if (TLBW == 2) TLB_ASK()
  (void)G.scale();
  const Cx zb = G.power(256.f);
  Cx b = G.C, z = G.power(2.f);
#pragma unroll 1
  for (int f = 1; f < 9;) {
    char* p = G.st + l * 8;
    const int fe = f == 1 ? 4 : 9;
#pragma unroll 1
    for (; f < fe; ++f) {
      b = cmul(b, zb);
      if (f == 2 || f == 4) z = cmul(z, z);
      Cx y = b;
      p = G.rows(p, y, z, f == 1 ? 8 : f < 4 ? 4 : 2);
    }
    const f32x4 d = G.tile<false>(l);
    const v4u r = {pkh(d.x, d.x), pkh(d.y, d.y), pkh(d.z, d.z), pkh(d.w, d.w)};
    if (f == 4) {
      ST128(r, rs, (g < 2 ? 8 * c + 4 * g : 64 * g + 4 * c) * 4, 0);
    } else {
      ST64((v2u{r.x, r.y}), rs, (256 + 64 * g + 2 * c) * 4, 0);
      ST64((v2u{r.z, r.w}), rs, (288 + 64 * g + 2 * c) * 4, 0);
    }
  }
  SST(2);
  DRAIN();
  asm volatile("" ::"v"(tp), "v"(tw), "v"(pf[0]), "v"(pf[1]), "v"(pf[2]), "v"(pf[3]), "v"(pf[4]), "v"(pf[5]));
  if (l == 0) __hip_atomic_store((gu64*)(A.ws + OFF_KG) + kg * 16 + w, tag, RLX_AGENT);
  if (XZP == 3) { X2_CLEAR() }
  const u64 rv = (XZP & 3) && w == 0 ? ready_look(A) : 0;
  if (TLBW == 3) {
    TLB_ASK()
    DRAIN();
    asm volatile("" ::"v"(tw));
  }
  DRAIN();
  __syncthreads();
  if ((XZP & 3) && w == 0) {
    ready_wait(A, tag, rv);
    if (l < 8) set_flag((unsigned*)(A.ws + OFF_FL) + FL_FZ + l * 64 + kg);
  }
  SST(3);
}

constexpr int C_SM = 131072, C_SO = C_SM + 8192, C_KT = C_SO + 2048, C_X = C_KT + 16384, C_SQ = C_X + 1024;
static_assert(C_SQ + 4096 <= 163840 && C_SQ % 512 == 0, "lds");
__device__ __forceinline__ v4u mk_srd(const void* p, const unsigned bytes) {
  const unsigned long long a = (unsigned long long)p;
  return v4u{(unsigned)a, (unsigned)(a >> 32) & 0xffffu, bytes, 0x00020000u};
}
template <bool ATT>
__device__ __forceinline__ void role_chain_t(const MegaArgs& A, char* smem, const int L, const int h, const int rep) {
  const int w = A.wv;
  char* ws = A.ws;
  unsigned* FL = (unsigned*)(ws + OFF_FL);
  const int l = lane_id(), c = l & 15, g = l >> 4;
  unsigned* FQ = FL + FL_FQ + L * 64 * 32 * 32;
  const int wb = (w + ROT_H) & 7, tb = ROT_T;
  const unsigned sb = lds_base(smem);
  __builtin_amdgcn_s_waitcnt(0x0f70);
  glds16(A.mem0, (unsigned)((c & 7) * DM + 64 * w + 16 * g + 8 * (c >> 3)) * 2, sb + C_SM + w * 1024);
#pragma unroll
  for (int i = 0; i < 16; ++i)
    glds16((const char*)A.Wo + (size_t)(h * DH + 32 * (i >> 2) + 4 * w + (i & 2)) * (DM * 2),
           (unsigned)((i & 1) * 1024 + ((l ^ (((i & 1) | ((w & 3) << 1)) << 1)) << 4)), sb + w * 16384 + i * 1024);
  const u64 tag = call_tag(A);
  const char* gK = ws + OFF_QK + L * SZ_QK + ((size_t)h * 16 + 2 * w) * 1024;
  const char* gV = ws + OFF_VF + L * SZ_O + ((size_t)h * 8 + 2 * w - 8) * 1024;
  v8u pv, pw;
  {
    const unsigned voff = g * 256 + c * 8, wp = sb + w * 16384 + l * 16;
    pv[0] = (c & 8) ? 0xEEEE : 0x4444;
    pv[1] = sb + C_SM + l * 16;
    const unsigned ra = sb + C_SQ + (g >> 1) * 512 + (c ^ ((g & 1) * 18)) * 8;
    pv[2] = ATT ? ra : voff;
    pv[3] = ATT ? ra ^ 0x108 : wp;
    pv[4] = sb + ((c & 2) ? C_SQ : C_SO) + w * 256 + (((c & 5) | ((c >> 2) & 2)) + 8 * (g >> 1)) * 16 + 8 * ((g ^ (c >> 1)) & 1);
    pv[5] = voff;
    pv[6] = wp;
    pv[7] = sb + C_SO + (l >> 5) * 1024 + w * 256 + (l & 31) * 8;
    const int q = (l >> 2) & 3, p = l & 3;
    const int f = (q & 1) | ((g & 1) << 1) | ((q >> 1) << 2);
#pragma unroll
    for (int i = 0; i < 4; ++i) pw[i] = (4 * i + g) * (HD * 2) + ((c ^ (((g & 1) | (i << 1)) << 1)) << 4) - 1024 * i;
    pw[4] = sb + (g >> 1) * 16384 + (4 * (g & 1) + (q & 1) + 8 * (q >> 1)) * 256 + (w ^ f) * 32 + p * 8;
    pw[5] = sb + (4 * (g >> 1) + 2 * (q >> 1) + (g & 1)) * 16384 + (q & 1) * 1024 + p * 8 + ((4 * wb) ^ f ^ tb) * 32;
    pw[6] = pw[7] = 0u;
  }
  const size_t wo = (size_t)(16 * w) * HD + h * DH;
#define POL 2
#define PG(w) (POL == 2 ? (w) * 4096 : ((w) & 1) * 4096 + ((w) >> 1) * 16384)
  const v4u rsA = mk_srd(ws + OFF_ACC + L * SZ_ACC + (POL == 1 ? 8192 : 0), SZ_ACC - 8192);
  const v4u rsF = mk_srd(FQ, 64 * 32 * 32 * 4);
  const v4u rsMK = mk_srd(ws + OFF_MEMK + L * SZ_MEM, SZ_MEM);
  const v4u rsMV = mk_srd(ws + OFF_MEMV + L * SZ_MEM, SZ_MEM);
  const v4u pAB = (v4u{0u, 2048u, 3072u, 1024u} ^ (1536u * tb)) + PG(wb);
  const v4u rsT = mk_srd(FL + FL_FK + L * 32, 64 * 1024 - 128);
  unsigned sc[8];
  sc[0] = h * 128;
  sc[1] = (h * 4 + w - 4) * 512;
  sc[2] = (h * 4 + w - 4) * 512;
  sc[3] = NSEG - 1;
  sc[5] = w;
  sc[6] = sb;
  const char* dbg = ws;
#define CHAIN_OPS                                                                                                          \
  ::"{v[0:7]}"(pv), "{v[24:31]}"(pw), "{s[4:5]}"(A.Wv + wo), "{s[6:7]}"(A.Wq + wo), "{s[8:9]}"(A.Wk + wo), "{s[10:11]}"(tag),         \
      "{s[12:13]}"(ws + OFF_RD + (blockIdx.x & 7) * 512), "{s[14:15]}"(ws + OFF_G + (blockIdx.x & 7) * 256), "{s[16:17]}"(ws + OFF_G + h * 8), \
      "{s18}"(w == 4 && L == 0 ? 255 : 0), "{s19}"(sb + w * 16384), "{s[20:21]}"(ws + WS_TOTAL + (w == 0 ? 534 * 256 + A.vb : 610 * 256) * 8), \
      "{s[40:43]}"(rsA), "{s[44:47]}"(rsF), "{s[48:51]}"(rsMK), "{s[52:55]}"(rsMV), "{s[56:59]}"(pAB), "{s60}"(PG(w)), "{s61}"(ATT ? PG(h >> 2) + (h & 3) * 1024 : L), \
      "{s[62:63]}"(gK), "{s[64:65]}"(gV), "{s66}"(0), "{s67}"(w == 4 ? (h >> 3) * 256 + (h & 7) * 8 : 0x40000000), "{s68}"(sc[0]), "{s69}"(sc[1]), "{s70}"(sc[2]),    \
      "{s71}"(sc[3]), "{s73}"(sc[5]), "{s74}"(sc[6]), "{s[36:37]}"(dbg),                   \
      "{s[0:3]}"(rsT)                                                                                                      \
      : "memory", "vcc", "scc", "s99", "v255"
  if (ATT) asm volatile("s_mov_b64 exec, -1\ns_nop 4\n" CHAIN_ASM_ATT CHAIN_OPS);
  else asm volatile("s_mov_b64 exec, -1\ns_nop 4\n" CHAIN_ASM_VO CHAIN_OPS);
#undef CHAIN_OPS
}

__device__ __forceinline__ void role_chain(const MegaArgs& A, char* smem, const int L, const int h, const int rep) {
  if (A.wv < 4) role_chain_t<true>(A, smem, L, h, rep);
  else role_chain_t<false>(A, smem, L, h, rep);
}

constexpr int AW_SO = 0, AW_TK = 32768, AW_CNT = AW_TK + 98304;
__device__ __forceinline__ unsigned umin3(unsigned a, unsigned b, unsigned c) {
  return __builtin_elementwise_min(__builtin_elementwise_min(a, b), c);
}
#define AW_ASM_X \
  "s_mov_b32 s61, 0\n" \
  "s_mov_b32 s67, 1\n" \
  "s_mov_b32 s68, 0\n" \
  "s_mov_b32 s70, 0\n" \
  "s_mov_b32 s73, 0\n" \
  "s_mov_b32 s71, 0x3e0293ee\n" \
  "s_mov_b32 s72, 0xbe0293ee\n" \
  "s_mov_b32 s65, 49152\n" \
  "s_bitcmp0_b32 s58, 2\n" \
  "s_cbranch_scc1 .LCX_OWN_%=\n" \
  "v_swap_b32 v192, v208\n" \
  "v_swap_b32 v193, v209\n" \
  "v_swap_b32 v194, v210\n" \
  "v_swap_b32 v195, v211\n" \
  "v_swap_b32 v196, v212\n" \
  "v_swap_b32 v197, v213\n" \
  "v_swap_b32 v198, v214\n" \
  "v_swap_b32 v199, v215\n" \
  "v_swap_b32 v200, v216\n" \
  "v_swap_b32 v201, v217\n" \
  "v_swap_b32 v202, v218\n" \
  "v_swap_b32 v203, v219\n" \
  "v_swap_b32 v204, v220\n" \
  "v_swap_b32 v205, v221\n" \
  "v_swap_b32 v206, v222\n" \
  "v_swap_b32 v207, v223\n" \
  "v_swap_b32 v224, v240\n" \
  "v_swap_b32 v225, v241\n" \
  "v_swap_b32 v226, v242\n" \
  "v_swap_b32 v227, v243\n" \
  "v_swap_b32 v228, v244\n" \
  "v_swap_b32 v229, v245\n" \
  "v_swap_b32 v230, v246\n" \
  "v_swap_b32 v231, v247\n" \
  "v_swap_b32 v232, v248\n" \
  "v_swap_b32 v233, v249\n" \
  "v_swap_b32 v234, v250\n" \
  "v_swap_b32 v235, v251\n" \
  "v_swap_b32 v236, v252\n" \
  "v_swap_b32 v237, v253\n" \
  "v_swap_b32 v238, v254\n" \
  "v_swap_b32 v239, v255\n" \
  ".LCX_OWN_%=:\n" \
  "v_mov_b32 v38, s58\n" \
  "s_memrealtime s[38:39]\n" \
  "s_waitcnt lgkmcnt(0)\n" \
  "s_mov_b32 s69, s38\n" \
  "s_setprio 3\n" \
  ".p2align 6\n" \
  ".LCX_TOP_%=:\n" \
  "s_mov_b32 s62, 0x200000\n" \
  "s_cmp_ge_u32 s73, s67\n" \
  "s_cbranch_scc1 .LCX_TOK_%=\n" \
  "s_cmp_eq_u32 s61, 0\n" \
  "s_cbranch_scc1 .LCX_SPIN_%=\n" \
  ".LCX_RE_%=:\n" \
  "ds_read_b32 v37, v38\n" \
  "s_add_u32 s62, s62, 1\n" \
  "s_waitcnt lgkmcnt(0)\n" \
  "v_readfirstlane_b32 s73, v37\n" \
  "s_bfe_u32 s64, s73, 0x80008\n" \
  "s_and_b32 s73, s73, 0xff\n" \
  "s_min_u32 s73, s73, s64\n" \
  "s_cmp_ge_u32 s73, s67\n" \
  "s_cbranch_scc1 .LCX_TOK_%=\n" \
  "s_sleep 1\n" \
  "s_cmp_lt_u32 s62, 2097158\n" \
  "s_cbranch_scc1 .LCX_RE_%=\n" \
  "s_waitcnt vmcnt(0)\n" \
  "v_mov_b32 v144, s59\n" \
  "v_mov_b32 v145, 1\n" \
  "s_mov_b64 exec, 1\n" \
  "ds_add_rtn_u32 v144, v144, v145\n" \
  "s_mov_b64 exec, -1\n" \
  "s_xor_b32 s59, s59, 4\n" \
  "s_mov_b32 s61, 0\n" \
  "s_waitcnt lgkmcnt(0)\n" \
  "v_readfirstlane_b32 s63, v144\n" \
  "v_lshlrev_b32 v144, 5, v5\n" \
  "s_and_b32 s63, s63, 7\n" \
  "s_cmp_eq_u32 s63, 7\n" \
  "s_cbranch_scc0 .LCX_W1_%=\n" \
  "s_mov_b64 exec, 15\n" \
  "global_store_dword v144, v145, s[52:53] sc1\n" \
  "s_mov_b64 exec, -1\n" \
  ".LCX_W1_%=:\n" \
  "s_add_u32 s52, s52, 2048\n" \
  "s_addc_u32 s53, s53, 0\n" \
  ".LCX_SPIN_%=:\n" \
  "s_sleep 2\n" \
  "s_memrealtime s[38:39]\n" \
  "ds_read_b32 v37, v38\n" \
  "s_sub_u32 s62, s62, 1\n" \
  "s_cbranch_scc1 .LCX_END_%=\n" \
  "s_waitcnt lgkmcnt(0)\n" \
  "v_readfirstlane_b32 s73, v37\n" \
  "s_add_u32 s38, s38, 80\n" \
  "s_bfe_u32 s64, s73, 0x80008\n" \
  "s_and_b32 s73, s73, 0xff\n" \
  "s_min_u32 s73, s73, s64\n" \
  "s_cmp_ge_u32 s73, s67\n" \
  "s_cbranch_scc0 .LCX_SPIN_%=\n" \
  "s_cmp_eq_u32 s70, 0\n" \
  "s_cbranch_scc1 .LCX_TOK_%=\n" \
  "s_sub_u32 s63, s38, s69\n" \
  "s_max_i32 s63, s63, 0\n" \
  "s_add_u32 s69, s69, s63\n" \
  ".LCX_TOK_%=:\n" \
  "ds_read_b128 v[48:51], v6 offset:0\n" \
  "ds_read_b128 v[64:67], v0 offset:8192\n" \
  "ds_read_b128 v[80:83], v0 offset:12288\n" \
  "ds_read_b128 v[52:55], v6 offset:1024\n" \
  "ds_read_b128 v[68:71], v0 offset:9216\n" \
  "ds_read_b128 v[84:87], v0 offset:13312\n" \
  "ds_read_b128 v[56:59], v6 offset:2048\n" \
  "ds_read_b128 v[72:75], v0 offset:10240\n" \
  "ds_read_b128 v[88:91], v0 offset:14336\n" \
  "ds_read_b128 v[60:63], v6 offset:3072\n" \
  "ds_read_b128 v[76:79], v0 offset:11264\n" \
  "ds_read_b128 v[92:95], v0 offset:15360\n" \
  "s_waitcnt lgkmcnt(9)\n" \
  "v_mfma_f32_16x16x32_bf16 v[20:23], v[64:67], v[48:51], v[8:11]\n" \
  "v_mfma_f32_16x16x32_bf16 v[24:27], v[80:83], v[48:51], v[12:15]\n" \
  "ds_read_b128 v[96:99], v0 offset:16384\n" \
  "ds_read_b128 v[100:103], v0 offset:17408\n" \
  "s_waitcnt lgkmcnt(8)\n" \
  "v_mfma_f32_16x16x32_bf16 v[20:23], v[68:71], v[52:55], v[20:23]\n" \
  "v_mfma_f32_16x16x32_bf16 v[24:27], v[84:87], v[52:55], v[24:27]\n" \
  "ds_read_b128 v[104:107], v0 offset:18432\n" \
  "ds_read_b128 v[108:111], v0 offset:19456\n" \
  "s_waitcnt lgkmcnt(7)\n" \
  "v_mfma_f32_16x16x32_bf16 v[20:23], v[72:75], v[56:59], v[20:23]\n" \
  "v_mfma_f32_16x16x32_bf16 v[24:27], v[88:91], v[56:59], v[24:27]\n" \
  "ds_read_b128 v[112:115], v0 offset:20480\n" \
  "ds_read_b128 v[116:119], v0 offset:21504\n" \
  "s_waitcnt lgkmcnt(6)\n" \
  "v_mfma_f32_16x16x32_bf16 v[20:23], v[76:79], v[60:63], v[20:23]\n" \
  "v_mfma_f32_16x16x32_bf16 v[24:27], v[92:95], v[60:63], v[24:27]\n" \
  "ds_read_b128 v[120:123], v0 offset:22528\n" \
  "ds_read_b128 v[124:127], v0 offset:23552\n" \
  "s_add_u32 s67, s67, 1\n" \
  "v_add_u32 v0, s65, v0\n" \
  "v_add_u32 v6, s65, v6\n" \
  "s_sub_u32 s65, 0, s65\n" \
  "s_nop 1\n" \
  "v_max3_f32 v32, v20, v21, v22\n" \
  "v_max3_f32 v33, v23, v24, v25\n" \
  "v_max3_f32 v32, v32, v26, v27\n" \
  "v_max_f32 v32, v32, v33\n" \
  "v_mov_b32 v33, v32\n" \
  "s_nop 1\n" \
  "v_permlane16_swap_b32 v32, v33\n" \
  "v_max_f32 v32, v32, v33\n" \
  "v_mov_b32 v33, v32\n" \
  "s_nop 1\n" \
  "v_permlane32_swap_b32 v32, v33\n" \
  "v_max_f32 v32, v32, v33\n" \
  "v_mul_f32 v33, s72, v32\n" \
  "v_fma_f32 v20, v20, s71, v33\n" \
  "v_fma_f32 v21, v21, s71, v33\n" \
  "v_fma_f32 v22, v22, s71, v33\n" \
  "v_fma_f32 v23, v23, s71, v33\n" \
  "v_fma_f32 v24, v24, s71, v33\n" \
  "v_fma_f32 v25, v25, s71, v33\n" \
  "v_fma_f32 v26, v26, s71, v33\n" \
  "v_fma_f32 v27, v27, s71, v33\n" \
  "v_exp_f32 v20, v20\n" \
  "v_exp_f32 v21, v21\n" \
  "v_exp_f32 v22, v22\n" \
  "v_exp_f32 v23, v23\n" \
  "v_exp_f32 v24, v24\n" \
  "v_exp_f32 v25, v25\n" \
  "v_exp_f32 v26, v26\n" \
  "v_exp_f32 v27, v27\n" \
  "v_add_f32 v34, v20, v21\n" \
  "v_add_f32 v33, v22, v23\n" \
  "v_add_f32 v36, v24, v25\n" \
  "v_add_f32 v34, v34, v33\n" \
  "v_add_f32 v33, v26, v27\n" \
  "v_add_f32 v34, v34, v36\n" \
  "v_add_f32 v34, v34, v33\n" \
  "v_mov_b64 v[28:29], 0\n" \
  "v_mov_b64 v[30:31], 0\n" \
  "s_waitcnt vmcnt(0) lgkmcnt(0)\n" \
  "s_cmp_eq_u32 s61, 0\n" \
  "s_cbranch_scc1 .LCX_NP_%=\n" \
  "v_mov_b32 v144, s59\n" \
  "v_mov_b32 v145, 1\n" \
  "s_mov_b64 exec, 1\n" \
  "ds_add_rtn_u32 v144, v144, v145\n" \
  "s_mov_b64 exec, -1\n" \
  "s_xor_b32 s59, s59, 4\n" \
  "s_cmp_lg_u32 s68, 0\n" \
  "s_cbranch_scc1 .LCX_TEST_%=\n" \
  "s_mov_b32 s61, 0\n" \
  "s_waitcnt lgkmcnt(0)\n" \
  "v_readfirstlane_b32 s63, v144\n" \
  "v_lshlrev_b32 v144, 5, v5\n" \
  "s_and_b32 s63, s63, 7\n" \
  "s_cmp_eq_u32 s63, 7\n" \
  "s_cbranch_scc0 .LCX_W2_%=\n" \
  "s_mov_b64 exec, 15\n" \
  "global_store_dword v144, v145, s[52:53] sc1\n" \
  "s_mov_b64 exec, -1\n" \
  ".LCX_W2_%=:\n" \
  "s_add_u32 s52, s52, 2048\n" \
  "s_addc_u32 s53, s53, 0\n" \
  ".LCX_NP_%=:\n" \
  "s_cmp_lg_u32 s68, 0\n" \
  "s_cbranch_scc1 .LCX_TEST_%=\n" \
  ".LCX_WF_%=:\n" \
  "s_memrealtime s[38:39]\n" \
  "s_waitcnt lgkmcnt(0)\n" \
  "s_sub_u32 s63, s69, s38\n" \
  "s_cmp_le_i32 s63, 0\n" \
  "s_cbranch_scc1 .LCX_GO_%=\n" \
  "s_cmp_lt_i32 s63, 12\n" \
  "s_cbranch_scc1 .LCX_WF_%=\n" \
  "s_sleep 3\n" \
  "s_branch .LCX_WF_%=\n" \
  ".LCX_GO_%=:\n" \
  "v_mov_b32 v35, 0\n" \
  "global_load_dwordx4 v[128:131], v5, s[48:49] offset:0 sc1\n" \
  "global_load_dwordx4 v[132:135], v5, s[48:49] offset:1024 sc1\n" \
  "global_load_dwordx4 v[136:139], v3, s[48:49] offset:0 sc1\n" \
  "global_load_dwordx4 v[140:143], v3, s[48:49] offset:512 sc1\n" \
  "global_load_dword v35, v35, s[50:51] sc1\n" \
  "s_sub_u32 s62, s62, 8\n" \
  "s_cbranch_scc1 .LCX_END_%=\n" \
  "s_waitcnt vmcnt(0)\n" \
  ".LCX_TEST_%=:\n" \
  "v_smfmac_f32_16x16x64_bf16 v[28:31], v[128:131], v[48:55], v1\n" \
  "v_smfmac_f32_16x16x64_bf16 v[28:31], v[132:135], v[56:63], v1\n" \
  "v_min3_u32 v33, v128, v129, v130\n" \
  "v_min3_u32 v36, v131, v132, v133\n" \
  "v_min3_u32 v33, v33, v134, v135\n" \
  "v_min3_u32 v36, v36, v136, v137\n" \
  "v_min3_u32 v33, v33, v138, v139\n" \
  "v_min3_u32 v36, v36, v140, v141\n" \
  "v_min3_u32 v33, v33, v142, v143\n" \
  "v_min_u32 v33, v33, v36\n" \
  "v_cmp_eq_u32 vcc, 0, v33\n" \
  "v_readfirstlane_b32 s64, v35\n" \
  "s_mov_b32 s68, 0\n" \
  "s_cmp_eq_u64 vcc, 0\n" \
  "s_cbranch_scc0 .LCX_FAIL_%=\n" \
  "v_permlane32_swap_b32 v28, v30\n" \
  "v_permlane32_swap_b32 v29, v31\n" \
  "v_mov_b32 v33, s67\n" \
  "ds_write_b32 v38, v33 offset:16\n" \
  "v_add_f32 v28, v28, v30\n" \
  "v_add_f32 v29, v29, v31\n" \
  "v_max3_f32 v36, v28, v29, v32\n" \
  "v_mov_b32 v33, v36\n" \
  "s_cmp_eq_u32 s64, 0\n" \
  "s_cselect_b32 s64, s38, s64\n" \
  "v_permlane16_swap_b32 v36, v33\n" \
  "v_max_f32 v36, v36, v33\n" \
  "v_mov_b32 v33, v36\n" \
  "s_sub_u32 s63, s64, s70\n" \
  "s_mov_b32 s70, s64\n" \
  "v_permlane32_swap_b32 v36, v33\n" \
  "v_max_f32 v36, v36, v33\n" \
  "v_mul_f32 v36, s72, v36\n" \
  "v_fma_f32 v32, v32, s71, v36\n" \
  "v_fma_f32 v28, v28, s71, v36\n" \
  "v_fma_f32 v29, v29, s71, v36\n" \
  "v_exp_f32 v32, v32\n" \
  "v_exp_f32 v28, v28\n" \
  "v_exp_f32 v29, v29\n" \
  "v_fma_f32 v34, v34, v32, v28\n" \
  "v_add_f32 v34, v34, v29\n" \
  "v_mov_b32 v33, v34\n" \
  "s_min_u32 s63, s63, 250\n" \
  "s_add_u32 s69, s64, s63\n" \
  "v_permlane16_swap_b32 v34, v33\n" \
  "v_add_f32 v34, v34, v33\n" \
  "v_mov_b32 v33, v34\n" \
  "s_add_u32 s69, s69, -48\n" \
  "s_add_u32 s48, s48, 0x10000\n" \
  "s_addc_u32 s49, s49, 0\n" \
  "v_permlane32_swap_b32 v34, v33\n" \
  "v_add_f32 v34, v34, v33\n" \
  "v_rcp_f32 v34, v34\n" \
  "s_add_u32 s50, s50, 1024\n" \
  "s_addc_u32 s51, s51, 0\n" \
  "v_mul_f32 v32, v32, v34\n" \
  "v_mul_f32 v20, v20, v32\n" \
  "v_mul_f32 v21, v21, v32\n" \
  "v_mul_f32 v22, v22, v32\n" \
  "v_mul_f32 v23, v23, v32\n" \
  "v_mul_f32 v24, v24, v32\n" \
  "v_mul_f32 v25, v25, v32\n" \
  "v_mul_f32 v26, v26, v32\n" \
  "v_mul_f32 v27, v27, v32\n" \
  "v_cvt_pk_bf16_f32 v40, v20, v21\n" \
  "v_cvt_pk_bf16_f32 v41, v22, v23\n" \
  "v_cvt_pk_bf16_f32 v42, v24, v25\n" \
  "v_cvt_pk_bf16_f32 v43, v26, v27\n" \
  "v_mul_f32 v28, v28, v34\n" \
  "v_mul_f32 v29, v29, v34\n" \
  "v_mfma_f32_16x16x32_bf16 v[64:67], v[96:99], v[40:43], 0\n" \
  "v_cvt_pk_bf16_f32 v44, v28, v29\n" \
  "v_mov_b32 v45, 0\n" \
  "v_mfma_f32_16x16x32_bf16 v[68:71], v[100:103], v[40:43], 0\n" \
  "v_cvt_pk_bf16_f32 v47, v28, v29\n" \
  "v_mov_b32 v46, 0\n" \
  "v_mfma_f32_16x16x32_bf16 v[72:75], v[104:107], v[40:43], 0\n" \
  "v_permlane32_swap_b32 v44, v45\n" \
  "v_mfma_f32_16x16x32_bf16 v[76:79], v[108:111], v[40:43], 0\n" \
  "v_permlane32_swap_b32 v46, v47\n" \
  "v_mfma_f32_16x16x32_bf16 v[80:83], v[112:115], v[40:43], 0\n" \
  "v_xor_b32 v16, 4096, v7\n" \
  "v_mfma_f32_16x16x32_bf16 v[84:87], v[116:119], v[40:43], 0\n" \
  "v_xor_b32 v17, 8192, v7\n" \
  "v_mfma_f32_16x16x32_bf16 v[88:91], v[120:123], v[40:43], 0\n" \
  "v_xor_b32 v18, 12288, v7\n" \
  "v_mfma_f32_16x16x32_bf16 v[92:95], v[124:127], v[40:43], 0\n" \
  "s_cmp_eq_u32 s61, 0\n" \
  "s_cbranch_scc1 .LCX_NQ_%=\n" \
  "s_mov_b32 s61, 0\n" \
  "s_waitcnt lgkmcnt(0)\n" \
  "v_readfirstlane_b32 s63, v144\n" \
  "v_lshlrev_b32 v144, 5, v5\n" \
  "s_and_b32 s63, s63, 7\n" \
  "s_cmp_eq_u32 s63, 7\n" \
  "s_cbranch_scc0 .LCX_W3_%=\n" \
  "s_mov_b64 exec, 15\n" \
  "global_store_dword v144, v145, s[52:53] sc1\n" \
  "s_mov_b64 exec, -1\n" \
  ".LCX_W3_%=:\n" \
  "s_add_u32 s52, s52, 2048\n" \
  "s_addc_u32 s53, s53, 0\n" \
  ".LCX_NQ_%=:\n" \
  "s_memrealtime s[38:39]\n" \
  "ds_read_b32 v37, v38\n" \
  "v_mfma_f32_16x16x16_bf16 v[64:67], v[136:137], v[44:45], v[64:67]\n" \
  "v_mfma_f32_16x16x16_bf16 v[68:71], v[140:141], v[44:45], v[68:71]\n" \
  "v_mfma_f32_16x16x16_bf16 v[72:75], v[136:137], v[46:47], v[72:75]\n" \
  "v_mfma_f32_16x16x16_bf16 v[76:79], v[140:141], v[46:47], v[76:79]\n" \
  "v_mfma_f32_16x16x16_bf16 v[80:83], v[138:139], v[44:45], v[80:83]\n" \
  "v_mfma_f32_16x16x16_bf16 v[84:87], v[142:143], v[44:45], v[84:87]\n" \
  "v_mfma_f32_16x16x16_bf16 v[88:91], v[138:139], v[46:47], v[88:91]\n" \
  "v_mfma_f32_16x16x16_bf16 v[92:95], v[142:143], v[46:47], v[92:95]\n" \
  "s_nop 0\n" \
  "v_cvt_pk_bf16_f32 v160, v64, v65\n" \
  "v_cvt_pk_bf16_f32 v161, v66, v67\n" \
  "v_cvt_pk_bf16_f32 v162, v68, v69\n" \
  "v_cvt_pk_bf16_f32 v163, v70, v71\n" \
  "ds_write_b128 v7, v[160:163] offset:0\n" \
  "v_cvt_pk_bf16_f32 v164, v72, v73\n" \
  "v_cvt_pk_bf16_f32 v165, v74, v75\n" \
  "v_cvt_pk_bf16_f32 v166, v76, v77\n" \
  "v_cvt_pk_bf16_f32 v167, v78, v79\n" \
  "ds_write_b128 v7, v[164:167] offset:1024\n" \
  "v_cvt_pk_bf16_f32 v168, v80, v81\n" \
  "v_cvt_pk_bf16_f32 v169, v82, v83\n" \
  "v_cvt_pk_bf16_f32 v170, v84, v85\n" \
  "v_cvt_pk_bf16_f32 v171, v86, v87\n" \
  "ds_write_b128 v7, v[168:171] offset:2048\n" \
  "v_cvt_pk_bf16_f32 v172, v88, v89\n" \
  "v_cvt_pk_bf16_f32 v173, v90, v91\n" \
  "v_cvt_pk_bf16_f32 v174, v92, v93\n" \
  "v_cvt_pk_bf16_f32 v175, v94, v95\n" \
  "ds_write_b128 v7, v[172:175] offset:3072\n" \
  "v_xor_b32 v7, 0x4000, v7\n" \
  "v_mfma_f32_16x16x32_bf16 v[112:115], v[192:195], v[160:163], 0\n" \
  "v_mfma_f32_16x16x32_bf16 v[116:119], v[224:227], v[160:163], 0\n" \
  "s_waitcnt lgkmcnt(0)\n" \
  "s_barrier\n" \
  "ds_read_b128 v[64:67], v16 offset:0\n" \
  "ds_read_b128 v[68:71], v16 offset:1024\n" \
  "ds_read_b128 v[72:75], v16 offset:2048\n" \
  "ds_read_b128 v[76:79], v16 offset:3072\n" \
  "ds_read_b128 v[80:83], v17 offset:0\n" \
  "ds_read_b128 v[84:87], v17 offset:1024\n" \
  "ds_read_b128 v[88:91], v17 offset:2048\n" \
  "ds_read_b128 v[92:95], v17 offset:3072\n" \
  "ds_read_b128 v[96:99], v18 offset:0\n" \
  "ds_read_b128 v[100:103], v18 offset:1024\n" \
  "ds_read_b128 v[104:107], v18 offset:2048\n" \
  "ds_read_b128 v[108:111], v18 offset:3072\n" \
  "v_readfirstlane_b32 s73, v37\n" \
  "s_sub_u32 s63, s38, s69\n" \
  "s_bfe_u32 s64, s73, 0x80010\n" \
  "s_sub_u32 s64, s64, s67\n" \
  "s_or_b32 s63, s63, s64\n" \
  "s_bfe_u32 s64, s73, 0x80008\n" \
  "s_and_b32 s73, s73, 0xff\n" \
  "s_min_u32 s73, s73, s64\n" \
  "s_or_b32 s63, s63, s68\n" \
  "s_cmp_lt_i32 s63, 0\n" \
  "s_cbranch_scc1 .LCX_E4_%=\n" \
  "v_mov_b32 v35, 0\n" \
  "global_load_dwordx4 v[128:131], v5, s[48:49] offset:0 sc1\n" \
  "global_load_dwordx4 v[132:135], v5, s[48:49] offset:1024 sc1\n" \
  "global_load_dwordx4 v[136:139], v3, s[48:49] offset:0 sc1\n" \
  "global_load_dwordx4 v[140:143], v3, s[48:49] offset:512 sc1\n" \
  "global_load_dword v35, v35, s[50:51] sc1\n" \
  "v_mov_b32 v33, s67\n" \
  "ds_write_b32 v38, v33 offset:8\n" \
  "s_mov_b32 s68, -1\n" \
  ".LCX_E4_%=:\n" \
  "v_mfma_f32_16x16x32_bf16 v[112:115], v[196:199], v[164:167], v[112:115]\n" \
  "v_mfma_f32_16x16x32_bf16 v[116:119], v[228:231], v[164:167], v[116:119]\n" \
  "v_mfma_f32_16x16x32_bf16 v[112:115], v[200:203], v[168:171], v[112:115]\n" \
  "v_mfma_f32_16x16x32_bf16 v[116:119], v[232:235], v[168:171], v[116:119]\n" \
  "v_mfma_f32_16x16x32_bf16 v[112:115], v[204:207], v[172:175], v[112:115]\n" \
  "v_mfma_f32_16x16x32_bf16 v[116:119], v[236:239], v[172:175], v[116:119]\n" \
  "s_waitcnt lgkmcnt(11)\n" \
  "v_mfma_f32_16x16x32_bf16 v[120:123], v[192:195], v[64:67], 0\n" \
  "v_mfma_f32_16x16x32_bf16 v[124:127], v[224:227], v[64:67], 0\n" \
  "s_waitcnt lgkmcnt(10)\n" \
  "v_mfma_f32_16x16x32_bf16 v[120:123], v[196:199], v[68:71], v[120:123]\n" \
  "v_mfma_f32_16x16x32_bf16 v[124:127], v[228:231], v[68:71], v[124:127]\n" \
  "s_waitcnt lgkmcnt(9)\n" \
  "v_mfma_f32_16x16x32_bf16 v[120:123], v[200:203], v[72:75], v[120:123]\n" \
  "v_mfma_f32_16x16x32_bf16 v[124:127], v[232:235], v[72:75], v[124:127]\n" \
  "s_waitcnt lgkmcnt(8)\n" \
  "v_mfma_f32_16x16x32_bf16 v[120:123], v[204:207], v[76:79], v[120:123]\n" \
  "v_mfma_f32_16x16x32_bf16 v[124:127], v[236:239], v[76:79], v[124:127]\n" \
  "s_waitcnt lgkmcnt(7)\n" \
  "v_mfma_f32_16x16x32_bf16 v[112:115], v[208:211], v[80:83], v[112:115]\n" \
  "v_mfma_f32_16x16x32_bf16 v[116:119], v[240:243], v[80:83], v[116:119]\n" \
  "s_waitcnt lgkmcnt(6)\n" \
  "v_mfma_f32_16x16x32_bf16 v[112:115], v[212:215], v[84:87], v[112:115]\n" \
  "v_mfma_f32_16x16x32_bf16 v[116:119], v[244:247], v[84:87], v[116:119]\n" \
  "s_waitcnt lgkmcnt(5)\n" \
  "v_mfma_f32_16x16x32_bf16 v[112:115], v[216:219], v[88:91], v[112:115]\n" \
  "v_mfma_f32_16x16x32_bf16 v[116:119], v[248:251], v[88:91], v[116:119]\n" \
  "s_waitcnt lgkmcnt(4)\n" \
  "v_mfma_f32_16x16x32_bf16 v[112:115], v[220:223], v[92:95], v[112:115]\n" \
  "v_mfma_f32_16x16x32_bf16 v[116:119], v[252:255], v[92:95], v[116:119]\n" \
  "s_memrealtime s[38:39]\n" \
  "ds_read_b32 v37, v38\n" \
  "s_waitcnt lgkmcnt(4)\n" \
  "v_mfma_f32_16x16x32_bf16 v[120:123], v[208:211], v[96:99], v[120:123]\n" \
  "v_mfma_f32_16x16x32_bf16 v[124:127], v[240:243], v[96:99], v[124:127]\n" \
  "s_waitcnt lgkmcnt(3)\n" \
  "v_mfma_f32_16x16x32_bf16 v[120:123], v[212:215], v[100:103], v[120:123]\n" \
  "v_mfma_f32_16x16x32_bf16 v[124:127], v[244:247], v[100:103], v[124:127]\n" \
  "v_cvt_pk_bf16_f32 v20, v112, v113\n" \
  "v_cvt_pk_bf16_f32 v21, v114, v115\n" \
  "v_cvt_pk_bf16_f32 v22, v116, v117\n" \
  "v_cvt_pk_bf16_f32 v23, v118, v119\n" \
  "s_waitcnt lgkmcnt(2)\n" \
  "v_mfma_f32_16x16x32_bf16 v[120:123], v[216:219], v[104:107], v[120:123]\n" \
  "v_mfma_f32_16x16x32_bf16 v[124:127], v[248:251], v[104:107], v[124:127]\n" \
  "buffer_store_dwordx4 v[20:23], v4, s[40:43], s54 offen sc1\n" \
  "s_waitcnt lgkmcnt(0)\n" \
  "v_mfma_f32_16x16x32_bf16 v[120:123], v[220:223], v[108:111], v[120:123]\n" \
  "v_mfma_f32_16x16x32_bf16 v[124:127], v[252:255], v[108:111], v[124:127]\n" \
  "s_xor_b32 s64, s54, 16384\n" \
  "s_mov_b32 s61, 1\n" \
  "s_nop 3\n" \
  "v_cvt_pk_bf16_f32 v24, v120, v121\n" \
  "v_cvt_pk_bf16_f32 v25, v122, v123\n" \
  "v_cvt_pk_bf16_f32 v26, v124, v125\n" \
  "v_cvt_pk_bf16_f32 v27, v126, v127\n" \
  "buffer_store_dwordx4 v[24:27], v4, s[40:43], s64 offen sc1\n" \
  "v_readfirstlane_b32 s73, v37\n" \
  "s_sub_u32 s63, s38, s69\n" \
  "s_bfe_u32 s64, s73, 0x80010\n" \
  "s_sub_u32 s64, s64, s67\n" \
  "s_or_b32 s63, s63, s64\n" \
  "s_bfe_u32 s64, s73, 0x80008\n" \
  "s_and_b32 s73, s73, 0xff\n" \
  "s_min_u32 s73, s73, s64\n" \
  "s_or_b32 s63, s63, s68\n" \
  "s_cmp_lt_i32 s63, 0\n" \
  "s_cbranch_scc1 .LCX_E5_%=\n" \
  "v_mov_b32 v35, 0\n" \
  "global_load_dwordx4 v[128:131], v5, s[48:49] offset:0 sc1\n" \
  "global_load_dwordx4 v[132:135], v5, s[48:49] offset:1024 sc1\n" \
  "global_load_dwordx4 v[136:139], v3, s[48:49] offset:0 sc1\n" \
  "global_load_dwordx4 v[140:143], v3, s[48:49] offset:512 sc1\n" \
  "global_load_dword v35, v35, s[50:51] sc1\n" \
  "v_mov_b32 v33, s67\n" \
  "ds_write_b32 v38, v33 offset:8\n" \
  "s_mov_b32 s68, -1\n" \
  ".LCX_E5_%=:\n" \
  "s_add_u32 s54, s54, 0x80000\n" \
  "s_sub_u32 s60, s60, 1\n" \
  "s_cmp_lg_u32 s60, 0\n" \
  "s_cbranch_scc1 .LCX_TOP_%=\n" \
  "s_waitcnt vmcnt(0)\n" \
  "v_mov_b32 v144, s59\n" \
  "v_mov_b32 v145, 1\n" \
  "s_mov_b64 exec, 1\n" \
  "ds_add_rtn_u32 v144, v144, v145\n" \
  "s_mov_b64 exec, -1\n" \
  "s_xor_b32 s59, s59, 4\n" \
  "s_mov_b32 s61, 0\n" \
  "s_waitcnt lgkmcnt(0)\n" \
  "v_readfirstlane_b32 s63, v144\n" \
  "v_lshlrev_b32 v144, 5, v5\n" \
  "s_and_b32 s63, s63, 7\n" \
  "s_cmp_eq_u32 s63, 7\n" \
  "s_cbranch_scc0 .LCX_W6_%=\n" \
  "s_mov_b64 exec, 15\n" \
  "global_store_dword v144, v145, s[52:53] sc1\n" \
  "s_mov_b64 exec, -1\n" \
  ".LCX_W6_%=:\n" \
  "s_add_u32 s52, s52, 2048\n" \
  "s_addc_u32 s53, s53, 0\n" \
  ".LCX_END_%=:\n" \
  "s_endpgm\n" \
  ".LCX_FAIL_%=:\n" \
  "v_mov_b64 v[28:29], 0\n" \
  "v_mov_b64 v[30:31], 0\n" \
  "s_cmp_eq_u32 s61, 0\n" \
  "s_cbranch_scc1 .LCX_FAIL2_%=\n" \
  "s_mov_b32 s61, 0\n" \
  "s_waitcnt lgkmcnt(0)\n" \
  "v_readfirstlane_b32 s63, v144\n" \
  "v_lshlrev_b32 v144, 5, v5\n" \
  "s_and_b32 s63, s63, 7\n" \
  "s_cmp_eq_u32 s63, 7\n" \
  "s_cbranch_scc0 .LCX_W7_%=\n" \
  "s_mov_b64 exec, 15\n" \
  "global_store_dword v144, v145, s[52:53] sc1\n" \
  "s_mov_b64 exec, -1\n" \
  ".LCX_W7_%=:\n" \
  "s_add_u32 s52, s52, 2048\n" \
  "s_addc_u32 s53, s53, 0\n" \
  ".LCX_FAIL2_%=:\n" \
  "s_add_u32 s69, s64, -48\n" \
  "s_cmp_lg_u32 s64, 0\n" \
  "s_cbranch_scc1 .LCX_WF_%=\n" \
  "s_sleep 4\n" \
  "global_load_dword v35, v35, s[50:51] sc1\n" \
  "s_sub_u32 s62, s62, 8\n" \
  "s_cbranch_scc1 .LCX_END_%=\n" \
  "s_waitcnt vmcnt(0)\n" \
  "v_readfirstlane_b32 s64, v35\n" \
  "s_branch .LCX_FAIL2_%=\n"
#define AW_ASM_Y \
  "s_mov_b32 s72, 0\n" \
  "s_mov_b32 s67, 0\n" \
  "s_mov_b32 s68, 2\n" \
  "s_mov_b32 s69, 0\n" \
  "s_mov_b32 s65, 49152\n" \
  "s_mov_b32 s66, 16384\n" \
  "v_mov_b32 v5, s58\n" \
  "v_mov_b32 v60, v3\n" \
  "v_add_u32 v3, 4096, v2\n" \
  "s_mov_b32 s62, 0x40000\n" \
  "s_branch .LCY_ASK_%=\n" \
  ".p2align 6\n" \
  ".LCY_BAR_%=:\n" \
  "s_barrier\n" \
  "ds_read_b128 v[8:11], v0 offset:0\n" \
  "ds_read_b128 v[12:15], v0 offset:1024\n" \
  "ds_read_b128 v[16:19], v0 offset:2048\n" \
  "s_add_u32 s68, s68, 1\n" \
  "s_add_u32 s64, s54, 16384\n" \
  "s_cmp_eq_u32 s69, 0\n" \
  "s_cbranch_scc1 .LCY_NI_%=\n" \
  "s_waitcnt vmcnt(3)\n" \
  "s_cmp_lg_u32 s72, 0\n" \
  "s_cbranch_scc1 .LCY_NJ_%=\n" \
  "s_waitcnt vmcnt(0)\n" \
  ".LCY_NJ_%=:\n" \
  "v_mov_b32 v6, s55\n" \
  "s_mov_b64 exec, 1\n" \
  "ds_add_u32 v5, v6\n" \
  "s_mov_b64 exec, -1\n" \
  "s_mov_b32 s69, 0\n" \
  ".LCY_NI_%=:\n" \
  "v_cmp_eq_u32 vcc, 0, v4\n" \
  "s_cmp_eq_u64 vcc, 0\n" \
  "s_cbranch_scc0 .LCY_NS_%=\n" \
  "v_lshlrev_b32 v6, 16, s55\n" \
  "s_mov_b64 exec, 1\n" \
  "ds_add_u32 v5, v6\n" \
  "s_mov_b64 exec, -1\n" \
  "s_mov_b32 m0, s56\n" \
  "s_add_u32 s71, s56, 4096\n" \
  "global_load_lds_dwordx4 v2, s[48:49] offset:0 sc1\n" \
  "global_load_lds_dwordx4 v2, s[48:49] offset:1024 sc1\n" \
  "global_load_lds_dwordx4 v2, s[48:49] offset:2048 sc1\n" \
  "global_load_lds_dwordx4 v2, s[48:49] offset:3072 sc1\n" \
  "s_mov_b32 m0, s71\n" \
  "s_add_u32 s56, s56, s65\n" \
  "global_load_lds_dwordx4 v3, s[48:49] offset:0 sc1\n" \
  "global_load_lds_dwordx4 v3, s[48:49] offset:1024 sc1\n" \
  ".LCY_NS_%=:\n" \
  "s_cmp_eq_u32 s72, 0\n" \
  "s_cbranch_scc1 .LCY_NK_%=\n" \
  "s_waitcnt vmcnt(6)\n" \
  "s_mov_b32 s72, 0\n" \
  "s_cbranch_vccz .LCY_NL_%=\n" \
  "s_waitcnt vmcnt(0)\n" \
  ".LCY_NL_%=:\n" \
  "v_mov_b32 v6, s59\n" \
  "v_mov_b32 v7, 1\n" \
  "s_mov_b64 exec, 1\n" \
  "ds_add_rtn_u32 v6, v6, v7\n" \
  "s_mov_b64 exec, -1\n" \
  "s_xor_b32 s59, s59, 4\n" \
  "s_mov_b32 s61, 0\n" \
  "s_waitcnt lgkmcnt(0)\n" \
  "v_readfirstlane_b32 s63, v6\n" \
  "v_lshlrev_b32 v6, 5, v2\n" \
  "s_and_b32 s63, s63, 7\n" \
  "s_cmp_eq_u32 s63, 7\n" \
  "s_cbranch_scc0 .LCY_W1_%=\n" \
  "s_mov_b64 exec, 15\n" \
  "global_store_dword v6, v7, s[52:53] sc1\n" \
  "s_mov_b64 exec, -1\n" \
  ".LCY_W1_%=:\n" \
  "s_add_u32 s52, s52, 2048\n" \
  "s_addc_u32 s53, s53, 0\n" \
  ".LCY_NK_%=:\n" \
  "ds_read_b128 v[20:23], v0 offset:3072\n" \
  "s_waitcnt lgkmcnt(3)\n" \
  "v_mfma_f32_16x16x32_bf16 v[32:35], v[64:67], v[8:11], 0\n" \
  "v_mfma_f32_16x16x32_bf16 v[36:39], v[96:99], v[8:11], 0\n" \
  "v_mfma_f32_16x16x32_bf16 v[40:43], v[128:131], v[8:11], 0\n" \
  "v_mfma_f32_16x16x32_bf16 v[44:47], v[160:163], v[8:11], 0\n" \
  "v_mfma_f32_16x16x32_bf16 v[48:51], v[192:195], v[8:11], 0\n" \
  "v_mfma_f32_16x16x32_bf16 v[52:55], v[224:227], v[8:11], 0\n" \
  "ds_read_b128 v[8:11], v0 offset:8192\n" \
  "s_waitcnt lgkmcnt(3)\n" \
  "v_mfma_f32_16x16x32_bf16 v[32:35], v[68:71], v[12:15], v[32:35]\n" \
  "v_mfma_f32_16x16x32_bf16 v[36:39], v[100:103], v[12:15], v[36:39]\n" \
  "v_mfma_f32_16x16x32_bf16 v[40:43], v[132:135], v[12:15], v[40:43]\n" \
  "v_mfma_f32_16x16x32_bf16 v[44:47], v[164:167], v[12:15], v[44:47]\n" \
  "v_mfma_f32_16x16x32_bf16 v[48:51], v[196:199], v[12:15], v[48:51]\n" \
  "v_mfma_f32_16x16x32_bf16 v[52:55], v[228:231], v[12:15], v[52:55]\n" \
  "s_cbranch_vccnz .LCY_D2_%=\n" \
  "global_load_lds_dwordx4 v3, s[48:49] offset:2048 sc1\n" \
  ".LCY_D2_%=:\n" \
  "ds_read_b128 v[12:15], v0 offset:9216\n" \
  "s_waitcnt lgkmcnt(3)\n" \
  "v_mfma_f32_16x16x32_bf16 v[32:35], v[72:75], v[16:19], v[32:35]\n" \
  "v_mfma_f32_16x16x32_bf16 v[36:39], v[104:107], v[16:19], v[36:39]\n" \
  "v_mfma_f32_16x16x32_bf16 v[40:43], v[136:139], v[16:19], v[40:43]\n" \
  "v_mfma_f32_16x16x32_bf16 v[44:47], v[168:171], v[16:19], v[44:47]\n" \
  "v_mfma_f32_16x16x32_bf16 v[48:51], v[200:203], v[16:19], v[48:51]\n" \
  "v_mfma_f32_16x16x32_bf16 v[52:55], v[232:235], v[16:19], v[52:55]\n" \
  "s_cbranch_vccnz .LCY_D3_%=\n" \
  "global_load_lds_dwordx4 v3, s[48:49] offset:3072 sc1\n" \
  "s_add_u32 s48, s48, 0x80000\n" \
  "s_addc_u32 s49, s49, 0\n" \
  ".LCY_D3_%=:\n" \
  "ds_read_b128 v[16:19], v0 offset:10240\n" \
  "s_waitcnt lgkmcnt(3)\n" \
  "v_mfma_f32_16x16x32_bf16 v[32:35], v[76:79], v[20:23], v[32:35]\n" \
  "v_mfma_f32_16x16x32_bf16 v[36:39], v[108:111], v[20:23], v[36:39]\n" \
  "v_mfma_f32_16x16x32_bf16 v[40:43], v[140:143], v[20:23], v[40:43]\n" \
  "v_mfma_f32_16x16x32_bf16 v[44:47], v[172:175], v[20:23], v[44:47]\n" \
  "v_mfma_f32_16x16x32_bf16 v[48:51], v[204:207], v[20:23], v[48:51]\n" \
  "v_mfma_f32_16x16x32_bf16 v[52:55], v[236:239], v[20:23], v[52:55]\n" \
  "s_cbranch_vccnz .LCY_D4_%=\n" \
  "s_mov_b32 m0, s57\n" \
  "s_add_u32 s57, s57, s65\n" \
  "global_load_lds_dwordx4 v2, s[50:51] offset:0 sc1\n" \
  ".LCY_D4_%=:\n" \
  "ds_read_b128 v[20:23], v0 offset:11264\n" \
  "s_waitcnt lgkmcnt(3)\n" \
  "v_mfma_f32_16x16x32_bf16 v[32:35], v[80:83], v[8:11], v[32:35]\n" \
  "v_mfma_f32_16x16x32_bf16 v[36:39], v[112:115], v[8:11], v[36:39]\n" \
  "v_mfma_f32_16x16x32_bf16 v[40:43], v[144:147], v[8:11], v[40:43]\n" \
  "v_mfma_f32_16x16x32_bf16 v[44:47], v[176:179], v[8:11], v[44:47]\n" \
  "v_mfma_f32_16x16x32_bf16 v[48:51], v[208:211], v[8:11], v[48:51]\n" \
  "v_mfma_f32_16x16x32_bf16 v[52:55], v[240:243], v[8:11], v[52:55]\n" \
  "s_cbranch_vccnz .LCY_D5_%=\n" \
  "global_load_lds_dwordx4 v2, s[50:51] offset:1024 sc1\n" \
  "s_sub_u32 s65, 0, s65\n" \
  "s_add_u32 s67, s67, 4096\n" \
  ".LCY_D5_%=:\n" \
  "ds_read_b128 v[8:11], v0 offset:4096\n" \
  "s_waitcnt lgkmcnt(3)\n" \
  "v_mfma_f32_16x16x32_bf16 v[32:35], v[84:87], v[12:15], v[32:35]\n" \
  "v_mfma_f32_16x16x32_bf16 v[36:39], v[116:119], v[12:15], v[36:39]\n" \
  "v_mfma_f32_16x16x32_bf16 v[40:43], v[148:151], v[12:15], v[40:43]\n" \
  "v_mfma_f32_16x16x32_bf16 v[44:47], v[180:183], v[12:15], v[44:47]\n" \
  "v_mfma_f32_16x16x32_bf16 v[48:51], v[212:215], v[12:15], v[48:51]\n" \
  "v_mfma_f32_16x16x32_bf16 v[52:55], v[244:247], v[12:15], v[52:55]\n" \
  "s_cbranch_vccnz .LCY_D6_%=\n" \
  "global_load_lds_dwordx4 v2, s[50:51] offset:2048 sc1\n" \
  "s_sub_u32 s68, s68, 1\n" \
  "s_mov_b32 s69, 1\n" \
  ".LCY_D6_%=:\n" \
  "ds_read_b128 v[12:15], v0 offset:5120\n" \
  "s_waitcnt lgkmcnt(3)\n" \
  "v_mfma_f32_16x16x32_bf16 v[32:35], v[88:91], v[16:19], v[32:35]\n" \
  "v_mfma_f32_16x16x32_bf16 v[36:39], v[120:123], v[16:19], v[36:39]\n" \
  "v_mfma_f32_16x16x32_bf16 v[40:43], v[152:155], v[16:19], v[40:43]\n" \
  "v_mfma_f32_16x16x32_bf16 v[44:47], v[184:187], v[16:19], v[44:47]\n" \
  "v_mfma_f32_16x16x32_bf16 v[48:51], v[216:219], v[16:19], v[48:51]\n" \
  "v_mfma_f32_16x16x32_bf16 v[52:55], v[248:251], v[16:19], v[52:55]\n" \
  "s_cbranch_vccnz .LCY_D7_%=\n" \
  "global_load_lds_dwordx4 v2, s[50:51] offset:3072 sc1\n" \
  "s_add_u32 s50, s50, 0x40000\n" \
  "s_addc_u32 s51, s51, 0\n" \
  ".LCY_D7_%=:\n" \
  "ds_read_b128 v[16:19], v0 offset:6144\n" \
  "s_waitcnt lgkmcnt(3)\n" \
  "v_mfma_f32_16x16x32_bf16 v[32:35], v[92:95], v[20:23], v[32:35]\n" \
  "v_mfma_f32_16x16x32_bf16 v[36:39], v[124:127], v[20:23], v[36:39]\n" \
  "v_mfma_f32_16x16x32_bf16 v[40:43], v[156:159], v[20:23], v[40:43]\n" \
  "v_mfma_f32_16x16x32_bf16 v[44:47], v[188:191], v[20:23], v[44:47]\n" \
  "v_mfma_f32_16x16x32_bf16 v[48:51], v[220:223], v[20:23], v[48:51]\n" \
  "v_mfma_f32_16x16x32_bf16 v[52:55], v[252:255], v[20:23], v[52:55]\n" \
  "ds_read_b128 v[20:23], v0 offset:7168\n" \
  "s_waitcnt lgkmcnt(3)\n" \
  "s_nop 1\n" \
  "v_cvt_pk_bf16_f32 v24, v32, v33\n" \
  "v_cvt_pk_bf16_f32 v25, v34, v35\n" \
  "v_cvt_pk_bf16_f32 v26, v36, v37\n" \
  "v_cvt_pk_bf16_f32 v27, v38, v39\n" \
  "s_nop 0\n" \
  "v_mfma_f32_16x16x32_bf16 v[32:35], v[64:67], v[8:11], 0\n" \
  "v_mfma_f32_16x16x32_bf16 v[36:39], v[96:99], v[8:11], 0\n" \
  "buffer_store_dwordx4 v[24:27], v1, s[40:43], s54 offen offset:0 sc1\n" \
  "v_cvt_pk_bf16_f32 v28, v40, v41\n" \
  "v_cvt_pk_bf16_f32 v29, v42, v43\n" \
  "v_cvt_pk_bf16_f32 v30, v44, v45\n" \
  "v_cvt_pk_bf16_f32 v31, v46, v47\n" \
  "s_nop 0\n" \
  "v_mfma_f32_16x16x32_bf16 v[40:43], v[128:131], v[8:11], 0\n" \
  "v_mfma_f32_16x16x32_bf16 v[44:47], v[160:163], v[8:11], 0\n" \
  "buffer_store_dwordx4 v[28:31], v1, s[40:43], s54 offen offset:128 sc1\n" \
  "v_cvt_pk_bf16_f32 v56, v48, v49\n" \
  "v_cvt_pk_bf16_f32 v57, v50, v51\n" \
  "v_cvt_pk_bf16_f32 v58, v52, v53\n" \
  "v_cvt_pk_bf16_f32 v59, v54, v55\n" \
  "s_nop 0\n" \
  "v_mfma_f32_16x16x32_bf16 v[48:51], v[192:195], v[8:11], 0\n" \
  "v_mfma_f32_16x16x32_bf16 v[52:55], v[224:227], v[8:11], 0\n" \
  "buffer_store_dwordx4 v[56:59], v1, s[40:43], s54 offen offset:256 sc1\n" \
  "ds_read_b128 v[8:11], v0 offset:12288\n" \
  "s_waitcnt lgkmcnt(3)\n" \
  "v_mfma_f32_16x16x32_bf16 v[32:35], v[68:71], v[12:15], v[32:35]\n" \
  "v_mfma_f32_16x16x32_bf16 v[36:39], v[100:103], v[12:15], v[36:39]\n" \
  "v_mfma_f32_16x16x32_bf16 v[40:43], v[132:135], v[12:15], v[40:43]\n" \
  "v_mfma_f32_16x16x32_bf16 v[44:47], v[164:167], v[12:15], v[44:47]\n" \
  "v_mfma_f32_16x16x32_bf16 v[48:51], v[196:199], v[12:15], v[48:51]\n" \
  "v_mfma_f32_16x16x32_bf16 v[52:55], v[228:231], v[12:15], v[52:55]\n" \
  "ds_read_b128 v[12:15], v0 offset:13312\n" \
  "s_waitcnt lgkmcnt(3)\n" \
  "v_mfma_f32_16x16x32_bf16 v[32:35], v[72:75], v[16:19], v[32:35]\n" \
  "v_mfma_f32_16x16x32_bf16 v[36:39], v[104:107], v[16:19], v[36:39]\n" \
  "v_mfma_f32_16x16x32_bf16 v[40:43], v[136:139], v[16:19], v[40:43]\n" \
  "v_mfma_f32_16x16x32_bf16 v[44:47], v[168:171], v[16:19], v[44:47]\n" \
  "v_mfma_f32_16x16x32_bf16 v[48:51], v[200:203], v[16:19], v[48:51]\n" \
  "v_mfma_f32_16x16x32_bf16 v[52:55], v[232:235], v[16:19], v[52:55]\n" \
  "ds_read_b128 v[16:19], v0 offset:14336\n" \
  "s_waitcnt lgkmcnt(3)\n" \
  "v_mfma_f32_16x16x32_bf16 v[32:35], v[76:79], v[20:23], v[32:35]\n" \
  "v_mfma_f32_16x16x32_bf16 v[36:39], v[108:111], v[20:23], v[36:39]\n" \
  "v_mfma_f32_16x16x32_bf16 v[40:43], v[140:143], v[20:23], v[40:43]\n" \
  "v_mfma_f32_16x16x32_bf16 v[44:47], v[172:175], v[20:23], v[44:47]\n" \
  "v_mfma_f32_16x16x32_bf16 v[48:51], v[204:207], v[20:23], v[48:51]\n" \
  "v_mfma_f32_16x16x32_bf16 v[52:55], v[236:239], v[20:23], v[52:55]\n" \
  "ds_read_b32 v7, v5 offset:8\n" \
  "ds_read_b128 v[20:23], v0 offset:15360\n" \
  "s_waitcnt lgkmcnt(4)\n" \
  "v_mfma_f32_16x16x32_bf16 v[32:35], v[80:83], v[8:11], v[32:35]\n" \
  "v_mfma_f32_16x16x32_bf16 v[36:39], v[112:115], v[8:11], v[36:39]\n" \
  "v_mfma_f32_16x16x32_bf16 v[40:43], v[144:147], v[8:11], v[40:43]\n" \
  "v_mfma_f32_16x16x32_bf16 v[44:47], v[176:179], v[8:11], v[44:47]\n" \
  "v_mfma_f32_16x16x32_bf16 v[48:51], v[208:211], v[8:11], v[48:51]\n" \
  "v_mfma_f32_16x16x32_bf16 v[52:55], v[240:243], v[8:11], v[52:55]\n" \
  "s_waitcnt lgkmcnt(3)\n" \
  "v_mfma_f32_16x16x32_bf16 v[32:35], v[84:87], v[12:15], v[32:35]\n" \
  "v_mfma_f32_16x16x32_bf16 v[36:39], v[116:119], v[12:15], v[36:39]\n" \
  "v_mfma_f32_16x16x32_bf16 v[40:43], v[148:151], v[12:15], v[40:43]\n" \
  "v_mfma_f32_16x16x32_bf16 v[44:47], v[180:183], v[12:15], v[44:47]\n" \
  "v_mfma_f32_16x16x32_bf16 v[48:51], v[212:215], v[12:15], v[48:51]\n" \
  "v_mfma_f32_16x16x32_bf16 v[52:55], v[244:247], v[12:15], v[52:55]\n" \
  "s_waitcnt lgkmcnt(2)\n" \
  "v_mfma_f32_16x16x32_bf16 v[32:35], v[88:91], v[16:19], v[32:35]\n" \
  "v_mfma_f32_16x16x32_bf16 v[36:39], v[120:123], v[16:19], v[36:39]\n" \
  "v_mfma_f32_16x16x32_bf16 v[40:43], v[152:155], v[16:19], v[40:43]\n" \
  "v_mfma_f32_16x16x32_bf16 v[44:47], v[184:187], v[16:19], v[44:47]\n" \
  "v_mfma_f32_16x16x32_bf16 v[48:51], v[216:219], v[16:19], v[48:51]\n" \
  "v_mfma_f32_16x16x32_bf16 v[52:55], v[248:251], v[16:19], v[52:55]\n" \
  "s_cbranch_vccnz .LCY_NT_%=\n" \
  "v_mov_b32 v4, 0\n" \
  "buffer_load_dword v4, v60, s[44:47], s67 offen sc1\n" \
  ".LCY_NT_%=:\n" \
  "s_waitcnt lgkmcnt(0)\n" \
  "v_mfma_f32_16x16x32_bf16 v[32:35], v[92:95], v[20:23], v[32:35]\n" \
  "v_mfma_f32_16x16x32_bf16 v[36:39], v[124:127], v[20:23], v[36:39]\n" \
  "v_mfma_f32_16x16x32_bf16 v[40:43], v[156:159], v[20:23], v[40:43]\n" \
  "v_mfma_f32_16x16x32_bf16 v[44:47], v[188:191], v[20:23], v[44:47]\n" \
  "v_mfma_f32_16x16x32_bf16 v[48:51], v[220:223], v[20:23], v[48:51]\n" \
  "v_mfma_f32_16x16x32_bf16 v[52:55], v[252:255], v[20:23], v[52:55]\n" \
  "v_add_u32 v0, s66, v0\n" \
  "s_sub_u32 s66, 0, s66\n" \
  "s_add_u32 s54, s54, 0x80000\n" \
  "s_mov_b32 s62, 0x40000\n" \
  "v_readfirstlane_b32 s63, v7\n" \
  "v_cvt_pk_bf16_f32 v24, v32, v33\n" \
  "v_cvt_pk_bf16_f32 v25, v34, v35\n" \
  "v_cvt_pk_bf16_f32 v26, v36, v37\n" \
  "v_cvt_pk_bf16_f32 v27, v38, v39\n" \
  "buffer_store_dwordx4 v[24:27], v1, s[40:43], s64 offen offset:0 sc1\n" \
  "v_cvt_pk_bf16_f32 v28, v40, v41\n" \
  "v_cvt_pk_bf16_f32 v29, v42, v43\n" \
  "v_cvt_pk_bf16_f32 v30, v44, v45\n" \
  "v_cvt_pk_bf16_f32 v31, v46, v47\n" \
  "buffer_store_dwordx4 v[28:31], v1, s[40:43], s64 offen offset:128 sc1\n" \
  "v_cvt_pk_bf16_f32 v56, v48, v49\n" \
  "v_cvt_pk_bf16_f32 v57, v50, v51\n" \
  "v_cvt_pk_bf16_f32 v58, v52, v53\n" \
  "v_cvt_pk_bf16_f32 v59, v54, v55\n" \
  "buffer_store_dwordx4 v[56:59], v1, s[40:43], s64 offen offset:256 sc1\n" \
  "s_cbranch_vccnz .LCY_ND_%=\n" \
  "s_add_u32 s63, s63, s60\n" \
  "s_cmp_eq_u32 s63, 66\n" \
  "s_cselect_b32 s72, 1, 0\n" \
  "s_cbranch_scc1 .LCY_DEF_%=\n" \
  "s_waitcnt vmcnt(0)\n" \
  "v_mov_b32 v6, s55\n" \
  "s_mov_b64 exec, 1\n" \
  "ds_add_u32 v5, v6\n" \
  "s_mov_b64 exec, -1\n" \
  "s_mov_b32 s69, 0\n" \
  "s_branch .LCY_WC_%=\n" \
  ".LCY_ND_%=:\n" \
  "v_mov_b32 v4, 0\n" \
  "buffer_load_dword v4, v60, s[44:47], s67 offen sc1\n" \
  "s_waitcnt vmcnt(0)\n" \
  ".LCY_WC_%=:\n" \
  "v_mov_b32 v6, s59\n" \
  "v_mov_b32 v7, 1\n" \
  "s_mov_b64 exec, 1\n" \
  "ds_add_rtn_u32 v6, v6, v7\n" \
  "s_mov_b64 exec, -1\n" \
  "s_xor_b32 s59, s59, 4\n" \
  "s_mov_b32 s61, 0\n" \
  "s_waitcnt lgkmcnt(0)\n" \
  "v_readfirstlane_b32 s63, v6\n" \
  "v_lshlrev_b32 v6, 5, v2\n" \
  "s_and_b32 s63, s63, 7\n" \
  "s_cmp_eq_u32 s63, 7\n" \
  "s_cbranch_scc0 .LCY_W8_%=\n" \
  "s_mov_b64 exec, 15\n" \
  "global_store_dword v6, v7, s[52:53] sc1\n" \
  "s_mov_b64 exec, -1\n" \
  ".LCY_W8_%=:\n" \
  "s_add_u32 s52, s52, 2048\n" \
  "s_addc_u32 s53, s53, 0\n" \
  ".LCY_DEF_%=:\n" \
  "s_sub_u32 s60, s60, 1\n" \
  "s_cmp_eq_u32 s60, 0\n" \
  "s_cbranch_scc1 .LCY_END_%=\n" \
  ".LCY_NEED_%=:\n" \
  "s_cmp_eq_u32 s68, 0\n" \
  "s_cbranch_scc1 .LCY_BAR_%=\n" \
  "v_cmp_eq_u32 vcc, 0, v4\n" \
  "s_cmp_eq_u64 vcc, 0\n" \
  "s_cbranch_scc1 .LCY_GET_%=\n" \
  "s_cmp_ge_u32 s67, 0x40000\n" \
  "s_cbranch_scc1 .LCY_BAR_%=\n" \
  "s_sleep 4\n" \
  "ds_read_b32 v6, v5 offset:16\n" \
  "s_waitcnt lgkmcnt(0)\n" \
  "v_readfirstlane_b32 s63, v6\n" \
  "s_add_u32 s63, s63, s60\n" \
  "s_cmp_ge_u32 s63, 66\n" \
  "s_cbranch_scc1 .LCY_BAR_%=\n" \
  ".LCY_ASK_%=:\n" \
  "v_mov_b32 v4, 0\n" \
  "buffer_load_dword v4, v60, s[44:47], s67 offen sc1\n" \
  "s_sub_u32 s62, s62, 1\n" \
  "s_cbranch_scc1 .LCY_END_%=\n" \
  "s_waitcnt vmcnt(0)\n" \
  "s_branch .LCY_NEED_%=\n" \
  ".LCY_GET_%=:\n" \
  "v_lshlrev_b32 v6, 16, s55\n" \
  "s_mov_b64 exec, 1\n" \
  "ds_add_u32 v5, v6\n" \
  "s_mov_b64 exec, -1\n" \
  "s_mov_b32 m0, s56\n" \
  "s_add_u32 s71, s56, 4096\n" \
  "global_load_lds_dwordx4 v2, s[48:49] offset:0 sc1\n" \
  "global_load_lds_dwordx4 v2, s[48:49] offset:1024 sc1\n" \
  "global_load_lds_dwordx4 v2, s[48:49] offset:2048 sc1\n" \
  "global_load_lds_dwordx4 v2, s[48:49] offset:3072 sc1\n" \
  "s_mov_b32 m0, s71\n" \
  "s_add_u32 s56, s56, s65\n" \
  "global_load_lds_dwordx4 v3, s[48:49] offset:0 sc1\n" \
  "global_load_lds_dwordx4 v3, s[48:49] offset:1024 sc1\n" \
  "global_load_lds_dwordx4 v3, s[48:49] offset:2048 sc1\n" \
  "global_load_lds_dwordx4 v3, s[48:49] offset:3072 sc1\n" \
  "s_add_u32 s48, s48, 0x80000\n" \
  "s_addc_u32 s49, s49, 0\n" \
  "s_mov_b32 m0, s57\n" \
  "s_add_u32 s57, s57, s65\n" \
  "global_load_lds_dwordx4 v2, s[50:51] offset:0 sc1\n" \
  "global_load_lds_dwordx4 v2, s[50:51] offset:1024 sc1\n" \
  "s_sub_u32 s65, 0, s65\n" \
  "s_add_u32 s67, s67, 4096\n" \
  "global_load_lds_dwordx4 v2, s[50:51] offset:2048 sc1\n" \
  "s_sub_u32 s68, s68, 1\n" \
  "s_mov_b32 s69, 1\n" \
  "global_load_lds_dwordx4 v2, s[50:51] offset:3072 sc1\n" \
  "s_add_u32 s50, s50, 0x40000\n" \
  "s_addc_u32 s51, s51, 0\n" \
  "v_mov_b32 v4, 0\n" \
  "buffer_load_dword v4, v60, s[44:47], s67 offen sc1\n" \
  "s_mov_b32 s61, 0\n" \
  "s_cmp_eq_u32 s68, 0\n" \
  "s_cbranch_scc1 .LCY_BAR_%=\n" \
  "s_waitcnt vmcnt(0)\n" \
  "v_mov_b32 v6, s55\n" \
  "s_mov_b64 exec, 1\n" \
  "ds_add_u32 v5, v6\n" \
  "s_mov_b64 exec, -1\n" \
  "s_mov_b32 s69, 0\n" \
  "s_branch .LCY_NEED_%=\n" \
  ".LCY_END_%=:\n" \
  "s_endpgm\n"
template <bool AT>
__device__ __forceinline__ void role_aw_t(const MegaArgs& A, char* smem, const int L, const int a, const u64 tag) {
  const int w = A.wv;
  const int l = lane_id(), c = l & 15, g = l >> 4;
  const int qt = w & 1, hh = (w >> 1) & 1, h = 2 * a + hh;
  char* ws = A.ws;
  unsigned* FL = (unsigned*)(ws + OFF_FL);
  const unsigned sb = lds_base(smem);
  if (w == 0 && l < 8) reinterpret_cast<unsigned*>(smem + AW_CNT)[l] = 0u;
  constexpr int NP = AT ? 1 : 3;
  const int cp0 = AT ? w : 3 * w - 8;
  v8u wo[2 * NP][4];
  {
    const int q = (l >> 2) & 3, p = l & 3, f = q | ((g & 1) << 2);
    const unsigned ra = sb + (4 * g + q) * 256 + (p & 1) * 16;
#pragma unroll
    for (int r = 0; r < 2; ++r) {
#pragma unroll
      for (int k = 0; k < 2; ++k) {
        const char* base_ = (const char*)A.Wo + ((size_t)((2 * a + r) * DH + 64 * (w & 1) + 32 * k) * DM + 128 * (w >> 1)) * 2;
#pragma unroll
        for (int i = 0; i < 8; ++i) {
          const int fd = g | ((i & 1) << 2);
          glds16(base_, (unsigned)((4 * i + g) * (DM * 2) + ((c ^ (fd << 1)) << 4)), sb + w * 16384 + k * 8192 + i * 1024);
        }
      }
      SST(4 + 3 * r);
      if (r == 1) ready_wait(A, tag, ready_look(A));
      WAIT_VM(0);
      RAW_BAR();
      SST(5 + 3 * r);
#pragma unroll
      for (int k2 = 0; k2 < 2; ++k2) {
#pragma unroll
        for (int ct = 0; ct < 2 * NP; ++ct) {
          const int cp = cp0 + (ct >> 1);
          const unsigned a_ = ra + (cp >> 2) * 32768 + k2 * 16384 + ((2 * (cp & 3) + (p >> 1)) ^ f) * 32 + (ct & 1) * 8;
          const v2u r0 = trd(a_), r1 = trd(a_ + 4096), r2 = trd(a_ + 8192), r3 = trd(a_ + 12288);
          wo[ct][2 * r + k2] = v8u{r0.x, r0.y, r1.x, r1.y, r2.x, r2.y, r3.x, r3.y};
          asm volatile("" : "+v"(wo[ct][2 * r + k2]));
        }
      }
      RAW_BAR();
      SST(6 + 3 * r);
    }
  }
  WSTAMP2();
  const v4u rsW = mk_srd(ws + OFF_PW, SZ_PW);
  const int dp = (FL_FP - FL_FQ + (L * 16 + a - L * 64 * 32 - h) * 32 + 2 * hh) * 4;
  const v4u rsF = mk_srd(FL + FL_FQ + (L * 64 * 32 + h) * 32, (qt ? 0 : dp) + 64 * 4096 - 128);
  const char* p0 = AT ? ws + OFF_MEMK + L * SZ_MEM + h * 2048 : ws + OFF_QK + L * SZ_QK + ((size_t)h * 16 + 8 * qt) * 1024;
  const char* p1 = AT ? (const char*)(FL + FL_FK + L * 32 + (h >> 3) * 64 + (h & 7) * 2) : ws + OFF_VF + L * SZ_O + ((size_t)h * 8 + 4 * qt) * 1024;
  const unsigned* pFW = FL + FL_FW + (L * 64 * 16 + (a >> 2)) * 32 + (a & 3);
  const unsigned tk = sb + AW_TK + hh * 24 * 1024, voff = (c >> 1) * 2048 + (c & 1) * 64 + g * 16;
  const char* dbg = ws + WS_TOTAL + A.vb * 64 * 8;
#define AW_SOPS                                                                                                                 \
  "{s[40:43]}"(rsW), "{s[44:47]}"(rsF), "{s[48:49]}"(p0), "{s[50:51]}"(p1), "{s[52:53]}"(pFW),                                   \
      "{s54}"((L * 64 * 16 + a) * 32768 + cp0 * 128 + (AT ? qt * 16384 : 0)), "{s55}"(1 << (8 * qt)), "{s56}"(tk + qt * 8192), "{s57}"(tk + 16384 + qt * 4096),                  \
      "{s58}"(sb + AW_CNT + 8 + 4 * hh), "{s59}"(sb + AW_CNT), "{s60}"(NSEG), "{s[36:37]}"(dbg)                                  \
      : "memory", "vcc", "scc", "s80"
  if constexpr (AT) {
    const v8u pv = {tk + l * 16, (c & 8) ? 0xEEEEu : 0x4444u, 0u, (unsigned)(OFF_MEMV - OFF_MEMK) + c * 32 + (g & 1) * 16 + (g >> 1) * 1024,
                    voff, (unsigned)l * 16, tk + qt * 4096 + l * 16, sb + AW_SO + w * 4096 + l * 16};
    const unsigned neg = 0xff61b1e6u;
    v8u bt;
#pragma unroll
    for (int i = 0; i < 4; ++i) {
      const unsigned d = 4 * g + i > c ? neg : 0u;
      bt[i] = qt ? 0u : d;
      bt[4 + i] = qt ? d : neg;
    }
    asm volatile("s_mov_b64 exec, -1\ns_waitcnt vmcnt(0) lgkmcnt(0)\ns_nop 4\n" AW_ASM_X ".LKG_LAST:\n"
                 ::"{v[192:199]}"(wo[0][0]), "{v[200:207]}"(wo[0][1]), "{v[208:215]}"(wo[0][2]), "{v[216:223]}"(wo[0][3]),
                 "{v[224:231]}"(wo[1][0]), "{v[232:239]}"(wo[1][1]), "{v[240:247]}"(wo[1][2]), "{v[248:255]}"(wo[1][3]),
                 "{v[0:7]}"(pv), "{v[8:15]}"(bt), AW_SOPS);
  } else {
    const v4u pv = {sb + AW_SO + l * 16, voff, (unsigned)l * 16, (l & 2) ? 16u + 8 * qt : qt ? 8u * (l & 1) : dp + 4 * (l & 1)};
    asm volatile("s_mov_b64 exec, -1\ns_waitcnt vmcnt(0) lgkmcnt(0)\ns_nop 4\n" AW_ASM_Y
                 ::"{v[64:71]}"(wo[0][0]), "{v[72:79]}"(wo[0][1]), "{v[80:87]}"(wo[0][2]), "{v[88:95]}"(wo[0][3]),
                 "{v[96:103]}"(wo[1][0]), "{v[104:111]}"(wo[1][1]), "{v[112:119]}"(wo[1][2]), "{v[120:127]}"(wo[1][3]),
                 "{v[128:135]}"(wo[2][0]), "{v[136:143]}"(wo[2][1]), "{v[144:151]}"(wo[2][2]), "{v[152:159]}"(wo[2][3]),
                 "{v[160:167]}"(wo[3][0]), "{v[168:175]}"(wo[3][1]), "{v[176:183]}"(wo[3][2]), "{v[184:191]}"(wo[3][3]),
                 "{v[192:199]}"(wo[4][0]), "{v[200:207]}"(wo[4][1]), "{v[208:215]}"(wo[4][2]), "{v[216:223]}"(wo[4][3]),
                 "{v[224:231]}"(wo[5][0]), "{v[232:239]}"(wo[5][1]), "{v[240:247]}"(wo[5][2]), "{v[248:255]}"(wo[5][3]),
                 "{v[0:3]}"(pv), AW_SOPS);
  }
#undef AW_SOPS
}
__device__ __forceinline__ void role_aw(const MegaArgs& A, char* smem, const int L, const int a, const u64 tag) {
  if (A.wv < 4) role_aw_t<true>(A, smem, L, a, tag);
  else role_aw_t<false>(A, smem, L, a, tag);
}

#define P_ASM \
  "s_mov_b32 s68, 0\n" \
  "s_mov_b32 s66, 3\n" \
  "s_mov_b32 s67, 2\n" \
  "s_mov_b32 s72, 0\n" \
  "v_mov_b32 v7, 1\n" \
  "v_mov_b32 v6, s70\n" \
  "v_mov_b32 v5, 0\n" \
  "v_add_u32 v10, 4096, v2\n" \
  ".p2align 6\n" \
  ".LCP_TOP_%=:\n" \
  "ds_read_b128 v[28:31], v1 offset:0\n" \
  "ds_read_b128 v[32:35], v1 offset:1024\n" \
  "s_add_u32 s66, s66, 3\n" \
  "s_add_u32 s67, s67, 2\n" \
  "s_mov_b32 s69, 0x400000\n" \
  "v_xor_b32 v0, 0x8000, v0\n" \
  "v_xor_b32 v3, 0x100, v3\n" \
  "v_readfirstlane_b32 s71, v5\n" \
  "s_cmp_ge_u32 s71, s66\n" \
  "s_cbranch_scc1 .LCP_GO_%=\n" \
  "s_cmp_eq_u32 s68, 1\n" \
  "s_cbranch_scc0 .LCP_X1_%=\n" \
  "s_waitcnt vmcnt(0)\n" \
  "s_mov_b64 exec, 1\n" \
  "buffer_store_dword v7, off, s[56:59], s72 sc1\n" \
  "s_mov_b64 exec, -1\n" \
  "s_add_u32 s72, s72, 0x1000\n" \
  "s_mov_b32 s68, 0\n" \
  ".LCP_X1_%=:\n" \
  ".LCP_S2_%=:\n" \
  "s_sleep 1\n" \
  "ds_read_b32 v5, v6 offset:0\n" \
  "s_sub_u32 s69, s69, 1\n" \
  "s_cbranch_scc1 .LCP_END_%=\n" \
  "s_waitcnt lgkmcnt(0)\n" \
  "v_readfirstlane_b32 s71, v5\n" \
  "s_cmp_ge_u32 s71, s66\n" \
  "s_cbranch_scc1 .LCP_GO_%=\n" \
  "s_branch .LCP_S2_%=\n" \
  ".LCP_GO_%=:\n" \
  "ds_read_b128 v[12:15], v0 offset:0\n" \
  "ds_read_b128 v[16:19], v0 offset:2048\n" \
  "ds_read_b128 v[20:23], v0 offset:4096\n" \
  "ds_read_b128 v[24:27], v0 offset:6144\n" \
  "ds_read_b128 v[36:39], v1 offset:2048\n" \
  "s_waitcnt lgkmcnt(4)\n" \
  "v_mfma_f32_16x16x32_bf16 v[48:51], v[64:67], v[12:15], 0\n" \
  "v_mfma_f32_16x16x32_bf16 v[52:55], v[128:131], v[12:15], 0\n" \
  "v_mfma_f32_16x16x32_bf16 v[56:59], v[192:195], v[12:15], 0\n" \
  "v_mfma_f32_16x16x32_bf16 v[60:63], v[28:31], v[12:15], 0\n" \
  "ds_read_b128 v[12:15], v0 offset:8192\n" \
  "ds_read_b128 v[28:31], v1 offset:3072\n" \
  "s_waitcnt lgkmcnt(5)\n" \
  "v_mfma_f32_16x16x32_bf16 v[48:51], v[68:71], v[16:19], v[48:51]\n" \
  "v_mfma_f32_16x16x32_bf16 v[52:55], v[132:135], v[16:19], v[52:55]\n" \
  "v_mfma_f32_16x16x32_bf16 v[56:59], v[196:199], v[16:19], v[56:59]\n" \
  "v_mfma_f32_16x16x32_bf16 v[60:63], v[32:35], v[16:19], v[60:63]\n" \
  "ds_read_b128 v[16:19], v0 offset:10240\n" \
  "ds_read_b128 v[32:35], v1 offset:4096\n" \
  "s_waitcnt lgkmcnt(6)\n" \
  "v_mfma_f32_16x16x32_bf16 v[48:51], v[72:75], v[20:23], v[48:51]\n" \
  "v_mfma_f32_16x16x32_bf16 v[52:55], v[136:139], v[20:23], v[52:55]\n" \
  "v_mfma_f32_16x16x32_bf16 v[56:59], v[200:203], v[20:23], v[56:59]\n" \
  "s_waitcnt lgkmcnt(4)\n" \
  "v_mfma_f32_16x16x32_bf16 v[60:63], v[36:39], v[20:23], v[60:63]\n" \
  "ds_read_b128 v[20:23], v0 offset:12288\n" \
  "ds_read_b128 v[36:39], v1 offset:5120\n" \
  "v_mfma_f32_16x16x32_bf16 v[48:51], v[76:79], v[24:27], v[48:51]\n" \
  "v_mfma_f32_16x16x32_bf16 v[52:55], v[140:143], v[24:27], v[52:55]\n" \
  "v_mfma_f32_16x16x32_bf16 v[56:59], v[204:207], v[24:27], v[56:59]\n" \
  "s_waitcnt lgkmcnt(4)\n" \
  "v_mfma_f32_16x16x32_bf16 v[60:63], v[28:31], v[24:27], v[60:63]\n" \
  "ds_read_b128 v[24:27], v0 offset:14336\n" \
  "ds_read_b128 v[28:31], v1 offset:6144\n" \
  "v_mfma_f32_16x16x32_bf16 v[48:51], v[80:83], v[12:15], v[48:51]\n" \
  "v_mfma_f32_16x16x32_bf16 v[52:55], v[144:147], v[12:15], v[52:55]\n" \
  "v_mfma_f32_16x16x32_bf16 v[56:59], v[208:211], v[12:15], v[56:59]\n" \
  "s_waitcnt lgkmcnt(4)\n" \
  "v_mfma_f32_16x16x32_bf16 v[60:63], v[32:35], v[12:15], v[60:63]\n" \
  "ds_read_b128 v[12:15], v0 offset:16384\n" \
  "ds_read_b128 v[32:35], v1 offset:7168\n" \
  "v_mfma_f32_16x16x32_bf16 v[48:51], v[84:87], v[16:19], v[48:51]\n" \
  "v_mfma_f32_16x16x32_bf16 v[52:55], v[148:151], v[16:19], v[52:55]\n" \
  "v_mfma_f32_16x16x32_bf16 v[56:59], v[212:215], v[16:19], v[56:59]\n" \
  "s_waitcnt lgkmcnt(4)\n" \
  "v_mfma_f32_16x16x32_bf16 v[60:63], v[36:39], v[16:19], v[60:63]\n" \
  "ds_read_b128 v[16:19], v0 offset:18432\n" \
  "ds_read_b128 v[36:39], v1 offset:8192\n" \
  "v_mfma_f32_16x16x32_bf16 v[48:51], v[88:91], v[20:23], v[48:51]\n" \
  "v_mfma_f32_16x16x32_bf16 v[52:55], v[152:155], v[20:23], v[52:55]\n" \
  "v_mfma_f32_16x16x32_bf16 v[56:59], v[216:219], v[20:23], v[56:59]\n" \
  "s_waitcnt lgkmcnt(4)\n" \
  "v_mfma_f32_16x16x32_bf16 v[60:63], v[28:31], v[20:23], v[60:63]\n" \
  "ds_read_b128 v[20:23], v0 offset:20480\n" \
  "ds_read_b128 v[28:31], v1 offset:9216\n" \
  "v_mfma_f32_16x16x32_bf16 v[48:51], v[92:95], v[24:27], v[48:51]\n" \
  "v_mfma_f32_16x16x32_bf16 v[52:55], v[156:159], v[24:27], v[52:55]\n" \
  "v_mfma_f32_16x16x32_bf16 v[56:59], v[220:223], v[24:27], v[56:59]\n" \
  "s_waitcnt lgkmcnt(4)\n" \
  "v_mfma_f32_16x16x32_bf16 v[60:63], v[32:35], v[24:27], v[60:63]\n" \
  "ds_read_b128 v[24:27], v0 offset:22528\n" \
  "ds_read_b128 v[32:35], v1 offset:10240\n" \
  "v_mfma_f32_16x16x32_bf16 v[48:51], v[96:99], v[12:15], v[48:51]\n" \
  "v_mfma_f32_16x16x32_bf16 v[52:55], v[160:163], v[12:15], v[52:55]\n" \
  "v_mfma_f32_16x16x32_bf16 v[56:59], v[224:227], v[12:15], v[56:59]\n" \
  "s_waitcnt lgkmcnt(4)\n" \
  "v_mfma_f32_16x16x32_bf16 v[60:63], v[36:39], v[12:15], v[60:63]\n" \
  "ds_read_b128 v[12:15], v0 offset:24576\n" \
  "ds_read_b128 v[36:39], v1 offset:11264\n" \
  "v_mfma_f32_16x16x32_bf16 v[48:51], v[100:103], v[16:19], v[48:51]\n" \
  "v_mfma_f32_16x16x32_bf16 v[52:55], v[164:167], v[16:19], v[52:55]\n" \
  "v_mfma_f32_16x16x32_bf16 v[56:59], v[228:231], v[16:19], v[56:59]\n" \
  "s_waitcnt lgkmcnt(4)\n" \
  "v_mfma_f32_16x16x32_bf16 v[60:63], v[28:31], v[16:19], v[60:63]\n" \
  "ds_read_b128 v[16:19], v0 offset:26624\n" \
  "ds_read_b128 v[28:31], v1 offset:12288\n" \
  "v_mfma_f32_16x16x32_bf16 v[48:51], v[104:107], v[20:23], v[48:51]\n" \
  "v_mfma_f32_16x16x32_bf16 v[52:55], v[168:171], v[20:23], v[52:55]\n" \
  "v_mfma_f32_16x16x32_bf16 v[56:59], v[232:235], v[20:23], v[56:59]\n" \
  "s_waitcnt lgkmcnt(4)\n" \
  "v_mfma_f32_16x16x32_bf16 v[60:63], v[32:35], v[20:23], v[60:63]\n" \
  "ds_read_b128 v[20:23], v0 offset:28672\n" \
  "ds_read_b128 v[32:35], v1 offset:13312\n" \
  "v_mfma_f32_16x16x32_bf16 v[48:51], v[108:111], v[24:27], v[48:51]\n" \
  "v_mfma_f32_16x16x32_bf16 v[52:55], v[172:175], v[24:27], v[52:55]\n" \
  "v_mfma_f32_16x16x32_bf16 v[56:59], v[236:239], v[24:27], v[56:59]\n" \
  "s_waitcnt lgkmcnt(4)\n" \
  "v_mfma_f32_16x16x32_bf16 v[60:63], v[36:39], v[24:27], v[60:63]\n" \
  "s_cmp_eq_u32 s68, 1\n" \
  "s_cbranch_scc0 .LCP_X3_%=\n" \
  "s_waitcnt vmcnt(0)\n" \
  "s_mov_b64 exec, 1\n" \
  "buffer_store_dword v7, off, s[56:59], s72 sc1\n" \
  "s_mov_b64 exec, -1\n" \
  "s_add_u32 s72, s72, 0x1000\n" \
  "s_mov_b32 s68, 0\n" \
  ".LCP_X3_%=:\n" \
  "ds_read_b32 v5, v6 offset:16\n" \
  "ds_read_b128 v[24:27], v0 offset:30720\n" \
  "ds_read_b128 v[36:39], v1 offset:14336\n" \
  "v_mfma_f32_16x16x32_bf16 v[48:51], v[112:115], v[12:15], v[48:51]\n" \
  "v_mfma_f32_16x16x32_bf16 v[52:55], v[176:179], v[12:15], v[52:55]\n" \
  "v_mfma_f32_16x16x32_bf16 v[56:59], v[240:243], v[12:15], v[56:59]\n" \
  "s_waitcnt lgkmcnt(5)\n" \
  "v_mfma_f32_16x16x32_bf16 v[60:63], v[28:31], v[12:15], v[60:63]\n" \
  "ds_read_b128 v[12:15], v0 offset:256\n" \
  "ds_read_b128 v[28:31], v1 offset:15360\n" \
  "ds_read_b128 v[40:43], v4 offset:0\n" \
  "ds_read_b128 v[44:47], v4 offset:64\n" \
  "v_mfma_f32_16x16x32_bf16 v[48:51], v[116:119], v[16:19], v[48:51]\n" \
  "v_mfma_f32_16x16x32_bf16 v[52:55], v[180:183], v[16:19], v[52:55]\n" \
  "v_mfma_f32_16x16x32_bf16 v[56:59], v[244:247], v[16:19], v[56:59]\n" \
  "s_waitcnt lgkmcnt(7)\n" \
  "v_mfma_f32_16x16x32_bf16 v[60:63], v[32:35], v[16:19], v[60:63]\n" \
  "s_waitcnt lgkmcnt(6)\n" \
  "v_readfirstlane_b32 s71, v5\n" \
  "s_cmp_ge_u32 s71, s67\n" \
  "s_cbranch_scc1 .LCP_ST_%=\n" \
  ".LCP_S4_%=:\n" \
  "s_sleep 1\n" \
  "ds_read_b32 v5, v6 offset:16\n" \
  "s_sub_u32 s69, s69, 1\n" \
  "s_cbranch_scc1 .LCP_END_%=\n" \
  "s_waitcnt lgkmcnt(0)\n" \
  "v_readfirstlane_b32 s71, v5\n" \
  "s_cmp_ge_u32 s71, s67\n" \
  "s_cbranch_scc1 .LCP_ST_%=\n" \
  "s_branch .LCP_S4_%=\n" \
  ".LCP_ST_%=:\n" \
  "ds_read_b128 v[16:19], v0 offset:2304\n" \
  "ds_read_b128 v[32:35], v4 offset:128\n" \
  "ds_read2_b32 v[8:9], v3 offset0:0 offset1:32\n" \
  "v_mfma_f32_16x16x32_bf16 v[48:51], v[120:123], v[20:23], v[48:51]\n" \
  "v_mfma_f32_16x16x32_bf16 v[52:55], v[184:187], v[20:23], v[52:55]\n" \
  "v_mfma_f32_16x16x32_bf16 v[56:59], v[248:251], v[20:23], v[56:59]\n" \
  "s_waitcnt lgkmcnt(7)\n" \
  "v_mfma_f32_16x16x32_bf16 v[60:63], v[36:39], v[20:23], v[60:63]\n" \
  "ds_read_b128 v[20:23], v0 offset:4352\n" \
  "ds_read_b128 v[36:39], v4 offset:192\n" \
  "v_mfma_f32_16x16x32_bf16 v[48:51], v[124:127], v[24:27], v[48:51]\n" \
  "v_mfma_f32_16x16x32_bf16 v[52:55], v[188:191], v[24:27], v[52:55]\n" \
  "v_mfma_f32_16x16x32_bf16 v[56:59], v[252:255], v[24:27], v[56:59]\n" \
  "s_waitcnt lgkmcnt(7)\n" \
  "v_mfma_f32_16x16x32_bf16 v[60:63], v[28:31], v[24:27], v[60:63]\n" \
  "s_waitcnt lgkmcnt(0)\n" \
  "s_nop 4\n" \
  "v_fma_f32 v48, -v8, v40, v48\n" \
  "v_fma_f32 v49, -v8, v41, v49\n" \
  "v_fma_f32 v50, -v8, v42, v50\n" \
  "v_fma_f32 v51, -v8, v43, v51\n" \
  "v_fma_f32 v52, -v8, v44, v52\n" \
  "v_fma_f32 v53, -v8, v45, v53\n" \
  "v_fma_f32 v54, -v8, v46, v54\n" \
  "v_fma_f32 v55, -v8, v47, v55\n" \
  "v_fma_f32 v56, -v8, v32, v56\n" \
  "v_fma_f32 v57, -v8, v33, v57\n" \
  "v_fma_f32 v58, -v8, v34, v58\n" \
  "v_fma_f32 v59, -v8, v35, v59\n" \
  "v_fma_f32 v60, -v8, v36, v60\n" \
  "v_fma_f32 v61, -v8, v37, v61\n" \
  "v_fma_f32 v62, -v8, v38, v62\n" \
  "v_fma_f32 v63, -v8, v39, v63\n" \
  "ds_read_b128 v[32:35], v1 offset:0\n" \
  "ds_read_b128 v[36:39], v1 offset:1024\n" \
  "v_mul_f32 v48, v48, v9\n" \
  "v_mul_f32 v49, v49, v9\n" \
  "v_mul_f32 v50, v50, v9\n" \
  "v_mul_f32 v51, v51, v9\n" \
  "v_mul_f32 v52, v52, v9\n" \
  "v_mul_f32 v53, v53, v9\n" \
  "v_mul_f32 v54, v54, v9\n" \
  "v_mul_f32 v55, v55, v9\n" \
  "v_cvt_pk_bf16_f32 v40, v48, v49\n" \
  "v_cvt_pk_bf16_f32 v41, v50, v51\n" \
  "v_cvt_pk_bf16_f32 v42, v52, v53\n" \
  "v_cvt_pk_bf16_f32 v43, v54, v55\n" \
  "buffer_store_dwordx4 v[40:43], v2, s[40:43], s65 offen offset:0 sc1\n" \
  "v_mul_f32 v56, v56, v9\n" \
  "v_mul_f32 v57, v57, v9\n" \
  "v_mul_f32 v58, v58, v9\n" \
  "v_mul_f32 v59, v59, v9\n" \
  "v_mul_f32 v60, v60, v9\n" \
  "v_mul_f32 v61, v61, v9\n" \
  "v_mul_f32 v62, v62, v9\n" \
  "v_mul_f32 v63, v63, v9\n" \
  "v_cvt_pk_bf16_f32 v44, v56, v57\n" \
  "v_cvt_pk_bf16_f32 v45, v58, v59\n" \
  "v_cvt_pk_bf16_f32 v46, v60, v61\n" \
  "v_cvt_pk_bf16_f32 v47, v62, v63\n" \
  "buffer_store_dwordx4 v[44:47], v2, s[40:43], s65 offen offset:1024 sc1\n" \
  "ds_read_b128 v[24:27], v0 offset:6400\n" \
  "ds_read_b128 v[28:31], v1 offset:2048\n" \
  "v_mfma_f32_16x16x32_bf16 v[48:51], v[64:67], v[12:15], 0\n" \
  "v_mfma_f32_16x16x32_bf16 v[52:55], v[128:131], v[12:15], 0\n" \
  "v_mfma_f32_16x16x32_bf16 v[56:59], v[192:195], v[12:15], 0\n" \
  "s_waitcnt lgkmcnt(3)\n" \
  "v_mfma_f32_16x16x32_bf16 v[60:63], v[32:35], v[12:15], 0\n" \
  "ds_read_b128 v[12:15], v0 offset:8448\n" \
  "ds_read_b128 v[32:35], v1 offset:3072\n" \
  "v_mfma_f32_16x16x32_bf16 v[48:51], v[68:71], v[16:19], v[48:51]\n" \
  "v_mfma_f32_16x16x32_bf16 v[52:55], v[132:135], v[16:19], v[52:55]\n" \
  "v_mfma_f32_16x16x32_bf16 v[56:59], v[196:199], v[16:19], v[56:59]\n" \
  "s_waitcnt lgkmcnt(4)\n" \
  "v_mfma_f32_16x16x32_bf16 v[60:63], v[36:39], v[16:19], v[60:63]\n" \
  "ds_read_b128 v[16:19], v0 offset:10496\n" \
  "ds_read_b128 v[36:39], v1 offset:4096\n" \
  "v_mfma_f32_16x16x32_bf16 v[48:51], v[72:75], v[20:23], v[48:51]\n" \
  "v_mfma_f32_16x16x32_bf16 v[52:55], v[136:139], v[20:23], v[52:55]\n" \
  "v_mfma_f32_16x16x32_bf16 v[56:59], v[200:203], v[20:23], v[56:59]\n" \
  "s_waitcnt lgkmcnt(4)\n" \
  "v_mfma_f32_16x16x32_bf16 v[60:63], v[28:31], v[20:23], v[60:63]\n" \
  "ds_read_b128 v[20:23], v0 offset:12544\n" \
  "ds_read_b128 v[28:31], v1 offset:5120\n" \
  "v_mfma_f32_16x16x32_bf16 v[48:51], v[76:79], v[24:27], v[48:51]\n" \
  "v_mfma_f32_16x16x32_bf16 v[52:55], v[140:143], v[24:27], v[52:55]\n" \
  "v_mfma_f32_16x16x32_bf16 v[56:59], v[204:207], v[24:27], v[56:59]\n" \
  "s_waitcnt lgkmcnt(4)\n" \
  "v_mfma_f32_16x16x32_bf16 v[60:63], v[32:35], v[24:27], v[60:63]\n" \
  "ds_read_b128 v[24:27], v0 offset:14592\n" \
  "ds_read_b128 v[32:35], v1 offset:6144\n" \
  "v_mfma_f32_16x16x32_bf16 v[48:51], v[80:83], v[12:15], v[48:51]\n" \
  "v_mfma_f32_16x16x32_bf16 v[52:55], v[144:147], v[12:15], v[52:55]\n" \
  "v_mfma_f32_16x16x32_bf16 v[56:59], v[208:211], v[12:15], v[56:59]\n" \
  "s_waitcnt lgkmcnt(4)\n" \
  "v_mfma_f32_16x16x32_bf16 v[60:63], v[36:39], v[12:15], v[60:63]\n" \
  "ds_read_b128 v[12:15], v0 offset:16640\n" \
  "ds_read_b128 v[36:39], v1 offset:7168\n" \
  "v_mfma_f32_16x16x32_bf16 v[48:51], v[84:87], v[16:19], v[48:51]\n" \
  "v_mfma_f32_16x16x32_bf16 v[52:55], v[148:151], v[16:19], v[52:55]\n" \
  "v_mfma_f32_16x16x32_bf16 v[56:59], v[212:215], v[16:19], v[56:59]\n" \
  "s_waitcnt lgkmcnt(4)\n" \
  "v_mfma_f32_16x16x32_bf16 v[60:63], v[28:31], v[16:19], v[60:63]\n" \
  "ds_read_b128 v[16:19], v0 offset:18688\n" \
  "ds_read_b128 v[28:31], v1 offset:8192\n" \
  "v_mfma_f32_16x16x32_bf16 v[48:51], v[88:91], v[20:23], v[48:51]\n" \
  "v_mfma_f32_16x16x32_bf16 v[52:55], v[152:155], v[20:23], v[52:55]\n" \
  "v_mfma_f32_16x16x32_bf16 v[56:59], v[216:219], v[20:23], v[56:59]\n" \
  "s_waitcnt lgkmcnt(4)\n" \
  "v_mfma_f32_16x16x32_bf16 v[60:63], v[32:35], v[20:23], v[60:63]\n" \
  "ds_read_b128 v[20:23], v0 offset:20736\n" \
  "ds_read_b128 v[32:35], v1 offset:9216\n" \
  "v_mfma_f32_16x16x32_bf16 v[48:51], v[92:95], v[24:27], v[48:51]\n" \
  "v_mfma_f32_16x16x32_bf16 v[52:55], v[156:159], v[24:27], v[52:55]\n" \
  "v_mfma_f32_16x16x32_bf16 v[56:59], v[220:223], v[24:27], v[56:59]\n" \
  "s_waitcnt lgkmcnt(4)\n" \
  "v_mfma_f32_16x16x32_bf16 v[60:63], v[36:39], v[24:27], v[60:63]\n" \
  "ds_read_b128 v[24:27], v0 offset:22784\n" \
  "ds_read_b128 v[36:39], v1 offset:10240\n" \
  "v_mfma_f32_16x16x32_bf16 v[48:51], v[96:99], v[12:15], v[48:51]\n" \
  "v_mfma_f32_16x16x32_bf16 v[52:55], v[160:163], v[12:15], v[52:55]\n" \
  "v_mfma_f32_16x16x32_bf16 v[56:59], v[224:227], v[12:15], v[56:59]\n" \
  "s_waitcnt lgkmcnt(4)\n" \
  "v_mfma_f32_16x16x32_bf16 v[60:63], v[28:31], v[12:15], v[60:63]\n" \
  "ds_read_b128 v[12:15], v0 offset:24832\n" \
  "ds_read_b128 v[28:31], v1 offset:11264\n" \
  "v_mfma_f32_16x16x32_bf16 v[48:51], v[100:103], v[16:19], v[48:51]\n" \
  "v_mfma_f32_16x16x32_bf16 v[52:55], v[164:167], v[16:19], v[52:55]\n" \
  "v_mfma_f32_16x16x32_bf16 v[56:59], v[228:231], v[16:19], v[56:59]\n" \
  "s_waitcnt lgkmcnt(4)\n" \
  "v_mfma_f32_16x16x32_bf16 v[60:63], v[32:35], v[16:19], v[60:63]\n" \
  "ds_read_b128 v[16:19], v0 offset:26880\n" \
  "ds_read_b128 v[32:35], v1 offset:12288\n" \
  "v_mfma_f32_16x16x32_bf16 v[48:51], v[104:107], v[20:23], v[48:51]\n" \
  "v_mfma_f32_16x16x32_bf16 v[52:55], v[168:171], v[20:23], v[52:55]\n" \
  "v_mfma_f32_16x16x32_bf16 v[56:59], v[232:235], v[20:23], v[56:59]\n" \
  "s_waitcnt lgkmcnt(4)\n" \
  "v_mfma_f32_16x16x32_bf16 v[60:63], v[36:39], v[20:23], v[60:63]\n" \
  "ds_read_b128 v[20:23], v0 offset:28928\n" \
  "ds_read_b128 v[36:39], v1 offset:13312\n" \
  "v_mfma_f32_16x16x32_bf16 v[48:51], v[108:111], v[24:27], v[48:51]\n" \
  "v_mfma_f32_16x16x32_bf16 v[52:55], v[172:175], v[24:27], v[52:55]\n" \
  "v_mfma_f32_16x16x32_bf16 v[56:59], v[236:239], v[24:27], v[56:59]\n" \
  "s_waitcnt lgkmcnt(4)\n" \
  "v_mfma_f32_16x16x32_bf16 v[60:63], v[28:31], v[24:27], v[60:63]\n" \
  "ds_read_b128 v[24:27], v0 offset:30976\n" \
  "ds_read_b128 v[28:31], v1 offset:14336\n" \
  "v_mfma_f32_16x16x32_bf16 v[48:51], v[112:115], v[12:15], v[48:51]\n" \
  "v_mfma_f32_16x16x32_bf16 v[52:55], v[176:179], v[12:15], v[52:55]\n" \
  "v_mfma_f32_16x16x32_bf16 v[56:59], v[240:243], v[12:15], v[56:59]\n" \
  "s_waitcnt lgkmcnt(4)\n" \
  "v_mfma_f32_16x16x32_bf16 v[60:63], v[32:35], v[12:15], v[60:63]\n" \
  "ds_read_b128 v[32:35], v1 offset:15360\n" \
  "ds_read_b128 v[40:43], v4 offset:0\n" \
  "ds_read_b128 v[44:47], v4 offset:64\n" \
  "v_mfma_f32_16x16x32_bf16 v[48:51], v[116:119], v[16:19], v[48:51]\n" \
  "v_mfma_f32_16x16x32_bf16 v[52:55], v[180:183], v[16:19], v[52:55]\n" \
  "v_mfma_f32_16x16x32_bf16 v[56:59], v[244:247], v[16:19], v[56:59]\n" \
  "s_waitcnt lgkmcnt(5)\n" \
  "v_mfma_f32_16x16x32_bf16 v[60:63], v[36:39], v[16:19], v[60:63]\n" \
  "ds_read_b128 v[36:39], v4 offset:128\n" \
  "ds_read2_b32 v[8:9], v3 offset0:16 offset1:48\n" \
  "v_mfma_f32_16x16x32_bf16 v[48:51], v[120:123], v[20:23], v[48:51]\n" \
  "v_mfma_f32_16x16x32_bf16 v[52:55], v[184:187], v[20:23], v[52:55]\n" \
  "v_mfma_f32_16x16x32_bf16 v[56:59], v[248:251], v[20:23], v[56:59]\n" \
  "s_waitcnt lgkmcnt(5)\n" \
  "v_mfma_f32_16x16x32_bf16 v[60:63], v[28:31], v[20:23], v[60:63]\n" \
  "ds_read_b128 v[28:31], v4 offset:192\n" \
  "s_mov_b64 exec, 1\n" \
  "ds_add_u32 v6, v7 offset:8\n" \
  "s_mov_b64 exec, -1\n" \
  "v_xor_b32 v6, 4, v6\n" \
  "v_mfma_f32_16x16x32_bf16 v[48:51], v[124:127], v[24:27], v[48:51]\n" \
  "v_mfma_f32_16x16x32_bf16 v[52:55], v[188:191], v[24:27], v[52:55]\n" \
  "v_mfma_f32_16x16x32_bf16 v[56:59], v[252:255], v[24:27], v[56:59]\n" \
  "s_waitcnt lgkmcnt(6)\n" \
  "v_mfma_f32_16x16x32_bf16 v[60:63], v[32:35], v[24:27], v[60:63]\n" \
  "s_waitcnt lgkmcnt(1)\n" \
  "s_nop 4\n" \
  "ds_read_b32 v5, v6 offset:0\n" \
  "v_fma_f32 v48, -v8, v40, v48\n" \
  "v_fma_f32 v49, -v8, v41, v49\n" \
  "v_fma_f32 v50, -v8, v42, v50\n" \
  "v_fma_f32 v51, -v8, v43, v51\n" \
  "v_fma_f32 v52, -v8, v44, v52\n" \
  "v_fma_f32 v53, -v8, v45, v53\n" \
  "v_fma_f32 v54, -v8, v46, v54\n" \
  "v_fma_f32 v55, -v8, v47, v55\n" \
  "v_fma_f32 v56, -v8, v36, v56\n" \
  "v_fma_f32 v57, -v8, v37, v57\n" \
  "v_fma_f32 v58, -v8, v38, v58\n" \
  "v_fma_f32 v59, -v8, v39, v59\n" \
  "v_fma_f32 v60, -v8, v28, v60\n" \
  "v_fma_f32 v61, -v8, v29, v61\n" \
  "v_fma_f32 v62, -v8, v30, v62\n" \
  "v_fma_f32 v63, -v8, v31, v63\n" \
  "v_mul_f32 v48, v48, v9\n" \
  "v_mul_f32 v49, v49, v9\n" \
  "v_mul_f32 v50, v50, v9\n" \
  "v_mul_f32 v51, v51, v9\n" \
  "v_mul_f32 v52, v52, v9\n" \
  "v_mul_f32 v53, v53, v9\n" \
  "v_mul_f32 v54, v54, v9\n" \
  "v_mul_f32 v55, v55, v9\n" \
  "v_cvt_pk_bf16_f32 v40, v48, v49\n" \
  "v_cvt_pk_bf16_f32 v41, v50, v51\n" \
  "v_cvt_pk_bf16_f32 v42, v52, v53\n" \
  "v_cvt_pk_bf16_f32 v43, v54, v55\n" \
  "buffer_store_dwordx4 v[40:43], v10, s[40:43], s65 offen offset:0 sc1\n" \
  "v_mul_f32 v56, v56, v9\n" \
  "v_mul_f32 v57, v57, v9\n" \
  "v_mul_f32 v58, v58, v9\n" \
  "v_mul_f32 v59, v59, v9\n" \
  "v_mul_f32 v60, v60, v9\n" \
  "v_mul_f32 v61, v61, v9\n" \
  "v_mul_f32 v62, v62, v9\n" \
  "v_mul_f32 v63, v63, v9\n" \
  "v_cvt_pk_bf16_f32 v44, v56, v57\n" \
  "v_cvt_pk_bf16_f32 v45, v58, v59\n" \
  "v_cvt_pk_bf16_f32 v46, v60, v61\n" \
  "v_cvt_pk_bf16_f32 v47, v62, v63\n" \
  "buffer_store_dwordx4 v[44:47], v10, s[40:43], s65 offen offset:1024 sc1\n" \
  "s_waitcnt lgkmcnt(0)\n" \
  "s_add_u32 s65, s65, 0x80000\n" \
  "s_sub_u32 s64, s64, 1\n" \
  "s_mov_b32 s68, 1\n" \
  "s_cmp_lg_u32 s64, 0\n" \
  "s_cbranch_scc1 .LCP_TOP_%=\n" \
  "s_waitcnt vmcnt(0)\n" \
  "s_mov_b64 exec, 1\n" \
  "buffer_store_dword v7, off, s[56:59], s72 sc1\n" \
  "s_mov_b64 exec, -1\n" \
  "s_add_u32 s72, s72, 0x1000\n" \
  "s_mov_b32 s68, 0\n" \
  ".LCP_END_%=:\n" \
  "s_endpgm\n"
#define V_ASM \
  "s_mov_b32 s68, 0\n" \
  "s_mov_b32 s66, 3\n" \
  "s_mov_b32 s67, 2\n" \
  "s_mov_b32 s72, 0\n" \
  "v_mov_b32 v7, 1\n" \
  "v_mov_b32 v6, s70\n" \
  "v_mov_b32 v5, 0\n" \
  "ds_read_b32 v8, v4 offset:0\n" \
  "ds_read_b32 v9, v4 offset:64\n" \
  "ds_read_b32 v10, v4 offset:128\n" \
  "ds_read_b32 v4, v4 offset:192\n" \
  "s_waitcnt lgkmcnt(0)\n" \
  ".p2align 6\n" \
  ".LCW_TOP_%=:\n" \
  "ds_read_b128 v[28:31], v1 offset:0\n" \
  "ds_read_b128 v[32:35], v1 offset:1024\n" \
  "s_add_u32 s66, s66, 3\n" \
  "s_add_u32 s67, s67, 2\n" \
  "s_mov_b32 s69, 0x400000\n" \
  "v_xor_b32 v0, 0x8000, v0\n" \
  "v_xor_b32 v3, 0x100, v3\n" \
  "v_readfirstlane_b32 s71, v5\n" \
  "s_cmp_ge_u32 s71, s66\n" \
  "s_cbranch_scc1 .LCW_GO_%=\n" \
  "s_cmp_eq_u32 s68, 1\n" \
  "s_cbranch_scc0 .LCW_X1_%=\n" \
  "s_waitcnt vmcnt(0)\n" \
  "s_mov_b64 exec, 1\n" \
  "buffer_store_dword v7, off, s[56:59], s72 sc1\n" \
  "s_mov_b64 exec, -1\n" \
  "s_add_u32 s72, s72, 0x1000\n" \
  "s_mov_b32 s68, 0\n" \
  ".LCW_X1_%=:\n" \
  ".LCW_S2_%=:\n" \
  "s_sleep 1\n" \
  "ds_read_b32 v5, v6 offset:0\n" \
  "s_sub_u32 s69, s69, 1\n" \
  "s_cbranch_scc1 .LCW_END_%=\n" \
  "s_waitcnt lgkmcnt(0)\n" \
  "v_readfirstlane_b32 s71, v5\n" \
  "s_cmp_ge_u32 s71, s66\n" \
  "s_cbranch_scc1 .LCW_GO_%=\n" \
  "s_branch .LCW_S2_%=\n" \
  ".LCW_GO_%=:\n" \
  "ds_read_b128 v[12:15], v0 offset:0\n" \
  "ds_read_b128 v[16:19], v0 offset:2048\n" \
  "ds_read_b128 v[20:23], v0 offset:4096\n" \
  "ds_read_b128 v[24:27], v0 offset:6144\n" \
  "ds_read_b128 v[36:39], v1 offset:2048\n" \
  "s_waitcnt lgkmcnt(4)\n" \
  "v_mfma_f32_16x16x32_bf16 v[42:45], v[12:15], v[64:67], 0\n" \
  "v_mfma_f32_16x16x32_bf16 v[48:51], v[12:15], v[128:131], 0\n" \
  "v_mfma_f32_16x16x32_bf16 v[54:57], v[12:15], v[192:195], 0\n" \
  "v_mfma_f32_16x16x32_bf16 v[60:63], v[12:15], v[28:31], 0\n" \
  "ds_read_b128 v[12:15], v0 offset:8192\n" \
  "ds_read_b128 v[28:31], v1 offset:3072\n" \
  "s_waitcnt lgkmcnt(5)\n" \
  "v_mfma_f32_16x16x32_bf16 v[42:45], v[16:19], v[68:71], v[42:45]\n" \
  "v_mfma_f32_16x16x32_bf16 v[48:51], v[16:19], v[132:135], v[48:51]\n" \
  "v_mfma_f32_16x16x32_bf16 v[54:57], v[16:19], v[196:199], v[54:57]\n" \
  "v_mfma_f32_16x16x32_bf16 v[60:63], v[16:19], v[32:35], v[60:63]\n" \
  "ds_read_b128 v[16:19], v0 offset:10240\n" \
  "ds_read_b128 v[32:35], v1 offset:4096\n" \
  "s_waitcnt lgkmcnt(6)\n" \
  "v_mfma_f32_16x16x32_bf16 v[42:45], v[20:23], v[72:75], v[42:45]\n" \
  "v_mfma_f32_16x16x32_bf16 v[48:51], v[20:23], v[136:139], v[48:51]\n" \
  "v_mfma_f32_16x16x32_bf16 v[54:57], v[20:23], v[200:203], v[54:57]\n" \
  "s_waitcnt lgkmcnt(4)\n" \
  "v_mfma_f32_16x16x32_bf16 v[60:63], v[20:23], v[36:39], v[60:63]\n" \
  "ds_read_b128 v[20:23], v0 offset:12288\n" \
  "ds_read_b128 v[36:39], v1 offset:5120\n" \
  "v_mfma_f32_16x16x32_bf16 v[42:45], v[24:27], v[76:79], v[42:45]\n" \
  "v_mfma_f32_16x16x32_bf16 v[48:51], v[24:27], v[140:143], v[48:51]\n" \
  "v_mfma_f32_16x16x32_bf16 v[54:57], v[24:27], v[204:207], v[54:57]\n" \
  "s_waitcnt lgkmcnt(4)\n" \
  "v_mfma_f32_16x16x32_bf16 v[60:63], v[24:27], v[28:31], v[60:63]\n" \
  "ds_read_b128 v[24:27], v0 offset:14336\n" \
  "ds_read_b128 v[28:31], v1 offset:6144\n" \
  "v_mfma_f32_16x16x32_bf16 v[42:45], v[12:15], v[80:83], v[42:45]\n" \
  "v_mfma_f32_16x16x32_bf16 v[48:51], v[12:15], v[144:147], v[48:51]\n" \
  "v_mfma_f32_16x16x32_bf16 v[54:57], v[12:15], v[208:211], v[54:57]\n" \
  "s_waitcnt lgkmcnt(4)\n" \
  "v_mfma_f32_16x16x32_bf16 v[60:63], v[12:15], v[32:35], v[60:63]\n" \
  "ds_read_b128 v[12:15], v0 offset:16384\n" \
  "ds_read_b128 v[32:35], v1 offset:7168\n" \
  "v_mfma_f32_16x16x32_bf16 v[42:45], v[16:19], v[84:87], v[42:45]\n" \
  "v_mfma_f32_16x16x32_bf16 v[48:51], v[16:19], v[148:151], v[48:51]\n" \
  "v_mfma_f32_16x16x32_bf16 v[54:57], v[16:19], v[212:215], v[54:57]\n" \
  "s_waitcnt lgkmcnt(4)\n" \
  "v_mfma_f32_16x16x32_bf16 v[60:63], v[16:19], v[36:39], v[60:63]\n" \
  "ds_read_b128 v[16:19], v0 offset:18432\n" \
  "ds_read_b128 v[36:39], v1 offset:8192\n" \
  "v_mfma_f32_16x16x32_bf16 v[42:45], v[20:23], v[88:91], v[42:45]\n" \
  "v_mfma_f32_16x16x32_bf16 v[48:51], v[20:23], v[152:155], v[48:51]\n" \
  "v_mfma_f32_16x16x32_bf16 v[54:57], v[20:23], v[216:219], v[54:57]\n" \
  "s_waitcnt lgkmcnt(4)\n" \
  "v_mfma_f32_16x16x32_bf16 v[60:63], v[20:23], v[28:31], v[60:63]\n" \
  "ds_read_b128 v[20:23], v0 offset:20480\n" \
  "ds_read_b128 v[28:31], v1 offset:9216\n" \
  "v_mfma_f32_16x16x32_bf16 v[42:45], v[24:27], v[92:95], v[42:45]\n" \
  "v_mfma_f32_16x16x32_bf16 v[48:51], v[24:27], v[156:159], v[48:51]\n" \
  "v_mfma_f32_16x16x32_bf16 v[54:57], v[24:27], v[220:223], v[54:57]\n" \
  "s_waitcnt lgkmcnt(4)\n" \
  "v_mfma_f32_16x16x32_bf16 v[60:63], v[24:27], v[32:35], v[60:63]\n" \
  "ds_read_b128 v[24:27], v0 offset:22528\n" \
  "ds_read_b128 v[32:35], v1 offset:10240\n" \
  "v_mfma_f32_16x16x32_bf16 v[42:45], v[12:15], v[96:99], v[42:45]\n" \
  "v_mfma_f32_16x16x32_bf16 v[48:51], v[12:15], v[160:163], v[48:51]\n" \
  "v_mfma_f32_16x16x32_bf16 v[54:57], v[12:15], v[224:227], v[54:57]\n" \
  "s_waitcnt lgkmcnt(4)\n" \
  "v_mfma_f32_16x16x32_bf16 v[60:63], v[12:15], v[36:39], v[60:63]\n" \
  "ds_read_b128 v[12:15], v0 offset:24576\n" \
  "ds_read_b128 v[36:39], v1 offset:11264\n" \
  "v_mfma_f32_16x16x32_bf16 v[42:45], v[16:19], v[100:103], v[42:45]\n" \
  "v_mfma_f32_16x16x32_bf16 v[48:51], v[16:19], v[164:167], v[48:51]\n" \
  "v_mfma_f32_16x16x32_bf16 v[54:57], v[16:19], v[228:231], v[54:57]\n" \
  "s_waitcnt lgkmcnt(4)\n" \
  "v_mfma_f32_16x16x32_bf16 v[60:63], v[16:19], v[28:31], v[60:63]\n" \
  "ds_read_b128 v[16:19], v0 offset:26624\n" \
  "ds_read_b128 v[28:31], v1 offset:12288\n" \
  "v_mfma_f32_16x16x32_bf16 v[42:45], v[20:23], v[104:107], v[42:45]\n" \
  "v_mfma_f32_16x16x32_bf16 v[48:51], v[20:23], v[168:171], v[48:51]\n" \
  "v_mfma_f32_16x16x32_bf16 v[54:57], v[20:23], v[232:235], v[54:57]\n" \
  "s_waitcnt lgkmcnt(4)\n" \
  "v_mfma_f32_16x16x32_bf16 v[60:63], v[20:23], v[32:35], v[60:63]\n" \
  "ds_read_b128 v[20:23], v0 offset:28672\n" \
  "ds_read_b128 v[32:35], v1 offset:13312\n" \
  "v_mfma_f32_16x16x32_bf16 v[42:45], v[24:27], v[108:111], v[42:45]\n" \
  "v_mfma_f32_16x16x32_bf16 v[48:51], v[24:27], v[172:175], v[48:51]\n" \
  "v_mfma_f32_16x16x32_bf16 v[54:57], v[24:27], v[236:239], v[54:57]\n" \
  "s_waitcnt lgkmcnt(4)\n" \
  "v_mfma_f32_16x16x32_bf16 v[60:63], v[24:27], v[36:39], v[60:63]\n" \
  "s_cmp_eq_u32 s68, 1\n" \
  "s_cbranch_scc0 .LCW_X3_%=\n" \
  "s_waitcnt vmcnt(0)\n" \
  "s_mov_b64 exec, 1\n" \
  "buffer_store_dword v7, off, s[56:59], s72 sc1\n" \
  "s_mov_b64 exec, -1\n" \
  "s_add_u32 s72, s72, 0x1000\n" \
  "s_mov_b32 s68, 0\n" \
  ".LCW_X3_%=:\n" \
  "ds_read_b32 v5, v6 offset:16\n" \
  "ds_read_b128 v[24:27], v0 offset:30720\n" \
  "ds_read_b128 v[36:39], v1 offset:14336\n" \
  "v_mfma_f32_16x16x32_bf16 v[42:45], v[12:15], v[112:115], v[42:45]\n" \
  "v_mfma_f32_16x16x32_bf16 v[48:51], v[12:15], v[176:179], v[48:51]\n" \
  "v_mfma_f32_16x16x32_bf16 v[54:57], v[12:15], v[240:243], v[54:57]\n" \
  "s_waitcnt lgkmcnt(5)\n" \
  "v_mfma_f32_16x16x32_bf16 v[60:63], v[12:15], v[28:31], v[60:63]\n" \
  "ds_read_b128 v[12:15], v0 offset:256\n" \
  "ds_read_b128 v[28:31], v1 offset:15360\n" \
  "v_mfma_f32_16x16x32_bf16 v[42:45], v[16:19], v[116:119], v[42:45]\n" \
  "v_mfma_f32_16x16x32_bf16 v[48:51], v[16:19], v[180:183], v[48:51]\n" \
  "v_mfma_f32_16x16x32_bf16 v[54:57], v[16:19], v[244:247], v[54:57]\n" \
  "s_waitcnt lgkmcnt(5)\n" \
  "v_mfma_f32_16x16x32_bf16 v[60:63], v[16:19], v[32:35], v[60:63]\n" \
  "s_waitcnt lgkmcnt(4)\n" \
  "v_readfirstlane_b32 s71, v5\n" \
  "s_cmp_ge_u32 s71, s67\n" \
  "s_cbranch_scc1 .LCW_ST_%=\n" \
  ".LCW_S4_%=:\n" \
  "s_sleep 1\n" \
  "ds_read_b32 v5, v6 offset:16\n" \
  "s_sub_u32 s69, s69, 1\n" \
  "s_cbranch_scc1 .LCW_END_%=\n" \
  "s_waitcnt lgkmcnt(0)\n" \
  "v_readfirstlane_b32 s71, v5\n" \
  "s_cmp_ge_u32 s71, s67\n" \
  "s_cbranch_scc1 .LCW_ST_%=\n" \
  "s_branch .LCW_S4_%=\n" \
  ".LCW_ST_%=:\n" \
  "ds_read_b128 v[16:19], v0 offset:2304\n" \
  "ds_read_b128 v[32:35], v3 offset:0\n" \
  "v_mfma_f32_16x16x32_bf16 v[42:45], v[20:23], v[120:123], v[42:45]\n" \
  "v_mfma_f32_16x16x32_bf16 v[48:51], v[20:23], v[184:187], v[48:51]\n" \
  "v_mfma_f32_16x16x32_bf16 v[54:57], v[20:23], v[248:251], v[54:57]\n" \
  "s_waitcnt lgkmcnt(4)\n" \
  "v_mfma_f32_16x16x32_bf16 v[60:63], v[20:23], v[36:39], v[60:63]\n" \
  "ds_read_b128 v[20:23], v0 offset:4352\n" \
  "ds_read_b128 v[36:39], v3 offset:128\n" \
  "v_mfma_f32_16x16x32_bf16 v[42:45], v[24:27], v[124:127], v[42:45]\n" \
  "v_mfma_f32_16x16x32_bf16 v[48:51], v[24:27], v[188:191], v[48:51]\n" \
  "v_mfma_f32_16x16x32_bf16 v[54:57], v[24:27], v[252:255], v[54:57]\n" \
  "s_waitcnt lgkmcnt(4)\n" \
  "v_mfma_f32_16x16x32_bf16 v[60:63], v[24:27], v[28:31], v[60:63]\n" \
  "s_waitcnt lgkmcnt(0)\n" \
  "s_nop 4\n" \
  "v_fma_f32 v42, -v32, v8, v42\n" \
  "v_fma_f32 v43, -v33, v8, v43\n" \
  "v_fma_f32 v44, -v34, v8, v44\n" \
  "v_fma_f32 v45, -v35, v8, v45\n" \
  "v_fma_f32 v48, -v32, v9, v48\n" \
  "v_fma_f32 v49, -v33, v9, v49\n" \
  "v_fma_f32 v50, -v34, v9, v50\n" \
  "v_fma_f32 v51, -v35, v9, v51\n" \
  "v_fma_f32 v54, -v32, v10, v54\n" \
  "v_fma_f32 v55, -v33, v10, v55\n" \
  "v_fma_f32 v56, -v34, v10, v56\n" \
  "v_fma_f32 v57, -v35, v10, v57\n" \
  "v_fma_f32 v60, -v32, v4, v60\n" \
  "v_fma_f32 v61, -v33, v4, v61\n" \
  "v_fma_f32 v62, -v34, v4, v62\n" \
  "v_fma_f32 v63, -v35, v4, v63\n" \
  "ds_read_b128 v[32:35], v1 offset:0\n" \
  "v_mul_f32 v42, v42, v36\n" \
  "v_mul_f32 v43, v43, v37\n" \
  "v_mul_f32 v44, v44, v38\n" \
  "v_mul_f32 v45, v45, v39\n" \
  "v_mul_f32 v48, v48, v36\n" \
  "v_mul_f32 v49, v49, v37\n" \
  "v_mul_f32 v50, v50, v38\n" \
  "v_mul_f32 v51, v51, v39\n" \
  "v_mul_f32 v54, v54, v36\n" \
  "v_mul_f32 v55, v55, v37\n" \
  "v_mul_f32 v56, v56, v38\n" \
  "v_mul_f32 v57, v57, v39\n" \
  "v_mul_f32 v60, v60, v36\n" \
  "v_mul_f32 v61, v61, v37\n" \
  "v_mul_f32 v62, v62, v38\n" \
  "v_mul_f32 v63, v63, v39\n" \
  "ds_read_b128 v[36:39], v1 offset:1024\n" \
  "v_cvt_pk_bf16_f32 v40, v42, v43\n" \
  "v_cvt_pk_bf16_f32 v41, v44, v45\n" \
  "v_cvt_pk_bf16_f32 v46, v48, v49\n" \
  "v_cvt_pk_bf16_f32 v47, v50, v51\n" \
  "v_cvt_pk_bf16_f32 v52, v54, v55\n" \
  "v_cvt_pk_bf16_f32 v53, v56, v57\n" \
  "v_cvt_pk_bf16_f32 v58, v60, v61\n" \
  "v_cvt_pk_bf16_f32 v59, v62, v63\n" \
  "ds_read_b128 v[24:27], v0 offset:6400\n" \
  "ds_read_b128 v[28:31], v1 offset:2048\n" \
  "v_mfma_f32_16x16x32_bf16 v[42:45], v[12:15], v[64:67], 0\n" \
  "v_mfma_f32_16x16x32_bf16 v[48:51], v[12:15], v[128:131], 0\n" \
  "v_mfma_f32_16x16x32_bf16 v[54:57], v[12:15], v[192:195], 0\n" \
  "s_waitcnt lgkmcnt(3)\n" \
  "v_mfma_f32_16x16x32_bf16 v[60:63], v[12:15], v[32:35], 0\n" \
  "ds_read_b128 v[12:15], v0 offset:8448\n" \
  "ds_read_b128 v[32:35], v1 offset:3072\n" \
  "v_mfma_f32_16x16x32_bf16 v[42:45], v[16:19], v[68:71], v[42:45]\n" \
  "v_mfma_f32_16x16x32_bf16 v[48:51], v[16:19], v[132:135], v[48:51]\n" \
  "v_mfma_f32_16x16x32_bf16 v[54:57], v[16:19], v[196:199], v[54:57]\n" \
  "s_waitcnt lgkmcnt(4)\n" \
  "v_mfma_f32_16x16x32_bf16 v[60:63], v[16:19], v[36:39], v[60:63]\n" \
  "ds_read_b128 v[16:19], v0 offset:10496\n" \
  "ds_read_b128 v[36:39], v1 offset:4096\n" \
  "v_mfma_f32_16x16x32_bf16 v[42:45], v[20:23], v[72:75], v[42:45]\n" \
  "v_mfma_f32_16x16x32_bf16 v[48:51], v[20:23], v[136:139], v[48:51]\n" \
  "v_mfma_f32_16x16x32_bf16 v[54:57], v[20:23], v[200:203], v[54:57]\n" \
  "s_waitcnt lgkmcnt(4)\n" \
  "v_mfma_f32_16x16x32_bf16 v[60:63], v[20:23], v[28:31], v[60:63]\n" \
  "ds_read_b128 v[20:23], v0 offset:12544\n" \
  "ds_read_b128 v[28:31], v1 offset:5120\n" \
  "v_mfma_f32_16x16x32_bf16 v[42:45], v[24:27], v[76:79], v[42:45]\n" \
  "v_mfma_f32_16x16x32_bf16 v[48:51], v[24:27], v[140:143], v[48:51]\n" \
  "v_mfma_f32_16x16x32_bf16 v[54:57], v[24:27], v[204:207], v[54:57]\n" \
  "s_waitcnt lgkmcnt(4)\n" \
  "v_mfma_f32_16x16x32_bf16 v[60:63], v[24:27], v[32:35], v[60:63]\n" \
  "ds_read_b128 v[24:27], v0 offset:14592\n" \
  "ds_read_b128 v[32:35], v1 offset:6144\n" \
  "v_mfma_f32_16x16x32_bf16 v[42:45], v[12:15], v[80:83], v[42:45]\n" \
  "v_mfma_f32_16x16x32_bf16 v[48:51], v[12:15], v[144:147], v[48:51]\n" \
  "v_mfma_f32_16x16x32_bf16 v[54:57], v[12:15], v[208:211], v[54:57]\n" \
  "s_waitcnt lgkmcnt(4)\n" \
  "v_mfma_f32_16x16x32_bf16 v[60:63], v[12:15], v[36:39], v[60:63]\n" \
  "ds_read_b128 v[12:15], v0 offset:16640\n" \
  "ds_read_b128 v[36:39], v1 offset:7168\n" \
  "v_mfma_f32_16x16x32_bf16 v[42:45], v[16:19], v[84:87], v[42:45]\n" \
  "v_mfma_f32_16x16x32_bf16 v[48:51], v[16:19], v[148:151], v[48:51]\n" \
  "v_mfma_f32_16x16x32_bf16 v[54:57], v[16:19], v[212:215], v[54:57]\n" \
  "s_waitcnt lgkmcnt(4)\n" \
  "v_mfma_f32_16x16x32_bf16 v[60:63], v[16:19], v[28:31], v[60:63]\n" \
  "ds_read_b128 v[16:19], v0 offset:18688\n" \
  "ds_read_b128 v[28:31], v1 offset:8192\n" \
  "v_mfma_f32_16x16x32_bf16 v[42:45], v[20:23], v[88:91], v[42:45]\n" \
  "v_mfma_f32_16x16x32_bf16 v[48:51], v[20:23], v[152:155], v[48:51]\n" \
  "v_mfma_f32_16x16x32_bf16 v[54:57], v[20:23], v[216:219], v[54:57]\n" \
  "s_waitcnt lgkmcnt(4)\n" \
  "v_mfma_f32_16x16x32_bf16 v[60:63], v[20:23], v[32:35], v[60:63]\n" \
  "ds_read_b128 v[20:23], v0 offset:20736\n" \
  "ds_read_b128 v[32:35], v1 offset:9216\n" \
  "v_mfma_f32_16x16x32_bf16 v[42:45], v[24:27], v[92:95], v[42:45]\n" \
  "v_mfma_f32_16x16x32_bf16 v[48:51], v[24:27], v[156:159], v[48:51]\n" \
  "v_mfma_f32_16x16x32_bf16 v[54:57], v[24:27], v[220:223], v[54:57]\n" \
  "s_waitcnt lgkmcnt(4)\n" \
  "v_mfma_f32_16x16x32_bf16 v[60:63], v[24:27], v[36:39], v[60:63]\n" \
  "ds_read_b128 v[24:27], v0 offset:22784\n" \
  "ds_read_b128 v[36:39], v1 offset:10240\n" \
  "v_mfma_f32_16x16x32_bf16 v[42:45], v[12:15], v[96:99], v[42:45]\n" \
  "v_mfma_f32_16x16x32_bf16 v[48:51], v[12:15], v[160:163], v[48:51]\n" \
  "v_mfma_f32_16x16x32_bf16 v[54:57], v[12:15], v[224:227], v[54:57]\n" \
  "s_waitcnt lgkmcnt(4)\n" \
  "v_mfma_f32_16x16x32_bf16 v[60:63], v[12:15], v[28:31], v[60:63]\n" \
  "ds_read_b128 v[12:15], v0 offset:24832\n" \
  "ds_read_b128 v[28:31], v1 offset:11264\n" \
  "v_mfma_f32_16x16x32_bf16 v[42:45], v[16:19], v[100:103], v[42:45]\n" \
  "v_mfma_f32_16x16x32_bf16 v[48:51], v[16:19], v[164:167], v[48:51]\n" \
  "v_mfma_f32_16x16x32_bf16 v[54:57], v[16:19], v[228:231], v[54:57]\n" \
  "s_waitcnt lgkmcnt(4)\n" \
  "v_mfma_f32_16x16x32_bf16 v[60:63], v[16:19], v[32:35], v[60:63]\n" \
  "ds_read_b128 v[16:19], v0 offset:26880\n" \
  "ds_read_b128 v[32:35], v1 offset:12288\n" \
  "v_mfma_f32_16x16x32_bf16 v[42:45], v[20:23], v[104:107], v[42:45]\n" \
  "v_mfma_f32_16x16x32_bf16 v[48:51], v[20:23], v[168:171], v[48:51]\n" \
  "v_mfma_f32_16x16x32_bf16 v[54:57], v[20:23], v[232:235], v[54:57]\n" \
  "s_waitcnt lgkmcnt(4)\n" \
  "v_mfma_f32_16x16x32_bf16 v[60:63], v[20:23], v[36:39], v[60:63]\n" \
  "ds_read_b128 v[20:23], v0 offset:28928\n" \
  "ds_read_b128 v[36:39], v1 offset:13312\n" \
  "v_mfma_f32_16x16x32_bf16 v[42:45], v[24:27], v[108:111], v[42:45]\n" \
  "v_mfma_f32_16x16x32_bf16 v[48:51], v[24:27], v[172:175], v[48:51]\n" \
  "v_mfma_f32_16x16x32_bf16 v[54:57], v[24:27], v[236:239], v[54:57]\n" \
  "s_waitcnt lgkmcnt(4)\n" \
  "v_mfma_f32_16x16x32_bf16 v[60:63], v[24:27], v[28:31], v[60:63]\n" \
  "ds_read_b128 v[24:27], v0 offset:30976\n" \
  "ds_read_b128 v[28:31], v1 offset:14336\n" \
  "v_mfma_f32_16x16x32_bf16 v[42:45], v[12:15], v[112:115], v[42:45]\n" \
  "v_mfma_f32_16x16x32_bf16 v[48:51], v[12:15], v[176:179], v[48:51]\n" \
  "v_mfma_f32_16x16x32_bf16 v[54:57], v[12:15], v[240:243], v[54:57]\n" \
  "s_waitcnt lgkmcnt(4)\n" \
  "v_mfma_f32_16x16x32_bf16 v[60:63], v[12:15], v[32:35], v[60:63]\n" \
  "ds_read_b128 v[32:35], v1 offset:15360\n" \
  "v_mfma_f32_16x16x32_bf16 v[42:45], v[16:19], v[116:119], v[42:45]\n" \
  "v_mfma_f32_16x16x32_bf16 v[48:51], v[16:19], v[180:183], v[48:51]\n" \
  "v_mfma_f32_16x16x32_bf16 v[54:57], v[16:19], v[244:247], v[54:57]\n" \
  "s_waitcnt lgkmcnt(3)\n" \
  "v_mfma_f32_16x16x32_bf16 v[60:63], v[16:19], v[36:39], v[60:63]\n" \
  "ds_read_b128 v[36:39], v3 offset:64\n" \
  "v_mfma_f32_16x16x32_bf16 v[42:45], v[20:23], v[120:123], v[42:45]\n" \
  "v_mfma_f32_16x16x32_bf16 v[48:51], v[20:23], v[184:187], v[48:51]\n" \
  "v_mfma_f32_16x16x32_bf16 v[54:57], v[20:23], v[248:251], v[54:57]\n" \
  "s_waitcnt lgkmcnt(2)\n" \
  "v_mfma_f32_16x16x32_bf16 v[60:63], v[20:23], v[28:31], v[60:63]\n" \
  "ds_read_b128 v[28:31], v3 offset:192\n" \
  "s_mov_b64 exec, 1\n" \
  "ds_add_u32 v6, v7 offset:8\n" \
  "s_mov_b64 exec, -1\n" \
  "v_xor_b32 v6, 4, v6\n" \
  "v_mfma_f32_16x16x32_bf16 v[42:45], v[24:27], v[124:127], v[42:45]\n" \
  "v_mfma_f32_16x16x32_bf16 v[48:51], v[24:27], v[188:191], v[48:51]\n" \
  "v_mfma_f32_16x16x32_bf16 v[54:57], v[24:27], v[252:255], v[54:57]\n" \
  "s_waitcnt lgkmcnt(3)\n" \
  "v_mfma_f32_16x16x32_bf16 v[60:63], v[24:27], v[32:35], v[60:63]\n" \
  "s_waitcnt lgkmcnt(1)\n" \
  "s_nop 4\n" \
  "ds_read_b32 v5, v6 offset:0\n" \
  "v_fma_f32 v42, -v36, v8, v42\n" \
  "v_fma_f32 v43, -v37, v8, v43\n" \
  "v_fma_f32 v44, -v38, v8, v44\n" \
  "v_fma_f32 v45, -v39, v8, v45\n" \
  "v_mul_f32 v42, v42, v28\n" \
  "v_mul_f32 v43, v43, v29\n" \
  "v_mul_f32 v44, v44, v30\n" \
  "v_mul_f32 v45, v45, v31\n" \
  "v_cvt_pk_bf16_f32 v42, v42, v43\n" \
  "v_cvt_pk_bf16_f32 v43, v44, v45\n" \
  "buffer_store_dwordx4 v[40:43], v2, s[40:43], s65 offen offset:0 sc1\n" \
  "v_fma_f32 v48, -v36, v9, v48\n" \
  "v_fma_f32 v49, -v37, v9, v49\n" \
  "v_fma_f32 v50, -v38, v9, v50\n" \
  "v_fma_f32 v51, -v39, v9, v51\n" \
  "v_mul_f32 v48, v48, v28\n" \
  "v_mul_f32 v49, v49, v29\n" \
  "v_mul_f32 v50, v50, v30\n" \
  "v_mul_f32 v51, v51, v31\n" \
  "v_cvt_pk_bf16_f32 v48, v48, v49\n" \
  "v_cvt_pk_bf16_f32 v49, v50, v51\n" \
  "buffer_store_dwordx4 v[46:49], v2, s[40:43], s65 offen offset:1024 sc1\n" \
  "v_fma_f32 v54, -v36, v10, v54\n" \
  "v_fma_f32 v55, -v37, v10, v55\n" \
  "v_fma_f32 v56, -v38, v10, v56\n" \
  "v_fma_f32 v57, -v39, v10, v57\n" \
  "v_mul_f32 v54, v54, v28\n" \
  "v_mul_f32 v55, v55, v29\n" \
  "v_mul_f32 v56, v56, v30\n" \
  "v_mul_f32 v57, v57, v31\n" \
  "v_cvt_pk_bf16_f32 v54, v54, v55\n" \
  "v_cvt_pk_bf16_f32 v55, v56, v57\n" \
  "buffer_store_dwordx4 v[52:55], v2, s[40:43], s65 offen offset:2048 sc1\n" \
  "v_fma_f32 v60, -v36, v4, v60\n" \
  "v_fma_f32 v61, -v37, v4, v61\n" \
  "v_fma_f32 v62, -v38, v4, v62\n" \
  "v_fma_f32 v63, -v39, v4, v63\n" \
  "v_mul_f32 v60, v60, v28\n" \
  "v_mul_f32 v61, v61, v29\n" \
  "v_mul_f32 v62, v62, v30\n" \
  "v_mul_f32 v63, v63, v31\n" \
  "v_cvt_pk_bf16_f32 v60, v60, v61\n" \
  "v_cvt_pk_bf16_f32 v61, v62, v63\n" \
  "buffer_store_dwordx4 v[58:61], v2, s[40:43], s65 offen offset:3072 sc1\n" \
  "s_waitcnt lgkmcnt(0)\n" \
  "s_add_u32 s65, s65, 0x40000\n" \
  "s_sub_u32 s64, s64, 1\n" \
  "s_mov_b32 s68, 1\n" \
  "s_cmp_lg_u32 s64, 0\n" \
  "s_cbranch_scc1 .LCW_TOP_%=\n" \
  "s_waitcnt vmcnt(0)\n" \
  "s_mov_b64 exec, 1\n" \
  "buffer_store_dword v7, off, s[56:59], s72 sc1\n" \
  "s_mov_b64 exec, -1\n" \
  "s_add_u32 s72, s72, 0x1000\n" \
  "s_mov_b32 s68, 0\n" \
  ".LCW_END_%=:\n" \
  "s_endpgm\n"
constexpr int RG = 2;
#define RFL(x) __builtin_amdgcn_readfirstlane(x)
#define HALF(x, i) BF8(((i) ? __builtin_shufflevector(x, x, 4, 5, 6, 7) : __builtin_shufflevector(x, x, 0, 1, 2, 3)))
constexpr int P_W = 65536, P_ST = 131072, P_SS = P_ST + 1024, P_CN = P_SS + 512;
template <bool PR>
__device__ __forceinline__ void role_proj_t(const MegaArgs& A, char* smem, const int L, const int idx, const bool kv) {
  const int w = A.wv;
  const int l = lane_id();
  const int c = l & 15, g = l >> 4;
  char* ws = A.ws;
  unsigned* FL = (unsigned*)(ws + OFF_FL);
  unsigned* TMO = FL + FL_TMO;
  const unsigned sb = lds_base(smem);
  auto cn = (volatile __attribute__((address_space(3))) unsigned*)(unsigned long)(sb + P_CN);
  u64 tag;
  const bool h4 = kv && L;
  const __amdgpu_buffer_rsrc_t rsM = MKRS(ws + OFF_MEMK + L * SZ_MEM, kv && w == 2 ? (4 - L) * SZ_MEM : 0);
  const bool mc = kv && w == 2 && (MCE < 2 || !L);
  constexpr int NMC = MCE == 2 ? 8 : 4;
  const int b0 = RFL(h4 ? (H4S ? 2 : 3) * w : w == 2 ? 0 : HX0),
            b1 = RFL(h4 ? (H4S ? 2 * w + 2 : w == 2 ? 8 : 3 * w + 3 * (w < 2)) : w == 2 ? HX0 : 8);
  const __amdgpu_buffer_rsrc_t rsF = MKRS(FL + FL_FC + L * 64 * 32 * 32, 64 * 4096);
  const int fo = ((blockIdx.x & 3) * 8 + ((H4S && h4) || w == 3 ? l >> 3 : min(b0 + (l >> 3), b1 - 1))) * 128 + (l & 7) * 4;
#define FC_LOOK(s) __builtin_amdgcn_raw_buffer_load_b32(rsF, fo, (s) * 4096, AUX_SC1)
#define MEM_CLEAR(s)                                                                                                          \
  _Pragma("unroll") for (int i = 0; i < NMC; ++i)                                                                             \
    ST128((v4u{0u, 0u, 0u, 0u}), rsM, l * 16 + (i & 1) * 1024, (s) * 65536 + idx * 2048 + (i >> 1) * (int)SZ_MEM * (MCE == 2 ? 1 : 2));
  const char* Xb = ws + (L ? OFF_X3 : OFF_XA);
  const __amdgpu_buffer_rsrc_t rsS = MKRS(ws + OFF_H + L * (SZ_X / 2), SZ_X / 2);
  unsigned nf = 0u;
  bool early = false;
  v4u t0[16];
#define X_ASK(s) \
  for (int i = b0; i < b1; ++i) glds64c(Xb + RFL((s) * 32768 + i * 4096), l * 16, RFL(sb + ((s) & 1) * 32768 + i * 4096));
#define S_ASK(s, t) _Pragma("unroll") for (int i = 0; i < 16; ++i) t[i] = LD128(rsS, l * 16, (s) * 16384 + i * 1024);
  {
    const int j = w & 3;
    const bool vt = kv && j >= 2;
    constexpr int NT = PR ? 3 : 1;
    v8u wq[NT][8];
    const u16* Wm = (kv ? (PR ? A.Wv : A.Wk) : A.Wq) - (PR ? 128 : 0) * HD;
    const int hm = kv ? idx : 2 * idx + PR;
    const int q = (l >> 2) & 3, p = l & 3, f = q | ((g & 1) << 2);
    unsigned ra[NT];
#pragma unroll
    for (int m = 0; m < NT; ++m) {
      const int ct = PR ? m : 3;
      ra[m] = sb + (j >> 1) * 65536 + (8 * g + q) * 256 + ((4 * (j & 1) + (vt ? ct : (ct & 2) + (p >> 1))) ^ f) * 32 + (vt ? p * 8 : (p & 1) * 16 + (ct & 1) * 8);
    }
#define W_ISSUE(n) wqkv_issue<false>(Wm + (128 * (n) - 256 * ((n) & 1)) * HD, (n) & 1, hm, w, l, sb)
#define W_STAGE(n, WAIT, NEXT)                                                                             \
  {                                                                                                        \
    WAIT;                                                                                                  \
    if (n == 3 && EL == 2 && !PR && L == 0 && w >= 2) nf = FC_LOOK(0);                                     \
    if (n == 3 && EL == 1 && !PR && L == 0 && w >= 2) {                                                    \
      early = __all(nf != 0u);                                                                             \
      if (early) {                                                                                         \
        X_ASK(0)                                                                                           \
        if (w == 3) { S_ASK(0, t0) }                                                                       \
      }                                                                                                    \
      nf = FC_LOOK(early);                                                                                 \
    }                                                                                                      \
    RAW_BAR();                                                                                             \
    if (n == 3 && EL == 3 && !PR && L == 0 && w >= 2) nf = FC_LOOK(0);                                     \
    _Pragma("unroll") for (int m = 0; m < NT; ++m) {                                                       \
      _Pragma("unroll") for (int t = 0; t < 2; ++t) {                                                      \
        const unsigned a_ = ra[m] + ((n) & 1) * 32768 + t * 16384;                                         \
        const v2u r0 = trd(a_), r1 = trd(a_ + 1024), r2 = trd(a_ + 8192), r3 = trd(a_ + 8192 + 1024);      \
        wq[m][2 * (n) + t] = v8u{r0.x, r0.y, r1.x, r1.y, r2.x, r2.y, r3.x, r3.y};                          \
      }                                                                                                    \
    }                                                                                                      \
    _Pragma("unroll") for (int m = 0; m < 2 * NT; ++m) asm volatile("" : "+v"(wq[m >> 1][2 * (n) + (m & 1)])); \
    RAW_BAR();                                                                                             \
    NEXT;                                                                                                  \
    _Pragma("unroll") for (int m = 0; m < 4 * NT; ++m)                                                     \
      cs[m % NT] = MFMA16(HALF(wq[m % NT][2 * (n) + m / NT / 2], (m / NT) & 1), BF8(one), cs[m % NT]);     \
    _Pragma("unroll") for (int m = 0; m < NT; ++m) asm volatile("" : "+v"(cs[m]));                         \
  }
    __builtin_amdgcn_s_waitcnt(0x0f70);
    if (EL && !MC3 && mc) { MEM_CLEAR(0) }
    W_ISSUE(0);
    W_ISSUE(1);
    tag = call_tag(A);
    if (w == 0 && l < 8) cn[l] = l == 1 ? 3u : l == 5 ? 2u : 0u;
    const v4u one = {0x3f803f80u, 0x3f803f80u, 0x3f803f80u, 0x3f803f80u};
    f32x4 cs[NT] = {};
    u64 rv = 0;
    W_STAGE(0, WAIT_VM(8), W_ISSUE(2); if (EL) RD_ASK(rv));
    SST(4);
    W_STAGE(1, if (EL) WAIT_VM(9); else WAIT_VM(8), W_ISSUE(3); if (EL != 0 && MC3 != 0 && mc) { MEM_CLEAR(0) if (MC3 == 2) { MEM_CLEAR(1) } });
    SST(5);
    W_STAGE(2, if (EL != 0 && MC3 == 2 && mc) WAIT_VM(16); else if (EL != 0 && MC3 != 0 && mc) WAIT_VM(12); else WAIT_VM(8), (void)0);
    SST(6);
    asm volatile("" : "+v"(rv));
    if (!EL) ready_wait(A, tag, ready_look(A));
    for (unsigned spins = 0; EL && !__all(rv == tag) && spins < SPIN_CAP; ++spins) {
      __builtin_amdgcn_s_sleep(8);
      RD_ASK(rv);
      asm volatile("s_waitcnt vmcnt(0)" : "+v"(rv)::"memory");
    }
    if (EL == 1 && !PR && L == 0 && w >= 2) nf = FC_LOOK(0);
    W_STAGE(3, if (EL >= 2 && MC3 == 2 && mc) WAIT_VM(8); else if (EL >= 2 && MC3 == 1 && mc) WAIT_VM(4); else WAIT_VM(0), if (!PR) {
      _Pragma("unroll") for (int ks = 0; ks < 16; ++ks)
        *reinterpret_cast<bf16x8*>(smem + P_W + j * 16384 + ks * 1024 + l * 16) = HALF(wq[0][ks >> 1], ks & 1);
    });
    SST(7);
#undef W_STAGE
#undef W_ISSUE
#pragma unroll
    for (int m = 0; m < NT; ++m) *reinterpret_cast<f32x4*>(smem + P_ST + j * 256 + (PR ? m : 3) * 64 + g * 16) = cs[m];
    RAW_BAR();
    SST(8);
    if constexpr (PR) {
      const v4u rsO = mk_srd(ws + RFL((unsigned)(vt ? OFF_VF + L * SZ_O : OFF_QK + L * SZ_QK)), RFL((unsigned)(vt ? SZ_O : SZ_QK)));
      const v4u rsP = mk_srd(FL + RFL(kv ? FL_FQ + (L * 64 * 32 + idx) * 32 + 2 * j : FL_FP + (L * 16 + idx) * 32 + j), 63 * 4096 + 4);
      const int so = RFL(vt ? idx * 8 + 4 * (j & 1) : (kv ? idx * 16 + 8 : (2 * idx + (j >> 1)) * 16) + 2 * (j & 1));
      const v8u pv = {sb + 32768 + g * 512 + c * 16, sb + P_W + j * 16384 + l * 16, (unsigned)l * 16, sb + P_SS + 256 + (vt ? g * 16 : c * 4),
                      sb + P_ST + j * 256 + (vt ? c * 4 : g * 16), 0u, 0u, 0u};
      const char* dbg = nullptr;
#define P_OPS                                                                                                                               \
  ::"{v[64:71]}"(wq[0][0]), "{v[72:79]}"(wq[0][1]), "{v[80:87]}"(wq[0][2]), "{v[88:95]}"(wq[0][3]), "{v[96:103]}"(wq[0][4]),                \
      "{v[104:111]}"(wq[0][5]), "{v[112:119]}"(wq[0][6]), "{v[120:127]}"(wq[0][7]), "{v[128:135]}"(wq[1][0]), "{v[136:143]}"(wq[1][1]),     \
      "{v[144:151]}"(wq[1][2]), "{v[152:159]}"(wq[1][3]), "{v[160:167]}"(wq[1][4]), "{v[168:175]}"(wq[1][5]), "{v[176:183]}"(wq[1][6]),     \
      "{v[184:191]}"(wq[1][7]), "{v[192:199]}"(wq[2][0]), "{v[200:207]}"(wq[2][1]), "{v[208:215]}"(wq[2][2]), "{v[216:223]}"(wq[2][3]),     \
      "{v[224:231]}"(wq[2][4]), "{v[232:239]}"(wq[2][5]), "{v[240:247]}"(wq[2][6]), "{v[248:255]}"(wq[2][7]), "{v[0:7]}"(pv),               \
      "{s[40:43]}"(rsO), "{s[56:59]}"(rsP), "{s64}"(NSEG), "{s65}"(so * 1024), "{s70}"(sb + P_CN), "{s[36:37]}"(dbg)                        \
      : "memory", "vcc", "scc", "s80", "v63"
      if (vt) asm volatile("s_mov_b64 exec, -1\ns_waitcnt vmcnt(0) lgkmcnt(0)\ns_nop 4\n" V_ASM P_OPS);
      else asm volatile("s_mov_b64 exec, -1\ns_waitcnt vmcnt(0) lgkmcnt(0)\ns_nop 4\n" P_ASM P_OPS);
#undef P_OPS
      return;
    }
  }
  __builtin_amdgcn_s_setprio(RPRIO);
  if (h4 || w >= 2) {
    if (!EL && mc) { MEM_CLEAR(0) }
    auto stats = [&](const v4u (&t)[16], const int s) {
      float a = 0.f, b = 0.f, qa = 0.f, qb = 0.f;
#pragma unroll
      for (int i = 0; i < 16; ++i) {
        a = one(a + __uint_as_float(t[i].x));
        b = one(b + __uint_as_float(t[i].z));
        qa = one(qa + __uint_as_float(t[i].y));
        qb = one(qb + __uint_as_float(t[i].w));
      }
      a = xsum32(xsum16(a));
      b = xsum32(xsum16(b));
      float da = 0.f, db = 0.f;
#pragma unroll
      for (int i = 0; i < 16; ++i) {
        const float u = one(__uint_as_float(t[i].x) - a * (1.0f / 64)), v = one(__uint_as_float(t[i].z) - b * (1.0f / 64));
        da = one(da + u * u);
        db = one(db + v * v);
      }
      const float var = xsum32(swap_add16(qa + da * 0.125f, qb + db * 0.125f)) * (1.0f / DM);
      const float ri = 1.0f / bfr1(sqrtf(bfr1(bfr1(var) + 1e-5f)));
      reinterpret_cast<float*>(smem + P_SS)[(s & 1) * 64 + (g >> 1) * 32 + 2 * c + (g & 1)] = g < 2 ? bfr1(((g & 1) ? b : a) * (1.0f / DM)) : ri;
      asm volatile("" ::: "memory");
      if (l == 0) cn[4 + (s & 1)] = 2 * s + 4;
    };
    static_assert(EL != 1 || MC3 == 2, "clear");
    if (early) {
      if (w == 3) WAIT_VM(17);
      else WAIT_VM(1);
      if (l == 0) __hip_atomic_fetch_add((__attribute__((address_space(3))) unsigned*)cn, 3u, __ATOMIC_RELAXED, __HIP_MEMORY_SCOPE_WORKGROUP);
      SSX(10, w == 2);
      SSX(9, w == 2);
      if (w == 3) stats(t0, 0);
      SSX(11, w == 3);
    }
    unsigned tn = now10(), tg = tn;
    for (int s = early; s < NSEG; ++s) {
      for (unsigned spins = 0; !__all(nf != 0u); ++spins) {
        if (spin_timeout(spins, TMO)) return;
        if (spins) __builtin_amdgcn_s_sleep(SLP_H);
        else if (GF_H && (h4 || (GF_P && L)) && s >= GF0 && s < NSEG - 6) gate(tg);
        tg = now10() + GF_H;
        nf = FC_LOOK(s);
        asm volatile("" ::: "memory");
      }
      if (NFW) asm volatile("" ::"v"(nf));
      TS(4, s, 128);
      for (unsigned spins = 0; cn[2 + (s & 1)] < 4u * (s >> 1); ++spins) {
        if (spins > 64u * SPIN_CAP) return;
        __builtin_amdgcn_s_sleep(2);
      }
      if (L == 0 && s >= PACE0) gate(tn);
      else tn = now10();
      tn += PACE;
      TS(5, s, 128);
      X_ASK(s)
      if (H4S && h4) {
        v4u t[4];
#pragma unroll
        for (int i = 0; i < 4; ++i) t[i] = LD128(rsS, (l >> 2) * 256 + (l & 3) * 16 + w * 64, s * 16384 + i * 4096);
        nf = 0u;
        if (MCE == 1 && w == 2 && s < NSEG - 1) {
          MEM_CLEAR(s + 1)
          WAIT_VM(8);
        } else {
          WAIT_VM(4);
        }
        if (l == 0) __hip_atomic_fetch_add((__attribute__((address_space(3))) unsigned*)cn + (s & 1), w < 2 ? 2u : 1u, __ATOMIC_RELAXED, __HIP_MEMORY_SCOPE_WORKGROUP);
        TS(6, s, 128);
#define F(x) __uint_as_float(x)
        const float a = red_mod4(one(F(t[0].x) + F(t[1].x)) + one(F(t[2].x) + F(t[3].x)));
        const float b = red_mod4(one(F(t[0].z) + F(t[1].z)) + one(F(t[2].z) + F(t[3].z)));
        float da = 0.f, db = 0.f;
#pragma unroll
        for (int i = 0; i < 4; ++i) {
          const float u = one(F(t[i].x) - a * (1.0f / 64)), v = one(F(t[i].z) - b * (1.0f / 64));
          da = one(da + u * u);
          db = one(db + v * v);
        }
        const float qa = one(F(t[0].y) + F(t[1].y)) + one(F(t[2].y) + F(t[3].y)), qb = one(F(t[0].w) + F(t[1].w)) + one(F(t[2].w) + F(t[3].w));
#undef F
        float var = xsum32(swap_add16(one(qa + da * 0.125f), one(qb + db * 0.125f)));
        var += dppr<0x124>(var);
        var += dppr<0x128>(var);
        const float ri = 1.0f / bfr1(sqrtf(bfr1(bfr1(var * (1.0f / DM)) + 1e-5f)));
        if (c < 4) reinterpret_cast<float*>(smem + P_SS)[(s & 1) * 64 + (g >> 1) * 32 + 8 * w + 2 * c + (g & 1)] = g < 2 ? bfr1(((g & 1) ? b : a) * (1.0f / DM)) : ri;
        asm volatile("" ::: "memory");
        if (l == 0) __hip_atomic_fetch_add((__attribute__((address_space(3))) unsigned*)cn + 4 + (s & 1), 1u, __ATOMIC_RELAXED, __HIP_MEMORY_SCOPE_WORKGROUP);
        TS(7, s, 192);
        if (!MCE && w == 2 && s < NSEG - 1) { MEM_CLEAR(s + 1) }
      } else if (w != 3) {
        nf = L ? 0u : FC_LOOK(s + 1);
        if (mc && s < NSEG - 1 && (EL == 0 || MC3 != 2 || s)) {
          MEM_CLEAR(s + 1)
          if (NMC == 8) WAIT_VM(8);
          else WAIT_VM(4);
        } else {
          WAIT_VM(0);
        }
        if (l == 0) __hip_atomic_fetch_add((__attribute__((address_space(3))) unsigned*)cn + (s & 1), h4 ? 2u : 3u, __ATOMIC_RELAXED, __HIP_MEMORY_SCOPE_WORKGROUP);
        TS(6, s, 128);
        SSX(10, s == 0 && w == 2);
      } else {
        v4u t[16];
        S_ASK(s, t)
        WAIT_VM(16);
        nf = L ? 0u : FC_LOOK(s + 1);
        if (l == 0 && !h4) __hip_atomic_fetch_add((__attribute__((address_space(3))) unsigned*)cn + (s & 1), 3u, __ATOMIC_RELAXED, __HIP_MEMORY_SCOPE_WORKGROUP);
        stats(t, s);
        SSX(11, s == 0);
        TS(7, s, 192);
      }
    }
    return;
  }
  const int RL = !kv, rp = idx & 15, u = RFL(kv ? idx >> 4 : L);
  const __amdgpu_buffer_rsrc_t rsW = MKRS(ws + OFF_PW, SZ_PW);
  const __amdgpu_buffer_rsrc_t rsX = MKRS(ws + (RL ? OFF_X3 : OFF_XA), SZ_X);
  const __amdgpu_buffer_rsrc_t rsY = RL ? MKRS(A.out, SZ_X) : MKRS(ws + OFF_X2, SZ_X);
  const int r = 2 * rp + (g >> 1), yo = r * (DM * 2) + (l & 31) * 16 + u * 512;
  if (kv) {
    if (XZD) gate(now10() + XZD);
    if (XZP & 3) {
      if (!wait_n<SLP_V, 32>(FL + FL_FZ + (blockIdx.x & 7) * 64, 64, TMO)) return;
    } else {
      for (int s = w; s < (XZP ? 2 * RG : NSEG); s += RG) ST128((v4u{0u, 0u, 0u, 0u}), rsY, yo, s * SEG * DM * 2);
      DRAIN();
      if (l < 8) set_flag(FL + FL_FZ + l * 64 + 2 * idx + w);
    }
  }
  v4u sel;
#pragma unroll
  for (int e = 0; e < 4; ++e) sel[e] = (g >> 1) == (c >> 3) && ((c & 7) >> 1) == e ? 0x3f80u << (16 * (c & 1)) : 0u;
  unsigned tl = now10();
  for (int s = w; s < NSEG; s += RG) {
    if (GF_R) {
      gate(tl);
      const gu32* f = (gu32*)(FL + FL_FW) + ((RL * 64 + s) * 4 + (blockIdx.x & 3)) * 4 * 32 + (c >> 2) * 32 + (c & 3);
      for (unsigned spins = 0;; ++spins) {
        tl = now10() + GF_R;
        if (__all(__hip_atomic_load(f, RLX_AGENT) != 0u) || spin_timeout(spins, TMO)) break;
        __builtin_amdgcn_s_sleep(SLP_R);
      }
      asm volatile("" ::: "memory");
    } else
    grp_wait<SLP_R, 4, GATE_R>(true, nullptr, 0, FL + FL_FW + ((RL * 64 + s) * 4 + (blockIdx.x & 3)) * 4 * 32, 16, TMO, tl);
    if (s >= NSEG - 6) tl = now10();
    DRAIN();
    TS(0, s, w * 64);
    v4u p[8][2];
    const v4u xr = LD128(rsX, (l & 31) * 512 + r * 16, s * 32768 + u * 16384);
#pragma unroll
    for (int k = 0; k < 8; ++k)
#pragma unroll
      for (int b = 0; b < 2; ++b)
        p[k][b] = LD128(rsW, (g & 1) * 32768 + (c >> 2) * 128 + (g >> 1) * 64 + (c & 3) * 16 + b * 512, ((RL * 64 + s) * 16 + 2 * k) * 32768 + rp * 2048 + u * 1024);
    __builtin_amdgcn_sched_barrier(0);
    f32x4 ac[2] = {};
#pragma unroll
    for (int k = 0; k < 8; ++k)
#pragma unroll
      for (int b = 0; b < 2; ++b) ac[b] = MFMA16(BF8(sel), BF8(p[k][b]), ac[b]);
    v4u xo;
    {
      const auto x = __builtin_amdgcn_permlane16_swap(pk2s(ac[0][0], ac[0][1]), pk2s(ac[1][0], ac[1][1]), false, false);
      const auto y = __builtin_amdgcn_permlane16_swap(pk2s(ac[0][2], ac[0][3]), pk2s(ac[1][2], ac[1][3]), false, false);
      const v4u a = {x[0], y[0], x[1], y[1]};
#pragma unroll
      for (int e = 0; e < 4; ++e) {
        xo[e] = pk2s(lo16(xr[e]) + lo16(a[e]), hi16(xr[e]) + hi16(a[e]));
        if (kv) xo[e] = xo[e] ? xo[e] : 0x8000u;
      }
      ST128(xo, rsY, yo, s * SEG * DM * 2);
    }
    TS(1, s, w * 64);
    if (kv) {
      const float pv = lo16(g < 2 ? __builtin_amdgcn_readlane(xo[0], 0) : __builtin_amdgcn_readlane(xo[0], 32));
      float sm = 0.f, q0 = 0.f, q1 = 0.f;
#pragma unroll
      for (int e = 0; e < 4; ++e) {
        const float a = one(lo16(xo[e]) - pv), b = one(hi16(xo[e]) - pv);
        sm = one(sm + one(a + b));
        q0 = one(q0 + a * a);
        q1 = one(q1 + b * b);
      }
      sm = half_sum(sm);
      const float q = fmaxf(one(half_sum(q0 + q1)) - sm * sm * (1.0f / 256), 0.f);
      const u64 st = (u64)__float_as_uint(sm + pv * 256.f) | (u64)(__float_as_uint(q) | 1u) << 32;
      if ((l & 31) < 4) __hip_atomic_store((gu64*)(FL + FL_FR + ((s * 4 + (l & 3)) * 4 + (r >> 3)) * 32 + (r & 7) * 4 + 2 * u), st, RLX_AGENT);
      if (XZP == 4 && s + 2 * RG < NSEG) ST128((v4u{0u, 0u, 0u, 0u}), rsY, yo, (s + 2 * RG) * SEG * DM * 2);
    }
    TS(3, s, w * 64);
  }
  if (L == 1 && idx == 0 && w == 0 && l == 0) __hip_atomic_store((gu64*)(ws + OFF_EP), tag, RLX_AGENT);
}
__device__ __forceinline__ void role_proj(const MegaArgs& A, char* smem, const int L, const int idx, const bool kv) {
  if (A.wv < 4) role_proj_t<false>(A, smem, L, idx, kv);
  else role_proj_t<true>(A, smem, L, idx, kv);
}

constexpr int CUN = 2144;
__device__ __forceinline__ v2u part_stats(const v4u o) {
  float v[8];
#pragma unroll
  for (int e = 0; e < 4; ++e) { v[2 * e] = lo16(o[e]); v[2 * e + 1] = hi16(o[e]); }
  const float s = ((v[0] + v[1]) + (v[2] + v[3])) + ((v[4] + v[5]) + (v[6] + v[7])), m = s * 0.125f;
  float q0 = 0.f, q1 = 0.f;
#pragma unroll
  for (int e = 0; e < 4; ++e) {
    const float a = v[e] - m, b = v[4 + e] - m;
    q0 += a * a;
    q1 += b * b;
  }
  return v2u{__float_as_uint(s), __float_as_uint(q0 + q1)};
}
constexpr int CRN = 176;
constexpr int CTN = 272;
constexpr int CV_LDS = (8 * CKN + 8 * 2 * CUN + 64 + 2 * 32 * 8 + 16 * CRN + 64 + 8 * CTN) * 2 + 8 * 1568;
__device__ __forceinline__ void role_conv(const MegaArgs& A, char* smem, const int cv) {
  u16* Kr = reinterpret_cast<u16*>(smem);
  u16* Uc = Kr + 8 * CKN;
  u16* Us = Uc + 8 * CUN + 63;
  u16* To = Uc + 8 * 2 * CUN + 64;
  u16* Sx = To + 32 * 8;
  u16* Rc = Sx + 32 * 8;
  u16* Rs = Rc + 8 * CRN + 63;
  u16* Ta = Rc + 16 * CRN + 64;
  const int w = A.wv;
  const int l = lane_id(), tid = 64 * w + l, c = l & 15, g = l >> 4;
  const int d = 8 * cv + w;
  char* ws = A.ws;
  unsigned* FL = (unsigned*)(ws + OFF_FL);
  unsigned* TMO = FL + FL_TMO;
  u16* kr = Kr + w * CKN;
  const u16* ka = kr + 2080 - 16 * c + 8 * g;
#define KFRAG(D0) (*reinterpret_cast<const f16x8*>(ka - (D0)))
  typedef __attribute__((address_space(3))) const unsigned* lpu;
#define BFRAG(a) __builtin_bit_cast(f16x8, (v4u{((lpu)(unsigned long)(a))[0], ((lpu)(unsigned long)(a))[1], ((lpu)(unsigned long)(a))[2], ((lpu)(unsigned long)(a))[3]}))
  f32x4 acc[8];
  v4u tx[4];
  const __amdgpu_buffer_rsrc_t rz1 = MKRS(ws + OFF_XA, SZ_X), rps = MKRS(ws + OFF_H, SZ_X);
  u16* uc = Uc + w * CUN;
  const unsigned ub = lds_base(reinterpret_cast<char*>(((c & 1) ? Us : Uc) + w * CUN + c + 8 * g + 32));
  const int t0 = 16 * c + 4 * g;
  float dsk, ysc;
#define SKIP(T) (*reinterpret_cast<const v2u*>(uc + 32 + 256 * (T) + t0))
#define TILE_OUT(dst, a, uu)                                                                   \
  {                                                                                            \
    v2u v_;                                                                                    \
    _Pragma("unroll") for (int e = 0; e < 2; ++e) {                                            \
      const unsigned yy = pk2(a[2 * e] * ysc, a[2 * e + 1] * ysc), u_ = uu[e];                 \
      const unsigned k_ = pk2(h2f((u16)u_) * dsk, h2f((u16)(u_ >> 16)) * dsk);                 \
      v_[e] = pk2(lo16(yy) + lo16(k_), hi16(yy) + hi16(k_));                                   \
    }                                                                                          \
    *reinterpret_cast<v2u*>((dst) + t0) = v_;                                                  \
  }
#define CHUNK_ST(i, src, N) CHUNK_STX(i, tx[i], src, N)
#define CHUNK_STX(i, X, src, N)                                                                \
  {                                                                                            \
    const unsigned p_ = lds_base((char*)((src) + (c >> 2) * (N) + (tid & ~15) + 4 * (c & 3))); \
    const v2u a0_ = trd(p_), a1_ = trd(p_ + 8 * (N));                                          \
    const v4u a_ = {a0_.x, a0_.y, a1_.x, a1_.y};                                               \
    v4u o;                                                                                     \
    _Pragma("unroll") for (int e = 0; e < 4; ++e) o[e] = pk2(lo16(X[e]) + lo16(a_[e]), hi16(X[e]) + hi16(a_[e])); \
    ST128(o, rz1, (l >> 5) * 32768 + (l & 31) * 16, (2 * (w + 8 * (i)) * 64 + cv) * 512);      \
    ST64(part_stats(o), rps, (l >> 5) * 16384 + (l & 31) * 8, (2 * (w + 8 * (i)) * 64 + cv) * 256); \
  }
#define CHUNK_FLAG(i) set_flag(FL + FL_FC + (((2 * w + 16 * ((i) + (l >> 3)) + ((l >> 2) & 1)) * 4 + (l & 3)) * 8 + (cv >> 3)) * 32 + (cv & 7))
  {
#define U_VAL(i)                                                                               \
  {                                                                                            \
    const float mb_ = lo16(st[i]), ri_ = 1024.0f / hi16(st[i]);                                 \
    _Pragma("unroll") for (int e = 0; e < 4; ++e) {                                            \
      const unsigned t_ = pk2(lo16(tx[i][e]) - mb_, hi16(tx[i][e]) - mb_);                     \
      const unsigned q_ = pk2(lo16(t_) * ri_, hi16(t_) * ri_);                                 \
      uv[e] = pkh(lo16(q_), hi16(q_));                                                         \
    }                                                                                          \
  }
    unsigned ta[3], k0p, st[4];
    v4u uv;
    v2u kn;
    u64 tag;
    const __amdgpu_buffer_rsrc_t rst = MKRS(ws + OFF_GT, (L_SEQ + 512) * 16);
    {
      KGen G;
      G.load(A, d, l);
      const u16 pk = A.Dskip[d];
      asm volatile("" ::: "memory");
      if (XL == 1) ask_slow(A, 16 * (w - 4) + (cv & 15), ta);
      if (XL) asm volatile("" : "+v"(G.pa), "+v"(G.pb), "+v"(G.pc), "+v"(G.pd), "+v"(G.pe)::"memory");
      tx[0] = *reinterpret_cast<const v4u*>(A.x0 + (size_t)tid * DM + 8 * cv);
      if (XL != 1) ask_slow(A, 16 * (w - 4) + (cv & 15), ta);
      tag = call_tag(A);
      G.setup();
      dsk = bf2f(pk) * (1.0f / 1024);
      G.st = smem + w * 16640;
      SST(0);
      ysc = G.scale();
      const Cx z = G.power(1.f);
      Cx y = cmul(G.C, z);
      G.rows(G.st + l * 8, y, z, 16);
      G.get(G.bh, G.bl, l);
      const float k0 = 2.0f * wave_sum(G.C.r.x + G.C.r.y);
      k0p = l == 0 ? pkh(k0, 0.f) : 0u;
      if (TP) {
        constexpr int R = 64 * TP, U0 = 2 * R + 32, U1 = 3 * R + 111, KN = 4 * R + 192;
        u16* k1 = reinterpret_cast<u16*>(smem + 8 * 16640) + w * KN;
        u16* t1 = reinterpret_cast<u16*>(smem + 8 * 16640) + 8 * KN;
        v4u s1 = {};
        u64 rv = 0;
        if (w < TP) {
          s1 = LD128(rst, tid * 16, (L_SEQ + R * (cv >> 4)) * 16);
          rv = ready_look(A);
        }
        if (l < R / 8 + 4) reinterpret_cast<v4u*>(k1)[l < R / 8 ? l + R / 8 + 4 : l - R / 8] = v4u{k0p, 0u, 0u, 0u};
        k1[U0 + (l < 32 ? l : R + l)] = k1[U1 + (l < 32 ? l : R + l)] = 0;
        G.columns(l, 0, R / 16);
        const f32x4 kt = G.tile<true>(l);
        if (c < R / 16) *reinterpret_cast<v2u*>(k1 + R + 28 - 16 * c - 4 * g) = v2u{pkh(kt.w, kt.z), pkh(kt.y, kt.x)};
        if (w < TP) {
          for (unsigned spins = 0; !__all(has_tag(tag, s1.x, s1.y) && has_tag(tag, s1.z, s1.w)) && spins < SPIN_CAP; ++spins) {
            __builtin_amdgcn_s_sleep(4);
            s1 = LD128(rst, tid * 16, (L_SEQ + R * (cv >> 4)) * 16);
          }
          st[0] = __builtin_amdgcn_perm(s1.w, s1.y, 0x07060302);
          U_VAL(0);
          u16* up = reinterpret_cast<u16*>(smem + 8 * 16640) + 32 + tid;
#pragma unroll
          for (int e = 0; e < 8; ++e) up[e * KN + U0] = up[e * KN + U1] = (u16)(uv[e >> 1] >> (16 * (e & 1)));
        }
        RAW_BAR();
        const unsigned u1 = lds_base(reinterpret_cast<char*>(k1 + ((c & 1) ? U1 : U0) + c + 8 * g));
        f32x4 a = {};
#pragma unroll
        for (int b = 0; b <= R / 32; ++b)
          a = MFMAH(BFRAG(u1 + 64 * b), *reinterpret_cast<const f16x8*>(k1 + R - 16 * (c & (R / 16 - 1)) + 8 * g + 32 * b), a);
        if (c < R / 16) {
          const v2u sk = *reinterpret_cast<const v2u*>(k1 + U0 + 32 + t0);
          TILE_OUT(t1 + w * (R + 16), a, sk);
        }
        RAW_BAR();
        SST(1);
        if (w < TP) CHUNK_ST(0, t1, R + 16);
        G.columns(l, R / 16, TPS);
        if (w < TP) {
          DRAIN();
          ready_wait(A, tag, rv);
          if (l < 8) CHUNK_FLAG(0);
        }
        SST(6);
        G.columns(l, TPS, 16);
      } else {
        G.columns(l, 0, 16);
      }
      const f32x4 kt = G.tile<true>(l);
      kn = v2u{pkh(kt.w, kt.z), pkh(kt.y, kt.x)};
    }
#define U_PUT(i)                                                                               \
  _Pragma("unroll") for (int e = 0; e < 4; ++e) {                                              \
    ((lph)(unsigned long)up0)[(2 * e) * CUN + 512 * (i)] = (u16)uv[e];                         \
    ((lph)(unsigned long)up0)[(2 * e + 1) * CUN + 512 * (i)] = (u16)(uv[e] >> 16);             \
    ((lph)(unsigned long)up1)[(2 * e) * CUN + 512 * (i)] = (u16)uv[e];                         \
    ((lph)(unsigned long)up1)[(2 * e + 1) * CUN + 512 * (i)] = (u16)(uv[e] >> 16);             \
  }
#define U_ROW(i) { U_VAL(i) U_PUT(i) }
    typedef __attribute__((address_space(3))) u16* lph;
    unsigned up0 = lds_base((char*)(Uc + 32 + tid)), up1 = lds_base((char*)(Us + 32 + tid));
    asm volatile("" : "+v"(up0), "+v"(up1), "+v"(kn), "+v"(k0p));
#define ST_GET(I0, I1)                                                                                                        \
  for (unsigned spins = 0; spins < SPIN_CAP; ++spins) {                                                                       \
    bool ok = true;                                                                                                           \
    v4u t_[4];                                                                                                                \
    _Pragma("unroll") for (int i = I0; i < I1; ++i) t_[i] = LD128(rst, (tid + 512 * i) * 16, 0);                              \
    _Pragma("unroll") for (int i = I0; i < I1; ++i) {                                                                         \
      ok = ok && has_tag(tag, t_[i].x, t_[i].y) && has_tag(tag, t_[i].z, t_[i].w);                                            \
      st[i] = __builtin_amdgcn_perm(t_[i].w, t_[i].y, 0x07060302);                                                            \
    }                                                                                                                         \
    if (__all(ok)) break;                                                                                                     \
    __builtin_amdgcn_s_sleep(4);                                                                                              \
    asm volatile("" ::: "memory");                                                                                            \
  }
    if (w < 4) {
      ST_GET(0, 2);
      U_VAL(0);
      asm volatile("" : "+v"(uv));
    }
#pragma unroll
    for (int i = 1; i < 4; ++i) tx[i] = *reinterpret_cast<const v4u*>(A.x0 + (size_t)(tid + 512 * i) * DM + 8 * cv);
    RAW_BAR();
    SST(2);
    if (w < 4) { U_PUT(0) }
    *reinterpret_cast<v2u*>(kr + 2076 - 16 * c - 4 * g) = kn;
    if (l < 38) reinterpret_cast<v4u*>(kr)[l < 32 ? 260 + l : l < 34 ? 194 + l : l - 34] = v4u{k0p, 0u, 0u, 0u};
    (l < 32 ? uc : Us + w * CUN - 32)[l] = 0;
    uc[32 + L_SEQ + l] = Us[w * CUN + 32 + L_SEQ + l] = 0;
    uc[32 + 256 + l] = Us[w * CUN + 32 + 256 + l] = 0;
    RAW_BAR();
    SST(11);
    {
      f32x4 a0 = {}, a1 = {};
      f16x8 bf[9], af[9];
#pragma unroll
      for (int b = 0; b < 9; ++b) {
        af[b] = KFRAG(32 - 32 * b);
        bf[b] = BFRAG(ub + 64 * (b - 1));
      }
      const v2u k0 = SKIP(0);
      __builtin_amdgcn_sched_barrier(0);
#pragma unroll
      for (int b = 0; b < 9; ++b) (b & 1 ? a1 : a0) = MFMAH(bf[b], af[b], (b & 1 ? a1 : a0));
      a0 += a1;
      TILE_OUT(Ta + w * CTN, a0, k0);
    }
    const u64 rv = ready_look(A);
    RAW_BAR();
    SST(3);
    if (w < 4) {
      if (w >= TP) CHUNK_ST(0, Ta, CTN);
      asm volatile("" : "+v"(tx[1]));
      U_ROW(1);
      DRAIN();
      ready_wait(A, tag, rv);
      if (l < 8 && w >= TP) CHUNK_FLAG(0);
      ST_GET(2, 4);
    } else {
      ST_GET(0, 4);
      ready_wait(A, tag, rv);
      asm volatile("" : "+v"(tx[0]), "+v"(tx[1]));
      U_ROW(0);
      U_ROW(1);
    }
    ASKED(ta);
    if (tid < (16 * CRN + 64) / 8) reinterpret_cast<v4u*>(Rc)[tid] = v4u{0u, 0u, 0u, 0u};
    SST(4);
    const gu64* fk = (gu64*)(ws + OFF_KG) + cv * 16 + w;
    u64 kg = __hip_atomic_load(fk, RLX_AGENT);
    U_ROW(2);
    for (unsigned spins = 0; !__all(kg == tag) && spins < SPIN_CAP; ++spins) {
      __builtin_amdgcn_s_sleep(8);
      kg = __hip_atomic_load(fk, RLX_AGENT);
    }
    asm volatile("" : "+v"(tx[3]), "+v"(st[3])::"memory");
    SST(5);
    const unsigned* kd = reinterpret_cast<const unsigned*>(Ta + 8 * CTN) + w * 392;
    glds16c(ws + OFF_KF + d * 2048, l * 16, lds_base((char*)kd));
    if (l < 34) glds16c(ws + OFF_KF + d * 2048 + 1024, l * 16, lds_base((char*)kd) + 1024);
    U_ROW(3);
    DRAIN();
    {
#define MIX(lo, hi) __builtin_amdgcn_perm(hi, lo, 0x07060100)
      const unsigned a0 = kd[384 - l], b0 = kd[383 - l], a1 = kd[320 - l], b1 = kd[319 - l];
      const unsigned a2 = kd[256 - 2 * l], m2 = kd[255 - 2 * l], b2 = kd[254 - 2 * l];
      const unsigned* k3 = kd + 124 - 4 * (l & 31);
      v4u* ko = reinterpret_cast<v4u*>(kr) + l;
      ko[4] = v4u{a0, a0, (l & 1) ? b0 : MIX(a0, b0), b0};
      ko[68] = v4u{a1, a1, (l & 1) ? b1 : MIX(a1, b1), b1};
      ko[132] = v4u{a2, MIX(a2, m2), m2, b2};
      if (l < 32) ko[196] = v4u{k3[4], MIX(k3[3], k3[2]), k3[2], MIX(k3[1], k3[0])};
#undef MIX
    }
    RAW_BAR();
    SST(7);
    {
      unsigned uq[4] = {ub, ub + 1024, ub + 2048, ub + 3072};
      f16x8 bf[8], a[4];
      const f16x8 bm = BFRAG(ub - 64);
      asm volatile("" : "+v"(uq[0]), "+v"(uq[1]), "+v"(uq[2]), "+v"(uq[3]));
#pragma unroll
      for (int j = 1; j < 8; ++j) bf[j] = BFRAG(uq[j >> 1] + 512 * (j & 1));
#pragma unroll
      for (int T = 1; T < 8; ++T) acc[T] = MFMAH(bm, KFRAG(256 * T + 32), (f32x4{}));
#pragma unroll
      for (int e = 0; e < 3; ++e) a[e] = KFRAG(256 * e);
      const u16* kq = ka - 256 * 7;
#pragma unroll 1
      for (int r = 0; r < 8; ++r) {
#pragma unroll
        for (int e = 0; e < 8; ++e) {
          a[(e + 3) & 3] = *reinterpret_cast<const f16x8*>(kq + 256 * (7 - ((e + 3) & 7)) + (e >= 5 ? 32 : 0));
          const int j = (8 - e) & 7;
          bf[j] = BFRAG(uq[j >> 1] + 512 * (j & 1) + (e ? 64 : 0));
          __builtin_amdgcn_sched_barrier(0);
#pragma unroll
          for (int T = 7; T >= (e > 1 ? e : 1); --T) acc[T] = MFMAH(bf[T - e], a[e & 3], acc[T]);
          __builtin_amdgcn_sched_barrier(0);
        }
        kq += 32;
#pragma unroll
        for (int j = 0; j < 4; ++j) uq[j] += 64;
        asm volatile("" : "+v"(uq[0]), "+v"(uq[1]), "+v"(uq[2]), "+v"(uq[3]));
      }
    }
    SST(8);
    {
      v2u k[8];
#pragma unroll
      for (int T = 1; T < 8; ++T) k[T] = SKIP(T);
#pragma unroll
      for (int T = 1; T < 8; ++T) TILE_OUT(uc + 32 + 256 * T, acc[T], k[T]);
    }
    RAW_BAR();
    SST(10);
    if (w >= 4) CHUNK_ST(0, Uc + 32, CUN);
    CHUNK_ST(1, Uc + 32 + 512, CUN);
    if (PP < 0) {
      if (CVSP) __builtin_amdgcn_s_sleep(CVSP);
      CHUNK_ST(2, Uc + 32 + 1024, CUN);
      if (CVSP) __builtin_amdgcn_s_sleep(CVSP);
      CHUNK_ST(3, Uc + 32 + 1536, CUN);
    }
    if (w == 1) (void)wait_n<SLP_V, 32>(FL + FL_FZ + (blockIdx.x & 7) * 64, 64, TMO);
    DRAIN();
    if (l < (PP < 0 ? 32 : 16)) CHUNK_FLAG(0);
    SST(9);
#undef U_ROW
#undef U_VAL
#undef U_PUT
#undef ST_GET
  }
  const unsigned rb = lds_base(reinterpret_cast<char*>(((c & 1) ? Rs : Rc) + w * CRN + c + 8 * g));
#pragma unroll
  for (int j = 0; j < 8; ++j) acc[j] = f32x4{};
  const __amdgpu_buffer_rsrc_t rx = MKRS(ws + OFF_X2, SZ_X), rz = MKRS(ws + OFF_X3, SZ_X);
  const __amdgpu_buffer_rsrc_t rf = MKRS(FL + FL_FR, 64 * 16 * 128);
  v2u st = {0u, 0u}, xx = {0u, 0u};
#define X2_LOAD(ss, far)                                                                                                      \
  {                                                                                                                      \
    xx = LD64(rx, (l >> 1) * (DM * 2) + (l & 1) * 8, (far) ? (int)SZ_X : ((ss) * SEG * DM + 8 * cv) * 2);                 \
    st = LD64(rf, l * 8, ((ss) * 4 + (blockIdx.x & 3)) * 512);                                                           \
  }
  unsigned tl = now10();
  bool late = false;
  for (int s = 0; s < NSEG; ++s) {
    const int q = s & 3, q8 = s & 7;
    if (w == 1) {
      late = __builtin_popcountll(__ballot(st.y != 0u)) >= CVLATE;
      for (unsigned spins = 0; !__all(umin3(xx.x, xx.y, st.y) != 0u); ++spins) {
        if (spin_timeout(spins, TMO)) break;
        if (GF_V && s >= GF0) {
          if (spins) __builtin_amdgcn_s_sleep(SLP_V);
          else if (s < NSEG - 6) gate(tl);
          tl = now10() + GF_V;
          X2_LOAD(s, CVT2 > 64 ? 0 : spins && __builtin_popcountll(__ballot(st.y != 0u)) < CVT2);
          continue;
        }
        if (GATE_V && spins == 0 && !__any(st.y != 0u)) gate(tl);
        if (spins && !__all(st.y != 0u)) __builtin_amdgcn_s_sleep(SLP_V);
        X2_LOAD(s, __builtin_popcountll(__ballot(st.y != 0u)) < CVTHR);
      }
      if (!GF_V || s < GF0) tl = now10() + GATE_V;
      TS(0, s, 64);
      const float s0 = __uint_as_float(st.x), s1 = dppr<0xb1>(s0), m2 = __uint_as_float(st.y);
      const float var = (m2 + dppr<0xb1>(m2) + (s0 - s1) * (s0 - s1) * (1.0f / DM)) * (1.0f / DM);
      const float mb = bfr1((s0 + s1) * (1.0f / DM)), ri = 1024.0f * __builtin_amdgcn_rcpf(bfr1(__builtin_amdgcn_sqrtf(bfr1(bfr1(var) + 1e-5f))));
      const unsigned t0 = pk2(lo16(xx.x) - mb, hi16(xx.x) - mb), t1 = pk2(lo16(xx.y) - mb, hi16(xx.y) - mb);
      const unsigned b0 = pk2(lo16(t0) * ri, hi16(t0) * ri), b1 = pk2(lo16(t1) * ri, hi16(t1) * ri);
      const unsigned u0 = pkh(lo16(b0), hi16(b0)), u1 = pkh(lo16(b1), hi16(b1));
      u16* rp = Rc + (l & 1) * 4 * CRN + 32 * q + (l >> 1);
#define R_PUT(o) { rp[o] = (u16)u0; rp[(o) + CRN] = (u16)(u0 >> 16); rp[(o) + 2 * CRN] = (u16)u1; rp[(o) + 3 * CRN] = (u16)(u1 >> 16); }
      R_PUT(0) R_PUT(8 * CRN + 63)
      if (q == 0) { R_PUT(128) R_PUT(8 * CRN + 63 + 128) }
#undef R_PUT
      *reinterpret_cast<v2u*>(Sx + l * 4) = xx;
    }
    const u16* kq = ka + 32 * q8;
    const f16x8 a0 = *reinterpret_cast<const f16x8*>(kq - 32), a1 = *reinterpret_cast<const f16x8*>(kq);
    __syncthreads();
    TS(1, s, 0);
    const f16x8 bp = BFRAG(rb + 64 * ((q + 3) & 3)), bc = BFRAG(rb + 64 * q);
    const float u0 = h2f(Rc[w * CRN + 32 * q + c]), u1 = h2f(Rc[w * CRN + 32 * q + 16 + c]);
    const float x0 = bf2f(Sx[c * 8 + w]), x1 = bf2f(Sx[(16 + c) * 8 + w]);
    acc[0] = MFMAH(a0, bp, (q8 == 0 ? acc[1] : acc[0]));
    const f32x4 tmp = MFMAH(a1, bc, acc[0]);
    const unsigned uu = pk2(u0 * dsk, u1 * dsk);
    const unsigned yy = pk2(((q & 1) ? tmp[2] : tmp[0]) * ysc, ((q & 1) ? tmp[3] : tmp[1]) * ysc);
    const unsigned aa = pk2(lo16(yy) + lo16(uu), hi16(yy) + hi16(uu)), oo = pk2(x0 + lo16(aa), x1 + hi16(aa));
    if (g == (q8 >> 1)) {
      To[c * 8 + w] = (u16)oo;
      To[(16 + c) * 8 + w] = (u16)(oo >> 16);
    }
    __syncthreads();
    if (w == 0 && l < 32) {
      const v4u o = *reinterpret_cast<const v4u*>(To + l * 8);
      ST128(o, rz, l * 16, (s * 64 + cv) * 512);
      ST64(part_stats(o), rps, l * 8, ((64 + s) * 64 + cv) * 256);
    }
    if (w == 1) {
      if (GF_V && s + 1 >= GF0) st.y = 0u;
      else X2_LOAD(s + 1, !late);
    }
    const int left = 7 - (s >> 3);
#define PUSH(J0, SH)                                                                                              \
  if (left >= (J0)) {                                                                                             \
    _Pragma("unroll") for (int j = (J0); j < (J0) + 2 && j < 8; ++j)                                              \
      acc[j] = MFMAH(*reinterpret_cast<const f16x8*>(kq - 32 - 256 * j), bp, (j + (SH) < 8 ? acc[(j + (SH)) & 7] : f32x4{})); \
  }
    TS(2, s, 0);
    if (w == 0) {
      DRAIN();
      if (l < 4) set_flag(FL + FL_FC + (((64 + s) * 4 + l) * 8 + (cv >> 3)) * 32 + (cv & 7));
    }
    TS(3, s, 0);
    if (q8 == 0) { PUSH(1, 1) PUSH(3, 1) PUSH(5, 1) PUSH(7, 1) }
    else { PUSH(1, 0) PUSH(3, 0) PUSH(5, 0) PUSH(7, 0) }
#undef PUSH
    if (PP >= 0 && (unsigned)(s - PP) < 3u) {
      const int i = 2 + s - PP;
      if (i > 2) {
        DRAIN();
        if (l < 8) CHUNK_FLAG(i - 1);
        tx[2] = tx[3];
      }
      if (i < 4) CHUNK_STX(i, tx[2], Uc + 32 + 512 * i, CUN);
    }
    MSTAMP();
  }
#undef KFRAG
#undef BFRAG
#undef X2_LOAD
#undef TILE_OUT
#undef SKIP
#undef CHUNK_ST
#undef CHUNK_STX
#undef CHUNK_FLAG
}

__device__ __forceinline__ v4u ln_row_packed(const v4u xr) {
  float v[8];
#pragma unroll
  for (int e = 0; e < 4; ++e) { v[2 * e] = lo16(xr[e]); v[2 * e + 1] = hi16(xr[e]); }
  float s = ((v[0] + v[1]) + (v[2] + v[3])) + ((v[4] + v[5]) + (v[6] + v[7]));
  s = wave_sum(s);
  const float mean = s * (1.0f / DM);
  float q = 0.f;
#pragma unroll
  for (int j = 0; j < 8; ++j) { float t = v[j] - mean; q += t * t; }
  q = wave_sum(q);
  const float var = q * (1.0f / DM);
  const float mb = bfr(mean);
  const float sd = bfr(sqrtf(bfr(bfr(var) + 1e-5f)));
  const float ri = 1.0f / sd;
  v4u o;
#pragma unroll
  for (int e = 0; e < 4; ++e) {
    const unsigned t = pk2(v[2 * e] - mb, v[2 * e + 1] - mb);
    const float x0 = lo16(t), x1 = hi16(t);
    float q0 = x0 * ri, q1 = x1 * ri;
    q0 = __builtin_fmaf(__builtin_fmaf(-q0, sd, x0), ri, q0);
    q1 = __builtin_fmaf(__builtin_fmaf(-q1, sd, x1), ri, q1);
    o[e] = pk2(q0, q1);
  }
  return o;
}

__device__ __forceinline__ void ln4_packed(v4u (&x)[4]) {
  f32x2_t v[16];
#pragma unroll
  for (int e = 0; e < 16; ++e) v[e] = f32x2_t{lo16(x[e >> 2][e & 3]), hi16(x[e >> 2][e & 3])};
  const f32x2_t t = (((v[0] + v[1]) + (v[2] + v[3])) + ((v[4] + v[5]) + (v[6] + v[7]))) +
                    (((v[8] + v[9]) + (v[10] + v[11])) + ((v[12] + v[13]) + (v[14] + v[15])));
  const float mean = red_mod4(t.x + t.y) * (1.0f / DM);
  const float mb = bfr1(mean);
  f32x2_t q0 = {0.f, 0.f}, q1 = {0.f, 0.f};
#pragma unroll
  for (int e = 0; e < 16; e += 2) {
    v[e] -= mb;
    v[e + 1] -= mb;
    q0 += v[e] * v[e];
    q1 += v[e + 1] * v[e + 1];
  }
  q0 += q1;
  const float dm = mean - mb;
  const float var = fmaxf(red_mod4(q0.x + q0.y) * (1.0f / DM) - dm * dm, 0.f);
  const float sd = bfr1(sqrtf(bfr1(bfr1(var) + 1e-5f)));
  const float ri = 1.0f / sd;
#pragma unroll
  for (int e = 0; e < 16; ++e) {
    const unsigned t2 = pk2(v[e].x, v[e].y);
    const f32x2_t q = f32x2_t{lo16(t2), hi16(t2)} * ri;
    x[e >> 2][e & 3] = pk2(q.x, q.y);
  }
}

constexpr int MEGA_WGS = 256;
constexpr size_t MEGA_LDS = (size_t)(32 * 4 * 64 * 8 + 8 * SMS + 15 * QS + 128 + 16 * 64 * 8 + 8 * SOS + 512) * 2;
static_assert(MEGA_LDS <= 163840, "lds");
static_assert(MEGA_LDS >= (size_t)AW_CNT + 128 && MEGA_LDS >= (size_t)P_CN + 512, "lds");
static_assert(MEGA_LDS >= (size_t)CV_LDS, "lds");
static_assert(MEGA_LDS >= (size_t)2 * 32 * SMS * 2 && MEGA_LDS >= 131072, "lds");
static_assert(MEGA_LDS >= (size_t)8 * (16640 + 2 * (704 + 144)), "lds");
__device__ __forceinline__ MegaArgs role_args(const int b, const int wv) {
  auto p = (const __attribute__((address_space(4))) u64*)__builtin_amdgcn_kernarg_segment_ptr();
  asm volatile("" : "+s"(p));
#define KA(i) ((u16*)(__attribute__((address_space(1))) u16*)p[i])
  return MegaArgs{KA(0), KA(1), (char*)KA(2), KA(3), KA(4), KA(5), KA(6), KA(7), KA(8), KA(9), KA(10), KA(11), KA(12), KA(13), b, wv};
#undef KA
}
__global__ __launch_bounds__(512, 2) void mega_kernel(const u16* mem0, const u16* Dskip, char* ws, u16* out, const u16* x0,
                                                      const u16* logAre, const u16* Aim, const u16* Cre, const u16* Cim,
                                                      const u16* logdt, const u16* Wq, const u16* Wk, const u16* Wv,
                                                      const u16* Wo) {
  extern __shared__ __attribute__((aligned(16))) char smem[];
  const int b = vbid(), wv = __builtin_amdgcn_readfirstlane(threadIdx.x >> 6);
#define A role_args(b, wv)
  u64 tag = 0;
  {
    const unsigned t0 = now10();
    if ((b >= 32 && b < 64) || (b >= 200 && b < 232)) start_kgen(A, smem, b < 64 ? b - 32 : b - 168);
    else if (b >= 64 && b < 96) tag = start_stats<4>(A, b - 64);
    else if (b >= 240) (void)start_stats<8>(A, b - 224);
    const unsigned dl = (b >= 32 && b < 64) ? DL_C : (b >= 64 && b < 96) ? DL_A : b >= 240 ? DL_R2 : ((b >= 96 && b < 104) || b >= 232) ? DL_R : (b >= 200 && b < 232) ? DL_Q : 0u;
    while (now10() - t0 < dl) __builtin_amdgcn_s_sleep(16);
  }
  if ((EXP_OFF & 1) && b >= 64 && b < 80) return;
  if ((EXP_OFF & 8) && b >= 200 && b < 232) return;
  if ((EXP_OFF & 16) && b >= 32 && b < 64) return;
  if ((EXP_OFF & 32) && b >= 80 && b < 96) return;
  if (b < 64) role_chain(A, smem, b >> 5, b & 31, 0);
  else if (b < 96) role_aw(A, smem, (b >> 4) & 1, b & 15, tag);
  else if (b >= 104 && b < 168) role_conv(A, smem, b - 104);
  else {
    const bool kv = b >= 168 && b < 232;
    role_proj(A, smem, b >= (kv ? 200 : 240), kv ? (b - 168) & 31 : b & 15, kv);
  }
#undef A
}

extern "C" void kernel_launch(void* const* d_in, const int* in_sizes, int n_in, void* d_out, int out_size, void* d_ws,
                              size_t ws_size, hipStream_t stream) {
  MegaArgs A;
  A.x0 = (const u16*)d_in[0];
  A.logAre = (const u16*)d_in[1];
  A.Aim = (const u16*)d_in[2];
  A.Cre = (const u16*)d_in[3];
  A.Cim = (const u16*)d_in[4];
  A.logdt = (const u16*)d_in[5];
  A.Dskip = (const u16*)d_in[6];
  A.mem0 = (const u16*)d_in[8];
  A.Wq = (const u16*)d_in[9];
  A.Wk = (const u16*)d_in[10];
  A.Wv = (const u16*)d_in[11];
  A.Wo = (const u16*)d_in[12];
  A.ws = (char*)d_ws;
  A.out = (u16*)d_out;

  (void)hipFuncSetAttribute((const void*)mega_kernel, hipFuncAttributeMaxDynamicSharedMemorySize, (int)MEGA_LDS);
  mega_kernel<<<MEGA_WGS, 512, MEGA_LDS, stream>>>(A.mem0, A.Dskip, A.ws, A.out, A.x0, A.logAre, A.Aim, A.Cre, A.Cim, A.logdt,
                                                   A.Wq, A.Wk, A.Wv, A.Wo);
}
```

```cpp
#include <hip/hip_runtime.h>
#include <hip/hip_bf16.h>
#include <stdint.h>

constexpr int L_SEQ = 2048;
constexpr int DM    = 512;
constexpr int NST   = 128;
constexpr int NH    = 32;
constexpr int DH    = 128;
constexpr int HD    = NH * DH;
constexpr int MM    = 8;
constexpr int SEG   = 32;
constexpr int NSEG  = L_SEQ / SEG;
constexpr int LK    = MM + SEG;

typedef unsigned short u16;
using bf16x8 = __attribute__((ext_vector_type(8))) short;
using f32x4  = __attribute__((ext_vector_type(4))) float;
using f32x16 = __attribute__((ext_vector_type(16))) float;

static __device__ __forceinline__ u16 f2bf(float f) {
  unsigned u = __float_as_uint(f);
  u += 0x7fffu + ((u >> 16) & 1u);
  return (u16)(u >> 16);
}
static __device__ __forceinline__ float bf2f(u16 h) { return __uint_as_float(((unsigned)h) << 16); }
static __device__ __forceinline__ float bfr(float f) { return bf2f(f2bf(f)); }

constexpr size_t SZ_X      = (size_t)L_SEQ * DM * 2;
constexpr size_t OFF_KF    = 0;
constexpr size_t OFF_UT    = OFF_KF + (size_t)DM * L_SEQ * 4;
constexpr size_t OFF_YT    = OFF_UT + SZ_X;
constexpr size_t OFF_XA    = OFF_YT + SZ_X;
constexpr size_t OFF_H     = OFF_XA + SZ_X;
constexpr size_t OFF_X2    = OFF_H + SZ_X;
constexpr size_t OFF_U2    = OFF_X2 + SZ_X;
constexpr size_t OFF_X3    = OFF_U2 + SZ_X;
constexpr size_t SZ_QK     = (size_t)L_SEQ * 2 * HD * 2;
constexpr size_t OFF_QK    = OFF_X3 + SZ_X;
constexpr size_t SZ_O      = (size_t)L_SEQ * HD * 2;
constexpr size_t OFF_VF    = OFF_QK + 2 * SZ_QK;
constexpr size_t SZ_PW     = (size_t)2 * NSEG * 16 * SEG * DM * 2;
constexpr size_t OFF_PW    = OFF_VF + 2 * SZ_O;
constexpr size_t SZ_MEM    = (size_t)NSEG * MM * HD * 2;
constexpr size_t OFF_MEMK  = OFF_PW + SZ_PW;
constexpr size_t OFF_MEMV  = OFF_MEMK + 2 * SZ_MEM;
constexpr size_t OFF_GT    = OFF_MEMV + 2 * SZ_MEM;
constexpr size_t OFF_EP    = OFF_GT + 65536;
constexpr size_t OFF_RD    = OFF_EP + 4096;
constexpr size_t OFF_KG    = OFF_RD + 4096;
constexpr size_t OFF_G     = OFF_KG + 8192;
constexpr size_t SZ_ACC    = (size_t)2 * 65536;
constexpr size_t OFF_Z     = OFF_GT + (size_t)128 * DM * 4;
constexpr size_t OFF_ACC   = OFF_Z;
constexpr size_t OFF_FL    = OFF_ACC + 2 * SZ_ACC;
constexpr int FL_FK  = 0;
constexpr int FL_FW  = FL_FK + 64 * 8 * 32;
constexpr int FL_FR  = FL_FW + 2 * 64 * 16 * 32;
constexpr int FL_FC  = FL_FR + 64 * 16 * 32;
constexpr int FL_FQ  = FL_FC + 2 * 64 * 32 * 32;
constexpr int FL_FP  = FL_FQ + 2 * 64 * 32 * 32;
constexpr int FL_FZ  = FL_FP + 64 * 32 * 32;
constexpr int FL_TMO = FL_FZ + 16 * 32;
constexpr int FL_END = FL_TMO + 64;
constexpr size_t Z_BYTES   = OFF_FL + (size_t)FL_END * 4 - OFF_Z;
constexpr size_t WS_TOTAL  = OFF_Z + Z_BYTES;
static_assert(Z_BYTES % 16 == 0 && OFF_Z % 256 == 0 && Z_BYTES <= 4 * 64 * 8192 && OFF_G + 2048 <= OFF_Z, "zero region");
static_assert(WS_TOTAL + (4 << 20) <= (size_t)256 * 1024 * 1024, "workspace too large");

static_assert(WS_TOTAL - OFF_XA <= (size_t)384 * 8 * 65536, "translation warm-up");
constexpr int CKN = 2336;
static_assert((size_t)DM * 2 * CKN * 2 <= (size_t)DM * L_SEQ * 4 + SZ_X, "K buffer");
__device__ __forceinline__ float pos_of_index(int p) {
  if (p < 256) return (float)p;
  if (p < 384) return (float)(256 + 2 * (p - 256));
  if (p < 512) return (float)(512 + 4 * (p - 384));
  if (p < 640) return (float)(1024 + 8 * (p - 512));
  return 2048.0f;
}
__device__ __forceinline__ int index_of_l(int l) {
  float pf = bfr((float)l);
  int p = (int)pf;
  if (p < 256) return p;
  if (p < 512) return 256 + ((p - 256) >> 1);
  if (p < 1024) return 384 + ((p - 512) >> 2);
  if (p < 2048) return 512 + ((p - 1024) >> 3);
  return 640;
}

template <int CTRL> __device__ __forceinline__ float dppr(float x) {
  return __builtin_bit_cast(float, __builtin_amdgcn_update_dpp(0, __builtin_bit_cast(int, x), CTRL, 0xf, 0xf, false));
}
__device__ __forceinline__ float wave_sum(float v) {
  v += dppr<0x121>(v);
  v += dppr<0x122>(v);
  v += dppr<0x124>(v);
  v += dppr<0x128>(v);
  {
    auto r = __builtin_amdgcn_permlane16_swap(__float_as_uint(v), __float_as_uint(v), false, false);
    v = __uint_as_float(r[0]) + __uint_as_float(r[1]);
  }
  {
    auto r = __builtin_amdgcn_permlane32_swap(__float_as_uint(v), __float_as_uint(v), false, false);
    v = __uint_as_float(r[0]) + __uint_as_float(r[1]);
  }
  return v;
}
__device__ __forceinline__ void ln_row8(float (&v)[8]) {
  float s = 0.f;
#pragma unroll
  for (int j = 0; j < 8; ++j) s += v[j];
  s = wave_sum(s);
  const float mean = s * (1.0f / DM);
  float q = 0.f;
#pragma unroll
  for (int j = 0; j < 8; ++j) { float t = v[j] - mean; q += t * t; }
  q = wave_sum(q);
  const float var = q * (1.0f / DM);
  const float mb = bfr(mean);
  const float sd = bfr(sqrtf(bfr(bfr(var) + 1e-5f)));
#pragma unroll
  for (int j = 0; j < 8; ++j) v[j] = bfr(bfr(v[j] - mb) / sd);
}

#define RLX_AGENT __ATOMIC_RELAXED, __HIP_MEMORY_SCOPE_AGENT
typedef __attribute__((address_space(1))) unsigned gu32;
typedef unsigned v4u __attribute__((ext_vector_type(4)));
constexpr int AUX_SC1 = 16;
constexpr unsigned SPIN_CAP = 1u << 18;

__device__ __forceinline__ bool wait_flags(const unsigned* flags, int lane, int nfl, unsigned want, unsigned* tmo) {
  for (unsigned spins = 0;; ++spins) {
    unsigned v = want;
    if (lane < nfl) v = __hip_atomic_load((gu32*)flags + lane * 32, RLX_AGENT);
    if (__all(v >= want)) break;
    if (spins > SPIN_CAP) {
      if (lane == 0) __hip_atomic_store((gu32*)tmo, 1u, RLX_AGENT);
      return false;
    }
  }
  __builtin_amdgcn_fence(__ATOMIC_ACQUIRE, "wavefront");
  return true;
}

#define STAMP(i)
#define STAMP_DUMP()
#define WSTAMP(i)
template <int CTRL> __device__ __forceinline__ float dppf(float old, float x) {
  return __builtin_bit_cast(float, __builtin_amdgcn_update_dpp(__builtin_bit_cast(int, old), __builtin_bit_cast(int, x), CTRL, 0xf, 0xf, false));
}
__device__ __forceinline__ float wave_max_dpp(float x) {
  const float ninf = -3e38f;
  x = fmaxf(x, dppf<0x111>(ninf, x));
  x = fmaxf(x, dppf<0x112>(ninf, x));
  x = fmaxf(x, dppf<0x114>(ninf, x));
  x = fmaxf(x, dppf<0x118>(ninf, x));
  x = fmaxf(x, dppf<0x142>(ninf, x));
  x = fmaxf(x, dppf<0x143>(ninf, x));
  return __builtin_bit_cast(float, __builtin_amdgcn_readlane(__builtin_bit_cast(int, x), 63));
}
__device__ __forceinline__ float wave_sum_dpp(float x) {
  x += dppf<0x111>(0.f, x);
  x += dppf<0x112>(0.f, x);
  x += dppf<0x114>(0.f, x);
  x += dppf<0x118>(0.f, x);
  x += dppf<0x142>(0.f, x);
  x += dppf<0x143>(0.f, x);
  return __builtin_bit_cast(float, __builtin_amdgcn_readlane(__builtin_bit_cast(int, x), 63));
}
constexpr int SMS = 528;
constexpr int QS = 144;
constexpr int VS = 80;
constexpr int SOS = 144;
#define VAR_NREP 4
constexpr int NREP = VAR_NREP;
__device__ __forceinline__ float xsum16(float x) {
  auto r = __builtin_amdgcn_permlane16_swap(__float_as_uint(x), __float_as_uint(x), false, false);
  return __uint_as_float(r[0]) + __uint_as_float(r[1]);
}
__device__ __forceinline__ float xsum32(float x) {
  auto r = __builtin_amdgcn_permlane32_swap(__float_as_uint(x), __float_as_uint(x), false, false);
  return __uint_as_float(r[0]) + __uint_as_float(r[1]);
}
__device__ __forceinline__ float xmax16(float x) {
  auto r = __builtin_amdgcn_permlane16_swap(__float_as_uint(x), __float_as_uint(x), false, false);
  return fmaxf(__uint_as_float(r[0]), __uint_as_float(r[1]));
}
__device__ __forceinline__ float xmax32(float x) {
  auto r = __builtin_amdgcn_permlane32_swap(__float_as_uint(x), __float_as_uint(x), false, false);
  return fmaxf(__uint_as_float(r[0]), __uint_as_float(r[1]));
}
__device__ __forceinline__ float red_mod4(float x) {
  x += dppf<0x124>(0.f, x);
  x += dppf<0x128>(0.f, x);
  return xsum32(xsum16(x));
}
typedef __bf16 bf16x2_t __attribute__((ext_vector_type(2)));
typedef float f32x2_t __attribute__((ext_vector_type(2)));
__device__ __forceinline__ unsigned pk2(float a, float b) {
  f32x2_t v = {a, b};
  bf16x2_t r = __builtin_convertvector(v, bf16x2_t);
  return __builtin_bit_cast(unsigned, r);
}
typedef _Float16 f16x2_t __attribute__((ext_vector_type(2)));
typedef _Float16 f16x8 __attribute__((ext_vector_type(8)));
__device__ __forceinline__ unsigned pkh(float a, float b) {
  f32x2_t v = {a, b};
  return __builtin_bit_cast(unsigned, __builtin_convertvector(v, f16x2_t));
}
__device__ __forceinline__ float h2f(u16 h) { return (float)__builtin_bit_cast(_Float16, h); }
__device__ __forceinline__ float one(float x) {
  asm("" : "+v"(x));
  return x;
}
__device__ __forceinline__ unsigned pk2s(float a, float b) { return pk2(one(a), one(b)); }
__device__ __forceinline__ float dot2sq(const unsigned x, const float acc) {
  return __builtin_amdgcn_fdot2_f32_bf16(__builtin_bit_cast(bf16x2_t, x), __builtin_bit_cast(bf16x2_t, x), acc, false);
}
__device__ __forceinline__ float lo16(unsigned u) { return __uint_as_float(u << 16); }
__device__ __forceinline__ float hi16(unsigned u) { return __uint_as_float(u & 0xffff0000u); }
__device__ __forceinline__ float bfr1(float x) { return lo16(pk2(x, x)); }
__device__ __forceinline__ float half_sum(float v) {
  v += dppr<0x121>(v);
  v += dppr<0x122>(v);
  v += dppr<0x124>(v);
  v += dppr<0x128>(v);
  return xsum16(v);
}
__device__ __forceinline__ void scale_pair(float a, float b, float scale, float& oa, float& ob) {
  unsigned u = pk2(a, b);
  unsigned v = pk2s(lo16(u) * scale, hi16(u) * scale);
  oa = lo16(v);
  ob = hi16(v);
}

#define XMAP 2
__device__ __forceinline__ int vbid() {
  const int b = blockIdx.x, x = b & 7, t = b >> 3;
  if (XMAP == 2) {
    const int q = (t >> 2) + 2 * (x & 4);
    const unsigned long long m = (q & 8) ? 0xf0d85040cdc9ada9ull : 0x76726e6a25210501ull;
    const int e = (int)(m >> (8 * (q & 7))) & 255;
    return (e & 252) + ((x & 3) << (2 + (e & 3))) + (t & 3) + (q == 14 ? ((x & 2) ? 8 : -120) : 0);
  }
  if (XMAP == 0) {
    if (b < 64) return (b & 32) + 4 * x + (t & 3);
    if (b < 96) return (b & ~15) + 2 * x + (t & 1);
    if (b >= 104 && b < 168) return 104 + 8 * x + t - 13;
    if (b >= 168 && b < 232) return 168 + ((b - 168) & 32) + 4 * x + ((t - 21) & 3);
    return b;
  }
  if (XMAP == 3) {
    const int y = x & 3;
    if (t >= 16 && t < 24) return 104 + 16 * y + 2 * (x & 4) + t - 16;
    if (x < 4) return t < 16 ? 4 * (t & 8) + 8 * x + (t & 7) : 64 + 4 * (t & 4) + 4 * y + (t & 3);
    if (t >= 24) return (t >= 28 ? 240 : y < 2 ? 96 : 224) + 4 * y + (t & 3);
    return 168 + 4 * (t & 8) + 8 * y + (t & 7);
  }
  if (XMAP == 4) {
    const int y = x & 3;
    if (x < 4) return t < 16 ? 4 * (t & 8) + 8 * x + (t & 7) : t < 28 ? 104 + 16 * y + t - 16 : 64 + 4 * y + (t & 3);
    if (t >= 24) return (t >= 28 ? 240 : y < 2 ? 96 : 224) + 4 * y + (t & 3);
    if (t < 16) return 168 + 4 * (t & 8) + 8 * y + (t & 7);
    return t < 20 ? 104 + 16 * y + t - 4 : 80 + 4 * y + (t & 3);
  }
  if (XMAP == 2 || XMAP >= 5) {
    if (XMAP == 5 && x < 4 && t < 16) return 16 * x + t;
    if (XMAP == 6 && x < 4 && t < 16) return 32 * (x & 1) + 8 * (x & 2) + t;
    if (XMAP == 7 && x < 4) return (x & 1) ? 104 + 16 * (x & 2) + t : 16 * x + t;
    if (x < 4) return t < 16 ? 4 * (t & 8) + 8 * x + (t & 7) : 104 + 16 * x + t - 16;
    const int y = x - 4;
    if (t >= 24) return (t >= 28 ? 240 : y < 2 ? 96 : 224) + 4 * y + (t & 3);
    if (t < 16) return 168 + 4 * (t & 8) + 8 * y + (t & 7);
    return 64 + 4 * (t & 4) + 4 * y + (t & 3);
  }
  if (x < 2) return 2 * (t & 16) + 16 * x + (t & 15);
  const int i = 16 * (x - 2) + (t & 15);
  if (t < 16) {
    const int k = (i * 171) >> 9, r = i - 3 * k;
    return r == 0 ? 168 + k : r == 1 ? 200 + k : 64 + 16 * (k & 1) + (k >> 1);
  }
  const int g = i >> 3, e = i & 7;
  if (g == 3) return 96 + e;
  if (g == 7) return 232 + e;
  if (g == 9) return 240 + e;
  if (g == 11) return 248 + e;
  return 104 + 8 * (g - (g > 3) - (g > 7) - (g > 9)) + e;
}
#define MSTAMP()
#define WSTAMP2()
#define SST(k)
#define SSX(k, c)
#define SSW(k, v)
#define TS(k, ss, t)
#define LS(k, ss)
#define LS_DUMP()
struct MegaArgs {
  const u16* mem0;
  const u16* Dskip;
  char* ws;
  u16* out;
  const u16* x0;
  const u16 *logAre, *Aim, *Cre, *Cim, *logdt;
  const u16 *Wq, *Wk, *Wv, *Wo;
  int vb, wv;
};

__device__ __forceinline__ int lane_id() {
  int l;
  asm volatile("v_mbcnt_lo_u32_b32 %0, -1, 0\n\tv_mbcnt_hi_u32_b32 %0, -1, %0" : "=v"(l));
  __builtin_assume(l >= 0 && l < 64);
  return l;
}
__device__ __forceinline__ bool spin_timeout(unsigned spins, unsigned* tmo) {
  if (spins > SPIN_CAP) {
    if (lane_id() == 0) __hip_atomic_store((gu32*)tmo, 1u, RLX_AGENT);
    return true;
  }
  return false;
}
#define ROT_H (4 * (h & 1))
#define ROT_T (h & 2)
#define C2G 420
#define CVSP 40
#define PFA 0
#define PFB 0
#define WO1 1
#define AWH 0
#define PEEK0 1
#define SLP0 2
#define GF_R 400
#define GF_V 0
#define PP 0
#define EL 1
#define IPF 3
#define XL 0
#define MC3 2
#define XZD 300
#define XZP 4
#define NFW 1
#define IPS 128u
#define WNT 17
#define OAUX 16
#define XAUX 16
#define TP 1
#define TPS (4 * TP + 4)
#define MCE 1
#define H4S 0
#define GF_H 150
#define CVT2 48
#define GF_P 0
#define GF0 8
#define SLP_H 12
#define HX0 6
#define CVSTEP 2
#define CVLATE 64
#define PACE 203
#define PACE0 6
#define RPRIO 3
#define DL_R2 1400
#define PNL 8
#define GSL 12
#define AWDIR 1
#define AWAT 0
#define AWRP 0
#define PEPS 4
#define FGATE 0
#define DL_C 0
#define DL_A 400
#define DL_R 0
#define DL_Q 0
#define KC0 0
#define CPRIO 1
#define P1PRIO 3
#define QLEAD 2
#define POLL_SLEEP 12
#define SLP_C POLL_SLEEP
#define SLP_A POLL_SLEEP
#define SLP_W POLL_SLEEP
#define SLP_R POLL_SLEEP
#define SLP_V POLL_SLEEP
#define SLP_Q POLL_SLEEP
#define SLP_M 4
#define AWPRIO 2
#define SLP_S 8
#define SPOS 4
#define CVPRE 12
#define QPD 3
#define QPRI 2
#define TLBW 3
#define QHV 0
#define QCHK 0
#define LEADN 99
#define GATE_V 170
#define GATE_R 380
#define GATE_N 380
#define AWAHEAD 1
#define GATE_M 150
#define PRI_A 3
#define PRI_WA 2
#define PRI_WB 1
#define AWSKIP 3
#define PRI_0 3
#define AWDEF 0
#define SLP_F 2
#define EXP_OFF 0
#define AWSPEC 1
#define CVTHR 48
#define NR_C 8
#define NR_R 8
#define NR_W 8
#define NR_N 2
#define QAHEAD 0
#define NS0 4
#define SLP_N POLL_SLEEP
template <int SLP, int WPL>
__device__ __forceinline__ bool wait_n(const unsigned* f, int n, unsigned* tmo) {
  const int l = lane_id();
  for (unsigned spins = 0;; ++spins) {
    unsigned v = 1u;
    if (l < n) v = __hip_atomic_load((gu32*)f + (l / WPL) * 32 + l % WPL, RLX_AGENT);
    if (__all(v != 0u)) break;
    if (spin_timeout(spins, tmo)) return false;
    __builtin_amdgcn_s_sleep(SLP);
  }
  asm volatile("" ::: "memory");
  return true;
}
__device__ __forceinline__ unsigned peek(const unsigned* base, const unsigned off) {
  unsigned r;
  asm volatile("global_atomic_or %0, %1, %2, %3 sc0\n\ts_waitcnt vmcnt(0)" : "=&v"(r) : "v"(off), "v"(0u), "s"(base) : "memory");
  return r;
}
__device__ __forceinline__ unsigned now10() { return (unsigned)__builtin_amdgcn_s_memrealtime(); }
__device__ __forceinline__ void gate(const unsigned t) {
#pragma unroll 1
  for (int i = 0; i < 64; ++i) {
    const int d = (int)(t - now10());
    if (d <= 0) break;
    if (d > 100) __builtin_amdgcn_s_sleep(32);
    else if (d > 25) __builtin_amdgcn_s_sleep(8);
    else __builtin_amdgcn_s_sleep(2);
  }
}
template <int SLP, int WPL, int GT>
__device__ __forceinline__ void grp_wait(const bool leader, volatile unsigned* sw, const unsigned val, const unsigned* f, int n,
                                         unsigned* tmo, unsigned& tl) {
  if (leader) {
    const int l = lane_id();
    for (unsigned spins = 0;; ++spins) {
      unsigned v = 1u;
      if (l < n) v = __hip_atomic_load((gu32*)f + (l / WPL) * 32 + l % WPL, RLX_AGENT);
      if (__all(v != 0u) || spin_timeout(spins, tmo)) break;
      if (GT && spins == 0) gate(tl);
      else __builtin_amdgcn_s_sleep(SLP);
    }
    if (GT) tl = now10() + GT;
    if (sw && l == 0) *sw = val;
  } else {
    for (unsigned spins = 0; *sw < val; ++spins) {
      if (spins > 64u * SPIN_CAP) break;
      if (FGATE && GT && spins == 0) gate(tl);
      __builtin_amdgcn_s_sleep(SLP_F);
    }
    if (FGATE && GT) tl = now10() + GT;
  }
  asm volatile("" ::: "memory");
}
template <int SLP, int WPL>
__device__ __forceinline__ bool wg_wait(const unsigned* f, int n, unsigned* tmo) {
  if (threadIdx.x < 64) (void)wait_n<SLP, WPL>(f, n, tmo);
  __syncthreads();
  asm volatile("" ::: "memory");
  return true;
}
__device__ __forceinline__ void set_flag(unsigned* f) { __hip_atomic_store((gu32*)f, 1u, RLX_AGENT); }
#define DRAIN() asm volatile("s_waitcnt vmcnt(0)" ::: "memory")
#define MKRS(ptr, bytes) __builtin_amdgcn_make_buffer_rsrc((void*)(ptr), 0, (int)(bytes), 0x00020000)
typedef unsigned v2u __attribute__((ext_vector_type(2)));
#define LD128(rs, voff, soff) __builtin_amdgcn_raw_buffer_load_b128(rs, voff, soff, AUX_SC1)
#define LD64(rs, voff, soff) __builtin_amdgcn_raw_buffer_load_b64(rs, voff, soff, AUX_SC1)
#define ST128(v, rs, voff, soff) ST128A(v, rs, voff, soff, AUX_SC1)
#define ST128A(v, rs, voff, soff, aux)                                        \
  do {                                                                        \
    const v4u st_d_ = (v);                                                    \
    __builtin_amdgcn_raw_buffer_store_b128(st_d_, rs, voff, soff, aux);       \
    asm volatile("s_nop 1" ::"v"(st_d_) : "memory");                          \
  } while (0)
#define ST64(v, rs, voff, soff) __builtin_amdgcn_raw_buffer_store_b64(v, rs, voff, soff, AUX_SC1)
#define BF8(x) __builtin_bit_cast(bf16x8, x)
#define MFMA16(a, b, c) __builtin_amdgcn_mfma_f32_16x16x32_bf16(a, b, c, 0, 0, 0)
typedef short bf16x4 __attribute__((ext_vector_type(4)));
#define MFMA16K(a, b, c) \
  __builtin_amdgcn_mfma_f32_16x16x16bf16_1k(__builtin_bit_cast(bf16x4, a), __builtin_bit_cast(bf16x4, b), c, 0, 0, 0)
__device__ __forceinline__ float fmax_nn(float a, float b) { return __builtin_elementwise_maximum(a, b); }
typedef __bf16 bfv8 __attribute__((ext_vector_type(8)));
typedef __bf16 bfv16 __attribute__((ext_vector_type(16)));
typedef unsigned v8u __attribute__((ext_vector_type(8)));
#define SMF(a, b, c, idx) \
  __builtin_amdgcn_smfmac_f32_16x16x64_bf16(__builtin_bit_cast(bfv8, a), __builtin_bit_cast(bfv16, b), c, idx, 0, 0)
__device__ __forceinline__ float swap_add32(float a, float b) {
  auto r = __builtin_amdgcn_permlane32_swap(__float_as_uint(a), __float_as_uint(b), false, false);
  return __uint_as_float(r[0]) + __uint_as_float(r[1]);
}
__device__ __forceinline__ float swap_add16(float a, float b) {
  auto r = __builtin_amdgcn_permlane16_swap(__float_as_uint(a), __float_as_uint(b), false, false);
  return __uint_as_float(r[0]) + __uint_as_float(r[1]);
}
__device__ __forceinline__ v4u smf_fix(const v4u d) {
  auto x = __builtin_amdgcn_permlane16_swap(d.x, d.z, false, false);
  auto y = __builtin_amdgcn_permlane16_swap(d.y, d.w, false, false);
  return v4u{x[0], x[1], y[0], y[1]};
}
__device__ __forceinline__ v8u smf_bfrag(const u16* p) {
  const v2u a = *reinterpret_cast<const v2u*>(p), b = *reinterpret_cast<const v2u*>(p + 8);
  const v2u c = *reinterpret_cast<const v2u*>(p + 32), d = *reinterpret_cast<const v2u*>(p + 40);
  return v8u{a.x, b.x, a.y, b.y, c.x, d.x, c.y, d.y};
}

#define VAR_PPA 0
#define VAR_PPB 0
#define VAR_THR 0
#define VAR_THR2 0
#define CV_LEAD 100
#define VAR_EARLY 1
#define VAR_DRAIN 1
#define VAR_D1 0
#define VAR_D2 0
#define VAR_DR2 1
#define VAR_S1 0
#define VAR_S2 0
typedef __attribute__((address_space(1))) unsigned long long gu64;
typedef __attribute__((address_space(1))) char gch;
__device__ __forceinline__ int cvt_rpi(float x) {
  int r;
  asm("v_cvt_rpi_i32_f32 %0, %1" : "=v"(r) : "v"(x));
  return r;
}
typedef unsigned long long u64;
__device__ __forceinline__ u64 call_tag(const MegaArgs& A) {
  u64 t = *(const __attribute__((address_space(4))) u64*)(A.ws + OFF_EP) * 6364136223846793005ull + 1442695040888963407ull;
  asm volatile("" : "+s"(t)::"memory");
  return t;
}
__device__ __forceinline__ u64 tagged(const u64 tag, const unsigned v) { return (tag & 0xffffffffffffull) | (u64)(v >> 16) << 48; }
__device__ __forceinline__ bool has_tag(const u64 tag, const unsigned lo, const unsigned hi) {
  return lo == (unsigned)tag && (hi & 0xffffu) == ((unsigned)(tag >> 32) & 0xffffu);
}
__device__ __forceinline__ u64 ready_look(const MegaArgs& A) {
  return __hip_atomic_load((gu64*)(A.ws + OFF_RD) + (blockIdx.x & 7) * 64 + lane_id(), RLX_AGENT);
}
#define RD_ASK(v) \
  asm volatile("global_load_dwordx2 %0, %1, %2 sc1" : "=v"(v) : "v"(lane_id() * 8), "s"(A.ws + OFF_RD + (blockIdx.x & 7) * 512) : "memory")
__device__ __forceinline__ void ready_wait(const MegaArgs& A, const u64 tag, u64 v) {
  for (unsigned spins = 0; !__all(v == tag) && spins < SPIN_CAP; ++spins) {
    __builtin_amdgcn_s_sleep(8);
    v = ready_look(A);
  }
  asm volatile("" ::: "memory");
}
typedef short v4s __attribute__((ext_vector_type(4)));
typedef __attribute__((address_space(3))) v4s* lp4s;
__device__ __forceinline__ void glds16(const void* sbase, const unsigned voff, const unsigned lds, const bool nt = false) {
  unsigned keep;
#define GLDS16(M)                                                                                                                     \
  asm volatile("s_nop 4\n\ts_mov_b32 %0, m0\n\ts_mov_b32 m0, %3\n\ts_nop 0\n\tglobal_load_lds_dwordx4 %1, %2" M "\n\ts_mov_b32 m0, %0" \
               : "=&s"(keep)                                                                                                          \
               : "v"(voff), "s"(sbase), "s"(lds)                                                                                      \
               : "memory")
  if (nt) GLDS16(" nt");
  else GLDS16("");
#undef GLDS16
}
__device__ __forceinline__ void glds16c(const void* sbase, const unsigned voff, const unsigned lds) {
  unsigned keep;
  asm volatile("s_nop 4\n\ts_mov_b32 %0, m0\n\ts_mov_b32 m0, %3\n\ts_nop 0\n\tglobal_load_lds_dwordx4 %1, %2 sc1\n\ts_mov_b32 m0, %0"
               : "=&s"(keep)
               : "v"(voff), "s"(sbase), "s"(lds)
               : "memory");
}
__device__ __forceinline__ void glds64c(const void* sbase, const unsigned voff, const unsigned lds) {
  unsigned keep;
  asm volatile("s_nop 4\n\ts_mov_b32 %0, m0\n\ts_mov_b32 m0, %3\n\ts_nop 0\n\tglobal_load_lds_dwordx4 %1, %2 sc1\n\t"
               "global_load_lds_dwordx4 %1, %2 offset:1024 sc1\n\tglobal_load_lds_dwordx4 %1, %2 offset:2048 sc1\n\t"
               "global_load_lds_dwordx4 %1, %2 offset:3072 sc1\n\ts_mov_b32 m0, %0"
               : "=&s"(keep)
               : "v"(voff), "s"(sbase), "s"(lds)
               : "memory");
}
__device__ __forceinline__ v2u trd(const unsigned a) {
  return __builtin_bit_cast(v2u, __builtin_amdgcn_ds_read_tr16_b64_v4i16((lp4s)(unsigned long)a));
}
__device__ __forceinline__ unsigned lds_base(char* smem) { return (unsigned)(unsigned long)(__attribute__((address_space(3))) char*)smem; }
#define RAW_BAR()                                           \
  do {                                                      \
    asm volatile("s_waitcnt lgkmcnt(0)" ::: "memory");      \
    __builtin_amdgcn_s_barrier();                           \
    asm volatile("" ::: "memory");                          \
  } while (0)
#define WAIT_VM(n) asm volatile("s_waitcnt vmcnt(" #n ")" ::: "memory")
template <bool SP>
__device__ __forceinline__ void wqkv_issue(const u16* Wm, const int hf, const int h, const int w, const int l, const unsigned sb, const bool nt) {
  const char* base = (const char*)Wm + (size_t)(256 * hf + 32 * w) * (HD * 2) + h * (DH * 2);
#define WQKV(NT_)                                                                                                       \
  _Pragma("unroll") for (int i = 0; i < 8; ++i) {                                                                      \
    const int r = l >> 4;                                                                                              \
    const int f = SP ? ((r & 1) | ((i & 1) << 1) | (((i >> 1) & 1) << 2)) : (r | (((i >> 1) & 1) << 2));               \
    glds16(base, (unsigned)((4 * i + r) * (HD * 2) + (((l & 15) ^ (f << 1)) << 4)),                                    \
           sb + (w >> 2) * 65536 + hf * 32768 + (w & 3) * 8192 + i * 1024, NT_);                                       \
  }
  if (nt) { WQKV(true) } else { WQKV(false) }
#undef WQKV
}

__device__ __forceinline__ unsigned row_stats(const v4u raw) {
  float v[8];
#pragma unroll
  for (int e = 0; e < 4; ++e) { v[2 * e] = lo16(raw[e]); v[2 * e + 1] = hi16(raw[e]); }
  float s = 0.f;
#pragma unroll
  for (int e = 0; e < 8; ++e) s += v[e];
  const float mean = wave_sum(s) * (1.0f / DM);
  float q = 0.f;
#pragma unroll
  for (int e = 0; e < 8; ++e) { const float t = v[e] - mean; q += t * t; }
  const float var = wave_sum(q) * (1.0f / DM);
  const float sd = bfr(sqrtf(bfr(bfr(var) + 1e-5f)));
  return (unsigned)f2bf(mean) | ((unsigned)f2bf(sd) << 16);
}
__device__ __forceinline__ void ask_slow(const MegaArgs& A, const int k, unsigned (&t)[3]) {
  const int l = lane_id(), x = blockIdx.x & 7;
  const char *wq = (const char*)A.Wq, *wv = (const char*)A.Wv;
  asm("" : "+s"(wq), "+s"(wv));
#pragma unroll
  for (int m = 0; m < 3; ++m) {
    const char* W = m == 1 ? (const char*)A.Wk : ((m == 0) == (x < 4)) ? wv : wq;
    const int a = (int)((unsigned long)W >> 8) + 8 * (x & 3), h = k < 32 ? ((1 - a) & 3) + 4 * (k & 1) : (31 - a) & 31;
    const int r = k < 32 ? k >> 1 : k - 32;
    t[m] = __builtin_amdgcn_raw_buffer_load_b32(MKRS(W, (unsigned)k < 48u && h < 8 ? DM * HD * 2 : 0), (l >> 1) * (HD * 2) + (l & 1) * 128,
                                                (32 * (r & 15)) * (HD * 2) + (8 * (x & 3) + (h & 7)) * 256, 0);
  }
}
#define ASKED(t) asm volatile("" ::"v"(t[0]), "v"(t[1]), "v"(t[2]))
template <int N>
__device__ __forceinline__ u64 start_stats(const MegaArgs& A, const int idx) {
  const int w = A.wv, l = lane_id();
  const int r0 = N * (8 * idx + w);
  unsigned ta[3], tb[3];
  v4u raw[N];
#pragma unroll
  for (int i = 0; i < N; ++i) raw[i] = *reinterpret_cast<const v4u*>(A.x0 + (size_t)(r0 + i) * DM + l * 8);
  asm volatile("" ::: "memory");
  ask_slow(A, 16 * w + (idx & 3) + (idx >> 4) * N, ta);
  if (N > 4) ask_slow(A, 16 * w + (idx & 3) + 12, tb);
  unsigned tc[2] = {};
  if (N == 4 && AWH > 0) tc[0] = *reinterpret_cast<const unsigned*>(A.Wo + (size_t)idx * 65536 + 32768 + (64 * w + l) * 64);
  if (N == 4 && AWH > 1 && w >= 4) tc[1] = *reinterpret_cast<const unsigned*>(A.Wo + (size_t)idx * 65536 + (64 * w + l) * 64);
  const u64 tag = call_tag(A);
  unsigned stw = 0u;
#pragma unroll
  for (int i = 0; i < N; ++i) {
    const unsigned t = row_stats(raw[i]);
    if ((l >> 1) == i) stw = t;
  }
  if (l < 2 * N) __hip_atomic_store((gu64*)(A.ws + OFF_GT) + 2 * r0 + l, tagged(tag, (l & 1) ? stw : stw << 16), RLX_AGENT);
  SST(0);
  ASKED(ta);
  if (N > 4) ASKED(tb);
  asm volatile("" ::"v"(tc[0]), "v"(tc[1]));
  return tag;
}
#define MFMAH(a, b, c) __builtin_amdgcn_mfma_f32_16x16x32_f16(a, b, c, 0, 0, 0)
struct Cx {
  f32x2_t r, i;
};
__device__ __forceinline__ Cx cmul(const Cx a, const Cx b) { return Cx{a.r * b.r - a.i * b.i, a.r * b.i + a.i * b.r}; }
struct KGen {
  unsigned pa, pb, pc, pd;
  u16 pe;
  f32x2_t ar, ai;
  Cx C;
  char* st;
  v4u bh[8], bl[8];
  __device__ __forceinline__ void load(const MegaArgs& A, const int d, const int l) {
    pa = *reinterpret_cast<const unsigned*>(A.logAre + d * NST + 2 * l);
    pb = *reinterpret_cast<const unsigned*>(A.Aim + d * NST + 2 * l);
    pc = *reinterpret_cast<const unsigned*>(A.Cre + d * NST + 2 * l);
    pd = *reinterpret_cast<const unsigned*>(A.Cim + d * NST + 2 * l);
    pe = A.logdt[d];
  }
  __device__ __forceinline__ void setup() {
    const float dt = bfr(expf(bf2f(pe)));
    ar = f32x2_t{-bfr(expf(lo16(pa))) * dt, -bfr(expf(hi16(pa))) * dt};
    ai = f32x2_t{lo16(pb) * dt, hi16(pb) * dt};
    C = Cx{f32x2_t{lo16(pc) * dt, hi16(pc) * dt}, f32x2_t{lo16(pd) * dt, hi16(pd) * dt}};
  }
  __device__ __forceinline__ float scale() {
    const float b = 2.0f * wave_sum((fabsf(C.r.x) + fabsf(C.r.y)) + (fabsf(C.i.x) + fabsf(C.i.y)));
    const unsigned eb = max(__float_as_uint(b) >> 23, 25u);
    const float ks = __uint_as_float((268u - eb) << 23);
    C.r *= ks;
    C.i *= ks;
    return __uint_as_float((eb - 24u) << 23);
  }
  __device__ __forceinline__ Cx power(const float p) const {
    Cx z;
#pragma unroll
    for (int e = 0; e < 2; ++e) {
      const float x = ai[e] * p;
      const float f = __builtin_fmaf(x, 6.4206382e-9f, __builtin_fmaf(x, 0.15915494f, -__builtin_rintf(x * 0.15915494f)));
      const float m = __expf(ar[e] * p);
      z.r[e] = m * __builtin_amdgcn_cosf(f);
      z.i[e] = m * __builtin_amdgcn_sinf(f);
    }
    return z;
  }
  static __device__ __forceinline__ unsigned rest(const f32x2_t y, const unsigned h) {
    unsigned r;
    asm("v_fma_mixlo_f16 %0, %1, -1.0, %2 op_sel_hi:[1,0,0]" : "=v"(r) : "v"(h), "v"(y.x));
    asm("v_fma_mixhi_f16 %0, %1, -1.0, %2 op_sel:[1,0,0] op_sel_hi:[1,0,0]" : "+v"(r) : "v"(h), "v"(y.y));
    return r;
  }
  __device__ __forceinline__ char* rows(char* p, Cx& y, const Cx s, const int n) const {
#pragma unroll 1
    for (int i = 0; i < n; i += 2) {
#pragma unroll
      for (int k = 0; k < 2; ++k) {
        const unsigned h0 = pkh(y.r.x, y.r.y), h1 = pkh(y.i.x, y.i.y);
        *reinterpret_cast<v2u*>(p + 1040 * k) = v2u{h0, h1};
        *reinterpret_cast<v2u*>(p + 1040 * k + 512) = v2u{rest(y.r, h0), rest(y.i, h1)};
        y = cmul(y, s);
      }
      p += 2080;
    }
    return p;
  }
  __device__ __forceinline__ void get(v4u (&h)[8], v4u (&o)[8], const int l) const {
    const char* p = st + (l & 15) * 1040 + (l >> 4) * 16;
    asm volatile("" ::: "memory");
#pragma unroll
    for (int k = 0; k < 8; ++k) {
      h[k] = *reinterpret_cast<const v4u*>(p + 64 * k);
      o[k] = *reinterpret_cast<const v4u*>(p + 512 + 64 * k);
    }
    asm volatile("" ::: "memory");
  }
  Cx cy, cs;
  __device__ __forceinline__ void columns(const int l, const int j0, const int j1) {
    if (j0 == 0) {
      cs = power(16.f);
      cs.i = -cs.i;
      cy = Cx{f32x2_t{16384.f, 16384.f}, f32x2_t{0.f, 0.f}};
    }
    rows(st + l * 8 + 1040 * j0, cy, cs, j1 - j0);
  }
  template <bool COL>
  __device__ __forceinline__ f32x4 tile(const int l) const {
    v4u h[8], o[8];
    get(h, o, l);
    f32x4 d = {}, e = {}, f = {};
#define H8(x) __builtin_bit_cast(f16x8, x)
#define KMM(a, b, d) (COL ? MFMAH(H8(b), H8(a), d) : MFMAH(H8(a), H8(b), d))
#pragma unroll
    for (int k = 0; k < 8; ++k) {
      d = KMM(h[k], bh[k], d);
      e = KMM(o[k], bh[k], e);
      f = KMM(h[k], bl[k], f);
    }
#undef KMM
#undef H8
    return (d + (e + f)) * (1.0f / 8192);
  }
};
#define ACC_INIT 0xc9b26c86c9b26ca0ull
#define CHAIN_ASM_ATT \
  "s_mov_b32 s95, 272\n" \
  "s_mov_b32 s91, 0x4c7c2000\n" \
  "s_mov_b32 s85, 0xc9b26c9b\n" \
  "s_mov_b32 s92, 0xc9326c9b\n" \
  "s_mov_b32 s93, 0xc9326c9b\n" \
  "s_mov_b32 s78, 0x3e0293ee\n" \
  "s_mov_b32 s76, 0x33333333\n" \
  "s_mov_b32 s77, 0x33333333\n" \
  "s_lshl_b32 s98, s73, 10\n" \
  "s_add_u32 s98, s98, s74\n" \
  "s_add_u32 s98, s98, 131072\n" \
  "s_lshl_b32 s97, s73, 9\n" \
  "s_add_u32 s97, s97, s74\n" \
  "s_add_u32 s97, s97, 158720\n" \
  "s_lshl_b32 s99, s73, 10\n" \
  "v_mbcnt_lo_u32_b32 v48, -1, 0\n" \
  "v_mbcnt_hi_u32_b32 v48, -1, v48\n" \
  "s_add_u32 s88, s74, 157760\n" \
  "v_mov_b32 v59, s88\n" \
  "v_lshrrev_b32 v49, 2, v48\n" \
  "v_lshrrev_b32 v50, 4, v48\n" \
  "v_bitop3_b32 v50, v49, v50, 3 bitop3:0x28\n" \
  "v_lshl_add_u32 v8, v50, 3, s97\n" \
  "v_and_b32 v50, 3, v48\n" \
  "v_lshl_add_u32 v8, v50, 5, v8\n" \
  "v_and_b32 v49, 1, v49\n" \
  "v_lshl_add_u32 v8, v49, 8, v8\n" \
  "v_and_b32 v49, 8, v48\n" \
  "v_lshl_add_u32 v8, v49, 4, v8\n" \
  "v_lshrrev_b32 v49, 5, v48\n" \
  "v_add_u32 v10, s99, v1\n" \
  "s_lshr_b32 s88, s99, 2\n" \
  "s_add_u32 s88, s88, s74\n" \
  "s_add_u32 s88, s88, 139264\n" \
  "v_lshl_add_u32 v7, v49, 10, s88\n" \
  "v_and_b32 v49, 31, v48\n" \
  "v_lshl_add_u32 v7, v49, 3, v7\n" \
  "v_mov_b64 v[12:13], 0\n" \
  "v_mov_b64 v[14:15], 0\n" \
  "v_mov_b64 v[16:17], 0\n" \
  "v_mov_b64 v[18:19], 0\n" \
  "v_mov_b64 v[20:21], 0\n" \
  "v_mov_b64 v[22:23], 0\n" \
  "v_mov_b32 v34, 0xc9b26ca0\n" \
  "v_mov_b32 v35, 0xc9b26c86\n" \
  "s_mov_b32 s87, 0x40000\n" \
  "v_subrev_u32 v30, 64, v59\n" \
  "v_add_u32 v17, 0x10000, v28\n" \
  "v_xor_b32 v18, 128, v28\n" \
  "v_xor_b32 v19, 128, v17\n" \
  "ds_write_b32 v30, v12 offset:0\n" \
  "ds_write_b32 v30, v12 offset:32\n" \
  "ds_write_b32 v30, v12 offset:64\n" \
  "v_xor_b32 v20, 0, v29\n" \
  "v_xor_b32 v21, 32, v29\n" \
  "v_xor_b32 v22, 64, v29\n" \
  "v_xor_b32 v23, 96, v29\n" \
  "s_waitcnt vmcnt(12) lgkmcnt(0)\n" \
  "s_barrier\n" \
  "ds_read_b64_tr_b16 v[160:161], v20 offset:0\n" \
  "ds_read_b64_tr_b16 v[162:163], v20 offset:2048\n" \
  "ds_read_b64_tr_b16 v[168:169], v21 offset:0\n" \
  "ds_read_b64_tr_b16 v[170:171], v21 offset:2048\n" \
  "ds_read_b64_tr_b16 v[176:177], v22 offset:0\n" \
  "ds_read_b64_tr_b16 v[178:179], v22 offset:2048\n" \
  "ds_read_b64_tr_b16 v[184:185], v23 offset:0\n" \
  "ds_read_b64_tr_b16 v[186:187], v23 offset:2048\n" \
  "s_lshl_b32 s88, s73, 10\n" \
  "v_add_u32 v48, s88, v1\n" \
  "ds_read_b128 v[52:55], v48 offset:0\n" \
  "s_waitcnt lgkmcnt(0)\n" \
  "v_mov_b32 v11, 0\n" \
  "v_dot2c_f32_bf16 v11, v52, v52\n" \
  "v_dot2c_f32_bf16 v11, v53, v53\n" \
  "v_dot2c_f32_bf16 v11, v54, v54\n" \
  "v_dot2c_f32_bf16 v11, v55, v55\n" \
  "s_nop 0\n" \
  "s_nop 1\n" \
  "v_max_u32_dpp v11, v11, v11 row_ror:1 row_mask:0xf bank_mask:0xf\n" \
  "s_nop 1\n" \
  "v_max_u32_dpp v11, v11, v11 row_ror:2 row_mask:0xf bank_mask:0xf\n" \
  "s_nop 1\n" \
  "v_max_u32_dpp v11, v11, v11 row_ror:4 row_mask:0xf bank_mask:0xf\n" \
  "s_nop 1\n" \
  "v_max_u32_dpp v11, v11, v11 row_ror:8 row_mask:0xf bank_mask:0xf\n" \
  "v_mov_b32 v31, v11\n" \
  "s_nop 1\n" \
  "v_permlane16_swap_b32 v11, v31\n" \
  "v_max_u32 v11, v11, v31\n" \
  "v_mov_b32 v31, v11\n" \
  "s_nop 1\n" \
  "v_permlane32_swap_b32 v11, v31\n" \
  "v_max_u32 v11, v11, v31\n" \
  "s_mov_b64 exec, 1\n" \
  "ds_max_u32 v59, v11\n" \
  "s_mov_b64 exec, -1\n" \
  "s_waitcnt vmcnt(8) lgkmcnt(0)\n" \
  "s_barrier\n" \
  "ds_read_b64_tr_b16 v[164:165], v20 offset:4096\n" \
  "ds_read_b64_tr_b16 v[166:167], v20 offset:6144\n" \
  "ds_read_b64_tr_b16 v[172:173], v21 offset:4096\n" \
  "ds_read_b64_tr_b16 v[174:175], v21 offset:6144\n" \
  "ds_read_b64_tr_b16 v[180:181], v22 offset:4096\n" \
  "ds_read_b64_tr_b16 v[182:183], v22 offset:6144\n" \
  "ds_read_b64_tr_b16 v[188:189], v23 offset:4096\n" \
  "ds_read_b64_tr_b16 v[190:191], v23 offset:6144\n" \
  "s_add_u32 m0, s19, 0\n" \
  "s_nop 0\n" \
  "global_load_lds_dwordx4 v24, s[4:5] offset:0\n" \
  "global_load_lds_dwordx4 v25, s[4:5] offset:1024\n" \
  "global_load_lds_dwordx4 v26, s[4:5] offset:2048\n" \
  "global_load_lds_dwordx4 v27, s[4:5] offset:3072\n" \
  "s_add_u32 s4, s4, 0x100000\n" \
  "s_addc_u32 s5, s5, 0\n" \
  "v_mfma_f32_16x16x32_bf16 v[64:67], v[160:163], v[160:163], 0\n" \
  "v_mfma_f32_16x16x32_bf16 v[68:71], v[168:171], v[168:171], 0\n" \
  "v_mfma_f32_16x16x32_bf16 v[72:75], v[176:179], v[176:179], 0\n" \
  "v_mfma_f32_16x16x32_bf16 v[76:79], v[184:187], v[184:187], 0\n" \
  "s_waitcnt vmcnt(8) lgkmcnt(0)\n" \
  "s_barrier\n" \
  "ds_read_b64_tr_b16 v[32:33], v20 offset:8192\n" \
  "ds_read_b64_tr_b16 v[34:35], v20 offset:10240\n" \
  "ds_read_b64_tr_b16 v[40:41], v21 offset:8192\n" \
  "ds_read_b64_tr_b16 v[42:43], v21 offset:10240\n" \
  "ds_read_b64_tr_b16 v[48:49], v22 offset:8192\n" \
  "ds_read_b64_tr_b16 v[50:51], v22 offset:10240\n" \
  "ds_read_b64_tr_b16 v[56:57], v23 offset:8192\n" \
  "ds_read_b64_tr_b16 v[58:59], v23 offset:10240\n" \
  "s_add_u32 m0, s19, 4096\n" \
  "s_nop 0\n" \
  "global_load_lds_dwordx4 v24, s[4:5] offset:0\n" \
  "global_load_lds_dwordx4 v25, s[4:5] offset:1024\n" \
  "global_load_lds_dwordx4 v26, s[4:5] offset:2048\n" \
  "global_load_lds_dwordx4 v27, s[4:5] offset:3072\n" \
  "s_add_u32 s4, s4, 0x100000\n" \
  "s_addc_u32 s5, s5, 0\n" \
  "v_mfma_f32_16x16x32_bf16 v[64:67], v[164:167], v[164:167], v[64:67]\n" \
  "v_mfma_f32_16x16x32_bf16 v[68:71], v[172:175], v[172:175], v[68:71]\n" \
  "v_mfma_f32_16x16x32_bf16 v[72:75], v[180:183], v[180:183], v[72:75]\n" \
  "v_mfma_f32_16x16x32_bf16 v[76:79], v[188:191], v[188:191], v[76:79]\n" \
  "s_waitcnt vmcnt(8) lgkmcnt(0)\n" \
  "s_barrier\n" \
  "ds_read_b64_tr_b16 v[36:37], v20 offset:12288\n" \
  "ds_read_b64_tr_b16 v[38:39], v20 offset:14336\n" \
  "ds_read_b64_tr_b16 v[44:45], v21 offset:12288\n" \
  "ds_read_b64_tr_b16 v[46:47], v21 offset:14336\n" \
  "ds_read_b64_tr_b16 v[52:53], v22 offset:12288\n" \
  "ds_read_b64_tr_b16 v[54:55], v22 offset:14336\n" \
  "ds_read_b64_tr_b16 v[60:61], v23 offset:12288\n" \
  "ds_read_b64_tr_b16 v[62:63], v23 offset:14336\n" \
  "s_add_u32 m0, s19, 8192\n" \
  "s_nop 0\n" \
  "global_load_lds_dwordx4 v24, s[4:5] offset:0\n" \
  "global_load_lds_dwordx4 v25, s[4:5] offset:1024\n" \
  "global_load_lds_dwordx4 v26, s[4:5] offset:2048\n" \
  "global_load_lds_dwordx4 v27, s[4:5] offset:3072\n" \
  "s_add_u32 s4, s4, 0x100000\n" \
  "s_addc_u32 s5, s5, 0\n" \
  "v_mfma_f32_16x16x32_bf16 v[64:67], v[32:35], v[32:35], v[64:67]\n" \
  "v_mfma_f32_16x16x32_bf16 v[68:71], v[40:43], v[40:43], v[68:71]\n" \
  "v_mfma_f32_16x16x32_bf16 v[72:75], v[48:51], v[48:51], v[72:75]\n" \
  "v_mfma_f32_16x16x32_bf16 v[76:79], v[56:59], v[56:59], v[76:79]\n" \
  "s_waitcnt vmcnt(8) lgkmcnt(0)\n" \
  "s_barrier\n" \
  "s_add_u32 m0, s19, 12288\n" \
  "s_nop 0\n" \
  "global_load_lds_dwordx4 v24, s[4:5] offset:0\n" \
  "global_load_lds_dwordx4 v25, s[4:5] offset:1024\n" \
  "global_load_lds_dwordx4 v26, s[4:5] offset:2048\n" \
  "global_load_lds_dwordx4 v27, s[4:5] offset:3072\n" \
  "s_add_u32 s4, s4, 0x100000\n" \
  "s_addc_u32 s5, s5, 0\n" \
  "v_mfma_f32_16x16x32_bf16 v[64:67], v[36:39], v[36:39], v[64:67]\n" \
  "v_mfma_f32_16x16x32_bf16 v[68:71], v[44:47], v[44:47], v[68:71]\n" \
  "v_mfma_f32_16x16x32_bf16 v[72:75], v[52:55], v[52:55], v[72:75]\n" \
  "v_mfma_f32_16x16x32_bf16 v[76:79], v[60:63], v[60:63], v[76:79]\n" \
  "s_nop 7\n" \
  "v_max3_f32 v13, v64, v65, v66\n" \
  "v_max3_f32 v13, v13, v67, v68\n" \
  "v_max3_f32 v13, v13, v69, v70\n" \
  "v_max3_f32 v13, v13, v71, v72\n" \
  "v_max3_f32 v13, v13, v73, v74\n" \
  "v_max3_f32 v13, v13, v75, v76\n" \
  "v_max3_f32 v13, v13, v77, v78\n" \
  "v_max_f32 v13, v13, v79\n" \
  "s_nop 1\n" \
  "v_max_f32_dpp v13, v13, v13 row_ror:1 row_mask:0xf bank_mask:0xf\n" \
  "s_nop 1\n" \
  "v_max_f32_dpp v13, v13, v13 row_ror:2 row_mask:0xf bank_mask:0xf\n" \
  "s_nop 1\n" \
  "v_max_f32_dpp v13, v13, v13 row_ror:4 row_mask:0xf bank_mask:0xf\n" \
  "s_nop 1\n" \
  "v_max_f32_dpp v13, v13, v13 row_ror:8 row_mask:0xf bank_mask:0xf\n" \
  "v_mov_b32 v31, v13\n" \
  "s_nop 1\n" \
  "v_permlane16_swap_b32 v13, v31\n" \
  "v_max_f32 v13, v13, v31\n" \
  "v_mov_b32 v31, v13\n" \
  "s_nop 1\n" \
  "v_permlane32_swap_b32 v13, v31\n" \
  "v_max_f32 v13, v13, v31\n" \
  "s_mov_b64 exec, 1\n" \
  "ds_max_u32 v30, v13 offset:32\n" \
  "s_mov_b64 exec, -1\n" \
  "s_waitcnt vmcnt(8) lgkmcnt(0)\n" \
  "s_barrier\n" \
  "s_add_u32 m0, s19, 0\n" \
  "s_nop 0\n" \
  "global_load_lds_dwordx4 v24, s[6:7] offset:0\n" \
  "global_load_lds_dwordx4 v25, s[6:7] offset:1024\n" \
  "global_load_lds_dwordx4 v26, s[6:7] offset:2048\n" \
  "global_load_lds_dwordx4 v27, s[6:7] offset:3072\n" \
  "s_add_u32 s6, s6, 0x100000\n" \
  "s_addc_u32 s7, s7, 0\n" \
  "s_waitcnt vmcnt(8) lgkmcnt(0)\n" \
  "s_barrier\n" \
  "s_add_u32 m0, s19, 4096\n" \
  "s_nop 0\n" \
  "global_load_lds_dwordx4 v24, s[6:7] offset:0\n" \
  "global_load_lds_dwordx4 v25, s[6:7] offset:1024\n" \
  "global_load_lds_dwordx4 v26, s[6:7] offset:2048\n" \
  "global_load_lds_dwordx4 v27, s[6:7] offset:3072\n" \
  "s_add_u32 s6, s6, 0x100000\n" \
  "s_addc_u32 s7, s7, 0\n" \
  "s_waitcnt vmcnt(8) lgkmcnt(0)\n" \
  "s_barrier\n" \
  "s_add_u32 m0, s19, 8192\n" \
  "s_nop 0\n" \
  "global_load_lds_dwordx4 v24, s[6:7] offset:0\n" \
  "global_load_lds_dwordx4 v25, s[6:7] offset:1024\n" \
  "global_load_lds_dwordx4 v26, s[6:7] offset:2048\n" \
  "global_load_lds_dwordx4 v27, s[6:7] offset:3072\n" \
  "s_add_u32 s6, s6, 0x100000\n" \
  "s_addc_u32 s7, s7, 0\n" \
  "s_waitcnt vmcnt(8) lgkmcnt(0)\n" \
  "s_barrier\n" \
  "ds_read_b64_tr_b16 v[64:65], v28 offset:0\n" \
  "ds_read_b64_tr_b16 v[66:67], v28 offset:512\n" \
  "ds_read_b64_tr_b16 v[68:69], v28 offset:32768\n" \
  "ds_read_b64_tr_b16 v[70:71], v28 offset:33280\n" \
  "ds_read_b64_tr_b16 v[72:73], v17 offset:0\n" \
  "ds_read_b64_tr_b16 v[74:75], v17 offset:512\n" \
  "ds_read_b64_tr_b16 v[76:77], v17 offset:32768\n" \
  "ds_read_b64_tr_b16 v[78:79], v17 offset:33280\n" \
  "ds_read_b64_tr_b16 v[192:193], v18 offset:0\n" \
  "ds_read_b64_tr_b16 v[194:195], v18 offset:512\n" \
  "ds_read_b64_tr_b16 v[196:197], v18 offset:32768\n" \
  "ds_read_b64_tr_b16 v[198:199], v18 offset:33280\n" \
  "ds_read_b64_tr_b16 v[200:201], v19 offset:0\n" \
  "ds_read_b64_tr_b16 v[202:203], v19 offset:512\n" \
  "ds_read_b64_tr_b16 v[204:205], v19 offset:32768\n" \
  "ds_read_b64_tr_b16 v[206:207], v19 offset:33280\n" \
  "s_add_u32 m0, s19, 12288\n" \
  "s_nop 0\n" \
  "global_load_lds_dwordx4 v24, s[6:7] offset:0\n" \
  "global_load_lds_dwordx4 v25, s[6:7] offset:1024\n" \
  "global_load_lds_dwordx4 v26, s[6:7] offset:2048\n" \
  "global_load_lds_dwordx4 v27, s[6:7] offset:3072\n" \
  "s_add_u32 s6, s6, 0x100000\n" \
  "s_addc_u32 s7, s7, 0\n" \
  "s_waitcnt vmcnt(8) lgkmcnt(0)\n" \
  "s_barrier\n" \
  "ds_read_b64_tr_b16 v[80:81], v28 offset:4096\n" \
  "ds_read_b64_tr_b16 v[82:83], v28 offset:4608\n" \
  "ds_read_b64_tr_b16 v[84:85], v28 offset:36864\n" \
  "ds_read_b64_tr_b16 v[86:87], v28 offset:37376\n" \
  "ds_read_b64_tr_b16 v[88:89], v17 offset:4096\n" \
  "ds_read_b64_tr_b16 v[90:91], v17 offset:4608\n" \
  "ds_read_b64_tr_b16 v[92:93], v17 offset:36864\n" \
  "ds_read_b64_tr_b16 v[94:95], v17 offset:37376\n" \
  "ds_read_b64_tr_b16 v[208:209], v18 offset:4096\n" \
  "ds_read_b64_tr_b16 v[210:211], v18 offset:4608\n" \
  "ds_read_b64_tr_b16 v[212:213], v18 offset:36864\n" \
  "ds_read_b64_tr_b16 v[214:215], v18 offset:37376\n" \
  "ds_read_b64_tr_b16 v[216:217], v19 offset:4096\n" \
  "ds_read_b64_tr_b16 v[218:219], v19 offset:4608\n" \
  "ds_read_b64_tr_b16 v[220:221], v19 offset:36864\n" \
  "ds_read_b64_tr_b16 v[222:223], v19 offset:37376\n" \
  "s_add_u32 m0, s19, 0\n" \
  "s_nop 0\n" \
  "global_load_lds_dwordx4 v24, s[8:9] offset:0\n" \
  "global_load_lds_dwordx4 v25, s[8:9] offset:1024\n" \
  "global_load_lds_dwordx4 v26, s[8:9] offset:2048\n" \
  "global_load_lds_dwordx4 v27, s[8:9] offset:3072\n" \
  "s_add_u32 s8, s8, 0x100000\n" \
  "s_addc_u32 s9, s9, 0\n" \
  "s_waitcnt vmcnt(8) lgkmcnt(0)\n" \
  "s_barrier\n" \
  "ds_read_b64_tr_b16 v[96:97], v28 offset:8192\n" \
  "ds_read_b64_tr_b16 v[98:99], v28 offset:8704\n" \
  "ds_read_b64_tr_b16 v[100:101], v28 offset:40960\n" \
  "ds_read_b64_tr_b16 v[102:103], v28 offset:41472\n" \
  "ds_read_b64_tr_b16 v[104:105], v17 offset:8192\n" \
  "ds_read_b64_tr_b16 v[106:107], v17 offset:8704\n" \
  "ds_read_b64_tr_b16 v[108:109], v17 offset:40960\n" \
  "ds_read_b64_tr_b16 v[110:111], v17 offset:41472\n" \
  "ds_read_b64_tr_b16 v[224:225], v18 offset:8192\n" \
  "ds_read_b64_tr_b16 v[226:227], v18 offset:8704\n" \
  "ds_read_b64_tr_b16 v[228:229], v18 offset:40960\n" \
  "ds_read_b64_tr_b16 v[230:231], v18 offset:41472\n" \
  "ds_read_b64_tr_b16 v[232:233], v19 offset:8192\n" \
  "ds_read_b64_tr_b16 v[234:235], v19 offset:8704\n" \
  "ds_read_b64_tr_b16 v[236:237], v19 offset:40960\n" \
  "ds_read_b64_tr_b16 v[238:239], v19 offset:41472\n" \
  "s_add_u32 m0, s19, 4096\n" \
  "s_nop 0\n" \
  "global_load_lds_dwordx4 v24, s[8:9] offset:0\n" \
  "global_load_lds_dwordx4 v25, s[8:9] offset:1024\n" \
  "global_load_lds_dwordx4 v26, s[8:9] offset:2048\n" \
  "global_load_lds_dwordx4 v27, s[8:9] offset:3072\n" \
  "s_add_u32 s8, s8, 0x100000\n" \
  "s_addc_u32 s9, s9, 0\n" \
  "s_waitcnt vmcnt(8) lgkmcnt(0)\n" \
  "s_barrier\n" \
  "ds_read_b64_tr_b16 v[112:113], v28 offset:12288\n" \
  "ds_read_b64_tr_b16 v[114:115], v28 offset:12800\n" \
  "ds_read_b64_tr_b16 v[116:117], v28 offset:45056\n" \
  "ds_read_b64_tr_b16 v[118:119], v28 offset:45568\n" \
  "ds_read_b64_tr_b16 v[120:121], v17 offset:12288\n" \
  "ds_read_b64_tr_b16 v[122:123], v17 offset:12800\n" \
  "ds_read_b64_tr_b16 v[124:125], v17 offset:45056\n" \
  "ds_read_b64_tr_b16 v[126:127], v17 offset:45568\n" \
  "ds_read_b64_tr_b16 v[240:241], v18 offset:12288\n" \
  "ds_read_b64_tr_b16 v[242:243], v18 offset:12800\n" \
  "ds_read_b64_tr_b16 v[244:245], v18 offset:45056\n" \
  "ds_read_b64_tr_b16 v[246:247], v18 offset:45568\n" \
  "ds_read_b64_tr_b16 v[248:249], v19 offset:12288\n" \
  "ds_read_b64_tr_b16 v[250:251], v19 offset:12800\n" \
  "ds_read_b64_tr_b16 v[252:253], v19 offset:45056\n" \
  "ds_read_b64_tr_b16 v[254:255], v19 offset:45568\n" \
  "s_add_u32 m0, s19, 8192\n" \
  "s_nop 0\n" \
  "global_load_lds_dwordx4 v24, s[8:9] offset:0\n" \
  "global_load_lds_dwordx4 v25, s[8:9] offset:1024\n" \
  "global_load_lds_dwordx4 v26, s[8:9] offset:2048\n" \
  "global_load_lds_dwordx4 v27, s[8:9] offset:3072\n" \
  "s_add_u32 s8, s8, 0x100000\n" \
  "s_addc_u32 s9, s9, 0\n" \
  "s_waitcnt vmcnt(8) lgkmcnt(0)\n" \
  "s_barrier\n" \
  "ds_read_b64_tr_b16 v[128:129], v28 offset:0\n" \
  "ds_read_b64_tr_b16 v[130:131], v28 offset:512\n" \
  "ds_read_b64_tr_b16 v[132:133], v28 offset:32768\n" \
  "ds_read_b64_tr_b16 v[134:135], v28 offset:33280\n" \
  "ds_read_b64_tr_b16 v[136:137], v17 offset:0\n" \
  "ds_read_b64_tr_b16 v[138:139], v17 offset:512\n" \
  "ds_read_b64_tr_b16 v[140:141], v17 offset:32768\n" \
  "ds_read_b64_tr_b16 v[142:143], v17 offset:33280\n" \
  "s_add_u32 m0, s19, 12288\n" \
  "s_nop 0\n" \
  "global_load_lds_dwordx4 v24, s[8:9] offset:0\n" \
  "global_load_lds_dwordx4 v25, s[8:9] offset:1024\n" \
  "global_load_lds_dwordx4 v26, s[8:9] offset:2048\n" \
  "global_load_lds_dwordx4 v27, s[8:9] offset:3072\n" \
  "s_add_u32 s8, s8, 0x100000\n" \
  "s_addc_u32 s9, s9, 0\n" \
  "s_waitcnt vmcnt(8) lgkmcnt(0)\n" \
  "s_barrier\n" \
  "ds_write_b128 v6, v[160:163] offset:0\n" \
  "ds_write_b128 v6, v[164:167] offset:1024\n" \
  "ds_write_b128 v6, v[168:171] offset:2048\n" \
  "ds_write_b128 v6, v[172:175] offset:3072\n" \
  "ds_read_b64_tr_b16 v[144:145], v28 offset:4096\n" \
  "ds_read_b64_tr_b16 v[146:147], v28 offset:4608\n" \
  "ds_read_b64_tr_b16 v[148:149], v28 offset:36864\n" \
  "ds_read_b64_tr_b16 v[150:151], v28 offset:37376\n" \
  "ds_read_b64_tr_b16 v[152:153], v17 offset:4096\n" \
  "ds_read_b64_tr_b16 v[154:155], v17 offset:4608\n" \
  "ds_read_b64_tr_b16 v[156:157], v17 offset:36864\n" \
  "ds_read_b64_tr_b16 v[158:159], v17 offset:37376\n" \
  "s_waitcnt vmcnt(4) lgkmcnt(0)\n" \
  "s_barrier\n" \
  "ds_write_b128 v6, v[176:179] offset:4096\n" \
  "ds_write_b128 v6, v[180:183] offset:5120\n" \
  "ds_write_b128 v6, v[184:187] offset:6144\n" \
  "ds_write_b128 v6, v[188:191] offset:7168\n" \
  "ds_read_b64_tr_b16 v[160:161], v28 offset:8192\n" \
  "ds_read_b64_tr_b16 v[162:163], v28 offset:8704\n" \
  "ds_read_b64_tr_b16 v[164:165], v28 offset:40960\n" \
  "ds_read_b64_tr_b16 v[166:167], v28 offset:41472\n" \
  "ds_read_b64_tr_b16 v[168:169], v17 offset:8192\n" \
  "ds_read_b64_tr_b16 v[170:171], v17 offset:8704\n" \
  "ds_read_b64_tr_b16 v[172:173], v17 offset:40960\n" \
  "ds_read_b64_tr_b16 v[174:175], v17 offset:41472\n" \
  "s_waitcnt vmcnt(0) lgkmcnt(0)\n" \
  "s_barrier\n" \
  "ds_write_b128 v6, v[32:35] offset:8192\n" \
  "ds_write_b128 v6, v[36:39] offset:9216\n" \
  "ds_write_b128 v6, v[40:43] offset:10240\n" \
  "ds_write_b128 v6, v[44:47] offset:11264\n" \
  "ds_read_b64_tr_b16 v[176:177], v28 offset:12288\n" \
  "ds_read_b64_tr_b16 v[178:179], v28 offset:12800\n" \
  "ds_read_b64_tr_b16 v[180:181], v28 offset:45056\n" \
  "ds_read_b64_tr_b16 v[182:183], v28 offset:45568\n" \
  "ds_read_b64_tr_b16 v[184:185], v17 offset:12288\n" \
  "ds_read_b64_tr_b16 v[186:187], v17 offset:12800\n" \
  "ds_read_b64_tr_b16 v[188:189], v17 offset:45056\n" \
  "ds_read_b64_tr_b16 v[190:191], v17 offset:45568\n" \
  "ds_read_b32 v31, v30 offset:0\n" \
  "s_waitcnt lgkmcnt(0)\n" \
  "s_barrier\n" \
  "ds_write_b128 v6, v[48:51] offset:12288\n" \
  "ds_write_b128 v6, v[52:55] offset:13312\n" \
  "ds_write_b128 v6, v[56:59] offset:14336\n" \
  "ds_write_b128 v6, v[60:63] offset:15360\n" \
  "v_mul_f32 v31, 0x3f8e353f, v31\n" \
  "v_mul_f32 v30, 0x44870000, v31\n" \
  "v_mov_b64 v[12:13], 0\n" \
  "v_mov_b64 v[14:15], 0\n" \
  "v_mov_b64 v[16:17], 0\n" \
  "v_mov_b64 v[18:19], 0\n" \
  "v_mov_b64 v[20:21], 0\n" \
  "v_mov_b64 v[22:23], 0\n" \
  "v_readfirstlane_b32 s72, v31\n" \
  "v_readfirstlane_b32 s86, v30\n" \
  "s_lshr_b32 s86, s86, 23\n" \
  "s_addk_i32 s86, 0xff83\n" \
  "s_ashr_i32 s86, s86, 1\n" \
  "s_max_i32 s86, s86, -20\n" \
  "s_min_i32 s86, s86, 100\n" \
  "s_mov_b32 s82, s86\n" \
  "s_lshr_b32 s83, s72, 23\n" \
  "s_sub_i32 s83, s83, 243\n" \
  "s_waitcnt lgkmcnt(0)\n" \
  "s_add_u32 s88, s74, 157760\n" \
  "v_mov_b32 v59, s88\n" \
  "v_mov_b32 v34, 0xc9b26ca0\n" \
  "v_mov_b32 v35, 0xc9b26c86\n" \
  ".p2align 6\n" \
  "s_nop 0\n" \
  "s_nop 0\n" \
  "s_nop 0\n" \
  ".LCA_TOP_%=:\n" \
  "s_waitcnt lgkmcnt(0)\n" \
  "s_barrier\n" \
  "ds_read_b128 v[40:43], v1 offset:0\n" \
  "ds_read_b128 v[44:47], v1 offset:1024\n" \
  "ds_read_b128 v[48:51], v1 offset:2048\n" \
  "ds_read_b128 v[52:55], v1 offset:3072\n" \
  "ds_read_b128 v[56:59], v1 offset:4096\n" \
  "ds_read_b128 v[60:63], v1 offset:5120\n" \
  "s_cmp_lg_u32 s73, 0\n" \
  "s_cbranch_scc1 .LCA_NOZ_%=\n" \
  "s_xor_b32 s88, s60, 0x10000\n" \
  "s_add_u32 s88, s88, s61\n" \
  "buffer_store_dwordx2 v[34:35], v5, s[40:43], s88 offen sc1\n" \
  ".LCA_NOZ_%=:\n" \
  "s_waitcnt lgkmcnt(5)\n" \
  "v_smfmac_f32_16x16x64_bf16 v[20:23], v[40:43], v[192:199], v0\n" \
  "v_smfmac_f32_16x16x64_bf16 v[12:15], v[40:43], v[64:71], v0\n" \
  "v_smfmac_f32_16x16x64_bf16 v[16:19], v[40:43], v[128:135], v0\n" \
  "ds_read_b128 v[40:43], v1 offset:6144\n" \
  "s_waitcnt lgkmcnt(5)\n" \
  "v_smfmac_f32_16x16x64_bf16 v[20:23], v[44:47], v[200:207], v0\n" \
  "v_smfmac_f32_16x16x64_bf16 v[12:15], v[44:47], v[72:79], v0\n" \
  "v_smfmac_f32_16x16x64_bf16 v[16:19], v[44:47], v[136:143], v0\n" \
  "ds_read_b128 v[44:47], v1 offset:7168\n" \
  "s_waitcnt lgkmcnt(5)\n" \
  "v_smfmac_f32_16x16x64_bf16 v[20:23], v[48:51], v[208:215], v0\n" \
  "v_smfmac_f32_16x16x64_bf16 v[12:15], v[48:51], v[80:87], v0\n" \
  "v_smfmac_f32_16x16x64_bf16 v[16:19], v[48:51], v[144:151], v0\n" \
  "ds_read_b128 v[24:27], v1 offset:10240\n" \
  "ds_read_b128 v[28:31], v1 offset:14336\n" \
  "s_waitcnt lgkmcnt(6)\n" \
  "v_smfmac_f32_16x16x64_bf16 v[20:23], v[52:55], v[216:223], v0\n" \
  "v_smfmac_f32_16x16x64_bf16 v[12:15], v[52:55], v[88:95], v0\n" \
  "v_smfmac_f32_16x16x64_bf16 v[16:19], v[52:55], v[152:159], v0\n" \
  "ds_read_b128 v[32:35], v1 offset:11264\n" \
  "ds_read_b128 v[36:39], v1 offset:15360\n" \
  "s_waitcnt lgkmcnt(7)\n" \
  "v_smfmac_f32_16x16x64_bf16 v[20:23], v[56:59], v[224:231], v0\n" \
  "v_smfmac_f32_16x16x64_bf16 v[12:15], v[56:59], v[96:103], v0\n" \
  "v_smfmac_f32_16x16x64_bf16 v[16:19], v[56:59], v[160:167], v0\n" \
  "ds_read_b128 v[56:59], v1 offset:12288\n" \
  "s_waitcnt lgkmcnt(7)\n" \
  "v_smfmac_f32_16x16x64_bf16 v[20:23], v[60:63], v[232:239], v0\n" \
  "v_smfmac_f32_16x16x64_bf16 v[12:15], v[60:63], v[104:111], v0\n" \
  "v_smfmac_f32_16x16x64_bf16 v[16:19], v[60:63], v[168:175], v0\n" \
  "ds_read_b128 v[60:63], v1 offset:16384\n" \
  "s_waitcnt lgkmcnt(7)\n" \
  "v_smfmac_f32_16x16x64_bf16 v[20:23], v[40:43], v[240:247], v0\n" \
  "v_smfmac_f32_16x16x64_bf16 v[12:15], v[40:43], v[112:119], v0\n" \
  "v_smfmac_f32_16x16x64_bf16 v[16:19], v[40:43], v[176:183], v0\n" \
  "s_waitcnt lgkmcnt(6)\n" \
  "v_smfmac_f32_16x16x64_bf16 v[20:23], v[44:47], v[248:255], v0\n" \
  "v_smfmac_f32_16x16x64_bf16 v[12:15], v[44:47], v[120:127], v0\n" \
  "v_smfmac_f32_16x16x64_bf16 v[16:19], v[44:47], v[184:191], v0\n" \
  "s_cmp_eq_u32 s71, 0\n" \
  "v_permlane32_swap_b32 v20, v22\n" \
  "v_permlane32_swap_b32 v21, v23\n" \
  "v_pk_add_f32 v[20:21], v[20:21], v[22:23]\n" \
  "s_mov_b64 vcc, s[76:77]\n" \
  "v_permlane32_swap_b32 v12, v14\n" \
  "v_permlane32_swap_b32 v13, v15\n" \
  "v_cvt_pk_bf16_f32 v20, v20, v21\n" \
  "v_pk_add_f32 v[12:13], v[12:13], v[14:15]\n" \
  "v_permlane32_swap_b32 v16, v18\n" \
  "v_permlane32_swap_b32 v17, v19\n" \
  "ds_write_b32 v8, v20 offset:2048\n" \
  "v_cvt_pk_bf16_f32 v12, v12, v13\n" \
  "v_pk_add_f32 v[16:17], v[16:17], v[18:19]\n" \
  "s_nop 0\n" \
  "v_cvt_pk_bf16_f32 v13, v16, v17\n" \
  "ds_write_b64 v8, v[12:13]\n" \
  "s_setprio 1\n" \
  "s_waitcnt lgkmcnt(0)\n" \
  "s_barrier\n" \
  "s_cbranch_scc1 .LCA_LAST_%=\n" \
  "ds_read_b64_tr_b16 v[40:41], v2 offset:0\n" \
  "ds_read_b64_tr_b16 v[42:43], v3 offset:0\n" \
  "ds_read_b64_tr_b16 v[44:45], v2 offset:1024\n" \
  "ds_read_b64_tr_b16 v[46:47], v3 offset:1024\n" \
  "ds_read_b64_tr_b16 v[48:49], v2 offset:2048\n" \
  "ds_read_b64_tr_b16 v[50:51], v3 offset:2048\n" \
  "ds_read_b64_tr_b16 v[52:53], v2 offset:3072\n" \
  "ds_read_b64_tr_b16 v[54:55], v3 offset:3072\n" \
  "ds_read_b32 v11, v1 offset:26688\n" \
  "s_waitcnt lgkmcnt(7)\n" \
  "v_mfma_f32_16x16x32_bf16 v[12:15], v[24:27], v[40:43], 0\n" \
  "v_mfma_f32_16x16x32_bf16 v[16:19], v[28:31], v[40:43], 0\n" \
  "ds_read_b128 v[24:27], v1 offset:13312\n" \
  "ds_read_b128 v[28:31], v1 offset:17408\n" \
  "v_mfma_f32_16x16x32_bf16 v[20:23], v[40:43], v[40:43], 0\n" \
  "s_waitcnt lgkmcnt(7)\n" \
  "v_mfma_f32_16x16x32_bf16 v[12:15], v[32:35], v[44:47], v[12:15]\n" \
  "v_mfma_f32_16x16x32_bf16 v[16:19], v[36:39], v[44:47], v[16:19]\n" \
  "v_mfma_f32_16x16x32_bf16 v[20:23], v[44:47], v[44:47], v[20:23]\n" \
  "s_waitcnt lgkmcnt(2)\n" \
  "v_mfma_f32_16x16x32_bf16 v[12:15], v[56:59], v[48:51], v[12:15]\n" \
  "v_readfirstlane_b32 s86, v11\n" \
  "v_mfma_f32_16x16x32_bf16 v[16:19], v[60:63], v[48:51], v[16:19]\n" \
  "ds_read_b128 v[32:35], v6 offset:8192\n" \
  "ds_read_b128 v[36:39], v6 offset:9216\n" \
  "v_mfma_f32_16x16x32_bf16 v[20:23], v[48:51], v[48:51], v[20:23]\n" \
  "s_lshr_b32 s86, s86, 23\n" \
  "s_add_i32 s86, s86, s83\n" \
  "s_ashr_i32 s86, s86, 1\n" \
  "ds_read_b128 v[48:51], v10 offset:18432\n" \
  "v_mfma_f32_16x16x32_bf16 v[20:23], v[52:55], v[52:55], v[20:23]\n" \
  "s_max_i32 s86, s86, s82\n" \
  "s_min_i32 s86, s86, 100\n" \
  "s_lshl_b32 s86, s86, 23\n" \
  "s_sub_i32 s84, 0xc8800000, s86\n" \
  "s_waitcnt lgkmcnt(3)\n" \
  "v_mfma_f32_16x16x32_bf16 v[12:15], v[24:27], v[52:55], v[12:15]\n" \
  "v_mfma_f32_16x16x32_bf16 v[16:19], v[28:31], v[52:55], v[16:19]\n" \
  "ds_read_b128 v[52:55], v10 offset:22528\n" \
  "ds_read_b128 v[56:59], v6 offset:0\n" \
  "ds_read_b128 v[60:63], v6 offset:1024\n" \
  "ds_read_b128 v[24:27], v6 offset:2048\n" \
  "ds_read_b128 v[28:31], v6 offset:3072\n" \
  "v_cndmask_b32_dpp v20, v23, v22, vcc quad_perm:[2,3,0,1] row_mask:0xf bank_mask:0xf\n" \
  "v_mov_b32 v41, 0\n" \
  "v_mov_b32 v42, 0\n" \
  "v_cndmask_b32_dpp v12, v16, v12, vcc quad_perm:[2,3,0,1] row_mask:0xf bank_mask:0xf\n" \
  "v_cndmask_b32_dpp v13, v17, v13, vcc quad_perm:[2,3,0,1] row_mask:0xf bank_mask:0xf\n" \
  "v_cndmask_b32_dpp v14, v18, v14, vcc quad_perm:[2,3,0,1] row_mask:0xf bank_mask:0xf\n" \
  "v_cndmask_b32_dpp v15, v19, v15, vcc quad_perm:[2,3,0,1] row_mask:0xf bank_mask:0xf\n" \
  "v_maximum3_f32 v16, v20, v12, v13\n" \
  "v_maximum3_f32 v16, v16, v14, v15\n" \
  "s_nop 1\n" \
  "v_max_f32_dpp v16, v16, v16 quad_perm:[2,3,0,1] row_mask:0xf bank_mask:0xf\n" \
  "v_mov_b32 v17, v16\n" \
  "s_nop 1\n" \
  "v_permlane16_swap_b32 v16, v17\n" \
  "v_max_f32 v16, v16, v17\n" \
  "v_mov_b32 v17, v16\n" \
  "s_nop 1\n" \
  "v_permlane32_swap_b32 v16, v17\n" \
  "v_max_f32 v16, v16, v17\n" \
  "v_mul_f32 v16, s78, v16\n" \
  "v_pk_fma_f32 v[12:13], v[12:13], s[78:79], v[16:17] op_sel_hi:[1,0,0] neg_lo:[0,0,1] neg_hi:[0,0,1]\n" \
  "v_pk_fma_f32 v[14:15], v[14:15], s[78:79], v[16:17] op_sel_hi:[1,0,0] neg_lo:[0,0,1] neg_hi:[0,0,1]\n" \
  "v_fma_f32 v20, v20, s78, -v16\n" \
  "v_exp_f32 v12, v12\n" \
  "v_exp_f32 v13, v13\n" \
  "v_exp_f32 v14, v14\n" \
  "v_exp_f32 v15, v15\n" \
  "v_exp_f32 v20, v20\n" \
  "s_waitcnt lgkmcnt(4)\n" \
  "v_cvt_pk_bf16_f32 v44, v12, v13\n" \
  "v_cvt_pk_bf16_f32 v45, v14, v15\n" \
  "v_pk_add_f32 v[8:9], v[12:13], v[14:15]\n" \
  "v_mov_b32_dpp v46, v44 quad_perm:[2,3,0,1] row_mask:0xf bank_mask:0xf\n" \
  "v_mov_b32_dpp v47, v45 quad_perm:[2,3,0,1] row_mask:0xf bank_mask:0xf\n" \
  "v_add_f32 v10, v8, v20\n" \
  "v_add_f32 v10, v10, v9\n" \
  "v_mfma_f32_16x16x32_bf16 v[12:15], v[48:51], v[44:47], 0\n" \
  "v_mov_b32_dpp v21, v20 quad_perm:[2,3,0,1] row_mask:0xf bank_mask:0xf\n" \
  "v_add_f32_dpp v10, v10, v10 quad_perm:[2,3,0,1] row_mask:0xf bank_mask:0xf\n" \
  "v_mov_b32 v11, v10\n" \
  "v_mfma_f32_16x16x32_bf16 v[16:19], v[52:55], v[44:47], 0\n" \
  "s_setprio 3\n" \
  "s_barrier\n" \
  "ds_read_b64 v[22:23], v7 offset:0\n" \
  "s_nop 0\n" \
  "v_permlane16_swap_b32 v10, v11\n" \
  "v_add_f32 v10, v10, v11\n" \
  "v_mov_b32 v11, v10\n" \
  "v_mov_b64 v[8:9], s[92:93]\n" \
  "s_nop 0\n" \
  "v_permlane32_swap_b32 v10, v11\n" \
  "v_add_f32 v10, v10, v11\n" \
  "v_rcp_f32 v10, v10\n" \
  "s_nop 0\n" \
  "v_mul_f32 v10, s84, v10\n" \
  "v_pk_mul_f32 v[20:21], v[20:21], v[10:11] op_sel_hi:[1,0]\n" \
  "v_pk_mul_f32 v[12:13], v[12:13], v[10:11] op_sel_hi:[1,0]\n" \
  "v_pk_mul_f32 v[14:15], v[14:15], v[10:11] op_sel_hi:[1,0]\n" \
  "v_cvt_pk_bf16_f32 v40, v20, v21\n" \
  "v_pk_mul_f32 v[16:17], v[16:17], v[10:11] op_sel_hi:[1,0]\n" \
  "v_cvt_pk_bf16_f32 v43, v20, v21\n" \
  "v_pk_mul_f32 v[18:19], v[18:19], v[10:11] op_sel_hi:[1,0]\n" \
  "s_waitcnt lgkmcnt(0)\n" \
  "v_mfma_f32_16x16x16_bf16 v[12:15], v[22:23], v[40:41], v[12:15]\n" \
  "v_mfma_f32_16x16x16_bf16 v[16:19], v[22:23], v[42:43], v[16:19]\n" \
  "v_mov_b64 v[10:11], s[92:93]\n" \
  "ds_read_b128 v[48:51], v6 offset:10240\n" \
  "ds_read_b128 v[52:55], v6 offset:11264\n" \
  "v_mov_b64 v[20:21], s[92:93]\n" \
  "v_mov_b64 v[22:23], s[92:93]\n" \
  "s_nop 1\n" \
  "v_cvt_pk_bf16_f32 v40, v12, v13\n" \
  "v_cvt_pk_bf16_f32 v41, v14, v15\n" \
  "v_cvt_pk_bf16_f32 v42, v16, v17\n" \
  "ds_write_b64 v4, v[40:41]\n" \
  "v_cvt_pk_bf16_f32 v43, v18, v19\n" \
  "ds_write_b64 v4, v[42:43] offset:1024\n" \
  "v_mov_b64 v[12:13], s[92:93]\n" \
  "v_mov_b64 v[14:15], s[92:93]\n" \
  "v_mov_b64 v[16:17], s[92:93]\n" \
  "v_mov_b64 v[18:19], s[92:93]\n" \
  "s_setprio 0\n" \
  "s_waitcnt lgkmcnt(0)\n" \
  "s_barrier\n" \
  "ds_read_b128 v[40:43], v1 offset:8192\n" \
  "ds_read_b128 v[44:47], v1 offset:9216\n" \
  "s_waitcnt lgkmcnt(1)\n" \
  "v_smfmac_f32_16x16x64_bf16 v[12:15], v[40:43], v[56:63], v0\n" \
  "ds_read_b128 v[56:59], v6 offset:4096\n" \
  "ds_read_b128 v[60:63], v6 offset:5120\n" \
  "v_smfmac_f32_16x16x64_bf16 v[16:19], v[40:43], v[24:31], v0\n" \
  "ds_read_b128 v[24:27], v6 offset:6144\n" \
  "ds_read_b128 v[28:31], v6 offset:7168\n" \
  "s_waitcnt lgkmcnt(4)\n" \
  "v_smfmac_f32_16x16x64_bf16 v[12:15], v[44:47], v[32:39], v0\n" \
  "ds_read_b128 v[32:35], v6 offset:12288\n" \
  "ds_read_b128 v[36:39], v6 offset:13312\n" \
  "v_smfmac_f32_16x16x64_bf16 v[16:19], v[44:47], v[48:55], v0\n" \
  "ds_read_b128 v[48:51], v6 offset:14336\n" \
  "ds_read_b128 v[52:55], v6 offset:15360\n" \
  "s_nop 5\n" \
  "v_permlane32_swap_b32 v12, v17\n" \
  "v_permlane32_swap_b32 v13, v16\n" \
  "v_permlane32_swap_b32 v14, v19\n" \
  "v_permlane32_swap_b32 v15, v18\n" \
  "v_pk_add_f32 v[12:13], v[12:13], v[16:17] op_sel:[0,1] op_sel_hi:[1,0]\n" \
  "v_pk_add_f32 v[14:15], v[14:15], v[18:19] op_sel:[0,1] op_sel_hi:[1,0]\n" \
  "buffer_atomic_add_x2 v[12:13], v5, s[40:43], s56 offen\n" \
  "buffer_atomic_add_x2 v[14:15], v5, s[40:43], s57 offen\n" \
  "s_waitcnt lgkmcnt(6)\n" \
  "v_smfmac_f32_16x16x64_bf16 v[20:23], v[40:43], v[56:63], v0\n" \
  "s_waitcnt lgkmcnt(4)\n" \
  "v_smfmac_f32_16x16x64_bf16 v[8:11], v[40:43], v[24:31], v0\n" \
  "s_waitcnt lgkmcnt(2)\n" \
  "v_smfmac_f32_16x16x64_bf16 v[20:23], v[44:47], v[32:39], v0\n" \
  "s_waitcnt lgkmcnt(0)\n" \
  "v_smfmac_f32_16x16x64_bf16 v[8:11], v[44:47], v[48:55], v0\n" \
  "s_nop 7\n" \
  "v_permlane32_swap_b32 v20, v9\n" \
  "v_permlane32_swap_b32 v21, v8\n" \
  "v_permlane32_swap_b32 v22, v11\n" \
  "v_permlane32_swap_b32 v23, v10\n" \
  "v_pk_add_f32 v[20:21], v[20:21], v[8:9] op_sel:[0,1] op_sel_hi:[1,0]\n" \
  "v_pk_add_f32 v[22:23], v[22:23], v[10:11] op_sel:[0,1] op_sel_hi:[1,0]\n" \
  "buffer_atomic_add_x2 v[20:21], v5, s[40:43], s58 offen\n" \
  "buffer_atomic_add_x2 v[22:23], v5, s[40:43], s59 offen\n" \
  "s_memrealtime s[38:39]\n" \
  "v_mbcnt_lo_u32_b32 v48, -1, 0\n" \
  "v_mbcnt_hi_u32_b32 v48, -1, v48\n" \
  "s_add_u32 s88, s74, 157760\n" \
  "v_mov_b32 v59, s88\n" \
  "v_lshl_add_u32 v52, v48, 2, v59\n" \
  "ds_read_b32 v52, v52 offset:192\n" \
  "v_lshrrev_b32 v49, 3, v48\n" \
  "v_and_b32 v50, 7, v48\n" \
  "v_lshlrev_b32 v50, 4, v50\n" \
  "v_lshl_or_b32 v44, v49, 8, v50\n" \
  "s_mov_b32 s87, 0x40000\n" \
  "v_lshrrev_b32 v49, 2, v48\n" \
  "v_lshrrev_b32 v50, 4, v48\n" \
  "v_bitop3_b32 v50, v49, v50, 3 bitop3:0x28\n" \
  "v_lshl_add_u32 v8, v50, 3, s97\n" \
  "v_and_b32 v50, 3, v48\n" \
  "v_lshl_add_u32 v8, v50, 5, v8\n" \
  "v_and_b32 v49, 1, v49\n" \
  "v_lshl_add_u32 v8, v49, 8, v8\n" \
  "v_and_b32 v49, 8, v48\n" \
  "v_lshl_add_u32 v8, v49, 4, v8\n" \
  "v_lshrrev_b32 v49, 5, v48\n" \
  "v_add_u32 v10, s99, v1\n" \
  "s_lshr_b32 s88, s99, 2\n" \
  "s_add_u32 s88, s88, s74\n" \
  "s_add_u32 s88, s88, 139264\n" \
  "v_lshl_add_u32 v7, v49, 10, s88\n" \
  "v_and_b32 v49, 31, v48\n" \
  "v_lshl_add_u32 v7, v49, 3, v7\n" \
  "v_mov_b64 v[12:13], 0\n" \
  "v_mov_b64 v[14:15], 0\n" \
  "v_mov_b64 v[16:17], 0\n" \
  "v_mov_b64 v[18:19], 0\n" \
  "v_mov_b64 v[20:21], 0\n" \
  "v_mov_b64 v[22:23], 0\n" \
  "v_mov_b32 v34, 0xc9b26ca0\n" \
  "v_mov_b32 v35, 0xc9b26c86\n" \
  "v_and_b32 v50, 3, v48\n" \
  "v_lshl_add_u32 v40, v50, 2, s98\n" \
  "v_lshrrev_b32 v50, 4, v48\n" \
  "v_mad_u32_u24 v40, v50, s95, v40\n" \
  "v_bfe_u32 v49, v48, 3, 1\n" \
  "v_lshl_add_u32 v40, v49, 6, v40\n" \
  "v_bfe_u32 v49, v48, 2, 1\n" \
  "v_lshl_add_u32 v40, v49, 7, v40\n" \
  "v_xor_b32 v41, 16, v40\n" \
  "v_xor_b32 v42, 32, v40\n" \
  "v_xor_b32 v43, 48, v40\n" \
  "s_add_i32 s80, s86, 0x35000000\n" \
  "v_mov_b32 v11, 0\n" \
  "s_mov_b32 s89, s60\n" \
  "s_bitcmp1_b32 s60, 7\n" \
  "s_cselect_b32 s88, 0x10000, 0\n" \
  "s_bitset1_b32 s88, 7\n" \
  "s_xor_b32 s56, s56, s88\n" \
  "s_xor_b32 s57, s57, s88\n" \
  "s_xor_b32 s58, s58, s88\n" \
  "s_xor_b32 s59, s59, s88\n" \
  "s_xor_b32 s60, s60, s88\n" \
  "s_sub_u32 s71, s71, 1\n" \
  "s_waitcnt lgkmcnt(0)\n" \
  "s_sub_u32 s39, s38, 150\n" \
  "v_subrev_u32 v52, s39, v52\n" \
  "v_cmp_lt_u32 vcc, 0x1000, v52\n" \
  "s_cmp_eq_u64 vcc, 0\n" \
  "s_cbranch_scc1 .LCA_OKM_%=\n" \
  ".LCA_AGN_%=:\n" \
  "buffer_load_dword v52, v5, s[0:3], s66 offen sc1\n" \
  "s_sub_u32 s87, s87, 1\n" \
  "s_cbranch_scc1 .LCA_END_%=\n" \
  "s_waitcnt vmcnt(0)\n" \
  "v_cmp_eq_u32 vcc, 0, v52\n" \
  "v_subrev_u32 v52, s39, v52\n" \
  "s_cmp_eq_u64 vcc, 0\n" \
  "s_cbranch_scc1 .LCA_OKM_%=\n" \
  "s_sleep 4\n" \
  "s_branch .LCA_AGN_%=\n" \
  ".LCA_OKM_%=:\n" \
  "s_nop 1\n" \
  "v_max_i32_dpp v52, v52, v52 row_ror:1 row_mask:0xf bank_mask:0xf\n" \
  "s_nop 1\n" \
  "v_max_i32_dpp v52, v52, v52 row_ror:2 row_mask:0xf bank_mask:0xf\n" \
  "s_nop 1\n" \
  "v_max_i32_dpp v52, v52, v52 row_ror:4 row_mask:0xf bank_mask:0xf\n" \
  "s_nop 1\n" \
  "v_max_i32_dpp v52, v52, v52 row_ror:8 row_mask:0xf bank_mask:0xf\n" \
  "s_movk_i32 vcc_lo, 150\n" \
  "v_readlane_b32 s88, v52, 0\n" \
  "s_max_i32 vcc_lo, vcc_lo, s88\n" \
  "v_readlane_b32 s88, v52, 16\n" \
  "s_max_i32 vcc_lo, vcc_lo, s88\n" \
  "v_readlane_b32 s88, v52, 32\n" \
  "s_max_i32 vcc_lo, vcc_lo, s88\n" \
  "v_readlane_b32 s88, v52, 48\n" \
  "s_max_i32 vcc_lo, vcc_lo, s88\n" \
  "s_add_u32 s66, s66, 1024\n" \
  "s_add_u32 vcc_lo, vcc_lo, s39\n" \
  "s_add_u32 vcc_lo, vcc_lo, 23\n" \
  ".LCA_TW_%=:\n" \
  "s_memrealtime s[38:39]\n" \
  "s_waitcnt lgkmcnt(0)\n" \
  "s_sub_u32 s38, s38, vcc_lo\n" \
  "s_cmp_lt_i32 s38, 0\n" \
  "s_cbranch_scc1 .LCA_TW_%=\n" \
  "buffer_load_dwordx4 v[24:27], v44, s[40:43], s89 offen sc1\n" \
  "buffer_load_dwordx4 v[28:31], v44, s[40:43], s89 offen offset:2048 sc1\n" \
  ".LCA_SEE_%=:\n" \
  "s_waitcnt vmcnt(1)\n" \
  "v_cvt_f32_i32 v24, v24\n" \
  "v_cvt_f32_i32 v25, v25\n" \
  "v_cvt_f32_i32 v26, v26\n" \
  "v_cvt_f32_i32 v27, v27\n" \
  "v_max_f32 v45, |v24|, |v26|\n" \
  "v_pk_mul_f32 v[24:25], v[24:25], s[80:81] op_sel_hi:[1,0]\n" \
  "v_pk_mul_f32 v[26:27], v[26:27], s[80:81] op_sel_hi:[1,0]\n" \
  "s_nop 0\n" \
  "v_cvt_pk_bf16_f32 v32, v24, v26\n" \
  "v_cvt_pk_bf16_f32 v33, v25, v27\n" \
  "ds_write_b32 v40, v32\n" \
  "ds_write_b32 v41, v33\n" \
  "s_waitcnt vmcnt(0)\n" \
  "v_cvt_f32_i32 v28, v28\n" \
  "v_cvt_f32_i32 v30, v30\n" \
  "v_cvt_f32_i32 v29, v29\n" \
  "v_max3_f32 v45, v45, |v28|, |v30|\n" \
  "v_cvt_f32_i32 v31, v31\n" \
  "v_cmp_lt_f32 vcc, s91, v45\n" \
  "v_pk_mul_f32 v[28:29], v[28:29], s[80:81] op_sel_hi:[1,0]\n" \
  "v_pk_mul_f32 v[30:31], v[30:31], s[80:81] op_sel_hi:[1,0]\n" \
  "s_cmp_eq_u64 vcc, 0\n" \
  "s_cbranch_scc0 .LCA_POLL_%=\n" \
  "v_cvt_pk_bf16_f32 v36, v28, v30\n" \
  "v_cvt_pk_bf16_f32 v37, v29, v31\n" \
  "ds_write_b32 v42, v36\n" \
  "ds_write_b32 v43, v37\n" \
  "v_dot2c_f32_bf16 v11, v32, v32\n" \
  "v_dot2c_f32_bf16 v11, v33, v33\n" \
  "v_dot2c_f32_bf16 v11, v36, v36\n" \
  "v_dot2c_f32_bf16 v11, v37, v37\n" \
  "s_nop 1\n" \
  "s_nop 0\n" \
  "v_cmp_lt_f32 vcc, 4.0, v11\n" \
  "s_cbranch_vccz .LCA_TOP_%=\n" \
  "s_mov_b64 exec, vcc\n" \
  "ds_max_u32 v59, v11\n" \
  "s_mov_b64 exec, -1\n" \
  "s_branch .LCA_TOP_%=\n" \
  ".LCA_POLL_%=:\n" \
  "buffer_load_dwordx4 v[24:27], v44, s[40:43], s89 offen sc1\n" \
  "buffer_load_dwordx4 v[28:31], v44, s[40:43], s89 offen offset:2048 sc1\n" \
  "s_sub_u32 s87, s87, 1\n" \
  "s_cbranch_scc1 .LCA_END_%=\n" \
  "s_branch .LCA_SEE_%=\n" \
  ".LCA_LAST_%=:\n" \
  ".LCA_END_%=:\n" \
  "s_endpgm\n"
#define CHAIN_ASM_VO \
  "s_mov_b32 s95, 272\n" \
  "s_mov_b32 s91, 0x4c7c2000\n" \
  "s_mov_b32 s85, 0xc9b26c9b\n" \
  "s_mov_b32 s92, 0xc9326c9b\n" \
  "s_mov_b32 s93, 0xc9326c9b\n" \
  "s_mov_b32 s78, 0x3e0293ee\n" \
  "s_mov_b32 s76, 0x33333333\n" \
  "s_mov_b32 s77, 0x33333333\n" \
  "s_lshl_b32 s98, s73, 10\n" \
  "s_add_u32 s98, s98, s74\n" \
  "s_add_u32 s98, s98, 131072\n" \
  "s_lshl_b32 s97, s73, 9\n" \
  "s_add_u32 s97, s97, s74\n" \
  "s_add_u32 s97, s97, 158724\n" \
  "s_lshl_b32 s99, s73, 11\n" \
  "s_add_u32 s99, s99, s74\n" \
  "s_add_u32 s99, s99, 133120\n" \
  "s_lshl_b32 s94, s73, 4\n" \
  "s_add_u32 s94, s94, s74\n" \
  "s_add_u32 s94, s94, 157760\n" \
  "v_mbcnt_lo_u32_b32 v48, -1, 0\n" \
  "v_mbcnt_hi_u32_b32 v48, -1, v48\n" \
  "s_add_u32 s88, s74, 157760\n" \
  "v_mov_b32 v59, s88\n" \
  "s_sub_u32 s88, s73, 4\n" \
  "v_lshrrev_b32 v49, 2, v48\n" \
  "v_lshrrev_b32 v50, 4, v48\n" \
  "v_bitop3_b32 v50, v49, v50, 3 bitop3:0x28\n" \
  "v_lshl_add_u32 v4, v50, 3, s97\n" \
  "v_and_b32 v50, 3, v48\n" \
  "v_lshl_add_u32 v4, v50, 5, v4\n" \
  "v_and_b32 v49, 1, v49\n" \
  "v_lshl_add_u32 v4, v49, 8, v4\n" \
  "v_and_b32 v49, 8, v48\n" \
  "v_lshl_add_u32 v4, v49, 4, v4\n" \
  "v_and_b32 v51, 15, v48\n" \
  "v_bfe_u32 v49, v48, 4, 1\n" \
  "v_lshrrev_b32 v50, 5, v48\n" \
  "s_lshl_b32 s89, s88, 10\n" \
  "s_add_u32 s89, s89, s74\n" \
  "s_add_u32 s89, s89, 158720\n" \
  "v_lshrrev_b32 v9, 3, v51\n" \
  "v_mul_u32_u24 v6, 41, v9\n" \
  "v_xor_b32 v6, v6, v51\n" \
  "v_lshl_add_u32 v6, v6, 3, s89\n" \
  "v_lshl_add_u32 v6, v49, 6, v6\n" \
  "v_lshl_add_u32 v6, v50, 9, v6\n" \
  "v_xor_b32 v9, 0x90, v6\n" \
  "v_lshlrev_b32 v8, 4, v49\n" \
  "v_lshl_add_u32 v8, v51, 5, v8\n" \
  "v_lshl_add_u32 v8, v50, 3, v8\n" \
  "s_lshl_b32 s89, s88, 8\n" \
  "s_add_u32 s89, s89, s74\n" \
  "s_add_u32 s89, s89, 139264\n" \
  "v_and_b32 v49, 31, v48\n" \
  "v_lshl_add_u32 v5, v49, 3, s89\n" \
  "v_lshl_add_u32 v5, v50, 10, v5\n" \
  "v_lshrrev_b32 v49, 2, v51\n" \
  "v_and_b32 v50, 5, v51\n" \
  "v_and_or_b32 v49, v49, 2, v50\n" \
  "v_and_b32 v50, 48, v48\n" \
  "v_lshlrev_b32 v7, 3, v50\n" \
  "v_lshl_add_u32 v7, v49, 4, v7\n" \
  "v_bitop3_b32 v49, v51, 2, 2 bitop3:0x0c\n" \
  "v_lshl_add_u32 v7, v49, 29, v7\n" \
  "v_mov_b64 v[12:13], 0\n" \
  "v_mov_b64 v[14:15], 0\n" \
  "v_mov_b64 v[16:17], 0\n" \
  "v_mov_b64 v[18:19], 0\n" \
  "v_mov_b64 v[20:21], 0\n" \
  "v_mov_b64 v[22:23], 0\n" \
  "v_lshlrev_b32 v10, 4, v48\n" \
  "s_add_u32 s78, s74, 157952\n" \
  "v_lshl_add_u32 v52, v48, 2, v59\n" \
  "ds_write_b32 v52, v12 offset:192\n" \
  "s_movk_i32 s79, 105\n" \
  "s_movk_i32 s96, 25\n" \
  "s_mov_b32 s87, 0x40000\n" \
  "v_subrev_u32 v30, 64, v59\n" \
  "v_add_u32 v17, 0x10000, v28\n" \
  "v_xor_b32 v18, 128, v28\n" \
  "v_xor_b32 v19, 128, v17\n" \
  "ds_write_b32 v30, v12 offset:0\n" \
  "ds_write_b32 v30, v12 offset:32\n" \
  "ds_write_b32 v30, v12 offset:64\n" \
  "v_xor_b32 v20, 0, v29\n" \
  "v_xor_b32 v21, 32, v29\n" \
  "v_xor_b32 v22, 64, v29\n" \
  "v_xor_b32 v23, 96, v29\n" \
  "s_waitcnt vmcnt(12) lgkmcnt(0)\n" \
  "s_barrier\n" \
  "ds_read_b64_tr_b16 v[96:97], v20 offset:0\n" \
  "ds_read_b64_tr_b16 v[98:99], v20 offset:2048\n" \
  "ds_read_b64_tr_b16 v[104:105], v21 offset:0\n" \
  "ds_read_b64_tr_b16 v[106:107], v21 offset:2048\n" \
  "ds_read_b64_tr_b16 v[112:113], v22 offset:0\n" \
  "ds_read_b64_tr_b16 v[114:115], v22 offset:2048\n" \
  "ds_read_b64_tr_b16 v[120:121], v23 offset:0\n" \
  "ds_read_b64_tr_b16 v[122:123], v23 offset:2048\n" \
  "s_lshl_b32 s88, s73, 10\n" \
  "v_add_u32 v48, s88, v1\n" \
  "ds_read_b128 v[52:55], v48 offset:0\n" \
  "s_waitcnt lgkmcnt(0)\n" \
  "v_mov_b32 v11, 0\n" \
  "v_dot2c_f32_bf16 v11, v52, v52\n" \
  "v_dot2c_f32_bf16 v11, v53, v53\n" \
  "v_dot2c_f32_bf16 v11, v54, v54\n" \
  "v_dot2c_f32_bf16 v11, v55, v55\n" \
  "s_nop 0\n" \
  "s_nop 1\n" \
  "v_max_u32_dpp v11, v11, v11 row_ror:1 row_mask:0xf bank_mask:0xf\n" \
  "s_nop 1\n" \
  "v_max_u32_dpp v11, v11, v11 row_ror:2 row_mask:0xf bank_mask:0xf\n" \
  "s_nop 1\n" \
  "v_max_u32_dpp v11, v11, v11 row_ror:4 row_mask:0xf bank_mask:0xf\n" \
  "s_nop 1\n" \
  "v_max_u32_dpp v11, v11, v11 row_ror:8 row_mask:0xf bank_mask:0xf\n" \
  "v_mov_b32 v31, v11\n" \
  "s_nop 1\n" \
  "v_permlane16_swap_b32 v11, v31\n" \
  "v_max_u32 v11, v11, v31\n" \
  "v_mov_b32 v31, v11\n" \
  "s_nop 1\n" \
  "v_permlane32_swap_b32 v11, v31\n" \
  "v_max_u32 v11, v11, v31\n" \
  "s_mov_b64 exec, 1\n" \
  "ds_max_u32 v59, v11\n" \
  "s_mov_b64 exec, -1\n" \
  "s_waitcnt vmcnt(8) lgkmcnt(0)\n" \
  "s_barrier\n" \
  "ds_read_b64_tr_b16 v[100:101], v20 offset:4096\n" \
  "ds_read_b64_tr_b16 v[102:103], v20 offset:6144\n" \
  "ds_read_b64_tr_b16 v[108:109], v21 offset:4096\n" \
  "ds_read_b64_tr_b16 v[110:111], v21 offset:6144\n" \
  "ds_read_b64_tr_b16 v[116:117], v22 offset:4096\n" \
  "ds_read_b64_tr_b16 v[118:119], v22 offset:6144\n" \
  "ds_read_b64_tr_b16 v[124:125], v23 offset:4096\n" \
  "ds_read_b64_tr_b16 v[126:127], v23 offset:6144\n" \
  "s_add_u32 m0, s19, 0\n" \
  "s_nop 0\n" \
  "global_load_lds_dwordx4 v24, s[4:5] offset:0\n" \
  "global_load_lds_dwordx4 v25, s[4:5] offset:1024\n" \
  "global_load_lds_dwordx4 v26, s[4:5] offset:2048\n" \
  "global_load_lds_dwordx4 v27, s[4:5] offset:3072\n" \
  "s_add_u32 s4, s4, 0x100000\n" \
  "s_addc_u32 s5, s5, 0\n" \
  "v_mfma_f32_16x16x32_bf16 v[64:67], v[96:99], v[96:99], 0\n" \
  "v_mfma_f32_16x16x32_bf16 v[68:71], v[104:107], v[104:107], 0\n" \
  "v_mfma_f32_16x16x32_bf16 v[72:75], v[112:115], v[112:115], 0\n" \
  "v_mfma_f32_16x16x32_bf16 v[76:79], v[120:123], v[120:123], 0\n" \
  "s_waitcnt vmcnt(8) lgkmcnt(0)\n" \
  "s_barrier\n" \
  "ds_read_b64_tr_b16 v[32:33], v20 offset:8192\n" \
  "ds_read_b64_tr_b16 v[34:35], v20 offset:10240\n" \
  "ds_read_b64_tr_b16 v[40:41], v21 offset:8192\n" \
  "ds_read_b64_tr_b16 v[42:43], v21 offset:10240\n" \
  "ds_read_b64_tr_b16 v[48:49], v22 offset:8192\n" \
  "ds_read_b64_tr_b16 v[50:51], v22 offset:10240\n" \
  "ds_read_b64_tr_b16 v[56:57], v23 offset:8192\n" \
  "ds_read_b64_tr_b16 v[58:59], v23 offset:10240\n" \
  "s_add_u32 m0, s19, 4096\n" \
  "s_nop 0\n" \
  "global_load_lds_dwordx4 v24, s[4:5] offset:0\n" \
  "global_load_lds_dwordx4 v25, s[4:5] offset:1024\n" \
  "global_load_lds_dwordx4 v26, s[4:5] offset:2048\n" \
  "global_load_lds_dwordx4 v27, s[4:5] offset:3072\n" \
  "s_add_u32 s4, s4, 0x100000\n" \
  "s_addc_u32 s5, s5, 0\n" \
  "v_mfma_f32_16x16x32_bf16 v[64:67], v[100:103], v[100:103], v[64:67]\n" \
  "v_mfma_f32_16x16x32_bf16 v[68:71], v[108:111], v[108:111], v[68:71]\n" \
  "v_mfma_f32_16x16x32_bf16 v[72:75], v[116:119], v[116:119], v[72:75]\n" \
  "v_mfma_f32_16x16x32_bf16 v[76:79], v[124:127], v[124:127], v[76:79]\n" \
  "s_waitcnt vmcnt(8) lgkmcnt(0)\n" \
  "s_barrier\n" \
  "ds_read_b64_tr_b16 v[36:37], v20 offset:12288\n" \
  "ds_read_b64_tr_b16 v[38:39], v20 offset:14336\n" \
  "ds_read_b64_tr_b16 v[44:45], v21 offset:12288\n" \
  "ds_read_b64_tr_b16 v[46:47], v21 offset:14336\n" \
  "ds_read_b64_tr_b16 v[52:53], v22 offset:12288\n" \
  "ds_read_b64_tr_b16 v[54:55], v22 offset:14336\n" \
  "ds_read_b64_tr_b16 v[60:61], v23 offset:12288\n" \
  "ds_read_b64_tr_b16 v[62:63], v23 offset:14336\n" \
  "s_add_u32 m0, s19, 8192\n" \
  "s_nop 0\n" \
  "global_load_lds_dwordx4 v24, s[4:5] offset:0\n" \
  "global_load_lds_dwordx4 v25, s[4:5] offset:1024\n" \
  "global_load_lds_dwordx4 v26, s[4:5] offset:2048\n" \
  "global_load_lds_dwordx4 v27, s[4:5] offset:3072\n" \
  "s_add_u32 s4, s4, 0x100000\n" \
  "s_addc_u32 s5, s5, 0\n" \
  "v_mfma_f32_16x16x32_bf16 v[64:67], v[32:35], v[32:35], v[64:67]\n" \
  "v_mfma_f32_16x16x32_bf16 v[68:71], v[40:43], v[40:43], v[68:71]\n" \
  "v_mfma_f32_16x16x32_bf16 v[72:75], v[48:51], v[48:51], v[72:75]\n" \
  "v_mfma_f32_16x16x32_bf16 v[76:79], v[56:59], v[56:59], v[76:79]\n" \
  "s_waitcnt vmcnt(8) lgkmcnt(0)\n" \
  "s_barrier\n" \
  "ds_read_b64_tr_b16 v[128:129], v18 offset:0\n" \
  "ds_read_b64_tr_b16 v[130:131], v18 offset:512\n" \
  "ds_read_b64_tr_b16 v[132:133], v18 offset:32768\n" \
  "ds_read_b64_tr_b16 v[134:135], v18 offset:33280\n" \
  "ds_read_b64_tr_b16 v[136:137], v19 offset:0\n" \
  "ds_read_b64_tr_b16 v[138:139], v19 offset:512\n" \
  "ds_read_b64_tr_b16 v[140:141], v19 offset:32768\n" \
  "ds_read_b64_tr_b16 v[142:143], v19 offset:33280\n" \
  "ds_read_b64_tr_b16 v[192:193], v28 offset:0\n" \
  "ds_read_b64_tr_b16 v[194:195], v28 offset:512\n" \
  "ds_read_b64_tr_b16 v[196:197], v28 offset:32768\n" \
  "ds_read_b64_tr_b16 v[198:199], v28 offset:33280\n" \
  "ds_read_b64_tr_b16 v[200:201], v17 offset:0\n" \
  "ds_read_b64_tr_b16 v[202:203], v17 offset:512\n" \
  "ds_read_b64_tr_b16 v[204:205], v17 offset:32768\n" \
  "ds_read_b64_tr_b16 v[206:207], v17 offset:33280\n" \
  "s_add_u32 m0, s19, 12288\n" \
  "s_nop 0\n" \
  "global_load_lds_dwordx4 v24, s[4:5] offset:0\n" \
  "global_load_lds_dwordx4 v25, s[4:5] offset:1024\n" \
  "global_load_lds_dwordx4 v26, s[4:5] offset:2048\n" \
  "global_load_lds_dwordx4 v27, s[4:5] offset:3072\n" \
  "s_add_u32 s4, s4, 0x100000\n" \
  "s_addc_u32 s5, s5, 0\n" \
  "v_mfma_f32_16x16x32_bf16 v[64:67], v[36:39], v[36:39], v[64:67]\n" \
  "v_mfma_f32_16x16x32_bf16 v[68:71], v[44:47], v[44:47], v[68:71]\n" \
  "v_mfma_f32_16x16x32_bf16 v[72:75], v[52:55], v[52:55], v[72:75]\n" \
  "v_mfma_f32_16x16x32_bf16 v[76:79], v[60:63], v[60:63], v[76:79]\n" \
  "s_nop 7\n" \
  "v_max3_f32 v13, v64, v65, v66\n" \
  "v_max3_f32 v13, v13, v67, v68\n" \
  "v_max3_f32 v13, v13, v69, v70\n" \
  "v_max3_f32 v13, v13, v71, v72\n" \
  "v_max3_f32 v13, v13, v73, v74\n" \
  "v_max3_f32 v13, v13, v75, v76\n" \
  "v_max3_f32 v13, v13, v77, v78\n" \
  "v_max_f32 v13, v13, v79\n" \
  "s_nop 1\n" \
  "v_max_f32_dpp v13, v13, v13 row_ror:1 row_mask:0xf bank_mask:0xf\n" \
  "s_nop 1\n" \
  "v_max_f32_dpp v13, v13, v13 row_ror:2 row_mask:0xf bank_mask:0xf\n" \
  "s_nop 1\n" \
  "v_max_f32_dpp v13, v13, v13 row_ror:4 row_mask:0xf bank_mask:0xf\n" \
  "s_nop 1\n" \
  "v_max_f32_dpp v13, v13, v13 row_ror:8 row_mask:0xf bank_mask:0xf\n" \
  "v_mov_b32 v31, v13\n" \
  "s_nop 1\n" \
  "v_permlane16_swap_b32 v13, v31\n" \
  "v_max_f32 v13, v13, v31\n" \
  "v_mov_b32 v31, v13\n" \
  "s_nop 1\n" \
  "v_permlane32_swap_b32 v13, v31\n" \
  "v_max_f32 v13, v13, v31\n" \
  "s_mov_b64 exec, 1\n" \
  "ds_max_u32 v30, v13 offset:32\n" \
  "s_mov_b64 exec, -1\n" \
  "s_waitcnt vmcnt(8) lgkmcnt(0)\n" \
  "s_barrier\n" \
  "ds_read_b64_tr_b16 v[144:145], v18 offset:4096\n" \
  "ds_read_b64_tr_b16 v[146:147], v18 offset:4608\n" \
  "ds_read_b64_tr_b16 v[148:149], v18 offset:36864\n" \
  "ds_read_b64_tr_b16 v[150:151], v18 offset:37376\n" \
  "ds_read_b64_tr_b16 v[152:153], v19 offset:4096\n" \
  "ds_read_b64_tr_b16 v[154:155], v19 offset:4608\n" \
  "ds_read_b64_tr_b16 v[156:157], v19 offset:36864\n" \
  "ds_read_b64_tr_b16 v[158:159], v19 offset:37376\n" \
  "ds_read_b64_tr_b16 v[208:209], v28 offset:4096\n" \
  "ds_read_b64_tr_b16 v[210:211], v28 offset:4608\n" \
  "ds_read_b64_tr_b16 v[212:213], v28 offset:36864\n" \
  "ds_read_b64_tr_b16 v[214:215], v28 offset:37376\n" \
  "ds_read_b64_tr_b16 v[216:217], v17 offset:4096\n" \
  "ds_read_b64_tr_b16 v[218:219], v17 offset:4608\n" \
  "ds_read_b64_tr_b16 v[220:221], v17 offset:36864\n" \
  "ds_read_b64_tr_b16 v[222:223], v17 offset:37376\n" \
  "s_add_u32 m0, s19, 0\n" \
  "s_nop 0\n" \
  "global_load_lds_dwordx4 v24, s[6:7] offset:0\n" \
  "global_load_lds_dwordx4 v25, s[6:7] offset:1024\n" \
  "global_load_lds_dwordx4 v26, s[6:7] offset:2048\n" \
  "global_load_lds_dwordx4 v27, s[6:7] offset:3072\n" \
  "s_add_u32 s6, s6, 0x100000\n" \
  "s_addc_u32 s7, s7, 0\n" \
  "v_mfma_f32_16x16x32_bf16 v[64:67], v[128:131], v[128:131], 0\n" \
  "v_mfma_f32_16x16x32_bf16 v[68:71], v[192:195], v[192:195], 0\n" \
  "v_mfma_f32_16x16x32_bf16 v[64:67], v[132:135], v[132:135], v[64:67]\n" \
  "v_mfma_f32_16x16x32_bf16 v[68:71], v[196:199], v[196:199], v[68:71]\n" \
  "v_mfma_f32_16x16x32_bf16 v[64:67], v[136:139], v[136:139], v[64:67]\n" \
  "v_mfma_f32_16x16x32_bf16 v[68:71], v[200:203], v[200:203], v[68:71]\n" \
  "v_mfma_f32_16x16x32_bf16 v[64:67], v[140:143], v[140:143], v[64:67]\n" \
  "v_mfma_f32_16x16x32_bf16 v[68:71], v[204:207], v[204:207], v[68:71]\n" \
  "s_waitcnt vmcnt(8) lgkmcnt(0)\n" \
  "s_barrier\n" \
  "ds_read_b64_tr_b16 v[160:161], v18 offset:8192\n" \
  "ds_read_b64_tr_b16 v[162:163], v18 offset:8704\n" \
  "ds_read_b64_tr_b16 v[164:165], v18 offset:40960\n" \
  "ds_read_b64_tr_b16 v[166:167], v18 offset:41472\n" \
  "ds_read_b64_tr_b16 v[168:169], v19 offset:8192\n" \
  "ds_read_b64_tr_b16 v[170:171], v19 offset:8704\n" \
  "ds_read_b64_tr_b16 v[172:173], v19 offset:40960\n" \
  "ds_read_b64_tr_b16 v[174:175], v19 offset:41472\n" \
  "ds_read_b64_tr_b16 v[224:225], v28 offset:8192\n" \
  "ds_read_b64_tr_b16 v[226:227], v28 offset:8704\n" \
  "ds_read_b64_tr_b16 v[228:229], v28 offset:40960\n" \
  "ds_read_b64_tr_b16 v[230:231], v28 offset:41472\n" \
  "ds_read_b64_tr_b16 v[232:233], v17 offset:8192\n" \
  "ds_read_b64_tr_b16 v[234:235], v17 offset:8704\n" \
  "ds_read_b64_tr_b16 v[236:237], v17 offset:40960\n" \
  "ds_read_b64_tr_b16 v[238:239], v17 offset:41472\n" \
  "s_add_u32 m0, s19, 4096\n" \
  "s_nop 0\n" \
  "global_load_lds_dwordx4 v24, s[6:7] offset:0\n" \
  "global_load_lds_dwordx4 v25, s[6:7] offset:1024\n" \
  "global_load_lds_dwordx4 v26, s[6:7] offset:2048\n" \
  "global_load_lds_dwordx4 v27, s[6:7] offset:3072\n" \
  "s_add_u32 s6, s6, 0x100000\n" \
  "s_addc_u32 s7, s7, 0\n" \
  "v_mfma_f32_16x16x32_bf16 v[64:67], v[144:147], v[144:147], v[64:67]\n" \
  "v_mfma_f32_16x16x32_bf16 v[68:71], v[208:211], v[208:211], v[68:71]\n" \
  "v_mfma_f32_16x16x32_bf16 v[64:67], v[148:151], v[148:151], v[64:67]\n" \
  "v_mfma_f32_16x16x32_bf16 v[68:71], v[212:215], v[212:215], v[68:71]\n" \
  "v_mfma_f32_16x16x32_bf16 v[64:67], v[152:155], v[152:155], v[64:67]\n" \
  "v_mfma_f32_16x16x32_bf16 v[68:71], v[216:219], v[216:219], v[68:71]\n" \
  "v_mfma_f32_16x16x32_bf16 v[64:67], v[156:159], v[156:159], v[64:67]\n" \
  "v_mfma_f32_16x16x32_bf16 v[68:71], v[220:223], v[220:223], v[68:71]\n" \
  "s_waitcnt vmcnt(8) lgkmcnt(0)\n" \
  "s_barrier\n" \
  "ds_read_b64_tr_b16 v[176:177], v18 offset:12288\n" \
  "ds_read_b64_tr_b16 v[178:179], v18 offset:12800\n" \
  "ds_read_b64_tr_b16 v[180:181], v18 offset:45056\n" \
  "ds_read_b64_tr_b16 v[182:183], v18 offset:45568\n" \
  "ds_read_b64_tr_b16 v[184:185], v19 offset:12288\n" \
  "ds_read_b64_tr_b16 v[186:187], v19 offset:12800\n" \
  "ds_read_b64_tr_b16 v[188:189], v19 offset:45056\n" \
  "ds_read_b64_tr_b16 v[190:191], v19 offset:45568\n" \
  "ds_read_b64_tr_b16 v[240:241], v28 offset:12288\n" \
  "ds_read_b64_tr_b16 v[242:243], v28 offset:12800\n" \
  "ds_read_b64_tr_b16 v[244:245], v28 offset:45056\n" \
  "ds_read_b64_tr_b16 v[246:247], v28 offset:45568\n" \
  "ds_read_b64_tr_b16 v[248:249], v17 offset:12288\n" \
  "ds_read_b64_tr_b16 v[250:251], v17 offset:12800\n" \
  "ds_read_b64_tr_b16 v[252:253], v17 offset:45056\n" \
  "ds_read_b64_tr_b16 v[254:255], v17 offset:45568\n" \
  "s_add_u32 m0, s19, 8192\n" \
  "s_nop 0\n" \
  "global_load_lds_dwordx4 v24, s[6:7] offset:0\n" \
  "global_load_lds_dwordx4 v25, s[6:7] offset:1024\n" \
  "global_load_lds_dwordx4 v26, s[6:7] offset:2048\n" \
  "global_load_lds_dwordx4 v27, s[6:7] offset:3072\n" \
  "s_add_u32 s6, s6, 0x100000\n" \
  "s_addc_u32 s7, s7, 0\n" \
  "v_mfma_f32_16x16x32_bf16 v[64:67], v[160:163], v[160:163], v[64:67]\n" \
  "v_mfma_f32_16x16x32_bf16 v[68:71], v[224:227], v[224:227], v[68:71]\n" \
  "v_mfma_f32_16x16x32_bf16 v[64:67], v[164:167], v[164:167], v[64:67]\n" \
  "v_mfma_f32_16x16x32_bf16 v[68:71], v[228:231], v[228:231], v[68:71]\n" \
  "v_mfma_f32_16x16x32_bf16 v[64:67], v[168:171], v[168:171], v[64:67]\n" \
  "v_mfma_f32_16x16x32_bf16 v[68:71], v[232:235], v[232:235], v[68:71]\n" \
  "v_mfma_f32_16x16x32_bf16 v[64:67], v[172:175], v[172:175], v[64:67]\n" \
  "v_mfma_f32_16x16x32_bf16 v[68:71], v[236:239], v[236:239], v[68:71]\n" \
  "s_waitcnt vmcnt(8) lgkmcnt(0)\n" \
  "s_barrier\n" \
  "s_add_u32 m0, s19, 12288\n" \
  "s_nop 0\n" \
  "global_load_lds_dwordx4 v24, s[6:7] offset:0\n" \
  "global_load_lds_dwordx4 v25, s[6:7] offset:1024\n" \
  "global_load_lds_dwordx4 v26, s[6:7] offset:2048\n" \
  "global_load_lds_dwordx4 v27, s[6:7] offset:3072\n" \
  "s_add_u32 s6, s6, 0x100000\n" \
  "s_addc_u32 s7, s7, 0\n" \
  "v_mfma_f32_16x16x32_bf16 v[64:67], v[176:179], v[176:179], v[64:67]\n" \
  "v_mfma_f32_16x16x32_bf16 v[68:71], v[240:243], v[240:243], v[68:71]\n" \
  "v_mfma_f32_16x16x32_bf16 v[64:67], v[180:183], v[180:183], v[64:67]\n" \
  "v_mfma_f32_16x16x32_bf16 v[68:71], v[244:247], v[244:247], v[68:71]\n" \
  "v_mfma_f32_16x16x32_bf16 v[64:67], v[184:187], v[184:187], v[64:67]\n" \
  "v_mfma_f32_16x16x32_bf16 v[68:71], v[248:251], v[248:251], v[68:71]\n" \
  "v_mfma_f32_16x16x32_bf16 v[64:67], v[188:191], v[188:191], v[64:67]\n" \
  "v_mfma_f32_16x16x32_bf16 v[68:71], v[252:255], v[252:255], v[68:71]\n" \
  "s_nop 7\n" \
  "s_mov_b32 exec_lo, 0x100001\n" \
  "s_mov_b32 exec_hi, 0x10000100\n" \
  "v_add_f32 v12, v64, v68\n" \
  "s_mov_b32 exec_lo, 0x200002\n" \
  "s_mov_b32 exec_hi, 0x20000200\n" \
  "v_add_f32 v12, v65, v69\n" \
  "s_mov_b32 exec_lo, 0x400004\n" \
  "s_mov_b32 exec_hi, 0x40000400\n" \
  "v_add_f32 v12, v66, v70\n" \
  "s_mov_b32 exec_lo, 0x800008\n" \
  "s_mov_b32 exec_hi, 0x80000800\n" \
  "v_add_f32 v12, v67, v71\n" \
  "s_mov_b64 exec, -1\n" \
  "s_nop 1\n" \
  "v_add_f32_dpp v12, v12, v12 row_ror:1 row_mask:0xf bank_mask:0xf\n" \
  "s_nop 1\n" \
  "v_add_f32_dpp v12, v12, v12 row_ror:2 row_mask:0xf bank_mask:0xf\n" \
  "s_nop 1\n" \
  "v_add_f32_dpp v12, v12, v12 row_ror:4 row_mask:0xf bank_mask:0xf\n" \
  "s_nop 1\n" \
  "v_add_f32_dpp v12, v12, v12 row_ror:8 row_mask:0xf bank_mask:0xf\n" \
  "v_mov_b32 v31, v12\n" \
  "s_nop 1\n" \
  "v_permlane16_swap_b32 v12, v31\n" \
  "v_add_f32 v12, v12, v31\n" \
  "v_mov_b32 v31, v12\n" \
  "s_nop 1\n" \
  "v_permlane32_swap_b32 v12, v31\n" \
  "v_add_f32 v12, v12, v31\n" \
  "v_lshl_add_u32 v31, s73, 2, v30\n" \
  "s_mov_b64 exec, 1\n" \
  "ds_write_b32 v31, v12\n" \
  "s_mov_b64 exec, -1\n" \
  "s_waitcnt vmcnt(8) lgkmcnt(0)\n" \
  "s_barrier\n" \
  "s_add_u32 m0, s19, 0\n" \
  "s_nop 0\n" \
  "global_load_lds_dwordx4 v24, s[8:9] offset:0\n" \
  "global_load_lds_dwordx4 v25, s[8:9] offset:1024\n" \
  "global_load_lds_dwordx4 v26, s[8:9] offset:2048\n" \
  "global_load_lds_dwordx4 v27, s[8:9] offset:3072\n" \
  "s_add_u32 s8, s8, 0x100000\n" \
  "s_addc_u32 s9, s9, 0\n" \
  "ds_read_b128 v[20:23], v30 offset:16\n" \
  "ds_read_b32 v31, v30 offset:32\n" \
  "v_mbcnt_lo_u32_b32 v16, -1, 0\n" \
  "v_mbcnt_hi_u32_b32 v16, -1, v16\n" \
  "v_lshlrev_b32 v15, 8, v16\n" \
  "s_waitcnt lgkmcnt(0)\n" \
  "v_add_f32 v20, v20, v21\n" \
  "v_add_f32 v22, v22, v23\n" \
  "v_add_f32 v20, v20, v22\n" \
  "v_mul_f32 v31, v20, v31\n" \
  "v_mul_f32 v31, 0x3f8147ae, v31\n" \
  "v_mov_b32 v12, s10\n" \
  "v_mov_b32 v13, 0xffff\n" \
  "v_bfi_b32 v13, v13, s11, v31\n" \
  "s_mov_b32 exec_hi, 0\n" \
  "s_mov_b32 exec_lo, s18\n" \
  "global_store_dwordx2 v15, v[12:13], s[16:17] sc1\n" \
  "s_mov_b64 exec, -1\n" \
  "v_lshlrev_b32 v16, 3, v16\n" \
  "s_waitcnt vmcnt(8) lgkmcnt(0)\n" \
  "s_barrier\n" \
  "s_add_u32 m0, s19, 4096\n" \
  "s_nop 0\n" \
  "global_load_lds_dwordx4 v24, s[8:9] offset:0\n" \
  "global_load_lds_dwordx4 v25, s[8:9] offset:1024\n" \
  "global_load_lds_dwordx4 v26, s[8:9] offset:2048\n" \
  "global_load_lds_dwordx4 v27, s[8:9] offset:3072\n" \
  "s_add_u32 s8, s8, 0x100000\n" \
  "s_addc_u32 s9, s9, 0\n" \
  "global_load_dwordx2 v[20:21], v16, s[12:13] sc1\n" \
  "s_waitcnt vmcnt(9) lgkmcnt(0)\n" \
  "s_barrier\n" \
  "s_add_u32 m0, s19, 8192\n" \
  "s_nop 0\n" \
  "global_load_lds_dwordx4 v24, s[8:9] offset:0\n" \
  "global_load_lds_dwordx4 v25, s[8:9] offset:1024\n" \
  "global_load_lds_dwordx4 v26, s[8:9] offset:2048\n" \
  "global_load_lds_dwordx4 v27, s[8:9] offset:3072\n" \
  "s_add_u32 s8, s8, 0x100000\n" \
  "s_addc_u32 s9, s9, 0\n" \
  "s_waitcnt vmcnt(9) lgkmcnt(0)\n" \
  "s_barrier\n" \
  "ds_read_b64_tr_b16 v[64:65], v28 offset:0\n" \
  "ds_read_b64_tr_b16 v[66:67], v28 offset:512\n" \
  "ds_read_b64_tr_b16 v[68:69], v28 offset:32768\n" \
  "ds_read_b64_tr_b16 v[70:71], v28 offset:33280\n" \
  "ds_read_b64_tr_b16 v[72:73], v17 offset:0\n" \
  "ds_read_b64_tr_b16 v[74:75], v17 offset:512\n" \
  "ds_read_b64_tr_b16 v[76:77], v17 offset:32768\n" \
  "ds_read_b64_tr_b16 v[78:79], v17 offset:33280\n" \
  "s_add_u32 m0, s19, 12288\n" \
  "s_nop 0\n" \
  "global_load_lds_dwordx4 v24, s[8:9] offset:0\n" \
  "global_load_lds_dwordx4 v25, s[8:9] offset:1024\n" \
  "global_load_lds_dwordx4 v26, s[8:9] offset:2048\n" \
  "global_load_lds_dwordx4 v27, s[8:9] offset:3072\n" \
  "s_add_u32 s8, s8, 0x100000\n" \
  "s_addc_u32 s9, s9, 0\n" \
  "s_waitcnt vmcnt(8)\n" \
  ".LCV_RD0_%=:\n" \
  "v_cmp_ne_u32 vcc, s10, v20\n" \
  "v_xor_b32 v31, s11, v21\n" \
  "v_cmp_ne_u32 s[22:23], 0, v31\n" \
  "s_or_b64 vcc, vcc, s[22:23]\n" \
  "s_cbranch_scc0 .LCV_RD1_%=\n" \
  "s_sleep 8\n" \
  "global_load_dwordx2 v[20:21], v16, s[12:13] sc1\n" \
  "s_sub_u32 s87, s87, 1\n" \
  "s_cbranch_scc1 .LCV_END_%=\n" \
  "s_waitcnt vmcnt(0)\n" \
  "s_branch .LCV_RD0_%=\n" \
  ".LCV_RD1_%=:\n" \
  "s_waitcnt vmcnt(8) lgkmcnt(0)\n" \
  "s_barrier\n" \
  "ds_write_b128 v3, v[96:99] offset:0\n" \
  "ds_write_b128 v3, v[100:103] offset:1024\n" \
  "ds_write_b128 v3, v[104:107] offset:2048\n" \
  "ds_write_b128 v3, v[108:111] offset:3072\n" \
  "ds_read_b64_tr_b16 v[80:81], v28 offset:4096\n" \
  "ds_read_b64_tr_b16 v[82:83], v28 offset:4608\n" \
  "ds_read_b64_tr_b16 v[84:85], v28 offset:36864\n" \
  "ds_read_b64_tr_b16 v[86:87], v28 offset:37376\n" \
  "ds_read_b64_tr_b16 v[88:89], v17 offset:4096\n" \
  "ds_read_b64_tr_b16 v[90:91], v17 offset:4608\n" \
  "ds_read_b64_tr_b16 v[92:93], v17 offset:36864\n" \
  "ds_read_b64_tr_b16 v[94:95], v17 offset:37376\n" \
  "v_and_b32 v16, 0xff, v16\n" \
  "global_load_dwordx2 v[22:23], v16, s[14:15] sc1\n" \
  "s_mov_b64 exec, 15\n" \
  "buffer_load_dword v13, v2, s[44:47], s68 offen sc1\n" \
  "s_mov_b64 exec, -1\n" \
  "s_waitcnt vmcnt(6) lgkmcnt(0)\n" \
  "s_barrier\n" \
  "ds_write_b128 v3, v[112:115] offset:4096\n" \
  "ds_write_b128 v3, v[116:119] offset:5120\n" \
  "ds_write_b128 v3, v[120:123] offset:6144\n" \
  "ds_write_b128 v3, v[124:127] offset:7168\n" \
  "ds_read_b64_tr_b16 v[96:97], v28 offset:8192\n" \
  "ds_read_b64_tr_b16 v[98:99], v28 offset:8704\n" \
  "ds_read_b64_tr_b16 v[100:101], v28 offset:40960\n" \
  "ds_read_b64_tr_b16 v[102:103], v28 offset:41472\n" \
  "ds_read_b64_tr_b16 v[104:105], v17 offset:8192\n" \
  "ds_read_b64_tr_b16 v[106:107], v17 offset:8704\n" \
  "ds_read_b64_tr_b16 v[108:109], v17 offset:40960\n" \
  "ds_read_b64_tr_b16 v[110:111], v17 offset:41472\n" \
  "s_waitcnt vmcnt(0)\n" \
  "v_cmp_eq_u32 vcc, 0, v13\n" \
  "s_and_b32 s90, vcc_lo, 15\n" \
  "s_cbranch_scc1 .LCV_GL0_%=\n" \
  "s_mov_b32 m0, s99\n" \
  "s_add_u32 s88, s99, 8192\n" \
  "global_load_lds_dwordx4 v10, s[62:63] sc1\n" \
  "global_load_lds_dwordx4 v10, s[62:63] offset:1024 sc1\n" \
  "s_mov_b32 m0, s88\n" \
  "s_nop 0\n" \
  "global_load_lds_dwordx4 v10, s[64:65] sc1\n" \
  "global_load_lds_dwordx4 v10, s[64:65] offset:1024 sc1\n" \
  ".LCV_GL0_%=:\n" \
  "v_cmp_ne_u32 vcc, s10, v22\n" \
  "v_xor_b32 v31, s11, v23\n" \
  "v_and_b32 v31, 0xffff, v31\n" \
  "v_cmp_ne_u32 s[22:23], 0, v31\n" \
  "s_or_b64 vcc, vcc, s[22:23]\n" \
  "s_cbranch_scc0 .LCV_GL1_%=\n" \
  "s_sleep 12\n" \
  "global_load_dwordx2 v[22:23], v16, s[14:15] sc1\n" \
  "s_sub_u32 s87, s87, 1\n" \
  "s_cbranch_scc1 .LCV_END_%=\n" \
  "s_waitcnt vmcnt(0)\n" \
  "s_branch .LCV_GL0_%=\n" \
  ".LCV_GL1_%=:\n" \
  "v_and_b32 v31, 0xffff0000, v23\n" \
  "s_nop 1\n" \
  "v_max_u32_dpp v31, v31, v31 row_ror:1 row_mask:0xf bank_mask:0xf\n" \
  "s_nop 1\n" \
  "v_max_u32_dpp v31, v31, v31 row_ror:2 row_mask:0xf bank_mask:0xf\n" \
  "s_nop 1\n" \
  "v_max_u32_dpp v31, v31, v31 row_ror:4 row_mask:0xf bank_mask:0xf\n" \
  "s_nop 1\n" \
  "v_max_u32_dpp v31, v31, v31 row_ror:8 row_mask:0xf bank_mask:0xf\n" \
  "v_mov_b32 v12, v31\n" \
  "s_nop 1\n" \
  "v_permlane16_swap_b32 v31, v12\n" \
  "v_max_u32 v31, v31, v12\n" \
  "v_mov_b32 v12, v31\n" \
  "s_nop 1\n" \
  "v_permlane32_swap_b32 v31, v12\n" \
  "v_max_u32 v31, v31, v12\n" \
  "s_mov_b64 exec, 1\n" \
  "ds_write_b32 v30, v31\n" \
  "s_mov_b64 exec, -1\n" \
  "s_waitcnt vmcnt(4) lgkmcnt(0)\n" \
  "s_barrier\n" \
  "ds_write_b128 v3, v[32:35] offset:8192\n" \
  "ds_write_b128 v3, v[36:39] offset:9216\n" \
  "ds_write_b128 v3, v[40:43] offset:10240\n" \
  "ds_write_b128 v3, v[44:47] offset:11264\n" \
  "ds_read_b64_tr_b16 v[112:113], v28 offset:12288\n" \
  "ds_read_b64_tr_b16 v[114:115], v28 offset:12800\n" \
  "ds_read_b64_tr_b16 v[116:117], v28 offset:45056\n" \
  "ds_read_b64_tr_b16 v[118:119], v28 offset:45568\n" \
  "ds_read_b64_tr_b16 v[120:121], v17 offset:12288\n" \
  "ds_read_b64_tr_b16 v[122:123], v17 offset:12800\n" \
  "ds_read_b64_tr_b16 v[124:125], v17 offset:45056\n" \
  "ds_read_b64_tr_b16 v[126:127], v17 offset:45568\n" \
  "ds_read_b32 v31, v30 offset:0\n" \
  "s_waitcnt lgkmcnt(0)\n" \
  "s_barrier\n" \
  "ds_write_b128 v3, v[48:51] offset:12288\n" \
  "ds_write_b128 v3, v[52:55] offset:13312\n" \
  "ds_write_b128 v3, v[56:59] offset:14336\n" \
  "ds_write_b128 v3, v[60:63] offset:15360\n" \
  "v_mul_f32 v31, 0x3f8e353f, v31\n" \
  "v_mul_f32 v30, 0x44870000, v31\n" \
  "v_mov_b64 v[12:13], 0\n" \
  "v_mov_b64 v[14:15], 0\n" \
  "v_mov_b64 v[16:17], 0\n" \
  "v_mov_b64 v[18:19], 0\n" \
  "v_mov_b64 v[20:21], 0\n" \
  "v_mov_b64 v[22:23], 0\n" \
  "v_readfirstlane_b32 s72, v31\n" \
  "v_readfirstlane_b32 s86, v30\n" \
  "s_lshr_b32 s86, s86, 23\n" \
  "s_addk_i32 s86, 0xff83\n" \
  "s_ashr_i32 s86, s86, 1\n" \
  "s_max_i32 s86, s86, -20\n" \
  "s_min_i32 s86, s86, 100\n" \
  "s_mov_b32 s82, s86\n" \
  "s_lshr_b32 s83, s72, 23\n" \
  "s_sub_i32 s83, s83, 243\n" \
  "s_waitcnt lgkmcnt(0)\n" \
  "s_add_u32 s88, s74, 157760\n" \
  "v_mov_b32 v59, s88\n" \
  "s_cmp_lg_u32 s61, 0\n" \
  "s_movk_i32 s76, -8000\n" \
  "s_cmovk_i32 s76, 9152\n" \
  "s_movk_i32 s77, 90\n" \
  "s_cmovk_i32 s77, 90\n" \
  "s_cselect_b32 s72, -1, 55\n" \
  "s_mov_b32 s87, 0x40000\n" \
  "s_cmp_eq_u32 s90, 0\n" \
  "s_cbranch_scc1 .LCV_W1_%=\n" \
  "s_branch .LCV_W2_%=\n" \
  ".LCV_W0_%=:\n" \
  "s_sleep 2\n" \
  "s_cmp_eq_u32 s61, 0\n" \
  "s_cbranch_scc1 .LCV_W2_%=\n" \
  "s_sleep 32\n" \
  ".LCV_W2_%=:\n" \
  "s_mov_b64 exec, 15\n" \
  "buffer_load_dword v58, v2, s[44:47], s68 offen sc1\n" \
  "s_mov_b64 exec, -1\n" \
  "s_sub_u32 s87, s87, 1\n" \
  "s_cbranch_scc1 .LCV_END_%=\n" \
  "s_waitcnt vmcnt(0)\n" \
  "v_cmp_eq_u32 vcc, 0, v58\n" \
  "s_and_b32 vcc_lo, vcc_lo, 15\n" \
  "s_cbranch_scc1 .LCV_W0_%=\n" \
  "s_mov_b32 m0, s99\n" \
  "s_add_u32 s88, s99, 8192\n" \
  "global_load_lds_dwordx4 v10, s[62:63] sc1\n" \
  "global_load_lds_dwordx4 v10, s[62:63] offset:1024 sc1\n" \
  "s_mov_b32 m0, s88\n" \
  "s_nop 0\n" \
  "global_load_lds_dwordx4 v10, s[64:65] sc1\n" \
  "global_load_lds_dwordx4 v10, s[64:65] offset:1024 sc1\n" \
  ".LCV_W1_%=:\n" \
  "s_add_u32 s62, s62, 0x80000\n" \
  "s_addc_u32 s63, s63, 0\n" \
  "s_add_u32 s64, s64, 0x40000\n" \
  "s_addc_u32 s65, s65, 0\n" \
  "s_add_u32 s68, s68, 0x1000\n" \
  "s_cmp_eq_u32 s61, 0\n" \
  "s_cbranch_scc1 .LCV_W4_%=\n" \
  "s_add_u32 s38, s67, 4992\n" \
  "v_mov_b32 v58, s38\n" \
  "buffer_load_dword v58, v58, s[0:3], 0 offen sc1\n" \
  "s_waitcnt vmcnt(0)\n" \
  "v_readfirstlane_b32 vcc_lo, v58\n" \
  "s_movk_i32 s87, 200\n" \
  "s_cmp_eq_u32 vcc_lo, 0\n" \
  "s_cbranch_scc1 .LCV_W4_%=\n" \
  "s_add_u32 vcc_lo, vcc_lo, 590\n" \
  ".LCV_W3_%=:\n" \
  "s_sleep 2\n" \
  "s_memrealtime s[38:39]\n" \
  "s_sub_u32 s87, s87, 1\n" \
  "s_cbranch_scc1 .LCV_W4_%=\n" \
  "s_waitcnt lgkmcnt(0)\n" \
  "s_sub_u32 s38, s38, vcc_lo\n" \
  "s_cmp_lt_i32 s38, 0\n" \
  "s_cbranch_scc1 .LCV_W3_%=\n" \
  ".LCV_W4_%=:\n" \
  "s_waitcnt vmcnt(0)\n" \
  ".p2align 6\n" \
  ".LCV_TOP_%=:\n" \
  "s_waitcnt lgkmcnt(0)\n" \
  "s_barrier\n" \
  "s_setprio 3\n" \
  "ds_read_b128 v[24:27], v1 offset:0\n" \
  "ds_read_b128 v[28:31], v1 offset:1024\n" \
  "ds_read_b128 v[32:35], v1 offset:2048\n" \
  "ds_read_b128 v[36:39], v1 offset:3072\n" \
  "ds_read_b128 v[40:43], v1 offset:4096\n" \
  "ds_read_b128 v[44:47], v1 offset:5120\n" \
  "ds_read_b128 v[48:51], v1 offset:6144\n" \
  "ds_read_b128 v[52:55], v1 offset:7168\n" \
  "s_memrealtime s[38:39]\n" \
  "s_waitcnt lgkmcnt(7)\n" \
  "v_smfmac_f32_16x16x64_bf16 v[12:15], v[24:27], v[64:71], v0\n" \
  "s_waitcnt lgkmcnt(6)\n" \
  "v_smfmac_f32_16x16x64_bf16 v[12:15], v[28:31], v[72:79], v0\n" \
  "s_waitcnt lgkmcnt(5)\n" \
  "v_smfmac_f32_16x16x64_bf16 v[12:15], v[32:35], v[80:87], v0\n" \
  "s_waitcnt lgkmcnt(4)\n" \
  "v_smfmac_f32_16x16x64_bf16 v[12:15], v[36:39], v[88:95], v0\n" \
  "s_waitcnt lgkmcnt(3)\n" \
  "v_smfmac_f32_16x16x64_bf16 v[12:15], v[40:43], v[96:103], v0\n" \
  "s_waitcnt lgkmcnt(2)\n" \
  "v_smfmac_f32_16x16x64_bf16 v[12:15], v[44:47], v[104:111], v0\n" \
  "s_waitcnt lgkmcnt(1)\n" \
  "v_smfmac_f32_16x16x64_bf16 v[12:15], v[48:51], v[112:119], v0\n" \
  "s_waitcnt lgkmcnt(0)\n" \
  "v_smfmac_f32_16x16x64_bf16 v[12:15], v[52:55], v[120:127], v0\n" \
  "ds_read_b32 v11, v1 offset:26688\n" \
  "s_mov_b32 s61, s38\n" \
  "s_add_u32 s38, s38, s79\n" \
  "s_add_u32 s38, s38, s96\n" \
  "s_mov_b32 s96, 0\n" \
  "v_or_b32 v57, 1, s38\n" \
  "v_add_u32 v56, s67, v10\n" \
  "s_add_u32 s39, s67, s76\n" \
  "s_add_u32 s38, s38, s77\n" \
  "s_bitset1_b32 s38, 0\n" \
  "v_writelane_b32 v56, s39, 1\n" \
  "v_writelane_b32 v57, s38, 1\n" \
  "s_sub_u32 s39, s71, s72\n" \
  "s_cmp_lt_u32 s39, 9\n" \
  "s_cselect_b64 exec, 19, 3\n" \
  "buffer_store_dword v57, v56, s[0:3], 0 offen sc1\n" \
  "s_mov_b64 exec, -1\n" \
  "v_permlane32_swap_b32 v12, v14\n" \
  "v_permlane32_swap_b32 v13, v15\n" \
  "v_pk_add_f32 v[12:13], v[12:13], v[14:15]\n" \
  "s_nop 0\n" \
  "v_cvt_pk_bf16_f32 v12, v12, v13\n" \
  "ds_write_b32 v4, v12\n" \
  "s_setprio 2\n" \
  "v_mov_b64 v[12:13], s[92:93]\n" \
  "v_mov_b64 v[14:15], s[92:93]\n" \
  "s_waitcnt lgkmcnt(1)\n" \
  "v_readfirstlane_b32 s86, v11\n" \
  "s_lshr_b32 s86, s86, 23\n" \
  "s_add_i32 s86, s86, s83\n" \
  "s_ashr_i32 s86, s86, 1\n" \
  "s_max_i32 s86, s86, s82\n" \
  "s_min_i32 s86, s86, 100\n" \
  "s_lshl_b32 s86, s86, 23\n" \
  "s_sub_i32 s84, 0xc8800000, s86\n" \
  "s_cmp_eq_u32 s73, 5\n" \
  "v_smfmac_f32_16x16x64_bf16 v[16:19], v[24:27], v[128:135], v0\n" \
  "v_smfmac_f32_16x16x64_bf16 v[16:19], v[28:31], v[136:143], v0\n" \
  "v_smfmac_f32_16x16x64_bf16 v[16:19], v[32:35], v[144:151], v0\n" \
  "v_smfmac_f32_16x16x64_bf16 v[16:19], v[36:39], v[152:159], v0\n" \
  "v_smfmac_f32_16x16x64_bf16 v[16:19], v[40:43], v[160:167], v0\n" \
  "v_smfmac_f32_16x16x64_bf16 v[16:19], v[44:47], v[168:175], v0\n" \
  "s_waitcnt lgkmcnt(0)\n" \
  "s_barrier\n" \
  "ds_read_b64_tr_b16 v[60:61], v6 offset:0\n" \
  "ds_read_b64_tr_b16 v[62:63], v9 offset:0\n" \
  "v_smfmac_f32_16x16x64_bf16 v[16:19], v[48:51], v[176:183], v0\n" \
  "v_smfmac_f32_16x16x64_bf16 v[16:19], v[52:55], v[184:191], v0\n" \
  "v_smfmac_f32_16x16x64_bf16 v[20:23], v[24:27], v[192:199], v0\n" \
  "v_smfmac_f32_16x16x64_bf16 v[20:23], v[28:31], v[200:207], v0\n" \
  "v_smfmac_f32_16x16x64_bf16 v[20:23], v[32:35], v[208:215], v0\n" \
  "v_smfmac_f32_16x16x64_bf16 v[20:23], v[36:39], v[216:223], v0\n" \
  "v_smfmac_f32_16x16x64_bf16 v[20:23], v[40:43], v[224:231], v0\n" \
  "v_smfmac_f32_16x16x64_bf16 v[20:23], v[44:47], v[232:239], v0\n" \
  "v_smfmac_f32_16x16x64_bf16 v[20:23], v[48:51], v[240:247], v0\n" \
  "v_smfmac_f32_16x16x64_bf16 v[20:23], v[52:55], v[248:255], v0\n" \
  "s_cmp_eq_u32 s71, 0\n" \
  "v_permlane32_swap_b32 v16, v18\n" \
  "v_permlane32_swap_b32 v17, v19\n" \
  "v_add_f32 v16, v16, v18\n" \
  "v_add_f32 v17, v17, v19\n" \
  "v_cvt_pk_bf16_f32 v16, v16, v17\n" \
  "s_waitcnt lgkmcnt(0)\n" \
  "v_max_u32 v60, 1, v60\n" \
  "v_max_u32 v61, 1, v61\n" \
  "v_max_u32 v62, 1, v62\n" \
  "v_max_u32 v63, 1, v63\n" \
  "buffer_store_dwordx4 v[60:63], v7, s[48:51], s69 offen sc1\n" \
  "v_permlane32_swap_b32 v20, v22\n" \
  "v_permlane32_swap_b32 v21, v23\n" \
  "v_pk_add_f32 v[20:21], v[20:21], v[22:23]\n" \
  "v_max_u32 v18, 1, v16\n" \
  "v_cvt_pk_bf16_f32 v17, v20, v21\n" \
  "ds_write_b64 v5, v[16:17]\n" \
  "v_max_u32 v19, 1, v17\n" \
  "s_nop 1\n" \
  "v_permlane32_swap_b32 v18, v19\n" \
  "buffer_store_dwordx2 v[18:19], v8, s[52:55], s70 offen sc1\n" \
  "ds_read_b128 v[24:27], v3 offset:0\n" \
  "ds_read_b128 v[28:31], v3 offset:1024\n" \
  "ds_read_b128 v[32:35], v3 offset:2048\n" \
  "ds_read_b128 v[36:39], v3 offset:3072\n" \
  "ds_read_b128 v[40:43], v3 offset:8192\n" \
  "ds_read_b128 v[44:47], v3 offset:9216\n" \
  "ds_read_b128 v[48:51], v3 offset:10240\n" \
  "ds_read_b128 v[52:55], v3 offset:11264\n" \
  "s_cbranch_scc1 .LCV_LAST_%=\n" \
  "s_cmp_eq_u32 s73, 5\n" \
  "s_cbranch_scc0 .LCV_NMK_%=\n" \
  "s_mov_b32 m0, s78\n" \
  "s_nop 0\n" \
  "buffer_load_dword v2, s[0:3], s66 offen sc1 lds\n" \
  ".LCV_NMK_%=:\n" \
  "s_setprio 0\n" \
  "s_waitcnt lgkmcnt(8)\n" \
  "s_barrier\n" \
  "s_add_u32 s67, s67, 1024\n" \
  "v_mov_b32 v11, 0\n" \
  "ds_write_b32 v59, v11\n" \
  "ds_read_b128 v[56:59], v3 offset:4096\n" \
  "ds_read_b128 v[60:63], v3 offset:5120\n" \
  "v_mov_b64 v[20:21], s[92:93]\n" \
  "v_mov_b64 v[22:23], s[92:93]\n" \
  "v_mov_b64 v[16:17], s[92:93]\n" \
  "v_mov_b64 v[18:19], s[92:93]\n" \
  "s_mov_b32 m0, s94\n" \
  "s_mov_b64 exec, 15\n" \
  "buffer_load_dword v2, s[44:47], s68 offen sc1 lds\n" \
  "s_mov_b64 exec, -1\n" \
  "s_setprio 2\n" \
  "s_waitcnt lgkmcnt(0)\n" \
  "s_barrier\n" \
  "ds_read_b128 v[4:7], v1 offset:8192\n" \
  "ds_read_b128 v[8:11], v1 offset:9216\n" \
  "s_waitcnt lgkmcnt(1)\n" \
  "v_smfmac_f32_16x16x64_bf16 v[12:15], v[4:7], v[24:31], v0\n" \
  "ds_read_b128 v[24:27], v3 offset:6144\n" \
  "ds_read_b128 v[28:31], v3 offset:7168\n" \
  "v_smfmac_f32_16x16x64_bf16 v[16:19], v[4:7], v[32:39], v0\n" \
  "ds_read_b128 v[32:35], v3 offset:12288\n" \
  "ds_read_b128 v[36:39], v3 offset:13312\n" \
  "s_waitcnt lgkmcnt(4)\n" \
  "v_smfmac_f32_16x16x64_bf16 v[12:15], v[8:11], v[40:47], v0\n" \
  "v_smfmac_f32_16x16x64_bf16 v[16:19], v[8:11], v[48:55], v0\n" \
  "v_smfmac_f32_16x16x64_bf16 v[20:23], v[4:7], v[56:63], v0\n" \
  "ds_read_b128 v[56:59], v3 offset:14336\n" \
  "ds_read_b128 v[60:63], v3 offset:15360\n" \
  "v_mov_b64 v[40:41], s[92:93]\n" \
  "v_mov_b64 v[42:43], s[92:93]\n" \
  "s_nop 2\n" \
  "v_permlane32_swap_b32 v12, v17\n" \
  "v_permlane32_swap_b32 v13, v16\n" \
  "v_permlane32_swap_b32 v14, v19\n" \
  "v_permlane32_swap_b32 v15, v18\n" \
  "v_pk_add_f32 v[12:13], v[12:13], v[16:17] op_sel:[0,1] op_sel_hi:[1,0]\n" \
  "v_pk_add_f32 v[14:15], v[14:15], v[18:19] op_sel:[0,1] op_sel_hi:[1,0]\n" \
  "buffer_atomic_add_x2 v[12:13], v2, s[40:43], s56 offen\n" \
  "buffer_atomic_add_x2 v[14:15], v2, s[40:43], s57 offen\n" \
  "s_waitcnt lgkmcnt(2)\n" \
  "v_smfmac_f32_16x16x64_bf16 v[20:23], v[8:11], v[32:39], v0\n" \
  "v_smfmac_f32_16x16x64_bf16 v[40:43], v[4:7], v[24:31], v0\n" \
  "s_waitcnt lgkmcnt(0)\n" \
  "v_smfmac_f32_16x16x64_bf16 v[40:43], v[8:11], v[56:63], v0\n" \
  "s_nop 7\n" \
  "v_permlane32_swap_b32 v20, v41\n" \
  "v_permlane32_swap_b32 v21, v40\n" \
  "v_permlane32_swap_b32 v22, v43\n" \
  "v_permlane32_swap_b32 v23, v42\n" \
  "v_pk_add_f32 v[20:21], v[20:21], v[40:41] op_sel:[0,1] op_sel_hi:[1,0]\n" \
  "v_pk_add_f32 v[22:23], v[22:23], v[42:43] op_sel:[0,1] op_sel_hi:[1,0]\n" \
  "buffer_atomic_add_x2 v[20:21], v2, s[40:43], s58 offen\n" \
  "buffer_atomic_add_x2 v[22:23], v2, s[40:43], s59 offen\n" \
  "s_setprio 0\n" \
  "s_memrealtime s[38:39]\n" \
  "v_mbcnt_lo_u32_b32 v48, -1, 0\n" \
  "v_mbcnt_hi_u32_b32 v48, -1, v48\n" \
  "s_add_u32 s88, s74, 157760\n" \
  "v_mov_b32 v59, s88\n" \
  "v_lshl_add_u32 v52, v48, 2, v59\n" \
  "ds_read_b32 v52, v52 offset:192\n" \
  "v_lshrrev_b32 v49, 3, v48\n" \
  "v_and_b32 v50, 7, v48\n" \
  "v_lshlrev_b32 v50, 4, v50\n" \
  "v_lshl_or_b32 v44, v49, 8, v50\n" \
  "s_mov_b32 s87, 0x40000\n" \
  "v_lshlrev_b32 v10, 4, v48\n" \
  "v_lshl_add_u32 v58, v48, 2, s94\n" \
  "s_waitcnt vmcnt(4)\n" \
  "ds_read_b32 v58, v58\n" \
  "s_waitcnt lgkmcnt(0)\n" \
  "v_cmp_eq_u32 vcc, 0, v58\n" \
  "s_and_b32 vcc_lo, vcc_lo, 15\n" \
  "s_cbranch_scc0 .LCV_GOT_%=\n" \
  "s_branch .LCV_ASK_%=\n" \
  ".LCV_LATE_%=:\n" \
  "v_cmp_eq_u32 vcc, 0, v58\n" \
  "s_and_b32 vcc_lo, vcc_lo, 15\n" \
  "s_cbranch_scc0 .LCV_GOT_%=\n" \
  "s_sleep 12\n" \
  ".LCV_ASK_%=:\n" \
  "s_mov_b64 exec, 15\n" \
  "buffer_load_dword v58, v2, s[44:47], s68 offen sc1\n" \
  "s_mov_b64 exec, -1\n" \
  "s_sub_u32 s87, s87, 1\n" \
  "s_cbranch_scc1 .LCV_END_%=\n" \
  "s_waitcnt vmcnt(0)\n" \
  "s_branch .LCV_LATE_%=\n" \
  ".LCV_GOT_%=:\n" \
  "s_mov_b32 m0, s99\n" \
  "s_add_u32 s88, s99, 8192\n" \
  "global_load_lds_dwordx4 v10, s[62:63] sc1\n" \
  "global_load_lds_dwordx4 v10, s[62:63] offset:1024 sc1\n" \
  "s_mov_b32 m0, s88\n" \
  "s_nop 0\n" \
  "global_load_lds_dwordx4 v10, s[64:65] sc1\n" \
  "global_load_lds_dwordx4 v10, s[64:65] offset:1024 sc1\n" \
  ".LCV_HAVE_%=:\n" \
  "s_waitcnt lgkmcnt(0)\n" \
  "s_sub_u32 s61, s38, s61\n" \
  "s_add_u32 s79, s79, 2\n" \
  "s_min_u32 s79, s79, s61\n" \
  "s_sub_u32 s39, s38, 150\n" \
  "v_subrev_u32 v52, s39, v52\n" \
  "v_cmp_lt_u32 vcc, 0x1000, v52\n" \
  "s_cmp_eq_u64 vcc, 0\n" \
  "s_cbranch_scc1 .LCV_OKM_%=\n" \
  ".LCV_AGN_%=:\n" \
  "buffer_load_dword v52, v2, s[0:3], s66 offen sc1\n" \
  "s_sub_u32 s87, s87, 1\n" \
  "s_cbranch_scc1 .LCV_END_%=\n" \
  "s_waitcnt vmcnt(0)\n" \
  "v_cmp_eq_u32 vcc, 0, v52\n" \
  "v_subrev_u32 v52, s39, v52\n" \
  "s_cmp_eq_u64 vcc, 0\n" \
  "s_cbranch_scc1 .LCV_OKM_%=\n" \
  "s_sleep 4\n" \
  "s_branch .LCV_AGN_%=\n" \
  ".LCV_OKM_%=:\n" \
  "s_nop 1\n" \
  "v_max_i32_dpp v52, v52, v52 row_ror:1 row_mask:0xf bank_mask:0xf\n" \
  "s_nop 1\n" \
  "v_max_i32_dpp v52, v52, v52 row_ror:2 row_mask:0xf bank_mask:0xf\n" \
  "s_nop 1\n" \
  "v_max_i32_dpp v52, v52, v52 row_ror:4 row_mask:0xf bank_mask:0xf\n" \
  "s_nop 1\n" \
  "v_max_i32_dpp v52, v52, v52 row_ror:8 row_mask:0xf bank_mask:0xf\n" \
  "s_movk_i32 vcc_lo, 150\n" \
  "v_readlane_b32 s88, v52, 0\n" \
  "s_max_i32 vcc_lo, vcc_lo, s88\n" \
  "v_readlane_b32 s88, v52, 16\n" \
  "s_max_i32 vcc_lo, vcc_lo, s88\n" \
  "v_readlane_b32 s88, v52, 32\n" \
  "s_max_i32 vcc_lo, vcc_lo, s88\n" \
  "v_readlane_b32 s88, v52, 48\n" \
  "s_max_i32 vcc_lo, vcc_lo, s88\n" \
  "s_add_u32 s66, s66, 1024\n" \
  "s_add_u32 vcc_lo, vcc_lo, s39\n" \
  "s_add_u32 vcc_lo, vcc_lo, 23\n" \
  ".LCV_TW_%=:\n" \
  "s_memrealtime s[38:39]\n" \
  "s_waitcnt lgkmcnt(0)\n" \
  "s_sub_u32 s38, s38, vcc_lo\n" \
  "s_cmp_lt_i32 s38, 0\n" \
  "s_cbranch_scc1 .LCV_TW_%=\n" \
  "buffer_load_dwordx4 v[24:27], v44, s[40:43], s60 offen sc1\n" \
  "buffer_load_dwordx4 v[28:31], v44, s[40:43], s60 offen offset:2048 sc1\n" \
  "s_add_u32 s62, s62, 0x80000\n" \
  "s_addc_u32 s63, s63, 0\n" \
  "s_add_u32 s64, s64, 0x40000\n" \
  "s_addc_u32 s65, s65, 0\n" \
  "s_add_u32 s68, s68, 0x1000\n" \
  "s_add_u32 s69, s69, 0x10000\n" \
  "s_add_u32 s70, s70, 0x10000\n" \
  "s_sub_u32 s88, s73, 4\n" \
  "v_lshrrev_b32 v49, 2, v48\n" \
  "v_lshrrev_b32 v50, 4, v48\n" \
  "v_bitop3_b32 v50, v49, v50, 3 bitop3:0x28\n" \
  "v_lshl_add_u32 v4, v50, 3, s97\n" \
  "v_and_b32 v50, 3, v48\n" \
  "v_lshl_add_u32 v4, v50, 5, v4\n" \
  "v_and_b32 v49, 1, v49\n" \
  "v_lshl_add_u32 v4, v49, 8, v4\n" \
  "v_and_b32 v49, 8, v48\n" \
  "v_lshl_add_u32 v4, v49, 4, v4\n" \
  "v_and_b32 v51, 15, v48\n" \
  "v_bfe_u32 v49, v48, 4, 1\n" \
  "v_lshrrev_b32 v50, 5, v48\n" \
  "s_lshl_b32 s89, s88, 10\n" \
  "s_add_u32 s89, s89, s74\n" \
  "s_add_u32 s89, s89, 158720\n" \
  "v_lshrrev_b32 v9, 3, v51\n" \
  "v_mul_u32_u24 v6, 41, v9\n" \
  "v_xor_b32 v6, v6, v51\n" \
  "v_lshl_add_u32 v6, v6, 3, s89\n" \
  "v_lshl_add_u32 v6, v49, 6, v6\n" \
  "v_lshl_add_u32 v6, v50, 9, v6\n" \
  "v_xor_b32 v9, 0x90, v6\n" \
  "v_lshlrev_b32 v8, 4, v49\n" \
  "v_lshl_add_u32 v8, v51, 5, v8\n" \
  "v_lshl_add_u32 v8, v50, 3, v8\n" \
  "s_lshl_b32 s89, s88, 8\n" \
  "s_add_u32 s89, s89, s74\n" \
  "s_add_u32 s89, s89, 139264\n" \
  "v_and_b32 v49, 31, v48\n" \
  "v_lshl_add_u32 v5, v49, 3, s89\n" \
  "v_lshl_add_u32 v5, v50, 10, v5\n" \
  "v_lshrrev_b32 v49, 2, v51\n" \
  "v_and_b32 v50, 5, v51\n" \
  "v_and_or_b32 v49, v49, 2, v50\n" \
  "v_and_b32 v50, 48, v48\n" \
  "v_lshlrev_b32 v7, 3, v50\n" \
  "v_lshl_add_u32 v7, v49, 4, v7\n" \
  "v_bitop3_b32 v49, v51, 2, 2 bitop3:0x0c\n" \
  "v_lshl_add_u32 v7, v49, 29, v7\n" \
  "v_mov_b64 v[12:13], 0\n" \
  "v_mov_b64 v[14:15], 0\n" \
  "v_mov_b64 v[16:17], 0\n" \
  "v_mov_b64 v[18:19], 0\n" \
  "v_mov_b64 v[20:21], 0\n" \
  "v_mov_b64 v[22:23], 0\n" \
  "v_and_b32 v50, 3, v48\n" \
  "v_lshl_add_u32 v40, v50, 2, s98\n" \
  "v_lshrrev_b32 v50, 4, v48\n" \
  "v_mad_u32_u24 v40, v50, s95, v40\n" \
  "v_bfe_u32 v49, v48, 3, 1\n" \
  "v_lshl_add_u32 v40, v49, 6, v40\n" \
  "v_bfe_u32 v49, v48, 2, 1\n" \
  "v_lshl_add_u32 v40, v49, 7, v40\n" \
  "v_xor_b32 v41, 16, v40\n" \
  "v_xor_b32 v42, 32, v40\n" \
  "v_xor_b32 v43, 48, v40\n" \
  "s_add_i32 s80, s86, 0x35000000\n" \
  "v_mov_b32 v11, 0\n" \
  "s_mov_b32 s89, s60\n" \
  "s_bitcmp1_b32 s60, 7\n" \
  "s_cselect_b32 s88, 0x10000, 0\n" \
  "s_bitset1_b32 s88, 7\n" \
  "s_xor_b32 s56, s56, s88\n" \
  "s_xor_b32 s57, s57, s88\n" \
  "s_xor_b32 s58, s58, s88\n" \
  "s_xor_b32 s59, s59, s88\n" \
  "s_xor_b32 s60, s60, s88\n" \
  "s_sub_u32 s71, s71, 1\n" \
  ".LCV_SEE_%=:\n" \
  "s_waitcnt vmcnt(1)\n" \
  "v_cvt_f32_i32 v24, v24\n" \
  "v_cvt_f32_i32 v25, v25\n" \
  "v_cvt_f32_i32 v26, v26\n" \
  "v_cvt_f32_i32 v27, v27\n" \
  "v_max_f32 v45, |v24|, |v26|\n" \
  "v_pk_mul_f32 v[24:25], v[24:25], s[80:81] op_sel_hi:[1,0]\n" \
  "v_pk_mul_f32 v[26:27], v[26:27], s[80:81] op_sel_hi:[1,0]\n" \
  "s_nop 0\n" \
  "v_cvt_pk_bf16_f32 v32, v24, v26\n" \
  "v_cvt_pk_bf16_f32 v33, v25, v27\n" \
  "ds_write_b32 v40, v32\n" \
  "ds_write_b32 v41, v33\n" \
  "s_waitcnt vmcnt(0)\n" \
  "v_cvt_f32_i32 v28, v28\n" \
  "v_cvt_f32_i32 v30, v30\n" \
  "v_cvt_f32_i32 v29, v29\n" \
  "v_max3_f32 v45, v45, |v28|, |v30|\n" \
  "v_cvt_f32_i32 v31, v31\n" \
  "v_cmp_lt_f32 vcc, s91, v45\n" \
  "v_pk_mul_f32 v[28:29], v[28:29], s[80:81] op_sel_hi:[1,0]\n" \
  "v_pk_mul_f32 v[30:31], v[30:31], s[80:81] op_sel_hi:[1,0]\n" \
  "s_cmp_eq_u64 vcc, 0\n" \
  "s_cbranch_scc0 .LCV_POLL_%=\n" \
  "v_cvt_pk_bf16_f32 v36, v28, v30\n" \
  "v_cvt_pk_bf16_f32 v37, v29, v31\n" \
  "ds_write_b32 v42, v36\n" \
  "ds_write_b32 v43, v37\n" \
  "v_dot2c_f32_bf16 v11, v32, v32\n" \
  "v_dot2c_f32_bf16 v11, v33, v33\n" \
  "v_dot2c_f32_bf16 v11, v36, v36\n" \
  "v_dot2c_f32_bf16 v11, v37, v37\n" \
  "s_nop 1\n" \
  "s_nop 0\n" \
  "v_cmp_lt_f32 vcc, 4.0, v11\n" \
  "s_cbranch_vccz .LCV_TOP_%=\n" \
  "s_mov_b64 exec, vcc\n" \
  "ds_max_u32 v59, v11\n" \
  "s_mov_b64 exec, -1\n" \
  "s_branch .LCV_TOP_%=\n" \
  ".LCV_POLL_%=:\n" \
  "buffer_load_dwordx4 v[24:27], v44, s[40:43], s89 offen sc1\n" \
  "buffer_load_dwordx4 v[28:31], v44, s[40:43], s89 offen offset:2048 sc1\n" \
  "s_sub_u32 s87, s87, 1\n" \
  "s_cbranch_scc1 .LCV_END_%=\n" \
  "s_branch .LCV_SEE_%=\n" \
  ".LCV_LAST_%=:\n" \
  "s_waitcnt vmcnt(0)\n" \
  ".LCV_END_%=:\n" \
  "s_endpgm\n"
__device__ __forceinline__ void start_kgen(const MegaArgs& A, char* smem, const int kg) {
  const int w = A.wv, l = lane_id(), tid = 64 * w + l, c = l & 15, g = l >> 4;
  KGen G;
  unsigned tp = 0u;
#define TEXT_ASK()                                                                                                     \
  {                                                                                                                    \
    const char* pc;                                                                                                    \
    unsigned n;                                                                                                        \
    asm volatile("s_getpc_b64 %0\n.LKG_PC:\n\ts_mov_b32 %1, .LKG_LAST-.LKG_PC" : "=s"(pc), "=s"(n));                    \
    const unsigned o = (unsigned)(tid * 8 + ((blockIdx.x >> 3) & 7)) * IPS;                                            \
    if ((int)o < (int)n) asm volatile("global_load_dword %0, %1, %2" : "=v"(tp) : "v"(o), "s"(pc) : "memory");         \
  }
  if (IPF == 2) TEXT_ASK()
  v4u raw[2] = {};
#pragma unroll
  for (int i = 0; i < TP; ++i)
    if (kg < 32) raw[i] = *reinterpret_cast<const v4u*>(A.x0 + (size_t)(TP * (8 * (kg & 7) + w) + i) * DM + l * 8);
  G.load(A, 8 * kg + w, l);
  {
    const __amdgpu_buffer_rsrc_t rz = MKRS(A.ws + OFF_Z, Z_BYTES);
#pragma unroll
    for (int i = 0; i < 4; ++i) {
      const u64 z = i == 0 && kg < (int)(2 * SZ_ACC / 8192) ? ACC_INIT : 0ull;
      ST128((v4u{(unsigned)z, (unsigned)(z >> 32), (unsigned)z, (unsigned)(z >> 32)}), rz, tid * 16, (i * 64 + kg) * 8192);
    }
  }
  const u64 tag = call_tag(A);
  if (TP && kg < 32) {
    unsigned t = row_stats(raw[0]);
    if (TP > 1) {
      const unsigned t1 = row_stats(raw[1]);
      t = l < 2 ? t : t1;
    }
    if (l < 2 * TP) __hip_atomic_store((gu64*)(A.ws + OFF_GT) + 2 * (L_SEQ + TP * (8 * kg + w)) + l, tagged(tag, (l & 1) ? t : t << 16), RLX_AGENT);
  }
  DRAIN();
  __syncthreads();
  if (tid < 8) __hip_atomic_store((gu64*)(A.ws + OFF_RD) + tid * 64 + kg, tag, RLX_AGENT);
  asm volatile("" : "+v"(G.pa), "+v"(G.pb), "+v"(G.pe));
  G.setup();
  G.st = smem + w * 16640;
  const __amdgpu_buffer_rsrc_t rs = MKRS(A.ws + OFF_KF + (8 * kg + w) * 2048, 386 * 4);
  SST(1);
  unsigned tw = 0u;
#define TLB_ASK()                                                                                                      \
  if (tid < 384) {                                                                                                     \
    const unsigned o = (unsigned)OFF_XA + (unsigned)(tid * 8 + ((blockIdx.x >> 3) & 7)) * 65536u;                      \
    if (o < WS_TOTAL) asm volatile("global_load_dword %0, %1, %2" : "=v"(tw) : "v"(o), "s"(A.ws) : "memory");          \
  }
#define X2_CLEAR() \
  _Pragma("unroll") for (int i = 0; i < 4; ++i) ST128((v4u{0u, 0u, 0u, 0u}), MKRS(A.ws + OFF_X2, SZ_X), tid * 16, (4 * kg + i) * 8192);
  if (XZP == 1) { X2_CLEAR() }
  if (IPF == 1) TEXT_ASK()
  if (TLBW == 1) TLB_ASK()
  unsigned pf[6] = {};
#define PF_ASK(M)                                                                                                                  \
  if (kg < 32) {                                                                                                                   \
    if ((M) & 1) asm volatile("global_load_dword %0, %2, %3\n\tglobal_load_dword %1, %2, %3 offset:128" : "=&v"(pf[0]), "=&v"(pf[1]) : "v"(kg * 131072 + tid * 256), "s"(A.Wo)); \
    if ((M) & 2) asm volatile("global_load_dword %0, %2, %3\n\tglobal_load_dword %1, %2, %3 offset:128" : "=&v"(pf[2]), "=&v"(pf[3]) : "v"(kg * 256 + tid * 8192), "s"(A.Wq)); \
    if ((M) & 4) asm volatile("global_load_dword %0, %2, %3\n\tglobal_load_dword %1, %2, %3 offset:128" : "=&v"(pf[4]), "=&v"(pf[5]) : "v"(kg * 256 + tid * 8192), "s"(A.Wk)); \
  }
  PF_ASK(PFA)
  G.columns(l, 0, 16);
  G.get(G.bh, G.bl, l);
  if (XZP == 2) { X2_CLEAR() }
  PF_ASK(PFB)
  if (TLBW == 2) TLB_ASK()
  (void)G.scale();
  const Cx zb = G.power(256.f);
  Cx b = G.C, z = G.power(2.f);
#pragma unroll 1
  for (int f = 1; f < 9;) {
    char* p = G.st + l * 8;
    const int fe = f == 1 ? 4 : 9;
#pragma unroll 1
    for (; f < fe; ++f) {
      b = cmul(b, zb);
      if (f == 2 || f == 4) z = cmul(z, z);
      Cx y = b;
      p = G.rows(p, y, z, f == 1 ? 8 : f < 4 ? 4 : 2);
    }
    const f32x4 d = G.tile<false>(l);
    const v4u r = {pkh(d.x, d.x), pkh(d.y, d.y), pkh(d.z, d.z), pkh(d.w, d.w)};
    if (f == 4) {
      ST128(r, rs, (g < 2 ? 8 * c + 4 * g : 64 * g + 4 * c) * 4, 0);
    } else {
      ST64((v2u{r.x, r.y}), rs, (256 + 64 * g + 2 * c) * 4, 0);
      ST64((v2u{r.z, r.w}), rs, (288 + 64 * g + 2 * c) * 4, 0);
    }
  }
  SST(2);
  DRAIN();
  asm volatile("" ::"v"(tp), "v"(tw), "v"(pf[0]), "v"(pf[1]), "v"(pf[2]), "v"(pf[3]), "v"(pf[4]), "v"(pf[5]));
  if (l == 0) __hip_atomic_store((gu64*)(A.ws + OFF_KG) + kg * 16 + w, tag, RLX_AGENT);
  if (XZP == 3) { X2_CLEAR() }
  const u64 rv = (XZP & 3) && w == 0 ? ready_look(A) : 0;
  if (TLBW == 3) {
    TLB_ASK()
    DRAIN();
    asm volatile("" ::"v"(tw));
  }
  DRAIN();
  __syncthreads();
  if ((XZP & 3) && w == 0) {
    ready_wait(A, tag, rv);
    if (l < 8) set_flag((unsigned*)(A.ws + OFF_FL) + FL_FZ + l * 64 + kg);
  }
  SST(3);
}

constexpr int C_SM = 131072, C_SO = C_SM + 8192, C_KT = C_SO + 2048, C_X = C_KT + 16384, C_SQ = C_X + 1024;
static_assert(C_SQ + 4096 <= 163840 && C_SQ % 512 == 0, "lds");
__device__ __forceinline__ v4u mk_srd(const void* p, const unsigned bytes) {
  const unsigned long long a = (unsigned long long)p;
  return v4u{(unsigned)a, (unsigned)(a >> 32) & 0xffffu, bytes, 0x00020000u};
}
template <bool ATT>
__device__ __forceinline__ void role_chain_t(const MegaArgs& A, char* smem, const int L, const int h, const int rep) {
  const int w = A.wv;
  char* ws = A.ws;
  unsigned* FL = (unsigned*)(ws + OFF_FL);
  const int l = lane_id(), c = l & 15, g = l >> 4;
  unsigned* FQ = FL + FL_FQ + L * 64 * 32 * 32;
  const int wb = (w + ROT_H) & 7, tb = ROT_T;
  const unsigned sb = lds_base(smem);
  glds16(A.mem0, (unsigned)((c & 7) * DM + 64 * w + 16 * g + 8 * (c >> 3)) * 2, sb + C_SM + w * 1024);
#pragma unroll
  for (int i = 0; i < 16; ++i)
    glds16((const char*)A.Wo + (size_t)(h * DH + 32 * (i >> 2) + 4 * w + (i & 2)) * (DM * 2),
           (unsigned)((i & 1) * 1024 + ((l ^ (((i & 1) | ((w & 3) << 1)) << 1)) << 4)), sb + w * 16384 + i * 1024, WNT & 1);
  const u64 tag = call_tag(A);
  const char* gK = ws + OFF_QK + L * SZ_QK + ((size_t)h * 16 + 2 * w) * 1024;
  const char* gV = ws + OFF_VF + L * SZ_O + ((size_t)h * 8 + 2 * w - 8) * 1024;
  v8u pv, pw;
  {
    const unsigned voff = g * 256 + c * 8, wp = sb + w * 16384 + l * 16;
    pv[0] = (c & 8) ? 0xEEEE : 0x4444;
    pv[1] = sb + C_SM + l * 16;
    const unsigned ra = sb + C_SQ + (g >> 1) * 512 + (c ^ ((g & 1) * 18)) * 8;
    pv[2] = ATT ? ra : voff;
    pv[3] = ATT ? ra ^ 0x108 : wp;
    pv[4] = sb + ((c & 2) ? C_SQ : C_SO) + w * 256 + (((c & 5) | ((c >> 2) & 2)) + 8 * (g >> 1)) * 16 + 8 * ((g ^ (c >> 1)) & 1);
    pv[5] = voff;
    pv[6] = wp;
    pv[7] = sb + C_SO + (l >> 5) * 1024 + w * 256 + (l & 31) * 8;
    const int q = (l >> 2) & 3, p = l & 3;
    const int f = (q & 1) | ((g & 1) << 1) | ((q >> 1) << 2);
#pragma unroll
    for (int i = 0; i < 4; ++i) pw[i] = (4 * i + g) * (HD * 2) + ((c ^ (((g & 1) | (i << 1)) << 1)) << 4) - 1024 * i;
    pw[4] = sb + (g >> 1) * 16384 + (4 * (g & 1) + (q & 1) + 8 * (q >> 1)) * 256 + (w ^ f) * 32 + p * 8;
    pw[5] = sb + (4 * (g >> 1) + 2 * (q >> 1) + (g & 1)) * 16384 + (q & 1) * 1024 + p * 8 + ((4 * wb) ^ f ^ tb) * 32;
    pw[6] = pw[7] = 0u;
  }
  const size_t wo = (size_t)(16 * w) * HD + h * DH;
#define POL 2
#define PG(w) (POL == 2 ? (w) * 4096 : ((w) & 1) * 4096 + ((w) >> 1) * 16384)
  const v4u rsA = mk_srd(ws + OFF_ACC + L * SZ_ACC + (POL == 1 ? 8192 : 0), SZ_ACC - 8192);
  const v4u rsF = mk_srd(FQ, 64 * 32 * 32 * 4);
  const v4u rsMK = mk_srd(ws + OFF_MEMK + L * SZ_MEM, SZ_MEM);
  const v4u rsMV = mk_srd(ws + OFF_MEMV + L * SZ_MEM, SZ_MEM);
  const v4u pAB = (v4u{0u, 2048u, 3072u, 1024u} ^ (1536u * tb)) + PG(wb);
  const v4u rsT = mk_srd(FL + FL_FK + L * 32, 64 * 1024 - 128);
  unsigned sc[8];
  sc[0] = h * 128;
  sc[1] = (h * 4 + w - 4) * 512;
  sc[2] = (h * 4 + w - 4) * 512;
  sc[3] = NSEG - 1;
  sc[5] = w;
  sc[6] = sb;
  const char* dbg = ws;
#define CHAIN_OPS                                                                                                          \
  ::"{v[0:7]}"(pv), "{v[24:31]}"(pw), "{s[4:5]}"(A.Wv + wo), "{s[6:7]}"(A.Wq + wo), "{s[8:9]}"(A.Wk + wo), "{s[10:11]}"(tag),         \
      "{s[12:13]}"(ws + OFF_RD + (blockIdx.x & 7) * 512), "{s[14:15]}"(ws + OFF_G + (blockIdx.x & 7) * 256), "{s[16:17]}"(ws + OFF_G + h * 8), \
      "{s18}"(w == 4 && L == 0 ? 255 : 0), "{s19}"(sb + w * 16384), "{s[20:21]}"(ws + WS_TOTAL + (w == 0 ? 534 * 256 + A.vb : 610 * 256) * 8), \
      "{s[40:43]}"(rsA), "{s[44:47]}"(rsF), "{s[48:51]}"(rsMK), "{s[52:55]}"(rsMV), "{s[56:59]}"(pAB), "{s60}"(PG(w)), "{s61}"(ATT ? PG(h >> 2) + (h & 3) * 1024 : L), \
      "{s[62:63]}"(gK), "{s[64:65]}"(gV), "{s66}"(0), "{s67}"(w == 4 ? (h >> 3) * 256 + (h & 7) * 8 : 0x40000000), "{s68}"(sc[0]), "{s69}"(sc[1]), "{s70}"(sc[2]),    \
      "{s71}"(sc[3]), "{s73}"(sc[5]), "{s74}"(sc[6]), "{s[36:37]}"(dbg),                   \
      "{s[0:3]}"(rsT)                                                                                                      \
      : "memory", "vcc", "scc", "s99", "v255"
  if (ATT) asm volatile("s_mov_b64 exec, -1\ns_nop 4\n" CHAIN_ASM_ATT ".LKG_LAS2:\n" CHAIN_OPS);
  else asm volatile("s_mov_b64 exec, -1\ns_nop 4\n" CHAIN_ASM_VO CHAIN_OPS);
#undef CHAIN_OPS
}

__device__ __forceinline__ void role_chain(const MegaArgs& A, char* smem, const int L, const int h, const int rep) {
  if (A.wv < 4) role_chain_t<true>(A, smem, L, h, rep);
  else role_chain_t<false>(A, smem, L, h, rep);
}

constexpr int AW_SO = 0, AW_TK = 32768, AW_CNT = AW_TK + 98304;
__device__ __forceinline__ unsigned umin3(unsigned a, unsigned b, unsigned c) {
  return __builtin_elementwise_min(__builtin_elementwise_min(a, b), c);
}
#define AW_ASM_X \
  "s_mov_b32 s61, 0\n" \
  "s_mov_b32 s67, 1\n" \
  "s_mov_b32 s68, 0\n" \
  "s_mov_b32 s70, 0\n" \
  "s_mov_b32 s73, 0\n" \
  "s_mov_b32 s71, 0x3e0293ee\n" \
  "s_mov_b32 s72, 0xbe0293ee\n" \
  "s_mov_b32 s65, 49152\n" \
  "s_bitcmp0_b32 s58, 2\n" \
  "s_cbranch_scc1 .LCX_OWN_%=\n" \
  "v_swap_b32 v192, v208\n" \
  "v_swap_b32 v193, v209\n" \
  "v_swap_b32 v194, v210\n" \
  "v_swap_b32 v195, v211\n" \
  "v_swap_b32 v196, v212\n" \
  "v_swap_b32 v197, v213\n" \
  "v_swap_b32 v198, v214\n" \
  "v_swap_b32 v199, v215\n" \
  "v_swap_b32 v200, v216\n" \
  "v_swap_b32 v201, v217\n" \
  "v_swap_b32 v202, v218\n" \
  "v_swap_b32 v203, v219\n" \
  "v_swap_b32 v204, v220\n" \
  "v_swap_b32 v205, v221\n" \
  "v_swap_b32 v206, v222\n" \
  "v_swap_b32 v207, v223\n" \
  "v_swap_b32 v224, v240\n" \
  "v_swap_b32 v225, v241\n" \
  "v_swap_b32 v226, v242\n" \
  "v_swap_b32 v227, v243\n" \
  "v_swap_b32 v228, v244\n" \
  "v_swap_b32 v229, v245\n" \
  "v_swap_b32 v230, v246\n" \
  "v_swap_b32 v231, v247\n" \
  "v_swap_b32 v232, v248\n" \
  "v_swap_b32 v233, v249\n" \
  "v_swap_b32 v234, v250\n" \
  "v_swap_b32 v235, v251\n" \
  "v_swap_b32 v236, v252\n" \
  "v_swap_b32 v237, v253\n" \
  "v_swap_b32 v238, v254\n" \
  "v_swap_b32 v239, v255\n" \
  ".LCX_OWN_%=:\n" \
  "v_mov_b32 v38, s58\n" \
  "s_memrealtime s[38:39]\n" \
  "s_waitcnt lgkmcnt(0)\n" \
  "s_mov_b32 s69, s38\n" \
  "s_setprio 3\n" \
  ".p2align 6\n" \
  ".LCX_TOP_%=:\n" \
  "s_mov_b32 s62, 0x200000\n" \
  "s_cmp_ge_u32 s73, s67\n" \
  "s_cbranch_scc1 .LCX_TOK_%=\n" \
  "s_cmp_eq_u32 s61, 0\n" \
  "s_cbranch_scc1 .LCX_SPIN_%=\n" \
  ".LCX_RE_%=:\n" \
  "ds_read_b32 v37, v38\n" \
  "s_add_u32 s62, s62, 1\n" \
  "s_waitcnt lgkmcnt(0)\n" \
  "v_readfirstlane_b32 s73, v37\n" \
  "s_bfe_u32 s64, s73, 0x80008\n" \
  "s_and_b32 s73, s73, 0xff\n" \
  "s_min_u32 s73, s73, s64\n" \
  "s_cmp_ge_u32 s73, s67\n" \
  "s_cbranch_scc1 .LCX_TOK_%=\n" \
  "s_sleep 1\n" \
  "s_cmp_lt_u32 s62, 2097158\n" \
  "s_cbranch_scc1 .LCX_RE_%=\n" \
  "s_waitcnt vmcnt(0)\n" \
  "v_mov_b32 v144, s59\n" \
  "v_mov_b32 v145, 1\n" \
  "s_mov_b64 exec, 1\n" \
  "ds_add_rtn_u32 v144, v144, v145\n" \
  "s_mov_b64 exec, -1\n" \
  "s_xor_b32 s59, s59, 4\n" \
  "s_mov_b32 s61, 0\n" \
  "s_waitcnt lgkmcnt(0)\n" \
  "v_readfirstlane_b32 s63, v144\n" \
  "v_lshlrev_b32 v144, 5, v5\n" \
  "s_and_b32 s63, s63, 7\n" \
  "s_cmp_eq_u32 s63, 7\n" \
  "s_cbranch_scc0 .LCX_W1_%=\n" \
  "s_mov_b64 exec, 15\n" \
  "global_store_dword v144, v145, s[52:53] sc1\n" \
  "s_mov_b64 exec, -1\n" \
  ".LCX_W1_%=:\n" \
  "s_add_u32 s52, s52, 2048\n" \
  "s_addc_u32 s53, s53, 0\n" \
  ".LCX_SPIN_%=:\n" \
  "s_sleep 2\n" \
  "s_memrealtime s[38:39]\n" \
  "ds_read_b32 v37, v38\n" \
  "s_sub_u32 s62, s62, 1\n" \
  "s_cbranch_scc1 .LCX_END_%=\n" \
  "s_waitcnt lgkmcnt(0)\n" \
  "v_readfirstlane_b32 s73, v37\n" \
  "s_add_u32 s38, s38, 80\n" \
  "s_bfe_u32 s64, s73, 0x80008\n" \
  "s_and_b32 s73, s73, 0xff\n" \
  "s_min_u32 s73, s73, s64\n" \
  "s_cmp_ge_u32 s73, s67\n" \
  "s_cbranch_scc0 .LCX_SPIN_%=\n" \
  "s_cmp_eq_u32 s70, 0\n" \
  "s_cbranch_scc1 .LCX_TOK_%=\n" \
  "s_sub_u32 s63, s38, s69\n" \
  "s_max_i32 s63, s63, 0\n" \
  "s_add_u32 s69, s69, s63\n" \
  ".LCX_TOK_%=:\n" \
  "ds_read_b128 v[48:51], v6 offset:0\n" \
  "ds_read_b128 v[64:67], v0 offset:8192\n" \
  "ds_read_b128 v[80:83], v0 offset:12288\n" \
  "ds_read_b128 v[52:55], v6 offset:1024\n" \
  "ds_read_b128 v[68:71], v0 offset:9216\n" \
  "ds_read_b128 v[84:87], v0 offset:13312\n" \
  "ds_read_b128 v[56:59], v6 offset:2048\n" \
  "ds_read_b128 v[72:75], v0 offset:10240\n" \
  "ds_read_b128 v[88:91], v0 offset:14336\n" \
  "ds_read_b128 v[60:63], v6 offset:3072\n" \
  "ds_read_b128 v[76:79], v0 offset:11264\n" \
  "ds_read_b128 v[92:95], v0 offset:15360\n" \
  "s_waitcnt lgkmcnt(9)\n" \
  "v_mfma_f32_16x16x32_bf16 v[20:23], v[64:67], v[48:51], v[8:11]\n" \
  "v_mfma_f32_16x16x32_bf16 v[24:27], v[80:83], v[48:51], v[12:15]\n" \
  "ds_read_b128 v[96:99], v0 offset:16384\n" \
  "ds_read_b128 v[100:103], v0 offset:17408\n" \
  "s_waitcnt lgkmcnt(8)\n" \
  "v_mfma_f32_16x16x32_bf16 v[20:23], v[68:71], v[52:55], v[20:23]\n" \
  "v_mfma_f32_16x16x32_bf16 v[24:27], v[84:87], v[52:55], v[24:27]\n" \
  "ds_read_b128 v[104:107], v0 offset:18432\n" \
  "ds_read_b128 v[108:111], v0 offset:19456\n" \
  "s_waitcnt lgkmcnt(7)\n" \
  "v_mfma_f32_16x16x32_bf16 v[20:23], v[72:75], v[56:59], v[20:23]\n" \
  "v_mfma_f32_16x16x32_bf16 v[24:27], v[88:91], v[56:59], v[24:27]\n" \
  "ds_read_b128 v[112:115], v0 offset:20480\n" \
  "ds_read_b128 v[116:119], v0 offset:21504\n" \
  "s_waitcnt lgkmcnt(6)\n" \
  "v_mfma_f32_16x16x32_bf16 v[20:23], v[76:79], v[60:63], v[20:23]\n" \
  "v_mfma_f32_16x16x32_bf16 v[24:27], v[92:95], v[60:63], v[24:27]\n" \
  "ds_read_b128 v[120:123], v0 offset:22528\n" \
  "ds_read_b128 v[124:127], v0 offset:23552\n" \
  "s_add_u32 s67, s67, 1\n" \
  "v_add_u32 v0, s65, v0\n" \
  "v_add_u32 v6, s65, v6\n" \
  "s_sub_u32 s65, 0, s65\n" \
  "s_nop 1\n" \
  "v_max3_f32 v32, v20, v21, v22\n" \
  "v_max3_f32 v33, v23, v24, v25\n" \
  "v_max3_f32 v32, v32, v26, v27\n" \
  "v_max_f32 v32, v32, v33\n" \
  "v_mov_b32 v33, v32\n" \
  "s_nop 1\n" \
  "v_permlane16_swap_b32 v32, v33\n" \
  "v_max_f32 v32, v32, v33\n" \
  "v_mov_b32 v33, v32\n" \
  "s_nop 1\n" \
  "v_permlane32_swap_b32 v32, v33\n" \
  "v_max_f32 v32, v32, v33\n" \
  "v_mul_f32 v33, s72, v32\n" \
  "v_fma_f32 v20, v20, s71, v33\n" \
  "v_fma_f32 v21, v21, s71, v33\n" \
  "v_fma_f32 v22, v22, s71, v33\n" \
  "v_fma_f32 v23, v23, s71, v33\n" \
  "v_fma_f32 v24, v24, s71, v33\n" \
  "v_fma_f32 v25, v25, s71, v33\n" \
  "v_fma_f32 v26, v26, s71, v33\n" \
  "v_fma_f32 v27, v27, s71, v33\n" \
  "v_exp_f32 v20, v20\n" \
  "v_exp_f32 v21, v21\n" \
  "v_exp_f32 v22, v22\n" \
  "v_exp_f32 v23, v23\n" \
  "v_exp_f32 v24, v24\n" \
  "v_exp_f32 v25, v25\n" \
  "v_exp_f32 v26, v26\n" \
  "v_exp_f32 v27, v27\n" \
  "v_add_f32 v34, v20, v21\n" \
  "v_add_f32 v33, v22, v23\n" \
  "v_add_f32 v36, v24, v25\n" \
  "v_add_f32 v34, v34, v33\n" \
  "v_add_f32 v33, v26, v27\n" \
  "v_add_f32 v34, v34, v36\n" \
  "v_add_f32 v34, v34, v33\n" \
  "v_mov_b64 v[28:29], 0\n" \
  "v_mov_b64 v[30:31], 0\n" \
  "s_waitcnt vmcnt(0) lgkmcnt(0)\n" \
  "s_cmp_eq_u32 s61, 0\n" \
  "s_cbranch_scc1 .LCX_NP_%=\n" \
  "v_mov_b32 v144, s59\n" \
  "v_mov_b32 v145, 1\n" \
  "s_mov_b64 exec, 1\n" \
  "ds_add_rtn_u32 v144, v144, v145\n" \
  "s_mov_b64 exec, -1\n" \
  "s_xor_b32 s59, s59, 4\n" \
  "s_cmp_lg_u32 s68, 0\n" \
  "s_cbranch_scc1 .LCX_TEST_%=\n" \
  "s_mov_b32 s61, 0\n" \
  "s_waitcnt lgkmcnt(0)\n" \
  "v_readfirstlane_b32 s63, v144\n" \
  "v_lshlrev_b32 v144, 5, v5\n" \
  "s_and_b32 s63, s63, 7\n" \
  "s_cmp_eq_u32 s63, 7\n" \
  "s_cbranch_scc0 .LCX_W2_%=\n" \
  "s_mov_b64 exec, 15\n" \
  "global_store_dword v144, v145, s[52:53] sc1\n" \
  "s_mov_b64 exec, -1\n" \
  ".LCX_W2_%=:\n" \
  "s_add_u32 s52, s52, 2048\n" \
  "s_addc_u32 s53, s53, 0\n" \
  ".LCX_NP_%=:\n" \
  "s_cmp_lg_u32 s68, 0\n" \
  "s_cbranch_scc1 .LCX_TEST_%=\n" \
  ".LCX_WF_%=:\n" \
  "s_memrealtime s[38:39]\n" \
  "s_waitcnt lgkmcnt(0)\n" \
  "s_sub_u32 s63, s69, s38\n" \
  "s_cmp_le_i32 s63, 0\n" \
  "s_cbranch_scc1 .LCX_GO_%=\n" \
  "s_cmp_lt_i32 s63, 12\n" \
  "s_cbranch_scc1 .LCX_WF_%=\n" \
  "s_sleep 3\n" \
  "s_branch .LCX_WF_%=\n" \
  ".LCX_GO_%=:\n" \
  "v_mov_b32 v35, 0\n" \
  "global_load_dwordx4 v[128:131], v5, s[48:49] offset:0 sc1\n" \
  "global_load_dwordx4 v[132:135], v5, s[48:49] offset:1024 sc1\n" \
  "global_load_dwordx4 v[136:139], v3, s[48:49] offset:0 sc1\n" \
  "global_load_dwordx4 v[140:143], v3, s[48:49] offset:512 sc1\n" \
  "global_load_dword v35, v35, s[50:51] sc1\n" \
  "s_sub_u32 s62, s62, 8\n" \
  "s_cbranch_scc1 .LCX_END_%=\n" \
  "s_waitcnt vmcnt(0)\n" \
  ".LCX_TEST_%=:\n" \
  "v_smfmac_f32_16x16x64_bf16 v[28:31], v[128:131], v[48:55], v1\n" \
  "v_smfmac_f32_16x16x64_bf16 v[28:31], v[132:135], v[56:63], v1\n" \
  "v_min3_u32 v33, v128, v129, v130\n" \
  "v_min3_u32 v36, v131, v132, v133\n" \
  "v_min3_u32 v33, v33, v134, v135\n" \
  "v_min3_u32 v36, v36, v136, v137\n" \
  "v_min3_u32 v33, v33, v138, v139\n" \
  "v_min3_u32 v36, v36, v140, v141\n" \
  "v_min3_u32 v33, v33, v142, v143\n" \
  "v_min_u32 v33, v33, v36\n" \
  "v_cmp_eq_u32 vcc, 0, v33\n" \
  "v_readfirstlane_b32 s64, v35\n" \
  "s_mov_b32 s68, 0\n" \
  "s_cmp_eq_u64 vcc, 0\n" \
  "s_cbranch_scc0 .LCX_FAIL_%=\n" \
  "v_permlane32_swap_b32 v28, v30\n" \
  "v_permlane32_swap_b32 v29, v31\n" \
  "v_mov_b32 v33, s67\n" \
  "ds_write_b32 v38, v33 offset:16\n" \
  "v_add_f32 v28, v28, v30\n" \
  "v_add_f32 v29, v29, v31\n" \
  "v_max3_f32 v36, v28, v29, v32\n" \
  "v_mov_b32 v33, v36\n" \
  "s_cmp_eq_u32 s64, 0\n" \
  "s_cselect_b32 s64, s38, s64\n" \
  "v_permlane16_swap_b32 v36, v33\n" \
  "v_max_f32 v36, v36, v33\n" \
  "v_mov_b32 v33, v36\n" \
  "s_sub_u32 s63, s64, s70\n" \
  "s_mov_b32 s70, s64\n" \
  "v_permlane32_swap_b32 v36, v33\n" \
  "v_max_f32 v36, v36, v33\n" \
  "v_mul_f32 v36, s72, v36\n" \
  "v_fma_f32 v32, v32, s71, v36\n" \
  "v_fma_f32 v28, v28, s71, v36\n" \
  "v_fma_f32 v29, v29, s71, v36\n" \
  "v_exp_f32 v32, v32\n" \
  "v_exp_f32 v28, v28\n" \
  "v_exp_f32 v29, v29\n" \
  "v_fma_f32 v34, v34, v32, v28\n" \
  "v_add_f32 v34, v34, v29\n" \
  "v_mov_b32 v33, v34\n" \
  "s_min_u32 s63, s63, 250\n" \
  "s_add_u32 s69, s64, s63\n" \
  "v_permlane16_swap_b32 v34, v33\n" \
  "v_add_f32 v34, v34, v33\n" \
  "v_mov_b32 v33, v34\n" \
  "s_add_u32 s69, s69, -48\n" \
  "s_add_u32 s48, s48, 0x10000\n" \
  "s_addc_u32 s49, s49, 0\n" \
  "v_permlane32_swap_b32 v34, v33\n" \
  "v_add_f32 v34, v34, v33\n" \
  "v_rcp_f32 v34, v34\n" \
  "s_add_u32 s50, s50, 1024\n" \
  "s_addc_u32 s51, s51, 0\n" \
  "v_mul_f32 v32, v32, v34\n" \
  "v_mul_f32 v20, v20, v32\n" \
  "v_mul_f32 v21, v21, v32\n" \
  "v_mul_f32 v22, v22, v32\n" \
  "v_mul_f32 v23, v23, v32\n" \
  "v_mul_f32 v24, v24, v32\n" \
  "v_mul_f32 v25, v25, v32\n" \
  "v_mul_f32 v26, v26, v32\n" \
  "v_mul_f32 v27, v27, v32\n" \
  "v_cvt_pk_bf16_f32 v40, v20, v21\n" \
  "v_cvt_pk_bf16_f32 v41, v22, v23\n" \
  "v_cvt_pk_bf16_f32 v42, v24, v25\n" \
  "v_cvt_pk_bf16_f32 v43, v26, v27\n" \
  "v_mul_f32 v28, v28, v34\n" \
  "v_mul_f32 v29, v29, v34\n" \
  "v_mfma_f32_16x16x32_bf16 v[64:67], v[96:99], v[40:43], 0\n" \
  "v_cvt_pk_bf16_f32 v44, v28, v29\n" \
  "v_mov_b32 v45, 0\n" \
  "v_mfma_f32_16x16x32_bf16 v[68:71], v[100:103], v[40:43], 0\n" \
  "v_cvt_pk_bf16_f32 v47, v28, v29\n" \
  "v_mov_b32 v46, 0\n" \
  "v_mfma_f32_16x16x32_bf16 v[72:75], v[104:107], v[40:43], 0\n" \
  "v_permlane32_swap_b32 v44, v45\n" \
  "v_mfma_f32_16x16x32_bf16 v[76:79], v[108:111], v[40:43], 0\n" \
  "v_permlane32_swap_b32 v46, v47\n" \
  "v_mfma_f32_16x16x32_bf16 v[80:83], v[112:115], v[40:43], 0\n" \
  "v_xor_b32 v16, 4096, v7\n" \
  "v_mfma_f32_16x16x32_bf16 v[84:87], v[116:119], v[40:43], 0\n" \
  "v_xor_b32 v17, 8192, v7\n" \
  "v_mfma_f32_16x16x32_bf16 v[88:91], v[120:123], v[40:43], 0\n" \
  "v_xor_b32 v18, 12288, v7\n" \
  "v_mfma_f32_16x16x32_bf16 v[92:95], v[124:127], v[40:43], 0\n" \
  "s_cmp_eq_u32 s61, 0\n" \
  "s_cbranch_scc1 .LCX_NQ_%=\n" \
  "s_mov_b32 s61, 0\n" \
  "s_waitcnt lgkmcnt(0)\n" \
  "v_readfirstlane_b32 s63, v144\n" \
  "v_lshlrev_b32 v144, 5, v5\n" \
  "s_and_b32 s63, s63, 7\n" \
  "s_cmp_eq_u32 s63, 7\n" \
  "s_cbranch_scc0 .LCX_W3_%=\n" \
  "s_mov_b64 exec, 15\n" \
  "global_store_dword v144, v145, s[52:53] sc1\n" \
  "s_mov_b64 exec, -1\n" \
  ".LCX_W3_%=:\n" \
  "s_add_u32 s52, s52, 2048\n" \
  "s_addc_u32 s53, s53, 0\n" \
  ".LCX_NQ_%=:\n" \
  "s_memrealtime s[38:39]\n" \
  "ds_read_b32 v37, v38\n" \
  "v_mfma_f32_16x16x16_bf16 v[64:67], v[136:137], v[44:45], v[64:67]\n" \
  "v_mfma_f32_16x16x16_bf16 v[68:71], v[140:141], v[44:45], v[68:71]\n" \
  "v_mfma_f32_16x16x16_bf16 v[72:75], v[136:137], v[46:47], v[72:75]\n" \
  "v_mfma_f32_16x16x16_bf16 v[76:79], v[140:141], v[46:47], v[76:79]\n" \
  "v_mfma_f32_16x16x16_bf16 v[80:83], v[138:139], v[44:45], v[80:83]\n" \
  "v_mfma_f32_16x16x16_bf16 v[84:87], v[142:143], v[44:45], v[84:87]\n" \
  "v_mfma_f32_16x16x16_bf16 v[88:91], v[138:139], v[46:47], v[88:91]\n" \
  "v_mfma_f32_16x16x16_bf16 v[92:95], v[142:143], v[46:47], v[92:95]\n" \
  "s_nop 0\n" \
  "v_cvt_pk_bf16_f32 v160, v64, v65\n" \
  "v_cvt_pk_bf16_f32 v161, v66, v67\n" \
  "v_cvt_pk_bf16_f32 v162, v68, v69\n" \
  "v_cvt_pk_bf16_f32 v163, v70, v71\n" \
  "ds_write_b128 v7, v[160:163] offset:0\n" \
  "v_cvt_pk_bf16_f32 v164, v72, v73\n" \
  "v_cvt_pk_bf16_f32 v165, v74, v75\n" \
  "v_cvt_pk_bf16_f32 v166, v76, v77\n" \
  "v_cvt_pk_bf16_f32 v167, v78, v79\n" \
  "ds_write_b128 v7, v[164:167] offset:1024\n" \
  "v_cvt_pk_bf16_f32 v168, v80, v81\n" \
  "v_cvt_pk_bf16_f32 v169, v82, v83\n" \
  "v_cvt_pk_bf16_f32 v170, v84, v85\n" \
  "v_cvt_pk_bf16_f32 v171, v86, v87\n" \
  "ds_write_b128 v7, v[168:171] offset:2048\n" \
  "v_cvt_pk_bf16_f32 v172, v88, v89\n" \
  "v_cvt_pk_bf16_f32 v173, v90, v91\n" \
  "v_cvt_pk_bf16_f32 v174, v92, v93\n" \
  "v_cvt_pk_bf16_f32 v175, v94, v95\n" \
  "ds_write_b128 v7, v[172:175] offset:3072\n" \
  "v_xor_b32 v7, 0x4000, v7\n" \
  "v_mfma_f32_16x16x32_bf16 v[112:115], v[192:195], v[160:163], 0\n" \
  "v_mfma_f32_16x16x32_bf16 v[116:119], v[224:227], v[160:163], 0\n" \
  "s_waitcnt lgkmcnt(0)\n" \
  "s_barrier\n" \
  "ds_read_b128 v[64:67], v16 offset:0\n" \
  "ds_read_b128 v[68:71], v16 offset:1024\n" \
  "ds_read_b128 v[72:75], v16 offset:2048\n" \
  "ds_read_b128 v[76:79], v16 offset:3072\n" \
  "ds_read_b128 v[80:83], v17 offset:0\n" \
  "ds_read_b128 v[84:87], v17 offset:1024\n" \
  "ds_read_b128 v[88:91], v17 offset:2048\n" \
  "ds_read_b128 v[92:95], v17 offset:3072\n" \
  "ds_read_b128 v[96:99], v18 offset:0\n" \
  "ds_read_b128 v[100:103], v18 offset:1024\n" \
  "ds_read_b128 v[104:107], v18 offset:2048\n" \
  "ds_read_b128 v[108:111], v18 offset:3072\n" \
  "v_readfirstlane_b32 s73, v37\n" \
  "s_sub_u32 s63, s38, s69\n" \
  "s_bfe_u32 s64, s73, 0x80010\n" \
  "s_sub_u32 s64, s64, s67\n" \
  "s_or_b32 s63, s63, s64\n" \
  "s_bfe_u32 s64, s73, 0x80008\n" \
  "s_and_b32 s73, s73, 0xff\n" \
  "s_min_u32 s73, s73, s64\n" \
  "s_or_b32 s63, s63, s68\n" \
  "s_cmp_lt_i32 s63, 0\n" \
  "s_cbranch_scc1 .LCX_E4_%=\n" \
  "v_mov_b32 v35, 0\n" \
  "global_load_dwordx4 v[128:131], v5, s[48:49] offset:0 sc1\n" \
  "global_load_dwordx4 v[132:135], v5, s[48:49] offset:1024 sc1\n" \
  "global_load_dwordx4 v[136:139], v3, s[48:49] offset:0 sc1\n" \
  "global_load_dwordx4 v[140:143], v3, s[48:49] offset:512 sc1\n" \
  "global_load_dword v35, v35, s[50:51] sc1\n" \
  "v_mov_b32 v33, s67\n" \
  "ds_write_b32 v38, v33 offset:8\n" \
  "s_mov_b32 s68, -1\n" \
  ".LCX_E4_%=:\n" \
  "v_mfma_f32_16x16x32_bf16 v[112:115], v[196:199], v[164:167], v[112:115]\n" \
  "v_mfma_f32_16x16x32_bf16 v[116:119], v[228:231], v[164:167], v[116:119]\n" \
  "v_mfma_f32_16x16x32_bf16 v[112:115], v[200:203], v[168:171], v[112:115]\n" \
  "v_mfma_f32_16x16x32_bf16 v[116:119], v[232:235], v[168:171], v[116:119]\n" \
  "v_mfma_f32_16x16x32_bf16 v[112:115], v[204:207], v[172:175], v[112:115]\n" \
  "v_mfma_f32_16x16x32_bf16 v[116:119], v[236:239], v[172:175], v[116:119]\n" \
  "s_waitcnt lgkmcnt(11)\n" \
  "v_mfma_f32_16x16x32_bf16 v[120:123], v[192:195], v[64:67], 0\n" \
  "v_mfma_f32_16x16x32_bf16 v[124:127], v[224:227], v[64:67], 0\n" \
  "s_waitcnt lgkmcnt(10)\n" \
  "v_mfma_f32_16x16x32_bf16 v[120:123], v[196:199], v[68:71], v[120:123]\n" \
  "v_mfma_f32_16x16x32_bf16 v[124:127], v[228:231], v[68:71], v[124:127]\n" \
  "s_waitcnt lgkmcnt(9)\n" \
  "v_mfma_f32_16x16x32_bf16 v[120:123], v[200:203], v[72:75], v[120:123]\n" \
  "v_mfma_f32_16x16x32_bf16 v[124:127], v[232:235], v[72:75], v[124:127]\n" \
  "s_waitcnt lgkmcnt(8)\n" \
  "v_mfma_f32_16x16x32_bf16 v[120:123], v[204:207], v[76:79], v[120:123]\n" \
  "v_mfma_f32_16x16x32_bf16 v[124:127], v[236:239], v[76:79], v[124:127]\n" \
  "s_waitcnt lgkmcnt(7)\n" \
  "v_mfma_f32_16x16x32_bf16 v[112:115], v[208:211], v[80:83], v[112:115]\n" \
  "v_mfma_f32_16x16x32_bf16 v[116:119], v[240:243], v[80:83], v[116:119]\n" \
  "s_waitcnt lgkmcnt(6)\n" \
  "v_mfma_f32_16x16x32_bf16 v[112:115], v[212:215], v[84:87], v[112:115]\n" \
  "v_mfma_f32_16x16x32_bf16 v[116:119], v[244:247], v[84:87], v[116:119]\n" \
  "s_waitcnt lgkmcnt(5)\n" \
  "v_mfma_f32_16x16x32_bf16 v[112:115], v[216:219], v[88:91], v[112:115]\n" \
  "v_mfma_f32_16x16x32_bf16 v[116:119], v[248:251], v[88:91], v[116:119]\n" \
  "s_waitcnt lgkmcnt(4)\n" \
  "v_mfma_f32_16x16x32_bf16 v[112:115], v[220:223], v[92:95], v[112:115]\n" \
  "v_mfma_f32_16x16x32_bf16 v[116:119], v[252:255], v[92:95], v[116:119]\n" \
  "s_memrealtime s[38:39]\n" \
  "ds_read_b32 v37, v38\n" \
  "s_waitcnt lgkmcnt(4)\n" \
  "v_mfma_f32_16x16x32_bf16 v[120:123], v[208:211], v[96:99], v[120:123]\n" \
  "v_mfma_f32_16x16x32_bf16 v[124:127], v[240:243], v[96:99], v[124:127]\n" \
  "s_waitcnt lgkmcnt(3)\n" \
  "v_mfma_f32_16x16x32_bf16 v[120:123], v[212:215], v[100:103], v[120:123]\n" \
  "v_mfma_f32_16x16x32_bf16 v[124:127], v[244:247], v[100:103], v[124:127]\n" \
  "v_cvt_pk_bf16_f32 v20, v112, v113\n" \
  "v_cvt_pk_bf16_f32 v21, v114, v115\n" \
  "v_cvt_pk_bf16_f32 v22, v116, v117\n" \
  "v_cvt_pk_bf16_f32 v23, v118, v119\n" \
  "s_waitcnt lgkmcnt(2)\n" \
  "v_mfma_f32_16x16x32_bf16 v[120:123], v[216:219], v[104:107], v[120:123]\n" \
  "v_mfma_f32_16x16x32_bf16 v[124:127], v[248:251], v[104:107], v[124:127]\n" \
  "buffer_store_dwordx4 v[20:23], v4, s[40:43], s54 offen sc1\n" \
  "s_waitcnt lgkmcnt(0)\n" \
  "v_mfma_f32_16x16x32_bf16 v[120:123], v[220:223], v[108:111], v[120:123]\n" \
  "v_mfma_f32_16x16x32_bf16 v[124:127], v[252:255], v[108:111], v[124:127]\n" \
  "s_xor_b32 s64, s54, 16384\n" \
  "s_mov_b32 s61, 1\n" \
  "s_nop 3\n" \
  "v_cvt_pk_bf16_f32 v24, v120, v121\n" \
  "v_cvt_pk_bf16_f32 v25, v122, v123\n" \
  "v_cvt_pk_bf16_f32 v26, v124, v125\n" \
  "v_cvt_pk_bf16_f32 v27, v126, v127\n" \
  "buffer_store_dwordx4 v[24:27], v4, s[40:43], s64 offen sc1\n" \
  "v_readfirstlane_b32 s73, v37\n" \
  "s_sub_u32 s63, s38, s69\n" \
  "s_bfe_u32 s64, s73, 0x80010\n" \
  "s_sub_u32 s64, s64, s67\n" \
  "s_or_b32 s63, s63, s64\n" \
  "s_bfe_u32 s64, s73, 0x80008\n" \
  "s_and_b32 s73, s73, 0xff\n" \
  "s_min_u32 s73, s73, s64\n" \
  "s_or_b32 s63, s63, s68\n" \
  "s_cmp_lt_i32 s63, 0\n" \
  "s_cbranch_scc1 .LCX_E5_%=\n" \
  "v_mov_b32 v35, 0\n" \
  "global_load_dwordx4 v[128:131], v5, s[48:49] offset:0 sc1\n" \
  "global_load_dwordx4 v[132:135], v5, s[48:49] offset:1024 sc1\n" \
  "global_load_dwordx4 v[136:139], v3, s[48:49] offset:0 sc1\n" \
  "global_load_dwordx4 v[140:143], v3, s[48:49] offset:512 sc1\n" \
  "global_load_dword v35, v35, s[50:51] sc1\n" \
  "v_mov_b32 v33, s67\n" \
  "ds_write_b32 v38, v33 offset:8\n" \
  "s_mov_b32 s68, -1\n" \
  ".LCX_E5_%=:\n" \
  "s_add_u32 s54, s54, 0x80000\n" \
  "s_sub_u32 s60, s60, 1\n" \
  "s_cmp_lg_u32 s60, 0\n" \
  "s_cbranch_scc1 .LCX_TOP_%=\n" \
  "s_waitcnt vmcnt(0)\n" \
  "v_mov_b32 v144, s59\n" \
  "v_mov_b32 v145, 1\n" \
  "s_mov_b64 exec, 1\n" \
  "ds_add_rtn_u32 v144, v144, v145\n" \
  "s_mov_b64 exec, -1\n" \
  "s_xor_b32 s59, s59, 4\n" \
  "s_mov_b32 s61, 0\n" \
  "s_waitcnt lgkmcnt(0)\n" \
  "v_readfirstlane_b32 s63, v144\n" \
  "v_lshlrev_b32 v144, 5, v5\n" \
  "s_and_b32 s63, s63, 7\n" \
  "s_cmp_eq_u32 s63, 7\n" \
  "s_cbranch_scc0 .LCX_W6_%=\n" \
  "s_mov_b64 exec, 15\n" \
  "global_store_dword v144, v145, s[52:53] sc1\n" \
  "s_mov_b64 exec, -1\n" \
  ".LCX_W6_%=:\n" \
  "s_add_u32 s52, s52, 2048\n" \
  "s_addc_u32 s53, s53, 0\n" \
  ".LCX_END_%=:\n" \
  "s_endpgm\n" \
  ".LCX_FAIL_%=:\n" \
  "v_mov_b64 v[28:29], 0\n" \
  "v_mov_b64 v[30:31], 0\n" \
  "s_cmp_eq_u32 s61, 0\n" \
  "s_cbranch_scc1 .LCX_FAIL2_%=\n" \
  "s_mov_b32 s61, 0\n" \
  "s_waitcnt lgkmcnt(0)\n" \
  "v_readfirstlane_b32 s63, v144\n" \
  "v_lshlrev_b32 v144, 5, v5\n" \
  "s_and_b32 s63, s63, 7\n" \
  "s_cmp_eq_u32 s63, 7\n" \
  "s_cbranch_scc0 .LCX_W7_%=\n" \
  "s_mov_b64 exec, 15\n" \
  "global_store_dword v144, v145, s[52:53] sc1\n" \
  "s_mov_b64 exec, -1\n" \
  ".LCX_W7_%=:\n" \
  "s_add_u32 s52, s52, 2048\n" \
  "s_addc_u32 s53, s53, 0\n" \
  ".LCX_FAIL2_%=:\n" \
  "s_add_u32 s69, s64, -48\n" \
  "s_cmp_lg_u32 s64, 0\n" \
  "s_cbranch_scc1 .LCX_WF_%=\n" \
  "s_sleep 4\n" \
  "global_load_dword v35, v35, s[50:51] sc1\n" \
  "s_sub_u32 s62, s62, 8\n" \
  "s_cbranch_scc1 .LCX_END_%=\n" \
  "s_waitcnt vmcnt(0)\n" \
  "v_readfirstlane_b32 s64, v35\n" \
  "s_branch .LCX_FAIL2_%=\n"
#define AW_ASM_Y \
  "s_mov_b32 s72, 0\n" \
  "s_mov_b32 s67, 0\n" \
  "s_mov_b32 s68, 2\n" \
  "s_mov_b32 s69, 0\n" \
  "s_mov_b32 s65, 49152\n" \
  "s_mov_b32 s66, 16384\n" \
  "v_mov_b32 v5, s58\n" \
  "v_mov_b32 v60, v3\n" \
  "v_add_u32 v3, 4096, v2\n" \
  "s_mov_b32 s62, 0x40000\n" \
  "s_branch .LCY_ASK_%=\n" \
  ".p2align 6\n" \
  ".LCY_BAR_%=:\n" \
  "s_barrier\n" \
  "ds_read_b128 v[8:11], v0 offset:0\n" \
  "ds_read_b128 v[12:15], v0 offset:1024\n" \
  "ds_read_b128 v[16:19], v0 offset:2048\n" \
  "s_add_u32 s68, s68, 1\n" \
  "s_add_u32 s64, s54, 16384\n" \
  "s_cmp_eq_u32 s69, 0\n" \
  "s_cbranch_scc1 .LCY_NI_%=\n" \
  "s_waitcnt vmcnt(3)\n" \
  "s_cmp_lg_u32 s72, 0\n" \
  "s_cbranch_scc1 .LCY_NJ_%=\n" \
  "s_waitcnt vmcnt(0)\n" \
  ".LCY_NJ_%=:\n" \
  "v_mov_b32 v6, s55\n" \
  "s_mov_b64 exec, 1\n" \
  "ds_add_u32 v5, v6\n" \
  "s_mov_b64 exec, -1\n" \
  "s_mov_b32 s69, 0\n" \
  ".LCY_NI_%=:\n" \
  "v_cmp_eq_u32 vcc, 0, v4\n" \
  "s_cmp_eq_u64 vcc, 0\n" \
  "s_cbranch_scc0 .LCY_NS_%=\n" \
  "v_lshlrev_b32 v6, 16, s55\n" \
  "s_mov_b64 exec, 1\n" \
  "ds_add_u32 v5, v6\n" \
  "s_mov_b64 exec, -1\n" \
  "s_mov_b32 m0, s56\n" \
  "s_add_u32 s71, s56, 4096\n" \
  "global_load_lds_dwordx4 v2, s[48:49] offset:0 sc1\n" \
  "global_load_lds_dwordx4 v2, s[48:49] offset:1024 sc1\n" \
  "global_load_lds_dwordx4 v2, s[48:49] offset:2048 sc1\n" \
  "global_load_lds_dwordx4 v2, s[48:49] offset:3072 sc1\n" \
  "s_mov_b32 m0, s71\n" \
  "s_add_u32 s56, s56, s65\n" \
  "global_load_lds_dwordx4 v3, s[48:49] offset:0 sc1\n" \
  "global_load_lds_dwordx4 v3, s[48:49] offset:1024 sc1\n" \
  ".LCY_NS_%=:\n" \
  "s_cmp_eq_u32 s72, 0\n" \
  "s_cbranch_scc1 .LCY_NK_%=\n" \
  "s_waitcnt vmcnt(6)\n" \
  "s_mov_b32 s72, 0\n" \
  "s_cbranch_vccz .LCY_NL_%=\n" \
  "s_waitcnt vmcnt(0)\n" \
  ".LCY_NL_%=:\n" \
  "v_mov_b32 v6, s59\n" \
  "v_mov_b32 v7, 1\n" \
  "s_mov_b64 exec, 1\n" \
  "ds_add_rtn_u32 v6, v6, v7\n" \
  "s_mov_b64 exec, -1\n" \
  "s_xor_b32 s59, s59, 4\n" \
  "s_mov_b32 s61, 0\n" \
  "s_waitcnt lgkmcnt(0)\n" \
  "v_readfirstlane_b32 s63, v6\n" \
  "v_lshlrev_b32 v6, 5, v2\n" \
  "s_and_b32 s63, s63, 7\n" \
  "s_cmp_eq_u32 s63, 7\n" \
  "s_cbranch_scc0 .LCY_W1_%=\n" \
  "s_mov_b64 exec, 15\n" \
  "global_store_dword v6, v7, s[52:53] sc1\n" \
  "s_mov_b64 exec, -1\n" \
  ".LCY_W1_%=:\n" \
  "s_add_u32 s52, s52, 2048\n" \
  "s_addc_u32 s53, s53, 0\n" \
  ".LCY_NK_%=:\n" \
  "ds_read_b128 v[20:23], v0 offset:3072\n" \
  "s_waitcnt lgkmcnt(3)\n" \
  "v_mfma_f32_16x16x32_bf16 v[32:35], v[64:67], v[8:11], 0\n" \
  "v_mfma_f32_16x16x32_bf16 v[36:39], v[96:99], v[8:11], 0\n" \
  "v_mfma_f32_16x16x32_bf16 v[40:43], v[128:131], v[8:11], 0\n" \
  "v_mfma_f32_16x16x32_bf16 v[44:47], v[160:163], v[8:11], 0\n" \
  "v_mfma_f32_16x16x32_bf16 v[48:51], v[192:195], v[8:11], 0\n" \
  "v_mfma_f32_16x16x32_bf16 v[52:55], v[224:227], v[8:11], 0\n" \
  "ds_read_b128 v[8:11], v0 offset:8192\n" \
  "s_waitcnt lgkmcnt(3)\n" \
  "v_mfma_f32_16x16x32_bf16 v[32:35], v[68:71], v[12:15], v[32:35]\n" \
  "v_mfma_f32_16x16x32_bf16 v[36:39], v[100:103], v[12:15], v[36:39]\n" \
  "v_mfma_f32_16x16x32_bf16 v[40:43], v[132:135], v[12:15], v[40:43]\n" \
  "v_mfma_f32_16x16x32_bf16 v[44:47], v[164:167], v[12:15], v[44:47]\n" \
  "v_mfma_f32_16x16x32_bf16 v[48:51], v[196:199], v[12:15], v[48:51]\n" \
  "v_mfma_f32_16x16x32_bf16 v[52:55], v[228:231], v[12:15], v[52:55]\n" \
  "s_cbranch_vccnz .LCY_D2_%=\n" \
  "global_load_lds_dwordx4 v3, s[48:49] offset:2048 sc1\n" \
  ".LCY_D2_%=:\n" \
  "ds_read_b128 v[12:15], v0 offset:9216\n" \
  "s_waitcnt lgkmcnt(3)\n" \
  "v_mfma_f32_16x16x32_bf16 v[32:35], v[72:75], v[16:19], v[32:35]\n" \
  "v_mfma_f32_16x16x32_bf16 v[36:39], v[104:107], v[16:19], v[36:39]\n" \
  "v_mfma_f32_16x16x32_bf16 v[40:43], v[136:139], v[16:19], v[40:43]\n" \
  "v_mfma_f32_16x16x32_bf16 v[44:47], v[168:171], v[16:19], v[44:47]\n" \
  "v_mfma_f32_16x16x32_bf16 v[48:51], v[200:203], v[16:19], v[48:51]\n" \
  "v_mfma_f32_16x16x32_bf16 v[52:55], v[232:235], v[16:19], v[52:55]\n" \
  "s_cbranch_vccnz .LCY_D3_%=\n" \
  "global_load_lds_dwordx4 v3, s[48:49] offset:3072 sc1\n" \
  "s_add_u32 s48, s48, 0x80000\n" \
  "s_addc_u32 s49, s49, 0\n" \
  ".LCY_D3_%=:\n" \
  "ds_read_b128 v[16:19], v0 offset:10240\n" \
  "s_waitcnt lgkmcnt(3)\n" \
  "v_mfma_f32_16x16x32_bf16 v[32:35], v[76:79], v[20:23], v[32:35]\n" \
  "v_mfma_f32_16x16x32_bf16 v[36:39], v[108:111], v[20:23], v[36:39]\n" \
  "v_mfma_f32_16x16x32_bf16 v[40:43], v[140:143], v[20:23], v[40:43]\n" \
  "v_mfma_f32_16x16x32_bf16 v[44:47], v[172:175], v[20:23], v[44:47]\n" \
  "v_mfma_f32_16x16x32_bf16 v[48:51], v[204:207], v[20:23], v[48:51]\n" \
  "v_mfma_f32_16x16x32_bf16 v[52:55], v[236:239], v[20:23], v[52:55]\n" \
  "s_cbranch_vccnz .LCY_D4_%=\n" \
  "s_mov_b32 m0, s57\n" \
  "s_add_u32 s57, s57, s65\n" \
  "global_load_lds_dwordx4 v2, s[50:51] offset:0 sc1\n" \
  ".LCY_D4_%=:\n" \
  "ds_read_b128 v[20:23], v0 offset:11264\n" \
  "s_waitcnt lgkmcnt(3)\n" \
  "v_mfma_f32_16x16x32_bf16 v[32:35], v[80:83], v[8:11], v[32:35]\n" \
  "v_mfma_f32_16x16x32_bf16 v[36:39], v[112:115], v[8:11], v[36:39]\n" \
  "v_mfma_f32_16x16x32_bf16 v[40:43], v[144:147], v[8:11], v[40:43]\n" \
  "v_mfma_f32_16x16x32_bf16 v[44:47], v[176:179], v[8:11], v[44:47]\n" \
  "v_mfma_f32_16x16x32_bf16 v[48:51], v[208:211], v[8:11], v[48:51]\n" \
  "v_mfma_f32_16x16x32_bf16 v[52:55], v[240:243], v[8:11], v[52:55]\n" \
  "s_cbranch_vccnz .LCY_D5_%=\n" \
  "global_load_lds_dwordx4 v2, s[50:51] offset:1024 sc1\n" \
  "s_sub_u32 s65, 0, s65\n" \
  "s_add_u32 s67, s67, 4096\n" \
  ".LCY_D5_%=:\n" \
  "ds_read_b128 v[8:11], v0 offset:4096\n" \
  "s_waitcnt lgkmcnt(3)\n" \
  "v_mfma_f32_16x16x32_bf16 v[32:35], v[84:87], v[12:15], v[32:35]\n" \
  "v_mfma_f32_16x16x32_bf16 v[36:39], v[116:119], v[12:15], v[36:39]\n" \
  "v_mfma_f32_16x16x32_bf16 v[40:43], v[148:151], v[12:15], v[40:43]\n" \
  "v_mfma_f32_16x16x32_bf16 v[44:47], v[180:183], v[12:15], v[44:47]\n" \
  "v_mfma_f32_16x16x32_bf16 v[48:51], v[212:215], v[12:15], v[48:51]\n" \
  "v_mfma_f32_16x16x32_bf16 v[52:55], v[244:247], v[12:15], v[52:55]\n" \
  "s_cbranch_vccnz .LCY_D6_%=\n" \
  "global_load_lds_dwordx4 v2, s[50:51] offset:2048 sc1\n" \
  "s_sub_u32 s68, s68, 1\n" \
  "s_mov_b32 s69, 1\n" \
  ".LCY_D6_%=:\n" \
  "ds_read_b128 v[12:15], v0 offset:5120\n" \
  "s_waitcnt lgkmcnt(3)\n" \
  "v_mfma_f32_16x16x32_bf16 v[32:35], v[88:91], v[16:19], v[32:35]\n" \
  "v_mfma_f32_16x16x32_bf16 v[36:39], v[120:123], v[16:19], v[36:39]\n" \
  "v_mfma_f32_16x16x32_bf16 v[40:43], v[152:155], v[16:19], v[40:43]\n" \
  "v_mfma_f32_16x16x32_bf16 v[44:47], v[184:187], v[16:19], v[44:47]\n" \
  "v_mfma_f32_16x16x32_bf16 v[48:51], v[216:219], v[16:19], v[48:51]\n" \
  "v_mfma_f32_16x16x32_bf16 v[52:55], v[248:251], v[16:19], v[52:55]\n" \
  "s_cbranch_vccnz .LCY_D7_%=\n" \
  "global_load_lds_dwordx4 v2, s[50:51] offset:3072 sc1\n" \
  "s_add_u32 s50, s50, 0x40000\n" \
  "s_addc_u32 s51, s51, 0\n" \
  ".LCY_D7_%=:\n" \
  "ds_read_b128 v[16:19], v0 offset:6144\n" \
  "s_waitcnt lgkmcnt(3)\n" \
  "v_mfma_f32_16x16x32_bf16 v[32:35], v[92:95], v[20:23], v[32:35]\n" \
  "v_mfma_f32_16x16x32_bf16 v[36:39], v[124:127], v[20:23], v[36:39]\n" \
  "v_mfma_f32_16x16x32_bf16 v[40:43], v[156:159], v[20:23], v[40:43]\n" \
  "v_mfma_f32_16x16x32_bf16 v[44:47], v[188:191], v[20:23], v[44:47]\n" \
  "v_mfma_f32_16x16x32_bf16 v[48:51], v[220:223], v[20:23], v[48:51]\n" \
  "v_mfma_f32_16x16x32_bf16 v[52:55], v[252:255], v[20:23], v[52:55]\n" \
  "ds_read_b128 v[20:23], v0 offset:7168\n" \
  "s_waitcnt lgkmcnt(3)\n" \
  "s_nop 1\n" \
  "v_cvt_pk_bf16_f32 v24, v32, v33\n" \
  "v_cvt_pk_bf16_f32 v25, v34, v35\n" \
  "v_cvt_pk_bf16_f32 v26, v36, v37\n" \
  "v_cvt_pk_bf16_f32 v27, v38, v39\n" \
  "s_nop 0\n" \
  "v_mfma_f32_16x16x32_bf16 v[32:35], v[64:67], v[8:11], 0\n" \
  "v_mfma_f32_16x16x32_bf16 v[36:39], v[96:99], v[8:11], 0\n" \
  "buffer_store_dwordx4 v[24:27], v1, s[40:43], s54 offen offset:0 sc1\n" \
  "v_cvt_pk_bf16_f32 v28, v40, v41\n" \
  "v_cvt_pk_bf16_f32 v29, v42, v43\n" \
  "v_cvt_pk_bf16_f32 v30, v44, v45\n" \
  "v_cvt_pk_bf16_f32 v31, v46, v47\n" \
  "s_nop 0\n" \
  "v_mfma_f32_16x16x32_bf16 v[40:43], v[128:131], v[8:11], 0\n" \
  "v_mfma_f32_16x16x32_bf16 v[44:47], v[160:163], v[8:11], 0\n" \
  "buffer_store_dwordx4 v[28:31], v1, s[40:43], s54 offen offset:128 sc1\n" \
  "v_cvt_pk_bf16_f32 v56, v48, v49\n" \
  "v_cvt_pk_bf16_f32 v57, v50, v51\n" \
  "v_cvt_pk_bf16_f32 v58, v52, v53\n" \
  "v_cvt_pk_bf16_f32 v59, v54, v55\n" \
  "s_nop 0\n" \
  "v_mfma_f32_16x16x32_bf16 v[48:51], v[192:195], v[8:11], 0\n" \
  "v_mfma_f32_16x16x32_bf16 v[52:55], v[224:227], v[8:11], 0\n" \
  "buffer_store_dwordx4 v[56:59], v1, s[40:43], s54 offen offset:256 sc1\n" \
  "ds_read_b128 v[8:11], v0 offset:12288\n" \
  "s_waitcnt lgkmcnt(3)\n" \
  "v_mfma_f32_16x16x32_bf16 v[32:35], v[68:71], v[12:15], v[32:35]\n" \
  "v_mfma_f32_16x16x32_bf16 v[36:39], v[100:103], v[12:15], v[36:39]\n" \
  "v_mfma_f32_16x16x32_bf16 v[40:43], v[132:135], v[12:15], v[40:43]\n" \
  "v_mfma_f32_16x16x32_bf16 v[44:47], v[164:167], v[12:15], v[44:47]\n" \
  "v_mfma_f32_16x16x32_bf16 v[48:51], v[196:199], v[12:15], v[48:51]\n" \
  "v_mfma_f32_16x16x32_bf16 v[52:55], v[228:231], v[12:15], v[52:55]\n" \
  "ds_read_b128 v[12:15], v0 offset:13312\n" \
  "s_waitcnt lgkmcnt(3)\n" \
  "v_mfma_f32_16x16x32_bf16 v[32:35], v[72:75], v[16:19], v[32:35]\n" \
  "v_mfma_f32_16x16x32_bf16 v[36:39], v[104:107], v[16:19], v[36:39]\n" \
  "v_mfma_f32_16x16x32_bf16 v[40:43], v[136:139], v[16:19], v[40:43]\n" \
  "v_mfma_f32_16x16x32_bf16 v[44:47], v[168:171], v[16:19], v[44:47]\n" \
  "v_mfma_f32_16x16x32_bf16 v[48:51], v[200:203], v[16:19], v[48:51]\n" \
  "v_mfma_f32_16x16x32_bf16 v[52:55], v[232:235], v[16:19], v[52:55]\n" \
  "ds_read_b128 v[16:19], v0 offset:14336\n" \
  "s_waitcnt lgkmcnt(3)\n" \
  "v_mfma_f32_16x16x32_bf16 v[32:35], v[76:79], v[20:23], v[32:35]\n" \
  "v_mfma_f32_16x16x32_bf16 v[36:39], v[108:111], v[20:23], v[36:39]\n" \
  "v_mfma_f32_16x16x32_bf16 v[40:43], v[140:143], v[20:23], v[40:43]\n" \
  "v_mfma_f32_16x16x32_bf16 v[44:47], v[172:175], v[20:23], v[44:47]\n" \
  "v_mfma_f32_16x16x32_bf16 v[48:51], v[204:207], v[20:23], v[48:51]\n" \
  "v_mfma_f32_16x16x32_bf16 v[52:55], v[236:239], v[20:23], v[52:55]\n" \
  "ds_read_b32 v7, v5 offset:8\n" \
  "ds_read_b128 v[20:23], v0 offset:15360\n" \
  "s_waitcnt lgkmcnt(4)\n" \
  "v_mfma_f32_16x16x32_bf16 v[32:35], v[80:83], v[8:11], v[32:35]\n" \
  "v_mfma_f32_16x16x32_bf16 v[36:39], v[112:115], v[8:11], v[36:39]\n" \
  "v_mfma_f32_16x16x32_bf16 v[40:43], v[144:147], v[8:11], v[40:43]\n" \
  "v_mfma_f32_16x16x32_bf16 v[44:47], v[176:179], v[8:11], v[44:47]\n" \
  "v_mfma_f32_16x16x32_bf16 v[48:51], v[208:211], v[8:11], v[48:51]\n" \
  "v_mfma_f32_16x16x32_bf16 v[52:55], v[240:243], v[8:11], v[52:55]\n" \
  "s_waitcnt lgkmcnt(3)\n" \
  "v_mfma_f32_16x16x32_bf16 v[32:35], v[84:87], v[12:15], v[32:35]\n" \
  "v_mfma_f32_16x16x32_bf16 v[36:39], v[116:119], v[12:15], v[36:39]\n" \
  "v_mfma_f32_16x16x32_bf16 v[40:43], v[148:151], v[12:15], v[40:43]\n" \
  "v_mfma_f32_16x16x32_bf16 v[44:47], v[180:183], v[12:15], v[44:47]\n" \
  "v_mfma_f32_16x16x32_bf16 v[48:51], v[212:215], v[12:15], v[48:51]\n" \
  "v_mfma_f32_16x16x32_bf16 v[52:55], v[244:247], v[12:15], v[52:55]\n" \
  "s_waitcnt lgkmcnt(2)\n" \
  "v_mfma_f32_16x16x32_bf16 v[32:35], v[88:91], v[16:19], v[32:35]\n" \
  "v_mfma_f32_16x16x32_bf16 v[36:39], v[120:123], v[16:19], v[36:39]\n" \
  "v_mfma_f32_16x16x32_bf16 v[40:43], v[152:155], v[16:19], v[40:43]\n" \
  "v_mfma_f32_16x16x32_bf16 v[44:47], v[184:187], v[16:19], v[44:47]\n" \
  "v_mfma_f32_16x16x32_bf16 v[48:51], v[216:219], v[16:19], v[48:51]\n" \
  "v_mfma_f32_16x16x32_bf16 v[52:55], v[248:251], v[16:19], v[52:55]\n" \
  "s_cbranch_vccnz .LCY_NT_%=\n" \
  "v_mov_b32 v4, 0\n" \
  "buffer_load_dword v4, v60, s[44:47], s67 offen sc1\n" \
  ".LCY_NT_%=:\n" \
  "s_waitcnt lgkmcnt(0)\n" \
  "v_mfma_f32_16x16x32_bf16 v[32:35], v[92:95], v[20:23], v[32:35]\n" \
  "v_mfma_f32_16x16x32_bf16 v[36:39], v[124:127], v[20:23], v[36:39]\n" \
  "v_mfma_f32_16x16x32_bf16 v[40:43], v[156:159], v[20:23], v[40:43]\n" \
  "v_mfma_f32_16x16x32_bf16 v[44:47], v[188:191], v[20:23], v[44:47]\n" \
  "v_mfma_f32_16x16x32_bf16 v[48:51], v[220:223], v[20:23], v[48:51]\n" \
  "v_mfma_f32_16x16x32_bf16 v[52:55], v[252:255], v[20:23], v[52:55]\n" \
  "v_add_u32 v0, s66, v0\n" \
  "s_sub_u32 s66, 0, s66\n" \
  "s_add_u32 s54, s54, 0x80000\n" \
  "s_mov_b32 s62, 0x40000\n" \
  "v_readfirstlane_b32 s63, v7\n" \
  "v_cvt_pk_bf16_f32 v24, v32, v33\n" \
  "v_cvt_pk_bf16_f32 v25, v34, v35\n" \
  "v_cvt_pk_bf16_f32 v26, v36, v37\n" \
  "v_cvt_pk_bf16_f32 v27, v38, v39\n" \
  "buffer_store_dwordx4 v[24:27], v1, s[40:43], s64 offen offset:0 sc1\n" \
  "v_cvt_pk_bf16_f32 v28, v40, v41\n" \
  "v_cvt_pk_bf16_f32 v29, v42, v43\n" \
  "v_cvt_pk_bf16_f32 v30, v44, v45\n" \
  "v_cvt_pk_bf16_f32 v31, v46, v47\n" \
  "buffer_store_dwordx4 v[28:31], v1, s[40:43], s64 offen offset:128 sc1\n" \
  "v_cvt_pk_bf16_f32 v56, v48, v49\n" \
  "v_cvt_pk_bf16_f32 v57, v50, v51\n" \
  "v_cvt_pk_bf16_f32 v58, v52, v53\n" \
  "v_cvt_pk_bf16_f32 v59, v54, v55\n" \
  "buffer_store_dwordx4 v[56:59], v1, s[40:43], s64 offen offset:256 sc1\n" \
  "s_cbranch_vccnz .LCY_ND_%=\n" \
  "s_add_u32 s63, s63, s60\n" \
  "s_cmp_eq_u32 s63, 66\n" \
  "s_cselect_b32 s72, 1, 0\n" \
  "s_cbranch_scc1 .LCY_DEF_%=\n" \
  "s_waitcnt vmcnt(0)\n" \
  "v_mov_b32 v6, s55\n" \
  "s_mov_b64 exec, 1\n" \
  "ds_add_u32 v5, v6\n" \
  "s_mov_b64 exec, -1\n" \
  "s_mov_b32 s69, 0\n" \
  "s_branch .LCY_WC_%=\n" \
  ".LCY_ND_%=:\n" \
  "v_mov_b32 v4, 0\n" \
  "buffer_load_dword v4, v60, s[44:47], s67 offen sc1\n" \
  "s_waitcnt vmcnt(0)\n" \
  ".LCY_WC_%=:\n" \
  "v_mov_b32 v6, s59\n" \
  "v_mov_b32 v7, 1\n" \
  "s_mov_b64 exec, 1\n" \
  "ds_add_rtn_u32 v6, v6, v7\n" \
  "s_mov_b64 exec, -1\n" \
  "s_xor_b32 s59, s59, 4\n" \
  "s_mov_b32 s61, 0\n" \
  "s_waitcnt lgkmcnt(0)\n" \
  "v_readfirstlane_b32 s63, v6\n" \
  "v_lshlrev_b32 v6, 5, v2\n" \
  "s_and_b32 s63, s63, 7\n" \
  "s_cmp_eq_u32 s63, 7\n" \
  "s_cbranch_scc0 .LCY_W8_%=\n" \
  "s_mov_b64 exec, 15\n" \
  "global_store_dword v6, v7, s[52:53] sc1\n" \
  "s_mov_b64 exec, -1\n" \
  ".LCY_W8_%=:\n" \
  "s_add_u32 s52, s52, 2048\n" \
  "s_addc_u32 s53, s53, 0\n" \
  ".LCY_DEF_%=:\n" \
  "s_sub_u32 s60, s60, 1\n" \
  "s_cmp_eq_u32 s60, 0\n" \
  "s_cbranch_scc1 .LCY_END_%=\n" \
  ".LCY_NEED_%=:\n" \
  "s_cmp_eq_u32 s68, 0\n" \
  "s_cbranch_scc1 .LCY_BAR_%=\n" \
  "v_cmp_eq_u32 vcc, 0, v4\n" \
  "s_cmp_eq_u64 vcc, 0\n" \
  "s_cbranch_scc1 .LCY_GET_%=\n" \
  "s_cmp_ge_u32 s67, 0x40000\n" \
  "s_cbranch_scc1 .LCY_BAR_%=\n" \
  "s_sleep 4\n" \
  "ds_read_b32 v6, v5 offset:16\n" \
  "s_waitcnt lgkmcnt(0)\n" \
  "v_readfirstlane_b32 s63, v6\n" \
  "s_add_u32 s63, s63, s60\n" \
  "s_cmp_ge_u32 s63, 66\n" \
  "s_cbranch_scc1 .LCY_BAR_%=\n" \
  ".LCY_ASK_%=:\n" \
  "v_mov_b32 v4, 0\n" \
  "buffer_load_dword v4, v60, s[44:47], s67 offen sc1\n" \
  "s_sub_u32 s62, s62, 1\n" \
  "s_cbranch_scc1 .LCY_END_%=\n" \
  "s_waitcnt vmcnt(0)\n" \
  "s_branch .LCY_NEED_%=\n" \
  ".LCY_GET_%=:\n" \
  "v_lshlrev_b32 v6, 16, s55\n" \
  "s_mov_b64 exec, 1\n" \
  "ds_add_u32 v5, v6\n" \
  "s_mov_b64 exec, -1\n" \
  "s_mov_b32 m0, s56\n" \
  "s_add_u32 s71, s56, 4096\n" \
  "global_load_lds_dwordx4 v2, s[48:49] offset:0 sc1\n" \
  "global_load_lds_dwordx4 v2, s[48:49] offset:1024 sc1\n" \
  "global_load_lds_dwordx4 v2, s[48:49] offset:2048 sc1\n" \
  "global_load_lds_dwordx4 v2, s[48:49] offset:3072 sc1\n" \
  "s_mov_b32 m0, s71\n" \
  "s_add_u32 s56, s56, s65\n" \
  "global_load_lds_dwordx4 v3, s[48:49] offset:0 sc1\n" \
  "global_load_lds_dwordx4 v3, s[48:49] offset:1024 sc1\n" \
  "global_load_lds_dwordx4 v3, s[48:49] offset:2048 sc1\n" \
  "global_load_lds_dwordx4 v3, s[48:49] offset:3072 sc1\n" \
  "s_add_u32 s48, s48, 0x80000\n" \
  "s_addc_u32 s49, s49, 0\n" \
  "s_mov_b32 m0, s57\n" \
  "s_add_u32 s57, s57, s65\n" \
  "global_load_lds_dwordx4 v2, s[50:51] offset:0 sc1\n" \
  "global_load_lds_dwordx4 v2, s[50:51] offset:1024 sc1\n" \
  "s_sub_u32 s65, 0, s65\n" \
  "s_add_u32 s67, s67, 4096\n" \
  "global_load_lds_dwordx4 v2, s[50:51] offset:2048 sc1\n" \
  "s_sub_u32 s68, s68, 1\n" \
  "s_mov_b32 s69, 1\n" \
  "global_load_lds_dwordx4 v2, s[50:51] offset:3072 sc1\n" \
  "s_add_u32 s50, s50, 0x40000\n" \
  "s_addc_u32 s51, s51, 0\n" \
  "v_mov_b32 v4, 0\n" \
  "buffer_load_dword v4, v60, s[44:47], s67 offen sc1\n" \
  "s_mov_b32 s61, 0\n" \
  "s_cmp_eq_u32 s68, 0\n" \
  "s_cbranch_scc1 .LCY_BAR_%=\n" \
  "s_waitcnt vmcnt(0)\n" \
  "v_mov_b32 v6, s55\n" \
  "s_mov_b64 exec, 1\n" \
  "ds_add_u32 v5, v6\n" \
  "s_mov_b64 exec, -1\n" \
  "s_mov_b32 s69, 0\n" \
  "s_branch .LCY_NEED_%=\n" \
  ".LCY_END_%=:\n" \
  "s_endpgm\n"
template <bool AT>
__device__ __forceinline__ void role_aw_t(const MegaArgs& A, char* smem, const int L, const int a, const u64 tag) {
  const int w = A.wv;
  const int l = lane_id(), c = l & 15, g = l >> 4;
  const int qt = w & 1, hh = (w >> 1) & 1, h = 2 * a + hh;
  char* ws = A.ws;
  unsigned* FL = (unsigned*)(ws + OFF_FL);
  const unsigned sb = lds_base(smem);
  if (w == 0 && l < 8) reinterpret_cast<unsigned*>(smem + AW_CNT)[l] = 0u;
  constexpr int NP = AT ? 1 : 3;
  const int cp0 = AT ? w : 3 * w - 8;
  v8u wo[2 * NP][4];
  {
    const int q = (l >> 2) & 3, p = l & 3, f = q | ((g & 1) << 2);
    const unsigned ra = sb + (4 * g + q) * 256 + (p & 1) * 16;
#pragma unroll
    for (int r = 0; r < 2; ++r) {
#pragma unroll
      for (int k = 0; k < 2; ++k) {
        const char* base_ = (const char*)A.Wo + ((size_t)((2 * a + r) * DH + 64 * (w & 1) + 32 * k) * DM + 128 * (w >> 1)) * 2;
#pragma unroll
        for (int i = 0; i < 8; ++i) {
          const int fd = g | ((i & 1) << 2);
          glds16(base_, (unsigned)((4 * i + g) * (DM * 2) + ((c ^ (fd << 1)) << 4)), sb + w * 16384 + k * 8192 + i * 1024, WNT & 16);
        }
      }
      SST(4 + 3 * r);
      if (r == 1) ready_wait(A, tag, ready_look(A));
      WAIT_VM(0);
      RAW_BAR();
      SST(5 + 3 * r);
#pragma unroll
      for (int k2 = 0; k2 < 2; ++k2) {
#pragma unroll
        for (int ct = 0; ct < 2 * NP; ++ct) {
          const int cp = cp0 + (ct >> 1);
          const unsigned a_ = ra + (cp >> 2) * 32768 + k2 * 16384 + ((2 * (cp & 3) + (p >> 1)) ^ f) * 32 + (ct & 1) * 8;
          const v2u r0 = trd(a_), r1 = trd(a_ + 4096), r2 = trd(a_ + 8192), r3 = trd(a_ + 12288);
          wo[ct][2 * r + k2] = v8u{r0.x, r0.y, r1.x, r1.y, r2.x, r2.y, r3.x, r3.y};
          asm volatile("" : "+v"(wo[ct][2 * r + k2]));
        }
      }
      RAW_BAR();
      SST(6 + 3 * r);
    }
  }
  WSTAMP2();
  const v4u rsW = mk_srd(ws + OFF_PW, SZ_PW);
  const int dp = (FL_FP - FL_FQ + (L * 16 + a - L * 64 * 32 - h) * 32 + 2 * hh) * 4;
  const v4u rsF = mk_srd(FL + FL_FQ + (L * 64 * 32 + h) * 32, (qt ? 0 : dp) + 64 * 4096 - 128);
  const char* p0 = AT ? ws + OFF_MEMK + L * SZ_MEM + h * 2048 : ws + OFF_QK + L * SZ_QK + ((size_t)h * 16 + 8 * qt) * 1024;
  const char* p1 = AT ? (const char*)(FL + FL_FK + L * 32 + (h >> 3) * 64 + (h & 7) * 2) : ws + OFF_VF + L * SZ_O + ((size_t)h * 8 + 4 * qt) * 1024;
  const unsigned* pFW = FL + FL_FW + (L * 64 * 16 + (a >> 2)) * 32 + (a & 3);
  const unsigned tk = sb + AW_TK + hh * 24 * 1024, voff = (c >> 1) * 2048 + (c & 1) * 64 + g * 16;
  const char* dbg = ws + WS_TOTAL + A.vb * 64 * 8;
#define AW_SOPS                                                                                                                 \
  "{s[40:43]}"(rsW), "{s[44:47]}"(rsF), "{s[48:49]}"(p0), "{s[50:51]}"(p1), "{s[52:53]}"(pFW),                                   \
      "{s54}"((L * 64 * 16 + a) * 32768 + cp0 * 128 + (AT ? qt * 16384 : 0)), "{s55}"(1 << (8 * qt)), "{s56}"(tk + qt * 8192), "{s57}"(tk + 16384 + qt * 4096),                  \
      "{s58}"(sb + AW_CNT + 8 + 4 * hh), "{s59}"(sb + AW_CNT), "{s60}"(NSEG), "{s[36:37]}"(dbg)                                  \
      : "memory", "vcc", "scc", "s80"
  if constexpr (AT) {
    const v8u pv = {tk + l * 16, (c & 8) ? 0xEEEEu : 0x4444u, 0u, (unsigned)(OFF_MEMV - OFF_MEMK) + c * 32 + (g & 1) * 16 + (g >> 1) * 1024,
                    voff, (unsigned)l * 16, tk + qt * 4096 + l * 16, sb + AW_SO + w * 4096 + l * 16};
    const unsigned neg = 0xff61b1e6u;
    v8u bt;
#pragma unroll
    for (int i = 0; i < 4; ++i) {
      const unsigned d = 4 * g + i > c ? neg : 0u;
      bt[i] = qt ? 0u : d;
      bt[4 + i] = qt ? d : neg;
    }
    asm volatile("s_mov_b64 exec, -1\ns_waitcnt vmcnt(0) lgkmcnt(0)\ns_nop 4\n" AW_ASM_X ".LKG_LAST:\n"
                 ::"{v[192:199]}"(wo[0][0]), "{v[200:207]}"(wo[0][1]), "{v[208:215]}"(wo[0][2]), "{v[216:223]}"(wo[0][3]),
                 "{v[224:231]}"(wo[1][0]), "{v[232:239]}"(wo[1][1]), "{v[240:247]}"(wo[1][2]), "{v[248:255]}"(wo[1][3]),
                 "{v[0:7]}"(pv), "{v[8:15]}"(bt), AW_SOPS);
  } else {
    const v4u pv = {sb + AW_SO + l * 16, voff, (unsigned)l * 16, (l & 2) ? 16u + 8 * qt : qt ? 8u * (l & 1) : dp + 4 * (l & 1)};
    asm volatile("s_mov_b64 exec, -1\ns_waitcnt vmcnt(0) lgkmcnt(0)\ns_nop 4\n" AW_ASM_Y
                 ::"{v[64:71]}"(wo[0][0]), "{v[72:79]}"(wo[0][1]), "{v[80:87]}"(wo[0][2]), "{v[88:95]}"(wo[0][3]),
                 "{v[96:103]}"(wo[1][0]), "{v[104:111]}"(wo[1][1]), "{v[112:119]}"(wo[1][2]), "{v[120:127]}"(wo[1][3]),
                 "{v[128:135]}"(wo[2][0]), "{v[136:143]}"(wo[2][1]), "{v[144:151]}"(wo[2][2]), "{v[152:159]}"(wo[2][3]),
                 "{v[160:167]}"(wo[3][0]), "{v[168:175]}"(wo[3][1]), "{v[176:183]}"(wo[3][2]), "{v[184:191]}"(wo[3][3]),
                 "{v[192:199]}"(wo[4][0]), "{v[200:207]}"(wo[4][1]), "{v[208:215]}"(wo[4][2]), "{v[216:223]}"(wo[4][3]),
                 "{v[224:231]}"(wo[5][0]), "{v[232:239]}"(wo[5][1]), "{v[240:247]}"(wo[5][2]), "{v[248:255]}"(wo[5][3]),
                 "{v[0:3]}"(pv), AW_SOPS);
  }
#undef AW_SOPS
}
__device__ __forceinline__ void role_aw(const MegaArgs& A, char* smem, const int L, const int a, const u64 tag) {
  if (A.wv < 4) role_aw_t<true>(A, smem, L, a, tag);
  else role_aw_t<false>(A, smem, L, a, tag);
}

#define P_ASM \
  "s_mov_b32 s68, 0\n" \
  "s_mov_b32 s66, 3\n" \
  "s_mov_b32 s67, 2\n" \
  "s_mov_b32 s72, 0\n" \
  "v_mov_b32 v7, 1\n" \
  "v_mov_b32 v6, s70\n" \
  "v_mov_b32 v5, 0\n" \
  "v_add_u32 v10, 4096, v2\n" \
  ".p2align 6\n" \
  ".LCP_TOP_%=:\n" \
  "ds_read_b128 v[28:31], v1 offset:0\n" \
  "ds_read_b128 v[32:35], v1 offset:1024\n" \
  "s_add_u32 s66, s66, 3\n" \
  "s_add_u32 s67, s67, 2\n" \
  "s_mov_b32 s69, 0x400000\n" \
  "v_xor_b32 v0, 0x8000, v0\n" \
  "v_xor_b32 v3, 0x100, v3\n" \
  "v_readfirstlane_b32 s71, v5\n" \
  "s_cmp_ge_u32 s71, s66\n" \
  "s_cbranch_scc1 .LCP_GO_%=\n" \
  "s_cmp_eq_u32 s68, 1\n" \
  "s_cbranch_scc0 .LCP_X1_%=\n" \
  "s_waitcnt vmcnt(0)\n" \
  "s_mov_b64 exec, 1\n" \
  "buffer_store_dword v7, off, s[56:59], s72 sc1\n" \
  "s_mov_b64 exec, -1\n" \
  "s_add_u32 s72, s72, 0x1000\n" \
  "s_mov_b32 s68, 0\n" \
  ".LCP_X1_%=:\n" \
  ".LCP_S2_%=:\n" \
  "s_sleep 1\n" \
  "ds_read_b32 v5, v6 offset:0\n" \
  "s_sub_u32 s69, s69, 1\n" \
  "s_cbranch_scc1 .LCP_END_%=\n" \
  "s_waitcnt lgkmcnt(0)\n" \
  "v_readfirstlane_b32 s71, v5\n" \
  "s_cmp_ge_u32 s71, s66\n" \
  "s_cbranch_scc1 .LCP_GO_%=\n" \
  "s_branch .LCP_S2_%=\n" \
  ".LCP_GO_%=:\n" \
  "ds_read_b128 v[12:15], v0 offset:0\n" \
  "ds_read_b128 v[16:19], v0 offset:2048\n" \
  "ds_read_b128 v[20:23], v0 offset:4096\n" \
  "ds_read_b128 v[24:27], v0 offset:6144\n" \
  "ds_read_b128 v[36:39], v1 offset:2048\n" \
  "s_waitcnt lgkmcnt(4)\n" \
  "v_mfma_f32_16x16x32_bf16 v[48:51], v[64:67], v[12:15], 0\n" \
  "v_mfma_f32_16x16x32_bf16 v[52:55], v[128:131], v[12:15], 0\n" \
  "v_mfma_f32_16x16x32_bf16 v[56:59], v[192:195], v[12:15], 0\n" \
  "v_mfma_f32_16x16x32_bf16 v[60:63], v[28:31], v[12:15], 0\n" \
  "ds_read_b128 v[12:15], v0 offset:8192\n" \
  "ds_read_b128 v[28:31], v1 offset:3072\n" \
  "s_waitcnt lgkmcnt(5)\n" \
  "v_mfma_f32_16x16x32_bf16 v[48:51], v[68:71], v[16:19], v[48:51]\n" \
  "v_mfma_f32_16x16x32_bf16 v[52:55], v[132:135], v[16:19], v[52:55]\n" \
  "v_mfma_f32_16x16x32_bf16 v[56:59], v[196:199], v[16:19], v[56:59]\n" \
  "v_mfma_f32_16x16x32_bf16 v[60:63], v[32:35], v[16:19], v[60:63]\n" \
  "ds_read_b128 v[16:19], v0 offset:10240\n" \
  "ds_read_b128 v[32:35], v1 offset:4096\n" \
  "s_waitcnt lgkmcnt(6)\n" \
  "v_mfma_f32_16x16x32_bf16 v[48:51], v[72:75], v[20:23], v[48:51]\n" \
  "v_mfma_f32_16x16x32_bf16 v[52:55], v[136:139], v[20:23], v[52:55]\n" \
  "v_mfma_f32_16x16x32_bf16 v[56:59], v[200:203], v[20:23], v[56:59]\n" \
  "s_waitcnt lgkmcnt(4)\n" \
  "v_mfma_f32_16x16x32_bf16 v[60:63], v[36:39], v[20:23], v[60:63]\n" \
  "ds_read_b128 v[20:23], v0 offset:12288\n" \
  "ds_read_b128 v[36:39], v1 offset:5120\n" \
  "v_mfma_f32_16x16x32_bf16 v[48:51], v[76:79], v[24:27], v[48:51]\n" \
  "v_mfma_f32_16x16x32_bf16 v[52:55], v[140:143], v[24:27], v[52:55]\n" \
  "v_mfma_f32_16x16x32_bf16 v[56:59], v[204:207], v[24:27], v[56:59]\n" \
  "s_waitcnt lgkmcnt(4)\n" \
  "v_mfma_f32_16x16x32_bf16 v[60:63], v[28:31], v[24:27], v[60:63]\n" \
  "ds_read_b128 v[24:27], v0 offset:14336\n" \
  "ds_read_b128 v[28:31], v1 offset:6144\n" \
  "v_mfma_f32_16x16x32_bf16 v[48:51], v[80:83], v[12:15], v[48:51]\n" \
  "v_mfma_f32_16x16x32_bf16 v[52:55], v[144:147], v[12:15], v[52:55]\n" \
  "v_mfma_f32_16x16x32_bf16 v[56:59], v[208:211], v[12:15], v[56:59]\n" \
  "s_waitcnt lgkmcnt(4)\n" \
  "v_mfma_f32_16x16x32_bf16 v[60:63], v[32:35], v[12:15], v[60:63]\n" \
  "ds_read_b128 v[12:15], v0 offset:16384\n" \
  "ds_read_b128 v[32:35], v1 offset:7168\n" \
  "v_mfma_f32_16x16x32_bf16 v[48:51], v[84:87], v[16:19], v[48:51]\n" \
  "v_mfma_f32_16x16x32_bf16 v[52:55], v[148:151], v[16:19], v[52:55]\n" \
  "v_mfma_f32_16x16x32_bf16 v[56:59], v[212:215], v[16:19], v[56:59]\n" \
  "s_waitcnt lgkmcnt(4)\n" \
  "v_mfma_f32_16x16x32_bf16 v[60:63], v[36:39], v[16:19], v[60:63]\n" \
  "ds_read_b128 v[16:19], v0 offset:18432\n" \
  "ds_read_b128 v[36:39], v1 offset:8192\n" \
  "v_mfma_f32_16x16x32_bf16 v[48:51], v[88:91], v[20:23], v[48:51]\n" \
  "v_mfma_f32_16x16x32_bf16 v[52:55], v[152:155], v[20:23], v[52:55]\n" \
  "v_mfma_f32_16x16x32_bf16 v[56:59], v[216:219], v[20:23], v[56:59]\n" \
  "s_waitcnt lgkmcnt(4)\n" \
  "v_mfma_f32_16x16x32_bf16 v[60:63], v[28:31], v[20:23], v[60:63]\n" \
  "ds_read_b128 v[20:23], v0 offset:20480\n" \
  "ds_read_b128 v[28:31], v1 offset:9216\n" \
  "v_mfma_f32_16x16x32_bf16 v[48:51], v[92:95], v[24:27], v[48:51]\n" \
  "v_mfma_f32_16x16x32_bf16 v[52:55], v[156:159], v[24:27], v[52:55]\n" \
  "v_mfma_f32_16x16x32_bf16 v[56:59], v[220:223], v[24:27], v[56:59]\n" \
  "s_waitcnt lgkmcnt(4)\n" \
  "v_mfma_f32_16x16x32_bf16 v[60:63], v[32:35], v[24:27], v[60:63]\n" \
  "ds_read_b128 v[24:27], v0 offset:22528\n" \
  "ds_read_b128 v[32:35], v1 offset:10240\n" \
  "v_mfma_f32_16x16x32_bf16 v[48:51], v[96:99], v[12:15], v[48:51]\n" \
  "v_mfma_f32_16x16x32_bf16 v[52:55], v[160:163], v[12:15], v[52:55]\n" \
  "v_mfma_f32_16x16x32_bf16 v[56:59], v[224:227], v[12:15], v[56:59]\n" \
  "s_waitcnt lgkmcnt(4)\n" \
  "v_mfma_f32_16x16x32_bf16 v[60:63], v[36:39], v[12:15], v[60:63]\n" \
  "ds_read_b128 v[12:15], v0 offset:24576\n" \
  "ds_read_b128 v[36:39], v1 offset:11264\n" \
  "v_mfma_f32_16x16x32_bf16 v[48:51], v[100:103], v[16:19], v[48:51]\n" \
  "v_mfma_f32_16x16x32_bf16 v[52:55], v[164:167], v[16:19], v[52:55]\n" \
  "v_mfma_f32_16x16x32_bf16 v[56:59], v[228:231], v[16:19], v[56:59]\n" \
  "s_waitcnt lgkmcnt(4)\n" \
  "v_mfma_f32_16x16x32_bf16 v[60:63], v[28:31], v[16:19], v[60:63]\n" \
  "ds_read_b128 v[16:19], v0 offset:26624\n" \
  "ds_read_b128 v[28:31], v1 offset:12288\n" \
  "v_mfma_f32_16x16x32_bf16 v[48:51], v[104:107], v[20:23], v[48:51]\n" \
  "v_mfma_f32_16x16x32_bf16 v[52:55], v[168:171], v[20:23], v[52:55]\n" \
  "v_mfma_f32_16x16x32_bf16 v[56:59], v[232:235], v[20:23], v[56:59]\n" \
  "s_waitcnt lgkmcnt(4)\n" \
  "v_mfma_f32_16x16x32_bf16 v[60:63], v[32:35], v[20:23], v[60:63]\n" \
  "ds_read_b128 v[20:23], v0 offset:28672\n" \
  "ds_read_b128 v[32:35], v1 offset:13312\n" \
  "v_mfma_f32_16x16x32_bf16 v[48:51], v[108:111], v[24:27], v[48:51]\n" \
  "v_mfma_f32_16x16x32_bf16 v[52:55], v[172:175], v[24:27], v[52:55]\n" \
  "v_mfma_f32_16x16x32_bf16 v[56:59], v[236:239], v[24:27], v[56:59]\n" \
  "s_waitcnt lgkmcnt(4)\n" \
  "v_mfma_f32_16x16x32_bf16 v[60:63], v[36:39], v[24:27], v[60:63]\n" \
  "s_cmp_eq_u32 s68, 1\n" \
  "s_cbranch_scc0 .LCP_X3_%=\n" \
  "s_waitcnt vmcnt(0)\n" \
  "s_mov_b64 exec, 1\n" \
  "buffer_store_dword v7, off, s[56:59], s72 sc1\n" \
  "s_mov_b64 exec, -1\n" \
  "s_add_u32 s72, s72, 0x1000\n" \
  "s_mov_b32 s68, 0\n" \
  ".LCP_X3_%=:\n" \
  "ds_read_b32 v5, v6 offset:16\n" \
  "ds_read_b128 v[24:27], v0 offset:30720\n" \
  "ds_read_b128 v[36:39], v1 offset:14336\n" \
  "v_mfma_f32_16x16x32_bf16 v[48:51], v[112:115], v[12:15], v[48:51]\n" \
  "v_mfma_f32_16x16x32_bf16 v[52:55], v[176:179], v[12:15], v[52:55]\n" \
  "v_mfma_f32_16x16x32_bf16 v[56:59], v[240:243], v[12:15], v[56:59]\n" \
  "s_waitcnt lgkmcnt(5)\n" \
  "v_mfma_f32_16x16x32_bf16 v[60:63], v[28:31], v[12:15], v[60:63]\n" \
  "ds_read_b128 v[12:15], v0 offset:256\n" \
  "ds_read_b128 v[28:31], v1 offset:15360\n" \
  "ds_read_b128 v[40:43], v4 offset:0\n" \
  "ds_read_b128 v[44:47], v4 offset:64\n" \
  "v_mfma_f32_16x16x32_bf16 v[48:51], v[116:119], v[16:19], v[48:51]\n" \
  "v_mfma_f32_16x16x32_bf16 v[52:55], v[180:183], v[16:19], v[52:55]\n" \
  "v_mfma_f32_16x16x32_bf16 v[56:59], v[244:247], v[16:19], v[56:59]\n" \
  "s_waitcnt lgkmcnt(7)\n" \
  "v_mfma_f32_16x16x32_bf16 v[60:63], v[32:35], v[16:19], v[60:63]\n" \
  "s_waitcnt lgkmcnt(6)\n" \
  "v_readfirstlane_b32 s71, v5\n" \
  "s_cmp_ge_u32 s71, s67\n" \
  "s_cbranch_scc1 .LCP_ST_%=\n" \
  ".LCP_S4_%=:\n" \
  "s_sleep 1\n" \
  "ds_read_b32 v5, v6 offset:16\n" \
  "s_sub_u32 s69, s69, 1\n" \
  "s_cbranch_scc1 .LCP_END_%=\n" \
  "s_waitcnt lgkmcnt(0)\n" \
  "v_readfirstlane_b32 s71, v5\n" \
  "s_cmp_ge_u32 s71, s67\n" \
  "s_cbranch_scc1 .LCP_ST_%=\n" \
  "s_branch .LCP_S4_%=\n" \
  ".LCP_ST_%=:\n" \
  "ds_read_b128 v[16:19], v0 offset:2304\n" \
  "ds_read_b128 v[32:35], v4 offset:128\n" \
  "ds_read2_b32 v[8:9], v3 offset0:0 offset1:32\n" \
  "v_mfma_f32_16x16x32_bf16 v[48:51], v[120:123], v[20:23], v[48:51]\n" \
  "v_mfma_f32_16x16x32_bf16 v[52:55], v[184:187], v[20:23], v[52:55]\n" \
  "v_mfma_f32_16x16x32_bf16 v[56:59], v[248:251], v[20:23], v[56:59]\n" \
  "s_waitcnt lgkmcnt(7)\n" \
  "v_mfma_f32_16x16x32_bf16 v[60:63], v[36:39], v[20:23], v[60:63]\n" \
  "ds_read_b128 v[20:23], v0 offset:4352\n" \
  "ds_read_b128 v[36:39], v4 offset:192\n" \
  "v_mfma_f32_16x16x32_bf16 v[48:51], v[124:127], v[24:27], v[48:51]\n" \
  "v_mfma_f32_16x16x32_bf16 v[52:55], v[188:191], v[24:27], v[52:55]\n" \
  "v_mfma_f32_16x16x32_bf16 v[56:59], v[252:255], v[24:27], v[56:59]\n" \
  "s_waitcnt lgkmcnt(7)\n" \
  "v_mfma_f32_16x16x32_bf16 v[60:63], v[28:31], v[24:27], v[60:63]\n" \
  "s_waitcnt lgkmcnt(0)\n" \
  "s_nop 4\n" \
  "v_fma_f32 v48, -v8, v40, v48\n" \
  "v_fma_f32 v49, -v8, v41, v49\n" \
  "v_fma_f32 v50, -v8, v42, v50\n" \
  "v_fma_f32 v51, -v8, v43, v51\n" \
  "v_fma_f32 v52, -v8, v44, v52\n" \
  "v_fma_f32 v53, -v8, v45, v53\n" \
  "v_fma_f32 v54, -v8, v46, v54\n" \
  "v_fma_f32 v55, -v8, v47, v55\n" \
  "v_fma_f32 v56, -v8, v32, v56\n" \
  "v_fma_f32 v57, -v8, v33, v57\n" \
  "v_fma_f32 v58, -v8, v34, v58\n" \
  "v_fma_f32 v59, -v8, v35, v59\n" \
  "v_fma_f32 v60, -v8, v36, v60\n" \
  "v_fma_f32 v61, -v8, v37, v61\n" \
  "v_fma_f32 v62, -v8, v38, v62\n" \
  "v_fma_f32 v63, -v8, v39, v63\n" \
  "ds_read_b128 v[32:35], v1 offset:0\n" \
  "ds_read_b128 v[36:39], v1 offset:1024\n" \
  "v_mul_f32 v48, v48, v9\n" \
  "v_mul_f32 v49, v49, v9\n" \
  "v_mul_f32 v50, v50, v9\n" \
  "v_mul_f32 v51, v51, v9\n" \
  "v_mul_f32 v52, v52, v9\n" \
  "v_mul_f32 v53, v53, v9\n" \
  "v_mul_f32 v54, v54, v9\n" \
  "v_mul_f32 v55, v55, v9\n" \
  "v_cvt_pk_bf16_f32 v40, v48, v49\n" \
  "v_cvt_pk_bf16_f32 v41, v50, v51\n" \
  "v_cvt_pk_bf16_f32 v42, v52, v53\n" \
  "v_cvt_pk_bf16_f32 v43, v54, v55\n" \
  "buffer_store_dwordx4 v[40:43], v2, s[40:43], s65 offen offset:0 sc1\n" \
  "v_mul_f32 v56, v56, v9\n" \
  "v_mul_f32 v57, v57, v9\n" \
  "v_mul_f32 v58, v58, v9\n" \
  "v_mul_f32 v59, v59, v9\n" \
  "v_mul_f32 v60, v60, v9\n" \
  "v_mul_f32 v61, v61, v9\n" \
  "v_mul_f32 v62, v62, v9\n" \
  "v_mul_f32 v63, v63, v9\n" \
  "v_cvt_pk_bf16_f32 v44, v56, v57\n" \
  "v_cvt_pk_bf16_f32 v45, v58, v59\n" \
  "v_cvt_pk_bf16_f32 v46, v60, v61\n" \
  "v_cvt_pk_bf16_f32 v47, v62, v63\n" \
  "buffer_store_dwordx4 v[44:47], v2, s[40:43], s65 offen offset:1024 sc1\n" \
  "ds_read_b128 v[24:27], v0 offset:6400\n" \
  "ds_read_b128 v[28:31], v1 offset:2048\n" \
  "v_mfma_f32_16x16x32_bf16 v[48:51], v[64:67], v[12:15], 0\n" \
  "v_mfma_f32_16x16x32_bf16 v[52:55], v[128:131], v[12:15], 0\n" \
  "v_mfma_f32_16x16x32_bf16 v[56:59], v[192:195], v[12:15], 0\n" \
  "s_waitcnt lgkmcnt(3)\n" \
  "v_mfma_f32_16x16x32_bf16 v[60:63], v[32:35], v[12:15], 0\n" \
  "ds_read_b128 v[12:15], v0 offset:8448\n" \
  "ds_read_b128 v[32:35], v1 offset:3072\n" \
  "v_mfma_f32_16x16x32_bf16 v[48:51], v[68:71], v[16:19], v[48:51]\n" \
  "v_mfma_f32_16x16x32_bf16 v[52:55], v[132:135], v[16:19], v[52:55]\n" \
  "v_mfma_f32_16x16x32_bf16 v[56:59], v[196:199], v[16:19], v[56:59]\n" \
  "s_waitcnt lgkmcnt(4)\n" \
  "v_mfma_f32_16x16x32_bf16 v[60:63], v[36:39], v[16:19], v[60:63]\n" \
  "ds_read_b128 v[16:19], v0 offset:10496\n" \
  "ds_read_b128 v[36:39], v1 offset:4096\n" \
  "v_mfma_f32_16x16x32_bf16 v[48:51], v[72:75], v[20:23], v[48:51]\n" \
  "v_mfma_f32_16x16x32_bf16 v[52:55], v[136:139], v[20:23], v[52:55]\n" \
  "v_mfma_f32_16x16x32_bf16 v[56:59], v[200:203], v[20:23], v[56:59]\n" \
  "s_waitcnt lgkmcnt(4)\n" \
  "v_mfma_f32_16x16x32_bf16 v[60:63], v[28:31], v[20:23], v[60:63]\n" \
  "ds_read_b128 v[20:23], v0 offset:12544\n" \
  "ds_read_b128 v[28:31], v1 offset:5120\n" \
  "v_mfma_f32_16x16x32_bf16 v[48:51], v[76:79], v[24:27], v[48:51]\n" \
  "v_mfma_f32_16x16x32_bf16 v[52:55], v[140:143], v[24:27], v[52:55]\n" \
  "v_mfma_f32_16x16x32_bf16 v[56:59], v[204:207], v[24:27], v[56:59]\n" \
  "s_waitcnt lgkmcnt(4)\n" \
  "v_mfma_f32_16x16x32_bf16 v[60:63], v[32:35], v[24:27], v[60:63]\n" \
  "ds_read_b128 v[24:27], v0 offset:14592\n" \
  "ds_read_b128 v[32:35], v1 offset:6144\n" \
  "v_mfma_f32_16x16x32_bf16 v[48:51], v[80:83], v[12:15], v[48:51]\n" \
  "v_mfma_f32_16x16x32_bf16 v[52:55], v[144:147], v[12:15], v[52:55]\n" \
  "v_mfma_f32_16x16x32_bf16 v[56:59], v[208:211], v[12:15], v[56:59]\n" \
  "s_waitcnt lgkmcnt(4)\n" \
  "v_mfma_f32_16x16x32_bf16 v[60:63], v[36:39], v[12:15], v[60:63]\n" \
  "ds_read_b128 v[12:15], v0 offset:16640\n" \
  "ds_read_b128 v[36:39], v1 offset:7168\n" \
  "v_mfma_f32_16x16x32_bf16 v[48:51], v[84:87], v[16:19], v[48:51]\n" \
  "v_mfma_f32_16x16x32_bf16 v[52:55], v[148:151], v[16:19], v[52:55]\n" \
  "v_mfma_f32_16x16x32_bf16 v[56:59], v[212:215], v[16:19], v[56:59]\n" \
  "s_waitcnt lgkmcnt(4)\n" \
  "v_mfma_f32_16x16x32_bf16 v[60:63], v[28:31], v[16:19], v[60:63]\n" \
  "ds_read_b128 v[16:19], v0 offset:18688\n" \
  "ds_read_b128 v[28:31], v1 offset:8192\n" \
  "v_mfma_f32_16x16x32_bf16 v[48:51], v[88:91], v[20:23], v[48:51]\n" \
  "v_mfma_f32_16x16x32_bf16 v[52:55], v[152:155], v[20:23], v[52:55]\n" \
  "v_mfma_f32_16x16x32_bf16 v[56:59], v[216:219], v[20:23], v[56:59]\n" \
  "s_waitcnt lgkmcnt(4)\n" \
  "v_mfma_f32_16x16x32_bf16 v[60:63], v[32:35], v[20:23], v[60:63]\n" \
  "ds_read_b128 v[20:23], v0 offset:20736\n" \
  "ds_read_b128 v[32:35], v1 offset:9216\n" \
  "v_mfma_f32_16x16x32_bf16 v[48:51], v[92:95], v[24:27], v[48:51]\n" \
  "v_mfma_f32_16x16x32_bf16 v[52:55], v[156:159], v[24:27], v[52:55]\n" \
  "v_mfma_f32_16x16x32_bf16 v[56:59], v[220:223], v[24:27], v[56:59]\n" \
  "s_waitcnt lgkmcnt(4)\n" \
  "v_mfma_f32_16x16x32_bf16 v[60:63], v[36:39], v[24:27], v[60:63]\n" \
  "ds_read_b128 v[24:27], v0 offset:22784\n" \
  "ds_read_b128 v[36:39], v1 offset:10240\n" \
  "v_mfma_f32_16x16x32_bf16 v[48:51], v[96:99], v[12:15], v[48:51]\n" \
  "v_mfma_f32_16x16x32_bf16 v[52:55], v[160:163], v[12:15], v[52:55]\n" \
  "v_mfma_f32_16x16x32_bf16 v[56:59], v[224:227], v[12:15], v[56:59]\n" \
  "s_waitcnt lgkmcnt(4)\n" \
  "v_mfma_f32_16x16x32_bf16 v[60:63], v[28:31], v[12:15], v[60:63]\n" \
  "ds_read_b128 v[12:15], v0 offset:24832\n" \
  "ds_read_b128 v[28:31], v1 offset:11264\n" \
  "v_mfma_f32_16x16x32_bf16 v[48:51], v[100:103], v[16:19], v[48:51]\n" \
  "v_mfma_f32_16x16x32_bf16 v[52:55], v[164:167], v[16:19], v[52:55]\n" \
  "v_mfma_f32_16x16x32_bf16 v[56:59], v[228:231], v[16:19], v[56:59]\n" \
  "s_waitcnt lgkmcnt(4)\n" \
  "v_mfma_f32_16x16x32_bf16 v[60:63], v[32:35], v[16:19], v[60:63]\n" \
  "ds_read_b128 v[16:19], v0 offset:26880\n" \
  "ds_read_b128 v[32:35], v1 offset:12288\n" \
  "v_mfma_f32_16x16x32_bf16 v[48:51], v[104:107], v[20:23], v[48:51]\n" \
  "v_mfma_f32_16x16x32_bf16 v[52:55], v[168:171], v[20:23], v[52:55]\n" \
  "v_mfma_f32_16x16x32_bf16 v[56:59], v[232:235], v[20:23], v[56:59]\n" \
  "s_waitcnt lgkmcnt(4)\n" \
  "v_mfma_f32_16x16x32_bf16 v[60:63], v[36:39], v[20:23], v[60:63]\n" \
  "ds_read_b128 v[20:23], v0 offset:28928\n" \
  "ds_read_b128 v[36:39], v1 offset:13312\n" \
  "v_mfma_f32_16x16x32_bf16 v[48:51], v[108:111], v[24:27], v[48:51]\n" \
  "v_mfma_f32_16x16x32_bf16 v[52:55], v[172:175], v[24:27], v[52:55]\n" \
  "v_mfma_f32_16x16x32_bf16 v[56:59], v[236:239], v[24:27], v[56:59]\n" \
  "s_waitcnt lgkmcnt(4)\n" \
  "v_mfma_f32_16x16x32_bf16 v[60:63], v[28:31], v[24:27], v[60:63]\n" \
  "ds_read_b128 v[24:27], v0 offset:30976\n" \
  "ds_read_b128 v[28:31], v1 offset:14336\n" \
  "v_mfma_f32_16x16x32_bf16 v[48:51], v[112:115], v[12:15], v[48:51]\n" \
  "v_mfma_f32_16x16x32_bf16 v[52:55], v[176:179], v[12:15], v[52:55]\n" \
  "v_mfma_f32_16x16x32_bf16 v[56:59], v[240:243], v[12:15], v[56:59]\n" \
  "s_waitcnt lgkmcnt(4)\n" \
  "v_mfma_f32_16x16x32_bf16 v[60:63], v[32:35], v[12:15], v[60:63]\n" \
  "ds_read_b128 v[32:35], v1 offset:15360\n" \
  "ds_read_b128 v[40:43], v4 offset:0\n" \
  "ds_read_b128 v[44:47], v4 offset:64\n" \
  "v_mfma_f32_16x16x32_bf16 v[48:51], v[116:119], v[16:19], v[48:51]\n" \
  "v_mfma_f32_16x16x32_bf16 v[52:55], v[180:183], v[16:19], v[52:55]\n" \
  "v_mfma_f32_16x16x32_bf16 v[56:59], v[244:247], v[16:19], v[56:59]\n" \
  "s_waitcnt lgkmcnt(5)\n" \
  "v_mfma_f32_16x16x32_bf16 v[60:63], v[36:39], v[16:19], v[60:63]\n" \
  "ds_read_b128 v[36:39], v4 offset:128\n" \
  "ds_read2_b32 v[8:9], v3 offset0:16 offset1:48\n" \
  "v_mfma_f32_16x16x32_bf16 v[48:51], v[120:123], v[20:23], v[48:51]\n" \
  "v_mfma_f32_16x16x32_bf16 v[52:55], v[184:187], v[20:23], v[52:55]\n" \
  "v_mfma_f32_16x16x32_bf16 v[56:59], v[248:251], v[20:23], v[56:59]\n" \
  "s_waitcnt lgkmcnt(5)\n" \
  "v_mfma_f32_16x16x32_bf16 v[60:63], v[28:31], v[20:23], v[60:63]\n" \
  "ds_read_b128 v[28:31], v4 offset:192\n" \
  "s_mov_b64 exec, 1\n" \
  "ds_add_u32 v6, v7 offset:8\n" \
  "s_mov_b64 exec, -1\n" \
  "v_xor_b32 v6, 4, v6\n" \
  "v_mfma_f32_16x16x32_bf16 v[48:51], v[124:127], v[24:27], v[48:51]\n" \
  "v_mfma_f32_16x16x32_bf16 v[52:55], v[188:191], v[24:27], v[52:55]\n" \
  "v_mfma_f32_16x16x32_bf16 v[56:59], v[252:255], v[24:27], v[56:59]\n" \
  "s_waitcnt lgkmcnt(6)\n" \
  "v_mfma_f32_16x16x32_bf16 v[60:63], v[32:35], v[24:27], v[60:63]\n" \
  "s_waitcnt lgkmcnt(1)\n" \
  "s_nop 4\n" \
  "ds_read_b32 v5, v6 offset:0\n" \
  "v_fma_f32 v48, -v8, v40, v48\n" \
  "v_fma_f32 v49, -v8, v41, v49\n" \
  "v_fma_f32 v50, -v8, v42, v50\n" \
  "v_fma_f32 v51, -v8, v43, v51\n" \
  "v_fma_f32 v52, -v8, v44, v52\n" \
  "v_fma_f32 v53, -v8, v45, v53\n" \
  "v_fma_f32 v54, -v8, v46, v54\n" \
  "v_fma_f32 v55, -v8, v47, v55\n" \
  "v_fma_f32 v56, -v8, v36, v56\n" \
  "v_fma_f32 v57, -v8, v37, v57\n" \
  "v_fma_f32 v58, -v8, v38, v58\n" \
  "v_fma_f32 v59, -v8, v39, v59\n" \
  "v_fma_f32 v60, -v8, v28, v60\n" \
  "v_fma_f32 v61, -v8, v29, v61\n" \
  "v_fma_f32 v62, -v8, v30, v62\n" \
  "v_fma_f32 v63, -v8, v31, v63\n" \
  "v_mul_f32 v48, v48, v9\n" \
  "v_mul_f32 v49, v49, v9\n" \
  "v_mul_f32 v50, v50, v9\n" \
  "v_mul_f32 v51, v51, v9\n" \
  "v_mul_f32 v52, v52, v9\n" \
  "v_mul_f32 v53, v53, v9\n" \
  "v_mul_f32 v54, v54, v9\n" \
  "v_mul_f32 v55, v55, v9\n" \
  "v_cvt_pk_bf16_f32 v40, v48, v49\n" \
  "v_cvt_pk_bf16_f32 v41, v50, v51\n" \
  "v_cvt_pk_bf16_f32 v42, v52, v53\n" \
  "v_cvt_pk_bf16_f32 v43, v54, v55\n" \
  "buffer_store_dwordx4 v[40:43], v10, s[40:43], s65 offen offset:0 sc1\n" \
  "v_mul_f32 v56, v56, v9\n" \
  "v_mul_f32 v57, v57, v9\n" \
  "v_mul_f32 v58, v58, v9\n" \
  "v_mul_f32 v59, v59, v9\n" \
  "v_mul_f32 v60, v60, v9\n" \
  "v_mul_f32 v61, v61, v9\n" \
  "v_mul_f32 v62, v62, v9\n" \
  "v_mul_f32 v63, v63, v9\n" \
  "v_cvt_pk_bf16_f32 v44, v56, v57\n" \
  "v_cvt_pk_bf16_f32 v45, v58, v59\n" \
  "v_cvt_pk_bf16_f32 v46, v60, v61\n" \
  "v_cvt_pk_bf16_f32 v47, v62, v63\n" \
  "buffer_store_dwordx4 v[44:47], v10, s[40:43], s65 offen offset:1024 sc1\n" \
  "s_waitcnt lgkmcnt(0)\n" \
  "s_add_u32 s65, s65, 0x80000\n" \
  "s_sub_u32 s64, s64, 1\n" \
  "s_mov_b32 s68, 1\n" \
  "s_cmp_lg_u32 s64, 0\n" \
  "s_cbranch_scc1 .LCP_TOP_%=\n" \
  "s_waitcnt vmcnt(0)\n" \
  "s_mov_b64 exec, 1\n" \
  "buffer_store_dword v7, off, s[56:59], s72 sc1\n" \
  "s_mov_b64 exec, -1\n" \
  "s_add_u32 s72, s72, 0x1000\n" \
  "s_mov_b32 s68, 0\n" \
  ".LCP_END_%=:\n" \
  "s_endpgm\n"
#define V_ASM \
  "s_mov_b32 s68, 0\n" \
  "s_mov_b32 s66, 3\n" \
  "s_mov_b32 s67, 2\n" \
  "s_mov_b32 s72, 0\n" \
  "v_mov_b32 v7, 1\n" \
  "v_mov_b32 v6, s70\n" \
  "v_mov_b32 v5, 0\n" \
  "ds_read_b32 v8, v4 offset:0\n" \
  "ds_read_b32 v9, v4 offset:64\n" \
  "ds_read_b32 v10, v4 offset:128\n" \
  "ds_read_b32 v4, v4 offset:192\n" \
  "s_waitcnt lgkmcnt(0)\n" \
  ".p2align 6\n" \
  ".LCW_TOP_%=:\n" \
  "ds_read_b128 v[28:31], v1 offset:0\n" \
  "ds_read_b128 v[32:35], v1 offset:1024\n" \
  "s_add_u32 s66, s66, 3\n" \
  "s_add_u32 s67, s67, 2\n" \
  "s_mov_b32 s69, 0x400000\n" \
  "v_xor_b32 v0, 0x8000, v0\n" \
  "v_xor_b32 v3, 0x100, v3\n" \
  "v_readfirstlane_b32 s71, v5\n" \
  "s_cmp_ge_u32 s71, s66\n" \
  "s_cbranch_scc1 .LCW_GO_%=\n" \
  "s_cmp_eq_u32 s68, 1\n" \
  "s_cbranch_scc0 .LCW_X1_%=\n" \
  "s_waitcnt vmcnt(0)\n" \
  "s_mov_b64 exec, 1\n" \
  "buffer_store_dword v7, off, s[56:59], s72 sc1\n" \
  "s_mov_b64 exec, -1\n" \
  "s_add_u32 s72, s72, 0x1000\n" \
  "s_mov_b32 s68, 0\n" \
  ".LCW_X1_%=:\n" \
  ".LCW_S2_%=:\n" \
  "s_sleep 1\n" \
  "ds_read_b32 v5, v6 offset:0\n" \
  "s_sub_u32 s69, s69, 1\n" \
  "s_cbranch_scc1 .LCW_END_%=\n" \
  "s_waitcnt lgkmcnt(0)\n" \
  "v_readfirstlane_b32 s71, v5\n" \
  "s_cmp_ge_u32 s71, s66\n" \
  "s_cbranch_scc1 .LCW_GO_%=\n" \
  "s_branch .LCW_S2_%=\n" \
  ".LCW_GO_%=:\n" \
  "ds_read_b128 v[12:15], v0 offset:0\n" \
  "ds_read_b128 v[16:19], v0 offset:2048\n" \
  "ds_read_b128 v[20:23], v0 offset:4096\n" \
  "ds_read_b128 v[24:27], v0 offset:6144\n" \
  "ds_read_b128 v[36:39], v1 offset:2048\n" \
  "s_waitcnt lgkmcnt(4)\n" \
  "v_mfma_f32_16x16x32_bf16 v[42:45], v[12:15], v[64:67], 0\n" \
  "v_mfma_f32_16x16x32_bf16 v[48:51], v[12:15], v[128:131], 0\n" \
  "v_mfma_f32_16x16x32_bf16 v[54:57], v[12:15], v[192:195], 0\n" \
  "v_mfma_f32_16x16x32_bf16 v[60:63], v[12:15], v[28:31], 0\n" \
  "ds_read_b128 v[12:15], v0 offset:8192\n" \
  "ds_read_b128 v[28:31], v1 offset:3072\n" \
  "s_waitcnt lgkmcnt(5)\n" \
  "v_mfma_f32_16x16x32_bf16 v[42:45], v[16:19], v[68:71], v[42:45]\n" \
  "v_mfma_f32_16x16x32_bf16 v[48:51], v[16:19], v[132:135], v[48:51]\n" \
  "v_mfma_f32_16x16x32_bf16 v[54:57], v[16:19], v[196:199], v[54:57]\n" \
  "v_mfma_f32_16x16x32_bf16 v[60:63], v[16:19], v[32:35], v[60:63]\n" \
  "ds_read_b128 v[16:19], v0 offset:10240\n" \
  "ds_read_b128 v[32:35], v1 offset:4096\n" \
  "s_waitcnt lgkmcnt(6)\n" \
  "v_mfma_f32_16x16x32_bf16 v[42:45], v[20:23], v[72:75], v[42:45]\n" \
  "v_mfma_f32_16x16x32_bf16 v[48:51], v[20:23], v[136:139], v[48:51]\n" \
  "v_mfma_f32_16x16x32_bf16 v[54:57], v[20:23], v[200:203], v[54:57]\n" \
  "s_waitcnt lgkmcnt(4)\n" \
  "v_mfma_f32_16x16x32_bf16 v[60:63], v[20:23], v[36:39], v[60:63]\n" \
  "ds_read_b128 v[20:23], v0 offset:12288\n" \
  "ds_read_b128 v[36:39], v1 offset:5120\n" \
  "v_mfma_f32_16x16x32_bf16 v[42:45], v[24:27], v[76:79], v[42:45]\n" \
  "v_mfma_f32_16x16x32_bf16 v[48:51], v[24:27], v[140:143], v[48:51]\n" \
  "v_mfma_f32_16x16x32_bf16 v[54:57], v[24:27], v[204:207], v[54:57]\n" \
  "s_waitcnt lgkmcnt(4)\n" \
  "v_mfma_f32_16x16x32_bf16 v[60:63], v[24:27], v[28:31], v[60:63]\n" \
  "ds_read_b128 v[24:27], v0 offset:14336\n" \
  "ds_read_b128 v[28:31], v1 offset:6144\n" \
  "v_mfma_f32_16x16x32_bf16 v[42:45], v[12:15], v[80:83], v[42:45]\n" \
  "v_mfma_f32_16x16x32_bf16 v[48:51], v[12:15], v[144:147], v[48:51]\n" \
  "v_mfma_f32_16x16x32_bf16 v[54:57], v[12:15], v[208:211], v[54:57]\n" \
  "s_waitcnt lgkmcnt(4)\n" \
  "v_mfma_f32_16x16x32_bf16 v[60:63], v[12:15], v[32:35], v[60:63]\n" \
  "ds_read_b128 v[12:15], v0 offset:16384\n" \
  "ds_read_b128 v[32:35], v1 offset:7168\n" \
  "v_mfma_f32_16x16x32_bf16 v[42:45], v[16:19], v[84:87], v[42:45]\n" \
  "v_mfma_f32_16x16x32_bf16 v[48:51], v[16:19], v[148:151], v[48:51]\n" \
  "v_mfma_f32_16x16x32_bf16 v[54:57], v[16:19], v[212:215], v[54:57]\n" \
  "s_waitcnt lgkmcnt(4)\n" \
  "v_mfma_f32_16x16x32_bf16 v[60:63], v[16:19], v[36:39], v[60:63]\n" \
  "ds_read_b128 v[16:19], v0 offset:18432\n" \
  "ds_read_b128 v[36:39], v1 offset:8192\n" \
  "v_mfma_f32_16x16x32_bf16 v[42:45], v[20:23], v[88:91], v[42:45]\n" \
  "v_mfma_f32_16x16x32_bf16 v[48:51], v[20:23], v[152:155], v[48:51]\n" \
  "v_mfma_f32_16x16x32_bf16 v[54:57], v[20:23], v[216:219], v[54:57]\n" \
  "s_waitcnt lgkmcnt(4)\n" \
  "v_mfma_f32_16x16x32_bf16 v[60:63], v[20:23], v[28:31], v[60:63]\n" \
  "ds_read_b128 v[20:23], v0 offset:20480\n" \
  "ds_read_b128 v[28:31], v1 offset:9216\n" \
  "v_mfma_f32_16x16x32_bf16 v[42:45], v[24:27], v[92:95], v[42:45]\n" \
  "v_mfma_f32_16x16x32_bf16 v[48:51], v[24:27], v[156:159], v[48:51]\n" \
  "v_mfma_f32_16x16x32_bf16 v[54:57], v[24:27], v[220:223], v[54:57]\n" \
  "s_waitcnt lgkmcnt(4)\n" \
  "v_mfma_f32_16x16x32_bf16 v[60:63], v[24:27], v[32:35], v[60:63]\n" \
  "ds_read_b128 v[24:27], v0 offset:22528\n" \
  "ds_read_b128 v[32:35], v1 offset:10240\n" \
  "v_mfma_f32_16x16x32_bf16 v[42:45], v[12:15], v[96:99], v[42:45]\n" \
  "v_mfma_f32_16x16x32_bf16 v[48:51], v[12:15], v[160:163], v[48:51]\n" \
  "v_mfma_f32_16x16x32_bf16 v[54:57], v[12:15], v[224:227], v[54:57]\n" \
  "s_waitcnt lgkmcnt(4)\n" \
  "v_mfma_f32_16x16x32_bf16 v[60:63], v[12:15], v[36:39], v[60:63]\n" \
  "ds_read_b128 v[12:15], v0 offset:24576\n" \
  "ds_read_b128 v[36:39], v1 offset:11264\n" \
  "v_mfma_f32_16x16x32_bf16 v[42:45], v[16:19], v[100:103], v[42:45]\n" \
  "v_mfma_f32_16x16x32_bf16 v[48:51], v[16:19], v[164:167], v[48:51]\n" \
  "v_mfma_f32_16x16x32_bf16 v[54:57], v[16:19], v[228:231], v[54:57]\n" \
  "s_waitcnt lgkmcnt(4)\n" \
  "v_mfma_f32_16x16x32_bf16 v[60:63], v[16:19], v[28:31], v[60:63]\n" \
  "ds_read_b128 v[16:19], v0 offset:26624\n" \
  "ds_read_b128 v[28:31], v1 offset:12288\n" \
  "v_mfma_f32_16x16x32_bf16 v[42:45], v[20:23], v[104:107], v[42:45]\n" \
  "v_mfma_f32_16x16x32_bf16 v[48:51], v[20:23], v[168:171], v[48:51]\n" \
  "v_mfma_f32_16x16x32_bf16 v[54:57], v[20:23], v[232:235], v[54:57]\n" \
  "s_waitcnt lgkmcnt(4)\n" \
  "v_mfma_f32_16x16x32_bf16 v[60:63], v[20:23], v[32:35], v[60:63]\n" \
  "ds_read_b128 v[20:23], v0 offset:28672\n" \
  "ds_read_b128 v[32:35], v1 offset:13312\n" \
  "v_mfma_f32_16x16x32_bf16 v[42:45], v[24:27], v[108:111], v[42:45]\n" \
  "v_mfma_f32_16x16x32_bf16 v[48:51], v[24:27], v[172:175], v[48:51]\n" \
  "v_mfma_f32_16x16x32_bf16 v[54:57], v[24:27], v[236:239], v[54:57]\n" \
  "s_waitcnt lgkmcnt(4)\n" \
  "v_mfma_f32_16x16x32_bf16 v[60:63], v[24:27], v[36:39], v[60:63]\n" \
  "s_cmp_eq_u32 s68, 1\n" \
  "s_cbranch_scc0 .LCW_X3_%=\n" \
  "s_waitcnt vmcnt(0)\n" \
  "s_mov_b64 exec, 1\n" \
  "buffer_store_dword v7, off, s[56:59], s72 sc1\n" \
  "s_mov_b64 exec, -1\n" \
  "s_add_u32 s72, s72, 0x1000\n" \
  "s_mov_b32 s68, 0\n" \
  ".LCW_X3_%=:\n" \
  "ds_read_b32 v5, v6 offset:16\n" \
  "ds_read_b128 v[24:27], v0 offset:30720\n" \
  "ds_read_b128 v[36:39], v1 offset:14336\n" \
  "v_mfma_f32_16x16x32_bf16 v[42:45], v[12:15], v[112:115], v[42:45]\n" \
  "v_mfma_f32_16x16x32_bf16 v[48:51], v[12:15], v[176:179], v[48:51]\n" \
  "v_mfma_f32_16x16x32_bf16 v[54:57], v[12:15], v[240:243], v[54:57]\n" \
  "s_waitcnt lgkmcnt(5)\n" \
  "v_mfma_f32_16x16x32_bf16 v[60:63], v[12:15], v[28:31], v[60:63]\n" \
  "ds_read_b128 v[12:15], v0 offset:256\n" \
  "ds_read_b128 v[28:31], v1 offset:15360\n" \
  "v_mfma_f32_16x16x32_bf16 v[42:45], v[16:19], v[116:119], v[42:45]\n" \
  "v_mfma_f32_16x16x32_bf16 v[48:51], v[16:19], v[180:183], v[48:51]\n" \
  "v_mfma_f32_16x16x32_bf16 v[54:57], v[16:19], v[244:247], v[54:57]\n" \
  "s_waitcnt lgkmcnt(5)\n" \
  "v_mfma_f32_16x16x32_bf16 v[60:63], v[16:19], v[32:35], v[60:63]\n" \
  "s_waitcnt lgkmcnt(4)\n" \
  "v_readfirstlane_b32 s71, v5\n" \
  "s_cmp_ge_u32 s71, s67\n" \
  "s_cbranch_scc1 .LCW_ST_%=\n" \
  ".LCW_S4_%=:\n" \
  "s_sleep 1\n" \
  "ds_read_b32 v5, v6 offset:16\n" \
  "s_sub_u32 s69, s69, 1\n" \
  "s_cbranch_scc1 .LCW_END_%=\n" \
  "s_waitcnt lgkmcnt(0)\n" \
  "v_readfirstlane_b32 s71, v5\n" \
  "s_cmp_ge_u32 s71, s67\n" \
  "s_cbranch_scc1 .LCW_ST_%=\n" \
  "s_branch .LCW_S4_%=\n" \
  ".LCW_ST_%=:\n" \
  "ds_read_b128 v[16:19], v0 offset:2304\n" \
  "ds_read_b128 v[32:35], v3 offset:0\n" \
  "v_mfma_f32_16x16x32_bf16 v[42:45], v[20:23], v[120:123], v[42:45]\n" \
  "v_mfma_f32_16x16x32_bf16 v[48:51], v[20:23], v[184:187], v[48:51]\n" \
  "v_mfma_f32_16x16x32_bf16 v[54:57], v[20:23], v[248:251], v[54:57]\n" \
  "s_waitcnt lgkmcnt(4)\n" \
  "v_mfma_f32_16x16x32_bf16 v[60:63], v[20:23], v[36:39], v[60:63]\n" \
  "ds_read_b128 v[20:23], v0 offset:4352\n" \
  "ds_read_b128 v[36:39], v3 offset:128\n" \
  "v_mfma_f32_16x16x32_bf16 v[42:45], v[24:27], v[124:127], v[42:45]\n" \
  "v_mfma_f32_16x16x32_bf16 v[48:51], v[24:27], v[188:191], v[48:51]\n" \
  "v_mfma_f32_16x16x32_bf16 v[54:57], v[24:27], v[252:255], v[54:57]\n" \
  "s_waitcnt lgkmcnt(4)\n" \
  "v_mfma_f32_16x16x32_bf16 v[60:63], v[24:27], v[28:31], v[60:63]\n" \
  "s_waitcnt lgkmcnt(0)\n" \
  "s_nop 4\n" \
  "v_fma_f32 v42, -v32, v8, v42\n" \
  "v_fma_f32 v43, -v33, v8, v43\n" \
  "v_fma_f32 v44, -v34, v8, v44\n" \
  "v_fma_f32 v45, -v35, v8, v45\n" \
  "v_fma_f32 v48, -v32, v9, v48\n" \
  "v_fma_f32 v49, -v33, v9, v49\n" \
  "v_fma_f32 v50, -v34, v9, v50\n" \
  "v_fma_f32 v51, -v35, v9, v51\n" \
  "v_fma_f32 v54, -v32, v10, v54\n" \
  "v_fma_f32 v55, -v33, v10, v55\n" \
  "v_fma_f32 v56, -v34, v10, v56\n" \
  "v_fma_f32 v57, -v35, v10, v57\n" \
  "v_fma_f32 v60, -v32, v4, v60\n" \
  "v_fma_f32 v61, -v33, v4, v61\n" \
  "v_fma_f32 v62, -v34, v4, v62\n" \
  "v_fma_f32 v63, -v35, v4, v63\n" \
  "ds_read_b128 v[32:35], v1 offset:0\n" \
  "v_mul_f32 v42, v42, v36\n" \
  "v_mul_f32 v43, v43, v37\n" \
  "v_mul_f32 v44, v44, v38\n" \
  "v_mul_f32 v45, v45, v39\n" \
  "v_mul_f32 v48, v48, v36\n" \
  "v_mul_f32 v49, v49, v37\n" \
  "v_mul_f32 v50, v50, v38\n" \
  "v_mul_f32 v51, v51, v39\n" \
  "v_mul_f32 v54, v54, v36\n" \
  "v_mul_f32 v55, v55, v37\n" \
  "v_mul_f32 v56, v56, v38\n" \
  "v_mul_f32 v57, v57, v39\n" \
  "v_mul_f32 v60, v60, v36\n" \
  "v_mul_f32 v61, v61, v37\n" \
  "v_mul_f32 v62, v62, v38\n" \
  "v_mul_f32 v63, v63, v39\n" \
  "ds_read_b128 v[36:39], v1 offset:1024\n" \
  "v_cvt_pk_bf16_f32 v40, v42, v43\n" \
  "v_cvt_pk_bf16_f32 v41, v44, v45\n" \
  "v_cvt_pk_bf16_f32 v46, v48, v49\n" \
  "v_cvt_pk_bf16_f32 v47, v50, v51\n" \
  "v_cvt_pk_bf16_f32 v52, v54, v55\n" \
  "v_cvt_pk_bf16_f32 v53, v56, v57\n" \
  "v_cvt_pk_bf16_f32 v58, v60, v61\n" \
  "v_cvt_pk_bf16_f32 v59, v62, v63\n" \
  "ds_read_b128 v[24:27], v0 offset:6400\n" \
  "ds_read_b128 v[28:31], v1 offset:2048\n" \
  "v_mfma_f32_16x16x32_bf16 v[42:45], v[12:15], v[64:67], 0\n" \
  "v_mfma_f32_16x16x32_bf16 v[48:51], v[12:15], v[128:131], 0\n" \
  "v_mfma_f32_16x16x32_bf16 v[54:57], v[12:15], v[192:195], 0\n" \
  "s_waitcnt lgkmcnt(3)\n" \
  "v_mfma_f32_16x16x32_bf16 v[60:63], v[12:15], v[32:35], 0\n" \
  "ds_read_b128 v[12:15], v0 offset:8448\n" \
  "ds_read_b128 v[32:35], v1 offset:3072\n" \
  "v_mfma_f32_16x16x32_bf16 v[42:45], v[16:19], v[68:71], v[42:45]\n" \
  "v_mfma_f32_16x16x32_bf16 v[48:51], v[16:19], v[132:135], v[48:51]\n" \
  "v_mfma_f32_16x16x32_bf16 v[54:57], v[16:19], v[196:199], v[54:57]\n" \
  "s_waitcnt lgkmcnt(4)\n" \
  "v_mfma_f32_16x16x32_bf16 v[60:63], v[16:19], v[36:39], v[60:63]\n" \
  "ds_read_b128 v[16:19], v0 offset:10496\n" \
  "ds_read_b128 v[36:39], v1 offset:4096\n" \
  "v_mfma_f32_16x16x32_bf16 v[42:45], v[20:23], v[72:75], v[42:45]\n" \
  "v_mfma_f32_16x16x32_bf16 v[48:51], v[20:23], v[136:139], v[48:51]\n" \
  "v_mfma_f32_16x16x32_bf16 v[54:57], v[20:23], v[200:203], v[54:57]\n" \
  "s_waitcnt lgkmcnt(4)\n" \
  "v_mfma_f32_16x16x32_bf16 v[60:63], v[20:23], v[28:31], v[60:63]\n" \
  "ds_read_b128 v[20:23], v0 offset:12544\n" \
  "ds_read_b128 v[28:31], v1 offset:5120\n" \
  "v_mfma_f32_16x16x32_bf16 v[42:45], v[24:27], v[76:79], v[42:45]\n" \
  "v_mfma_f32_16x16x32_bf16 v[48:51], v[24:27], v[140:143], v[48:51]\n" \
  "v_mfma_f32_16x16x32_bf16 v[54:57], v[24:27], v[204:207], v[54:57]\n" \
  "s_waitcnt lgkmcnt(4)\n" \
  "v_mfma_f32_16x16x32_bf16 v[60:63], v[24:27], v[32:35], v[60:63]\n" \
  "ds_read_b128 v[24:27], v0 offset:14592\n" \
  "ds_read_b128 v[32:35], v1 offset:6144\n" \
  "v_mfma_f32_16x16x32_bf16 v[42:45], v[12:15], v[80:83], v[42:45]\n" \
  "v_mfma_f32_16x16x32_bf16 v[48:51], v[12:15], v[144:147], v[48:51]\n" \
  "v_mfma_f32_16x16x32_bf16 v[54:57], v[12:15], v[208:211], v[54:57]\n" \
  "s_waitcnt lgkmcnt(4)\n" \
  "v_mfma_f32_16x16x32_bf16 v[60:63], v[12:15], v[36:39], v[60:63]\n" \
  "ds_read_b128 v[12:15], v0 offset:16640\n" \
  "ds_read_b128 v[36:39], v1 offset:7168\n" \
  "v_mfma_f32_16x16x32_bf16 v[42:45], v[16:19], v[84:87], v[42:45]\n" \
  "v_mfma_f32_16x16x32_bf16 v[48:51], v[16:19], v[148:151], v[48:51]\n" \
  "v_mfma_f32_16x16x32_bf16 v[54:57], v[16:19], v[212:215], v[54:57]\n" \
  "s_waitcnt lgkmcnt(4)\n" \
  "v_mfma_f32_16x16x32_bf16 v[60:63], v[16:19], v[28:31], v[60:63]\n" \
  "ds_read_b128 v[16:19], v0 offset:18688\n" \
  "ds_read_b128 v[28:31], v1 offset:8192\n" \
  "v_mfma_f32_16x16x32_bf16 v[42:45], v[20:23], v[88:91], v[42:45]\n" \
  "v_mfma_f32_16x16x32_bf16 v[48:51], v[20:23], v[152:155], v[48:51]\n" \
  "v_mfma_f32_16x16x32_bf16 v[54:57], v[20:23], v[216:219], v[54:57]\n" \
  "s_waitcnt lgkmcnt(4)\n" \
  "v_mfma_f32_16x16x32_bf16 v[60:63], v[20:23], v[32:35], v[60:63]\n" \
  "ds_read_b128 v[20:23], v0 offset:20736\n" \
  "ds_read_b128 v[32:35], v1 offset:9216\n" \
  "v_mfma_f32_16x16x32_bf16 v[42:45], v[24:27], v[92:95], v[42:45]\n" \
  "v_mfma_f32_16x16x32_bf16 v[48:51], v[24:27], v[156:159], v[48:51]\n" \
  "v_mfma_f32_16x16x32_bf16 v[54:57], v[24:27], v[220:223], v[54:57]\n" \
  "s_waitcnt lgkmcnt(4)\n" \
  "v_mfma_f32_16x16x32_bf16 v[60:63], v[24:27], v[36:39], v[60:63]\n" \
  "ds_read_b128 v[24:27], v0 offset:22784\n" \
  "ds_read_b128 v[36:39], v1 offset:10240\n" \
  "v_mfma_f32_16x16x32_bf16 v[42:45], v[12:15], v[96:99], v[42:45]\n" \
  "v_mfma_f32_16x16x32_bf16 v[48:51], v[12:15], v[160:163], v[48:51]\n" \
  "v_mfma_f32_16x16x32_bf16 v[54:57], v[12:15], v[224:227], v[54:57]\n" \
  "s_waitcnt lgkmcnt(4)\n" \
  "v_mfma_f32_16x16x32_bf16 v[60:63], v[12:15], v[28:31], v[60:63]\n" \
  "ds_read_b128 v[12:15], v0 offset:24832\n" \
  "ds_read_b128 v[28:31], v1 offset:11264\n" \
  "v_mfma_f32_16x16x32_bf16 v[42:45], v[16:19], v[100:103], v[42:45]\n" \
  "v_mfma_f32_16x16x32_bf16 v[48:51], v[16:19], v[164:167], v[48:51]\n" \
  "v_mfma_f32_16x16x32_bf16 v[54:57], v[16:19], v[228:231], v[54:57]\n" \
  "s_waitcnt lgkmcnt(4)\n" \
  "v_mfma_f32_16x16x32_bf16 v[60:63], v[16:19], v[32:35], v[60:63]\n" \
  "ds_read_b128 v[16:19], v0 offset:26880\n" \
  "ds_read_b128 v[32:35], v1 offset:12288\n" \
  "v_mfma_f32_16x16x32_bf16 v[42:45], v[20:23], v[104:107], v[42:45]\n" \
  "v_mfma_f32_16x16x32_bf16 v[48:51], v[20:23], v[168:171], v[48:51]\n" \
  "v_mfma_f32_16x16x32_bf16 v[54:57], v[20:23], v[232:235], v[54:57]\n" \
  "s_waitcnt lgkmcnt(4)\n" \
  "v_mfma_f32_16x16x32_bf16 v[60:63], v[20:23], v[36:39], v[60:63]\n" \
  "ds_read_b128 v[20:23], v0 offset:28928\n" \
  "ds_read_b128 v[36:39], v1 offset:13312\n" \
  "v_mfma_f32_16x16x32_bf16 v[42:45], v[24:27], v[108:111], v[42:45]\n" \
  "v_mfma_f32_16x16x32_bf16 v[48:51], v[24:27], v[172:175], v[48:51]\n" \
  "v_mfma_f32_16x16x32_bf16 v[54:57], v[24:27], v[236:239], v[54:57]\n" \
  "s_waitcnt lgkmcnt(4)\n" \
  "v_mfma_f32_16x16x32_bf16 v[60:63], v[24:27], v[28:31], v[60:63]\n" \
  "ds_read_b128 v[24:27], v0 offset:30976\n" \
  "ds_read_b128 v[28:31], v1 offset:14336\n" \
  "v_mfma_f32_16x16x32_bf16 v[42:45], v[12:15], v[112:115], v[42:45]\n" \
  "v_mfma_f32_16x16x32_bf16 v[48:51], v[12:15], v[176:179], v[48:51]\n" \
  "v_mfma_f32_16x16x32_bf16 v[54:57], v[12:15], v[240:243], v[54:57]\n" \
  "s_waitcnt lgkmcnt(4)\n" \
  "v_mfma_f32_16x16x32_bf16 v[60:63], v[12:15], v[32:35], v[60:63]\n" \
  "ds_read_b128 v[32:35], v1 offset:15360\n" \
  "v_mfma_f32_16x16x32_bf16 v[42:45], v[16:19], v[116:119], v[42:45]\n" \
  "v_mfma_f32_16x16x32_bf16 v[48:51], v[16:19], v[180:183], v[48:51]\n" \
  "v_mfma_f32_16x16x32_bf16 v[54:57], v[16:19], v[244:247], v[54:57]\n" \
  "s_waitcnt lgkmcnt(3)\n" \
  "v_mfma_f32_16x16x32_bf16 v[60:63], v[16:19], v[36:39], v[60:63]\n" \
  "ds_read_b128 v[36:39], v3 offset:64\n" \
  "v_mfma_f32_16x16x32_bf16 v[42:45], v[20:23], v[120:123], v[42:45]\n" \
  "v_mfma_f32_16x16x32_bf16 v[48:51], v[20:23], v[184:187], v[48:51]\n" \
  "v_mfma_f32_16x16x32_bf16 v[54:57], v[20:23], v[248:251], v[54:57]\n" \
  "s_waitcnt lgkmcnt(2)\n" \
  "v_mfma_f32_16x16x32_bf16 v[60:63], v[20:23], v[28:31], v[60:63]\n" \
  "ds_read_b128 v[28:31], v3 offset:192\n" \
  "s_mov_b64 exec, 1\n" \
  "ds_add_u32 v6, v7 offset:8\n" \
  "s_mov_b64 exec, -1\n" \
  "v_xor_b32 v6, 4, v6\n" \
  "v_mfma_f32_16x16x32_bf16 v[42:45], v[24:27], v[124:127], v[42:45]\n" \
  "v_mfma_f32_16x16x32_bf16 v[48:51], v[24:27], v[188:191], v[48:51]\n" \
  "v_mfma_f32_16x16x32_bf16 v[54:57], v[24:27], v[252:255], v[54:57]\n" \
  "s_waitcnt lgkmcnt(3)\n" \
  "v_mfma_f32_16x16x32_bf16 v[60:63], v[24:27], v[32:35], v[60:63]\n" \
  "s_waitcnt lgkmcnt(1)\n" \
  "s_nop 4\n" \
  "ds_read_b32 v5, v6 offset:0\n" \
  "v_fma_f32 v42, -v36, v8, v42\n" \
  "v_fma_f32 v43, -v37, v8, v43\n" \
  "v_fma_f32 v44, -v38, v8, v44\n" \
  "v_fma_f32 v45, -v39, v8, v45\n" \
  "v_mul_f32 v42, v42, v28\n" \
  "v_mul_f32 v43, v43, v29\n" \
  "v_mul_f32 v44, v44, v30\n" \
  "v_mul_f32 v45, v45, v31\n" \
  "v_cvt_pk_bf16_f32 v42, v42, v43\n" \
  "v_cvt_pk_bf16_f32 v43, v44, v45\n" \
  "buffer_store_dwordx4 v[40:43], v2, s[40:43], s65 offen offset:0 sc1\n" \
  "v_fma_f32 v48, -v36, v9, v48\n" \
  "v_fma_f32 v49, -v37, v9, v49\n" \
  "v_fma_f32 v50, -v38, v9, v50\n" \
  "v_fma_f32 v51, -v39, v9, v51\n" \
  "v_mul_f32 v48, v48, v28\n" \
  "v_mul_f32 v49, v49, v29\n" \
  "v_mul_f32 v50, v50, v30\n" \
  "v_mul_f32 v51, v51, v31\n" \
  "v_cvt_pk_bf16_f32 v48, v48, v49\n" \
  "v_cvt_pk_bf16_f32 v49, v50, v51\n" \
  "buffer_store_dwordx4 v[46:49], v2, s[40:43], s65 offen offset:1024 sc1\n" \
  "v_fma_f32 v54, -v36, v10, v54\n" \
  "v_fma_f32 v55, -v37, v10, v55\n" \
  "v_fma_f32 v56, -v38, v10, v56\n" \
  "v_fma_f32 v57, -v39, v10, v57\n" \
  "v_mul_f32 v54, v54, v28\n" \
  "v_mul_f32 v55, v55, v29\n" \
  "v_mul_f32 v56, v56, v30\n" \
  "v_mul_f32 v57, v57, v31\n" \
  "v_cvt_pk_bf16_f32 v54, v54, v55\n" \
  "v_cvt_pk_bf16_f32 v55, v56, v57\n" \
  "buffer_store_dwordx4 v[52:55], v2, s[40:43], s65 offen offset:2048 sc1\n" \
  "v_fma_f32 v60, -v36, v4, v60\n" \
  "v_fma_f32 v61, -v37, v4, v61\n" \
  "v_fma_f32 v62, -v38, v4, v62\n" \
  "v_fma_f32 v63, -v39, v4, v63\n" \
  "v_mul_f32 v60, v60, v28\n" \
  "v_mul_f32 v61, v61, v29\n" \
  "v_mul_f32 v62, v62, v30\n" \
  "v_mul_f32 v63, v63, v31\n" \
  "v_cvt_pk_bf16_f32 v60, v60, v61\n" \
  "v_cvt_pk_bf16_f32 v61, v62, v63\n" \
  "buffer_store_dwordx4 v[58:61], v2, s[40:43], s65 offen offset:3072 sc1\n" \
  "s_waitcnt lgkmcnt(0)\n" \
  "s_add_u32 s65, s65, 0x40000\n" \
  "s_sub_u32 s64, s64, 1\n" \
  "s_mov_b32 s68, 1\n" \
  "s_cmp_lg_u32 s64, 0\n" \
  "s_cbranch_scc1 .LCW_TOP_%=\n" \
  "s_waitcnt vmcnt(0)\n" \
  "s_mov_b64 exec, 1\n" \
  "buffer_store_dword v7, off, s[56:59], s72 sc1\n" \
  "s_mov_b64 exec, -1\n" \
  "s_add_u32 s72, s72, 0x1000\n" \
  "s_mov_b32 s68, 0\n" \
  ".LCW_END_%=:\n" \
  "s_endpgm\n"
constexpr int RG = 2;
#define RFL(x) __builtin_amdgcn_readfirstlane(x)
#define HALF(x, i) BF8(((i) ? __builtin_shufflevector(x, x, 4, 5, 6, 7) : __builtin_shufflevector(x, x, 0, 1, 2, 3)))
constexpr int P_W = 65536, P_ST = 131072, P_SS = P_ST + 1024, P_CN = P_SS + 512;
template <bool PR>
__device__ __forceinline__ void role_proj_t(const MegaArgs& A, char* smem, const int L, const int idx, const bool kv) {
  const int w = A.wv;
  const int l = lane_id();
  const int c = l & 15, g = l >> 4;
  char* ws = A.ws;
  unsigned* FL = (unsigned*)(ws + OFF_FL);
  unsigned* TMO = FL + FL_TMO;
  const unsigned sb = lds_base(smem);
  auto cn = (volatile __attribute__((address_space(3))) unsigned*)(unsigned long)(sb + P_CN);
  u64 tag;
  const bool h4 = kv && L;
  const __amdgpu_buffer_rsrc_t rsM = MKRS(ws + OFF_MEMK + L * SZ_MEM, kv && w == 2 ? (4 - L) * SZ_MEM : 0);
  const bool mc = kv && w == 2 && (MCE < 2 || !L);
  constexpr int NMC = MCE == 2 ? 8 : 4;
  const int b0 = RFL(h4 ? (H4S ? 2 : 3) * w : w == 2 ? 0 : HX0),
            b1 = RFL(h4 ? (H4S ? 2 * w + 2 : w == 2 ? 8 : 3 * w + 3 * (w < 2)) : w == 2 ? HX0 : 8);
  const __amdgpu_buffer_rsrc_t rsF = MKRS(FL + FL_FC + L * 64 * 32 * 32, 64 * 4096);
  const int fo = ((blockIdx.x & 3) * 8 + ((H4S && h4) || w == 3 ? l >> 3 : min(b0 + (l >> 3), b1 - 1))) * 128 + (l & 7) * 4;
#define FC_LOOK(s) __builtin_amdgcn_raw_buffer_load_b32(rsF, fo, (s) * 4096, AUX_SC1)
#define MEM_CLEAR(s)                                                                                                          \
  _Pragma("unroll") for (int i = 0; i < NMC; ++i)                                                                             \
    ST128((v4u{0u, 0u, 0u, 0u}), rsM, l * 16 + (i & 1) * 1024, (s) * 65536 + idx * 2048 + (i >> 1) * (int)SZ_MEM * (MCE == 2 ? 1 : 2));
  const char* Xb = ws + (L ? OFF_X3 : OFF_XA);
  const __amdgpu_buffer_rsrc_t rsS = MKRS(ws + OFF_H + L * (SZ_X / 2), SZ_X / 2);
  unsigned nf = 0u;
  bool early = false;
  v4u t0[16];
#define X_ASK(s) \
  for (int i = b0; i < b1; ++i) glds64c(Xb + RFL((s) * 32768 + i * 4096), l * 16, RFL(sb + ((s) & 1) * 32768 + i * 4096));
#define S_ASK(s, t) _Pragma("unroll") for (int i = 0; i < 16; ++i) t[i] = LD128(rsS, l * 16, (s) * 16384 + i * 1024);
  {
    const int j = w & 3;
    const bool vt = kv && j >= 2;
    constexpr int NT = PR ? 3 : 1;
    v8u wq[NT][8];
    const u16* Wm = (kv ? (PR ? A.Wv : A.Wk) : A.Wq) - (PR ? 128 : 0) * HD;
    const int hm = kv ? idx : 2 * idx + PR;
    const int q = (l >> 2) & 3, p = l & 3, f = q | ((g & 1) << 2);
    unsigned ra[NT];
#pragma unroll
    for (int m = 0; m < NT; ++m) {
      const int ct = PR ? m : 3;
      ra[m] = sb + (j >> 1) * 65536 + (8 * g + q) * 256 + ((4 * (j & 1) + (vt ? ct : (ct & 2) + (p >> 1))) ^ f) * 32 + (vt ? p * 8 : (p & 1) * 16 + (ct & 1) * 8);
    }
#define W_ISSUE(n) wqkv_issue<false>(Wm + (128 * (n) - 256 * ((n) & 1)) * HD, (n) & 1, hm, w, l, sb, (WNT >> (kv ? 1 + PR : 3) & 1) && (L == 0 || (WNT & 32)))
#define W_STAGE(n, WAIT, NEXT)                                                                             \
  {                                                                                                        \
    WAIT;                                                                                                  \
    if (n == 3 && EL == 2 && !PR && L == 0 && w >= 2) nf = FC_LOOK(0);                                     \
    if (n == 3 && EL == 1 && !PR && L == 0 && w >= 2) {                                                    \
      early = __all(nf != 0u);                                                                             \
      if (early) {                                                                                         \
        X_ASK(0)                                                                                           \
        if (w == 3) { S_ASK(0, t0) }                                                                       \
      }                                                                                                    \
      nf = FC_LOOK(early);                                                                                 \
    }                                                                                                      \
    RAW_BAR();                                                                                             \
    if (n == 3 && EL == 3 && !PR && L == 0 && w >= 2) nf = FC_LOOK(0);                                     \
    _Pragma("unroll") for (int m = 0; m < NT; ++m) {                                                       \
      _Pragma("unroll") for (int t = 0; t < 2; ++t) {                                                      \
        const unsigned a_ = ra[m] + ((n) & 1) * 32768 + t * 16384;                                         \
        const v2u r0 = trd(a_), r1 = trd(a_ + 1024), r2 = trd(a_ + 8192), r3 = trd(a_ + 8192 + 1024);      \
        wq[m][2 * (n) + t] = v8u{r0.x, r0.y, r1.x, r1.y, r2.x, r2.y, r3.x, r3.y};                          \
      }                                                                                                    \
    }                                                                                                      \
    _Pragma("unroll") for (int m = 0; m < 2 * NT; ++m) asm volatile("" : "+v"(wq[m >> 1][2 * (n) + (m & 1)])); \
    RAW_BAR();                                                                                             \
    NEXT;                                                                                                  \
    _Pragma("unroll") for (int m = 0; m < 4 * NT; ++m)                                                     \
      cs[m % NT] = MFMA16(HALF(wq[m % NT][2 * (n) + m / NT / 2], (m / NT) & 1), BF8(one), cs[m % NT]);     \
    _Pragma("unroll") for (int m = 0; m < NT; ++m) asm volatile("" : "+v"(cs[m]));                         \
  }
    if (EL && !MC3 && mc) { MEM_CLEAR(0) }
    W_ISSUE(0);
    W_ISSUE(1);
    tag = call_tag(A);
    if (w == 0 && l < 8) cn[l] = l == 1 ? 3u : l == 5 ? 2u : 0u;
    const v4u one = {0x3f803f80u, 0x3f803f80u, 0x3f803f80u, 0x3f803f80u};
    f32x4 cs[NT] = {};
    u64 rv = 0;
    W_STAGE(0, WAIT_VM(8), W_ISSUE(2); if (EL) RD_ASK(rv));
    SST(4);
    W_STAGE(1, if (EL) WAIT_VM(9); else WAIT_VM(8), W_ISSUE(3); if (EL != 0 && MC3 != 0 && mc) { MEM_CLEAR(0) if (MC3 == 2) { MEM_CLEAR(1) } });
    SST(5);
    W_STAGE(2, if (EL != 0 && MC3 == 2 && mc) WAIT_VM(16); else if (EL != 0 && MC3 != 0 && mc) WAIT_VM(12); else WAIT_VM(8), (void)0);
    SST(6);
    asm volatile("" : "+v"(rv));
    if (!EL) ready_wait(A, tag, ready_look(A));
    for (unsigned spins = 0; EL && !__all(rv == tag) && spins < SPIN_CAP; ++spins) {
      __builtin_amdgcn_s_sleep(8);
      RD_ASK(rv);
      asm volatile("s_waitcnt vmcnt(0)" : "+v"(rv)::"memory");
    }
    if (EL == 1 && !PR && L == 0 && w >= 2) nf = FC_LOOK(0);
    W_STAGE(3, if (EL >= 2 && MC3 == 2 && mc) WAIT_VM(8); else if (EL >= 2 && MC3 == 1 && mc) WAIT_VM(4); else WAIT_VM(0), if (!PR) {
      _Pragma("unroll") for (int ks = 0; ks < 16; ++ks)
        *reinterpret_cast<bf16x8*>(smem + P_W + j * 16384 + ks * 1024 + l * 16) = HALF(wq[0][ks >> 1], ks & 1);
    });
    SST(7);
#undef W_STAGE
#undef W_ISSUE
#pragma unroll
    for (int m = 0; m < NT; ++m) *reinterpret_cast<f32x4*>(smem + P_ST + j * 256 + (PR ? m : 3) * 64 + g * 16) = cs[m];
    RAW_BAR();
    SST(8);
    if constexpr (PR) {
      const v4u rsO = mk_srd(ws + RFL((unsigned)(vt ? OFF_VF + L * SZ_O : OFF_QK + L * SZ_QK)), RFL((unsigned)(vt ? SZ_O : SZ_QK)));
      const v4u rsP = mk_srd(FL + RFL(kv ? FL_FQ + (L * 64 * 32 + idx) * 32 + 2 * j : FL_FP + (L * 16 + idx) * 32 + j), 63 * 4096 + 4);
      const int so = RFL(vt ? idx * 8 + 4 * (j & 1) : (kv ? idx * 16 + 8 : (2 * idx + (j >> 1)) * 16) + 2 * (j & 1));
      const v8u pv = {sb + 32768 + g * 512 + c * 16, sb + P_W + j * 16384 + l * 16, (unsigned)l * 16, sb + P_SS + 256 + (vt ? g * 16 : c * 4),
                      sb + P_ST + j * 256 + (vt ? c * 4 : g * 16), 0u, 0u, 0u};
      const char* dbg = nullptr;
#define P_OPS                                                                                                                               \
  ::"{v[64:71]}"(wq[0][0]), "{v[72:79]}"(wq[0][1]), "{v[80:87]}"(wq[0][2]), "{v[88:95]}"(wq[0][3]), "{v[96:103]}"(wq[0][4]),                \
      "{v[104:111]}"(wq[0][5]), "{v[112:119]}"(wq[0][6]), "{v[120:127]}"(wq[0][7]), "{v[128:135]}"(wq[1][0]), "{v[136:143]}"(wq[1][1]),     \
      "{v[144:151]}"(wq[1][2]), "{v[152:159]}"(wq[1][3]), "{v[160:167]}"(wq[1][4]), "{v[168:175]}"(wq[1][5]), "{v[176:183]}"(wq[1][6]),     \
      "{v[184:191]}"(wq[1][7]), "{v[192:199]}"(wq[2][0]), "{v[200:207]}"(wq[2][1]), "{v[208:215]}"(wq[2][2]), "{v[216:223]}"(wq[2][3]),     \
      "{v[224:231]}"(wq[2][4]), "{v[232:239]}"(wq[2][5]), "{v[240:247]}"(wq[2][6]), "{v[248:255]}"(wq[2][7]), "{v[0:7]}"(pv),               \
      "{s[40:43]}"(rsO), "{s[56:59]}"(rsP), "{s64}"(NSEG), "{s65}"(so * 1024), "{s70}"(sb + P_CN), "{s[36:37]}"(dbg)                        \
      : "memory", "vcc", "scc", "s80", "v63"
      if (vt) asm volatile("s_mov_b64 exec, -1\ns_waitcnt vmcnt(0) lgkmcnt(0)\ns_nop 4\n" V_ASM P_OPS);
      else asm volatile("s_mov_b64 exec, -1\ns_waitcnt vmcnt(0) lgkmcnt(0)\ns_nop 4\n" P_ASM P_OPS);
#undef P_OPS
      return;
    }
  }
  __builtin_amdgcn_s_setprio(RPRIO);
  if (h4 || w >= 2) {
    if (!EL && mc) { MEM_CLEAR(0) }
    auto stats = [&](const v4u (&t)[16], const int s) {
      float a = 0.f, b = 0.f, qa = 0.f, qb = 0.f;
#pragma unroll
      for (int i = 0; i < 16; ++i) {
        a = one(a + __uint_as_float(t[i].x));
        b = one(b + __uint_as_float(t[i].z));
        qa = one(qa + __uint_as_float(t[i].y));
        qb = one(qb + __uint_as_float(t[i].w));
      }
      a = xsum32(xsum16(a));
      b = xsum32(xsum16(b));
      float da = 0.f, db = 0.f;
#pragma unroll
      for (int i = 0; i < 16; ++i) {
        const float u = one(__uint_as_float(t[i].x) - a * (1.0f / 64)), v = one(__uint_as_float(t[i].z) - b * (1.0f / 64));
        da = one(da + u * u);
        db = one(db + v * v);
      }
      const float var = xsum32(swap_add16(qa + da * 0.125f, qb + db * 0.125f)) * (1.0f / DM);
      const float ri = 1.0f / bfr1(sqrtf(bfr1(bfr1(var) + 1e-5f)));
      reinterpret_cast<float*>(smem + P_SS)[(s & 1) * 64 + (g >> 1) * 32 + 2 * c + (g & 1)] = g < 2 ? bfr1(((g & 1) ? b : a) * (1.0f / DM)) : ri;
      asm volatile("" ::: "memory");
      if (l == 0) cn[4 + (s & 1)] = 2 * s + 4;
    };
    static_assert(EL != 1 || MC3 == 2, "clear");
    if (early) {
      if (w == 3) WAIT_VM(17);
      else WAIT_VM(1);
      if (l == 0) __hip_atomic_fetch_add((__attribute__((address_space(3))) unsigned*)cn, 3u, __ATOMIC_RELAXED, __HIP_MEMORY_SCOPE_WORKGROUP);
      SSX(10, w == 2);
      SSX(9, w == 2);
      if (w == 3) stats(t0, 0);
      SSX(11, w == 3);
    }
    unsigned tn = now10(), tg = tn;
    for (int s = early; s < NSEG; ++s) {
      for (unsigned spins = 0; !__all(nf != 0u); ++spins) {
        if (spin_timeout(spins, TMO)) return;
        if (spins) __builtin_amdgcn_s_sleep(SLP_H);
        else if (GF_H && (h4 || (GF_P && L)) && s >= GF0 && s < NSEG - 6) gate(tg);
        tg = now10() + GF_H;
        nf = FC_LOOK(s);
        asm volatile("" ::: "memory");
      }
      if (NFW) asm volatile("" ::"v"(nf));
      TS(4, s, 128);
      for (unsigned spins = 0; cn[2 + (s & 1)] < 4u * (s >> 1); ++spins) {
        if (spins > 64u * SPIN_CAP) return;
        __builtin_amdgcn_s_sleep(2);
      }
      if (L == 0 && s >= PACE0) gate(tn);
      else tn = now10();
      tn += PACE;
      TS(5, s, 128);
      X_ASK(s)
      if (H4S && h4) {
        v4u t[4];
#pragma unroll
        for (int i = 0; i < 4; ++i) t[i] = LD128(rsS, (l >> 2) * 256 + (l & 3) * 16 + w * 64, s * 16384 + i * 4096);
        nf = 0u;
        if (MCE == 1 && w == 2 && s < NSEG - 1) {
          MEM_CLEAR(s + 1)
          WAIT_VM(8);
        } else {
          WAIT_VM(4);
        }
        if (l == 0) __hip_atomic_fetch_add((__attribute__((address_space(3))) unsigned*)cn + (s & 1), w < 2 ? 2u : 1u, __ATOMIC_RELAXED, __HIP_MEMORY_SCOPE_WORKGROUP);
        TS(6, s, 128);
#define F(x) __uint_as_float(x)
        const float a = red_mod4(one(F(t[0].x) + F(t[1].x)) + one(F(t[2].x) + F(t[3].x)));
        const float b = red_mod4(one(F(t[0].z) + F(t[1].z)) + one(F(t[2].z) + F(t[3].z)));
        float da = 0.f, db = 0.f;
#pragma unroll
        for (int i = 0; i < 4; ++i) {
          const float u = one(F(t[i].x) - a * (1.0f / 64)), v = one(F(t[i].z) - b * (1.0f / 64));
          da = one(da + u * u);
          db = one(db + v * v);
        }
        const float qa = one(F(t[0].y) + F(t[1].y)) + one(F(t[2].y) + F(t[3].y)), qb = one(F(t[0].w) + F(t[1].w)) + one(F(t[2].w) + F(t[3].w));
#undef F
        float var = xsum32(swap_add16(one(qa + da * 0.125f), one(qb + db * 0.125f)));
        var += dppr<0x124>(var);
        var += dppr<0x128>(var);
        const float ri = 1.0f / bfr1(sqrtf(bfr1(bfr1(var * (1.0f / DM)) + 1e-5f)));
        if (c < 4) reinterpret_cast<float*>(smem + P_SS)[(s & 1) * 64 + (g >> 1) * 32 + 8 * w + 2 * c + (g & 1)] = g < 2 ? bfr1(((g & 1) ? b : a) * (1.0f / DM)) : ri;
        asm volatile("" ::: "memory");
        if (l == 0) __hip_atomic_fetch_add((__attribute__((address_space(3))) unsigned*)cn + 4 + (s & 1), 1u, __ATOMIC_RELAXED, __HIP_MEMORY_SCOPE_WORKGROUP);
        TS(7, s, 192);
        if (!MCE && w == 2 && s < NSEG - 1) { MEM_CLEAR(s + 1) }
      } else if (w != 3) {
        nf = L ? 0u : FC_LOOK(s + 1);
        if (mc && s < NSEG - 1 && (EL == 0 || MC3 != 2 || s)) {
          MEM_CLEAR(s + 1)
          if (NMC == 8) WAIT_VM(8);
          else WAIT_VM(4);
        } else {
          WAIT_VM(0);
        }
        if (l == 0) __hip_atomic_fetch_add((__attribute__((address_space(3))) unsigned*)cn + (s & 1), h4 ? 2u : 3u, __ATOMIC_RELAXED, __HIP_MEMORY_SCOPE_WORKGROUP);
        TS(6, s, 128);
        SSX(10, s == 0 && w == 2);
      } else {
        v4u t[16];
        S_ASK(s, t)
        WAIT_VM(16);
        nf = L ? 0u : FC_LOOK(s + 1);
        if (l == 0 && !h4) __hip_atomic_fetch_add((__attribute__((address_space(3))) unsigned*)cn + (s & 1), 3u, __ATOMIC_RELAXED, __HIP_MEMORY_SCOPE_WORKGROUP);
        stats(t, s);
        SSX(11, s == 0);
        TS(7, s, 192);
      }
    }
    return;
  }
  const int RL = !kv, rp = idx & 15, u = RFL(kv ? idx >> 4 : L);
  const __amdgpu_buffer_rsrc_t rsW = MKRS(ws + OFF_PW, SZ_PW);
  const __amdgpu_buffer_rsrc_t rsX = MKRS(ws + (RL ? OFF_X3 : OFF_XA), SZ_X);
  const __amdgpu_buffer_rsrc_t rsY = RL ? MKRS(A.out, SZ_X) : MKRS(ws + OFF_X2, SZ_X);
  const int r = 2 * rp + (g >> 1), yo = r * (DM * 2) + (l & 31) * 16 + u * 512;
  if (kv) {
    if (XZD) gate(now10() + XZD);
    if (XZP & 3) {
      if (!wait_n<SLP_V, 32>(FL + FL_FZ + (blockIdx.x & 7) * 64, 64, TMO)) return;
    } else {
      for (int s = w; s < (XZP ? 2 * RG : NSEG); s += RG) ST128((v4u{0u, 0u, 0u, 0u}), rsY, yo, s * SEG * DM * 2);
      DRAIN();
      if (l < 8) set_flag(FL + FL_FZ + l * 64 + 2 * idx + w);
    }
  }
  v4u sel;
#pragma unroll
  for (int e = 0; e < 4; ++e) sel[e] = (g >> 1) == (c >> 3) && ((c & 7) >> 1) == e ? 0x3f80u << (16 * (c & 1)) : 0u;
  unsigned tl = now10();
  for (int s = w; s < NSEG; s += RG) {
    if (GF_R) {
      gate(tl);
      const gu32* f = (gu32*)(FL + FL_FW) + ((RL * 64 + s) * 4 + (blockIdx.x & 3)) * 4 * 32 + (c >> 2) * 32 + (c & 3);
      for (unsigned spins = 0;; ++spins) {
        tl = now10() + GF_R;
        if (__all(__hip_atomic_load(f, RLX_AGENT) != 0u) || spin_timeout(spins, TMO)) break;
        __builtin_amdgcn_s_sleep(SLP_R);
      }
      asm volatile("" ::: "memory");
    } else
    grp_wait<SLP_R, 4, GATE_R>(true, nullptr, 0, FL + FL_FW + ((RL * 64 + s) * 4 + (blockIdx.x & 3)) * 4 * 32, 16, TMO, tl);
    if (s >= NSEG - 6) tl = now10();
    DRAIN();
    TS(0, s, w * 64);
    v4u p[8][2];
    const v4u xr = LD128(rsX, (l & 31) * 512 + r * 16, s * 32768 + u * 16384);
#pragma unroll
    for (int k = 0; k < 8; ++k)
#pragma unroll
      for (int b = 0; b < 2; ++b)
        p[k][b] = LD128(rsW, (g & 1) * 32768 + (c >> 2) * 128 + (g >> 1) * 64 + (c & 3) * 16 + b * 512, ((RL * 64 + s) * 16 + 2 * k) * 32768 + rp * 2048 + u * 1024);
    __builtin_amdgcn_sched_barrier(0);
    f32x4 ac[2] = {};
#pragma unroll
    for (int k = 0; k < 8; ++k)
#pragma unroll
      for (int b = 0; b < 2; ++b) ac[b] = MFMA16(BF8(sel), BF8(p[k][b]), ac[b]);
    v4u xo;
    {
      const auto x = __builtin_amdgcn_permlane16_swap(pk2s(ac[0][0], ac[0][1]), pk2s(ac[1][0], ac[1][1]), false, false);
      const auto y = __builtin_amdgcn_permlane16_swap(pk2s(ac[0][2], ac[0][3]), pk2s(ac[1][2], ac[1][3]), false, false);
      const v4u a = {x[0], y[0], x[1], y[1]};
#pragma unroll
      for (int e = 0; e < 4; ++e) {
        xo[e] = pk2s(lo16(xr[e]) + lo16(a[e]), hi16(xr[e]) + hi16(a[e]));
        if (kv) xo[e] = xo[e] ? xo[e] : 0x8000u;
      }
      if (kv) ST128(xo, rsY, yo, s * SEG * DM * 2);
      else ST128A(xo, rsY, yo, s * SEG * DM * 2, OAUX);
    }
    TS(1, s, w * 64);
    if (kv) {
      const float pv = lo16(g < 2 ? __builtin_amdgcn_readlane(xo[0], 0) : __builtin_amdgcn_readlane(xo[0], 32));
      float sm = 0.f, q0 = 0.f, q1 = 0.f;
#pragma unroll
      for (int e = 0; e < 4; ++e) {
        const float a = one(lo16(xo[e]) - pv), b = one(hi16(xo[e]) - pv);
        sm = one(sm + one(a + b));
        q0 = one(q0 + a * a);
        q1 = one(q1 + b * b);
      }
      sm = half_sum(sm);
      const float q = fmaxf(one(half_sum(q0 + q1)) - sm * sm * (1.0f / 256), 0.f);
      const u64 st = (u64)__float_as_uint(sm + pv * 256.f) | (u64)(__float_as_uint(q) | 1u) << 32;
      if ((l & 31) < 4) __hip_atomic_store((gu64*)(FL + FL_FR + ((s * 4 + (l & 3)) * 4 + (r >> 3)) * 32 + (r & 7) * 4 + 2 * u), st, RLX_AGENT);
      if (XZP == 4 && s + 2 * RG < NSEG) ST128((v4u{0u, 0u, 0u, 0u}), rsY, yo, (s + 2 * RG) * SEG * DM * 2);
    }
    TS(3, s, w * 64);
  }
  if (L == 1 && idx == 0 && w == 0 && l == 0) __hip_atomic_store((gu64*)(ws + OFF_EP), tag, RLX_AGENT);
}
__device__ __forceinline__ void role_proj(const MegaArgs& A, char* smem, const int L, const int idx, const bool kv) {
  if (A.wv < 4) role_proj_t<false>(A, smem, L, idx, kv);
  else role_proj_t<true>(A, smem, L, idx, kv);
}

constexpr int CUN = 2144;
__device__ __forceinline__ v2u part_stats(const v4u o) {
  float v[8];
#pragma unroll
  for (int e = 0; e < 4; ++e) { v[2 * e] = lo16(o[e]); v[2 * e + 1] = hi16(o[e]); }
  const float s = ((v[0] + v[1]) + (v[2] + v[3])) + ((v[4] + v[5]) + (v[6] + v[7])), m = s * 0.125f;
  float q0 = 0.f, q1 = 0.f;
#pragma unroll
  for (int e = 0; e < 4; ++e) {
    const float a = v[e] - m, b = v[4 + e] - m;
    q0 += a * a;
    q1 += b * b;
  }
  return v2u{__float_as_uint(s), __float_as_uint(q0 + q1)};
}
constexpr int CRN = 176;
constexpr int CTN = 272;
constexpr int CV_LDS = (8 * CKN + 8 * 2 * CUN + 64 + 2 * 32 * 8 + 16 * CRN + 64 + 8 * CTN) * 2 + 8 * 1568;
__device__ __forceinline__ void role_conv(const MegaArgs& A, char* smem, const int cv) {
  u16* Kr = reinterpret_cast<u16*>(smem);
  u16* Uc = Kr + 8 * CKN;
  u16* Us = Uc + 8 * CUN + 63;
  u16* To = Uc + 8 * 2 * CUN + 64;
  u16* Sx = To + 32 * 8;
  u16* Rc = Sx + 32 * 8;
  u16* Rs = Rc + 8 * CRN + 63;
  u16* Ta = Rc + 16 * CRN + 64;
  const int w = A.wv;
  const int l = lane_id(), tid = 64 * w + l, c = l & 15, g = l >> 4;
  const int d = 8 * cv + w;
  char* ws = A.ws;
  unsigned* FL = (unsigned*)(ws + OFF_FL);
  unsigned* TMO = FL + FL_TMO;
  u16* kr = Kr + w * CKN;
  const u16* ka = kr + 2080 - 16 * c + 8 * g;
#define KFRAG(D0) (*reinterpret_cast<const f16x8*>(ka - (D0)))
  typedef __attribute__((address_space(3))) const unsigned* lpu;
#define BFRAG(a) __builtin_bit_cast(f16x8, (v4u{((lpu)(unsigned long)(a))[0], ((lpu)(unsigned long)(a))[1], ((lpu)(unsigned long)(a))[2], ((lpu)(unsigned long)(a))[3]}))
  f32x4 acc[8];
  v4u tx[4];
  const __amdgpu_buffer_rsrc_t rz1 = MKRS(ws + OFF_XA, SZ_X), rps = MKRS(ws + OFF_H, SZ_X);
  u16* uc = Uc + w * CUN;
  const unsigned ub = lds_base(reinterpret_cast<char*>(((c & 1) ? Us : Uc) + w * CUN + c + 8 * g + 32));
  const int t0 = 16 * c + 4 * g;
  float dsk, ysc;
#define SKIP(T) (*reinterpret_cast<const v2u*>(uc + 32 + 256 * (T) + t0))
#define TILE_OUT(dst, a, uu)                                                                   \
  {                                                                                            \
    v2u v_;                                                                                    \
    _Pragma("unroll") for (int e = 0; e < 2; ++e) {                                            \
      const unsigned yy = pk2(a[2 * e] * ysc, a[2 * e + 1] * ysc), u_ = uu[e];                 \
      const unsigned k_ = pk2(h2f((u16)u_) * dsk, h2f((u16)(u_ >> 16)) * dsk);                 \
      v_[e] = pk2(lo16(yy) + lo16(k_), hi16(yy) + hi16(k_));                                   \
    }                                                                                          \
    *reinterpret_cast<v2u*>((dst) + t0) = v_;                                                  \
  }
#define CHUNK_ST(i, src, N) CHUNK_STX(i, tx[i], src, N)
#define CHUNK_STX(i, X, src, N)                                                                \
  {                                                                                            \
    const unsigned p_ = lds_base((char*)((src) + (c >> 2) * (N) + (tid & ~15) + 4 * (c & 3))); \
    const v2u a0_ = trd(p_), a1_ = trd(p_ + 8 * (N));                                          \
    const v4u a_ = {a0_.x, a0_.y, a1_.x, a1_.y};                                               \
    v4u o;                                                                                     \
    _Pragma("unroll") for (int e = 0; e < 4; ++e) o[e] = pk2(lo16(X[e]) + lo16(a_[e]), hi16(X[e]) + hi16(a_[e])); \
    ST128(o, rz1, (l >> 5) * 32768 + (l & 31) * 16, (2 * (w + 8 * (i)) * 64 + cv) * 512);      \
    ST64(part_stats(o), rps, (l >> 5) * 16384 + (l & 31) * 8, (2 * (w + 8 * (i)) * 64 + cv) * 256); \
  }
#define CHUNK_FLAG(i) set_flag(FL + FL_FC + (((2 * w + 16 * ((i) + (l >> 3)) + ((l >> 2) & 1)) * 4 + (l & 3)) * 8 + (cv >> 3)) * 32 + (cv & 7))
  {
#define U_VAL(i)                                                                               \
  {                                                                                            \
    const float mb_ = lo16(st[i]), ri_ = 1024.0f / hi16(st[i]);                                 \
    _Pragma("unroll") for (int e = 0; e < 4; ++e) {                                            \
      const unsigned t_ = pk2(lo16(tx[i][e]) - mb_, hi16(tx[i][e]) - mb_);                     \
      const unsigned q_ = pk2(lo16(t_) * ri_, hi16(t_) * ri_);                                 \
      uv[e] = pkh(lo16(q_), hi16(q_));                                                         \
    }                                                                                          \
  }
    unsigned ta[3], k0p, st[4];
    v4u uv;
    v2u kn;
    u64 tag;
    const __amdgpu_buffer_rsrc_t rst = MKRS(ws + OFF_GT, (L_SEQ + 512) * 16);
    {
      KGen G;
      G.load(A, d, l);
      const u16 pk = A.Dskip[d];
      asm volatile("" ::: "memory");
      if (XL == 1) ask_slow(A, 16 * (w - 4) + (cv & 15), ta);
      if (XL) asm volatile("" : "+v"(G.pa), "+v"(G.pb), "+v"(G.pc), "+v"(G.pd), "+v"(G.pe)::"memory");
      tx[0] = *reinterpret_cast<const v4u*>(A.x0 + (size_t)tid * DM + 8 * cv);
      if (XL != 1) ask_slow(A, 16 * (w - 4) + (cv & 15), ta);
      tag = call_tag(A);
      G.setup();
      dsk = bf2f(pk) * (1.0f / 1024);
      G.st = smem + w * 16640;
      SST(0);
      ysc = G.scale();
      const Cx z = G.power(1.f);
      Cx y = cmul(G.C, z);
      G.rows(G.st + l * 8, y, z, 16);
      G.get(G.bh, G.bl, l);
      const float k0 = 2.0f * wave_sum(G.C.r.x + G.C.r.y);
      k0p = l == 0 ? pkh(k0, 0.f) : 0u;
      if (TP) {
        constexpr int R = 64 * TP, U0 = 2 * R + 32, U1 = 3 * R + 111, KN = 4 * R + 192;
        u16* k1 = reinterpret_cast<u16*>(smem + 8 * 16640) + w * KN;
        u16* t1 = reinterpret_cast<u16*>(smem + 8 * 16640) + 8 * KN;
        v4u s1 = {};
        u64 rv = 0;
        if (w < TP) {
          s1 = LD128(rst, tid * 16, (L_SEQ + R * (cv >> 4)) * 16);
          rv = ready_look(A);
        }
        if (l < R / 8 + 4) reinterpret_cast<v4u*>(k1)[l < R / 8 ? l + R / 8 + 4 : l - R / 8] = v4u{k0p, 0u, 0u, 0u};
        k1[U0 + (l < 32 ? l : R + l)] = k1[U1 + (l < 32 ? l : R + l)] = 0;
        G.columns(l, 0, R / 16);
        const f32x4 kt = G.tile<true>(l);
        if (c < R / 16) *reinterpret_cast<v2u*>(k1 + R + 28 - 16 * c - 4 * g) = v2u{pkh(kt.w, kt.z), pkh(kt.y, kt.x)};
        if (w < TP) {
          for (unsigned spins = 0; !__all(has_tag(tag, s1.x, s1.y) && has_tag(tag, s1.z, s1.w)) && spins < SPIN_CAP; ++spins) {
            __builtin_amdgcn_s_sleep(4);
            s1 = LD128(rst, tid * 16, (L_SEQ + R * (cv >> 4)) * 16);
          }
          st[0] = __builtin_amdgcn_perm(s1.w, s1.y, 0x07060302);
          U_VAL(0);
          u16* up = reinterpret_cast<u16*>(smem + 8 * 16640) + 32 + tid;
#pragma unroll
          for (int e = 0; e < 8; ++e) up[e * KN + U0] = up[e * KN + U1] = (u16)(uv[e >> 1] >> (16 * (e & 1)));
        }
        RAW_BAR();
        const unsigned u1 = lds_base(reinterpret_cast<char*>(k1 + ((c & 1) ? U1 : U0) + c + 8 * g));
        f32x4 a = {};
#pragma unroll
        for (int b = 0; b <= R / 32; ++b)
          a = MFMAH(BFRAG(u1 + 64 * b), *reinterpret_cast<const f16x8*>(k1 + R - 16 * (c & (R / 16 - 1)) + 8 * g + 32 * b), a);
        if (c < R / 16) {
          const v2u sk = *reinterpret_cast<const v2u*>(k1 + U0 + 32 + t0);
          TILE_OUT(t1 + w * (R + 16), a, sk);
        }
        RAW_BAR();
        SST(1);
        if (w < TP) CHUNK_ST(0, t1, R + 16);
        G.columns(l, R / 16, TPS);
        if (w < TP) {
          DRAIN();
          ready_wait(A, tag, rv);
          if (l < 8) CHUNK_FLAG(0);
        }
        SST(6);
        G.columns(l, TPS, 16);
      } else {
        G.columns(l, 0, 16);
      }
      const f32x4 kt = G.tile<true>(l);
      kn = v2u{pkh(kt.w, kt.z), pkh(kt.y, kt.x)};
    }
#define U_PUT(i)                                                                               \
  _Pragma("unroll") for (int e = 0; e < 4; ++e) {                                              \
    ((lph)(unsigned long)up0)[(2 * e) * CUN + 512 * (i)] = (u16)uv[e];                         \
    ((lph)(unsigned long)up0)[(2 * e + 1) * CUN + 512 * (i)] = (u16)(uv[e] >> 16);             \
    ((lph)(unsigned long)up1)[(2 * e) * CUN + 512 * (i)] = (u16)uv[e];                         \
    ((lph)(unsigned long)up1)[(2 * e + 1) * CUN + 512 * (i)] = (u16)(uv[e] >> 16);             \
  }
#define U_ROW(i) { U_VAL(i) U_PUT(i) }
    typedef __attribute__((address_space(3))) u16* lph;
    unsigned up0 = lds_base((char*)(Uc + 32 + tid)), up1 = lds_base((char*)(Us + 32 + tid));
    asm volatile("" : "+v"(up0), "+v"(up1), "+v"(kn), "+v"(k0p));
#define ST_GET(I0, I1)                                                                                                        \
  for (unsigned spins = 0; spins < SPIN_CAP; ++spins) {                                                                       \
    bool ok = true;                                                                                                           \
    v4u t_[4];                                                                                                                \
    _Pragma("unroll") for (int i = I0; i < I1; ++i) t_[i] = LD128(rst, (tid + 512 * i) * 16, 0);                              \
    _Pragma("unroll") for (int i = I0; i < I1; ++i) {                                                                         \
      ok = ok && has_tag(tag, t_[i].x, t_[i].y) && has_tag(tag, t_[i].z, t_[i].w);                                            \
      st[i] = __builtin_amdgcn_perm(t_[i].w, t_[i].y, 0x07060302);                                                            \
    }                                                                                                                         \
    if (__all(ok)) break;                                                                                                     \
    __builtin_amdgcn_s_sleep(4);                                                                                              \
    asm volatile("" ::: "memory");                                                                                            \
  }
    if (w < 4) {
      ST_GET(0, 2);
      U_VAL(0);
      asm volatile("" : "+v"(uv));
    }
#pragma unroll
    for (int i = 1; i < 4; ++i) tx[i] = *reinterpret_cast<const v4u*>(A.x0 + (size_t)(tid + 512 * i) * DM + 8 * cv);
    RAW_BAR();
    SST(2);
    if (w < 4) { U_PUT(0) }
    *reinterpret_cast<v2u*>(kr + 2076 - 16 * c - 4 * g) = kn;
    if (l < 38) reinterpret_cast<v4u*>(kr)[l < 32 ? 260 + l : l < 34 ? 194 + l : l - 34] = v4u{k0p, 0u, 0u, 0u};
    (l < 32 ? uc : Us + w * CUN - 32)[l] = 0;
    uc[32 + L_SEQ + l] = Us[w * CUN + 32 + L_SEQ + l] = 0;
    uc[32 + 256 + l] = Us[w * CUN + 32 + 256 + l] = 0;
    RAW_BAR();
    SST(11);
    {
      f32x4 a0 = {}, a1 = {};
      f16x8 bf[9], af[9];
#pragma unroll
      for (int b = 0; b < 9; ++b) {
        af[b] = KFRAG(32 - 32 * b);
        bf[b] = BFRAG(ub + 64 * (b - 1));
      }
      const v2u k0 = SKIP(0);
      __builtin_amdgcn_sched_barrier(0);
#pragma unroll
      for (int b = 0; b < 9; ++b) (b & 1 ? a1 : a0) = MFMAH(bf[b], af[b], (b & 1 ? a1 : a0));
      a0 += a1;
      TILE_OUT(Ta + w * CTN, a0, k0);
    }
    const u64 rv = ready_look(A);
    RAW_BAR();
    SST(3);
    if (w < 4) {
      if (w >= TP) CHUNK_ST(0, Ta, CTN);
      asm volatile("" : "+v"(tx[1]));
      U_ROW(1);
      DRAIN();
      ready_wait(A, tag, rv);
      if (l < 8 && w >= TP) CHUNK_FLAG(0);
      ST_GET(2, 4);
    } else {
      ST_GET(0, 4);
      ready_wait(A, tag, rv);
      asm volatile("" : "+v"(tx[0]), "+v"(tx[1]));
      U_ROW(0);
      U_ROW(1);
    }
    ASKED(ta);
    if (tid < (16 * CRN + 64) / 8) reinterpret_cast<v4u*>(Rc)[tid] = v4u{0u, 0u, 0u, 0u};
    SST(4);
    const gu64* fk = (gu64*)(ws + OFF_KG) + cv * 16 + w;
    u64 kg = __hip_atomic_load(fk, RLX_AGENT);
    U_ROW(2);
    for (unsigned spins = 0; !__all(kg == tag) && spins < SPIN_CAP; ++spins) {
      __builtin_amdgcn_s_sleep(8);
      kg = __hip_atomic_load(fk, RLX_AGENT);
    }
    asm volatile("" : "+v"(tx[3]), "+v"(st[3])::"memory");
    SST(5);
    const unsigned* kd = reinterpret_cast<const unsigned*>(Ta + 8 * CTN) + w * 392;
    glds16c(ws + OFF_KF + d * 2048, l * 16, lds_base((char*)kd));
    if (l < 34) glds16c(ws + OFF_KF + d * 2048 + 1024, l * 16, lds_base((char*)kd) + 1024);
    U_ROW(3);
    DRAIN();
    {
#define MIX(lo, hi) __builtin_amdgcn_perm(hi, lo, 0x07060100)
      const unsigned a0 = kd[384 - l], b0 = kd[383 - l], a1 = kd[320 - l], b1 = kd[319 - l];
      const unsigned a2 = kd[256 - 2 * l], m2 = kd[255 - 2 * l], b2 = kd[254 - 2 * l];
      const unsigned* k3 = kd + 124 - 4 * (l & 31);
      v4u* ko = reinterpret_cast<v4u*>(kr) + l;
      ko[4] = v4u{a0, a0, (l & 1) ? b0 : MIX(a0, b0), b0};
      ko[68] = v4u{a1, a1, (l & 1) ? b1 : MIX(a1, b1), b1};
      ko[132] = v4u{a2, MIX(a2, m2), m2, b2};
      if (l < 32) ko[196] = v4u{k3[4], MIX(k3[3], k3[2]), k3[2], MIX(k3[1], k3[0])};
#undef MIX
    }
    RAW_BAR();
    SST(7);
    {
      unsigned uq[4] = {ub, ub + 1024, ub + 2048, ub + 3072};
      f16x8 bf[8], a[4];
      const f16x8 bm = BFRAG(ub - 64);
      asm volatile("" : "+v"(uq[0]), "+v"(uq[1]), "+v"(uq[2]), "+v"(uq[3]));
#pragma unroll
      for (int j = 1; j < 8; ++j) bf[j] = BFRAG(uq[j >> 1] + 512 * (j & 1));
#pragma unroll
      for (int T = 1; T < 8; ++T) acc[T] = MFMAH(bm, KFRAG(256 * T + 32), (f32x4{}));
#pragma unroll
      for (int e = 0; e < 3; ++e) a[e] = KFRAG(256 * e);
      const u16* kq = ka - 256 * 7;
#pragma unroll 1
      for (int r = 0; r < 8; ++r) {
#pragma unroll
        for (int e = 0; e < 8; ++e) {
          a[(e + 3) & 3] = *reinterpret_cast<const f16x8*>(kq + 256 * (7 - ((e + 3) & 7)) + (e >= 5 ? 32 : 0));
          const int j = (8 - e) & 7;
          bf[j] = BFRAG(uq[j >> 1] + 512 * (j & 1) + (e ? 64 : 0));
          __builtin_amdgcn_sched_barrier(0);
#pragma unroll
          for (int T = 7; T >= (e > 1 ? e : 1); --T) acc[T] = MFMAH(bf[T - e], a[e & 3], acc[T]);
          __builtin_amdgcn_sched_barrier(0);
        }
        kq += 32;
#pragma unroll
        for (int j = 0; j < 4; ++j) uq[j] += 64;
        asm volatile("" : "+v"(uq[0]), "+v"(uq[1]), "+v"(uq[2]), "+v"(uq[3]));
      }
    }
    SST(8);
    {
      v2u k[8];
#pragma unroll
      for (int T = 1; T < 8; ++T) k[T] = SKIP(T);
#pragma unroll
      for (int T = 1; T < 8; ++T) TILE_OUT(uc + 32 + 256 * T, acc[T], k[T]);
    }
    RAW_BAR();
    SST(10);
    if (w >= 4) CHUNK_ST(0, Uc + 32, CUN);
    CHUNK_ST(1, Uc + 32 + 512, CUN);
    if (PP < 0) {
      if (CVSP) __builtin_amdgcn_s_sleep(CVSP);
      CHUNK_ST(2, Uc + 32 + 1024, CUN);
      if (CVSP) __builtin_amdgcn_s_sleep(CVSP);
      CHUNK_ST(3, Uc + 32 + 1536, CUN);
    }
    if (w == 1) (void)wait_n<SLP_V, 32>(FL + FL_FZ + (blockIdx.x & 7) * 64, 64, TMO);
    DRAIN();
    if (l < (PP < 0 ? 32 : 16)) CHUNK_FLAG(0);
    SST(9);
#undef U_ROW
#undef U_VAL
#undef U_PUT
#undef ST_GET
  }
  const unsigned rb = lds_base(reinterpret_cast<char*>(((c & 1) ? Rs : Rc) + w * CRN + c + 8 * g));
#pragma unroll
  for (int j = 0; j < 8; ++j) acc[j] = f32x4{};
  const __amdgpu_buffer_rsrc_t rx = MKRS(ws + OFF_X2, SZ_X), rz = MKRS(ws + OFF_X3, SZ_X);
  const __amdgpu_buffer_rsrc_t rf = MKRS(FL + FL_FR, 64 * 16 * 128);
  v2u st = {0u, 0u}, xx = {0u, 0u};
#define X2_LOAD(ss, far)                                                                                                      \
  {                                                                                                                      \
    xx = LD64(rx, (l >> 1) * (DM * 2) + (l & 1) * 8, (far) ? (int)SZ_X : ((ss) * SEG * DM + 8 * cv) * 2);                 \
    st = LD64(rf, l * 8, ((ss) * 4 + (blockIdx.x & 3)) * 512);                                                           \
  }
  unsigned tl = now10();
  bool late = false;
  for (int s = 0; s < NSEG; ++s) {
    const int q = s & 3, q8 = s & 7;
    if (w == 1) {
      late = __builtin_popcountll(__ballot(st.y != 0u)) >= CVLATE;
      for (unsigned spins = 0; !__all(umin3(xx.x, xx.y, st.y) != 0u); ++spins) {
        if (spin_timeout(spins, TMO)) break;
        if (GF_V && s >= GF0) {
          if (spins) __builtin_amdgcn_s_sleep(SLP_V);
          else if (s < NSEG - 6) gate(tl);
          tl = now10() + GF_V;
          X2_LOAD(s, CVT2 > 64 ? 0 : spins && __builtin_popcountll(__ballot(st.y != 0u)) < CVT2);
          continue;
        }
        if (GATE_V && spins == 0 && !__any(st.y != 0u)) gate(tl);
        if (spins && !__all(st.y != 0u)) __builtin_amdgcn_s_sleep(SLP_V);
        X2_LOAD(s, __builtin_popcountll(__ballot(st.y != 0u)) < CVTHR);
      }
      if (!GF_V || s < GF0) tl = now10() + GATE_V;
      TS(0, s, 64);
      const float s0 = __uint_as_float(st.x), s1 = dppr<0xb1>(s0), m2 = __uint_as_float(st.y);
      const float var = (m2 + dppr<0xb1>(m2) + (s0 - s1) * (s0 - s1) * (1.0f / DM)) * (1.0f / DM);
      const float mb = bfr1((s0 + s1) * (1.0f / DM)), ri = 1024.0f * __builtin_amdgcn_rcpf(bfr1(__builtin_amdgcn_sqrtf(bfr1(bfr1(var) + 1e-5f))));
      const unsigned t0 = pk2(lo16(xx.x) - mb, hi16(xx.x) - mb), t1 = pk2(lo16(xx.y) - mb, hi16(xx.y) - mb);
      const unsigned b0 = pk2(lo16(t0) * ri, hi16(t0) * ri), b1 = pk2(lo16(t1) * ri, hi16(t1) * ri);
      const unsigned u0 = pkh(lo16(b0), hi16(b0)), u1 = pkh(lo16(b1), hi16(b1));
      u16* rp = Rc + (l & 1) * 4 * CRN + 32 * q + (l >> 1);
#define R_PUT(o) { rp[o] = (u16)u0; rp[(o) + CRN] = (u16)(u0 >> 16); rp[(o) + 2 * CRN] = (u16)u1; rp[(o) + 3 * CRN] = (u16)(u1 >> 16); }
      R_PUT(0) R_PUT(8 * CRN + 63)
      if (q == 0) { R_PUT(128) R_PUT(8 * CRN + 63 + 128) }
#undef R_PUT
      *reinterpret_cast<v2u*>(Sx + l * 4) = xx;
    }
    const u16* kq = ka + 32 * q8;
    const f16x8 a0 = *reinterpret_cast<const f16x8*>(kq - 32), a1 = *reinterpret_cast<const f16x8*>(kq);
    __syncthreads();
    TS(1, s, 0);
    const f16x8 bp = BFRAG(rb + 64 * ((q + 3) & 3)), bc = BFRAG(rb + 64 * q);
    const float u0 = h2f(Rc[w * CRN + 32 * q + c]), u1 = h2f(Rc[w * CRN + 32 * q + 16 + c]);
    const float x0 = bf2f(Sx[c * 8 + w]), x1 = bf2f(Sx[(16 + c) * 8 + w]);
    acc[0] = MFMAH(a0, bp, (q8 == 0 ? acc[1] : acc[0]));
    const f32x4 tmp = MFMAH(a1, bc, acc[0]);
    const unsigned uu = pk2(u0 * dsk, u1 * dsk);
    const unsigned yy = pk2(((q & 1) ? tmp[2] : tmp[0]) * ysc, ((q & 1) ? tmp[3] : tmp[1]) * ysc);
    const unsigned aa = pk2(lo16(yy) + lo16(uu), hi16(yy) + hi16(uu)), oo = pk2(x0 + lo16(aa), x1 + hi16(aa));
    if (g == (q8 >> 1)) {
      To[c * 8 + w] = (u16)oo;
      To[(16 + c) * 8 + w] = (u16)(oo >> 16);
    }
    __syncthreads();
    if (w == 0 && l < 32) {
      const v4u o = *reinterpret_cast<const v4u*>(To + l * 8);
      ST128A(o, rz, l * 16, (s * 64 + cv) * 512, XAUX);
      ST64(part_stats(o), rps, l * 8, ((64 + s) * 64 + cv) * 256);
    }
    if (w == 1) {
      if (GF_V && s + 1 >= GF0) st.y = 0u;
      else X2_LOAD(s + 1, !late);
    }
    const int left = 7 - (s >> 3);
#define PUSH(J0, SH)                                                                                              \
  if (left >= (J0)) {                                                                                             \
    _Pragma("unroll") for (int j = (J0); j < (J0) + 2 && j < 8; ++j)                                              \
      acc[j] = MFMAH(*reinterpret_cast<const f16x8*>(kq - 32 - 256 * j), bp, (j + (SH) < 8 ? acc[(j + (SH)) & 7] : f32x4{})); \
  }
    TS(2, s, 0);
    if (w == 0) {
      DRAIN();
      if (l < 4) set_flag(FL + FL_FC + (((64 + s) * 4 + l) * 8 + (cv >> 3)) * 32 + (cv & 7));
    }
    TS(3, s, 0);
    if (q8 == 0) { PUSH(1, 1) PUSH(3, 1) PUSH(5, 1) PUSH(7, 1) }
    else { PUSH(1, 0) PUSH(3, 0) PUSH(5, 0) PUSH(7, 0) }
#undef PUSH
    if (PP >= 0 && (unsigned)(s - PP) < 3u) {
      const int i = 2 + s - PP;
      if (i > 2) {
        DRAIN();
        if (l < 8) CHUNK_FLAG(i - 1);
        tx[2] = tx[3];
      }
      if (i < 4) CHUNK_STX(i, tx[2], Uc + 32 + 512 * i, CUN);
    }
    MSTAMP();
  }
#undef KFRAG
#undef BFRAG
#undef X2_LOAD
#undef TILE_OUT
#undef SKIP
#undef CHUNK_ST
#undef CHUNK_STX
#undef CHUNK_FLAG
}

__device__ __forceinline__ v4u ln_row_packed(const v4u xr) {
  float v[8];
#pragma unroll
  for (int e = 0; e < 4; ++e) { v[2 * e] = lo16(xr[e]); v[2 * e + 1] = hi16(xr[e]); }
  float s = ((v[0] + v[1]) + (v[2] + v[3])) + ((v[4] + v[5]) + (v[6] + v[7]));
  s = wave_sum(s);
  const float mean = s * (1.0f / DM);
  float q = 0.f;
#pragma unroll
  for (int j = 0; j < 8; ++j) { float t = v[j] - mean; q += t * t; }
  q = wave_sum(q);
  const float var = q * (1.0f / DM);
  const float mb = bfr(mean);
  const float sd = bfr(sqrtf(bfr(bfr(var) + 1e-5f)));
  const float ri = 1.0f / sd;
  v4u o;
#pragma unroll
  for (int e = 0; e < 4; ++e) {
    const unsigned t = pk2(v[2 * e] - mb, v[2 * e + 1] - mb);
    const float x0 = lo16(t), x1 = hi16(t);
    float q0 = x0 * ri, q1 = x1 * ri;
    q0 = __builtin_fmaf(__builtin_fmaf(-q0, sd, x0), ri, q0);
    q1 = __builtin_fmaf(__builtin_fmaf(-q1, sd, x1), ri, q1);
    o[e] = pk2(q0, q1);
  }
  return o;
}

__device__ __forceinline__ void ln4_packed(v4u (&x)[4]) {
  f32x2_t v[16];
#pragma unroll
  for (int e = 0; e < 16; ++e) v[e] = f32x2_t{lo16(x[e >> 2][e & 3]), hi16(x[e >> 2][e & 3])};
  const f32x2_t t = (((v[0] + v[1]) + (v[2] + v[3])) + ((v[4] + v[5]) + (v[6] + v[7]))) +
                    (((v[8] + v[9]) + (v[10] + v[11])) + ((v[12] + v[13]) + (v[14] + v[15])));
  const float mean = red_mod4(t.x + t.y) * (1.0f / DM);
  const float mb = bfr1(mean);
  f32x2_t q0 = {0.f, 0.f}, q1 = {0.f, 0.f};
#pragma unroll
  for (int e = 0; e < 16; e += 2) {
    v[e] -= mb;
    v[e + 1] -= mb;
    q0 += v[e] * v[e];
    q1 += v[e + 1] * v[e + 1];
  }
  q0 += q1;
  const float dm = mean - mb;
  const float var = fmaxf(red_mod4(q0.x + q0.y) * (1.0f / DM) - dm * dm, 0.f);
  const float sd = bfr1(sqrtf(bfr1(bfr1(var) + 1e-5f)));
  const float ri = 1.0f / sd;
#pragma unroll
  for (int e = 0; e < 16; ++e) {
    const unsigned t2 = pk2(v[e].x, v[e].y);
    const f32x2_t q = f32x2_t{lo16(t2), hi16(t2)} * ri;
    x[e >> 2][e & 3] = pk2(q.x, q.y);
  }
}

constexpr int MEGA_WGS = 256;
constexpr size_t MEGA_LDS = (size_t)(32 * 4 * 64 * 8 + 8 * SMS + 15 * QS + 128 + 16 * 64 * 8 + 8 * SOS + 512) * 2;
static_assert(MEGA_LDS <= 163840 && MEGA_LDS - 480 >= C_SQ + 4096, "lds");
static_assert(MEGA_LDS >= (size_t)AW_CNT + 128 && MEGA_LDS >= (size_t)P_CN + 512, "lds");
static_assert(MEGA_LDS >= (size_t)CV_LDS, "lds");
static_assert(MEGA_LDS >= (size_t)2 * 32 * SMS * 2 && MEGA_LDS >= 131072, "lds");
static_assert(MEGA_LDS >= (size_t)8 * (16640 + 2 * (704 + 144)), "lds");
__device__ __forceinline__ MegaArgs role_args(const int b, const int wv, const unsigned k0, const unsigned k1) {
  auto p = (const __attribute__((address_space(4))) u64*)__builtin_amdgcn_kernarg_segment_ptr();
  asm volatile("s_waitcnt lgkmcnt(0)" : "+s"(p) : "s"(k0), "s"(k1));
#define KA(i) ((u16*)(__attribute__((address_space(1))) u16*)p[i])
  return MegaArgs{KA(0), KA(1), (char*)KA(2), KA(3), KA(4), KA(5), KA(6), KA(7), KA(8), KA(9), KA(10), KA(11), KA(12), KA(13), b, wv};
#undef KA
}
__global__ __launch_bounds__(512, 2) void mega_kernel(const u16* mem0, const u16* Dskip, char* ws, u16* out, const u16* x0,
                                                      const u16* logAre, const u16* Aim, const u16* Cre, const u16* Cim,
                                                      const u16* logdt, const u16* Wq, const u16* Wk, const u16* Wv,
                                                      const u16* Wo) {
  extern __shared__ __attribute__((aligned(16))) char smem[];
  unsigned k0 = 0u, k1 = 0u;
  if (IPF == 3) {
    unsigned o;
    asm volatile(
        "s_load_dword %0, %3, 0\n\ts_load_dword %1, %3, 64\n\ts_getpc_b64 s[6:7]\n.LKG_PC:\n\ts_bfe_u32 s4, %4, 0x30003\n\t"
        "v_lshl_add_u32 %2, %5, 3, s4\n\tv_lshlrev_b32 %2, 7, %2\n\ts_mov_b32 m0, %6\n\ts_mov_b32 s4, .LKG_LAST-.LKG_PC\n\t"
        "s_max_i32 s4, s4, .LKG_LAS2-.LKG_PC\n\tv_cmp_gt_i32 vcc, s4, %2\n\t"
        "s_and_saveexec_b64 s[4:5], vcc\n\tglobal_load_lds_dword %2, s[6:7]\n\ts_mov_b64 exec, s[4:5]"
        : "=&s"(k0), "=&s"(k1), "=&v"(o)
        : "s"(__builtin_amdgcn_kernarg_segment_ptr()), "s"(blockIdx.x), "v"(threadIdx.x), "s"(lds_base(smem) + (unsigned)MEGA_LDS - 480)
        : "memory", "vcc", "scc", "s4", "s5", "s6", "s7");
  }
  const int b = vbid(), wv = __builtin_amdgcn_readfirstlane(threadIdx.x >> 6);
#define A role_args(b, wv, k0, k1)
  u64 tag = 0;
  if (__builtin_expect((0xde000ff0u >> (b >> 3)) & 1u, 0)) {
    const unsigned t0 = now10();
    if ((b >= 32 && b < 64) || (b >= 200 && b < 232)) start_kgen(A, smem, b < 64 ? b - 32 : b - 168);
    else if (b >= 64 && b < 96) tag = start_stats<4>(A, b - 64);
    else if (b >= 240) (void)start_stats<8>(A, b - 224);
    const unsigned dl = (b >= 32 && b < 64) ? DL_C : (b >= 64 && b < 96) ? DL_A : b >= 240 ? DL_R2 : ((b >= 96 && b < 104) || b >= 232) ? DL_R : (b >= 200 && b < 232) ? DL_Q : 0u;
    while (now10() - t0 < dl) __builtin_amdgcn_s_sleep(16);
    __builtin_amdgcn_s_waitcnt(0x0f70);
  }
  if ((EXP_OFF & 1) && b >= 64 && b < 80) return;
  if ((EXP_OFF & 8) && b >= 200 && b < 232) return;
  if ((EXP_OFF & 16) && b >= 32 && b < 64) return;
  if ((EXP_OFF & 32) && b >= 80 && b < 96) return;
  if (b < 64) role_chain(A, smem, b >> 5, b & 31, 0);
  else if (b < 96) role_aw(A, smem, (b >> 4) & 1, b & 15, tag);
  else if (b >= 104 && b < 168) role_conv(A, smem, b - 104);
  else {
    const bool kv = b >= 168 && b < 232;
    role_proj(A, smem, b >= (kv ? 200 : 240), kv ? (b - 168) & 31 : b & 15, kv);
  }
#undef A
}

extern "C" void kernel_launch(void* const* d_in, const int* in_sizes, int n_in, void* d_out, int out_size, void* d_ws,
                              size_t ws_size, hipStream_t stream) {
  MegaArgs A;
  A.x0 = (const u16*)d_in[0];
  A.logAre = (const u16*)d_in[1];
  A.Aim = (const u16*)d_in[2];
  A.Cre = (const u16*)d_in[3];
  A.Cim = (const u16*)d_in[4];
  A.logdt = (const u16*)d_in[5];
  A.Dskip = (const u16*)d_in[6];
  A.mem0 = (const u16*)d_in[8];
  A.Wq = (const u16*)d_in[9];
  A.Wk = (const u16*)d_in[10];
  A.Wv = (const u16*)d_in[11];
  A.Wo = (const u16*)d_in[12];
  A.ws = (char*)d_ws;
  A.out = (u16*)d_out;

  (void)hipFuncSetAttribute((const void*)mega_kernel, hipFuncAttributeMaxDynamicSharedMemorySize, (int)MEGA_LDS);
  mega_kernel<<<MEGA_WGS, 512, MEGA_LDS, stream>>>(A.mem0, A.Dskip, A.ws, A.out, A.x0, A.logAre, A.Aim, A.Cre, A.Cim, A.logdt,
                                                   A.Wq, A.Wk, A.Wv, A.Wo);
}
```

```cpp
#define MEGA_STAMPS
#define NO_TS
#include <hip/hip_runtime.h>
#include <hip/hip_bf16.h>
#include <stdint.h>

constexpr int L_SEQ = 2048;
constexpr int DM    = 512;
constexpr int NST   = 128;
constexpr int NH    = 32;
constexpr int DH    = 128;
constexpr int HD    = NH * DH;
constexpr int MM    = 8;
constexpr int SEG   = 32;
constexpr int NSEG  = L_SEQ / SEG;
constexpr int LK    = MM + SEG;

typedef unsigned short u16;
using bf16x8 = __attribute__((ext_vector_type(8))) short;
using f32x4  = __attribute__((ext_vector_type(4))) float;
using f32x16 = __attribute__((ext_vector_type(16))) float;

static __device__ __forceinline__ u16 f2bf(float f) {
  unsigned u = __float_as_uint(f);
  u += 0x7fffu + ((u >> 16) & 1u);
  return (u16)(u >> 16);
}
static __device__ __forceinline__ float bf2f(u16 h) { return __uint_as_float(((unsigned)h) << 16); }
static __device__ __forceinline__ float bfr(float f) { return bf2f(f2bf(f)); }

constexpr size_t SZ_X      = (size_t)L_SEQ * DM * 2;
constexpr size_t OFF_KF    = 0;
constexpr size_t OFF_UT    = OFF_KF + (size_t)DM * L_SEQ * 4;
constexpr size_t OFF_YT    = OFF_UT + SZ_X;
constexpr size_t OFF_XA    = OFF_YT + SZ_X;
constexpr size_t OFF_H     = OFF_XA + SZ_X;
constexpr size_t OFF_X2    = OFF_H + SZ_X;
constexpr size_t OFF_U2    = OFF_X2 + SZ_X;
constexpr size_t OFF_X3    = OFF_U2 + SZ_X;
constexpr size_t SZ_QK     = (size_t)L_SEQ * 2 * HD * 2;
constexpr size_t OFF_QK    = OFF_X3 + SZ_X;
constexpr size_t SZ_O      = (size_t)L_SEQ * HD * 2;
constexpr size_t OFF_VF    = OFF_QK + 2 * SZ_QK;
constexpr size_t SZ_PW     = (size_t)2 * NSEG * 16 * SEG * DM * 2;
constexpr size_t OFF_PW    = OFF_VF + 2 * SZ_O;
constexpr size_t SZ_MEM    = (size_t)NSEG * MM * HD * 2;
constexpr size_t OFF_MEMK  = OFF_PW + SZ_PW;
constexpr size_t OFF_MEMV  = OFF_MEMK + 2 * SZ_MEM;
constexpr size_t OFF_GT    = OFF_MEMV + 2 * SZ_MEM;
constexpr size_t OFF_EP    = OFF_GT + 65536;
constexpr size_t OFF_RD    = OFF_EP + 4096;
constexpr size_t OFF_KG    = OFF_RD + 4096;
constexpr size_t OFF_G     = OFF_KG + 8192;
constexpr size_t SZ_ACC    = (size_t)2 * 65536;
constexpr size_t OFF_Z     = OFF_GT + (size_t)128 * DM * 4;
constexpr size_t OFF_ACC   = OFF_Z;
constexpr size_t OFF_FL    = OFF_ACC + 2 * SZ_ACC;
constexpr int FL_FK  = 0;
constexpr int FL_FW  = FL_FK + 64 * 8 * 32;
constexpr int FL_FR  = FL_FW + 2 * 64 * 16 * 32;
constexpr int FL_FC  = FL_FR + 64 * 16 * 32;
constexpr int FL_FQ  = FL_FC + 2 * 64 * 32 * 32;
constexpr int FL_FP  = FL_FQ + 2 * 64 * 32 * 32;
constexpr int FL_FZ  = FL_FP + 64 * 32 * 32;
constexpr int FL_TMO = FL_FZ + 16 * 32;
constexpr int FL_END = FL_TMO + 64;
constexpr size_t Z_BYTES   = OFF_FL + (size_t)FL_END * 4 - OFF_Z;
constexpr size_t WS_TOTAL  = OFF_Z + Z_BYTES;
static_assert(Z_BYTES % 16 == 0 && OFF_Z % 256 == 0 && Z_BYTES <= 4 * 64 * 8192 && OFF_G + 2048 <= OFF_Z, "zero region");
static_assert(WS_TOTAL + (4 << 20) <= (size_t)256 * 1024 * 1024, "workspace too large");

static_assert(WS_TOTAL - OFF_XA <= (size_t)384 * 8 * 65536, "translation warm-up");
constexpr int CKN = 2336;
static_assert((size_t)DM * 2 * CKN * 2 <= (size_t)DM * L_SEQ * 4 + SZ_X, "K buffer");
__device__ __forceinline__ float pos_of_index(int p) {
  if (p < 256) return (float)p;
  if (p < 384) return (float)(256 + 2 * (p - 256));
  if (p < 512) return (float)(512 + 4 * (p - 384));
  if (p < 640) return (float)(1024 + 8 * (p - 512));
  return 2048.0f;
}
__device__ __forceinline__ int index_of_l(int l) {
  float pf = bfr((float)l);
  int p = (int)pf;
  if (p < 256) return p;
  if (p < 512) return 256 + ((p - 256) >> 1);
  if (p < 1024) return 384 + ((p - 512) >> 2);
  if (p < 2048) return 512 + ((p - 1024) >> 3);
  return 640;
}

template <int CTRL> __device__ __forceinline__ float dppr(float x) {
  return __builtin_bit_cast(float, __builtin_amdgcn_update_dpp(0, __builtin_bit_cast(int, x), CTRL, 0xf, 0xf, false));
}
__device__ __forceinline__ float wave_sum(float v) {
  v += dppr<0x121>(v);
  v += dppr<0x122>(v);
  v += dppr<0x124>(v);
  v += dppr<0x128>(v);
  {
    auto r = __builtin_amdgcn_permlane16_swap(__float_as_uint(v), __float_as_uint(v), false, false);
    v = __uint_as_float(r[0]) + __uint_as_float(r[1]);
  }
  {
    auto r = __builtin_amdgcn_permlane32_swap(__float_as_uint(v), __float_as_uint(v), false, false);
    v = __uint_as_float(r[0]) + __uint_as_float(r[1]);
  }
  return v;
}
__device__ __forceinline__ void ln_row8(float (&v)[8]) {
  float s = 0.f;
#pragma unroll
  for (int j = 0; j < 8; ++j) s += v[j];
  s = wave_sum(s);
  const float mean = s * (1.0f / DM);
  float q = 0.f;
#pragma unroll
  for (int j = 0; j < 8; ++j) { float t = v[j] - mean; q += t * t; }
  q = wave_sum(q);
  const float var = q * (1.0f / DM);
  const float mb = bfr(mean);
  const float sd = bfr(sqrtf(bfr(bfr(var) + 1e-5f)));
#pragma unroll
  for (int j = 0; j < 8; ++j) v[j] = bfr(bfr(v[j] - mb) / sd);
}

#define RLX_AGENT __ATOMIC_RELAXED, __HIP_MEMORY_SCOPE_AGENT
typedef __attribute__((address_space(1))) unsigned gu32;
typedef unsigned v4u __attribute__((ext_vector_type(4)));
constexpr int AUX_SC1 = 16;
constexpr unsigned SPIN_CAP = 1u << 18;

__device__ __forceinline__ bool wait_flags(const unsigned* flags, int lane, int nfl, unsigned want, unsigned* tmo) {
  for (unsigned spins = 0;; ++spins) {
    unsigned v = want;
    if (lane < nfl) v = __hip_atomic_load((gu32*)flags + lane * 32, RLX_AGENT);
    if (__all(v >= want)) break;
    if (spins > SPIN_CAP) {
      if (lane == 0) __hip_atomic_store((gu32*)tmo, 1u, RLX_AGENT);
      return false;
    }
  }
  __builtin_amdgcn_fence(__ATOMIC_ACQUIRE, "wavefront");
  return true;
}

#define STAMP(i)
#define STAMP_DUMP()
#define WSTAMP(i)
template <int CTRL> __device__ __forceinline__ float dppf(float old, float x) {
  return __builtin_bit_cast(float, __builtin_amdgcn_update_dpp(__builtin_bit_cast(int, old), __builtin_bit_cast(int, x), CTRL, 0xf, 0xf, false));
}
__device__ __forceinline__ float wave_max_dpp(float x) {
  const float ninf = -3e38f;
  x = fmaxf(x, dppf<0x111>(ninf, x));
  x = fmaxf(x, dppf<0x112>(ninf, x));
  x = fmaxf(x, dppf<0x114>(ninf, x));
  x = fmaxf(x, dppf<0x118>(ninf, x));
  x = fmaxf(x, dppf<0x142>(ninf, x));
  x = fmaxf(x, dppf<0x143>(ninf, x));
  return __builtin_bit_cast(float, __builtin_amdgcn_readlane(__builtin_bit_cast(int, x), 63));
}
__device__ __forceinline__ float wave_sum_dpp(float x) {
  x += dppf<0x111>(0.f, x);
  x += dppf<0x112>(0.f, x);
  x += dppf<0x114>(0.f, x);
  x += dppf<0x118>(0.f, x);
  x += dppf<0x142>(0.f, x);
  x += dppf<0x143>(0.f, x);
  return __builtin_bit_cast(float, __builtin_amdgcn_readlane(__builtin_bit_cast(int, x), 63));
}
constexpr int SMS = 528;
constexpr int QS = 144;
constexpr int VS = 80;
constexpr int SOS = 144;
#define VAR_NREP 4
constexpr int NREP = VAR_NREP;
__device__ __forceinline__ float xsum16(float x) {
  auto r = __builtin_amdgcn_permlane16_swap(__float_as_uint(x), __float_as_uint(x), false, false);
  return __uint_as_float(r[0]) + __uint_as_float(r[1]);
}
__device__ __forceinline__ float xsum32(float x) {
  auto r = __builtin_amdgcn_permlane32_swap(__float_as_uint(x), __float_as_uint(x), false, false);
  return __uint_as_float(r[0]) + __uint_as_float(r[1]);
}
__device__ __forceinline__ float xmax16(float x) {
  auto r = __builtin_amdgcn_permlane16_swap(__float_as_uint(x), __float_as_uint(x), false, false);
  return fmaxf(__uint_as_float(r[0]), __uint_as_float(r[1]));
}
__device__ __forceinline__ float xmax32(float x) {
  auto r = __builtin_amdgcn_permlane32_swap(__float_as_uint(x), __float_as_uint(x), false, false);
  return fmaxf(__uint_as_float(r[0]), __uint_as_float(r[1]));
}
__device__ __forceinline__ float red_mod4(float x) {
  x += dppf<0x124>(0.f, x);
  x += dppf<0x128>(0.f, x);
  return xsum32(xsum16(x));
}
typedef __bf16 bf16x2_t __attribute__((ext_vector_type(2)));
typedef float f32x2_t __attribute__((ext_vector_type(2)));
__device__ __forceinline__ unsigned pk2(float a, float b) {
  f32x2_t v = {a, b};
  bf16x2_t r = __builtin_convertvector(v, bf16x2_t);
  return __builtin_bit_cast(unsigned, r);
}
typedef _Float16 f16x2_t __attribute__((ext_vector_type(2)));
typedef _Float16 f16x8 __attribute__((ext_vector_type(8)));
__device__ __forceinline__ unsigned pkh(float a, float b) {
  f32x2_t v = {a, b};
  return __builtin_bit_cast(unsigned, __builtin_convertvector(v, f16x2_t));
}
__device__ __forceinline__ float h2f(u16 h) { return (float)__builtin_bit_cast(_Float16, h); }
__device__ __forceinline__ float one(float x) {
  asm("" : "+v"(x));
  return x;
}
__device__ __forceinline__ unsigned pk2s(float a, float b) { return pk2(one(a), one(b)); }
__device__ __forceinline__ float dot2sq(const unsigned x, const float acc) {
  return __builtin_amdgcn_fdot2_f32_bf16(__builtin_bit_cast(bf16x2_t, x), __builtin_bit_cast(bf16x2_t, x), acc, false);
}
__device__ __forceinline__ float lo16(unsigned u) { return __uint_as_float(u << 16); }
__device__ __forceinline__ float hi16(unsigned u) { return __uint_as_float(u & 0xffff0000u); }
__device__ __forceinline__ float bfr1(float x) { return lo16(pk2(x, x)); }
__device__ __forceinline__ float half_sum(float v) {
  v += dppr<0x121>(v);
  v += dppr<0x122>(v);
  v += dppr<0x124>(v);
  v += dppr<0x128>(v);
  return xsum16(v);
}
__device__ __forceinline__ void scale_pair(float a, float b, float scale, float& oa, float& ob) {
  unsigned u = pk2(a, b);
  unsigned v = pk2s(lo16(u) * scale, hi16(u) * scale);
  oa = lo16(v);
  ob = hi16(v);
}

#define XMAP 2
__device__ __forceinline__ int vbid() {
  const int b = blockIdx.x, x = b & 7, t = b >> 3;
  if (XMAP == 0) {
    if (b < 64) return (b & 32) + 4 * x + (t & 3);
    if (b < 96) return (b & ~15) + 2 * x + (t & 1);
    if (b >= 104 && b < 168) return 104 + 8 * x + t - 13;
    if (b >= 168 && b < 232) return 168 + ((b - 168) & 32) + 4 * x + ((t - 21) & 3);
    return b;
  }
  if (XMAP == 3) {
    const int y = x & 3;
    if (t >= 16 && t < 24) return 104 + 16 * y + 2 * (x & 4) + t - 16;
    if (x < 4) return t < 16 ? 4 * (t & 8) + 8 * x + (t & 7) : 64 + 4 * (t & 4) + 4 * y + (t & 3);
    if (t >= 24) return (t >= 28 ? 240 : y < 2 ? 96 : 224) + 4 * y + (t & 3);
    return 168 + 4 * (t & 8) + 8 * y + (t & 7);
  }
  if (XMAP == 4) {
    const int y = x & 3;
    if (x < 4) return t < 16 ? 4 * (t & 8) + 8 * x + (t & 7) : t < 28 ? 104 + 16 * y + t - 16 : 64 + 4 * y + (t & 3);
    if (t >= 24) return (t >= 28 ? 240 : y < 2 ? 96 : 224) + 4 * y + (t & 3);
    if (t < 16) return 168 + 4 * (t & 8) + 8 * y + (t & 7);
    return t < 20 ? 104 + 16 * y + t - 4 : 80 + 4 * y + (t & 3);
  }
  if (XMAP == 2 || XMAP >= 5) {
    if (XMAP == 5 && x < 4 && t < 16) return 16 * x + t;
    if (XMAP == 6 && x < 4 && t < 16) return 32 * (x & 1) + 8 * (x & 2) + t;
    if (XMAP == 7 && x < 4) return (x & 1) ? 104 + 16 * (x & 2) + t : 16 * x + t;
    if (x < 4) return t < 16 ? 4 * (t & 8) + 8 * x + (t & 7) : 104 + 16 * x + t - 16;
    const int y = x - 4;
    if (t >= 24) return (t >= 28 ? 240 : y < 2 ? 96 : 224) + 4 * y + (t & 3);
    if (t < 16) return 168 + 4 * (t & 8) + 8 * y + (t & 7);
    return 64 + 4 * (t & 4) + 4 * y + (t & 3);
  }
  if (x < 2) return 2 * (t & 16) + 16 * x + (t & 15);
  const int i = 16 * (x - 2) + (t & 15);
  if (t < 16) {
    const int k = (i * 171) >> 9, r = i - 3 * k;
    return r == 0 ? 168 + k : r == 1 ? 200 + k : 64 + 16 * (k & 1) + (k >> 1);
  }
  const int g = i >> 3, e = i & 7;
  if (g == 3) return 96 + e;
  if (g == 7) return 232 + e;
  if (g == 9) return 240 + e;
  if (g == 11) return 248 + e;
  return 104 + 8 * (g - (g > 3) - (g > 7) - (g > 9)) + e;
}
#define MSTAMP() do { if (A.wv == 0 && lane_id() == 0) { ((unsigned long long*)(A.ws + WS_TOTAL))[A.vb * 64 + s] = __builtin_amdgcn_s_memrealtime(); } } while (0)
#define WSTAMP2() do { DRAIN(); __syncthreads(); if (A.wv == 0 && lane_id() == 0) ((unsigned long long*)(A.ws + WS_TOTAL))[520 * 64 + A.vb] = __builtin_amdgcn_s_memrealtime(); } while (0)
#define TS(k, ss, t)
#define SSW(k, v) do { if (A.wv == 4 && lane_id() == 0) ((unsigned long long*)(A.ws + WS_TOTAL))[(530 + (k)) * 256 + A.vb] = (v); } while (0)
#define SSX(k, c) do { if ((c) && lane_id() == 0) ((unsigned long long*)(A.ws + WS_TOTAL))[(530 + (k)) * 256 + A.vb] = __builtin_amdgcn_s_memrealtime(); } while (0)
#define SST(k) do { if (A.wv == 0 && lane_id() == 0) ((unsigned long long*)(A.ws + WS_TOTAL))[(530 + (k)) * 256 + A.vb] = __builtin_amdgcn_s_memrealtime(); } while (0)
#define LS(k, ss)
#define LS_DUMP()
struct MegaArgs {
  const u16* mem0;
  const u16* Dskip;
  char* ws;
  u16* out;
  const u16* x0;
  const u16 *logAre, *Aim, *Cre, *Cim, *logdt;
  const u16 *Wq, *Wk, *Wv, *Wo;
  int vb, wv;
};

__device__ __forceinline__ int lane_id() {
  int l;
  asm volatile("v_mbcnt_lo_u32_b32 %0, -1, 0\n\tv_mbcnt_hi_u32_b32 %0, -1, %0" : "=v"(l));
  __builtin_assume(l >= 0 && l < 64);
  return l;
}
__device__ __forceinline__ bool spin_timeout(unsigned spins, unsigned* tmo) {
  if (spins > SPIN_CAP) {
    if (lane_id() == 0) __hip_atomic_store((gu32*)tmo, 1u, RLX_AGENT);
    return true;
  }
  return false;
}
#define ROT_H (4 * (h & 1))
#define ROT_T (h & 2)
#define C2G 420
#define CVSP 40
#define PFA 0
#define PFB 0
#define WO1 1
#define AWH 0
#define PEEK0 1
#define SLP0 2
#define GF_R 400
#define GF_V 0
#define PP 0
#define EL 1
#define IPF 1
#define XL 0
#define MC3 2
#define XZD 300
#define XZP 4
#define NFW 1
#define IPS 128u
#define WNT 17
#define OAUX 16
#define XAUX 16
#define TP 1
#define TPS (4 * TP + 4)
#define MCE 1
#define H4S 0
#define GF_H 150
#define CVT2 48
#define GF_P 0
#define GF0 8
#define SLP_H 12
#define HX0 6
#define CVSTEP 2
#define CVLATE 64
#define PACE 203
#define PACE0 6
#define RPRIO 3
#define DL_R2 1400
#define PNL 8
#define GSL 12
#define AWDIR 1
#define AWAT 0
#define AWRP 0
#define PEPS 4
#define FGATE 0
#define DL_C 0
#define DL_A 400
#define DL_R 0
#define DL_Q 0
#define KC0 0
#define CPRIO 1
#define P1PRIO 3
#define QLEAD 2
#define POLL_SLEEP 12
#define SLP_C POLL_SLEEP
#define SLP_A POLL_SLEEP
#define SLP_W POLL_SLEEP
#define SLP_R POLL_SLEEP
#define SLP_V POLL_SLEEP
#define SLP_Q POLL_SLEEP
#define SLP_M 4
#define AWPRIO 2
#define SLP_S 8
#define SPOS 4
#define CVPRE 12
#define QPD 3
#define QPRI 2
#define TLBW 3
#define QHV 0
#define QCHK 0
#define LEADN 99
#define GATE_V 170
#define GATE_R 380
#define GATE_N 380
#define AWAHEAD 1
#define GATE_M 150
#define PRI_A 3
#define PRI_WA 2
#define PRI_WB 1
#define AWSKIP 3
#define PRI_0 3
#define AWDEF 0
#define SLP_F 2
#define EXP_OFF 0
#define AWSPEC 1
#define CVTHR 48
#define NR_C 8
#define NR_R 8
#define NR_W 8
#define NR_N 2
#define QAHEAD 0
#define NS0 4
#define SLP_N POLL_SLEEP
template <int SLP, int WPL>
__device__ __forceinline__ bool wait_n(const unsigned* f, int n, unsigned* tmo) {
  const int l = lane_id();
  for (unsigned spins = 0;; ++spins) {
    unsigned v = 1u;
    if (l < n) v = __hip_atomic_load((gu32*)f + (l / WPL) * 32 + l % WPL, RLX_AGENT);
    if (__all(v != 0u)) break;
    if (spin_timeout(spins, tmo)) return false;
    __builtin_amdgcn_s_sleep(SLP);
  }
  asm volatile("" ::: "memory");
  return true;
}
__device__ __forceinline__ unsigned peek(const unsigned* base, const unsigned off) {
  unsigned r;
  asm volatile("global_atomic_or %0, %1, %2, %3 sc0\n\ts_waitcnt vmcnt(0)" : "=&v"(r) : "v"(off), "v"(0u), "s"(base) : "memory");
  return r;
}
__device__ __forceinline__ unsigned now10() { return (unsigned)__builtin_amdgcn_s_memrealtime(); }
__device__ __forceinline__ void gate(const unsigned t) {
#pragma unroll 1
  for (int i = 0; i < 64; ++i) {
    const int d = (int)(t - now10());
    if (d <= 0) break;
    if (d > 100) __builtin_amdgcn_s_sleep(32);
    else if (d > 25) __builtin_amdgcn_s_sleep(8);
    else __builtin_amdgcn_s_sleep(2);
  }
}
template <int SLP, int WPL, int GT>
__device__ __forceinline__ void grp_wait(const bool leader, volatile unsigned* sw, const unsigned val, const unsigned* f, int n,
                                         unsigned* tmo, unsigned& tl) {
  if (leader) {
    const int l = lane_id();
    for (unsigned spins = 0;; ++spins) {
      unsigned v = 1u;
      if (l < n) v = __hip_atomic_load((gu32*)f + (l / WPL) * 32 + l % WPL, RLX_AGENT);
      if (__all(v != 0u) || spin_timeout(spins, tmo)) break;
      if (GT && spins == 0) gate(tl);
      else __builtin_amdgcn_s_sleep(SLP);
    }
    if (GT) tl = now10() + GT;
    if (sw && l == 0) *sw = val;
  } else {
    for (unsigned spins = 0; *sw < val; ++spins) {
      if (spins > 64u * SPIN_CAP) break;
      if (FGATE && GT && spins == 0) gate(tl);
      __builtin_amdgcn_s_sleep(SLP_F);
    }
    if (FGATE && GT) tl = now10() + GT;
  }
  asm volatile("" ::: "memory");
}
template <int SLP, int WPL>
__device__ __forceinline__ bool wg_wait(const unsigned* f, int n, unsigned* tmo) {
  if (threadIdx.x < 64) (void)wait_n<SLP, WPL>(f, n, tmo);
  __syncthreads();
  asm volatile("" ::: "memory");
  return true;
}
__device__ __forceinline__ void set_flag(unsigned* f) { __hip_atomic_store((gu32*)f, 1u, RLX_AGENT); }
#define DRAIN() asm volatile("s_waitcnt vmcnt(0)" ::: "memory")
#define MKRS(ptr, bytes) __builtin_amdgcn_make_buffer_rsrc((void*)(ptr), 0, (int)(bytes), 0x00020000)
typedef unsigned v2u __attribute__((ext_vector_type(2)));
#define LD128(rs, voff, soff) __builtin_amdgcn_raw_buffer_load_b128(rs, voff, soff, AUX_SC1)
#define LD64(rs, voff, soff) __builtin_amdgcn_raw_buffer_load_b64(rs, voff, soff, AUX_SC1)
#define ST128(v, rs, voff, soff) ST128A(v, rs, voff, soff, AUX_SC1)
#define ST128A(v, rs, voff, soff, aux)                                        \
  do {                                                                        \
    const v4u st_d_ = (v);                                                    \
    __builtin_amdgcn_raw_buffer_store_b128(st_d_, rs, voff, soff, aux);       \
    asm volatile("s_nop 1" ::"v"(st_d_) : "memory");                          \
  } while (0)
#define ST64(v, rs, voff, soff) __builtin_amdgcn_raw_buffer_store_b64(v, rs, voff, soff, AUX_SC1)
#define BF8(x) __builtin_bit_cast(bf16x8, x)
#define MFMA16(a, b, c) __builtin_amdgcn_mfma_f32_16x16x32_bf16(a, b, c, 0, 0, 0)
typedef short bf16x4 __attribute__((ext_vector_type(4)));
#define MFMA16K(a, b, c) \
  __builtin_amdgcn_mfma_f32_16x16x16bf16_1k(__builtin_bit_cast(bf16x4, a), __builtin_bit_cast(bf16x4, b), c, 0, 0, 0)
__device__ __forceinline__ float fmax_nn(float a, float b) { return __builtin_elementwise_maximum(a, b); }
typedef __bf16 bfv8 __attribute__((ext_vector_type(8)));
typedef __bf16 bfv16 __attribute__((ext_vector_type(16)));
typedef unsigned v8u __attribute__((ext_vector_type(8)));
#define SMF(a, b, c, idx) \
  __builtin_amdgcn_smfmac_f32_16x16x64_bf16(__builtin_bit_cast(bfv8, a), __builtin_bit_cast(bfv16, b), c, idx, 0, 0)
__device__ __forceinline__ float swap_add32(float a, float b) {
  auto r = __builtin_amdgcn_permlane32_swap(__float_as_uint(a), __float_as_uint(b), false, false);
  return __uint_as_float(r[0]) + __uint_as_float(r[1]);
}
__device__ __forceinline__ float swap_add16(float a, float b) {
  auto r = __builtin_amdgcn_permlane16_swap(__float_as_uint(a), __float_as_uint(b), false, false);
  return __uint_as_float(r[0]) + __uint_as_float(r[1]);
}
__device__ __forceinline__ v4u smf_fix(const v4u d) {
  auto x = __builtin_amdgcn_permlane16_swap(d.x, d.z, false, false);
  auto y = __builtin_amdgcn_permlane16_swap(d.y, d.w, false, false);
  return v4u{x[0], x[1], y[0], y[1]};
}
__device__ __forceinline__ v8u smf_bfrag(const u16* p) {
  const v2u a = *reinterpret_cast<const v2u*>(p), b = *reinterpret_cast<const v2u*>(p + 8);
  const v2u c = *reinterpret_cast<const v2u*>(p + 32), d = *reinterpret_cast<const v2u*>(p + 40);
  return v8u{a.x, b.x, a.y, b.y, c.x, d.x, c.y, d.y};
}

#define VAR_PPA 0
#define VAR_PPB 0
#define VAR_THR 0
#define VAR_THR2 0
#define CV_LEAD 100
#define VAR_EARLY 1
#define VAR_DRAIN 1
#define VAR_D1 0
#define VAR_D2 0
#define VAR_DR2 1
#define VAR_S1 0
#define VAR_S2 0
typedef __attribute__((address_space(1))) unsigned long long gu64;
typedef __attribute__((address_space(1))) char gch;
__device__ __forceinline__ int cvt_rpi(float x) {
  int r;
  asm("v_cvt_rpi_i32_f32 %0, %1" : "=v"(r) : "v"(x));
  return r;
}
typedef unsigned long long u64;
__device__ __forceinline__ u64 call_tag(const MegaArgs& A) {
  u64 t = *(const __attribute__((address_space(4))) u64*)(A.ws + OFF_EP) * 6364136223846793005ull + 1442695040888963407ull;
  asm volatile("" : "+s"(t)::"memory");
  return t;
}
__device__ __forceinline__ u64 tagged(const u64 tag, const unsigned v) { return (tag & 0xffffffffffffull) | (u64)(v >> 16) << 48; }
__device__ __forceinline__ bool has_tag(const u64 tag, const unsigned lo, const unsigned hi) {
  return lo == (unsigned)tag && (hi & 0xffffu) == ((unsigned)(tag >> 32) & 0xffffu);
}
__device__ __forceinline__ u64 ready_look(const MegaArgs& A) {
  return __hip_atomic_load((gu64*)(A.ws + OFF_RD) + (blockIdx.x & 7) * 64 + lane_id(), RLX_AGENT);
}
#define RD_ASK(v) \
  asm volatile("global_load_dwordx2 %0, %1, %2 sc1" : "=v"(v) : "v"(lane_id() * 8), "s"(A.ws + OFF_RD + (blockIdx.x & 7) * 512) : "memory")
__device__ __forceinline__ void ready_wait(const MegaArgs& A, const u64 tag, u64 v) {
  for (unsigned spins = 0; !__all(v == tag) && spins < SPIN_CAP; ++spins) {
    __builtin_amdgcn_s_sleep(8);
    v = ready_look(A);
  }
  asm volatile("" ::: "memory");
}
typedef short v4s __attribute__((ext_vector_type(4)));
typedef __attribute__((address_space(3))) v4s* lp4s;
__device__ __forceinline__ void glds16(const void* sbase, const unsigned voff, const unsigned lds, const bool nt = false) {
  unsigned keep;
#define GLDS16(M)                                                                                                                     \
  asm volatile("s_nop 4\n\ts_mov_b32 %0, m0\n\ts_mov_b32 m0, %3\n\ts_nop 0\n\tglobal_load_lds_dwordx4 %1, %2" M "\n\ts_mov_b32 m0, %0" \
               : "=&s"(keep)                                                                                                          \
               : "v"(voff), "s"(sbase), "s"(lds)                                                                                      \
               : "memory")
  if (nt) GLDS16(" nt");
  else GLDS16("");
#undef GLDS16
}
__device__ __forceinline__ void glds16c(const void* sbase, const unsigned voff, const unsigned lds) {
  unsigned keep;
  asm volatile("s_nop 4\n\ts_mov_b32 %0, m0\n\ts_mov_b32 m0, %3\n\ts_nop 0\n\tglobal_load_lds_dwordx4 %1, %2 sc1\n\ts_mov_b32 m0, %0"
               : "=&s"(keep)
               : "v"(voff), "s"(sbase), "s"(lds)
               : "memory");
}
__device__ __forceinline__ void glds64c(const void* sbase, const unsigned voff, const unsigned lds) {
  unsigned keep;
  asm volatile("s_nop 4\n\ts_mov_b32 %0, m0\n\ts_mov_b32 m0, %3\n\ts_nop 0\n\tglobal_load_lds_dwordx4 %1, %2 sc1\n\t"
               "global_load_lds_dwordx4 %1, %2 offset:1024 sc1\n\tglobal_load_lds_dwordx4 %1, %2 offset:2048 sc1\n\t"
               "global_load_lds_dwordx4 %1, %2 offset:3072 sc1\n\ts_mov_b32 m0, %0"
               : "=&s"(keep)
               : "v"(voff), "s"(sbase), "s"(lds)
               : "memory");
}
__device__ __forceinline__ v2u trd(const unsigned a) {
  return __builtin_bit_cast(v2u, __builtin_amdgcn_ds_read_tr16_b64_v4i16((lp4s)(unsigned long)a));
}
__device__ __forceinline__ unsigned lds_base(char* smem) { return (unsigned)(unsigned long)(__attribute__((address_space(3))) char*)smem; }
#define RAW_BAR()                                           \
  do {                                                      \
    asm volatile("s_waitcnt lgkmcnt(0)" ::: "memory");      \
    __builtin_amdgcn_s_barrier();                           \
    asm volatile("" ::: "memory");                          \
  } while (0)
#define WAIT_VM(n) asm volatile("s_waitcnt vmcnt(" #n ")" ::: "memory")
template <bool SP>
__device__ __forceinline__ void wqkv_issue(const u16* Wm, const int hf, const int h, const int w, const int l, const unsigned sb, const bool nt) {
  const char* base = (const char*)Wm + (size_t)(256 * hf + 32 * w) * (HD * 2) + h * (DH * 2);
#define WQKV(NT_)                                                                                                       \
  _Pragma("unroll") for (int i = 0; i < 8; ++i) {                                                                      \
    const int r = l >> 4;                                                                                              \
    const int f = SP ? ((r & 1) | ((i & 1) << 1) | (((i >> 1) & 1) << 2)) : (r | (((i >> 1) & 1) << 2));               \
    glds16(base, (unsigned)((4 * i + r) * (HD * 2) + (((l & 15) ^ (f << 1)) << 4)),                                    \
           sb + (w >> 2) * 65536 + hf * 32768 + (w & 3) * 8192 + i * 1024, NT_);                                       \
  }
  if (nt) { WQKV(true) } else { WQKV(false) }
#undef WQKV
}

__device__ __forceinline__ unsigned row_stats(const v4u raw) {
  float v[8];
#pragma unroll
  for (int e = 0; e < 4; ++e) { v[2 * e] = lo16(raw[e]); v[2 * e + 1] = hi16(raw[e]); }
  float s = 0.f;
#pragma unroll
  for (int e = 0; e < 8; ++e) s += v[e];
  const float mean = wave_sum(s) * (1.0f / DM);
  float q = 0.f;
#pragma unroll
  for (int e = 0; e < 8; ++e) { const float t = v[e] - mean; q += t * t; }
  const float var = wave_sum(q) * (1.0f / DM);
  const float sd = bfr(sqrtf(bfr(bfr(var) + 1e-5f)));
  return (unsigned)f2bf(mean) | ((unsigned)f2bf(sd) << 16);
}
__device__ __forceinline__ void ask_slow(const MegaArgs& A, const int k, unsigned (&t)[3]) {
  const int l = lane_id(), x = blockIdx.x & 7;
  const char *wq = (const char*)A.Wq, *wv = (const char*)A.Wv;
  asm("" : "+s"(wq), "+s"(wv));
#pragma unroll
  for (int m = 0; m < 3; ++m) {
    const char* W = m == 1 ? (const char*)A.Wk : ((m == 0) == (x < 4)) ? wv : wq;
    const int a = (int)((unsigned long)W >> 8) + 8 * (x & 3), h = k < 32 ? ((1 - a) & 3) + 4 * (k & 1) : (31 - a) & 31;
    const int r = k < 32 ? k >> 1 : k - 32;
    t[m] = __builtin_amdgcn_raw_buffer_load_b32(MKRS(W, (unsigned)k < 48u && h < 8 ? DM * HD * 2 : 0), (l >> 1) * (HD * 2) + (l & 1) * 128,
                                                (32 * (r & 15)) * (HD * 2) + (8 * (x & 3) + (h & 7)) * 256, 0);
  }
}
#define ASKED(t) asm volatile("" ::"v"(t[0]), "v"(t[1]), "v"(t[2]))
template <int N>
__device__ __forceinline__ u64 start_stats(const MegaArgs& A, const int idx) {
  const int w = A.wv, l = lane_id();
  const int r0 = N * (8 * idx + w);
  unsigned ta[3], tb[3];
  v4u raw[N];
#pragma unroll
  for (int i = 0; i < N; ++i) raw[i] = *reinterpret_cast<const v4u*>(A.x0 + (size_t)(r0 + i) * DM + l * 8);
  asm volatile("" ::: "memory");
  ask_slow(A, 16 * w + (idx & 3) + (idx >> 4) * N, ta);
  if (N > 4) ask_slow(A, 16 * w + (idx & 3) + 12, tb);
  unsigned tc[2] = {};
  if (N == 4 && AWH > 0) tc[0] = *reinterpret_cast<const unsigned*>(A.Wo + (size_t)idx * 65536 + 32768 + (64 * w + l) * 64);
  if (N == 4 && AWH > 1 && w >= 4) tc[1] = *reinterpret_cast<const unsigned*>(A.Wo + (size_t)idx * 65536 + (64 * w + l) * 64);
  const u64 tag = call_tag(A);
  unsigned stw = 0u;
#pragma unroll
  for (int i = 0; i < N; ++i) {
    const unsigned t = row_stats(raw[i]);
    if ((l >> 1) == i) stw = t;
  }
  if (l < 2 * N) __hip_atomic_store((gu64*)(A.ws + OFF_GT) + 2 * r0 + l, tagged(tag, (l & 1) ? stw : stw << 16), RLX_AGENT);
  SST(0);
  ASKED(ta);
  if (N > 4) ASKED(tb);
  asm volatile("" ::"v"(tc[0]), "v"(tc[1]));
  return tag;
}
#define MFMAH(a, b, c) __builtin_amdgcn_mfma_f32_16x16x32_f16(a, b, c, 0, 0, 0)
struct Cx {
  f32x2_t r, i;
};
__device__ __forceinline__ Cx cmul(const Cx a, const Cx b) { return Cx{a.r * b.r - a.i * b.i, a.r * b.i + a.i * b.r}; }
struct KGen {
  unsigned pa, pb, pc, pd;
  u16 pe;
  f32x2_t ar, ai;
  Cx C;
  char* st;
  v4u bh[8], bl[8];
  __device__ __forceinline__ void load(const MegaArgs& A, const int d, const int l) {
    pa = *reinterpret_cast<const unsigned*>(A.logAre + d * NST + 2 * l);
    pb = *reinterpret_cast<const unsigned*>(A.Aim + d * NST + 2 * l);
    pc = *reinterpret_cast<const unsigned*>(A.Cre + d * NST + 2 * l);
    pd = *reinterpret_cast<const unsigned*>(A.Cim + d * NST + 2 * l);
    pe = A.logdt[d];
  }
  __device__ __forceinline__ void setup() {
    const float dt = bfr(expf(bf2f(pe)));
    ar = f32x2_t{-bfr(expf(lo16(pa))) * dt, -bfr(expf(hi16(pa))) * dt};
    ai = f32x2_t{lo16(pb) * dt, hi16(pb) * dt};
    C = Cx{f32x2_t{lo16(pc) * dt, hi16(pc) * dt}, f32x2_t{lo16(pd) * dt, hi16(pd) * dt}};
  }
  __device__ __forceinline__ float scale() {
    const float b = 2.0f * wave_sum((fabsf(C.r.x) + fabsf(C.r.y)) + (fabsf(C.i.x) + fabsf(C.i.y)));
    const unsigned eb = max(__float_as_uint(b) >> 23, 25u);
    const float ks = __uint_as_float((268u - eb) << 23);
    C.r *= ks;
    C.i *= ks;
    return __uint_as_float((eb - 24u) << 23);
  }
  __device__ __forceinline__ Cx power(const float p) const {
    Cx z;
#pragma unroll
    for (int e = 0; e < 2; ++e) {
      const float x = ai[e] * p;
      const float f = __builtin_fmaf(x, 6.4206382e-9f, __builtin_fmaf(x, 0.15915494f, -__builtin_rintf(x * 0.15915494f)));
      const float m = __expf(ar[e] * p);
      z.r[e] = m * __builtin_amdgcn_cosf(f);
      z.i[e] = m * __builtin_amdgcn_sinf(f);
    }
    return z;
  }
  static __device__ __forceinline__ unsigned rest(const f32x2_t y, const unsigned h) {
    unsigned r;
    asm("v_fma_mixlo_f16 %0, %1, -1.0, %2 op_sel_hi:[1,0,0]" : "=v"(r) : "v"(h), "v"(y.x));
    asm("v_fma_mixhi_f16 %0, %1, -1.0, %2 op_sel:[1,0,0] op_sel_hi:[1,0,0]" : "+v"(r) : "v"(h), "v"(y.y));
    return r;
  }
  __device__ __forceinline__ char* rows(char* p, Cx& y, const Cx s, const int n) const {
#pragma unroll 1
    for (int i = 0; i < n; i += 2) {
#pragma unroll
      for (int k = 0; k < 2; ++k) {
        const unsigned h0 = pkh(y.r.x, y.r.y), h1 = pkh(y.i.x, y.i.y);
        *reinterpret_cast<v2u*>(p + 1040 * k) = v2u{h0, h1};
        *reinterpret_cast<v2u*>(p + 1040 * k + 512) = v2u{rest(y.r, h0), rest(y.i, h1)};
        y = cmul(y, s);
      }
      p += 2080;
    }
    return p;
  }
  __device__ __forceinline__ void get(v4u (&h)[8], v4u (&o)[8], const int l) const {
    const char* p = st + (l & 15) * 1040 + (l >> 4) * 16;
    asm volatile("" ::: "memory");
#pragma unroll
    for (int k = 0; k < 8; ++k) {
      h[k] = *reinterpret_cast<const v4u*>(p + 64 * k);
      o[k] = *reinterpret_cast<const v4u*>(p + 512 + 64 * k);
    }
    asm volatile("" ::: "memory");
  }
  Cx cy, cs;
  __device__ __forceinline__ void columns(const int l, const int j0, const int j1) {
    if (j0 == 0) {
      cs = power(16.f);
      cs.i = -cs.i;
      cy = Cx{f32x2_t{16384.f, 16384.f}, f32x2_t{0.f, 0.f}};
    }
    rows(st + l * 8 + 1040 * j0, cy, cs, j1 - j0);
  }
  template <bool COL>
  __device__ __forceinline__ f32x4 tile(const int l) const {
    v4u h[8], o[8];
    get(h, o, l);
    f32x4 d = {}, e = {}, f = {};
#define H8(x) __builtin_bit_cast(f16x8, x)
#define KMM(a, b, d) (COL ? MFMAH(H8(b), H8(a), d) : MFMAH(H8(a), H8(b), d))
#pragma unroll
    for (int k = 0; k < 8; ++k) {
      d = KMM(h[k], bh[k], d);
      e = KMM(o[k], bh[k], e);
      f = KMM(h[k], bl[k], f);
    }
#undef KMM
#undef H8
    return (d + (e + f)) * (1.0f / 8192);
  }
};
#define ACC_INIT 0xc9b26c86c9b26ca0ull
#define CHAIN_ASM_ATT \
  "s_mov_b32 s95, 272\n" \
  "s_mov_b32 s91, 0x4c7c2000\n" \
  "s_mov_b32 s85, 0xc9b26c9b\n" \
  "s_mov_b32 s92, 0xc9326c9b\n" \
  "s_mov_b32 s93, 0xc9326c9b\n" \
  "s_mov_b32 s78, 0x3e0293ee\n" \
  "s_mov_b32 s76, 0x33333333\n" \
  "s_mov_b32 s77, 0x33333333\n" \
  "s_lshl_b32 s98, s73, 10\n" \
  "s_add_u32 s98, s98, s74\n" \
  "s_add_u32 s98, s98, 131072\n" \
  "s_lshl_b32 s97, s73, 9\n" \
  "s_add_u32 s97, s97, s74\n" \
  "s_add_u32 s97, s97, 158720\n" \
  "s_lshl_b32 s99, s73, 10\n" \
  "v_mbcnt_lo_u32_b32 v48, -1, 0\n" \
  "v_mbcnt_hi_u32_b32 v48, -1, v48\n" \
  "s_add_u32 s88, s74, 157760\n" \
  "v_mov_b32 v59, s88\n" \
  "v_lshrrev_b32 v49, 2, v48\n" \
  "v_lshrrev_b32 v50, 4, v48\n" \
  "v_bitop3_b32 v50, v49, v50, 3 bitop3:0x28\n" \
  "v_lshl_add_u32 v8, v50, 3, s97\n" \
  "v_and_b32 v50, 3, v48\n" \
  "v_lshl_add_u32 v8, v50, 5, v8\n" \
  "v_and_b32 v49, 1, v49\n" \
  "v_lshl_add_u32 v8, v49, 8, v8\n" \
  "v_and_b32 v49, 8, v48\n" \
  "v_lshl_add_u32 v8, v49, 4, v8\n" \
  "v_lshrrev_b32 v49, 5, v48\n" \
  "v_add_u32 v10, s99, v1\n" \
  "s_lshr_b32 s88, s99, 2\n" \
  "s_add_u32 s88, s88, s74\n" \
  "s_add_u32 s88, s88, 139264\n" \
  "v_lshl_add_u32 v7, v49, 10, s88\n" \
  "v_and_b32 v49, 31, v48\n" \
  "v_lshl_add_u32 v7, v49, 3, v7\n" \
  "v_mov_b64 v[12:13], 0\n" \
  "v_mov_b64 v[14:15], 0\n" \
  "v_mov_b64 v[16:17], 0\n" \
  "v_mov_b64 v[18:19], 0\n" \
  "v_mov_b64 v[20:21], 0\n" \
  "v_mov_b64 v[22:23], 0\n" \
  "v_mov_b32 v34, 0xc9b26ca0\n" \
  "v_mov_b32 v35, 0xc9b26c86\n" \
  "s_mov_b32 s87, 0x40000\n" \
  "v_subrev_u32 v30, 64, v59\n" \
  "v_add_u32 v17, 0x10000, v28\n" \
  "v_xor_b32 v18, 128, v28\n" \
  "v_xor_b32 v19, 128, v17\n" \
  "ds_write_b32 v30, v12 offset:0\n" \
  "ds_write_b32 v30, v12 offset:32\n" \
  "ds_write_b32 v30, v12 offset:64\n" \
  "v_xor_b32 v20, 0, v29\n" \
  "v_xor_b32 v21, 32, v29\n" \
  "v_xor_b32 v22, 64, v29\n" \
  "v_xor_b32 v23, 96, v29\n" \
  "s_waitcnt vmcnt(12) lgkmcnt(0)\n" \
  "s_barrier\n" \
  "ds_read_b64_tr_b16 v[160:161], v20 offset:0\n" \
  "ds_read_b64_tr_b16 v[162:163], v20 offset:2048\n" \
  "ds_read_b64_tr_b16 v[168:169], v21 offset:0\n" \
  "ds_read_b64_tr_b16 v[170:171], v21 offset:2048\n" \
  "ds_read_b64_tr_b16 v[176:177], v22 offset:0\n" \
  "ds_read_b64_tr_b16 v[178:179], v22 offset:2048\n" \
  "ds_read_b64_tr_b16 v[184:185], v23 offset:0\n" \
  "ds_read_b64_tr_b16 v[186:187], v23 offset:2048\n" \
  "s_lshl_b32 s88, s73, 10\n" \
  "v_add_u32 v48, s88, v1\n" \
  "ds_read_b128 v[52:55], v48 offset:0\n" \
  "s_waitcnt lgkmcnt(0)\n" \
  "v_mov_b32 v11, 0\n" \
  "v_dot2c_f32_bf16 v11, v52, v52\n" \
  "v_dot2c_f32_bf16 v11, v53, v53\n" \
  "v_dot2c_f32_bf16 v11, v54, v54\n" \
  "v_dot2c_f32_bf16 v11, v55, v55\n" \
  "s_nop 0\n" \
  "s_nop 1\n" \
  "v_max_u32_dpp v11, v11, v11 row_ror:1 row_mask:0xf bank_mask:0xf\n" \
  "s_nop 1\n" \
  "v_max_u32_dpp v11, v11, v11 row_ror:2 row_mask:0xf bank_mask:0xf\n" \
  "s_nop 1\n" \
  "v_max_u32_dpp v11, v11, v11 row_ror:4 row_mask:0xf bank_mask:0xf\n" \
  "s_nop 1\n" \
  "v_max_u32_dpp v11, v11, v11 row_ror:8 row_mask:0xf bank_mask:0xf\n" \
  "v_mov_b32 v31, v11\n" \
  "s_nop 1\n" \
  "v_permlane16_swap_b32 v11, v31\n" \
  "v_max_u32 v11, v11, v31\n" \
  "v_mov_b32 v31, v11\n" \
  "s_nop 1\n" \
  "v_permlane32_swap_b32 v11, v31\n" \
  "v_max_u32 v11, v11, v31\n" \
  "s_mov_b64 exec, 1\n" \
  "ds_max_u32 v59, v11\n" \
  "s_mov_b64 exec, -1\n" \
  "s_waitcnt vmcnt(8) lgkmcnt(0)\n" \
  "s_barrier\n" \
  "ds_read_b64_tr_b16 v[164:165], v20 offset:4096\n" \
  "ds_read_b64_tr_b16 v[166:167], v20 offset:6144\n" \
  "ds_read_b64_tr_b16 v[172:173], v21 offset:4096\n" \
  "ds_read_b64_tr_b16 v[174:175], v21 offset:6144\n" \
  "ds_read_b64_tr_b16 v[180:181], v22 offset:4096\n" \
  "ds_read_b64_tr_b16 v[182:183], v22 offset:6144\n" \
  "ds_read_b64_tr_b16 v[188:189], v23 offset:4096\n" \
  "ds_read_b64_tr_b16 v[190:191], v23 offset:6144\n" \
  "s_add_u32 m0, s19, 0\n" \
  "s_nop 0\n" \
  "global_load_lds_dwordx4 v24, s[4:5] offset:0\n" \
  "global_load_lds_dwordx4 v25, s[4:5] offset:1024\n" \
  "global_load_lds_dwordx4 v26, s[4:5] offset:2048\n" \
  "global_load_lds_dwordx4 v27, s[4:5] offset:3072\n" \
  "s_add_u32 s4, s4, 0x100000\n" \
  "s_addc_u32 s5, s5, 0\n" \
  "v_mfma_f32_16x16x32_bf16 v[64:67], v[160:163], v[160:163], 0\n" \
  "v_mfma_f32_16x16x32_bf16 v[68:71], v[168:171], v[168:171], 0\n" \
  "v_mfma_f32_16x16x32_bf16 v[72:75], v[176:179], v[176:179], 0\n" \
  "v_mfma_f32_16x16x32_bf16 v[76:79], v[184:187], v[184:187], 0\n" \
  "s_waitcnt vmcnt(8) lgkmcnt(0)\n" \
  "s_barrier\n" \
  "ds_read_b64_tr_b16 v[32:33], v20 offset:8192\n" \
  "ds_read_b64_tr_b16 v[34:35], v20 offset:10240\n" \
  "ds_read_b64_tr_b16 v[40:41], v21 offset:8192\n" \
  "ds_read_b64_tr_b16 v[42:43], v21 offset:10240\n" \
  "ds_read_b64_tr_b16 v[48:49], v22 offset:8192\n" \
  "ds_read_b64_tr_b16 v[50:51], v22 offset:10240\n" \
  "ds_read_b64_tr_b16 v[56:57], v23 offset:8192\n" \
  "ds_read_b64_tr_b16 v[58:59], v23 offset:10240\n" \
  "s_add_u32 m0, s19, 4096\n" \
  "s_nop 0\n" \
  "global_load_lds_dwordx4 v24, s[4:5] offset:0\n" \
  "global_load_lds_dwordx4 v25, s[4:5] offset:1024\n" \
  "global_load_lds_dwordx4 v26, s[4:5] offset:2048\n" \
  "global_load_lds_dwordx4 v27, s[4:5] offset:3072\n" \
  "s_add_u32 s4, s4, 0x100000\n" \
  "s_addc_u32 s5, s5, 0\n" \
  "v_mfma_f32_16x16x32_bf16 v[64:67], v[164:167], v[164:167], v[64:67]\n" \
  "v_mfma_f32_16x16x32_bf16 v[68:71], v[172:175], v[172:175], v[68:71]\n" \
  "v_mfma_f32_16x16x32_bf16 v[72:75], v[180:183], v[180:183], v[72:75]\n" \
  "v_mfma_f32_16x16x32_bf16 v[76:79], v[188:191], v[188:191], v[76:79]\n" \
  "s_waitcnt vmcnt(8) lgkmcnt(0)\n" \
  "s_barrier\n" \
  "ds_read_b64_tr_b16 v[36:37], v20 offset:12288\n" \
  "ds_read_b64_tr_b16 v[38:39], v20 offset:14336\n" \
  "ds_read_b64_tr_b16 v[44:45], v21 offset:12288\n" \
  "ds_read_b64_tr_b16 v[46:47], v21 offset:14336\n" \
  "ds_read_b64_tr_b16 v[52:53], v22 offset:12288\n" \
  "ds_read_b64_tr_b16 v[54:55], v22 offset:14336\n" \
  "ds_read_b64_tr_b16 v[60:61], v23 offset:12288\n" \
  "ds_read_b64_tr_b16 v[62:63], v23 offset:14336\n" \
  "s_add_u32 m0, s19, 8192\n" \
  "s_nop 0\n" \
  "global_load_lds_dwordx4 v24, s[4:5] offset:0\n" \
  "global_load_lds_dwordx4 v25, s[4:5] offset:1024\n" \
  "global_load_lds_dwordx4 v26, s[4:5] offset:2048\n" \
  "global_load_lds_dwordx4 v27, s[4:5] offset:3072\n" \
  "s_add_u32 s4, s4, 0x100000\n" \
  "s_addc_u32 s5, s5, 0\n" \
  "v_mfma_f32_16x16x32_bf16 v[64:67], v[32:35], v[32:35], v[64:67]\n" \
  "v_mfma_f32_16x16x32_bf16 v[68:71], v[40:43], v[40:43], v[68:71]\n" \
  "v_mfma_f32_16x16x32_bf16 v[72:75], v[48:51], v[48:51], v[72:75]\n" \
  "v_mfma_f32_16x16x32_bf16 v[76:79], v[56:59], v[56:59], v[76:79]\n" \
  "s_waitcnt vmcnt(8) lgkmcnt(0)\n" \
  "s_barrier\n" \
  "s_memrealtime s[24:25]\n" \
  "s_add_u32 m0, s19, 12288\n" \
  "s_nop 0\n" \
  "global_load_lds_dwordx4 v24, s[4:5] offset:0\n" \
  "global_load_lds_dwordx4 v25, s[4:5] offset:1024\n" \
  "global_load_lds_dwordx4 v26, s[4:5] offset:2048\n" \
  "global_load_lds_dwordx4 v27, s[4:5] offset:3072\n" \
  "s_add_u32 s4, s4, 0x100000\n" \
  "s_addc_u32 s5, s5, 0\n" \
  "v_mfma_f32_16x16x32_bf16 v[64:67], v[36:39], v[36:39], v[64:67]\n" \
  "v_mfma_f32_16x16x32_bf16 v[68:71], v[44:47], v[44:47], v[68:71]\n" \
  "v_mfma_f32_16x16x32_bf16 v[72:75], v[52:55], v[52:55], v[72:75]\n" \
  "v_mfma_f32_16x16x32_bf16 v[76:79], v[60:63], v[60:63], v[76:79]\n" \
  "s_nop 7\n" \
  "v_max3_f32 v13, v64, v65, v66\n" \
  "v_max3_f32 v13, v13, v67, v68\n" \
  "v_max3_f32 v13, v13, v69, v70\n" \
  "v_max3_f32 v13, v13, v71, v72\n" \
  "v_max3_f32 v13, v13, v73, v74\n" \
  "v_max3_f32 v13, v13, v75, v76\n" \
  "v_max3_f32 v13, v13, v77, v78\n" \
  "v_max_f32 v13, v13, v79\n" \
  "s_nop 1\n" \
  "v_max_f32_dpp v13, v13, v13 row_ror:1 row_mask:0xf bank_mask:0xf\n" \
  "s_nop 1\n" \
  "v_max_f32_dpp v13, v13, v13 row_ror:2 row_mask:0xf bank_mask:0xf\n" \
  "s_nop 1\n" \
  "v_max_f32_dpp v13, v13, v13 row_ror:4 row_mask:0xf bank_mask:0xf\n" \
  "s_nop 1\n" \
  "v_max_f32_dpp v13, v13, v13 row_ror:8 row_mask:0xf bank_mask:0xf\n" \
  "v_mov_b32 v31, v13\n" \
  "s_nop 1\n" \
  "v_permlane16_swap_b32 v13, v31\n" \
  "v_max_f32 v13, v13, v31\n" \
  "v_mov_b32 v31, v13\n" \
  "s_nop 1\n" \
  "v_permlane32_swap_b32 v13, v31\n" \
  "v_max_f32 v13, v13, v31\n" \
  "s_mov_b64 exec, 1\n" \
  "ds_max_u32 v30, v13 offset:32\n" \
  "s_mov_b64 exec, -1\n" \
  "s_waitcnt vmcnt(8) lgkmcnt(0)\n" \
  "s_barrier\n" \
  "s_add_u32 m0, s19, 0\n" \
  "s_nop 0\n" \
  "global_load_lds_dwordx4 v24, s[6:7] offset:0\n" \
  "global_load_lds_dwordx4 v25, s[6:7] offset:1024\n" \
  "global_load_lds_dwordx4 v26, s[6:7] offset:2048\n" \
  "global_load_lds_dwordx4 v27, s[6:7] offset:3072\n" \
  "s_add_u32 s6, s6, 0x100000\n" \
  "s_addc_u32 s7, s7, 0\n" \
  "s_waitcnt vmcnt(8) lgkmcnt(0)\n" \
  "s_barrier\n" \
  "s_add_u32 m0, s19, 4096\n" \
  "s_nop 0\n" \
  "global_load_lds_dwordx4 v24, s[6:7] offset:0\n" \
  "global_load_lds_dwordx4 v25, s[6:7] offset:1024\n" \
  "global_load_lds_dwordx4 v26, s[6:7] offset:2048\n" \
  "global_load_lds_dwordx4 v27, s[6:7] offset:3072\n" \
  "s_add_u32 s6, s6, 0x100000\n" \
  "s_addc_u32 s7, s7, 0\n" \
  "s_waitcnt vmcnt(8) lgkmcnt(0)\n" \
  "s_barrier\n" \
  "s_add_u32 m0, s19, 8192\n" \
  "s_nop 0\n" \
  "global_load_lds_dwordx4 v24, s[6:7] offset:0\n" \
  "global_load_lds_dwordx4 v25, s[6:7] offset:1024\n" \
  "global_load_lds_dwordx4 v26, s[6:7] offset:2048\n" \
  "global_load_lds_dwordx4 v27, s[6:7] offset:3072\n" \
  "s_add_u32 s6, s6, 0x100000\n" \
  "s_addc_u32 s7, s7, 0\n" \
  "s_waitcnt vmcnt(8) lgkmcnt(0)\n" \
  "s_barrier\n" \
  "s_memrealtime s[26:27]\n" \
  "ds_read_b64_tr_b16 v[64:65], v28 offset:0\n" \
  "ds_read_b64_tr_b16 v[66:67], v28 offset:512\n" \
  "ds_read_b64_tr_b16 v[68:69], v28 offset:32768\n" \
  "ds_read_b64_tr_b16 v[70:71], v28 offset:33280\n" \
  "ds_read_b64_tr_b16 v[72:73], v17 offset:0\n" \
  "ds_read_b64_tr_b16 v[74:75], v17 offset:512\n" \
  "ds_read_b64_tr_b16 v[76:77], v17 offset:32768\n" \
  "ds_read_b64_tr_b16 v[78:79], v17 offset:33280\n" \
  "ds_read_b64_tr_b16 v[192:193], v18 offset:0\n" \
  "ds_read_b64_tr_b16 v[194:195], v18 offset:512\n" \
  "ds_read_b64_tr_b16 v[196:197], v18 offset:32768\n" \
  "ds_read_b64_tr_b16 v[198:199], v18 offset:33280\n" \
  "ds_read_b64_tr_b16 v[200:201], v19 offset:0\n" \
  "ds_read_b64_tr_b16 v[202:203], v19 offset:512\n" \
  "ds_read_b64_tr_b16 v[204:205], v19 offset:32768\n" \
  "ds_read_b64_tr_b16 v[206:207], v19 offset:33280\n" \
  "s_add_u32 m0, s19, 12288\n" \
  "s_nop 0\n" \
  "global_load_lds_dwordx4 v24, s[6:7] offset:0\n" \
  "global_load_lds_dwordx4 v25, s[6:7] offset:1024\n" \
  "global_load_lds_dwordx4 v26, s[6:7] offset:2048\n" \
  "global_load_lds_dwordx4 v27, s[6:7] offset:3072\n" \
  "s_add_u32 s6, s6, 0x100000\n" \
  "s_addc_u32 s7, s7, 0\n" \
  "s_waitcnt vmcnt(8) lgkmcnt(0)\n" \
  "s_barrier\n" \
  "ds_read_b64_tr_b16 v[80:81], v28 offset:4096\n" \
  "ds_read_b64_tr_b16 v[82:83], v28 offset:4608\n" \
  "ds_read_b64_tr_b16 v[84:85], v28 offset:36864\n" \
  "ds_read_b64_tr_b16 v[86:87], v28 offset:37376\n" \
  "ds_read_b64_tr_b16 v[88:89], v17 offset:4096\n" \
  "ds_read_b64_tr_b16 v[90:91], v17 offset:4608\n" \
  "ds_read_b64_tr_b16 v[92:93], v17 offset:36864\n" \
  "ds_read_b64_tr_b16 v[94:95], v17 offset:37376\n" \
  "ds_read_b64_tr_b16 v[208:209], v18 offset:4096\n" \
  "ds_read_b64_tr_b16 v[210:211], v18 offset:4608\n" \
  "ds_read_b64_tr_b16 v[212:213], v18 offset:36864\n" \
  "ds_read_b64_tr_b16 v[214:215], v18 offset:37376\n" \
  "ds_read_b64_tr_b16 v[216:217], v19 offset:4096\n" \
  "ds_read_b64_tr_b16 v[218:219], v19 offset:4608\n" \
  "ds_read_b64_tr_b16 v[220:221], v19 offset:36864\n" \
  "ds_read_b64_tr_b16 v[222:223], v19 offset:37376\n" \
  "s_add_u32 m0, s19, 0\n" \
  "s_nop 0\n" \
  "global_load_lds_dwordx4 v24, s[8:9] offset:0\n" \
  "global_load_lds_dwordx4 v25, s[8:9] offset:1024\n" \
  "global_load_lds_dwordx4 v26, s[8:9] offset:2048\n" \
  "global_load_lds_dwordx4 v27, s[8:9] offset:3072\n" \
  "s_add_u32 s8, s8, 0x100000\n" \
  "s_addc_u32 s9, s9, 0\n" \
  "s_waitcnt vmcnt(8) lgkmcnt(0)\n" \
  "s_barrier\n" \
  "ds_read_b64_tr_b16 v[96:97], v28 offset:8192\n" \
  "ds_read_b64_tr_b16 v[98:99], v28 offset:8704\n" \
  "ds_read_b64_tr_b16 v[100:101], v28 offset:40960\n" \
  "ds_read_b64_tr_b16 v[102:103], v28 offset:41472\n" \
  "ds_read_b64_tr_b16 v[104:105], v17 offset:8192\n" \
  "ds_read_b64_tr_b16 v[106:107], v17 offset:8704\n" \
  "ds_read_b64_tr_b16 v[108:109], v17 offset:40960\n" \
  "ds_read_b64_tr_b16 v[110:111], v17 offset:41472\n" \
  "ds_read_b64_tr_b16 v[224:225], v18 offset:8192\n" \
  "ds_read_b64_tr_b16 v[226:227], v18 offset:8704\n" \
  "ds_read_b64_tr_b16 v[228:229], v18 offset:40960\n" \
  "ds_read_b64_tr_b16 v[230:231], v18 offset:41472\n" \
  "ds_read_b64_tr_b16 v[232:233], v19 offset:8192\n" \
  "ds_read_b64_tr_b16 v[234:235], v19 offset:8704\n" \
  "ds_read_b64_tr_b16 v[236:237], v19 offset:40960\n" \
  "ds_read_b64_tr_b16 v[238:239], v19 offset:41472\n" \
  "s_add_u32 m0, s19, 4096\n" \
  "s_nop 0\n" \
  "global_load_lds_dwordx4 v24, s[8:9] offset:0\n" \
  "global_load_lds_dwordx4 v25, s[8:9] offset:1024\n" \
  "global_load_lds_dwordx4 v26, s[8:9] offset:2048\n" \
  "global_load_lds_dwordx4 v27, s[8:9] offset:3072\n" \
  "s_add_u32 s8, s8, 0x100000\n" \
  "s_addc_u32 s9, s9, 0\n" \
  "s_waitcnt vmcnt(8) lgkmcnt(0)\n" \
  "s_barrier\n" \
  "ds_read_b64_tr_b16 v[112:113], v28 offset:12288\n" \
  "ds_read_b64_tr_b16 v[114:115], v28 offset:12800\n" \
  "ds_read_b64_tr_b16 v[116:117], v28 offset:45056\n" \
  "ds_read_b64_tr_b16 v[118:119], v28 offset:45568\n" \
  "ds_read_b64_tr_b16 v[120:121], v17 offset:12288\n" \
  "ds_read_b64_tr_b16 v[122:123], v17 offset:12800\n" \
  "ds_read_b64_tr_b16 v[124:125], v17 offset:45056\n" \
  "ds_read_b64_tr_b16 v[126:127], v17 offset:45568\n" \
  "ds_read_b64_tr_b16 v[240:241], v18 offset:12288\n" \
  "ds_read_b64_tr_b16 v[242:243], v18 offset:12800\n" \
  "ds_read_b64_tr_b16 v[244:245], v18 offset:45056\n" \
  "ds_read_b64_tr_b16 v[246:247], v18 offset:45568\n" \
  "ds_read_b64_tr_b16 v[248:249], v19 offset:12288\n" \
  "ds_read_b64_tr_b16 v[250:251], v19 offset:12800\n" \
  "ds_read_b64_tr_b16 v[252:253], v19 offset:45056\n" \
  "ds_read_b64_tr_b16 v[254:255], v19 offset:45568\n" \
  "s_add_u32 m0, s19, 8192\n" \
  "s_nop 0\n" \
  "global_load_lds_dwordx4 v24, s[8:9] offset:0\n" \
  "global_load_lds_dwordx4 v25, s[8:9] offset:1024\n" \
  "global_load_lds_dwordx4 v26, s[8:9] offset:2048\n" \
  "global_load_lds_dwordx4 v27, s[8:9] offset:3072\n" \
  "s_add_u32 s8, s8, 0x100000\n" \
  "s_addc_u32 s9, s9, 0\n" \
  "s_waitcnt vmcnt(8) lgkmcnt(0)\n" \
  "s_barrier\n" \
  "s_memrealtime s[28:29]\n" \
  "ds_read_b64_tr_b16 v[128:129], v28 offset:0\n" \
  "ds_read_b64_tr_b16 v[130:131], v28 offset:512\n" \
  "ds_read_b64_tr_b16 v[132:133], v28 offset:32768\n" \
  "ds_read_b64_tr_b16 v[134:135], v28 offset:33280\n" \
  "ds_read_b64_tr_b16 v[136:137], v17 offset:0\n" \
  "ds_read_b64_tr_b16 v[138:139], v17 offset:512\n" \
  "ds_read_b64_tr_b16 v[140:141], v17 offset:32768\n" \
  "ds_read_b64_tr_b16 v[142:143], v17 offset:33280\n" \
  "s_add_u32 m0, s19, 12288\n" \
  "s_nop 0\n" \
  "global_load_lds_dwordx4 v24, s[8:9] offset:0\n" \
  "global_load_lds_dwordx4 v25, s[8:9] offset:1024\n" \
  "global_load_lds_dwordx4 v26, s[8:9] offset:2048\n" \
  "global_load_lds_dwordx4 v27, s[8:9] offset:3072\n" \
  "s_add_u32 s8, s8, 0x100000\n" \
  "s_addc_u32 s9, s9, 0\n" \
  "s_waitcnt vmcnt(8) lgkmcnt(0)\n" \
  "s_barrier\n" \
  "ds_write_b128 v6, v[160:163] offset:0\n" \
  "ds_write_b128 v6, v[164:167] offset:1024\n" \
  "ds_write_b128 v6, v[168:171] offset:2048\n" \
  "ds_write_b128 v6, v[172:175] offset:3072\n" \
  "ds_read_b64_tr_b16 v[144:145], v28 offset:4096\n" \
  "ds_read_b64_tr_b16 v[146:147], v28 offset:4608\n" \
  "ds_read_b64_tr_b16 v[148:149], v28 offset:36864\n" \
  "ds_read_b64_tr_b16 v[150:151], v28 offset:37376\n" \
  "ds_read_b64_tr_b16 v[152:153], v17 offset:4096\n" \
  "ds_read_b64_tr_b16 v[154:155], v17 offset:4608\n" \
  "ds_read_b64_tr_b16 v[156:157], v17 offset:36864\n" \
  "ds_read_b64_tr_b16 v[158:159], v17 offset:37376\n" \
  "s_waitcnt vmcnt(4) lgkmcnt(0)\n" \
  "s_barrier\n" \
  "ds_write_b128 v6, v[176:179] offset:4096\n" \
  "ds_write_b128 v6, v[180:183] offset:5120\n" \
  "ds_write_b128 v6, v[184:187] offset:6144\n" \
  "ds_write_b128 v6, v[188:191] offset:7168\n" \
  "ds_read_b64_tr_b16 v[160:161], v28 offset:8192\n" \
  "ds_read_b64_tr_b16 v[162:163], v28 offset:8704\n" \
  "ds_read_b64_tr_b16 v[164:165], v28 offset:40960\n" \
  "ds_read_b64_tr_b16 v[166:167], v28 offset:41472\n" \
  "ds_read_b64_tr_b16 v[168:169], v17 offset:8192\n" \
  "ds_read_b64_tr_b16 v[170:171], v17 offset:8704\n" \
  "ds_read_b64_tr_b16 v[172:173], v17 offset:40960\n" \
  "ds_read_b64_tr_b16 v[174:175], v17 offset:41472\n" \
  "s_waitcnt vmcnt(0) lgkmcnt(0)\n" \
  "s_barrier\n" \
  "ds_write_b128 v6, v[32:35] offset:8192\n" \
  "ds_write_b128 v6, v[36:39] offset:9216\n" \
  "ds_write_b128 v6, v[40:43] offset:10240\n" \
  "ds_write_b128 v6, v[44:47] offset:11264\n" \
  "ds_read_b64_tr_b16 v[176:177], v28 offset:12288\n" \
  "ds_read_b64_tr_b16 v[178:179], v28 offset:12800\n" \
  "ds_read_b64_tr_b16 v[180:181], v28 offset:45056\n" \
  "ds_read_b64_tr_b16 v[182:183], v28 offset:45568\n" \
  "ds_read_b64_tr_b16 v[184:185], v17 offset:12288\n" \
  "ds_read_b64_tr_b16 v[186:187], v17 offset:12800\n" \
  "ds_read_b64_tr_b16 v[188:189], v17 offset:45056\n" \
  "ds_read_b64_tr_b16 v[190:191], v17 offset:45568\n" \
  "ds_read_b32 v31, v30 offset:0\n" \
  "s_waitcnt lgkmcnt(0)\n" \
  "s_barrier\n" \
  "ds_write_b128 v6, v[48:51] offset:12288\n" \
  "ds_write_b128 v6, v[52:55] offset:13312\n" \
  "ds_write_b128 v6, v[56:59] offset:14336\n" \
  "ds_write_b128 v6, v[60:63] offset:15360\n" \
  "v_mul_f32 v31, 0x3f8e353f, v31\n" \
  "v_mul_f32 v30, 0x44870000, v31\n" \
  "v_mov_b64 v[12:13], 0\n" \
  "v_mov_b64 v[14:15], 0\n" \
  "v_mov_b64 v[16:17], 0\n" \
  "v_mov_b64 v[18:19], 0\n" \
  "v_mov_b64 v[20:21], 0\n" \
  "v_mov_b64 v[22:23], 0\n" \
  "v_readfirstlane_b32 s72, v31\n" \
  "v_readfirstlane_b32 s86, v30\n" \
  "s_lshr_b32 s86, s86, 23\n" \
  "s_addk_i32 s86, 0xff83\n" \
  "s_ashr_i32 s86, s86, 1\n" \
  "s_max_i32 s86, s86, -20\n" \
  "s_min_i32 s86, s86, 100\n" \
  "s_mov_b32 s82, s86\n" \
  "s_lshr_b32 s83, s72, 23\n" \
  "s_sub_i32 s83, s83, 243\n" \
  "s_memrealtime s[30:31]\n" \
  "s_waitcnt lgkmcnt(0)\n" \
  "s_mov_b64 exec, 1\n" \
  "v_mov_b32 v24, 0\n" \
  "v_mov_b32 v26, s24\n" \
  "v_mov_b32 v27, s25\n" \
  "global_store_dwordx2 v24, v[26:27], s[20:21] offset:0\n" \
  "v_add_u32 v24, 0, v24\n" \
  "v_mov_b32 v26, s26\n" \
  "v_mov_b32 v27, s27\n" \
  "global_store_dwordx2 v24, v[26:27], s[20:21] offset:2048\n" \
  "v_add_u32 v24, 4096, v24\n" \
  "v_mov_b32 v26, s28\n" \
  "v_mov_b32 v27, s29\n" \
  "global_store_dwordx2 v24, v[26:27], s[20:21] offset:0\n" \
  "v_add_u32 v24, 0, v24\n" \
  "v_mov_b32 v26, s30\n" \
  "v_mov_b32 v27, s31\n" \
  "global_store_dwordx2 v24, v[26:27], s[20:21] offset:2048\n" \
  "v_add_u32 v24, 4096, v24\n" \
  "s_mov_b64 exec, -1\n" \
  "s_waitcnt lgkmcnt(0)\n" \
  "s_add_u32 s88, s74, 157760\n" \
  "v_mov_b32 v59, s88\n" \
  "v_mov_b32 v34, 0xc9b26ca0\n" \
  "v_mov_b32 v35, 0xc9b26c86\n" \
  ".p2align 6\n" \
  "s_nop 0\n" \
  "s_nop 0\n" \
  "s_nop 0\n" \
  "s_nop 0\n" \
  "s_nop 0\n" \
  "s_nop 0\n" \
  "s_nop 0\n" \
  "s_nop 0\n" \
  ".LCA_TOP_%=:\n" \
  "s_waitcnt lgkmcnt(0)\n" \
  "s_barrier\n" \
  "s_memrealtime s[4:5]\n" \
  "s_waitcnt lgkmcnt(0)\n" \
  "ds_read_b128 v[40:43], v1 offset:0\n" \
  "ds_read_b128 v[44:47], v1 offset:1024\n" \
  "ds_read_b128 v[48:51], v1 offset:2048\n" \
  "ds_read_b128 v[52:55], v1 offset:3072\n" \
  "ds_read_b128 v[56:59], v1 offset:4096\n" \
  "ds_read_b128 v[60:63], v1 offset:5120\n" \
  "s_cmp_lg_u32 s73, 0\n" \
  "s_cbranch_scc1 .LCA_NOZ_%=\n" \
  "s_xor_b32 s88, s60, 0x10000\n" \
  "s_add_u32 s88, s88, s61\n" \
  "buffer_store_dwordx2 v[34:35], v5, s[40:43], s88 offen sc1\n" \
  ".LCA_NOZ_%=:\n" \
  "s_waitcnt lgkmcnt(5)\n" \
  "v_smfmac_f32_16x16x64_bf16 v[20:23], v[40:43], v[192:199], v0\n" \
  "v_smfmac_f32_16x16x64_bf16 v[12:15], v[40:43], v[64:71], v0\n" \
  "v_smfmac_f32_16x16x64_bf16 v[16:19], v[40:43], v[128:135], v0\n" \
  "ds_read_b128 v[40:43], v1 offset:6144\n" \
  "s_waitcnt lgkmcnt(5)\n" \
  "v_smfmac_f32_16x16x64_bf16 v[20:23], v[44:47], v[200:207], v0\n" \
  "v_smfmac_f32_16x16x64_bf16 v[12:15], v[44:47], v[72:79], v0\n" \
  "v_smfmac_f32_16x16x64_bf16 v[16:19], v[44:47], v[136:143], v0\n" \
  "ds_read_b128 v[44:47], v1 offset:7168\n" \
  "s_waitcnt lgkmcnt(5)\n" \
  "v_smfmac_f32_16x16x64_bf16 v[20:23], v[48:51], v[208:215], v0\n" \
  "v_smfmac_f32_16x16x64_bf16 v[12:15], v[48:51], v[80:87], v0\n" \
  "v_smfmac_f32_16x16x64_bf16 v[16:19], v[48:51], v[144:151], v0\n" \
  "ds_read_b128 v[24:27], v1 offset:10240\n" \
  "ds_read_b128 v[28:31], v1 offset:14336\n" \
  "s_waitcnt lgkmcnt(6)\n" \
  "v_smfmac_f32_16x16x64_bf16 v[20:23], v[52:55], v[216:223], v0\n" \
  "v_smfmac_f32_16x16x64_bf16 v[12:15], v[52:55], v[88:95], v0\n" \
  "v_smfmac_f32_16x16x64_bf16 v[16:19], v[52:55], v[152:159], v0\n" \
  "ds_read_b128 v[32:35], v1 offset:11264\n" \
  "ds_read_b128 v[36:39], v1 offset:15360\n" \
  "s_waitcnt lgkmcnt(7)\n" \
  "v_smfmac_f32_16x16x64_bf16 v[20:23], v[56:59], v[224:231], v0\n" \
  "v_smfmac_f32_16x16x64_bf16 v[12:15], v[56:59], v[96:103], v0\n" \
  "v_smfmac_f32_16x16x64_bf16 v[16:19], v[56:59], v[160:167], v0\n" \
  "ds_read_b128 v[56:59], v1 offset:12288\n" \
  "s_waitcnt lgkmcnt(7)\n" \
  "v_smfmac_f32_16x16x64_bf16 v[20:23], v[60:63], v[232:239], v0\n" \
  "v_smfmac_f32_16x16x64_bf16 v[12:15], v[60:63], v[104:111], v0\n" \
  "v_smfmac_f32_16x16x64_bf16 v[16:19], v[60:63], v[168:175], v0\n" \
  "ds_read_b128 v[60:63], v1 offset:16384\n" \
  "s_waitcnt lgkmcnt(7)\n" \
  "v_smfmac_f32_16x16x64_bf16 v[20:23], v[40:43], v[240:247], v0\n" \
  "v_smfmac_f32_16x16x64_bf16 v[12:15], v[40:43], v[112:119], v0\n" \
  "v_smfmac_f32_16x16x64_bf16 v[16:19], v[40:43], v[176:183], v0\n" \
  "s_waitcnt lgkmcnt(6)\n" \
  "v_smfmac_f32_16x16x64_bf16 v[20:23], v[44:47], v[248:255], v0\n" \
  "v_smfmac_f32_16x16x64_bf16 v[12:15], v[44:47], v[120:127], v0\n" \
  "v_smfmac_f32_16x16x64_bf16 v[16:19], v[44:47], v[184:191], v0\n" \
  "s_cmp_eq_u32 s71, 0\n" \
  "v_permlane32_swap_b32 v20, v22\n" \
  "v_permlane32_swap_b32 v21, v23\n" \
  "v_pk_add_f32 v[20:21], v[20:21], v[22:23]\n" \
  "s_mov_b64 vcc, s[76:77]\n" \
  "v_permlane32_swap_b32 v12, v14\n" \
  "v_permlane32_swap_b32 v13, v15\n" \
  "v_cvt_pk_bf16_f32 v20, v20, v21\n" \
  "v_pk_add_f32 v[12:13], v[12:13], v[14:15]\n" \
  "v_permlane32_swap_b32 v16, v18\n" \
  "v_permlane32_swap_b32 v17, v19\n" \
  "ds_write_b32 v8, v20 offset:2048\n" \
  "v_cvt_pk_bf16_f32 v12, v12, v13\n" \
  "v_pk_add_f32 v[16:17], v[16:17], v[18:19]\n" \
  "s_nop 0\n" \
  "v_cvt_pk_bf16_f32 v13, v16, v17\n" \
  "ds_write_b64 v8, v[12:13]\n" \
  "s_setprio 1\n" \
  "s_waitcnt lgkmcnt(0)\n" \
  "s_barrier\n" \
  "s_cbranch_scc1 .LCA_LAST_%=\n" \
  "ds_read_b64_tr_b16 v[40:41], v2 offset:0\n" \
  "ds_read_b64_tr_b16 v[42:43], v3 offset:0\n" \
  "ds_read_b64_tr_b16 v[44:45], v2 offset:1024\n" \
  "ds_read_b64_tr_b16 v[46:47], v3 offset:1024\n" \
  "ds_read_b64_tr_b16 v[48:49], v2 offset:2048\n" \
  "ds_read_b64_tr_b16 v[50:51], v3 offset:2048\n" \
  "ds_read_b64_tr_b16 v[52:53], v2 offset:3072\n" \
  "ds_read_b64_tr_b16 v[54:55], v3 offset:3072\n" \
  "ds_read_b32 v11, v1 offset:26688\n" \
  "s_waitcnt lgkmcnt(7)\n" \
  "v_mfma_f32_16x16x32_bf16 v[12:15], v[24:27], v[40:43], 0\n" \
  "v_mfma_f32_16x16x32_bf16 v[16:19], v[28:31], v[40:43], 0\n" \
  "ds_read_b128 v[24:27], v1 offset:13312\n" \
  "ds_read_b128 v[28:31], v1 offset:17408\n" \
  "v_mfma_f32_16x16x32_bf16 v[20:23], v[40:43], v[40:43], 0\n" \
  "s_waitcnt lgkmcnt(7)\n" \
  "v_mfma_f32_16x16x32_bf16 v[12:15], v[32:35], v[44:47], v[12:15]\n" \
  "v_mfma_f32_16x16x32_bf16 v[16:19], v[36:39], v[44:47], v[16:19]\n" \
  "v_mfma_f32_16x16x32_bf16 v[20:23], v[44:47], v[44:47], v[20:23]\n" \
  "s_waitcnt lgkmcnt(2)\n" \
  "v_mfma_f32_16x16x32_bf16 v[12:15], v[56:59], v[48:51], v[12:15]\n" \
  "v_readfirstlane_b32 s86, v11\n" \
  "v_mfma_f32_16x16x32_bf16 v[16:19], v[60:63], v[48:51], v[16:19]\n" \
  "ds_read_b128 v[32:35], v6 offset:8192\n" \
  "ds_read_b128 v[36:39], v6 offset:9216\n" \
  "v_mfma_f32_16x16x32_bf16 v[20:23], v[48:51], v[48:51], v[20:23]\n" \
  "s_lshr_b32 s86, s86, 23\n" \
  "s_add_i32 s86, s86, s83\n" \
  "s_ashr_i32 s86, s86, 1\n" \
  "ds_read_b128 v[48:51], v10 offset:18432\n" \
  "v_mfma_f32_16x16x32_bf16 v[20:23], v[52:55], v[52:55], v[20:23]\n" \
  "s_max_i32 s86, s86, s82\n" \
  "s_min_i32 s86, s86, 100\n" \
  "s_lshl_b32 s86, s86, 23\n" \
  "s_sub_i32 s84, 0xc8800000, s86\n" \
  "s_waitcnt lgkmcnt(3)\n" \
  "v_mfma_f32_16x16x32_bf16 v[12:15], v[24:27], v[52:55], v[12:15]\n" \
  "v_mfma_f32_16x16x32_bf16 v[16:19], v[28:31], v[52:55], v[16:19]\n" \
  "ds_read_b128 v[52:55], v10 offset:22528\n" \
  "ds_read_b128 v[56:59], v6 offset:0\n" \
  "ds_read_b128 v[60:63], v6 offset:1024\n" \
  "ds_read_b128 v[24:27], v6 offset:2048\n" \
  "ds_read_b128 v[28:31], v6 offset:3072\n" \
  "v_cndmask_b32_dpp v20, v23, v22, vcc quad_perm:[2,3,0,1] row_mask:0xf bank_mask:0xf\n" \
  "v_mov_b32 v41, 0\n" \
  "v_mov_b32 v42, 0\n" \
  "v_cndmask_b32_dpp v12, v16, v12, vcc quad_perm:[2,3,0,1] row_mask:0xf bank_mask:0xf\n" \
  "v_cndmask_b32_dpp v13, v17, v13, vcc quad_perm:[2,3,0,1] row_mask:0xf bank_mask:0xf\n" \
  "v_cndmask_b32_dpp v14, v18, v14, vcc quad_perm:[2,3,0,1] row_mask:0xf bank_mask:0xf\n" \
  "v_cndmask_b32_dpp v15, v19, v15, vcc quad_perm:[2,3,0,1] row_mask:0xf bank_mask:0xf\n" \
  "v_maximum3_f32 v16, v20, v12, v13\n" \
  "v_maximum3_f32 v16, v16, v14, v15\n" \
  "s_nop 1\n" \
  "v_max_f32_dpp v16, v16, v16 quad_perm:[2,3,0,1] row_mask:0xf bank_mask:0xf\n" \
  "v_mov_b32 v17, v16\n" \
  "s_nop 1\n" \
  "v_permlane16_swap_b32 v16, v17\n" \
  "v_max_f32 v16, v16, v17\n" \
  "v_mov_b32 v17, v16\n" \
  "s_nop 1\n" \
  "v_permlane32_swap_b32 v16, v17\n" \
  "v_max_f32 v16, v16, v17\n" \
  "v_mul_f32 v16, s78, v16\n" \
  "v_pk_fma_f32 v[12:13], v[12:13], s[78:79], v[16:17] op_sel_hi:[1,0,0] neg_lo:[0,0,1] neg_hi:[0,0,1]\n" \
  "v_pk_fma_f32 v[14:15], v[14:15], s[78:79], v[16:17] op_sel_hi:[1,0,0] neg_lo:[0,0,1] neg_hi:[0,0,1]\n" \
  "v_fma_f32 v20, v20, s78, -v16\n" \
  "v_exp_f32 v12, v12\n" \
  "v_exp_f32 v13, v13\n" \
  "v_exp_f32 v14, v14\n" \
  "v_exp_f32 v15, v15\n" \
  "v_exp_f32 v20, v20\n" \
  "s_waitcnt lgkmcnt(4)\n" \
  "v_cvt_pk_bf16_f32 v44, v12, v13\n" \
  "v_cvt_pk_bf16_f32 v45, v14, v15\n" \
  "v_pk_add_f32 v[8:9], v[12:13], v[14:15]\n" \
  "v_mov_b32_dpp v46, v44 quad_perm:[2,3,0,1] row_mask:0xf bank_mask:0xf\n" \
  "v_mov_b32_dpp v47, v45 quad_perm:[2,3,0,1] row_mask:0xf bank_mask:0xf\n" \
  "v_add_f32 v10, v8, v20\n" \
  "v_add_f32 v10, v10, v9\n" \
  "v_mfma_f32_16x16x32_bf16 v[12:15], v[48:51], v[44:47], 0\n" \
  "v_mov_b32_dpp v21, v20 quad_perm:[2,3,0,1] row_mask:0xf bank_mask:0xf\n" \
  "v_add_f32_dpp v10, v10, v10 quad_perm:[2,3,0,1] row_mask:0xf bank_mask:0xf\n" \
  "v_mov_b32 v11, v10\n" \
  "v_mfma_f32_16x16x32_bf16 v[16:19], v[52:55], v[44:47], 0\n" \
  "s_setprio 3\n" \
  "s_barrier\n" \
  "ds_read_b64 v[22:23], v7 offset:0\n" \
  "s_nop 0\n" \
  "v_permlane16_swap_b32 v10, v11\n" \
  "v_add_f32 v10, v10, v11\n" \
  "v_mov_b32 v11, v10\n" \
  "v_mov_b64 v[8:9], s[92:93]\n" \
  "s_nop 0\n" \
  "v_permlane32_swap_b32 v10, v11\n" \
  "v_add_f32 v10, v10, v11\n" \
  "v_rcp_f32 v10, v10\n" \
  "s_nop 0\n" \
  "v_mul_f32 v10, s84, v10\n" \
  "v_pk_mul_f32 v[20:21], v[20:21], v[10:11] op_sel_hi:[1,0]\n" \
  "v_pk_mul_f32 v[12:13], v[12:13], v[10:11] op_sel_hi:[1,0]\n" \
  "v_pk_mul_f32 v[14:15], v[14:15], v[10:11] op_sel_hi:[1,0]\n" \
  "v_cvt_pk_bf16_f32 v40, v20, v21\n" \
  "v_pk_mul_f32 v[16:17], v[16:17], v[10:11] op_sel_hi:[1,0]\n" \
  "v_cvt_pk_bf16_f32 v43, v20, v21\n" \
  "v_pk_mul_f32 v[18:19], v[18:19], v[10:11] op_sel_hi:[1,0]\n" \
  "s_waitcnt lgkmcnt(0)\n" \
  "v_mfma_f32_16x16x16_bf16 v[12:15], v[22:23], v[40:41], v[12:15]\n" \
  "v_mfma_f32_16x16x16_bf16 v[16:19], v[22:23], v[42:43], v[16:19]\n" \
  "v_mov_b64 v[10:11], s[92:93]\n" \
  "ds_read_b128 v[48:51], v6 offset:10240\n" \
  "ds_read_b128 v[52:55], v6 offset:11264\n" \
  "v_mov_b64 v[20:21], s[92:93]\n" \
  "v_mov_b64 v[22:23], s[92:93]\n" \
  "s_nop 1\n" \
  "v_cvt_pk_bf16_f32 v40, v12, v13\n" \
  "v_cvt_pk_bf16_f32 v41, v14, v15\n" \
  "v_cvt_pk_bf16_f32 v42, v16, v17\n" \
  "ds_write_b64 v4, v[40:41]\n" \
  "v_cvt_pk_bf16_f32 v43, v18, v19\n" \
  "ds_write_b64 v4, v[42:43] offset:1024\n" \
  "v_mov_b64 v[12:13], s[92:93]\n" \
  "v_mov_b64 v[14:15], s[92:93]\n" \
  "v_mov_b64 v[16:17], s[92:93]\n" \
  "v_mov_b64 v[18:19], s[92:93]\n" \
  "s_setprio 0\n" \
  "s_waitcnt lgkmcnt(0)\n" \
  "s_barrier\n" \
  "ds_read_b128 v[40:43], v1 offset:8192\n" \
  "ds_read_b128 v[44:47], v1 offset:9216\n" \
  "s_waitcnt lgkmcnt(1)\n" \
  "v_smfmac_f32_16x16x64_bf16 v[12:15], v[40:43], v[56:63], v0\n" \
  "ds_read_b128 v[56:59], v6 offset:4096\n" \
  "ds_read_b128 v[60:63], v6 offset:5120\n" \
  "v_smfmac_f32_16x16x64_bf16 v[16:19], v[40:43], v[24:31], v0\n" \
  "ds_read_b128 v[24:27], v6 offset:6144\n" \
  "ds_read_b128 v[28:31], v6 offset:7168\n" \
  "s_waitcnt lgkmcnt(4)\n" \
  "v_smfmac_f32_16x16x64_bf16 v[12:15], v[44:47], v[32:39], v0\n" \
  "ds_read_b128 v[32:35], v6 offset:12288\n" \
  "ds_read_b128 v[36:39], v6 offset:13312\n" \
  "v_smfmac_f32_16x16x64_bf16 v[16:19], v[44:47], v[48:55], v0\n" \
  "ds_read_b128 v[48:51], v6 offset:14336\n" \
  "ds_read_b128 v[52:55], v6 offset:15360\n" \
  "s_nop 5\n" \
  "v_permlane32_swap_b32 v12, v17\n" \
  "v_permlane32_swap_b32 v13, v16\n" \
  "v_permlane32_swap_b32 v14, v19\n" \
  "v_permlane32_swap_b32 v15, v18\n" \
  "v_pk_add_f32 v[12:13], v[12:13], v[16:17] op_sel:[0,1] op_sel_hi:[1,0]\n" \
  "v_pk_add_f32 v[14:15], v[14:15], v[18:19] op_sel:[0,1] op_sel_hi:[1,0]\n" \
  "buffer_atomic_add_x2 v[12:13], v5, s[40:43], s56 offen\n" \
  "buffer_atomic_add_x2 v[14:15], v5, s[40:43], s57 offen\n" \
  "s_waitcnt lgkmcnt(6)\n" \
  "v_smfmac_f32_16x16x64_bf16 v[20:23], v[40:43], v[56:63], v0\n" \
  "s_waitcnt lgkmcnt(4)\n" \
  "v_smfmac_f32_16x16x64_bf16 v[8:11], v[40:43], v[24:31], v0\n" \
  "s_waitcnt lgkmcnt(2)\n" \
  "v_smfmac_f32_16x16x64_bf16 v[20:23], v[44:47], v[32:39], v0\n" \
  "s_waitcnt lgkmcnt(0)\n" \
  "v_smfmac_f32_16x16x64_bf16 v[8:11], v[44:47], v[48:55], v0\n" \
  "s_nop 7\n" \
  "v_permlane32_swap_b32 v20, v9\n" \
  "v_permlane32_swap_b32 v21, v8\n" \
  "v_permlane32_swap_b32 v22, v11\n" \
  "v_permlane32_swap_b32 v23, v10\n" \
  "v_pk_add_f32 v[20:21], v[20:21], v[8:9] op_sel:[0,1] op_sel_hi:[1,0]\n" \
  "v_pk_add_f32 v[22:23], v[22:23], v[10:11] op_sel:[0,1] op_sel_hi:[1,0]\n" \
  "buffer_atomic_add_x2 v[20:21], v5, s[40:43], s58 offen\n" \
  "buffer_atomic_add_x2 v[22:23], v5, s[40:43], s59 offen\n" \
  "s_memrealtime s[38:39]\n" \
  "s_mov_b64 exec, 1\n" \
  "v_mov_b32 v50, s4\n" \
  "v_mov_b32 v51, s5\n" \
  "v_mov_b32 v48, 0\n" \
  "global_store_dwordx2 v48, v[50:51], s[36:37]\n" \
  "s_mov_b64 exec, -1\n" \
  "s_add_u32 s36, s36, 8\n" \
  "s_addc_u32 s37, s37, 0\n" \
  "s_nop 0\n" \
  "s_nop 0\n" \
  "s_nop 0\n" \
  "s_nop 0\n" \
  "s_nop 0\n" \
  "s_nop 0\n" \
  "s_nop 0\n" \
  "v_mbcnt_lo_u32_b32 v48, -1, 0\n" \
  "v_mbcnt_hi_u32_b32 v48, -1, v48\n" \
  "s_add_u32 s88, s74, 157760\n" \
  "v_mov_b32 v59, s88\n" \
  "v_lshl_add_u32 v52, v48, 2, v59\n" \
  "ds_read_b32 v52, v52 offset:192\n" \
  "v_lshrrev_b32 v49, 3, v48\n" \
  "v_and_b32 v50, 7, v48\n" \
  "v_lshlrev_b32 v50, 4, v50\n" \
  "v_lshl_or_b32 v44, v49, 8, v50\n" \
  "s_mov_b32 s87, 0x40000\n" \
  "v_lshrrev_b32 v49, 2, v48\n" \
  "v_lshrrev_b32 v50, 4, v48\n" \
  "v_bitop3_b32 v50, v49, v50, 3 bitop3:0x28\n" \
  "v_lshl_add_u32 v8, v50, 3, s97\n" \
  "v_and_b32 v50, 3, v48\n" \
  "v_lshl_add_u32 v8, v50, 5, v8\n" \
  "v_and_b32 v49, 1, v49\n" \
  "v_lshl_add_u32 v8, v49, 8, v8\n" \
  "v_and_b32 v49, 8, v48\n" \
  "v_lshl_add_u32 v8, v49, 4, v8\n" \
  "v_lshrrev_b32 v49, 5, v48\n" \
  "v_add_u32 v10, s99, v1\n" \
  "s_lshr_b32 s88, s99, 2\n" \
  "s_add_u32 s88, s88, s74\n" \
  "s_add_u32 s88, s88, 139264\n" \
  "v_lshl_add_u32 v7, v49, 10, s88\n" \
  "v_and_b32 v49, 31, v48\n" \
  "v_lshl_add_u32 v7, v49, 3, v7\n" \
  "v_mov_b64 v[12:13], 0\n" \
  "v_mov_b64 v[14:15], 0\n" \
  "v_mov_b64 v[16:17], 0\n" \
  "v_mov_b64 v[18:19], 0\n" \
  "v_mov_b64 v[20:21], 0\n" \
  "v_mov_b64 v[22:23], 0\n" \
  "v_mov_b32 v34, 0xc9b26ca0\n" \
  "v_mov_b32 v35, 0xc9b26c86\n" \
  "v_and_b32 v50, 3, v48\n" \
  "v_lshl_add_u32 v40, v50, 2, s98\n" \
  "v_lshrrev_b32 v50, 4, v48\n" \
  "v_mad_u32_u24 v40, v50, s95, v40\n" \
  "v_bfe_u32 v49, v48, 3, 1\n" \
  "v_lshl_add_u32 v40, v49, 6, v40\n" \
  "v_bfe_u32 v49, v48, 2, 1\n" \
  "v_lshl_add_u32 v40, v49, 7, v40\n" \
  "v_xor_b32 v41, 16, v40\n" \
  "v_xor_b32 v42, 32, v40\n" \
  "v_xor_b32 v43, 48, v40\n" \
  "s_add_i32 s80, s86, 0x35000000\n" \
  "v_mov_b32 v11, 0\n" \
  "s_mov_b32 s89, s60\n" \
  "s_bitcmp1_b32 s60, 7\n" \
  "s_cselect_b32 s88, 0x10000, 0\n" \
  "s_bitset1_b32 s88, 7\n" \
  "s_xor_b32 s56, s56, s88\n" \
  "s_xor_b32 s57, s57, s88\n" \
  "s_xor_b32 s58, s58, s88\n" \
  "s_xor_b32 s59, s59, s88\n" \
  "s_xor_b32 s60, s60, s88\n" \
  "s_sub_u32 s71, s71, 1\n" \
  "s_waitcnt lgkmcnt(0)\n" \
  "s_sub_u32 s39, s38, 150\n" \
  "v_subrev_u32 v52, s39, v52\n" \
  "v_cmp_lt_u32 vcc, 0x1000, v52\n" \
  "s_cmp_eq_u64 vcc, 0\n" \
  "s_cbranch_scc1 .LCA_OKM_%=\n" \
  ".LCA_AGN_%=:\n" \
  "buffer_load_dword v52, v5, s[0:3], s66 offen sc1\n" \
  "s_sub_u32 s87, s87, 1\n" \
  "s_cbranch_scc1 .LCA_END_%=\n" \
  "s_waitcnt vmcnt(0)\n" \
  "v_cmp_eq_u32 vcc, 0, v52\n" \
  "v_subrev_u32 v52, s39, v52\n" \
  "s_cmp_eq_u64 vcc, 0\n" \
  "s_cbranch_scc1 .LCA_OKM_%=\n" \
  "s_sleep 4\n" \
  "s_branch .LCA_AGN_%=\n" \
  ".LCA_OKM_%=:\n" \
  "s_nop 1\n" \
  "v_max_i32_dpp v52, v52, v52 row_ror:1 row_mask:0xf bank_mask:0xf\n" \
  "s_nop 1\n" \
  "v_max_i32_dpp v52, v52, v52 row_ror:2 row_mask:0xf bank_mask:0xf\n" \
  "s_nop 1\n" \
  "v_max_i32_dpp v52, v52, v52 row_ror:4 row_mask:0xf bank_mask:0xf\n" \
  "s_nop 1\n" \
  "v_max_i32_dpp v52, v52, v52 row_ror:8 row_mask:0xf bank_mask:0xf\n" \
  "s_movk_i32 vcc_lo, 150\n" \
  "v_readlane_b32 s88, v52, 0\n" \
  "s_max_i32 vcc_lo, vcc_lo, s88\n" \
  "v_readlane_b32 s88, v52, 16\n" \
  "s_max_i32 vcc_lo, vcc_lo, s88\n" \
  "v_readlane_b32 s88, v52, 32\n" \
  "s_max_i32 vcc_lo, vcc_lo, s88\n" \
  "v_readlane_b32 s88, v52, 48\n" \
  "s_max_i32 vcc_lo, vcc_lo, s88\n" \
  "s_add_u32 s66, s66, 1024\n" \
  "s_add_u32 vcc_lo, vcc_lo, s39\n" \
  "s_add_u32 vcc_lo, vcc_lo, 23\n" \
  ".LCA_TW_%=:\n" \
  "s_memrealtime s[38:39]\n" \
  "s_waitcnt lgkmcnt(0)\n" \
  "s_sub_u32 s38, s38, vcc_lo\n" \
  "s_cmp_lt_i32 s38, 0\n" \
  "s_cbranch_scc1 .LCA_TW_%=\n" \
  "buffer_load_dwordx4 v[24:27], v44, s[40:43], s89 offen sc1\n" \
  "buffer_load_dwordx4 v[28:31], v44, s[40:43], s89 offen offset:2048 sc1\n" \
  ".LCA_SEE_%=:\n" \
  "s_waitcnt vmcnt(1)\n" \
  "v_cvt_f32_i32 v24, v24\n" \
  "v_cvt_f32_i32 v25, v25\n" \
  "v_cvt_f32_i32 v26, v26\n" \
  "v_cvt_f32_i32 v27, v27\n" \
  "v_max_f32 v45, |v24|, |v26|\n" \
  "v_pk_mul_f32 v[24:25], v[24:25], s[80:81] op_sel_hi:[1,0]\n" \
  "v_pk_mul_f32 v[26:27], v[26:27], s[80:81] op_sel_hi:[1,0]\n" \
  "s_nop 0\n" \
  "v_cvt_pk_bf16_f32 v32, v24, v26\n" \
  "v_cvt_pk_bf16_f32 v33, v25, v27\n" \
  "ds_write_b32 v40, v32\n" \
  "ds_write_b32 v41, v33\n" \
  "s_waitcnt vmcnt(0)\n" \
  "v_cvt_f32_i32 v28, v28\n" \
  "v_cvt_f32_i32 v30, v30\n" \
  "v_cvt_f32_i32 v29, v29\n" \
  "v_max3_f32 v45, v45, |v28|, |v30|\n" \
  "v_cvt_f32_i32 v31, v31\n" \
  "v_cmp_lt_f32 vcc, s91, v45\n" \
  "v_pk_mul_f32 v[28:29], v[28:29], s[80:81] op_sel_hi:[1,0]\n" \
  "v_pk_mul_f32 v[30:31], v[30:31], s[80:81] op_sel_hi:[1,0]\n" \
  "s_cmp_eq_u64 vcc, 0\n" \
  "s_cbranch_scc0 .LCA_POLL_%=\n" \
  "v_cvt_pk_bf16_f32 v36, v28, v30\n" \
  "v_cvt_pk_bf16_f32 v37, v29, v31\n" \
  "ds_write_b32 v42, v36\n" \
  "ds_write_b32 v43, v37\n" \
  "v_dot2c_f32_bf16 v11, v32, v32\n" \
  "v_dot2c_f32_bf16 v11, v33, v33\n" \
  "v_dot2c_f32_bf16 v11, v36, v36\n" \
  "v_dot2c_f32_bf16 v11, v37, v37\n" \
  "s_nop 1\n" \
  "s_nop 0\n" \
  "v_cmp_lt_f32 vcc, 4.0, v11\n" \
  "s_cbranch_vccz .LCA_TOP_%=\n" \
  "s_mov_b64 exec, vcc\n" \
  "ds_max_u32 v59, v11\n" \
  "s_mov_b64 exec, -1\n" \
  "s_branch .LCA_TOP_%=\n" \
  ".LCA_POLL_%=:\n" \
  "buffer_load_dwordx4 v[24:27], v44, s[40:43], s89 offen sc1\n" \
  "buffer_load_dwordx4 v[28:31], v44, s[40:43], s89 offen offset:2048 sc1\n" \
  "s_sub_u32 s87, s87, 1\n" \
  "s_cbranch_scc1 .LCA_END_%=\n" \
  "s_branch .LCA_SEE_%=\n" \
  ".LCA_LAST_%=:\n" \
  "s_mov_b64 exec, 1\n" \
  "v_mov_b32 v50, s4\n" \
  "v_mov_b32 v51, s5\n" \
  "v_mov_b32 v48, 0\n" \
  "global_store_dwordx2 v48, v[50:51], s[36:37]\n" \
  "s_mov_b64 exec, -1\n" \
  "s_add_u32 s36, s36, 8\n" \
  "s_addc_u32 s37, s37, 0\n" \
  "s_nop 0\n" \
  "s_nop 0\n" \
  "s_nop 0\n" \
  "s_nop 0\n" \
  "s_nop 0\n" \
  "s_nop 0\n" \
  "s_nop 0\n" \
  ".LCA_END_%=:\n" \
  "s_endpgm\n"
#define CHAIN_ASM_VO \
  "s_mov_b32 s95, 272\n" \
  "s_mov_b32 s91, 0x4c7c2000\n" \
  "s_mov_b32 s85, 0xc9b26c9b\n" \
  "s_mov_b32 s92, 0xc9326c9b\n" \
  "s_mov_b32 s93, 0xc9326c9b\n" \
  "s_mov_b32 s78, 0x3e0293ee\n" \
  "s_mov_b32 s76, 0x33333333\n" \
  "s_mov_b32 s77, 0x33333333\n" \
  "s_lshl_b32 s98, s73, 10\n" \
  "s_add_u32 s98, s98, s74\n" \
  "s_add_u32 s98, s98, 131072\n" \
  "s_lshl_b32 s97, s73, 9\n" \
  "s_add_u32 s97, s97, s74\n" \
  "s_add_u32 s97, s97, 158724\n" \
  "s_lshl_b32 s99, s73, 11\n" \
  "s_add_u32 s99, s99, s74\n" \
  "s_add_u32 s99, s99, 133120\n" \
  "s_lshl_b32 s94, s73, 4\n" \
  "s_add_u32 s94, s94, s74\n" \
  "s_add_u32 s94, s94, 157760\n" \
  "v_mbcnt_lo_u32_b32 v48, -1, 0\n" \
  "v_mbcnt_hi_u32_b32 v48, -1, v48\n" \
  "s_add_u32 s88, s74, 157760\n" \
  "v_mov_b32 v59, s88\n" \
  "s_sub_u32 s88, s73, 4\n" \
  "v_lshrrev_b32 v49, 2, v48\n" \
  "v_lshrrev_b32 v50, 4, v48\n" \
  "v_bitop3_b32 v50, v49, v50, 3 bitop3:0x28\n" \
  "v_lshl_add_u32 v4, v50, 3, s97\n" \
  "v_and_b32 v50, 3, v48\n" \
  "v_lshl_add_u32 v4, v50, 5, v4\n" \
  "v_and_b32 v49, 1, v49\n" \
  "v_lshl_add_u32 v4, v49, 8, v4\n" \
  "v_and_b32 v49, 8, v48\n" \
  "v_lshl_add_u32 v4, v49, 4, v4\n" \
  "v_and_b32 v51, 15, v48\n" \
  "v_bfe_u32 v49, v48, 4, 1\n" \
  "v_lshrrev_b32 v50, 5, v48\n" \
  "s_lshl_b32 s89, s88, 10\n" \
  "s_add_u32 s89, s89, s74\n" \
  "s_add_u32 s89, s89, 158720\n" \
  "v_lshrrev_b32 v9, 3, v51\n" \
  "v_mul_u32_u24 v6, 41, v9\n" \
  "v_xor_b32 v6, v6, v51\n" \
  "v_lshl_add_u32 v6, v6, 3, s89\n" \
  "v_lshl_add_u32 v6, v49, 6, v6\n" \
  "v_lshl_add_u32 v6, v50, 9, v6\n" \
  "v_xor_b32 v9, 0x90, v6\n" \
  "v_lshlrev_b32 v8, 4, v49\n" \
  "v_lshl_add_u32 v8, v51, 5, v8\n" \
  "v_lshl_add_u32 v8, v50, 3, v8\n" \
  "s_lshl_b32 s89, s88, 8\n" \
  "s_add_u32 s89, s89, s74\n" \
  "s_add_u32 s89, s89, 139264\n" \
  "v_and_b32 v49, 31, v48\n" \
  "v_lshl_add_u32 v5, v49, 3, s89\n" \
  "v_lshl_add_u32 v5, v50, 10, v5\n" \
  "v_lshrrev_b32 v49, 2, v51\n" \
  "v_and_b32 v50, 5, v51\n" \
  "v_and_or_b32 v49, v49, 2, v50\n" \
  "v_and_b32 v50, 48, v48\n" \
  "v_lshlrev_b32 v7, 3, v50\n" \
  "v_lshl_add_u32 v7, v49, 4, v7\n" \
  "v_bitop3_b32 v49, v51, 2, 2 bitop3:0x0c\n" \
  "v_lshl_add_u32 v7, v49, 29, v7\n" \
  "v_mov_b64 v[12:13], 0\n" \
  "v_mov_b64 v[14:15], 0\n" \
  "v_mov_b64 v[16:17], 0\n" \
  "v_mov_b64 v[18:19], 0\n" \
  "v_mov_b64 v[20:21], 0\n" \
  "v_mov_b64 v[22:23], 0\n" \
  "v_lshlrev_b32 v10, 4, v48\n" \
  "s_add_u32 s78, s74, 157952\n" \
  "v_lshl_add_u32 v52, v48, 2, v59\n" \
  "ds_write_b32 v52, v12 offset:192\n" \
  "s_movk_i32 s79, 105\n" \
  "s_movk_i32 s96, 25\n" \
  "s_mov_b32 s87, 0x40000\n" \
  "v_subrev_u32 v30, 64, v59\n" \
  "v_add_u32 v17, 0x10000, v28\n" \
  "v_xor_b32 v18, 128, v28\n" \
  "v_xor_b32 v19, 128, v17\n" \
  "ds_write_b32 v30, v12 offset:0\n" \
  "ds_write_b32 v30, v12 offset:32\n" \
  "ds_write_b32 v30, v12 offset:64\n" \
  "v_xor_b32 v20, 0, v29\n" \
  "v_xor_b32 v21, 32, v29\n" \
  "v_xor_b32 v22, 64, v29\n" \
  "v_xor_b32 v23, 96, v29\n" \
  "s_waitcnt vmcnt(12) lgkmcnt(0)\n" \
  "s_barrier\n" \
  "ds_read_b64_tr_b16 v[96:97], v20 offset:0\n" \
  "ds_read_b64_tr_b16 v[98:99], v20 offset:2048\n" \
  "ds_read_b64_tr_b16 v[104:105], v21 offset:0\n" \
  "ds_read_b64_tr_b16 v[106:107], v21 offset:2048\n" \
  "ds_read_b64_tr_b16 v[112:113], v22 offset:0\n" \
  "ds_read_b64_tr_b16 v[114:115], v22 offset:2048\n" \
  "ds_read_b64_tr_b16 v[120:121], v23 offset:0\n" \
  "ds_read_b64_tr_b16 v[122:123], v23 offset:2048\n" \
  "s_lshl_b32 s88, s73, 10\n" \
  "v_add_u32 v48, s88, v1\n" \
  "ds_read_b128 v[52:55], v48 offset:0\n" \
  "s_waitcnt lgkmcnt(0)\n" \
  "v_mov_b32 v11, 0\n" \
  "v_dot2c_f32_bf16 v11, v52, v52\n" \
  "v_dot2c_f32_bf16 v11, v53, v53\n" \
  "v_dot2c_f32_bf16 v11, v54, v54\n" \
  "v_dot2c_f32_bf16 v11, v55, v55\n" \
  "s_nop 0\n" \
  "s_nop 1\n" \
  "v_max_u32_dpp v11, v11, v11 row_ror:1 row_mask:0xf bank_mask:0xf\n" \
  "s_nop 1\n" \
  "v_max_u32_dpp v11, v11, v11 row_ror:2 row_mask:0xf bank_mask:0xf\n" \
  "s_nop 1\n" \
  "v_max_u32_dpp v11, v11, v11 row_ror:4 row_mask:0xf bank_mask:0xf\n" \
  "s_nop 1\n" \
  "v_max_u32_dpp v11, v11, v11 row_ror:8 row_mask:0xf bank_mask:0xf\n" \
  "v_mov_b32 v31, v11\n" \
  "s_nop 1\n" \
  "v_permlane16_swap_b32 v11, v31\n" \
  "v_max_u32 v11, v11, v31\n" \
  "v_mov_b32 v31, v11\n" \
  "s_nop 1\n" \
  "v_permlane32_swap_b32 v11, v31\n" \
  "v_max_u32 v11, v11, v31\n" \
  "s_mov_b64 exec, 1\n" \
  "ds_max_u32 v59, v11\n" \
  "s_mov_b64 exec, -1\n" \
  "s_waitcnt vmcnt(8) lgkmcnt(0)\n" \
  "s_barrier\n" \
  "ds_read_b64_tr_b16 v[100:101], v20 offset:4096\n" \
  "ds_read_b64_tr_b16 v[102:103], v20 offset:6144\n" \
  "ds_read_b64_tr_b16 v[108:109], v21 offset:4096\n" \
  "ds_read_b64_tr_b16 v[110:111], v21 offset:6144\n" \
  "ds_read_b64_tr_b16 v[116:117], v22 offset:4096\n" \
  "ds_read_b64_tr_b16 v[118:119], v22 offset:6144\n" \
  "ds_read_b64_tr_b16 v[124:125], v23 offset:4096\n" \
  "ds_read_b64_tr_b16 v[126:127], v23 offset:6144\n" \
  "s_add_u32 m0, s19, 0\n" \
  "s_nop 0\n" \
  "global_load_lds_dwordx4 v24, s[4:5] offset:0\n" \
  "global_load_lds_dwordx4 v25, s[4:5] offset:1024\n" \
  "global_load_lds_dwordx4 v26, s[4:5] offset:2048\n" \
  "global_load_lds_dwordx4 v27, s[4:5] offset:3072\n" \
  "s_add_u32 s4, s4, 0x100000\n" \
  "s_addc_u32 s5, s5, 0\n" \
  "v_mfma_f32_16x16x32_bf16 v[64:67], v[96:99], v[96:99], 0\n" \
  "v_mfma_f32_16x16x32_bf16 v[68:71], v[104:107], v[104:107], 0\n" \
  "v_mfma_f32_16x16x32_bf16 v[72:75], v[112:115], v[112:115], 0\n" \
  "v_mfma_f32_16x16x32_bf16 v[76:79], v[120:123], v[120:123], 0\n" \
  "s_waitcnt vmcnt(8) lgkmcnt(0)\n" \
  "s_barrier\n" \
  "ds_read_b64_tr_b16 v[32:33], v20 offset:8192\n" \
  "ds_read_b64_tr_b16 v[34:35], v20 offset:10240\n" \
  "ds_read_b64_tr_b16 v[40:41], v21 offset:8192\n" \
  "ds_read_b64_tr_b16 v[42:43], v21 offset:10240\n" \
  "ds_read_b64_tr_b16 v[48:49], v22 offset:8192\n" \
  "ds_read_b64_tr_b16 v[50:51], v22 offset:10240\n" \
  "ds_read_b64_tr_b16 v[56:57], v23 offset:8192\n" \
  "ds_read_b64_tr_b16 v[58:59], v23 offset:10240\n" \
  "s_add_u32 m0, s19, 4096\n" \
  "s_nop 0\n" \
  "global_load_lds_dwordx4 v24, s[4:5] offset:0\n" \
  "global_load_lds_dwordx4 v25, s[4:5] offset:1024\n" \
  "global_load_lds_dwordx4 v26, s[4:5] offset:2048\n" \
  "global_load_lds_dwordx4 v27, s[4:5] offset:3072\n" \
  "s_add_u32 s4, s4, 0x100000\n" \
  "s_addc_u32 s5, s5, 0\n" \
  "v_mfma_f32_16x16x32_bf16 v[64:67], v[100:103], v[100:103], v[64:67]\n" \
  "v_mfma_f32_16x16x32_bf16 v[68:71], v[108:111], v[108:111], v[68:71]\n" \
  "v_mfma_f32_16x16x32_bf16 v[72:75], v[116:119], v[116:119], v[72:75]\n" \
  "v_mfma_f32_16x16x32_bf16 v[76:79], v[124:127], v[124:127], v[76:79]\n" \
  "s_waitcnt vmcnt(8) lgkmcnt(0)\n" \
  "s_barrier\n" \
  "ds_read_b64_tr_b16 v[36:37], v20 offset:12288\n" \
  "ds_read_b64_tr_b16 v[38:39], v20 offset:14336\n" \
  "ds_read_b64_tr_b16 v[44:45], v21 offset:12288\n" \
  "ds_read_b64_tr_b16 v[46:47], v21 offset:14336\n" \
  "ds_read_b64_tr_b16 v[52:53], v22 offset:12288\n" \
  "ds_read_b64_tr_b16 v[54:55], v22 offset:14336\n" \
  "ds_read_b64_tr_b16 v[60:61], v23 offset:12288\n" \
  "ds_read_b64_tr_b16 v[62:63], v23 offset:14336\n" \
  "s_add_u32 m0, s19, 8192\n" \
  "s_nop 0\n" \
  "global_load_lds_dwordx4 v24, s[4:5] offset:0\n" \
  "global_load_lds_dwordx4 v25, s[4:5] offset:1024\n" \
  "global_load_lds_dwordx4 v26, s[4:5] offset:2048\n" \
  "global_load_lds_dwordx4 v27, s[4:5] offset:3072\n" \
  "s_add_u32 s4, s4, 0x100000\n" \
  "s_addc_u32 s5, s5, 0\n" \
  "v_mfma_f32_16x16x32_bf16 v[64:67], v[32:35], v[32:35], v[64:67]\n" \
  "v_mfma_f32_16x16x32_bf16 v[68:71], v[40:43], v[40:43], v[68:71]\n" \
  "v_mfma_f32_16x16x32_bf16 v[72:75], v[48:51], v[48:51], v[72:75]\n" \
  "v_mfma_f32_16x16x32_bf16 v[76:79], v[56:59], v[56:59], v[76:79]\n" \
  "s_waitcnt vmcnt(8) lgkmcnt(0)\n" \
  "s_barrier\n" \
  "s_memrealtime s[24:25]\n" \
  "ds_read_b64_tr_b16 v[128:129], v18 offset:0\n" \
  "ds_read_b64_tr_b16 v[130:131], v18 offset:512\n" \
  "ds_read_b64_tr_b16 v[132:133], v18 offset:32768\n" \
  "ds_read_b64_tr_b16 v[134:135], v18 offset:33280\n" \
  "ds_read_b64_tr_b16 v[136:137], v19 offset:0\n" \
  "ds_read_b64_tr_b16 v[138:139], v19 offset:512\n" \
  "ds_read_b64_tr_b16 v[140:141], v19 offset:32768\n" \
  "ds_read_b64_tr_b16 v[142:143], v19 offset:33280\n" \
  "ds_read_b64_tr_b16 v[192:193], v28 offset:0\n" \
  "ds_read_b64_tr_b16 v[194:195], v28 offset:512\n" \
  "ds_read_b64_tr_b16 v[196:197], v28 offset:32768\n" \
  "ds_read_b64_tr_b16 v[198:199], v28 offset:33280\n" \
  "ds_read_b64_tr_b16 v[200:201], v17 offset:0\n" \
  "ds_read_b64_tr_b16 v[202:203], v17 offset:512\n" \
  "ds_read_b64_tr_b16 v[204:205], v17 offset:32768\n" \
  "ds_read_b64_tr_b16 v[206:207], v17 offset:33280\n" \
  "s_add_u32 m0, s19, 12288\n" \
  "s_nop 0\n" \
  "global_load_lds_dwordx4 v24, s[4:5] offset:0\n" \
  "global_load_lds_dwordx4 v25, s[4:5] offset:1024\n" \
  "global_load_lds_dwordx4 v26, s[4:5] offset:2048\n" \
  "global_load_lds_dwordx4 v27, s[4:5] offset:3072\n" \
  "s_add_u32 s4, s4, 0x100000\n" \
  "s_addc_u32 s5, s5, 0\n" \
  "v_mfma_f32_16x16x32_bf16 v[64:67], v[36:39], v[36:39], v[64:67]\n" \
  "v_mfma_f32_16x16x32_bf16 v[68:71], v[44:47], v[44:47], v[68:71]\n" \
  "v_mfma_f32_16x16x32_bf16 v[72:75], v[52:55], v[52:55], v[72:75]\n" \
  "v_mfma_f32_16x16x32_bf16 v[76:79], v[60:63], v[60:63], v[76:79]\n" \
  "s_nop 7\n" \
  "v_max3_f32 v13, v64, v65, v66\n" \
  "v_max3_f32 v13, v13, v67, v68\n" \
  "v_max3_f32 v13, v13, v69, v70\n" \
  "v_max3_f32 v13, v13, v71, v72\n" \
  "v_max3_f32 v13, v13, v73, v74\n" \
  "v_max3_f32 v13, v13, v75, v76\n" \
  "v_max3_f32 v13, v13, v77, v78\n" \
  "v_max_f32 v13, v13, v79\n" \
  "s_nop 1\n" \
  "v_max_f32_dpp v13, v13, v13 row_ror:1 row_mask:0xf bank_mask:0xf\n" \
  "s_nop 1\n" \
  "v_max_f32_dpp v13, v13, v13 row_ror:2 row_mask:0xf bank_mask:0xf\n" \
  "s_nop 1\n" \
  "v_max_f32_dpp v13, v13, v13 row_ror:4 row_mask:0xf bank_mask:0xf\n" \
  "s_nop 1\n" \
  "v_max_f32_dpp v13, v13, v13 row_ror:8 row_mask:0xf bank_mask:0xf\n" \
  "v_mov_b32 v31, v13\n" \
  "s_nop 1\n" \
  "v_permlane16_swap_b32 v13, v31\n" \
  "v_max_f32 v13, v13, v31\n" \
  "v_mov_b32 v31, v13\n" \
  "s_nop 1\n" \
  "v_permlane32_swap_b32 v13, v31\n" \
  "v_max_f32 v13, v13, v31\n" \
  "s_mov_b64 exec, 1\n" \
  "ds_max_u32 v30, v13 offset:32\n" \
  "s_mov_b64 exec, -1\n" \
  "s_waitcnt vmcnt(8) lgkmcnt(0)\n" \
  "s_barrier\n" \
  "ds_read_b64_tr_b16 v[144:145], v18 offset:4096\n" \
  "ds_read_b64_tr_b16 v[146:147], v18 offset:4608\n" \
  "ds_read_b64_tr_b16 v[148:149], v18 offset:36864\n" \
  "ds_read_b64_tr_b16 v[150:151], v18 offset:37376\n" \
  "ds_read_b64_tr_b16 v[152:153], v19 offset:4096\n" \
  "ds_read_b64_tr_b16 v[154:155], v19 offset:4608\n" \
  "ds_read_b64_tr_b16 v[156:157], v19 offset:36864\n" \
  "ds_read_b64_tr_b16 v[158:159], v19 offset:37376\n" \
  "ds_read_b64_tr_b16 v[208:209], v28 offset:4096\n" \
  "ds_read_b64_tr_b16 v[210:211], v28 offset:4608\n" \
  "ds_read_b64_tr_b16 v[212:213], v28 offset:36864\n" \
  "ds_read_b64_tr_b16 v[214:215], v28 offset:37376\n" \
  "ds_read_b64_tr_b16 v[216:217], v17 offset:4096\n" \
  "ds_read_b64_tr_b16 v[218:219], v17 offset:4608\n" \
  "ds_read_b64_tr_b16 v[220:221], v17 offset:36864\n" \
  "ds_read_b64_tr_b16 v[222:223], v17 offset:37376\n" \
  "s_add_u32 m0, s19, 0\n" \
  "s_nop 0\n" \
  "global_load_lds_dwordx4 v24, s[6:7] offset:0\n" \
  "global_load_lds_dwordx4 v25, s[6:7] offset:1024\n" \
  "global_load_lds_dwordx4 v26, s[6:7] offset:2048\n" \
  "global_load_lds_dwordx4 v27, s[6:7] offset:3072\n" \
  "s_add_u32 s6, s6, 0x100000\n" \
  "s_addc_u32 s7, s7, 0\n" \
  "v_mfma_f32_16x16x32_bf16 v[64:67], v[128:131], v[128:131], 0\n" \
  "v_mfma_f32_16x16x32_bf16 v[68:71], v[192:195], v[192:195], 0\n" \
  "v_mfma_f32_16x16x32_bf16 v[64:67], v[132:135], v[132:135], v[64:67]\n" \
  "v_mfma_f32_16x16x32_bf16 v[68:71], v[196:199], v[196:199], v[68:71]\n" \
  "v_mfma_f32_16x16x32_bf16 v[64:67], v[136:139], v[136:139], v[64:67]\n" \
  "v_mfma_f32_16x16x32_bf16 v[68:71], v[200:203], v[200:203], v[68:71]\n" \
  "v_mfma_f32_16x16x32_bf16 v[64:67], v[140:143], v[140:143], v[64:67]\n" \
  "v_mfma_f32_16x16x32_bf16 v[68:71], v[204:207], v[204:207], v[68:71]\n" \
  "s_waitcnt vmcnt(8) lgkmcnt(0)\n" \
  "s_barrier\n" \
  "ds_read_b64_tr_b16 v[160:161], v18 offset:8192\n" \
  "ds_read_b64_tr_b16 v[162:163], v18 offset:8704\n" \
  "ds_read_b64_tr_b16 v[164:165], v18 offset:40960\n" \
  "ds_read_b64_tr_b16 v[166:167], v18 offset:41472\n" \
  "ds_read_b64_tr_b16 v[168:169], v19 offset:8192\n" \
  "ds_read_b64_tr_b16 v[170:171], v19 offset:8704\n" \
  "ds_read_b64_tr_b16 v[172:173], v19 offset:40960\n" \
  "ds_read_b64_tr_b16 v[174:175], v19 offset:41472\n" \
  "ds_read_b64_tr_b16 v[224:225], v28 offset:8192\n" \
  "ds_read_b64_tr_b16 v[226:227], v28 offset:8704\n" \
  "ds_read_b64_tr_b16 v[228:229], v28 offset:40960\n" \
  "ds_read_b64_tr_b16 v[230:231], v28 offset:41472\n" \
  "ds_read_b64_tr_b16 v[232:233], v17 offset:8192\n" \
  "ds_read_b64_tr_b16 v[234:235], v17 offset:8704\n" \
  "ds_read_b64_tr_b16 v[236:237], v17 offset:40960\n" \
  "ds_read_b64_tr_b16 v[238:239], v17 offset:41472\n" \
  "s_add_u32 m0, s19, 4096\n" \
  "s_nop 0\n" \
  "global_load_lds_dwordx4 v24, s[6:7] offset:0\n" \
  "global_load_lds_dwordx4 v25, s[6:7] offset:1024\n" \
  "global_load_lds_dwordx4 v26, s[6:7] offset:2048\n" \
  "global_load_lds_dwordx4 v27, s[6:7] offset:3072\n" \
  "s_add_u32 s6, s6, 0x100000\n" \
  "s_addc_u32 s7, s7, 0\n" \
  "v_mfma_f32_16x16x32_bf16 v[64:67], v[144:147], v[144:147], v[64:67]\n" \
  "v_mfma_f32_16x16x32_bf16 v[68:71], v[208:211], v[208:211], v[68:71]\n" \
  "v_mfma_f32_16x16x32_bf16 v[64:67], v[148:151], v[148:151], v[64:67]\n" \
  "v_mfma_f32_16x16x32_bf16 v[68:71], v[212:215], v[212:215], v[68:71]\n" \
  "v_mfma_f32_16x16x32_bf16 v[64:67], v[152:155], v[152:155], v[64:67]\n" \
  "v_mfma_f32_16x16x32_bf16 v[68:71], v[216:219], v[216:219], v[68:71]\n" \
  "v_mfma_f32_16x16x32_bf16 v[64:67], v[156:159], v[156:159], v[64:67]\n" \
  "v_mfma_f32_16x16x32_bf16 v[68:71], v[220:223], v[220:223], v[68:71]\n" \
  "s_waitcnt vmcnt(8) lgkmcnt(0)\n" \
  "s_barrier\n" \
  "ds_read_b64_tr_b16 v[176:177], v18 offset:12288\n" \
  "ds_read_b64_tr_b16 v[178:179], v18 offset:12800\n" \
  "ds_read_b64_tr_b16 v[180:181], v18 offset:45056\n" \
  "ds_read_b64_tr_b16 v[182:183], v18 offset:45568\n" \
  "ds_read_b64_tr_b16 v[184:185], v19 offset:12288\n" \
  "ds_read_b64_tr_b16 v[186:187], v19 offset:12800\n" \
  "ds_read_b64_tr_b16 v[188:189], v19 offset:45056\n" \
  "ds_read_b64_tr_b16 v[190:191], v19 offset:45568\n" \
  "ds_read_b64_tr_b16 v[240:241], v28 offset:12288\n" \
  "ds_read_b64_tr_b16 v[242:243], v28 offset:12800\n" \
  "ds_read_b64_tr_b16 v[244:245], v28 offset:45056\n" \
  "ds_read_b64_tr_b16 v[246:247], v28 offset:45568\n" \
  "ds_read_b64_tr_b16 v[248:249], v17 offset:12288\n" \
  "ds_read_b64_tr_b16 v[250:251], v17 offset:12800\n" \
  "ds_read_b64_tr_b16 v[252:253], v17 offset:45056\n" \
  "ds_read_b64_tr_b16 v[254:255], v17 offset:45568\n" \
  "s_add_u32 m0, s19, 8192\n" \
  "s_nop 0\n" \
  "global_load_lds_dwordx4 v24, s[6:7] offset:0\n" \
  "global_load_lds_dwordx4 v25, s[6:7] offset:1024\n" \
  "global_load_lds_dwordx4 v26, s[6:7] offset:2048\n" \
  "global_load_lds_dwordx4 v27, s[6:7] offset:3072\n" \
  "s_add_u32 s6, s6, 0x100000\n" \
  "s_addc_u32 s7, s7, 0\n" \
  "v_mfma_f32_16x16x32_bf16 v[64:67], v[160:163], v[160:163], v[64:67]\n" \
  "v_mfma_f32_16x16x32_bf16 v[68:71], v[224:227], v[224:227], v[68:71]\n" \
  "v_mfma_f32_16x16x32_bf16 v[64:67], v[164:167], v[164:167], v[64:67]\n" \
  "v_mfma_f32_16x16x32_bf16 v[68:71], v[228:231], v[228:231], v[68:71]\n" \
  "v_mfma_f32_16x16x32_bf16 v[64:67], v[168:171], v[168:171], v[64:67]\n" \
  "v_mfma_f32_16x16x32_bf16 v[68:71], v[232:235], v[232:235], v[68:71]\n" \
  "v_mfma_f32_16x16x32_bf16 v[64:67], v[172:175], v[172:175], v[64:67]\n" \
  "v_mfma_f32_16x16x32_bf16 v[68:71], v[236:239], v[236:239], v[68:71]\n" \
  "s_waitcnt vmcnt(8) lgkmcnt(0)\n" \
  "s_barrier\n" \
  "s_memrealtime s[26:27]\n" \
  "s_add_u32 m0, s19, 12288\n" \
  "s_nop 0\n" \
  "global_load_lds_dwordx4 v24, s[6:7] offset:0\n" \
  "global_load_lds_dwordx4 v25, s[6:7] offset:1024\n" \
  "global_load_lds_dwordx4 v26, s[6:7] offset:2048\n" \
  "global_load_lds_dwordx4 v27, s[6:7] offset:3072\n" \
  "s_add_u32 s6, s6, 0x100000\n" \
  "s_addc_u32 s7, s7, 0\n" \
  "v_mfma_f32_16x16x32_bf16 v[64:67], v[176:179], v[176:179], v[64:67]\n" \
  "v_mfma_f32_16x16x32_bf16 v[68:71], v[240:243], v[240:243], v[68:71]\n" \
  "v_mfma_f32_16x16x32_bf16 v[64:67], v[180:183], v[180:183], v[64:67]\n" \
  "v_mfma_f32_16x16x32_bf16 v[68:71], v[244:247], v[244:247], v[68:71]\n" \
  "v_mfma_f32_16x16x32_bf16 v[64:67], v[184:187], v[184:187], v[64:67]\n" \
  "v_mfma_f32_16x16x32_bf16 v[68:71], v[248:251], v[248:251], v[68:71]\n" \
  "v_mfma_f32_16x16x32_bf16 v[64:67], v[188:191], v[188:191], v[64:67]\n" \
  "v_mfma_f32_16x16x32_bf16 v[68:71], v[252:255], v[252:255], v[68:71]\n" \
  "s_nop 7\n" \
  "s_mov_b32 exec_lo, 0x100001\n" \
  "s_mov_b32 exec_hi, 0x10000100\n" \
  "v_add_f32 v12, v64, v68\n" \
  "s_mov_b32 exec_lo, 0x200002\n" \
  "s_mov_b32 exec_hi, 0x20000200\n" \
  "v_add_f32 v12, v65, v69\n" \
  "s_mov_b32 exec_lo, 0x400004\n" \
  "s_mov_b32 exec_hi, 0x40000400\n" \
  "v_add_f32 v12, v66, v70\n" \
  "s_mov_b32 exec_lo, 0x800008\n" \
  "s_mov_b32 exec_hi, 0x80000800\n" \
  "v_add_f32 v12, v67, v71\n" \
  "s_mov_b64 exec, -1\n" \
  "s_nop 1\n" \
  "v_add_f32_dpp v12, v12, v12 row_ror:1 row_mask:0xf bank_mask:0xf\n" \
  "s_nop 1\n" \
  "v_add_f32_dpp v12, v12, v12 row_ror:2 row_mask:0xf bank_mask:0xf\n" \
  "s_nop 1\n" \
  "v_add_f32_dpp v12, v12, v12 row_ror:4 row_mask:0xf bank_mask:0xf\n" \
  "s_nop 1\n" \
  "v_add_f32_dpp v12, v12, v12 row_ror:8 row_mask:0xf bank_mask:0xf\n" \
  "v_mov_b32 v31, v12\n" \
  "s_nop 1\n" \
  "v_permlane16_swap_b32 v12, v31\n" \
  "v_add_f32 v12, v12, v31\n" \
  "v_mov_b32 v31, v12\n" \
  "s_nop 1\n" \
  "v_permlane32_swap_b32 v12, v31\n" \
  "v_add_f32 v12, v12, v31\n" \
  "v_lshl_add_u32 v31, s73, 2, v30\n" \
  "s_mov_b64 exec, 1\n" \
  "ds_write_b32 v31, v12\n" \
  "s_mov_b64 exec, -1\n" \
  "s_waitcnt vmcnt(8) lgkmcnt(0)\n" \
  "s_barrier\n" \
  "s_add_u32 m0, s19, 0\n" \
  "s_nop 0\n" \
  "global_load_lds_dwordx4 v24, s[8:9] offset:0\n" \
  "global_load_lds_dwordx4 v25, s[8:9] offset:1024\n" \
  "global_load_lds_dwordx4 v26, s[8:9] offset:2048\n" \
  "global_load_lds_dwordx4 v27, s[8:9] offset:3072\n" \
  "s_add_u32 s8, s8, 0x100000\n" \
  "s_addc_u32 s9, s9, 0\n" \
  "ds_read_b128 v[20:23], v30 offset:16\n" \
  "ds_read_b32 v31, v30 offset:32\n" \
  "v_mbcnt_lo_u32_b32 v16, -1, 0\n" \
  "v_mbcnt_hi_u32_b32 v16, -1, v16\n" \
  "v_lshlrev_b32 v15, 8, v16\n" \
  "s_waitcnt lgkmcnt(0)\n" \
  "v_add_f32 v20, v20, v21\n" \
  "v_add_f32 v22, v22, v23\n" \
  "v_add_f32 v20, v20, v22\n" \
  "v_mul_f32 v31, v20, v31\n" \
  "v_mul_f32 v31, 0x3f8147ae, v31\n" \
  "v_mov_b32 v12, s10\n" \
  "v_mov_b32 v13, 0xffff\n" \
  "v_bfi_b32 v13, v13, s11, v31\n" \
  "s_mov_b32 exec_hi, 0\n" \
  "s_mov_b32 exec_lo, s18\n" \
  "global_store_dwordx2 v15, v[12:13], s[16:17] sc1\n" \
  "s_mov_b64 exec, -1\n" \
  "v_lshlrev_b32 v16, 3, v16\n" \
  "s_waitcnt vmcnt(8) lgkmcnt(0)\n" \
  "s_barrier\n" \
  "s_add_u32 m0, s19, 4096\n" \
  "s_nop 0\n" \
  "global_load_lds_dwordx4 v24, s[8:9] offset:0\n" \
  "global_load_lds_dwordx4 v25, s[8:9] offset:1024\n" \
  "global_load_lds_dwordx4 v26, s[8:9] offset:2048\n" \
  "global_load_lds_dwordx4 v27, s[8:9] offset:3072\n" \
  "s_add_u32 s8, s8, 0x100000\n" \
  "s_addc_u32 s9, s9, 0\n" \
  "global_load_dwordx2 v[20:21], v16, s[12:13] sc1\n" \
  "s_waitcnt vmcnt(9) lgkmcnt(0)\n" \
  "s_barrier\n" \
  "s_add_u32 m0, s19, 8192\n" \
  "s_nop 0\n" \
  "global_load_lds_dwordx4 v24, s[8:9] offset:0\n" \
  "global_load_lds_dwordx4 v25, s[8:9] offset:1024\n" \
  "global_load_lds_dwordx4 v26, s[8:9] offset:2048\n" \
  "global_load_lds_dwordx4 v27, s[8:9] offset:3072\n" \
  "s_add_u32 s8, s8, 0x100000\n" \
  "s_addc_u32 s9, s9, 0\n" \
  "s_waitcnt vmcnt(9) lgkmcnt(0)\n" \
  "s_barrier\n" \
  "s_memrealtime s[28:29]\n" \
  "ds_read_b64_tr_b16 v[64:65], v28 offset:0\n" \
  "ds_read_b64_tr_b16 v[66:67], v28 offset:512\n" \
  "ds_read_b64_tr_b16 v[68:69], v28 offset:32768\n" \
  "ds_read_b64_tr_b16 v[70:71], v28 offset:33280\n" \
  "ds_read_b64_tr_b16 v[72:73], v17 offset:0\n" \
  "ds_read_b64_tr_b16 v[74:75], v17 offset:512\n" \
  "ds_read_b64_tr_b16 v[76:77], v17 offset:32768\n" \
  "ds_read_b64_tr_b16 v[78:79], v17 offset:33280\n" \
  "s_add_u32 m0, s19, 12288\n" \
  "s_nop 0\n" \
  "global_load_lds_dwordx4 v24, s[8:9] offset:0\n" \
  "global_load_lds_dwordx4 v25, s[8:9] offset:1024\n" \
  "global_load_lds_dwordx4 v26, s[8:9] offset:2048\n" \
  "global_load_lds_dwordx4 v27, s[8:9] offset:3072\n" \
  "s_add_u32 s8, s8, 0x100000\n" \
  "s_addc_u32 s9, s9, 0\n" \
  "s_waitcnt vmcnt(8)\n" \
  ".LCV_RD0_%=:\n" \
  "v_cmp_ne_u32 vcc, s10, v20\n" \
  "v_xor_b32 v31, s11, v21\n" \
  "v_cmp_ne_u32 s[22:23], 0, v31\n" \
  "s_or_b64 vcc, vcc, s[22:23]\n" \
  "s_cbranch_scc0 .LCV_RD1_%=\n" \
  "s_sleep 8\n" \
  "global_load_dwordx2 v[20:21], v16, s[12:13] sc1\n" \
  "s_sub_u32 s87, s87, 1\n" \
  "s_cbranch_scc1 .LCV_END_%=\n" \
  "s_waitcnt vmcnt(0)\n" \
  "s_branch .LCV_RD0_%=\n" \
  ".LCV_RD1_%=:\n" \
  "s_waitcnt vmcnt(8) lgkmcnt(0)\n" \
  "s_barrier\n" \
  "ds_write_b128 v3, v[96:99] offset:0\n" \
  "ds_write_b128 v3, v[100:103] offset:1024\n" \
  "ds_write_b128 v3, v[104:107] offset:2048\n" \
  "ds_write_b128 v3, v[108:111] offset:3072\n" \
  "ds_read_b64_tr_b16 v[80:81], v28 offset:4096\n" \
  "ds_read_b64_tr_b16 v[82:83], v28 offset:4608\n" \
  "ds_read_b64_tr_b16 v[84:85], v28 offset:36864\n" \
  "ds_read_b64_tr_b16 v[86:87], v28 offset:37376\n" \
  "ds_read_b64_tr_b16 v[88:89], v17 offset:4096\n" \
  "ds_read_b64_tr_b16 v[90:91], v17 offset:4608\n" \
  "ds_read_b64_tr_b16 v[92:93], v17 offset:36864\n" \
  "ds_read_b64_tr_b16 v[94:95], v17 offset:37376\n" \
  "v_and_b32 v16, 0xff, v16\n" \
  "global_load_dwordx2 v[22:23], v16, s[14:15] sc1\n" \
  "s_mov_b64 exec, 15\n" \
  "buffer_load_dword v13, v2, s[44:47], s68 offen sc1\n" \
  "s_mov_b64 exec, -1\n" \
  "s_waitcnt vmcnt(6) lgkmcnt(0)\n" \
  "s_barrier\n" \
  "ds_write_b128 v3, v[112:115] offset:4096\n" \
  "ds_write_b128 v3, v[116:119] offset:5120\n" \
  "ds_write_b128 v3, v[120:123] offset:6144\n" \
  "ds_write_b128 v3, v[124:127] offset:7168\n" \
  "ds_read_b64_tr_b16 v[96:97], v28 offset:8192\n" \
  "ds_read_b64_tr_b16 v[98:99], v28 offset:8704\n" \
  "ds_read_b64_tr_b16 v[100:101], v28 offset:40960\n" \
  "ds_read_b64_tr_b16 v[102:103], v28 offset:41472\n" \
  "ds_read_b64_tr_b16 v[104:105], v17 offset:8192\n" \
  "ds_read_b64_tr_b16 v[106:107], v17 offset:8704\n" \
  "ds_read_b64_tr_b16 v[108:109], v17 offset:40960\n" \
  "ds_read_b64_tr_b16 v[110:111], v17 offset:41472\n" \
  "s_waitcnt vmcnt(0)\n" \
  "v_cmp_eq_u32 vcc, 0, v13\n" \
  "s_and_b32 s90, vcc_lo, 15\n" \
  "s_cbranch_scc1 .LCV_GL0_%=\n" \
  "s_mov_b32 m0, s99\n" \
  "s_add_u32 s88, s99, 8192\n" \
  "global_load_lds_dwordx4 v10, s[62:63] sc1\n" \
  "global_load_lds_dwordx4 v10, s[62:63] offset:1024 sc1\n" \
  "s_mov_b32 m0, s88\n" \
  "s_nop 0\n" \
  "global_load_lds_dwordx4 v10, s[64:65] sc1\n" \
  "global_load_lds_dwordx4 v10, s[64:65] offset:1024 sc1\n" \
  ".LCV_GL0_%=:\n" \
  "v_cmp_ne_u32 vcc, s10, v22\n" \
  "v_xor_b32 v31, s11, v23\n" \
  "v_and_b32 v31, 0xffff, v31\n" \
  "v_cmp_ne_u32 s[22:23], 0, v31\n" \
  "s_or_b64 vcc, vcc, s[22:23]\n" \
  "s_cbranch_scc0 .LCV_GL1_%=\n" \
  "s_sleep 12\n" \
  "global_load_dwordx2 v[22:23], v16, s[14:15] sc1\n" \
  "s_sub_u32 s87, s87, 1\n" \
  "s_cbranch_scc1 .LCV_END_%=\n" \
  "s_waitcnt vmcnt(0)\n" \
  "s_branch .LCV_GL0_%=\n" \
  ".LCV_GL1_%=:\n" \
  "v_and_b32 v31, 0xffff0000, v23\n" \
  "s_nop 1\n" \
  "v_max_u32_dpp v31, v31, v31 row_ror:1 row_mask:0xf bank_mask:0xf\n" \
  "s_nop 1\n" \
  "v_max_u32_dpp v31, v31, v31 row_ror:2 row_mask:0xf bank_mask:0xf\n" \
  "s_nop 1\n" \
  "v_max_u32_dpp v31, v31, v31 row_ror:4 row_mask:0xf bank_mask:0xf\n" \
  "s_nop 1\n" \
  "v_max_u32_dpp v31, v31, v31 row_ror:8 row_mask:0xf bank_mask:0xf\n" \
  "v_mov_b32 v12, v31\n" \
  "s_nop 1\n" \
  "v_permlane16_swap_b32 v31, v12\n" \
  "v_max_u32 v31, v31, v12\n" \
  "v_mov_b32 v12, v31\n" \
  "s_nop 1\n" \
  "v_permlane32_swap_b32 v31, v12\n" \
  "v_max_u32 v31, v31, v12\n" \
  "s_mov_b64 exec, 1\n" \
  "ds_write_b32 v30, v31\n" \
  "s_mov_b64 exec, -1\n" \
  "s_waitcnt vmcnt(4) lgkmcnt(0)\n" \
  "s_barrier\n" \
  "ds_write_b128 v3, v[32:35] offset:8192\n" \
  "ds_write_b128 v3, v[36:39] offset:9216\n" \
  "ds_write_b128 v3, v[40:43] offset:10240\n" \
  "ds_write_b128 v3, v[44:47] offset:11264\n" \
  "ds_read_b64_tr_b16 v[112:113], v28 offset:12288\n" \
  "ds_read_b64_tr_b16 v[114:115], v28 offset:12800\n" \
  "ds_read_b64_tr_b16 v[116:117], v28 offset:45056\n" \
  "ds_read_b64_tr_b16 v[118:119], v28 offset:45568\n" \
  "ds_read_b64_tr_b16 v[120:121], v17 offset:12288\n" \
  "ds_read_b64_tr_b16 v[122:123], v17 offset:12800\n" \
  "ds_read_b64_tr_b16 v[124:125], v17 offset:45056\n" \
  "ds_read_b64_tr_b16 v[126:127], v17 offset:45568\n" \
  "ds_read_b32 v31, v30 offset:0\n" \
  "s_waitcnt lgkmcnt(0)\n" \
  "s_barrier\n" \
  "ds_write_b128 v3, v[48:51] offset:12288\n" \
  "ds_write_b128 v3, v[52:55] offset:13312\n" \
  "ds_write_b128 v3, v[56:59] offset:14336\n" \
  "ds_write_b128 v3, v[60:63] offset:15360\n" \
  "v_mul_f32 v31, 0x3f8e353f, v31\n" \
  "v_mul_f32 v30, 0x44870000, v31\n" \
  "v_mov_b64 v[12:13], 0\n" \
  "v_mov_b64 v[14:15], 0\n" \
  "v_mov_b64 v[16:17], 0\n" \
  "v_mov_b64 v[18:19], 0\n" \
  "v_mov_b64 v[20:21], 0\n" \
  "v_mov_b64 v[22:23], 0\n" \
  "v_readfirstlane_b32 s72, v31\n" \
  "v_readfirstlane_b32 s86, v30\n" \
  "s_lshr_b32 s86, s86, 23\n" \
  "s_addk_i32 s86, 0xff83\n" \
  "s_ashr_i32 s86, s86, 1\n" \
  "s_max_i32 s86, s86, -20\n" \
  "s_min_i32 s86, s86, 100\n" \
  "s_mov_b32 s82, s86\n" \
  "s_lshr_b32 s83, s72, 23\n" \
  "s_sub_i32 s83, s83, 243\n" \
  "s_memrealtime s[30:31]\n" \
  "s_waitcnt lgkmcnt(0)\n" \
  "s_mov_b64 exec, 1\n" \
  "v_mov_b32 v24, 0\n" \
  "v_mov_b32 v26, s24\n" \
  "v_mov_b32 v27, s25\n" \
  "global_store_dwordx2 v24, v[26:27], s[20:21] offset:0\n" \
  "v_add_u32 v24, 0, v24\n" \
  "v_mov_b32 v26, s26\n" \
  "v_mov_b32 v27, s27\n" \
  "global_store_dwordx2 v24, v[26:27], s[20:21] offset:2048\n" \
  "v_add_u32 v24, 4096, v24\n" \
  "v_mov_b32 v26, s28\n" \
  "v_mov_b32 v27, s29\n" \
  "global_store_dwordx2 v24, v[26:27], s[20:21] offset:0\n" \
  "v_add_u32 v24, 0, v24\n" \
  "v_mov_b32 v26, s30\n" \
  "v_mov_b32 v27, s31\n" \
  "global_store_dwordx2 v24, v[26:27], s[20:21] offset:2048\n" \
  "v_add_u32 v24, 4096, v24\n" \
  "s_mov_b64 exec, -1\n" \
  "s_waitcnt lgkmcnt(0)\n" \
  "s_add_u32 s88, s74, 157760\n" \
  "v_mov_b32 v59, s88\n" \
  "s_cmp_lg_u32 s61, 0\n" \
  "s_movk_i32 s76, -8000\n" \
  "s_cmovk_i32 s76, 9152\n" \
  "s_movk_i32 s77, 90\n" \
  "s_cmovk_i32 s77, 90\n" \
  "s_cselect_b32 s72, -1, 55\n" \
  "s_mov_b32 s87, 0x40000\n" \
  "s_cmp_eq_u32 s90, 0\n" \
  "s_cbranch_scc1 .LCV_W1_%=\n" \
  "s_branch .LCV_W2_%=\n" \
  ".LCV_W0_%=:\n" \
  "s_sleep 2\n" \
  "s_cmp_eq_u32 s61, 0\n" \
  "s_cbranch_scc1 .LCV_W2_%=\n" \
  "s_sleep 32\n" \
  ".LCV_W2_%=:\n" \
  "s_mov_b64 exec, 15\n" \
  "buffer_load_dword v58, v2, s[44:47], s68 offen sc1\n" \
  "s_mov_b64 exec, -1\n" \
  "s_sub_u32 s87, s87, 1\n" \
  "s_cbranch_scc1 .LCV_END_%=\n" \
  "s_waitcnt vmcnt(0)\n" \
  "v_cmp_eq_u32 vcc, 0, v58\n" \
  "s_and_b32 vcc_lo, vcc_lo, 15\n" \
  "s_cbranch_scc1 .LCV_W0_%=\n" \
  "s_mov_b32 m0, s99\n" \
  "s_add_u32 s88, s99, 8192\n" \
  "global_load_lds_dwordx4 v10, s[62:63] sc1\n" \
  "global_load_lds_dwordx4 v10, s[62:63] offset:1024 sc1\n" \
  "s_mov_b32 m0, s88\n" \
  "s_nop 0\n" \
  "global_load_lds_dwordx4 v10, s[64:65] sc1\n" \
  "global_load_lds_dwordx4 v10, s[64:65] offset:1024 sc1\n" \
  ".LCV_W1_%=:\n" \
  "s_add_u32 s62, s62, 0x80000\n" \
  "s_addc_u32 s63, s63, 0\n" \
  "s_add_u32 s64, s64, 0x40000\n" \
  "s_addc_u32 s65, s65, 0\n" \
  "s_add_u32 s68, s68, 0x1000\n" \
  "s_cmp_eq_u32 s61, 0\n" \
  "s_cbranch_scc1 .LCV_W4_%=\n" \
  "s_add_u32 s38, s67, 4992\n" \
  "v_mov_b32 v58, s38\n" \
  "buffer_load_dword v58, v58, s[0:3], 0 offen sc1\n" \
  "s_waitcnt vmcnt(0)\n" \
  "v_readfirstlane_b32 vcc_lo, v58\n" \
  "s_movk_i32 s87, 200\n" \
  "s_cmp_eq_u32 vcc_lo, 0\n" \
  "s_cbranch_scc1 .LCV_W4_%=\n" \
  "s_add_u32 vcc_lo, vcc_lo, 590\n" \
  ".LCV_W3_%=:\n" \
  "s_sleep 2\n" \
  "s_memrealtime s[38:39]\n" \
  "s_sub_u32 s87, s87, 1\n" \
  "s_cbranch_scc1 .LCV_W4_%=\n" \
  "s_waitcnt lgkmcnt(0)\n" \
  "s_sub_u32 s38, s38, vcc_lo\n" \
  "s_cmp_lt_i32 s38, 0\n" \
  "s_cbranch_scc1 .LCV_W3_%=\n" \
  ".LCV_W4_%=:\n" \
  "s_waitcnt vmcnt(0)\n" \
  ".p2align 6\n" \
  ".LCV_TOP_%=:\n" \
  "s_waitcnt lgkmcnt(0)\n" \
  "s_barrier\n" \
  "s_setprio 3\n" \
  "ds_read_b128 v[24:27], v1 offset:0\n" \
  "ds_read_b128 v[28:31], v1 offset:1024\n" \
  "ds_read_b128 v[32:35], v1 offset:2048\n" \
  "ds_read_b128 v[36:39], v1 offset:3072\n" \
  "ds_read_b128 v[40:43], v1 offset:4096\n" \
  "ds_read_b128 v[44:47], v1 offset:5120\n" \
  "ds_read_b128 v[48:51], v1 offset:6144\n" \
  "ds_read_b128 v[52:55], v1 offset:7168\n" \
  "s_memrealtime s[38:39]\n" \
  "s_waitcnt lgkmcnt(7)\n" \
  "v_smfmac_f32_16x16x64_bf16 v[12:15], v[24:27], v[64:71], v0\n" \
  "s_waitcnt lgkmcnt(6)\n" \
  "v_smfmac_f32_16x16x64_bf16 v[12:15], v[28:31], v[72:79], v0\n" \
  "s_waitcnt lgkmcnt(5)\n" \
  "v_smfmac_f32_16x16x64_bf16 v[12:15], v[32:35], v[80:87], v0\n" \
  "s_waitcnt lgkmcnt(4)\n" \
  "v_smfmac_f32_16x16x64_bf16 v[12:15], v[36:39], v[88:95], v0\n" \
  "s_waitcnt lgkmcnt(3)\n" \
  "v_smfmac_f32_16x16x64_bf16 v[12:15], v[40:43], v[96:103], v0\n" \
  "s_waitcnt lgkmcnt(2)\n" \
  "v_smfmac_f32_16x16x64_bf16 v[12:15], v[44:47], v[104:111], v0\n" \
  "s_waitcnt lgkmcnt(1)\n" \
  "v_smfmac_f32_16x16x64_bf16 v[12:15], v[48:51], v[112:119], v0\n" \
  "s_waitcnt lgkmcnt(0)\n" \
  "v_smfmac_f32_16x16x64_bf16 v[12:15], v[52:55], v[120:127], v0\n" \
  "ds_read_b32 v11, v1 offset:26688\n" \
  "s_mov_b32 s61, s38\n" \
  "s_add_u32 s38, s38, s79\n" \
  "s_add_u32 s38, s38, s96\n" \
  "s_mov_b32 s96, 0\n" \
  "v_or_b32 v57, 1, s38\n" \
  "v_add_u32 v56, s67, v10\n" \
  "s_add_u32 s39, s67, s76\n" \
  "s_add_u32 s38, s38, s77\n" \
  "s_bitset1_b32 s38, 0\n" \
  "v_writelane_b32 v56, s39, 1\n" \
  "v_writelane_b32 v57, s38, 1\n" \
  "s_sub_u32 s39, s71, s72\n" \
  "s_cmp_lt_u32 s39, 9\n" \
  "s_cselect_b64 exec, 19, 3\n" \
  "buffer_store_dword v57, v56, s[0:3], 0 offen sc1\n" \
  "s_mov_b64 exec, -1\n" \
  "v_permlane32_swap_b32 v12, v14\n" \
  "v_permlane32_swap_b32 v13, v15\n" \
  "v_pk_add_f32 v[12:13], v[12:13], v[14:15]\n" \
  "s_nop 0\n" \
  "v_cvt_pk_bf16_f32 v12, v12, v13\n" \
  "ds_write_b32 v4, v12\n" \
  "s_setprio 2\n" \
  "v_mov_b64 v[12:13], s[92:93]\n" \
  "v_mov_b64 v[14:15], s[92:93]\n" \
  "s_waitcnt lgkmcnt(1)\n" \
  "v_readfirstlane_b32 s86, v11\n" \
  "s_lshr_b32 s86, s86, 23\n" \
  "s_add_i32 s86, s86, s83\n" \
  "s_ashr_i32 s86, s86, 1\n" \
  "s_max_i32 s86, s86, s82\n" \
  "s_min_i32 s86, s86, 100\n" \
  "s_lshl_b32 s86, s86, 23\n" \
  "s_sub_i32 s84, 0xc8800000, s86\n" \
  "s_cmp_eq_u32 s73, 5\n" \
  "v_smfmac_f32_16x16x64_bf16 v[16:19], v[24:27], v[128:135], v0\n" \
  "v_smfmac_f32_16x16x64_bf16 v[16:19], v[28:31], v[136:143], v0\n" \
  "v_smfmac_f32_16x16x64_bf16 v[16:19], v[32:35], v[144:151], v0\n" \
  "v_smfmac_f32_16x16x64_bf16 v[16:19], v[36:39], v[152:159], v0\n" \
  "v_smfmac_f32_16x16x64_bf16 v[16:19], v[40:43], v[160:167], v0\n" \
  "v_smfmac_f32_16x16x64_bf16 v[16:19], v[44:47], v[168:175], v0\n" \
  "s_waitcnt lgkmcnt(0)\n" \
  "s_barrier\n" \
  "ds_read_b64_tr_b16 v[60:61], v6 offset:0\n" \
  "ds_read_b64_tr_b16 v[62:63], v9 offset:0\n" \
  "v_smfmac_f32_16x16x64_bf16 v[16:19], v[48:51], v[176:183], v0\n" \
  "v_smfmac_f32_16x16x64_bf16 v[16:19], v[52:55], v[184:191], v0\n" \
  "v_smfmac_f32_16x16x64_bf16 v[20:23], v[24:27], v[192:199], v0\n" \
  "v_smfmac_f32_16x16x64_bf16 v[20:23], v[28:31], v[200:207], v0\n" \
  "v_smfmac_f32_16x16x64_bf16 v[20:23], v[32:35], v[208:215], v0\n" \
  "v_smfmac_f32_16x16x64_bf16 v[20:23], v[36:39], v[216:223], v0\n" \
  "v_smfmac_f32_16x16x64_bf16 v[20:23], v[40:43], v[224:231], v0\n" \
  "v_smfmac_f32_16x16x64_bf16 v[20:23], v[44:47], v[232:239], v0\n" \
  "v_smfmac_f32_16x16x64_bf16 v[20:23], v[48:51], v[240:247], v0\n" \
  "v_smfmac_f32_16x16x64_bf16 v[20:23], v[52:55], v[248:255], v0\n" \
  "s_cmp_eq_u32 s71, 0\n" \
  "v_permlane32_swap_b32 v16, v18\n" \
  "v_permlane32_swap_b32 v17, v19\n" \
  "v_add_f32 v16, v16, v18\n" \
  "v_add_f32 v17, v17, v19\n" \
  "v_cvt_pk_bf16_f32 v16, v16, v17\n" \
  "s_waitcnt lgkmcnt(0)\n" \
  "v_max_u32 v60, 1, v60\n" \
  "v_max_u32 v61, 1, v61\n" \
  "v_max_u32 v62, 1, v62\n" \
  "v_max_u32 v63, 1, v63\n" \
  "buffer_store_dwordx4 v[60:63], v7, s[48:51], s69 offen sc1\n" \
  "v_permlane32_swap_b32 v20, v22\n" \
  "v_permlane32_swap_b32 v21, v23\n" \
  "v_pk_add_f32 v[20:21], v[20:21], v[22:23]\n" \
  "v_max_u32 v18, 1, v16\n" \
  "v_cvt_pk_bf16_f32 v17, v20, v21\n" \
  "ds_write_b64 v5, v[16:17]\n" \
  "v_max_u32 v19, 1, v17\n" \
  "s_nop 1\n" \
  "v_permlane32_swap_b32 v18, v19\n" \
  "buffer_store_dwordx2 v[18:19], v8, s[52:55], s70 offen sc1\n" \
  "ds_read_b128 v[24:27], v3 offset:0\n" \
  "ds_read_b128 v[28:31], v3 offset:1024\n" \
  "ds_read_b128 v[32:35], v3 offset:2048\n" \
  "ds_read_b128 v[36:39], v3 offset:3072\n" \
  "ds_read_b128 v[40:43], v3 offset:8192\n" \
  "ds_read_b128 v[44:47], v3 offset:9216\n" \
  "ds_read_b128 v[48:51], v3 offset:10240\n" \
  "ds_read_b128 v[52:55], v3 offset:11264\n" \
  "s_cbranch_scc1 .LCV_LAST_%=\n" \
  "s_cmp_eq_u32 s73, 5\n" \
  "s_cbranch_scc0 .LCV_NMK_%=\n" \
  "s_mov_b32 m0, s78\n" \
  "s_nop 0\n" \
  "buffer_load_dword v2, s[0:3], s66 offen sc1 lds\n" \
  ".LCV_NMK_%=:\n" \
  "s_setprio 0\n" \
  "s_waitcnt lgkmcnt(8)\n" \
  "s_barrier\n" \
  "s_add_u32 s67, s67, 1024\n" \
  "v_mov_b32 v11, 0\n" \
  "ds_write_b32 v59, v11\n" \
  "ds_read_b128 v[56:59], v3 offset:4096\n" \
  "ds_read_b128 v[60:63], v3 offset:5120\n" \
  "v_mov_b64 v[20:21], s[92:93]\n" \
  "v_mov_b64 v[22:23], s[92:93]\n" \
  "v_mov_b64 v[16:17], s[92:93]\n" \
  "v_mov_b64 v[18:19], s[92:93]\n" \
  "s_mov_b32 m0, s94\n" \
  "s_mov_b64 exec, 15\n" \
  "buffer_load_dword v2, s[44:47], s68 offen sc1 lds\n" \
  "s_mov_b64 exec, -1\n" \
  "s_setprio 2\n" \
  "s_waitcnt lgkmcnt(0)\n" \
  "s_barrier\n" \
  "ds_read_b128 v[4:7], v1 offset:8192\n" \
  "ds_read_b128 v[8:11], v1 offset:9216\n" \
  "s_waitcnt lgkmcnt(1)\n" \
  "v_smfmac_f32_16x16x64_bf16 v[12:15], v[4:7], v[24:31], v0\n" \
  "ds_read_b128 v[24:27], v3 offset:6144\n" \
  "ds_read_b128 v[28:31], v3 offset:7168\n" \
  "v_smfmac_f32_16x16x64_bf16 v[16:19], v[4:7], v[32:39], v0\n" \
  "ds_read_b128 v[32:35], v3 offset:12288\n" \
  "ds_read_b128 v[36:39], v3 offset:13312\n" \
  "s_waitcnt lgkmcnt(4)\n" \
  "v_smfmac_f32_16x16x64_bf16 v[12:15], v[8:11], v[40:47], v0\n" \
  "v_smfmac_f32_16x16x64_bf16 v[16:19], v[8:11], v[48:55], v0\n" \
  "v_smfmac_f32_16x16x64_bf16 v[20:23], v[4:7], v[56:63], v0\n" \
  "ds_read_b128 v[56:59], v3 offset:14336\n" \
  "ds_read_b128 v[60:63], v3 offset:15360\n" \
  "v_mov_b64 v[40:41], s[92:93]\n" \
  "v_mov_b64 v[42:43], s[92:93]\n" \
  "s_nop 2\n" \
  "v_permlane32_swap_b32 v12, v17\n" \
  "v_permlane32_swap_b32 v13, v16\n" \
  "v_permlane32_swap_b32 v14, v19\n" \
  "v_permlane32_swap_b32 v15, v18\n" \
  "v_pk_add_f32 v[12:13], v[12:13], v[16:17] op_sel:[0,1] op_sel_hi:[1,0]\n" \
  "v_pk_add_f32 v[14:15], v[14:15], v[18:19] op_sel:[0,1] op_sel_hi:[1,0]\n" \
  "buffer_atomic_add_x2 v[12:13], v2, s[40:43], s56 offen\n" \
  "buffer_atomic_add_x2 v[14:15], v2, s[40:43], s57 offen\n" \
  "s_waitcnt lgkmcnt(2)\n" \
  "v_smfmac_f32_16x16x64_bf16 v[20:23], v[8:11], v[32:39], v0\n" \
  "v_smfmac_f32_16x16x64_bf16 v[40:43], v[4:7], v[24:31], v0\n" \
  "s_waitcnt lgkmcnt(0)\n" \
  "v_smfmac_f32_16x16x64_bf16 v[40:43], v[8:11], v[56:63], v0\n" \
  "s_nop 7\n" \
  "v_permlane32_swap_b32 v20, v41\n" \
  "v_permlane32_swap_b32 v21, v40\n" \
  "v_permlane32_swap_b32 v22, v43\n" \
  "v_permlane32_swap_b32 v23, v42\n" \
  "v_pk_add_f32 v[20:21], v[20:21], v[40:41] op_sel:[0,1] op_sel_hi:[1,0]\n" \
  "v_pk_add_f32 v[22:23], v[22:23], v[42:43] op_sel:[0,1] op_sel_hi:[1,0]\n" \
  "buffer_atomic_add_x2 v[20:21], v2, s[40:43], s58 offen\n" \
  "buffer_atomic_add_x2 v[22:23], v2, s[40:43], s59 offen\n" \
  "s_setprio 0\n" \
  "s_memrealtime s[38:39]\n" \
  "s_mov_b64 exec, 1\n" \
  "v_mov_b32 v50, s4\n" \
  "v_mov_b32 v51, s5\n" \
  "v_mov_b32 v48, 0\n" \
  "global_store_dwordx2 v48, v[50:51], s[36:37]\n" \
  "s_mov_b64 exec, -1\n" \
  "s_add_u32 s36, s36, 8\n" \
  "s_addc_u32 s37, s37, 0\n" \
  "s_nop 0\n" \
  "s_nop 0\n" \
  "s_nop 0\n" \
  "s_nop 0\n" \
  "s_nop 0\n" \
  "s_nop 0\n" \
  "s_nop 0\n" \
  "v_mbcnt_lo_u32_b32 v48, -1, 0\n" \
  "v_mbcnt_hi_u32_b32 v48, -1, v48\n" \
  "s_add_u32 s88, s74, 157760\n" \
  "v_mov_b32 v59, s88\n" \
  "v_lshl_add_u32 v52, v48, 2, v59\n" \
  "ds_read_b32 v52, v52 offset:192\n" \
  "v_lshrrev_b32 v49, 3, v48\n" \
  "v_and_b32 v50, 7, v48\n" \
  "v_lshlrev_b32 v50, 4, v50\n" \
  "v_lshl_or_b32 v44, v49, 8, v50\n" \
  "s_mov_b32 s87, 0x40000\n" \
  "v_lshlrev_b32 v10, 4, v48\n" \
  "v_lshl_add_u32 v58, v48, 2, s94\n" \
  "s_waitcnt vmcnt(5)\n" \
  "ds_read_b32 v58, v58\n" \
  "s_waitcnt lgkmcnt(0)\n" \
  "v_cmp_eq_u32 vcc, 0, v58\n" \
  "s_and_b32 vcc_lo, vcc_lo, 15\n" \
  "s_cbranch_scc0 .LCV_GOT_%=\n" \
  "s_branch .LCV_ASK_%=\n" \
  ".LCV_LATE_%=:\n" \
  "v_cmp_eq_u32 vcc, 0, v58\n" \
  "s_and_b32 vcc_lo, vcc_lo, 15\n" \
  "s_cbranch_scc0 .LCV_GOT_%=\n" \
  "s_sleep 12\n" \
  ".LCV_ASK_%=:\n" \
  "s_mov_b64 exec, 15\n" \
  "buffer_load_dword v58, v2, s[44:47], s68 offen sc1\n" \
  "s_mov_b64 exec, -1\n" \
  "s_sub_u32 s87, s87, 1\n" \
  "s_cbranch_scc1 .LCV_END_%=\n" \
  "s_waitcnt vmcnt(0)\n" \
  "s_branch .LCV_LATE_%=\n" \
  ".LCV_GOT_%=:\n" \
  "s_mov_b32 m0, s99\n" \
  "s_add_u32 s88, s99, 8192\n" \
  "global_load_lds_dwordx4 v10, s[62:63] sc1\n" \
  "global_load_lds_dwordx4 v10, s[62:63] offset:1024 sc1\n" \
  "s_mov_b32 m0, s88\n" \
  "s_nop 0\n" \
  "global_load_lds_dwordx4 v10, s[64:65] sc1\n" \
  "global_load_lds_dwordx4 v10, s[64:65] offset:1024 sc1\n" \
  ".LCV_HAVE_%=:\n" \
  "s_waitcnt lgkmcnt(0)\n" \
  "s_sub_u32 s61, s38, s61\n" \
  "s_add_u32 s79, s79, 2\n" \
  "s_min_u32 s79, s79, s61\n" \
  "s_sub_u32 s39, s38, 150\n" \
  "v_subrev_u32 v52, s39, v52\n" \
  "v_cmp_lt_u32 vcc, 0x1000, v52\n" \
  "s_cmp_eq_u64 vcc, 0\n" \
  "s_cbranch_scc1 .LCV_OKM_%=\n" \
  ".LCV_AGN_%=:\n" \
  "buffer_load_dword v52, v2, s[0:3], s66 offen sc1\n" \
  "s_sub_u32 s87, s87, 1\n" \
  "s_cbranch_scc1 .LCV_END_%=\n" \
  "s_waitcnt vmcnt(0)\n" \
  "v_cmp_eq_u32 vcc, 0, v52\n" \
  "v_subrev_u32 v52, s39, v52\n" \
  "s_cmp_eq_u64 vcc, 0\n" \
  "s_cbranch_scc1 .LCV_OKM_%=\n" \
  "s_sleep 4\n" \
  "s_branch .LCV_AGN_%=\n" \
  ".LCV_OKM_%=:\n" \
  "s_nop 1\n" \
  "v_max_i32_dpp v52, v52, v52 row_ror:1 row_mask:0xf bank_mask:0xf\n" \
  "s_nop 1\n" \
  "v_max_i32_dpp v52, v52, v52 row_ror:2 row_mask:0xf bank_mask:0xf\n" \
  "s_nop 1\n" \
  "v_max_i32_dpp v52, v52, v52 row_ror:4 row_mask:0xf bank_mask:0xf\n" \
  "s_nop 1\n" \
  "v_max_i32_dpp v52, v52, v52 row_ror:8 row_mask:0xf bank_mask:0xf\n" \
  "s_movk_i32 vcc_lo, 150\n" \
  "v_readlane_b32 s88, v52, 0\n" \
  "s_max_i32 vcc_lo, vcc_lo, s88\n" \
  "v_readlane_b32 s88, v52, 16\n" \
  "s_max_i32 vcc_lo, vcc_lo, s88\n" \
  "v_readlane_b32 s88, v52, 32\n" \
  "s_max_i32 vcc_lo, vcc_lo, s88\n" \
  "v_readlane_b32 s88, v52, 48\n" \
  "s_max_i32 vcc_lo, vcc_lo, s88\n" \
  "s_add_u32 s66, s66, 1024\n" \
  "s_add_u32 vcc_lo, vcc_lo, s39\n" \
  "s_add_u32 vcc_lo, vcc_lo, 23\n" \
  ".LCV_TW_%=:\n" \
  "s_memrealtime s[38:39]\n" \
  "s_waitcnt lgkmcnt(0)\n" \
  "s_sub_u32 s38, s38, vcc_lo\n" \
  "s_cmp_lt_i32 s38, 0\n" \
  "s_cbranch_scc1 .LCV_TW_%=\n" \
  "buffer_load_dwordx4 v[24:27], v44, s[40:43], s60 offen sc1\n" \
  "buffer_load_dwordx4 v[28:31], v44, s[40:43], s60 offen offset:2048 sc1\n" \
  "s_add_u32 s62, s62, 0x80000\n" \
  "s_addc_u32 s63, s63, 0\n" \
  "s_add_u32 s64, s64, 0x40000\n" \
  "s_addc_u32 s65, s65, 0\n" \
  "s_add_u32 s68, s68, 0x1000\n" \
  "s_add_u32 s69, s69, 0x10000\n" \
  "s_add_u32 s70, s70, 0x10000\n" \
  "s_sub_u32 s88, s73, 4\n" \
  "v_lshrrev_b32 v49, 2, v48\n" \
  "v_lshrrev_b32 v50, 4, v48\n" \
  "v_bitop3_b32 v50, v49, v50, 3 bitop3:0x28\n" \
  "v_lshl_add_u32 v4, v50, 3, s97\n" \
  "v_and_b32 v50, 3, v48\n" \
  "v_lshl_add_u32 v4, v50, 5, v4\n" \
  "v_and_b32 v49, 1, v49\n" \
  "v_lshl_add_u32 v4, v49, 8, v4\n" \
  "v_and_b32 v49, 8, v48\n" \
  "v_lshl_add_u32 v4, v49, 4, v4\n" \
  "v_and_b32 v51, 15, v48\n" \
  "v_bfe_u32 v49, v48, 4, 1\n" \
  "v_lshrrev_b32 v50, 5, v48\n" \
  "s_lshl_b32 s89, s88, 10\n" \
  "s_add_u32 s89, s89, s74\n" \
  "s_add_u32 s89, s89, 158720\n" \
  "v_lshrrev_b32 v9, 3, v51\n" \
  "v_mul_u32_u24 v6, 41, v9\n" \
  "v_xor_b32 v6, v6, v51\n" \
  "v_lshl_add_u32 v6, v6, 3, s89\n" \
  "v_lshl_add_u32 v6, v49, 6, v6\n" \
  "v_lshl_add_u32 v6, v50, 9, v6\n" \
  "v_xor_b32 v9, 0x90, v6\n" \
  "v_lshlrev_b32 v8, 4, v49\n" \
  "v_lshl_add_u32 v8, v51, 5, v8\n" \
  "v_lshl_add_u32 v8, v50, 3, v8\n" \
  "s_lshl_b32 s89, s88, 8\n" \
  "s_add_u32 s89, s89, s74\n" \
  "s_add_u32 s89, s89, 139264\n" \
  "v_and_b32 v49, 31, v48\n" \
  "v_lshl_add_u32 v5, v49, 3, s89\n" \
  "v_lshl_add_u32 v5, v50, 10, v5\n" \
  "v_lshrrev_b32 v49, 2, v51\n" \
  "v_and_b32 v50, 5, v51\n" \
  "v_and_or_b32 v49, v49, 2, v50\n" \
  "v_and_b32 v50, 48, v48\n" \
  "v_lshlrev_b32 v7, 3, v50\n" \
  "v_lshl_add_u32 v7, v49, 4, v7\n" \
  "v_bitop3_b32 v49, v51, 2, 2 bitop3:0x0c\n" \
  "v_lshl_add_u32 v7, v49, 29, v7\n" \
  "v_mov_b64 v[12:13], 0\n" \
  "v_mov_b64 v[14:15], 0\n" \
  "v_mov_b64 v[16:17], 0\n" \
  "v_mov_b64 v[18:19], 0\n" \
  "v_mov_b64 v[20:21], 0\n" \
  "v_mov_b64 v[22:23], 0\n" \
  "v_and_b32 v50, 3, v48\n" \
  "v_lshl_add_u32 v40, v50, 2, s98\n" \
  "v_lshrrev_b32 v50, 4, v48\n" \
  "v_mad_u32_u24 v40, v50, s95, v40\n" \
  "v_bfe_u32 v49, v48, 3, 1\n" \
  "v_lshl_add_u32 v40, v49, 6, v40\n" \
  "v_bfe_u32 v49, v48, 2, 1\n" \
  "v_lshl_add_u32 v40, v49, 7, v40\n" \
  "v_xor_b32 v41, 16, v40\n" \
  "v_xor_b32 v42, 32, v40\n" \
  "v_xor_b32 v43, 48, v40\n" \
  "s_add_i32 s80, s86, 0x35000000\n" \
  "v_mov_b32 v11, 0\n" \
  "s_mov_b32 s89, s60\n" \
  "s_bitcmp1_b32 s60, 7\n" \
  "s_cselect_b32 s88, 0x10000, 0\n" \
  "s_bitset1_b32 s88, 7\n" \
  "s_xor_b32 s56, s56, s88\n" \
  "s_xor_b32 s57, s57, s88\n" \
  "s_xor_b32 s58, s58, s88\n" \
  "s_xor_b32 s59, s59, s88\n" \
  "s_xor_b32 s60, s60, s88\n" \
  "s_sub_u32 s71, s71, 1\n" \
  ".LCV_SEE_%=:\n" \
  "s_waitcnt vmcnt(1)\n" \
  "v_cvt_f32_i32 v24, v24\n" \
  "v_cvt_f32_i32 v25, v25\n" \
  "v_cvt_f32_i32 v26, v26\n" \
  "v_cvt_f32_i32 v27, v27\n" \
  "v_max_f32 v45, |v24|, |v26|\n" \
  "v_pk_mul_f32 v[24:25], v[24:25], s[80:81] op_sel_hi:[1,0]\n" \
  "v_pk_mul_f32 v[26:27], v[26:27], s[80:81] op_sel_hi:[1,0]\n" \
  "s_nop 0\n" \
  "v_cvt_pk_bf16_f32 v32, v24, v26\n" \
  "v_cvt_pk_bf16_f32 v33, v25, v27\n" \
  "ds_write_b32 v40, v32\n" \
  "ds_write_b32 v41, v33\n" \
  "s_waitcnt vmcnt(0)\n" \
  "v_cvt_f32_i32 v28, v28\n" \
  "v_cvt_f32_i32 v30, v30\n" \
  "v_cvt_f32_i32 v29, v29\n" \
  "v_max3_f32 v45, v45, |v28|, |v30|\n" \
  "v_cvt_f32_i32 v31, v31\n" \
  "v_cmp_lt_f32 vcc, s91, v45\n" \
  "v_pk_mul_f32 v[28:29], v[28:29], s[80:81] op_sel_hi:[1,0]\n" \
  "v_pk_mul_f32 v[30:31], v[30:31], s[80:81] op_sel_hi:[1,0]\n" \
  "s_cmp_eq_u64 vcc, 0\n" \
  "s_cbranch_scc0 .LCV_POLL_%=\n" \
  "v_cvt_pk_bf16_f32 v36, v28, v30\n" \
  "v_cvt_pk_bf16_f32 v37, v29, v31\n" \
  "ds_write_b32 v42, v36\n" \
  "ds_write_b32 v43, v37\n" \
  "v_dot2c_f32_bf16 v11, v32, v32\n" \
  "v_dot2c_f32_bf16 v11, v33, v33\n" \
  "v_dot2c_f32_bf16 v11, v36, v36\n" \
  "v_dot2c_f32_bf16 v11, v37, v37\n" \
  "s_nop 1\n" \
  "s_nop 0\n" \
  "v_cmp_lt_f32 vcc, 4.0, v11\n" \
  "s_cbranch_vccz .LCV_TOP_%=\n" \
  "s_mov_b64 exec, vcc\n" \
  "ds_max_u32 v59, v11\n" \
  "s_mov_b64 exec, -1\n" \
  "s_branch .LCV_TOP_%=\n" \
  ".LCV_POLL_%=:\n" \
  "buffer_load_dwordx4 v[24:27], v44, s[40:43], s89 offen sc1\n" \
  "buffer_load_dwordx4 v[28:31], v44, s[40:43], s89 offen offset:2048 sc1\n" \
  "s_sub_u32 s87, s87, 1\n" \
  "s_cbranch_scc1 .LCV_END_%=\n" \
  "s_branch .LCV_SEE_%=\n" \
  ".LCV_LAST_%=:\n" \
  "s_waitcnt vmcnt(0)\n" \
  "s_mov_b64 exec, 1\n" \
  "v_mov_b32 v50, s4\n" \
  "v_mov_b32 v51, s5\n" \
  "v_mov_b32 v48, 0\n" \
  "global_store_dwordx2 v48, v[50:51], s[36:37]\n" \
  "s_mov_b64 exec, -1\n" \
  "s_add_u32 s36, s36, 8\n" \
  "s_addc_u32 s37, s37, 0\n" \
  "s_nop 0\n" \
  "s_nop 0\n" \
  "s_nop 0\n" \
  "s_nop 0\n" \
  "s_nop 0\n" \
  "s_nop 0\n" \
  "s_nop 0\n" \
  ".LCV_END_%=:\n" \
  "s_endpgm\n"
__device__ __forceinline__ void start_kgen(const MegaArgs& A, char* smem, const int kg) {
  const int w = A.wv, l = lane_id(), tid = 64 * w + l, c = l & 15, g = l >> 4;
  KGen G;
  unsigned tp = 0u;
#define TEXT_ASK()                                                                                                     \
  {                                                                                                                    \
    const char* pc;                                                                                                    \
    unsigned n;                                                                                                        \
    asm volatile("s_getpc_b64 %0\n.LKG_PC:\n\ts_mov_b32 %1, .LKG_LAST-.LKG_PC" : "=s"(pc), "=s"(n));                    \
    const unsigned o = (unsigned)(tid * 8 + ((blockIdx.x >> 3) & 7)) * IPS;                                            \
    if ((int)o < (int)n) asm volatile("global_load_dword %0, %1, %2" : "=v"(tp) : "v"(o), "s"(pc) : "memory");         \
  }
  if (IPF == 2) TEXT_ASK()
  v4u raw[2] = {};
#pragma unroll
  for (int i = 0; i < TP; ++i)
    if (kg < 32) raw[i] = *reinterpret_cast<const v4u*>(A.x0 + (size_t)(TP * (8 * (kg & 7) + w) + i) * DM + l * 8);
  G.load(A, 8 * kg + w, l);
  {
    const __amdgpu_buffer_rsrc_t rz = MKRS(A.ws + OFF_Z, Z_BYTES);
#pragma unroll
    for (int i = 0; i < 4; ++i) {
      const u64 z = i == 0 && kg < (int)(2 * SZ_ACC / 8192) ? ACC_INIT : 0ull;
      ST128((v4u{(unsigned)z, (unsigned)(z >> 32), (unsigned)z, (unsigned)(z >> 32)}), rz, tid * 16, (i * 64 + kg) * 8192);
    }
  }
  const u64 tag = call_tag(A);
  if (TP && kg < 32) {
    unsigned t = row_stats(raw[0]);
    if (TP > 1) {
      const unsigned t1 = row_stats(raw[1]);
      t = l < 2 ? t : t1;
    }
    if (l < 2 * TP) __hip_atomic_store((gu64*)(A.ws + OFF_GT) + 2 * (L_SEQ + TP * (8 * kg + w)) + l, tagged(tag, (l & 1) ? t : t << 16), RLX_AGENT);
  }
  DRAIN();
  __syncthreads();
  if (tid < 8) __hip_atomic_store((gu64*)(A.ws + OFF_RD) + tid * 64 + kg, tag, RLX_AGENT);
  asm volatile("" : "+v"(G.pa), "+v"(G.pb), "+v"(G.pe));
  G.setup();
  G.st = smem + w * 16640;
  const __amdgpu_buffer_rsrc_t rs = MKRS(A.ws + OFF_KF + (8 * kg + w) * 2048, 386 * 4);
  SST(1);
  unsigned tw = 0u;
#define TLB_ASK()                                                                                                      \
  if (tid < 384) {                                                                                                     \
    const unsigned o = (unsigned)OFF_XA + (unsigned)(tid * 8 + ((blockIdx.x >> 3) & 7)) * 65536u;                      \
    if (o < WS_TOTAL) asm volatile("global_load_dword %0, %1, %2" : "=v"(tw) : "v"(o), "s"(A.ws) : "memory");          \
  }
#define X2_CLEAR() \
  _Pragma("unroll") for (int i = 0; i < 4; ++i) ST128((v4u{0u, 0u, 0u, 0u}), MKRS(A.ws + OFF_X2, SZ_X), tid * 16, (4 * kg + i) * 8192);
  if (XZP == 1) { X2_CLEAR() }
  if (IPF == 1) TEXT_ASK()
  if (TLBW == 1) TLB_ASK()
  unsigned pf[6] = {};
#define PF_ASK(M)                                                                                                                  \
  if (kg < 32) {                                                                                                                   \
    if ((M) & 1) asm volatile("global_load_dword %0, %2, %3\n\tglobal_load_dword %1, %2, %3 offset:128" : "=&v"(pf[0]), "=&v"(pf[1]) : "v"(kg * 131072 + tid * 256), "s"(A.Wo)); \
    if ((M) & 2) asm volatile("global_load_dword %0, %2, %3\n\tglobal_load_dword %1, %2, %3 offset:128" : "=&v"(pf[2]), "=&v"(pf[3]) : "v"(kg * 256 + tid * 8192), "s"(A.Wq)); \
    if ((M) & 4) asm volatile("global_load_dword %0, %2, %3\n\tglobal_load_dword %1, %2, %3 offset:128" : "=&v"(pf[4]), "=&v"(pf[5]) : "v"(kg * 256 + tid * 8192), "s"(A.Wk)); \
  }
  PF_ASK(PFA)
  G.columns(l, 0, 16);
  G.get(G.bh, G.bl, l);
  if (XZP == 2) { X2_CLEAR() }
  PF_ASK(PFB)
  if (TLBW == 2) TLB_ASK()
  (void)G.scale();
  const Cx zb = G.power(256.f);
  Cx b = G.C, z = G.power(2.f);
#pragma unroll 1
  for (int f = 1; f < 9;) {
    char* p = G.st + l * 8;
    const int fe = f == 1 ? 4 : 9;
#pragma unroll 1
    for (; f < fe; ++f) {
      b = cmul(b, zb);
      if (f == 2 || f == 4) z = cmul(z, z);
      Cx y = b;
      p = G.rows(p, y, z, f == 1 ? 8 : f < 4 ? 4 : 2);
    }
    const f32x4 d = G.tile<false>(l);
    const v4u r = {pkh(d.x, d.x), pkh(d.y, d.y), pkh(d.z, d.z), pkh(d.w, d.w)};
    if (f == 4) {
      ST128(r, rs, (g < 2 ? 8 * c + 4 * g : 64 * g + 4 * c) * 4, 0);
    } else {
      ST64((v2u{r.x, r.y}), rs, (256 + 64 * g + 2 * c) * 4, 0);
      ST64((v2u{r.z, r.w}), rs, (288 + 64 * g + 2 * c) * 4, 0);
    }
  }
  SST(2);
  DRAIN();
  asm volatile("" ::"v"(tp), "v"(tw), "v"(pf[0]), "v"(pf[1]), "v"(pf[2]), "v"(pf[3]), "v"(pf[4]), "v"(pf[5]));
  if (l == 0) __hip_atomic_store((gu64*)(A.ws + OFF_KG) + kg * 16 + w, tag, RLX_AGENT);
  if (XZP == 3) { X2_CLEAR() }
  const u64 rv = (XZP & 3) && w == 0 ? ready_look(A) : 0;
  if (TLBW == 3) {
    TLB_ASK()
    DRAIN();
    asm volatile("" ::"v"(tw));
  }
  DRAIN();
  __syncthreads();
  if ((XZP & 3) && w == 0) {
    ready_wait(A, tag, rv);
    if (l < 8) set_flag((unsigned*)(A.ws + OFF_FL) + FL_FZ + l * 64 + kg);
  }
  SST(3);
}

constexpr int C_SM = 131072, C_SO = C_SM + 8192, C_KT = C_SO + 2048, C_X = C_KT + 16384, C_SQ = C_X + 1024;
static_assert(C_SQ + 4096 <= 163840 && C_SQ % 512 == 0, "lds");
__device__ __forceinline__ v4u mk_srd(const void* p, const unsigned bytes) {
  const unsigned long long a = (unsigned long long)p;
  return v4u{(unsigned)a, (unsigned)(a >> 32) & 0xffffu, bytes, 0x00020000u};
}
template <bool ATT>
__device__ __forceinline__ void role_chain_t(const MegaArgs& A, char* smem, const int L, const int h, const int rep) {
  const int w = A.wv;
  char* ws = A.ws;
  unsigned* FL = (unsigned*)(ws + OFF_FL);
  const int l = lane_id(), c = l & 15, g = l >> 4;
  unsigned* FQ = FL + FL_FQ + L * 64 * 32 * 32;
  const int wb = (w + ROT_H) & 7, tb = ROT_T;
  const unsigned sb = lds_base(smem);
  __builtin_amdgcn_s_waitcnt(0x0f70);
  glds16(A.mem0, (unsigned)((c & 7) * DM + 64 * w + 16 * g + 8 * (c >> 3)) * 2, sb + C_SM + w * 1024);
#pragma unroll
  for (int i = 0; i < 16; ++i)
    glds16((const char*)A.Wo + (size_t)(h * DH + 32 * (i >> 2) + 4 * w + (i & 2)) * (DM * 2),
           (unsigned)((i & 1) * 1024 + ((l ^ (((i & 1) | ((w & 3) << 1)) << 1)) << 4)), sb + w * 16384 + i * 1024, WNT & 1);
  const u64 tag = call_tag(A);
  const char* gK = ws + OFF_QK + L * SZ_QK + ((size_t)h * 16 + 2 * w) * 1024;
  const char* gV = ws + OFF_VF + L * SZ_O + ((size_t)h * 8 + 2 * w - 8) * 1024;
  v8u pv, pw;
  {
    const unsigned voff = g * 256 + c * 8, wp = sb + w * 16384 + l * 16;
    pv[0] = (c & 8) ? 0xEEEE : 0x4444;
    pv[1] = sb + C_SM + l * 16;
    const unsigned ra = sb + C_SQ + (g >> 1) * 512 + (c ^ ((g & 1) * 18)) * 8;
    pv[2] = ATT ? ra : voff;
    pv[3] = ATT ? ra ^ 0x108 : wp;
    pv[4] = sb + ((c & 2) ? C_SQ : C_SO) + w * 256 + (((c & 5) | ((c >> 2) & 2)) + 8 * (g >> 1)) * 16 + 8 * ((g ^ (c >> 1)) & 1);
    pv[5] = voff;
    pv[6] = wp;
    pv[7] = sb + C_SO + (l >> 5) * 1024 + w * 256 + (l & 31) * 8;
    const int q = (l >> 2) & 3, p = l & 3;
    const int f = (q & 1) | ((g & 1) << 1) | ((q >> 1) << 2);
#pragma unroll
    for (int i = 0; i < 4; ++i) pw[i] = (4 * i + g) * (HD * 2) + ((c ^ (((g & 1) | (i << 1)) << 1)) << 4) - 1024 * i;
    pw[4] = sb + (g >> 1) * 16384 + (4 * (g & 1) + (q & 1) + 8 * (q >> 1)) * 256 + (w ^ f) * 32 + p * 8;
    pw[5] = sb + (4 * (g >> 1) + 2 * (q >> 1) + (g & 1)) * 16384 + (q & 1) * 1024 + p * 8 + ((4 * wb) ^ f ^ tb) * 32;
    pw[6] = pw[7] = 0u;
  }
  const size_t wo = (size_t)(16 * w) * HD + h * DH;
#define POL 2
#define PG(w) (POL == 2 ? (w) * 4096 : ((w) & 1) * 4096 + ((w) >> 1) * 16384)
  const v4u rsA = mk_srd(ws + OFF_ACC + L * SZ_ACC + (POL == 1 ? 8192 : 0), SZ_ACC - 8192);
  const v4u rsF = mk_srd(FQ, 64 * 32 * 32 * 4);
  const v4u rsMK = mk_srd(ws + OFF_MEMK + L * SZ_MEM, SZ_MEM);
  const v4u rsMV = mk_srd(ws + OFF_MEMV + L * SZ_MEM, SZ_MEM);
  const v4u pAB = (v4u{0u, 2048u, 3072u, 1024u} ^ (1536u * tb)) + PG(wb);
  const v4u rsT = mk_srd(FL + FL_FK + L * 32, 64 * 1024 - 128);
  unsigned sc[8];
  sc[0] = h * 128;
  sc[1] = (h * 4 + w - 4) * 512;
  sc[2] = (h * 4 + w - 4) * 512;
  sc[3] = NSEG - 1;
  sc[5] = w;
  sc[6] = sb;
  const char* dbg = ws + WS_TOTAL + (w == 0 ? A.vb * 64 * 8 : 600 * 256 * 8);
#define CHAIN_OPS                                                                                                          \
  ::"{v[0:7]}"(pv), "{v[24:31]}"(pw), "{s[4:5]}"(A.Wv + wo), "{s[6:7]}"(A.Wq + wo), "{s[8:9]}"(A.Wk + wo), "{s[10:11]}"(tag),         \
      "{s[12:13]}"(ws + OFF_RD + (blockIdx.x & 7) * 512), "{s[14:15]}"(ws + OFF_G + (blockIdx.x & 7) * 256), "{s[16:17]}"(ws + OFF_G + h * 8), \
      "{s18}"(w == 4 && L == 0 ? 255 : 0), "{s19}"(sb + w * 16384), "{s[20:21]}"(ws + WS_TOTAL + (w == 0 ? 534 * 256 + A.vb : 610 * 256) * 8), \
      "{s[40:43]}"(rsA), "{s[44:47]}"(rsF), "{s[48:51]}"(rsMK), "{s[52:55]}"(rsMV), "{s[56:59]}"(pAB), "{s60}"(PG(w)), "{s61}"(ATT ? PG(h >> 2) + (h & 3) * 1024 : L), \
      "{s[62:63]}"(gK), "{s[64:65]}"(gV), "{s66}"(0), "{s67}"(w == 4 ? (h >> 3) * 256 + (h & 7) * 8 : 0x40000000), "{s68}"(sc[0]), "{s69}"(sc[1]), "{s70}"(sc[2]),    \
      "{s71}"(sc[3]), "{s73}"(sc[5]), "{s74}"(sc[6]), "{s[36:37]}"(dbg),                   \
      "{s[0:3]}"(rsT)                                                                                                      \
      : "memory", "vcc", "scc", "s99", "v255"
  if (ATT) asm volatile("s_mov_b64 exec, -1\ns_nop 4\n" CHAIN_ASM_ATT CHAIN_OPS);
  else asm volatile("s_mov_b64 exec, -1\ns_nop 4\n" CHAIN_ASM_VO CHAIN_OPS);
#undef CHAIN_OPS
}

__device__ __forceinline__ void role_chain(const MegaArgs& A, char* smem, const int L, const int h, const int rep) {
  if (A.wv < 4) role_chain_t<true>(A, smem, L, h, rep);
  else role_chain_t<false>(A, smem, L, h, rep);
}

constexpr int AW_SO = 0, AW_TK = 32768, AW_CNT = AW_TK + 98304;
__device__ __forceinline__ unsigned umin3(unsigned a, unsigned b, unsigned c) {
  return __builtin_elementwise_min(__builtin_elementwise_min(a, b), c);
}
#define AW_ASM_X \
  "s_mov_b32 s61, 0\n" \
  "s_mov_b32 s67, 1\n" \
  "s_mov_b32 s68, 0\n" \
  "s_mov_b32 s70, 0\n" \
  "s_mov_b32 s73, 0\n" \
  "s_mov_b32 s71, 0x3e0293ee\n" \
  "s_mov_b32 s72, 0xbe0293ee\n" \
  "s_mov_b32 s65, 49152\n" \
  "s_bitcmp0_b32 s58, 2\n" \
  "s_cbranch_scc1 .LCX_OWN_%=\n" \
  "v_swap_b32 v192, v208\n" \
  "v_swap_b32 v193, v209\n" \
  "v_swap_b32 v194, v210\n" \
  "v_swap_b32 v195, v211\n" \
  "v_swap_b32 v196, v212\n" \
  "v_swap_b32 v197, v213\n" \
  "v_swap_b32 v198, v214\n" \
  "v_swap_b32 v199, v215\n" \
  "v_swap_b32 v200, v216\n" \
  "v_swap_b32 v201, v217\n" \
  "v_swap_b32 v202, v218\n" \
  "v_swap_b32 v203, v219\n" \
  "v_swap_b32 v204, v220\n" \
  "v_swap_b32 v205, v221\n" \
  "v_swap_b32 v206, v222\n" \
  "v_swap_b32 v207, v223\n" \
  "v_swap_b32 v224, v240\n" \
  "v_swap_b32 v225, v241\n" \
  "v_swap_b32 v226, v242\n" \
  "v_swap_b32 v227, v243\n" \
  "v_swap_b32 v228, v244\n" \
  "v_swap_b32 v229, v245\n" \
  "v_swap_b32 v230, v246\n" \
  "v_swap_b32 v231, v247\n" \
  "v_swap_b32 v232, v248\n" \
  "v_swap_b32 v233, v249\n" \
  "v_swap_b32 v234, v250\n" \
  "v_swap_b32 v235, v251\n" \
  "v_swap_b32 v236, v252\n" \
  "v_swap_b32 v237, v253\n" \
  "v_swap_b32 v238, v254\n" \
  "v_swap_b32 v239, v255\n" \
  ".LCX_OWN_%=:\n" \
  "v_mov_b32 v38, s58\n" \
  "s_memrealtime s[38:39]\n" \
  "s_waitcnt lgkmcnt(0)\n" \
  "s_mov_b32 s69, s38\n" \
  "s_setprio 3\n" \
  ".p2align 6\n" \
  ".LCX_TOP_%=:\n" \
  "s_mov_b32 s62, 0x200000\n" \
  "s_cmp_ge_u32 s73, s67\n" \
  "s_cbranch_scc1 .LCX_TOK_%=\n" \
  "s_cmp_eq_u32 s61, 0\n" \
  "s_cbranch_scc1 .LCX_SPIN_%=\n" \
  ".LCX_RE_%=:\n" \
  "ds_read_b32 v37, v38\n" \
  "s_add_u32 s62, s62, 1\n" \
  "s_waitcnt lgkmcnt(0)\n" \
  "v_readfirstlane_b32 s73, v37\n" \
  "s_bfe_u32 s64, s73, 0x80008\n" \
  "s_and_b32 s73, s73, 0xff\n" \
  "s_min_u32 s73, s73, s64\n" \
  "s_cmp_ge_u32 s73, s67\n" \
  "s_cbranch_scc1 .LCX_TOK_%=\n" \
  "s_sleep 1\n" \
  "s_cmp_lt_u32 s62, 2097158\n" \
  "s_cbranch_scc1 .LCX_RE_%=\n" \
  "s_waitcnt vmcnt(0)\n" \
  "v_mov_b32 v144, s59\n" \
  "v_mov_b32 v145, 1\n" \
  "s_mov_b64 exec, 1\n" \
  "ds_add_rtn_u32 v144, v144, v145\n" \
  "s_mov_b64 exec, -1\n" \
  "s_xor_b32 s59, s59, 4\n" \
  "s_mov_b32 s61, 0\n" \
  "s_waitcnt lgkmcnt(0)\n" \
  "v_readfirstlane_b32 s63, v144\n" \
  "v_lshlrev_b32 v144, 5, v5\n" \
  "s_and_b32 s63, s63, 7\n" \
  "s_cmp_eq_u32 s63, 7\n" \
  "s_cbranch_scc0 .LCX_W1_%=\n" \
  "s_mov_b64 exec, 15\n" \
  "global_store_dword v144, v145, s[52:53] sc1\n" \
  "s_mov_b64 exec, -1\n" \
  "s_memrealtime s[4:5]\n" \
  "s_waitcnt lgkmcnt(0)\n" \
  "s_mov_b64 exec, 1\n" \
  "v_mov_b32 v144, s4\n" \
  "v_mov_b32 v145, s5\n" \
  "global_store_dwordx2 v5, v[144:145], s[36:37]\n" \
  "s_mov_b64 exec, -1\n" \
  ".LCX_W1_%=:\n" \
  "s_add_u32 s52, s52, 2048\n" \
  "s_addc_u32 s53, s53, 0\n" \
  "s_add_u32 s36, s36, 8\n" \
  "s_addc_u32 s37, s37, 0\n" \
  ".LCX_SPIN_%=:\n" \
  "s_sleep 2\n" \
  "s_memrealtime s[38:39]\n" \
  "ds_read_b32 v37, v38\n" \
  "s_sub_u32 s62, s62, 1\n" \
  "s_cbranch_scc1 .LCX_END_%=\n" \
  "s_waitcnt lgkmcnt(0)\n" \
  "v_readfirstlane_b32 s73, v37\n" \
  "s_add_u32 s38, s38, 80\n" \
  "s_bfe_u32 s64, s73, 0x80008\n" \
  "s_and_b32 s73, s73, 0xff\n" \
  "s_min_u32 s73, s73, s64\n" \
  "s_cmp_ge_u32 s73, s67\n" \
  "s_cbranch_scc0 .LCX_SPIN_%=\n" \
  "s_cmp_eq_u32 s70, 0\n" \
  "s_cbranch_scc1 .LCX_TOK_%=\n" \
  "s_sub_u32 s63, s38, s69\n" \
  "s_max_i32 s63, s63, 0\n" \
  "s_add_u32 s69, s69, s63\n" \
  ".LCX_TOK_%=:\n" \
  "ds_read_b128 v[48:51], v6 offset:0\n" \
  "ds_read_b128 v[64:67], v0 offset:8192\n" \
  "ds_read_b128 v[80:83], v0 offset:12288\n" \
  "ds_read_b128 v[52:55], v6 offset:1024\n" \
  "ds_read_b128 v[68:71], v0 offset:9216\n" \
  "ds_read_b128 v[84:87], v0 offset:13312\n" \
  "ds_read_b128 v[56:59], v6 offset:2048\n" \
  "ds_read_b128 v[72:75], v0 offset:10240\n" \
  "ds_read_b128 v[88:91], v0 offset:14336\n" \
  "ds_read_b128 v[60:63], v6 offset:3072\n" \
  "ds_read_b128 v[76:79], v0 offset:11264\n" \
  "ds_read_b128 v[92:95], v0 offset:15360\n" \
  "s_waitcnt lgkmcnt(9)\n" \
  "v_mfma_f32_16x16x32_bf16 v[20:23], v[64:67], v[48:51], v[8:11]\n" \
  "v_mfma_f32_16x16x32_bf16 v[24:27], v[80:83], v[48:51], v[12:15]\n" \
  "ds_read_b128 v[96:99], v0 offset:16384\n" \
  "ds_read_b128 v[100:103], v0 offset:17408\n" \
  "s_waitcnt lgkmcnt(8)\n" \
  "v_mfma_f32_16x16x32_bf16 v[20:23], v[68:71], v[52:55], v[20:23]\n" \
  "v_mfma_f32_16x16x32_bf16 v[24:27], v[84:87], v[52:55], v[24:27]\n" \
  "ds_read_b128 v[104:107], v0 offset:18432\n" \
  "ds_read_b128 v[108:111], v0 offset:19456\n" \
  "s_waitcnt lgkmcnt(7)\n" \
  "v_mfma_f32_16x16x32_bf16 v[20:23], v[72:75], v[56:59], v[20:23]\n" \
  "v_mfma_f32_16x16x32_bf16 v[24:27], v[88:91], v[56:59], v[24:27]\n" \
  "ds_read_b128 v[112:115], v0 offset:20480\n" \
  "ds_read_b128 v[116:119], v0 offset:21504\n" \
  "s_waitcnt lgkmcnt(6)\n" \
  "v_mfma_f32_16x16x32_bf16 v[20:23], v[76:79], v[60:63], v[20:23]\n" \
  "v_mfma_f32_16x16x32_bf16 v[24:27], v[92:95], v[60:63], v[24:27]\n" \
  "ds_read_b128 v[120:123], v0 offset:22528\n" \
  "ds_read_b128 v[124:127], v0 offset:23552\n" \
  "s_add_u32 s67, s67, 1\n" \
  "v_add_u32 v0, s65, v0\n" \
  "v_add_u32 v6, s65, v6\n" \
  "s_sub_u32 s65, 0, s65\n" \
  "s_nop 1\n" \
  "v_max3_f32 v32, v20, v21, v22\n" \
  "v_max3_f32 v33, v23, v24, v25\n" \
  "v_max3_f32 v32, v32, v26, v27\n" \
  "v_max_f32 v32, v32, v33\n" \
  "v_mov_b32 v33, v32\n" \
  "s_nop 1\n" \
  "v_permlane16_swap_b32 v32, v33\n" \
  "v_max_f32 v32, v32, v33\n" \
  "v_mov_b32 v33, v32\n" \
  "s_nop 1\n" \
  "v_permlane32_swap_b32 v32, v33\n" \
  "v_max_f32 v32, v32, v33\n" \
  "v_mul_f32 v33, s72, v32\n" \
  "v_fma_f32 v20, v20, s71, v33\n" \
  "v_fma_f32 v21, v21, s71, v33\n" \
  "v_fma_f32 v22, v22, s71, v33\n" \
  "v_fma_f32 v23, v23, s71, v33\n" \
  "v_fma_f32 v24, v24, s71, v33\n" \
  "v_fma_f32 v25, v25, s71, v33\n" \
  "v_fma_f32 v26, v26, s71, v33\n" \
  "v_fma_f32 v27, v27, s71, v33\n" \
  "v_exp_f32 v20, v20\n" \
  "v_exp_f32 v21, v21\n" \
  "v_exp_f32 v22, v22\n" \
  "v_exp_f32 v23, v23\n" \
  "v_exp_f32 v24, v24\n" \
  "v_exp_f32 v25, v25\n" \
  "v_exp_f32 v26, v26\n" \
  "v_exp_f32 v27, v27\n" \
  "v_add_f32 v34, v20, v21\n" \
  "v_add_f32 v33, v22, v23\n" \
  "v_add_f32 v36, v24, v25\n" \
  "v_add_f32 v34, v34, v33\n" \
  "v_add_f32 v33, v26, v27\n" \
  "v_add_f32 v34, v34, v36\n" \
  "v_add_f32 v34, v34, v33\n" \
  "v_mov_b64 v[28:29], 0\n" \
  "v_mov_b64 v[30:31], 0\n" \
  "s_waitcnt vmcnt(0) lgkmcnt(0)\n" \
  "s_cmp_eq_u32 s61, 0\n" \
  "s_cbranch_scc1 .LCX_NP_%=\n" \
  "v_mov_b32 v144, s59\n" \
  "v_mov_b32 v145, 1\n" \
  "s_mov_b64 exec, 1\n" \
  "ds_add_rtn_u32 v144, v144, v145\n" \
  "s_mov_b64 exec, -1\n" \
  "s_xor_b32 s59, s59, 4\n" \
  "s_cmp_lg_u32 s68, 0\n" \
  "s_cbranch_scc1 .LCX_TEST_%=\n" \
  "s_mov_b32 s61, 0\n" \
  "s_waitcnt lgkmcnt(0)\n" \
  "v_readfirstlane_b32 s63, v144\n" \
  "v_lshlrev_b32 v144, 5, v5\n" \
  "s_and_b32 s63, s63, 7\n" \
  "s_cmp_eq_u32 s63, 7\n" \
  "s_cbranch_scc0 .LCX_W2_%=\n" \
  "s_mov_b64 exec, 15\n" \
  "global_store_dword v144, v145, s[52:53] sc1\n" \
  "s_mov_b64 exec, -1\n" \
  "s_memrealtime s[4:5]\n" \
  "s_waitcnt lgkmcnt(0)\n" \
  "s_mov_b64 exec, 1\n" \
  "v_mov_b32 v144, s4\n" \
  "v_mov_b32 v145, s5\n" \
  "global_store_dwordx2 v5, v[144:145], s[36:37]\n" \
  "s_mov_b64 exec, -1\n" \
  ".LCX_W2_%=:\n" \
  "s_add_u32 s52, s52, 2048\n" \
  "s_addc_u32 s53, s53, 0\n" \
  "s_add_u32 s36, s36, 8\n" \
  "s_addc_u32 s37, s37, 0\n" \
  ".LCX_NP_%=:\n" \
  "s_cmp_lg_u32 s68, 0\n" \
  "s_cbranch_scc1 .LCX_TEST_%=\n" \
  ".LCX_WF_%=:\n" \
  "s_memrealtime s[38:39]\n" \
  "s_waitcnt lgkmcnt(0)\n" \
  "s_sub_u32 s63, s69, s38\n" \
  "s_cmp_le_i32 s63, 0\n" \
  "s_cbranch_scc1 .LCX_GO_%=\n" \
  "s_cmp_lt_i32 s63, 12\n" \
  "s_cbranch_scc1 .LCX_WF_%=\n" \
  "s_sleep 3\n" \
  "s_branch .LCX_WF_%=\n" \
  ".LCX_GO_%=:\n" \
  "v_mov_b32 v35, 0\n" \
  "global_load_dwordx4 v[128:131], v5, s[48:49] offset:0 sc1\n" \
  "global_load_dwordx4 v[132:135], v5, s[48:49] offset:1024 sc1\n" \
  "global_load_dwordx4 v[136:139], v3, s[48:49] offset:0 sc1\n" \
  "global_load_dwordx4 v[140:143], v3, s[48:49] offset:512 sc1\n" \
  "global_load_dword v35, v35, s[50:51] sc1\n" \
  "s_sub_u32 s62, s62, 8\n" \
  "s_cbranch_scc1 .LCX_END_%=\n" \
  "s_waitcnt vmcnt(0)\n" \
  ".LCX_TEST_%=:\n" \
  "v_smfmac_f32_16x16x64_bf16 v[28:31], v[128:131], v[48:55], v1\n" \
  "v_smfmac_f32_16x16x64_bf16 v[28:31], v[132:135], v[56:63], v1\n" \
  "v_min3_u32 v33, v128, v129, v130\n" \
  "v_min3_u32 v36, v131, v132, v133\n" \
  "v_min3_u32 v33, v33, v134, v135\n" \
  "v_min3_u32 v36, v36, v136, v137\n" \
  "v_min3_u32 v33, v33, v138, v139\n" \
  "v_min3_u32 v36, v36, v140, v141\n" \
  "v_min3_u32 v33, v33, v142, v143\n" \
  "v_min_u32 v33, v33, v36\n" \
  "v_cmp_eq_u32 vcc, 0, v33\n" \
  "v_readfirstlane_b32 s64, v35\n" \
  "s_mov_b32 s68, 0\n" \
  "s_cmp_eq_u64 vcc, 0\n" \
  "s_cbranch_scc0 .LCX_FAIL_%=\n" \
  "v_permlane32_swap_b32 v28, v30\n" \
  "v_permlane32_swap_b32 v29, v31\n" \
  "v_mov_b32 v33, s67\n" \
  "ds_write_b32 v38, v33 offset:16\n" \
  "v_add_f32 v28, v28, v30\n" \
  "v_add_f32 v29, v29, v31\n" \
  "v_max3_f32 v36, v28, v29, v32\n" \
  "v_mov_b32 v33, v36\n" \
  "s_cmp_eq_u32 s64, 0\n" \
  "s_cselect_b32 s64, s38, s64\n" \
  "v_permlane16_swap_b32 v36, v33\n" \
  "v_max_f32 v36, v36, v33\n" \
  "v_mov_b32 v33, v36\n" \
  "s_sub_u32 s63, s64, s70\n" \
  "s_mov_b32 s70, s64\n" \
  "v_permlane32_swap_b32 v36, v33\n" \
  "v_max_f32 v36, v36, v33\n" \
  "v_mul_f32 v36, s72, v36\n" \
  "v_fma_f32 v32, v32, s71, v36\n" \
  "v_fma_f32 v28, v28, s71, v36\n" \
  "v_fma_f32 v29, v29, s71, v36\n" \
  "v_exp_f32 v32, v32\n" \
  "v_exp_f32 v28, v28\n" \
  "v_exp_f32 v29, v29\n" \
  "v_fma_f32 v34, v34, v32, v28\n" \
  "v_add_f32 v34, v34, v29\n" \
  "v_mov_b32 v33, v34\n" \
  "s_min_u32 s63, s63, 250\n" \
  "s_add_u32 s69, s64, s63\n" \
  "v_permlane16_swap_b32 v34, v33\n" \
  "v_add_f32 v34, v34, v33\n" \
  "v_mov_b32 v33, v34\n" \
  "s_add_u32 s69, s69, -48\n" \
  "s_add_u32 s48, s48, 0x10000\n" \
  "s_addc_u32 s49, s49, 0\n" \
  "v_permlane32_swap_b32 v34, v33\n" \
  "v_add_f32 v34, v34, v33\n" \
  "v_rcp_f32 v34, v34\n" \
  "s_add_u32 s50, s50, 1024\n" \
  "s_addc_u32 s51, s51, 0\n" \
  "v_mul_f32 v32, v32, v34\n" \
  "v_mul_f32 v20, v20, v32\n" \
  "v_mul_f32 v21, v21, v32\n" \
  "v_mul_f32 v22, v22, v32\n" \
  "v_mul_f32 v23, v23, v32\n" \
  "v_mul_f32 v24, v24, v32\n" \
  "v_mul_f32 v25, v25, v32\n" \
  "v_mul_f32 v26, v26, v32\n" \
  "v_mul_f32 v27, v27, v32\n" \
  "v_cvt_pk_bf16_f32 v40, v20, v21\n" \
  "v_cvt_pk_bf16_f32 v41, v22, v23\n" \
  "v_cvt_pk_bf16_f32 v42, v24, v25\n" \
  "v_cvt_pk_bf16_f32 v43, v26, v27\n" \
  "v_mul_f32 v28, v28, v34\n" \
  "v_mul_f32 v29, v29, v34\n" \
  "v_mfma_f32_16x16x32_bf16 v[64:67], v[96:99], v[40:43], 0\n" \
  "v_cvt_pk_bf16_f32 v44, v28, v29\n" \
  "v_mov_b32 v45, 0\n" \
  "v_mfma_f32_16x16x32_bf16 v[68:71], v[100:103], v[40:43], 0\n" \
  "v_cvt_pk_bf16_f32 v47, v28, v29\n" \
  "v_mov_b32 v46, 0\n" \
  "v_mfma_f32_16x16x32_bf16 v[72:75], v[104:107], v[40:43], 0\n" \
  "v_permlane32_swap_b32 v44, v45\n" \
  "v_mfma_f32_16x16x32_bf16 v[76:79], v[108:111], v[40:43], 0\n" \
  "v_permlane32_swap_b32 v46, v47\n" \
  "v_mfma_f32_16x16x32_bf16 v[80:83], v[112:115], v[40:43], 0\n" \
  "v_xor_b32 v16, 4096, v7\n" \
  "v_mfma_f32_16x16x32_bf16 v[84:87], v[116:119], v[40:43], 0\n" \
  "v_xor_b32 v17, 8192, v7\n" \
  "v_mfma_f32_16x16x32_bf16 v[88:91], v[120:123], v[40:43], 0\n" \
  "v_xor_b32 v18, 12288, v7\n" \
  "v_mfma_f32_16x16x32_bf16 v[92:95], v[124:127], v[40:43], 0\n" \
  "s_cmp_eq_u32 s61, 0\n" \
  "s_cbranch_scc1 .LCX_NQ_%=\n" \
  "s_mov_b32 s61, 0\n" \
  "s_waitcnt lgkmcnt(0)\n" \
  "v_readfirstlane_b32 s63, v144\n" \
  "v_lshlrev_b32 v144, 5, v5\n" \
  "s_and_b32 s63, s63, 7\n" \
  "s_cmp_eq_u32 s63, 7\n" \
  "s_cbranch_scc0 .LCX_W3_%=\n" \
  "s_mov_b64 exec, 15\n" \
  "global_store_dword v144, v145, s[52:53] sc1\n" \
  "s_mov_b64 exec, -1\n" \
  "s_memrealtime s[4:5]\n" \
  "s_waitcnt lgkmcnt(0)\n" \
  "s_mov_b64 exec, 1\n" \
  "v_mov_b32 v144, s4\n" \
  "v_mov_b32 v145, s5\n" \
  "global_store_dwordx2 v5, v[144:145], s[36:37]\n" \
  "s_mov_b64 exec, -1\n" \
  ".LCX_W3_%=:\n" \
  "s_add_u32 s52, s52, 2048\n" \
  "s_addc_u32 s53, s53, 0\n" \
  "s_add_u32 s36, s36, 8\n" \
  "s_addc_u32 s37, s37, 0\n" \
  ".LCX_NQ_%=:\n" \
  "s_memrealtime s[38:39]\n" \
  "ds_read_b32 v37, v38\n" \
  "v_mfma_f32_16x16x16_bf16 v[64:67], v[136:137], v[44:45], v[64:67]\n" \
  "v_mfma_f32_16x16x16_bf16 v[68:71], v[140:141], v[44:45], v[68:71]\n" \
  "v_mfma_f32_16x16x16_bf16 v[72:75], v[136:137], v[46:47], v[72:75]\n" \
  "v_mfma_f32_16x16x16_bf16 v[76:79], v[140:141], v[46:47], v[76:79]\n" \
  "v_mfma_f32_16x16x16_bf16 v[80:83], v[138:139], v[44:45], v[80:83]\n" \
  "v_mfma_f32_16x16x16_bf16 v[84:87], v[142:143], v[44:45], v[84:87]\n" \
  "v_mfma_f32_16x16x16_bf16 v[88:91], v[138:139], v[46:47], v[88:91]\n" \
  "v_mfma_f32_16x16x16_bf16 v[92:95], v[142:143], v[46:47], v[92:95]\n" \
  "s_nop 0\n" \
  "v_cvt_pk_bf16_f32 v160, v64, v65\n" \
  "v_cvt_pk_bf16_f32 v161, v66, v67\n" \
  "v_cvt_pk_bf16_f32 v162, v68, v69\n" \
  "v_cvt_pk_bf16_f32 v163, v70, v71\n" \
  "ds_write_b128 v7, v[160:163] offset:0\n" \
  "v_cvt_pk_bf16_f32 v164, v72, v73\n" \
  "v_cvt_pk_bf16_f32 v165, v74, v75\n" \
  "v_cvt_pk_bf16_f32 v166, v76, v77\n" \
  "v_cvt_pk_bf16_f32 v167, v78, v79\n" \
  "ds_write_b128 v7, v[164:167] offset:1024\n" \
  "v_cvt_pk_bf16_f32 v168, v80, v81\n" \
  "v_cvt_pk_bf16_f32 v169, v82, v83\n" \
  "v_cvt_pk_bf16_f32 v170, v84, v85\n" \
  "v_cvt_pk_bf16_f32 v171, v86, v87\n" \
  "ds_write_b128 v7, v[168:171] offset:2048\n" \
  "v_cvt_pk_bf16_f32 v172, v88, v89\n" \
  "v_cvt_pk_bf16_f32 v173, v90, v91\n" \
  "v_cvt_pk_bf16_f32 v174, v92, v93\n" \
  "v_cvt_pk_bf16_f32 v175, v94, v95\n" \
  "ds_write_b128 v7, v[172:175] offset:3072\n" \
  "v_xor_b32 v7, 0x4000, v7\n" \
  "v_mfma_f32_16x16x32_bf16 v[112:115], v[192:195], v[160:163], 0\n" \
  "v_mfma_f32_16x16x32_bf16 v[116:119], v[224:227], v[160:163], 0\n" \
  "s_waitcnt lgkmcnt(0)\n" \
  "s_barrier\n" \
  "ds_read_b128 v[64:67], v16 offset:0\n" \
  "ds_read_b128 v[68:71], v16 offset:1024\n" \
  "ds_read_b128 v[72:75], v16 offset:2048\n" \
  "ds_read_b128 v[76:79], v16 offset:3072\n" \
  "ds_read_b128 v[80:83], v17 offset:0\n" \
  "ds_read_b128 v[84:87], v17 offset:1024\n" \
  "ds_read_b128 v[88:91], v17 offset:2048\n" \
  "ds_read_b128 v[92:95], v17 offset:3072\n" \
  "ds_read_b128 v[96:99], v18 offset:0\n" \
  "ds_read_b128 v[100:103], v18 offset:1024\n" \
  "ds_read_b128 v[104:107], v18 offset:2048\n" \
  "ds_read_b128 v[108:111], v18 offset:3072\n" \
  "v_readfirstlane_b32 s73, v37\n" \
  "s_sub_u32 s63, s38, s69\n" \
  "s_bfe_u32 s64, s73, 0x80010\n" \
  "s_sub_u32 s64, s64, s67\n" \
  "s_or_b32 s63, s63, s64\n" \
  "s_bfe_u32 s64, s73, 0x80008\n" \
  "s_and_b32 s73, s73, 0xff\n" \
  "s_min_u32 s73, s73, s64\n" \
  "s_or_b32 s63, s63, s68\n" \
  "s_cmp_lt_i32 s63, 0\n" \
  "s_cbranch_scc1 .LCX_E4_%=\n" \
  "v_mov_b32 v35, 0\n" \
  "global_load_dwordx4 v[128:131], v5, s[48:49] offset:0 sc1\n" \
  "global_load_dwordx4 v[132:135], v5, s[48:49] offset:1024 sc1\n" \
  "global_load_dwordx4 v[136:139], v3, s[48:49] offset:0 sc1\n" \
  "global_load_dwordx4 v[140:143], v3, s[48:49] offset:512 sc1\n" \
  "global_load_dword v35, v35, s[50:51] sc1\n" \
  "v_mov_b32 v33, s67\n" \
  "ds_write_b32 v38, v33 offset:8\n" \
  "s_mov_b32 s68, -1\n" \
  ".LCX_E4_%=:\n" \
  "v_mfma_f32_16x16x32_bf16 v[112:115], v[196:199], v[164:167], v[112:115]\n" \
  "v_mfma_f32_16x16x32_bf16 v[116:119], v[228:231], v[164:167], v[116:119]\n" \
  "v_mfma_f32_16x16x32_bf16 v[112:115], v[200:203], v[168:171], v[112:115]\n" \
  "v_mfma_f32_16x16x32_bf16 v[116:119], v[232:235], v[168:171], v[116:119]\n" \
  "v_mfma_f32_16x16x32_bf16 v[112:115], v[204:207], v[172:175], v[112:115]\n" \
  "v_mfma_f32_16x16x32_bf16 v[116:119], v[236:239], v[172:175], v[116:119]\n" \
  "s_waitcnt lgkmcnt(11)\n" \
  "v_mfma_f32_16x16x32_bf16 v[120:123], v[192:195], v[64:67], 0\n" \
  "v_mfma_f32_16x16x32_bf16 v[124:127], v[224:227], v[64:67], 0\n" \
  "s_waitcnt lgkmcnt(10)\n" \
  "v_mfma_f32_16x16x32_bf16 v[120:123], v[196:199], v[68:71], v[120:123]\n" \
  "v_mfma_f32_16x16x32_bf16 v[124:127], v[228:231], v[68:71], v[124:127]\n" \
  "s_waitcnt lgkmcnt(9)\n" \
  "v_mfma_f32_16x16x32_bf16 v[120:123], v[200:203], v[72:75], v[120:123]\n" \
  "v_mfma_f32_16x16x32_bf16 v[124:127], v[232:235], v[72:75], v[124:127]\n" \
  "s_waitcnt lgkmcnt(8)\n" \
  "v_mfma_f32_16x16x32_bf16 v[120:123], v[204:207], v[76:79], v[120:123]\n" \
  "v_mfma_f32_16x16x32_bf16 v[124:127], v[236:239], v[76:79], v[124:127]\n" \
  "s_waitcnt lgkmcnt(7)\n" \
  "v_mfma_f32_16x16x32_bf16 v[112:115], v[208:211], v[80:83], v[112:115]\n" \
  "v_mfma_f32_16x16x32_bf16 v[116:119], v[240:243], v[80:83], v[116:119]\n" \
  "s_waitcnt lgkmcnt(6)\n" \
  "v_mfma_f32_16x16x32_bf16 v[112:115], v[212:215], v[84:87], v[112:115]\n" \
  "v_mfma_f32_16x16x32_bf16 v[116:119], v[244:247], v[84:87], v[116:119]\n" \
  "s_waitcnt lgkmcnt(5)\n" \
  "v_mfma_f32_16x16x32_bf16 v[112:115], v[216:219], v[88:91], v[112:115]\n" \
  "v_mfma_f32_16x16x32_bf16 v[116:119], v[248:251], v[88:91], v[116:119]\n" \
  "s_waitcnt lgkmcnt(4)\n" \
  "v_mfma_f32_16x16x32_bf16 v[112:115], v[220:223], v[92:95], v[112:115]\n" \
  "v_mfma_f32_16x16x32_bf16 v[116:119], v[252:255], v[92:95], v[116:119]\n" \
  "s_memrealtime s[38:39]\n" \
  "ds_read_b32 v37, v38\n" \
  "s_waitcnt lgkmcnt(4)\n" \
  "v_mfma_f32_16x16x32_bf16 v[120:123], v[208:211], v[96:99], v[120:123]\n" \
  "v_mfma_f32_16x16x32_bf16 v[124:127], v[240:243], v[96:99], v[124:127]\n" \
  "s_waitcnt lgkmcnt(3)\n" \
  "v_mfma_f32_16x16x32_bf16 v[120:123], v[212:215], v[100:103], v[120:123]\n" \
  "v_mfma_f32_16x16x32_bf16 v[124:127], v[244:247], v[100:103], v[124:127]\n" \
  "v_cvt_pk_bf16_f32 v20, v112, v113\n" \
  "v_cvt_pk_bf16_f32 v21, v114, v115\n" \
  "v_cvt_pk_bf16_f32 v22, v116, v117\n" \
  "v_cvt_pk_bf16_f32 v23, v118, v119\n" \
  "s_waitcnt lgkmcnt(2)\n" \
  "v_mfma_f32_16x16x32_bf16 v[120:123], v[216:219], v[104:107], v[120:123]\n" \
  "v_mfma_f32_16x16x32_bf16 v[124:127], v[248:251], v[104:107], v[124:127]\n" \
  "buffer_store_dwordx4 v[20:23], v4, s[40:43], s54 offen sc1\n" \
  "s_waitcnt lgkmcnt(0)\n" \
  "v_mfma_f32_16x16x32_bf16 v[120:123], v[220:223], v[108:111], v[120:123]\n" \
  "v_mfma_f32_16x16x32_bf16 v[124:127], v[252:255], v[108:111], v[124:127]\n" \
  "s_xor_b32 s64, s54, 16384\n" \
  "s_mov_b32 s61, 1\n" \
  "s_nop 3\n" \
  "v_cvt_pk_bf16_f32 v24, v120, v121\n" \
  "v_cvt_pk_bf16_f32 v25, v122, v123\n" \
  "v_cvt_pk_bf16_f32 v26, v124, v125\n" \
  "v_cvt_pk_bf16_f32 v27, v126, v127\n" \
  "buffer_store_dwordx4 v[24:27], v4, s[40:43], s64 offen sc1\n" \
  "v_readfirstlane_b32 s73, v37\n" \
  "s_sub_u32 s63, s38, s69\n" \
  "s_bfe_u32 s64, s73, 0x80010\n" \
  "s_sub_u32 s64, s64, s67\n" \
  "s_or_b32 s63, s63, s64\n" \
  "s_bfe_u32 s64, s73, 0x80008\n" \
  "s_and_b32 s73, s73, 0xff\n" \
  "s_min_u32 s73, s73, s64\n" \
  "s_or_b32 s63, s63, s68\n" \
  "s_cmp_lt_i32 s63, 0\n" \
  "s_cbranch_scc1 .LCX_E5_%=\n" \
  "v_mov_b32 v35, 0\n" \
  "global_load_dwordx4 v[128:131], v5, s[48:49] offset:0 sc1\n" \
  "global_load_dwordx4 v[132:135], v5, s[48:49] offset:1024 sc1\n" \
  "global_load_dwordx4 v[136:139], v3, s[48:49] offset:0 sc1\n" \
  "global_load_dwordx4 v[140:143], v3, s[48:49] offset:512 sc1\n" \
  "global_load_dword v35, v35, s[50:51] sc1\n" \
  "v_mov_b32 v33, s67\n" \
  "ds_write_b32 v38, v33 offset:8\n" \
  "s_mov_b32 s68, -1\n" \
  ".LCX_E5_%=:\n" \
  "s_add_u32 s54, s54, 0x80000\n" \
  "s_sub_u32 s60, s60, 1\n" \
  "s_cmp_lg_u32 s60, 0\n" \
  "s_cbranch_scc1 .LCX_TOP_%=\n" \
  "s_waitcnt vmcnt(0)\n" \
  "v_mov_b32 v144, s59\n" \
  "v_mov_b32 v145, 1\n" \
  "s_mov_b64 exec, 1\n" \
  "ds_add_rtn_u32 v144, v144, v145\n" \
  "s_mov_b64 exec, -1\n" \
  "s_xor_b32 s59, s59, 4\n" \
  "s_mov_b32 s61, 0\n" \
  "s_waitcnt lgkmcnt(0)\n" \
  "v_readfirstlane_b32 s63, v144\n" \
  "v_lshlrev_b32 v144, 5, v5\n" \
  "s_and_b32 s63, s63, 7\n" \
  "s_cmp_eq_u32 s63, 7\n" \
  "s_cbranch_scc0 .LCX_W6_%=\n" \
  "s_mov_b64 exec, 15\n" \
  "global_store_dword v144, v145, s[52:53] sc1\n" \
  "s_mov_b64 exec, -1\n" \
  "s_memrealtime s[4:5]\n" \
  "s_waitcnt lgkmcnt(0)\n" \
  "s_mov_b64 exec, 1\n" \
  "v_mov_b32 v144, s4\n" \
  "v_mov_b32 v145, s5\n" \
  "global_store_dwordx2 v5, v[144:145], s[36:37]\n" \
  "s_mov_b64 exec, -1\n" \
  ".LCX_W6_%=:\n" \
  "s_add_u32 s52, s52, 2048\n" \
  "s_addc_u32 s53, s53, 0\n" \
  "s_add_u32 s36, s36, 8\n" \
  "s_addc_u32 s37, s37, 0\n" \
  ".LCX_END_%=:\n" \
  "s_endpgm\n" \
  ".LCX_FAIL_%=:\n" \
  "v_mov_b64 v[28:29], 0\n" \
  "v_mov_b64 v[30:31], 0\n" \
  "s_cmp_eq_u32 s61, 0\n" \
  "s_cbranch_scc1 .LCX_FAIL2_%=\n" \
  "s_mov_b32 s61, 0\n" \
  "s_waitcnt lgkmcnt(0)\n" \
  "v_readfirstlane_b32 s63, v144\n" \
  "v_lshlrev_b32 v144, 5, v5\n" \
  "s_and_b32 s63, s63, 7\n" \
  "s_cmp_eq_u32 s63, 7\n" \
  "s_cbranch_scc0 .LCX_W7_%=\n" \
  "s_mov_b64 exec, 15\n" \
  "global_store_dword v144, v145, s[52:53] sc1\n" \
  "s_mov_b64 exec, -1\n" \
  "s_memrealtime s[4:5]\n" \
  "s_waitcnt lgkmcnt(0)\n" \
  "s_mov_b64 exec, 1\n" \
  "v_mov_b32 v144, s4\n" \
  "v_mov_b32 v145, s5\n" \
  "global_store_dwordx2 v5, v[144:145], s[36:37]\n" \
  "s_mov_b64 exec, -1\n" \
  ".LCX_W7_%=:\n" \
  "s_add_u32 s52, s52, 2048\n" \
  "s_addc_u32 s53, s53, 0\n" \
  "s_add_u32 s36, s36, 8\n" \
  "s_addc_u32 s37, s37, 0\n" \
  ".LCX_FAIL2_%=:\n" \
  "s_add_u32 s69, s64, -48\n" \
  "s_cmp_lg_u32 s64, 0\n" \
  "s_cbranch_scc1 .LCX_WF_%=\n" \
  "s_sleep 4\n" \
  "global_load_dword v35, v35, s[50:51] sc1\n" \
  "s_sub_u32 s62, s62, 8\n" \
  "s_cbranch_scc1 .LCX_END_%=\n" \
  "s_waitcnt vmcnt(0)\n" \
  "v_readfirstlane_b32 s64, v35\n" \
  "s_branch .LCX_FAIL2_%=\n"
#define AW_ASM_Y \
  "s_mov_b32 s72, 0\n" \
  "s_mov_b32 s67, 0\n" \
  "s_mov_b32 s68, 2\n" \
  "s_mov_b32 s69, 0\n" \
  "s_mov_b32 s65, 49152\n" \
  "s_mov_b32 s66, 16384\n" \
  "v_mov_b32 v5, s58\n" \
  "v_mov_b32 v60, v3\n" \
  "v_add_u32 v3, 4096, v2\n" \
  "s_mov_b32 s62, 0x40000\n" \
  "s_branch .LCY_ASK_%=\n" \
  ".p2align 6\n" \
  ".LCY_BAR_%=:\n" \
  "s_barrier\n" \
  "ds_read_b128 v[8:11], v0 offset:0\n" \
  "ds_read_b128 v[12:15], v0 offset:1024\n" \
  "ds_read_b128 v[16:19], v0 offset:2048\n" \
  "s_add_u32 s68, s68, 1\n" \
  "s_add_u32 s64, s54, 16384\n" \
  "s_cmp_eq_u32 s69, 0\n" \
  "s_cbranch_scc1 .LCY_NI_%=\n" \
  "s_waitcnt vmcnt(3)\n" \
  "s_cmp_lg_u32 s72, 0\n" \
  "s_cbranch_scc1 .LCY_NJ_%=\n" \
  "s_waitcnt vmcnt(0)\n" \
  ".LCY_NJ_%=:\n" \
  "v_mov_b32 v6, s55\n" \
  "s_mov_b64 exec, 1\n" \
  "ds_add_u32 v5, v6\n" \
  "s_mov_b64 exec, -1\n" \
  "s_mov_b32 s69, 0\n" \
  ".LCY_NI_%=:\n" \
  "v_cmp_eq_u32 vcc, 0, v4\n" \
  "s_cmp_eq_u64 vcc, 0\n" \
  "s_cbranch_scc0 .LCY_NS_%=\n" \
  "v_lshlrev_b32 v6, 16, s55\n" \
  "s_mov_b64 exec, 1\n" \
  "ds_add_u32 v5, v6\n" \
  "s_mov_b64 exec, -1\n" \
  "s_mov_b32 m0, s56\n" \
  "s_add_u32 s71, s56, 4096\n" \
  "global_load_lds_dwordx4 v2, s[48:49] offset:0 sc1\n" \
  "global_load_lds_dwordx4 v2, s[48:49] offset:1024 sc1\n" \
  "global_load_lds_dwordx4 v2, s[48:49] offset:2048 sc1\n" \
  "global_load_lds_dwordx4 v2, s[48:49] offset:3072 sc1\n" \
  "s_mov_b32 m0, s71\n" \
  "s_add_u32 s56, s56, s65\n" \
  "global_load_lds_dwordx4 v3, s[48:49] offset:0 sc1\n" \
  "global_load_lds_dwordx4 v3, s[48:49] offset:1024 sc1\n" \
  ".LCY_NS_%=:\n" \
  "s_cmp_eq_u32 s72, 0\n" \
  "s_cbranch_scc1 .LCY_NK_%=\n" \
  "s_waitcnt vmcnt(6)\n" \
  "s_mov_b32 s72, 0\n" \
  "s_cbranch_vccz .LCY_NL_%=\n" \
  "s_waitcnt vmcnt(0)\n" \
  ".LCY_NL_%=:\n" \
  "v_mov_b32 v6, s59\n" \
  "v_mov_b32 v7, 1\n" \
  "s_mov_b64 exec, 1\n" \
  "ds_add_rtn_u32 v6, v6, v7\n" \
  "s_mov_b64 exec, -1\n" \
  "s_xor_b32 s59, s59, 4\n" \
  "s_mov_b32 s61, 0\n" \
  "s_waitcnt lgkmcnt(0)\n" \
  "v_readfirstlane_b32 s63, v6\n" \
  "v_lshlrev_b32 v6, 5, v2\n" \
  "s_and_b32 s63, s63, 7\n" \
  "s_cmp_eq_u32 s63, 7\n" \
  "s_cbranch_scc0 .LCY_W1_%=\n" \
  "s_mov_b64 exec, 15\n" \
  "global_store_dword v6, v7, s[52:53] sc1\n" \
  "s_mov_b64 exec, -1\n" \
  "s_memrealtime s[4:5]\n" \
  "s_waitcnt lgkmcnt(0)\n" \
  "s_mov_b64 exec, 1\n" \
  "v_mov_b32 v6, s4\n" \
  "v_mov_b32 v7, s5\n" \
  "global_store_dwordx2 v2, v[6:7], s[36:37]\n" \
  "s_mov_b64 exec, -1\n" \
  ".LCY_W1_%=:\n" \
  "s_add_u32 s52, s52, 2048\n" \
  "s_addc_u32 s53, s53, 0\n" \
  "s_add_u32 s36, s36, 8\n" \
  "s_addc_u32 s37, s37, 0\n" \
  ".LCY_NK_%=:\n" \
  "ds_read_b128 v[20:23], v0 offset:3072\n" \
  "s_waitcnt lgkmcnt(3)\n" \
  "v_mfma_f32_16x16x32_bf16 v[32:35], v[64:67], v[8:11], 0\n" \
  "v_mfma_f32_16x16x32_bf16 v[36:39], v[96:99], v[8:11], 0\n" \
  "v_mfma_f32_16x16x32_bf16 v[40:43], v[128:131], v[8:11], 0\n" \
  "v_mfma_f32_16x16x32_bf16 v[44:47], v[160:163], v[8:11], 0\n" \
  "v_mfma_f32_16x16x32_bf16 v[48:51], v[192:195], v[8:11], 0\n" \
  "v_mfma_f32_16x16x32_bf16 v[52:55], v[224:227], v[8:11], 0\n" \
  "ds_read_b128 v[8:11], v0 offset:8192\n" \
  "s_waitcnt lgkmcnt(3)\n" \
  "v_mfma_f32_16x16x32_bf16 v[32:35], v[68:71], v[12:15], v[32:35]\n" \
  "v_mfma_f32_16x16x32_bf16 v[36:39], v[100:103], v[12:15], v[36:39]\n" \
  "v_mfma_f32_16x16x32_bf16 v[40:43], v[132:135], v[12:15], v[40:43]\n" \
  "v_mfma_f32_16x16x32_bf16 v[44:47], v[164:167], v[12:15], v[44:47]\n" \
  "v_mfma_f32_16x16x32_bf16 v[48:51], v[196:199], v[12:15], v[48:51]\n" \
  "v_mfma_f32_16x16x32_bf16 v[52:55], v[228:231], v[12:15], v[52:55]\n" \
  "s_cbranch_vccnz .LCY_D2_%=\n" \
  "global_load_lds_dwordx4 v3, s[48:49] offset:2048 sc1\n" \
  ".LCY_D2_%=:\n" \
  "ds_read_b128 v[12:15], v0 offset:9216\n" \
  "s_waitcnt lgkmcnt(3)\n" \
  "v_mfma_f32_16x16x32_bf16 v[32:35], v[72:75], v[16:19], v[32:35]\n" \
  "v_mfma_f32_16x16x32_bf16 v[36:39], v[104:107], v[16:19], v[36:39]\n" \
  "v_mfma_f32_16x16x32_bf16 v[40:43], v[136:139], v[16:19], v[40:43]\n" \
  "v_mfma_f32_16x16x32_bf16 v[44:47], v[168:171], v[16:19], v[44:47]\n" \
  "v_mfma_f32_16x16x32_bf16 v[48:51], v[200:203], v[16:19], v[48:51]\n" \
  "v_mfma_f32_16x16x32_bf16 v[52:55], v[232:235], v[16:19], v[52:55]\n" \
  "s_cbranch_vccnz .LCY_D3_%=\n" \
  "global_load_lds_dwordx4 v3, s[48:49] offset:3072 sc1\n" \
  "s_add_u32 s48, s48, 0x80000\n" \
  "s_addc_u32 s49, s49, 0\n" \
  ".LCY_D3_%=:\n" \
  "ds_read_b128 v[16:19], v0 offset:10240\n" \
  "s_waitcnt lgkmcnt(3)\n" \
  "v_mfma_f32_16x16x32_bf16 v[32:35], v[76:79], v[20:23], v[32:35]\n" \
  "v_mfma_f32_16x16x32_bf16 v[36:39], v[108:111], v[20:23], v[36:39]\n" \
  "v_mfma_f32_16x16x32_bf16 v[40:43], v[140:143], v[20:23], v[40:43]\n" \
  "v_mfma_f32_16x16x32_bf16 v[44:47], v[172:175], v[20:23], v[44:47]\n" \
  "v_mfma_f32_16x16x32_bf16 v[48:51], v[204:207], v[20:23], v[48:51]\n" \
  "v_mfma_f32_16x16x32_bf16 v[52:55], v[236:239], v[20:23], v[52:55]\n" \
  "s_cbranch_vccnz .LCY_D4_%=\n" \
  "s_mov_b32 m0, s57\n" \
  "s_add_u32 s57, s57, s65\n" \
  "global_load_lds_dwordx4 v2, s[50:51] offset:0 sc1\n" \
  ".LCY_D4_%=:\n" \
  "ds_read_b128 v[20:23], v0 offset:11264\n" \
  "s_waitcnt lgkmcnt(3)\n" \
  "v_mfma_f32_16x16x32_bf16 v[32:35], v[80:83], v[8:11], v[32:35]\n" \
  "v_mfma_f32_16x16x32_bf16 v[36:39], v[112:115], v[8:11], v[36:39]\n" \
  "v_mfma_f32_16x16x32_bf16 v[40:43], v[144:147], v[8:11], v[40:43]\n" \
  "v_mfma_f32_16x16x32_bf16 v[44:47], v[176:179], v[8:11], v[44:47]\n" \
  "v_mfma_f32_16x16x32_bf16 v[48:51], v[208:211], v[8:11], v[48:51]\n" \
  "v_mfma_f32_16x16x32_bf16 v[52:55], v[240:243], v[8:11], v[52:55]\n" \
  "s_cbranch_vccnz .LCY_D5_%=\n" \
  "global_load_lds_dwordx4 v2, s[50:51] offset:1024 sc1\n" \
  "s_sub_u32 s65, 0, s65\n" \
  "s_add_u32 s67, s67, 4096\n" \
  ".LCY_D5_%=:\n" \
  "ds_read_b128 v[8:11], v0 offset:4096\n" \
  "s_waitcnt lgkmcnt(3)\n" \
  "v_mfma_f32_16x16x32_bf16 v[32:35], v[84:87], v[12:15], v[32:35]\n" \
  "v_mfma_f32_16x16x32_bf16 v[36:39], v[116:119], v[12:15], v[36:39]\n" \
  "v_mfma_f32_16x16x32_bf16 v[40:43], v[148:151], v[12:15], v[40:43]\n" \
  "v_mfma_f32_16x16x32_bf16 v[44:47], v[180:183], v[12:15], v[44:47]\n" \
  "v_mfma_f32_16x16x32_bf16 v[48:51], v[212:215], v[12:15], v[48:51]\n" \
  "v_mfma_f32_16x16x32_bf16 v[52:55], v[244:247], v[12:15], v[52:55]\n" \
  "s_cbranch_vccnz .LCY_D6_%=\n" \
  "global_load_lds_dwordx4 v2, s[50:51] offset:2048 sc1\n" \
  "s_sub_u32 s68, s68, 1\n" \
  "s_mov_b32 s69, 1\n" \
  ".LCY_D6_%=:\n" \
  "ds_read_b128 v[12:15], v0 offset:5120\n" \
  "s_waitcnt lgkmcnt(3)\n" \
  "v_mfma_f32_16x16x32_bf16 v[32:35], v[88:91], v[16:19], v[32:35]\n" \
  "v_mfma_f32_16x16x32_bf16 v[36:39], v[120:123], v[16:19], v[36:39]\n" \
  "v_mfma_f32_16x16x32_bf16 v[40:43], v[152:155], v[16:19], v[40:43]\n" \
  "v_mfma_f32_16x16x32_bf16 v[44:47], v[184:187], v[16:19], v[44:47]\n" \
  "v_mfma_f32_16x16x32_bf16 v[48:51], v[216:219], v[16:19], v[48:51]\n" \
  "v_mfma_f32_16x16x32_bf16 v[52:55], v[248:251], v[16:19], v[52:55]\n" \
  "s_cbranch_vccnz .LCY_D7_%=\n" \
  "global_load_lds_dwordx4 v2, s[50:51] offset:3072 sc1\n" \
  "s_add_u32 s50, s50, 0x40000\n" \
  "s_addc_u32 s51, s51, 0\n" \
  ".LCY_D7_%=:\n" \
  "ds_read_b128 v[16:19], v0 offset:6144\n" \
  "s_waitcnt lgkmcnt(3)\n" \
  "v_mfma_f32_16x16x32_bf16 v[32:35], v[92:95], v[20:23], v[32:35]\n" \
  "v_mfma_f32_16x16x32_bf16 v[36:39], v[124:127], v[20:23], v[36:39]\n" \
  "v_mfma_f32_16x16x32_bf16 v[40:43], v[156:159], v[20:23], v[40:43]\n" \
  "v_mfma_f32_16x16x32_bf16 v[44:47], v[188:191], v[20:23], v[44:47]\n" \
  "v_mfma_f32_16x16x32_bf16 v[48:51], v[220:223], v[20:23], v[48:51]\n" \
  "v_mfma_f32_16x16x32_bf16 v[52:55], v[252:255], v[20:23], v[52:55]\n" \
  "ds_read_b128 v[20:23], v0 offset:7168\n" \
  "s_waitcnt lgkmcnt(3)\n" \
  "s_nop 1\n" \
  "v_cvt_pk_bf16_f32 v24, v32, v33\n" \
  "v_cvt_pk_bf16_f32 v25, v34, v35\n" \
  "v_cvt_pk_bf16_f32 v26, v36, v37\n" \
  "v_cvt_pk_bf16_f32 v27, v38, v39\n" \
  "s_nop 0\n" \
  "v_mfma_f32_16x16x32_bf16 v[32:35], v[64:67], v[8:11], 0\n" \
  "v_mfma_f32_16x16x32_bf16 v[36:39], v[96:99], v[8:11], 0\n" \
  "buffer_store_dwordx4 v[24:27], v1, s[40:43], s54 offen offset:0 sc1\n" \
  "v_cvt_pk_bf16_f32 v28, v40, v41\n" \
  "v_cvt_pk_bf16_f32 v29, v42, v43\n" \
  "v_cvt_pk_bf16_f32 v30, v44, v45\n" \
  "v_cvt_pk_bf16_f32 v31, v46, v47\n" \
  "s_nop 0\n" \
  "v_mfma_f32_16x16x32_bf16 v[40:43], v[128:131], v[8:11], 0\n" \
  "v_mfma_f32_16x16x32_bf16 v[44:47], v[160:163], v[8:11], 0\n" \
  "buffer_store_dwordx4 v[28:31], v1, s[40:43], s54 offen offset:128 sc1\n" \
  "v_cvt_pk_bf16_f32 v56, v48, v49\n" \
  "v_cvt_pk_bf16_f32 v57, v50, v51\n" \
  "v_cvt_pk_bf16_f32 v58, v52, v53\n" \
  "v_cvt_pk_bf16_f32 v59, v54, v55\n" \
  "s_nop 0\n" \
  "v_mfma_f32_16x16x32_bf16 v[48:51], v[192:195], v[8:11], 0\n" \
  "v_mfma_f32_16x16x32_bf16 v[52:55], v[224:227], v[8:11], 0\n" \
  "buffer_store_dwordx4 v[56:59], v1, s[40:43], s54 offen offset:256 sc1\n" \
  "ds_read_b128 v[8:11], v0 offset:12288\n" \
  "s_waitcnt lgkmcnt(3)\n" \
  "v_mfma_f32_16x16x32_bf16 v[32:35], v[68:71], v[12:15], v[32:35]\n" \
  "v_mfma_f32_16x16x32_bf16 v[36:39], v[100:103], v[12:15], v[36:39]\n" \
  "v_mfma_f32_16x16x32_bf16 v[40:43], v[132:135], v[12:15], v[40:43]\n" \
  "v_mfma_f32_16x16x32_bf16 v[44:47], v[164:167], v[12:15], v[44:47]\n" \
  "v_mfma_f32_16x16x32_bf16 v[48:51], v[196:199], v[12:15], v[48:51]\n" \
  "v_mfma_f32_16x16x32_bf16 v[52:55], v[228:231], v[12:15], v[52:55]\n" \
  "ds_read_b128 v[12:15], v0 offset:13312\n" \
  "s_waitcnt lgkmcnt(3)\n" \
  "v_mfma_f32_16x16x32_bf16 v[32:35], v[72:75], v[16:19], v[32:35]\n" \
  "v_mfma_f32_16x16x32_bf16 v[36:39], v[104:107], v[16:19], v[36:39]\n" \
  "v_mfma_f32_16x16x32_bf16 v[40:43], v[136:139], v[16:19], v[40:43]\n" \
  "v_mfma_f32_16x16x32_bf16 v[44:47], v[168:171], v[16:19], v[44:47]\n" \
  "v_mfma_f32_16x16x32_bf16 v[48:51], v[200:203], v[16:19], v[48:51]\n" \
  "v_mfma_f32_16x16x32_bf16 v[52:55], v[232:235], v[16:19], v[52:55]\n" \
  "ds_read_b128 v[16:19], v0 offset:14336\n" \
  "s_waitcnt lgkmcnt(3)\n" \
  "v_mfma_f32_16x16x32_bf16 v[32:35], v[76:79], v[20:23], v[32:35]\n" \
  "v_mfma_f32_16x16x32_bf16 v[36:39], v[108:111], v[20:23], v[36:39]\n" \
  "v_mfma_f32_16x16x32_bf16 v[40:43], v[140:143], v[20:23], v[40:43]\n" \
  "v_mfma_f32_16x16x32_bf16 v[44:47], v[172:175], v[20:23], v[44:47]\n" \
  "v_mfma_f32_16x16x32_bf16 v[48:51], v[204:207], v[20:23], v[48:51]\n" \
  "v_mfma_f32_16x16x32_bf16 v[52:55], v[236:239], v[20:23], v[52:55]\n" \
  "ds_read_b32 v7, v5 offset:8\n" \
  "ds_read_b128 v[20:23], v0 offset:15360\n" \
  "s_waitcnt lgkmcnt(4)\n" \
  "v_mfma_f32_16x16x32_bf16 v[32:35], v[80:83], v[8:11], v[32:35]\n" \
  "v_mfma_f32_16x16x32_bf16 v[36:39], v[112:115], v[8:11], v[36:39]\n" \
  "v_mfma_f32_16x16x32_bf16 v[40:43], v[144:147], v[8:11], v[40:43]\n" \
  "v_mfma_f32_16x16x32_bf16 v[44:47], v[176:179], v[8:11], v[44:47]\n" \
  "v_mfma_f32_16x16x32_bf16 v[48:51], v[208:211], v[8:11], v[48:51]\n" \
  "v_mfma_f32_16x16x32_bf16 v[52:55], v[240:243], v[8:11], v[52:55]\n" \
  "s_waitcnt lgkmcnt(3)\n" \
  "v_mfma_f32_16x16x32_bf16 v[32:35], v[84:87], v[12:15], v[32:35]\n" \
  "v_mfma_f32_16x16x32_bf16 v[36:39], v[116:119], v[12:15], v[36:39]\n" \
  "v_mfma_f32_16x16x32_bf16 v[40:43], v[148:151], v[12:15], v[40:43]\n" \
  "v_mfma_f32_16x16x32_bf16 v[44:47], v[180:183], v[12:15], v[44:47]\n" \
  "v_mfma_f32_16x16x32_bf16 v[48:51], v[212:215], v[12:15], v[48:51]\n" \
  "v_mfma_f32_16x16x32_bf16 v[52:55], v[244:247], v[12:15], v[52:55]\n" \
  "s_waitcnt lgkmcnt(2)\n" \
  "v_mfma_f32_16x16x32_bf16 v[32:35], v[88:91], v[16:19], v[32:35]\n" \
  "v_mfma_f32_16x16x32_bf16 v[36:39], v[120:123], v[16:19], v[36:39]\n" \
  "v_mfma_f32_16x16x32_bf16 v[40:43], v[152:155], v[16:19], v[40:43]\n" \
  "v_mfma_f32_16x16x32_bf16 v[44:47], v[184:187], v[16:19], v[44:47]\n" \
  "v_mfma_f32_16x16x32_bf16 v[48:51], v[216:219], v[16:19], v[48:51]\n" \
  "v_mfma_f32_16x16x32_bf16 v[52:55], v[248:251], v[16:19], v[52:55]\n" \
  "s_cbranch_vccnz .LCY_NT_%=\n" \
  "v_mov_b32 v4, 0\n" \
  "buffer_load_dword v4, v60, s[44:47], s67 offen sc1\n" \
  ".LCY_NT_%=:\n" \
  "s_waitcnt lgkmcnt(0)\n" \
  "v_mfma_f32_16x16x32_bf16 v[32:35], v[92:95], v[20:23], v[32:35]\n" \
  "v_mfma_f32_16x16x32_bf16 v[36:39], v[124:127], v[20:23], v[36:39]\n" \
  "v_mfma_f32_16x16x32_bf16 v[40:43], v[156:159], v[20:23], v[40:43]\n" \
  "v_mfma_f32_16x16x32_bf16 v[44:47], v[188:191], v[20:23], v[44:47]\n" \
  "v_mfma_f32_16x16x32_bf16 v[48:51], v[220:223], v[20:23], v[48:51]\n" \
  "v_mfma_f32_16x16x32_bf16 v[52:55], v[252:255], v[20:23], v[52:55]\n" \
  "v_add_u32 v0, s66, v0\n" \
  "s_sub_u32 s66, 0, s66\n" \
  "s_add_u32 s54, s54, 0x80000\n" \
  "s_mov_b32 s62, 0x40000\n" \
  "v_readfirstlane_b32 s63, v7\n" \
  "v_cvt_pk_bf16_f32 v24, v32, v33\n" \
  "v_cvt_pk_bf16_f32 v25, v34, v35\n" \
  "v_cvt_pk_bf16_f32 v26, v36, v37\n" \
  "v_cvt_pk_bf16_f32 v27, v38, v39\n" \
  "buffer_store_dwordx4 v[24:27], v1, s[40:43], s64 offen offset:0 sc1\n" \
  "v_cvt_pk_bf16_f32 v28, v40, v41\n" \
  "v_cvt_pk_bf16_f32 v29, v42, v43\n" \
  "v_cvt_pk_bf16_f32 v30, v44, v45\n" \
  "v_cvt_pk_bf16_f32 v31, v46, v47\n" \
  "buffer_store_dwordx4 v[28:31], v1, s[40:43], s64 offen offset:128 sc1\n" \
  "v_cvt_pk_bf16_f32 v56, v48, v49\n" \
  "v_cvt_pk_bf16_f32 v57, v50, v51\n" \
  "v_cvt_pk_bf16_f32 v58, v52, v53\n" \
  "v_cvt_pk_bf16_f32 v59, v54, v55\n" \
  "buffer_store_dwordx4 v[56:59], v1, s[40:43], s64 offen offset:256 sc1\n" \
  "s_cbranch_vccnz .LCY_ND_%=\n" \
  "s_add_u32 s63, s63, s60\n" \
  "s_cmp_eq_u32 s63, 66\n" \
  "s_cselect_b32 s72, 1, 0\n" \
  "s_cbranch_scc1 .LCY_DEF_%=\n" \
  "s_waitcnt vmcnt(0)\n" \
  "v_mov_b32 v6, s55\n" \
  "s_mov_b64 exec, 1\n" \
  "ds_add_u32 v5, v6\n" \
  "s_mov_b64 exec, -1\n" \
  "s_mov_b32 s69, 0\n" \
  "s_branch .LCY_WC_%=\n" \
  ".LCY_ND_%=:\n" \
  "v_mov_b32 v4, 0\n" \
  "buffer_load_dword v4, v60, s[44:47], s67 offen sc1\n" \
  "s_waitcnt vmcnt(0)\n" \
  ".LCY_WC_%=:\n" \
  "v_mov_b32 v6, s59\n" \
  "v_mov_b32 v7, 1\n" \
  "s_mov_b64 exec, 1\n" \
  "ds_add_rtn_u32 v6, v6, v7\n" \
  "s_mov_b64 exec, -1\n" \
  "s_xor_b32 s59, s59, 4\n" \
  "s_mov_b32 s61, 0\n" \
  "s_waitcnt lgkmcnt(0)\n" \
  "v_readfirstlane_b32 s63, v6\n" \
  "v_lshlrev_b32 v6, 5, v2\n" \
  "s_and_b32 s63, s63, 7\n" \
  "s_cmp_eq_u32 s63, 7\n" \
  "s_cbranch_scc0 .LCY_W8_%=\n" \
  "s_mov_b64 exec, 15\n" \
  "global_store_dword v6, v7, s[52:53] sc1\n" \
  "s_mov_b64 exec, -1\n" \
  "s_memrealtime s[4:5]\n" \
  "s_waitcnt lgkmcnt(0)\n" \
  "s_mov_b64 exec, 1\n" \
  "v_mov_b32 v6, s4\n" \
  "v_mov_b32 v7, s5\n" \
  "global_store_dwordx2 v2, v[6:7], s[36:37]\n" \
  "s_mov_b64 exec, -1\n" \
  ".LCY_W8_%=:\n" \
  "s_add_u32 s52, s52, 2048\n" \
  "s_addc_u32 s53, s53, 0\n" \
  "s_add_u32 s36, s36, 8\n" \
  "s_addc_u32 s37, s37, 0\n" \
  ".LCY_DEF_%=:\n" \
  "s_sub_u32 s60, s60, 1\n" \
  "s_cmp_eq_u32 s60, 0\n" \
  "s_cbranch_scc1 .LCY_END_%=\n" \
  ".LCY_NEED_%=:\n" \
  "s_cmp_eq_u32 s68, 0\n" \
  "s_cbranch_scc1 .LCY_BAR_%=\n" \
  "v_cmp_eq_u32 vcc, 0, v4\n" \
  "s_cmp_eq_u64 vcc, 0\n" \
  "s_cbranch_scc1 .LCY_GET_%=\n" \
  "s_cmp_ge_u32 s67, 0x40000\n" \
  "s_cbranch_scc1 .LCY_BAR_%=\n" \
  "s_sleep 4\n" \
  "ds_read_b32 v6, v5 offset:16\n" \
  "s_waitcnt lgkmcnt(0)\n" \
  "v_readfirstlane_b32 s63, v6\n" \
  "s_add_u32 s63, s63, s60\n" \
  "s_cmp_ge_u32 s63, 66\n" \
  "s_cbranch_scc1 .LCY_BAR_%=\n" \
  ".LCY_ASK_%=:\n" \
  "v_mov_b32 v4, 0\n" \
  "buffer_load_dword v4, v60, s[44:47], s67 offen sc1\n" \
  "s_sub_u32 s62, s62, 1\n" \
  "s_cbranch_scc1 .LCY_END_%=\n" \
  "s_waitcnt vmcnt(0)\n" \
  "s_branch .LCY_NEED_%=\n" \
  ".LCY_GET_%=:\n" \
  "v_lshlrev_b32 v6, 16, s55\n" \
  "s_mov_b64 exec, 1\n" \
  "ds_add_u32 v5, v6\n" \
  "s_mov_b64 exec, -1\n" \
  "s_mov_b32 m0, s56\n" \
  "s_add_u32 s71, s56, 4096\n" \
  "global_load_lds_dwordx4 v2, s[48:49] offset:0 sc1\n" \
  "global_load_lds_dwordx4 v2, s[48:49] offset:1024 sc1\n" \
  "global_load_lds_dwordx4 v2, s[48:49] offset:2048 sc1\n" \
  "global_load_lds_dwordx4 v2, s[48:49] offset:3072 sc1\n" \
  "s_mov_b32 m0, s71\n" \
  "s_add_u32 s56, s56, s65\n" \
  "global_load_lds_dwordx4 v3, s[48:49] offset:0 sc1\n" \
  "global_load_lds_dwordx4 v3, s[48:49] offset:1024 sc1\n" \
  "global_load_lds_dwordx4 v3, s[48:49] offset:2048 sc1\n" \
  "global_load_lds_dwordx4 v3, s[48:49] offset:3072 sc1\n" \
  "s_add_u32 s48, s48, 0x80000\n" \
  "s_addc_u32 s49, s49, 0\n" \
  "s_mov_b32 m0, s57\n" \
  "s_add_u32 s57, s57, s65\n" \
  "global_load_lds_dwordx4 v2, s[50:51] offset:0 sc1\n" \
  "global_load_lds_dwordx4 v2, s[50:51] offset:1024 sc1\n" \
  "s_sub_u32 s65, 0, s65\n" \
  "s_add_u32 s67, s67, 4096\n" \
  "global_load_lds_dwordx4 v2, s[50:51] offset:2048 sc1\n" \
  "s_sub_u32 s68, s68, 1\n" \
  "s_mov_b32 s69, 1\n" \
  "global_load_lds_dwordx4 v2, s[50:51] offset:3072 sc1\n" \
  "s_add_u32 s50, s50, 0x40000\n" \
  "s_addc_u32 s51, s51, 0\n" \
  "v_mov_b32 v4, 0\n" \
  "buffer_load_dword v4, v60, s[44:47], s67 offen sc1\n" \
  "s_mov_b32 s61, 0\n" \
  "s_cmp_eq_u32 s68, 0\n" \
  "s_cbranch_scc1 .LCY_BAR_%=\n" \
  "s_waitcnt vmcnt(0)\n" \
  "v_mov_b32 v6, s55\n" \
  "s_mov_b64 exec, 1\n" \
  "ds_add_u32 v5, v6\n" \
  "s_mov_b64 exec, -1\n" \
  "s_mov_b32 s69, 0\n" \
  "s_branch .LCY_NEED_%=\n" \
  ".LCY_END_%=:\n" \
  "s_endpgm\n"
template <bool AT>
__device__ __forceinline__ void role_aw_t(const MegaArgs& A, char* smem, const int L, const int a, const u64 tag) {
  const int w = A.wv;
  const int l = lane_id(), c = l & 15, g = l >> 4;
  const int qt = w & 1, hh = (w >> 1) & 1, h = 2 * a + hh;
  char* ws = A.ws;
  unsigned* FL = (unsigned*)(ws + OFF_FL);
  const unsigned sb = lds_base(smem);
  if (w == 0 && l < 8) reinterpret_cast<unsigned*>(smem + AW_CNT)[l] = 0u;
  constexpr int NP = AT ? 1 : 3;
  const int cp0 = AT ? w : 3 * w - 8;
  v8u wo[2 * NP][4];
  {
    const int q = (l >> 2) & 3, p = l & 3, f = q | ((g & 1) << 2);
    const unsigned ra = sb + (4 * g + q) * 256 + (p & 1) * 16;
#pragma unroll
    for (int r = 0; r < 2; ++r) {
#pragma unroll
      for (int k = 0; k < 2; ++k) {
        const char* base_ = (const char*)A.Wo + ((size_t)((2 * a + r) * DH + 64 * (w & 1) + 32 * k) * DM + 128 * (w >> 1)) * 2;
#pragma unroll
        for (int i = 0; i < 8; ++i) {
          const int fd = g | ((i & 1) << 2);
          glds16(base_, (unsigned)((4 * i + g) * (DM * 2) + ((c ^ (fd << 1)) << 4)), sb + w * 16384 + k * 8192 + i * 1024, WNT & 16);
        }
      }
      SST(4 + 3 * r);
      if (r == 1) ready_wait(A, tag, ready_look(A));
      WAIT_VM(0);
      RAW_BAR();
      SST(5 + 3 * r);
#pragma unroll
      for (int k2 = 0; k2 < 2; ++k2) {
#pragma unroll
        for (int ct = 0; ct < 2 * NP; ++ct) {
          const int cp = cp0 + (ct >> 1);
          const unsigned a_ = ra + (cp >> 2) * 32768 + k2 * 16384 + ((2 * (cp & 3) + (p >> 1)) ^ f) * 32 + (ct & 1) * 8;
          const v2u r0 = trd(a_), r1 = trd(a_ + 4096), r2 = trd(a_ + 8192), r3 = trd(a_ + 12288);
          wo[ct][2 * r + k2] = v8u{r0.x, r0.y, r1.x, r1.y, r2.x, r2.y, r3.x, r3.y};
          asm volatile("" : "+v"(wo[ct][2 * r + k2]));
        }
      }
      RAW_BAR();
      SST(6 + 3 * r);
    }
  }
  WSTAMP2();
  const v4u rsW = mk_srd(ws + OFF_PW, SZ_PW);
  const int dp = (FL_FP - FL_FQ + (L * 16 + a - L * 64 * 32 - h) * 32 + 2 * hh) * 4;
  const v4u rsF = mk_srd(FL + FL_FQ + (L * 64 * 32 + h) * 32, (qt ? 0 : dp) + 64 * 4096 - 128);
  const char* p0 = AT ? ws + OFF_MEMK + L * SZ_MEM + h * 2048 : ws + OFF_QK + L * SZ_QK + ((size_t)h * 16 + 8 * qt) * 1024;
  const char* p1 = AT ? (const char*)(FL + FL_FK + L * 32 + (h >> 3) * 64 + (h & 7) * 2) : ws + OFF_VF + L * SZ_O + ((size_t)h * 8 + 4 * qt) * 1024;
  const unsigned* pFW = FL + FL_FW + (L * 64 * 16 + (a >> 2)) * 32 + (a & 3);
  const unsigned tk = sb + AW_TK + hh * 24 * 1024, voff = (c >> 1) * 2048 + (c & 1) * 64 + g * 16;
  const char* dbg = ws + WS_TOTAL + A.vb * 64 * 8;
#define AW_SOPS                                                                                                                 \
  "{s[40:43]}"(rsW), "{s[44:47]}"(rsF), "{s[48:49]}"(p0), "{s[50:51]}"(p1), "{s[52:53]}"(pFW),                                   \
      "{s54}"((L * 64 * 16 + a) * 32768 + cp0 * 128 + (AT ? qt * 16384 : 0)), "{s55}"(1 << (8 * qt)), "{s56}"(tk + qt * 8192), "{s57}"(tk + 16384 + qt * 4096),                  \
      "{s58}"(sb + AW_CNT + 8 + 4 * hh), "{s59}"(sb + AW_CNT), "{s60}"(NSEG), "{s[36:37]}"(dbg)                                  \
      : "memory", "vcc", "scc", "s80"
  if constexpr (AT) {
    const v8u pv = {tk + l * 16, (c & 8) ? 0xEEEEu : 0x4444u, 0u, (unsigned)(OFF_MEMV - OFF_MEMK) + c * 32 + (g & 1) * 16 + (g >> 1) * 1024,
                    voff, (unsigned)l * 16, tk + qt * 4096 + l * 16, sb + AW_SO + w * 4096 + l * 16};
    const unsigned neg = 0xff61b1e6u;
    v8u bt;
#pragma unroll
    for (int i = 0; i < 4; ++i) {
      const unsigned d = 4 * g + i > c ? neg : 0u;
      bt[i] = qt ? 0u : d;
      bt[4 + i] = qt ? d : neg;
    }
    asm volatile("s_mov_b64 exec, -1\ns_waitcnt vmcnt(0) lgkmcnt(0)\ns_nop 4\n" AW_ASM_X ".LKG_LAST:\n"
                 ::"{v[192:199]}"(wo[0][0]), "{v[200:207]}"(wo[0][1]), "{v[208:215]}"(wo[0][2]), "{v[216:223]}"(wo[0][3]),
                 "{v[224:231]}"(wo[1][0]), "{v[232:239]}"(wo[1][1]), "{v[240:247]}"(wo[1][2]), "{v[248:255]}"(wo[1][3]),
                 "{v[0:7]}"(pv), "{v[8:15]}"(bt), AW_SOPS);
  } else {
    const v4u pv = {sb + AW_SO + l * 16, voff, (unsigned)l * 16, (l & 2) ? 16u + 8 * qt : qt ? 8u * (l & 1) : dp + 4 * (l & 1)};
    asm volatile("s_mov_b64 exec, -1\ns_waitcnt vmcnt(0) lgkmcnt(0)\ns_nop 4\n" AW_ASM_Y
                 ::"{v[64:71]}"(wo[0][0]), "{v[72:79]}"(wo[0][1]), "{v[80:87]}"(wo[0][2]), "{v[88:95]}"(wo[0][3]),
                 "{v[96:103]}"(wo[1][0]), "{v[104:111]}"(wo[1][1]), "{v[112:119]}"(wo[1][2]), "{v[120:127]}"(wo[1][3]),
                 "{v[128:135]}"(wo[2][0]), "{v[136:143]}"(wo[2][1]), "{v[144:151]}"(wo[2][2]), "{v[152:159]}"(wo[2][3]),
                 "{v[160:167]}"(wo[3][0]), "{v[168:175]}"(wo[3][1]), "{v[176:183]}"(wo[3][2]), "{v[184:191]}"(wo[3][3]),
                 "{v[192:199]}"(wo[4][0]), "{v[200:207]}"(wo[4][1]), "{v[208:215]}"(wo[4][2]), "{v[216:223]}"(wo[4][3]),
                 "{v[224:231]}"(wo[5][0]), "{v[232:239]}"(wo[5][1]), "{v[240:247]}"(wo[5][2]), "{v[248:255]}"(wo[5][3]),
                 "{v[0:3]}"(pv), AW_SOPS);
  }
#undef AW_SOPS
}
__device__ __forceinline__ void role_aw(const MegaArgs& A, char* smem, const int L, const int a, const u64 tag) {
  if (A.wv < 4) role_aw_t<true>(A, smem, L, a, tag);
  else role_aw_t<false>(A, smem, L, a, tag);
}

#define P_ASM \
  "s_mov_b32 s68, 0\n" \
  "s_mov_b32 s66, 3\n" \
  "s_mov_b32 s67, 2\n" \
  "s_mov_b32 s72, 0\n" \
  "v_mov_b32 v7, 1\n" \
  "v_mov_b32 v6, s70\n" \
  "v_mov_b32 v5, 0\n" \
  "v_add_u32 v10, 4096, v2\n" \
  ".p2align 6\n" \
  ".LCP_TOP_%=:\n" \
  "ds_read_b128 v[28:31], v1 offset:0\n" \
  "ds_read_b128 v[32:35], v1 offset:1024\n" \
  "s_add_u32 s66, s66, 3\n" \
  "s_add_u32 s67, s67, 2\n" \
  "s_mov_b32 s69, 0x400000\n" \
  "v_xor_b32 v0, 0x8000, v0\n" \
  "v_xor_b32 v3, 0x100, v3\n" \
  "v_readfirstlane_b32 s71, v5\n" \
  "s_cmp_ge_u32 s71, s66\n" \
  "s_cbranch_scc1 .LCP_GO_%=\n" \
  "s_cmp_eq_u32 s68, 1\n" \
  "s_cbranch_scc0 .LCP_X1_%=\n" \
  "s_waitcnt vmcnt(0)\n" \
  "s_mov_b64 exec, 1\n" \
  "buffer_store_dword v7, off, s[56:59], s72 sc1\n" \
  "s_memrealtime s[4:5]\n" \
  "s_lshr_b32 s6, s72, 9\n" \
  "s_add_u32 s6, s6, s36\n" \
  "s_addc_u32 s7, s37, 0\n" \
  "s_waitcnt lgkmcnt(0)\n" \
  "v_mov_b32 v11, s4\n" \
  "global_store_dword v2, v11, s[6:7]\n" \
  "v_mov_b32 v11, s5\n" \
  "global_store_dword v2, v11, s[6:7] offset:4\n" \
  "s_mov_b64 exec, -1\n" \
  "s_add_u32 s72, s72, 0x1000\n" \
  "s_mov_b32 s68, 0\n" \
  ".LCP_X1_%=:\n" \
  ".LCP_S2_%=:\n" \
  "s_sleep 1\n" \
  "ds_read_b32 v5, v6 offset:0\n" \
  "s_sub_u32 s69, s69, 1\n" \
  "s_cbranch_scc1 .LCP_END_%=\n" \
  "s_waitcnt lgkmcnt(0)\n" \
  "v_readfirstlane_b32 s71, v5\n" \
  "s_cmp_ge_u32 s71, s66\n" \
  "s_cbranch_scc1 .LCP_GO_%=\n" \
  "s_branch .LCP_S2_%=\n" \
  ".LCP_GO_%=:\n" \
  "ds_read_b128 v[12:15], v0 offset:0\n" \
  "ds_read_b128 v[16:19], v0 offset:2048\n" \
  "ds_read_b128 v[20:23], v0 offset:4096\n" \
  "ds_read_b128 v[24:27], v0 offset:6144\n" \
  "ds_read_b128 v[36:39], v1 offset:2048\n" \
  "s_waitcnt lgkmcnt(4)\n" \
  "v_mfma_f32_16x16x32_bf16 v[48:51], v[64:67], v[12:15], 0\n" \
  "v_mfma_f32_16x16x32_bf16 v[52:55], v[128:131], v[12:15], 0\n" \
  "v_mfma_f32_16x16x32_bf16 v[56:59], v[192:195], v[12:15], 0\n" \
  "v_mfma_f32_16x16x32_bf16 v[60:63], v[28:31], v[12:15], 0\n" \
  "ds_read_b128 v[12:15], v0 offset:8192\n" \
  "ds_read_b128 v[28:31], v1 offset:3072\n" \
  "s_waitcnt lgkmcnt(5)\n" \
  "v_mfma_f32_16x16x32_bf16 v[48:51], v[68:71], v[16:19], v[48:51]\n" \
  "v_mfma_f32_16x16x32_bf16 v[52:55], v[132:135], v[16:19], v[52:55]\n" \
  "v_mfma_f32_16x16x32_bf16 v[56:59], v[196:199], v[16:19], v[56:59]\n" \
  "v_mfma_f32_16x16x32_bf16 v[60:63], v[32:35], v[16:19], v[60:63]\n" \
  "ds_read_b128 v[16:19], v0 offset:10240\n" \
  "ds_read_b128 v[32:35], v1 offset:4096\n" \
  "s_waitcnt lgkmcnt(6)\n" \
  "v_mfma_f32_16x16x32_bf16 v[48:51], v[72:75], v[20:23], v[48:51]\n" \
  "v_mfma_f32_16x16x32_bf16 v[52:55], v[136:139], v[20:23], v[52:55]\n" \
  "v_mfma_f32_16x16x32_bf16 v[56:59], v[200:203], v[20:23], v[56:59]\n" \
  "s_waitcnt lgkmcnt(4)\n" \
  "v_mfma_f32_16x16x32_bf16 v[60:63], v[36:39], v[20:23], v[60:63]\n" \
  "ds_read_b128 v[20:23], v0 offset:12288\n" \
  "ds_read_b128 v[36:39], v1 offset:5120\n" \
  "v_mfma_f32_16x16x32_bf16 v[48:51], v[76:79], v[24:27], v[48:51]\n" \
  "v_mfma_f32_16x16x32_bf16 v[52:55], v[140:143], v[24:27], v[52:55]\n" \
  "v_mfma_f32_16x16x32_bf16 v[56:59], v[204:207], v[24:27], v[56:59]\n" \
  "s_waitcnt lgkmcnt(4)\n" \
  "v_mfma_f32_16x16x32_bf16 v[60:63], v[28:31], v[24:27], v[60:63]\n" \
  "ds_read_b128 v[24:27], v0 offset:14336\n" \
  "ds_read_b128 v[28:31], v1 offset:6144\n" \
  "v_mfma_f32_16x16x32_bf16 v[48:51], v[80:83], v[12:15], v[48:51]\n" \
  "v_mfma_f32_16x16x32_bf16 v[52:55], v[144:147], v[12:15], v[52:55]\n" \
  "v_mfma_f32_16x16x32_bf16 v[56:59], v[208:211], v[12:15], v[56:59]\n" \
  "s_waitcnt lgkmcnt(4)\n" \
  "v_mfma_f32_16x16x32_bf16 v[60:63], v[32:35], v[12:15], v[60:63]\n" \
  "ds_read_b128 v[12:15], v0 offset:16384\n" \
  "ds_read_b128 v[32:35], v1 offset:7168\n" \
  "v_mfma_f32_16x16x32_bf16 v[48:51], v[84:87], v[16:19], v[48:51]\n" \
  "v_mfma_f32_16x16x32_bf16 v[52:55], v[148:151], v[16:19], v[52:55]\n" \
  "v_mfma_f32_16x16x32_bf16 v[56:59], v[212:215], v[16:19], v[56:59]\n" \
  "s_waitcnt lgkmcnt(4)\n" \
  "v_mfma_f32_16x16x32_bf16 v[60:63], v[36:39], v[16:19], v[60:63]\n" \
  "ds_read_b128 v[16:19], v0 offset:18432\n" \
  "ds_read_b128 v[36:39], v1 offset:8192\n" \
  "v_mfma_f32_16x16x32_bf16 v[48:51], v[88:91], v[20:23], v[48:51]\n" \
  "v_mfma_f32_16x16x32_bf16 v[52:55], v[152:155], v[20:23], v[52:55]\n" \
  "v_mfma_f32_16x16x32_bf16 v[56:59], v[216:219], v[20:23], v[56:59]\n" \
  "s_waitcnt lgkmcnt(4)\n" \
  "v_mfma_f32_16x16x32_bf16 v[60:63], v[28:31], v[20:23], v[60:63]\n" \
  "ds_read_b128 v[20:23], v0 offset:20480\n" \
  "ds_read_b128 v[28:31], v1 offset:9216\n" \
  "v_mfma_f32_16x16x32_bf16 v[48:51], v[92:95], v[24:27], v[48:51]\n" \
  "v_mfma_f32_16x16x32_bf16 v[52:55], v[156:159], v[24:27], v[52:55]\n" \
  "v_mfma_f32_16x16x32_bf16 v[56:59], v[220:223], v[24:27], v[56:59]\n" \
  "s_waitcnt lgkmcnt(4)\n" \
  "v_mfma_f32_16x16x32_bf16 v[60:63], v[32:35], v[24:27], v[60:63]\n" \
  "ds_read_b128 v[24:27], v0 offset:22528\n" \
  "ds_read_b128 v[32:35], v1 offset:10240\n" \
  "v_mfma_f32_16x16x32_bf16 v[48:51], v[96:99], v[12:15], v[48:51]\n" \
  "v_mfma_f32_16x16x32_bf16 v[52:55], v[160:163], v[12:15], v[52:55]\n" \
  "v_mfma_f32_16x16x32_bf16 v[56:59], v[224:227], v[12:15], v[56:59]\n" \
  "s_waitcnt lgkmcnt(4)\n" \
  "v_mfma_f32_16x16x32_bf16 v[60:63], v[36:39], v[12:15], v[60:63]\n" \
  "ds_read_b128 v[12:15], v0 offset:24576\n" \
  "ds_read_b128 v[36:39], v1 offset:11264\n" \
  "v_mfma_f32_16x16x32_bf16 v[48:51], v[100:103], v[16:19], v[48:51]\n" \
  "v_mfma_f32_16x16x32_bf16 v[52:55], v[164:167], v[16:19], v[52:55]\n" \
  "v_mfma_f32_16x16x32_bf16 v[56:59], v[228:231], v[16:19], v[56:59]\n" \
  "s_waitcnt lgkmcnt(4)\n" \
  "v_mfma_f32_16x16x32_bf16 v[60:63], v[28:31], v[16:19], v[60:63]\n" \
  "ds_read_b128 v[16:19], v0 offset:26624\n" \
  "ds_read_b128 v[28:31], v1 offset:12288\n" \
  "v_mfma_f32_16x16x32_bf16 v[48:51], v[104:107], v[20:23], v[48:51]\n" \
  "v_mfma_f32_16x16x32_bf16 v[52:55], v[168:171], v[20:23], v[52:55]\n" \
  "v_mfma_f32_16x16x32_bf16 v[56:59], v[232:235], v[20:23], v[56:59]\n" \
  "s_waitcnt lgkmcnt(4)\n" \
  "v_mfma_f32_16x16x32_bf16 v[60:63], v[32:35], v[20:23], v[60:63]\n" \
  "ds_read_b128 v[20:23], v0 offset:28672\n" \
  "ds_read_b128 v[32:35], v1 offset:13312\n" \
  "v_mfma_f32_16x16x32_bf16 v[48:51], v[108:111], v[24:27], v[48:51]\n" \
  "v_mfma_f32_16x16x32_bf16 v[52:55], v[172:175], v[24:27], v[52:55]\n" \
  "v_mfma_f32_16x16x32_bf16 v[56:59], v[236:239], v[24:27], v[56:59]\n" \
  "s_waitcnt lgkmcnt(4)\n" \
  "v_mfma_f32_16x16x32_bf16 v[60:63], v[36:39], v[24:27], v[60:63]\n" \
  "s_cmp_eq_u32 s68, 1\n" \
  "s_cbranch_scc0 .LCP_X3_%=\n" \
  "s_waitcnt vmcnt(0)\n" \
  "s_mov_b64 exec, 1\n" \
  "buffer_store_dword v7, off, s[56:59], s72 sc1\n" \
  "s_memrealtime s[4:5]\n" \
  "s_lshr_b32 s6, s72, 9\n" \
  "s_add_u32 s6, s6, s36\n" \
  "s_addc_u32 s7, s37, 0\n" \
  "s_waitcnt lgkmcnt(0)\n" \
  "v_mov_b32 v11, s4\n" \
  "global_store_dword v2, v11, s[6:7]\n" \
  "v_mov_b32 v11, s5\n" \
  "global_store_dword v2, v11, s[6:7] offset:4\n" \
  "s_mov_b64 exec, -1\n" \
  "s_add_u32 s72, s72, 0x1000\n" \
  "s_mov_b32 s68, 0\n" \
  ".LCP_X3_%=:\n" \
  "ds_read_b32 v5, v6 offset:16\n" \
  "ds_read_b128 v[24:27], v0 offset:30720\n" \
  "ds_read_b128 v[36:39], v1 offset:14336\n" \
  "v_mfma_f32_16x16x32_bf16 v[48:51], v[112:115], v[12:15], v[48:51]\n" \
  "v_mfma_f32_16x16x32_bf16 v[52:55], v[176:179], v[12:15], v[52:55]\n" \
  "v_mfma_f32_16x16x32_bf16 v[56:59], v[240:243], v[12:15], v[56:59]\n" \
  "s_waitcnt lgkmcnt(5)\n" \
  "v_mfma_f32_16x16x32_bf16 v[60:63], v[28:31], v[12:15], v[60:63]\n" \
  "ds_read_b128 v[12:15], v0 offset:256\n" \
  "ds_read_b128 v[28:31], v1 offset:15360\n" \
  "ds_read_b128 v[40:43], v4 offset:0\n" \
  "ds_read_b128 v[44:47], v4 offset:64\n" \
  "v_mfma_f32_16x16x32_bf16 v[48:51], v[116:119], v[16:19], v[48:51]\n" \
  "v_mfma_f32_16x16x32_bf16 v[52:55], v[180:183], v[16:19], v[52:55]\n" \
  "v_mfma_f32_16x16x32_bf16 v[56:59], v[244:247], v[16:19], v[56:59]\n" \
  "s_waitcnt lgkmcnt(7)\n" \
  "v_mfma_f32_16x16x32_bf16 v[60:63], v[32:35], v[16:19], v[60:63]\n" \
  "s_waitcnt lgkmcnt(6)\n" \
  "v_readfirstlane_b32 s71, v5\n" \
  "s_cmp_ge_u32 s71, s67\n" \
  "s_cbranch_scc1 .LCP_ST_%=\n" \
  ".LCP_S4_%=:\n" \
  "s_sleep 1\n" \
  "ds_read_b32 v5, v6 offset:16\n" \
  "s_sub_u32 s69, s69, 1\n" \
  "s_cbranch_scc1 .LCP_END_%=\n" \
  "s_waitcnt lgkmcnt(0)\n" \
  "v_readfirstlane_b32 s71, v5\n" \
  "s_cmp_ge_u32 s71, s67\n" \
  "s_cbranch_scc1 .LCP_ST_%=\n" \
  "s_branch .LCP_S4_%=\n" \
  ".LCP_ST_%=:\n" \
  "ds_read_b128 v[16:19], v0 offset:2304\n" \
  "ds_read_b128 v[32:35], v4 offset:128\n" \
  "ds_read2_b32 v[8:9], v3 offset0:0 offset1:32\n" \
  "v_mfma_f32_16x16x32_bf16 v[48:51], v[120:123], v[20:23], v[48:51]\n" \
  "v_mfma_f32_16x16x32_bf16 v[52:55], v[184:187], v[20:23], v[52:55]\n" \
  "v_mfma_f32_16x16x32_bf16 v[56:59], v[248:251], v[20:23], v[56:59]\n" \
  "s_waitcnt lgkmcnt(7)\n" \
  "v_mfma_f32_16x16x32_bf16 v[60:63], v[36:39], v[20:23], v[60:63]\n" \
  "ds_read_b128 v[20:23], v0 offset:4352\n" \
  "ds_read_b128 v[36:39], v4 offset:192\n" \
  "v_mfma_f32_16x16x32_bf16 v[48:51], v[124:127], v[24:27], v[48:51]\n" \
  "v_mfma_f32_16x16x32_bf16 v[52:55], v[188:191], v[24:27], v[52:55]\n" \
  "v_mfma_f32_16x16x32_bf16 v[56:59], v[252:255], v[24:27], v[56:59]\n" \
  "s_waitcnt lgkmcnt(7)\n" \
  "v_mfma_f32_16x16x32_bf16 v[60:63], v[28:31], v[24:27], v[60:63]\n" \
  "s_waitcnt lgkmcnt(0)\n" \
  "s_nop 4\n" \
  "v_fma_f32 v48, -v8, v40, v48\n" \
  "v_fma_f32 v49, -v8, v41, v49\n" \
  "v_fma_f32 v50, -v8, v42, v50\n" \
  "v_fma_f32 v51, -v8, v43, v51\n" \
  "v_fma_f32 v52, -v8, v44, v52\n" \
  "v_fma_f32 v53, -v8, v45, v53\n" \
  "v_fma_f32 v54, -v8, v46, v54\n" \
  "v_fma_f32 v55, -v8, v47, v55\n" \
  "v_fma_f32 v56, -v8, v32, v56\n" \
  "v_fma_f32 v57, -v8, v33, v57\n" \
  "v_fma_f32 v58, -v8, v34, v58\n" \
  "v_fma_f32 v59, -v8, v35, v59\n" \
  "v_fma_f32 v60, -v8, v36, v60\n" \
  "v_fma_f32 v61, -v8, v37, v61\n" \
  "v_fma_f32 v62, -v8, v38, v62\n" \
  "v_fma_f32 v63, -v8, v39, v63\n" \
  "ds_read_b128 v[32:35], v1 offset:0\n" \
  "ds_read_b128 v[36:39], v1 offset:1024\n" \
  "v_mul_f32 v48, v48, v9\n" \
  "v_mul_f32 v49, v49, v9\n" \
  "v_mul_f32 v50, v50, v9\n" \
  "v_mul_f32 v51, v51, v9\n" \
  "v_mul_f32 v52, v52, v9\n" \
  "v_mul_f32 v53, v53, v9\n" \
  "v_mul_f32 v54, v54, v9\n" \
  "v_mul_f32 v55, v55, v9\n" \
  "v_cvt_pk_bf16_f32 v40, v48, v49\n" \
  "v_cvt_pk_bf16_f32 v41, v50, v51\n" \
  "v_cvt_pk_bf16_f32 v42, v52, v53\n" \
  "v_cvt_pk_bf16_f32 v43, v54, v55\n" \
  "buffer_store_dwordx4 v[40:43], v2, s[40:43], s65 offen offset:0 sc1\n" \
  "v_mul_f32 v56, v56, v9\n" \
  "v_mul_f32 v57, v57, v9\n" \
  "v_mul_f32 v58, v58, v9\n" \
  "v_mul_f32 v59, v59, v9\n" \
  "v_mul_f32 v60, v60, v9\n" \
  "v_mul_f32 v61, v61, v9\n" \
  "v_mul_f32 v62, v62, v9\n" \
  "v_mul_f32 v63, v63, v9\n" \
  "v_cvt_pk_bf16_f32 v44, v56, v57\n" \
  "v_cvt_pk_bf16_f32 v45, v58, v59\n" \
  "v_cvt_pk_bf16_f32 v46, v60, v61\n" \
  "v_cvt_pk_bf16_f32 v47, v62, v63\n" \
  "buffer_store_dwordx4 v[44:47], v2, s[40:43], s65 offen offset:1024 sc1\n" \
  "ds_read_b128 v[24:27], v0 offset:6400\n" \
  "ds_read_b128 v[28:31], v1 offset:2048\n" \
  "v_mfma_f32_16x16x32_bf16 v[48:51], v[64:67], v[12:15], 0\n" \
  "v_mfma_f32_16x16x32_bf16 v[52:55], v[128:131], v[12:15], 0\n" \
  "v_mfma_f32_16x16x32_bf16 v[56:59], v[192:195], v[12:15], 0\n" \
  "s_waitcnt lgkmcnt(3)\n" \
  "v_mfma_f32_16x16x32_bf16 v[60:63], v[32:35], v[12:15], 0\n" \
  "ds_read_b128 v[12:15], v0 offset:8448\n" \
  "ds_read_b128 v[32:35], v1 offset:3072\n" \
  "v_mfma_f32_16x16x32_bf16 v[48:51], v[68:71], v[16:19], v[48:51]\n" \
  "v_mfma_f32_16x16x32_bf16 v[52:55], v[132:135], v[16:19], v[52:55]\n" \
  "v_mfma_f32_16x16x32_bf16 v[56:59], v[196:199], v[16:19], v[56:59]\n" \
  "s_waitcnt lgkmcnt(4)\n" \
  "v_mfma_f32_16x16x32_bf16 v[60:63], v[36:39], v[16:19], v[60:63]\n" \
  "ds_read_b128 v[16:19], v0 offset:10496\n" \
  "ds_read_b128 v[36:39], v1 offset:4096\n" \
  "v_mfma_f32_16x16x32_bf16 v[48:51], v[72:75], v[20:23], v[48:51]\n" \
  "v_mfma_f32_16x16x32_bf16 v[52:55], v[136:139], v[20:23], v[52:55]\n" \
  "v_mfma_f32_16x16x32_bf16 v[56:59], v[200:203], v[20:23], v[56:59]\n" \
  "s_waitcnt lgkmcnt(4)\n" \
  "v_mfma_f32_16x16x32_bf16 v[60:63], v[28:31], v[20:23], v[60:63]\n" \
  "ds_read_b128 v[20:23], v0 offset:12544\n" \
  "ds_read_b128 v[28:31], v1 offset:5120\n" \
  "v_mfma_f32_16x16x32_bf16 v[48:51], v[76:79], v[24:27], v[48:51]\n" \
  "v_mfma_f32_16x16x32_bf16 v[52:55], v[140:143], v[24:27], v[52:55]\n" \
  "v_mfma_f32_16x16x32_bf16 v[56:59], v[204:207], v[24:27], v[56:59]\n" \
  "s_waitcnt lgkmcnt(4)\n" \
  "v_mfma_f32_16x16x32_bf16 v[60:63], v[32:35], v[24:27], v[60:63]\n" \
  "ds_read_b128 v[24:27], v0 offset:14592\n" \
  "ds_read_b128 v[32:35], v1 offset:6144\n" \
  "v_mfma_f32_16x16x32_bf16 v[48:51], v[80:83], v[12:15], v[48:51]\n" \
  "v_mfma_f32_16x16x32_bf16 v[52:55], v[144:147], v[12:15], v[52:55]\n" \
  "v_mfma_f32_16x16x32_bf16 v[56:59], v[208:211], v[12:15], v[56:59]\n" \
  "s_waitcnt lgkmcnt(4)\n" \
  "v_mfma_f32_16x16x32_bf16 v[60:63], v[36:39], v[12:15], v[60:63]\n" \
  "ds_read_b128 v[12:15], v0 offset:16640\n" \
  "ds_read_b128 v[36:39], v1 offset:7168\n" \
  "v_mfma_f32_16x16x32_bf16 v[48:51], v[84:87], v[16:19], v[48:51]\n" \
  "v_mfma_f32_16x16x32_bf16 v[52:55], v[148:151], v[16:19], v[52:55]\n" \
  "v_mfma_f32_16x16x32_bf16 v[56:59], v[212:215], v[16:19], v[56:59]\n" \
  "s_waitcnt lgkmcnt(4)\n" \
  "v_mfma_f32_16x16x32_bf16 v[60:63], v[28:31], v[16:19], v[60:63]\n" \
  "ds_read_b128 v[16:19], v0 offset:18688\n" \
  "ds_read_b128 v[28:31], v1 offset:8192\n" \
  "v_mfma_f32_16x16x32_bf16 v[48:51], v[88:91], v[20:23], v[48:51]\n" \
  "v_mfma_f32_16x16x32_bf16 v[52:55], v[152:155], v[20:23], v[52:55]\n" \
  "v_mfma_f32_16x16x32_bf16 v[56:59], v[216:219], v[20:23], v[56:59]\n" \
  "s_waitcnt lgkmcnt(4)\n" \
  "v_mfma_f32_16x16x32_bf16 v[60:63], v[32:35], v[20:23], v[60:63]\n" \
  "ds_read_b128 v[20:23], v0 offset:20736\n" \
  "ds_read_b128 v[32:35], v1 offset:9216\n" \
  "v_mfma_f32_16x16x32_bf16 v[48:51], v[92:95], v[24:27], v[48:51]\n" \
  "v_mfma_f32_16x16x32_bf16 v[52:55], v[156:159], v[24:27], v[52:55]\n" \
  "v_mfma_f32_16x16x32_bf16 v[56:59], v[220:223], v[24:27], v[56:59]\n" \
  "s_waitcnt lgkmcnt(4)\n" \
  "v_mfma_f32_16x16x32_bf16 v[60:63], v[36:39], v[24:27], v[60:63]\n" \
  "ds_read_b128 v[24:27], v0 offset:22784\n" \
  "ds_read_b128 v[36:39], v1 offset:10240\n" \
  "v_mfma_f32_16x16x32_bf16 v[48:51], v[96:99], v[12:15], v[48:51]\n" \
  "v_mfma_f32_16x16x32_bf16 v[52:55], v[160:163], v[12:15], v[52:55]\n" \
  "v_mfma_f32_16x16x32_bf16 v[56:59], v[224:227], v[12:15], v[56:59]\n" \
  "s_waitcnt lgkmcnt(4)\n" \
  "v_mfma_f32_16x16x32_bf16 v[60:63], v[28:31], v[12:15], v[60:63]\n" \
  "ds_read_b128 v[12:15], v0 offset:24832\n" \
  "ds_read_b128 v[28:31], v1 offset:11264\n" \
  "v_mfma_f32_16x16x32_bf16 v[48:51], v[100:103], v[16:19], v[48:51]\n" \
  "v_mfma_f32_16x16x32_bf16 v[52:55], v[164:167], v[16:19], v[52:55]\n" \
  "v_mfma_f32_16x16x32_bf16 v[56:59], v[228:231], v[16:19], v[56:59]\n" \
  "s_waitcnt lgkmcnt(4)\n" \
  "v_mfma_f32_16x16x32_bf16 v[60:63], v[32:35], v[16:19], v[60:63]\n" \
  "ds_read_b128 v[16:19], v0 offset:26880\n" \
  "ds_read_b128 v[32:35], v1 offset:12288\n" \
  "v_mfma_f32_16x16x32_bf16 v[48:51], v[104:107], v[20:23], v[48:51]\n" \
  "v_mfma_f32_16x16x32_bf16 v[52:55], v[168:171], v[20:23], v[52:55]\n" \
  "v_mfma_f32_16x16x32_bf16 v[56:59], v[232:235], v[20:23], v[56:59]\n" \
  "s_waitcnt lgkmcnt(4)\n" \
  "v_mfma_f32_16x16x32_bf16 v[60:63], v[36:39], v[20:23], v[60:63]\n" \
  "ds_read_b128 v[20:23], v0 offset:28928\n" \
  "ds_read_b128 v[36:39], v1 offset:13312\n" \
  "v_mfma_f32_16x16x32_bf16 v[48:51], v[108:111], v[24:27], v[48:51]\n" \
  "v_mfma_f32_16x16x32_bf16 v[52:55], v[172:175], v[24:27], v[52:55]\n" \
  "v_mfma_f32_16x16x32_bf16 v[56:59], v[236:239], v[24:27], v[56:59]\n" \
  "s_waitcnt lgkmcnt(4)\n" \
  "v_mfma_f32_16x16x32_bf16 v[60:63], v[28:31], v[24:27], v[60:63]\n" \
  "ds_read_b128 v[24:27], v0 offset:30976\n" \
  "ds_read_b128 v[28:31], v1 offset:14336\n" \
  "v_mfma_f32_16x16x32_bf16 v[48:51], v[112:115], v[12:15], v[48:51]\n" \
  "v_mfma_f32_16x16x32_bf16 v[52:55], v[176:179], v[12:15], v[52:55]\n" \
  "v_mfma_f32_16x16x32_bf16 v[56:59], v[240:243], v[12:15], v[56:59]\n" \
  "s_waitcnt lgkmcnt(4)\n" \
  "v_mfma_f32_16x16x32_bf16 v[60:63], v[32:35], v[12:15], v[60:63]\n" \
  "ds_read_b128 v[32:35], v1 offset:15360\n" \
  "ds_read_b128 v[40:43], v4 offset:0\n" \
  "ds_read_b128 v[44:47], v4 offset:64\n" \
  "v_mfma_f32_16x16x32_bf16 v[48:51], v[116:119], v[16:19], v[48:51]\n" \
  "v_mfma_f32_16x16x32_bf16 v[52:55], v[180:183], v[16:19], v[52:55]\n" \
  "v_mfma_f32_16x16x32_bf16 v[56:59], v[244:247], v[16:19], v[56:59]\n" \
  "s_waitcnt lgkmcnt(5)\n" \
  "v_mfma_f32_16x16x32_bf16 v[60:63], v[36:39], v[16:19], v[60:63]\n" \
  "ds_read_b128 v[36:39], v4 offset:128\n" \
  "ds_read2_b32 v[8:9], v3 offset0:16 offset1:48\n" \
  "v_mfma_f32_16x16x32_bf16 v[48:51], v[120:123], v[20:23], v[48:51]\n" \
  "v_mfma_f32_16x16x32_bf16 v[52:55], v[184:187], v[20:23], v[52:55]\n" \
  "v_mfma_f32_16x16x32_bf16 v[56:59], v[248:251], v[20:23], v[56:59]\n" \
  "s_waitcnt lgkmcnt(5)\n" \
  "v_mfma_f32_16x16x32_bf16 v[60:63], v[28:31], v[20:23], v[60:63]\n" \
  "ds_read_b128 v[28:31], v4 offset:192\n" \
  "s_mov_b64 exec, 1\n" \
  "ds_add_u32 v6, v7 offset:8\n" \
  "s_mov_b64 exec, -1\n" \
  "v_xor_b32 v6, 4, v6\n" \
  "v_mfma_f32_16x16x32_bf16 v[48:51], v[124:127], v[24:27], v[48:51]\n" \
  "v_mfma_f32_16x16x32_bf16 v[52:55], v[188:191], v[24:27], v[52:55]\n" \
  "v_mfma_f32_16x16x32_bf16 v[56:59], v[252:255], v[24:27], v[56:59]\n" \
  "s_waitcnt lgkmcnt(6)\n" \
  "v_mfma_f32_16x16x32_bf16 v[60:63], v[32:35], v[24:27], v[60:63]\n" \
  "s_waitcnt lgkmcnt(1)\n" \
  "s_nop 4\n" \
  "ds_read_b32 v5, v6 offset:0\n" \
  "v_fma_f32 v48, -v8, v40, v48\n" \
  "v_fma_f32 v49, -v8, v41, v49\n" \
  "v_fma_f32 v50, -v8, v42, v50\n" \
  "v_fma_f32 v51, -v8, v43, v51\n" \
  "v_fma_f32 v52, -v8, v44, v52\n" \
  "v_fma_f32 v53, -v8, v45, v53\n" \
  "v_fma_f32 v54, -v8, v46, v54\n" \
  "v_fma_f32 v55, -v8, v47, v55\n" \
  "v_fma_f32 v56, -v8, v36, v56\n" \
  "v_fma_f32 v57, -v8, v37, v57\n" \
  "v_fma_f32 v58, -v8, v38, v58\n" \
  "v_fma_f32 v59, -v8, v39, v59\n" \
  "v_fma_f32 v60, -v8, v28, v60\n" \
  "v_fma_f32 v61, -v8, v29, v61\n" \
  "v_fma_f32 v62, -v8, v30, v62\n" \
  "v_fma_f32 v63, -v8, v31, v63\n" \
  "v_mul_f32 v48, v48, v9\n" \
  "v_mul_f32 v49, v49, v9\n" \
  "v_mul_f32 v50, v50, v9\n" \
  "v_mul_f32 v51, v51, v9\n" \
  "v_mul_f32 v52, v52, v9\n" \
  "v_mul_f32 v53, v53, v9\n" \
  "v_mul_f32 v54, v54, v9\n" \
  "v_mul_f32 v55, v55, v9\n" \
  "v_cvt_pk_bf16_f32 v40, v48, v49\n" \
  "v_cvt_pk_bf16_f32 v41, v50, v51\n" \
  "v_cvt_pk_bf16_f32 v42, v52, v53\n" \
  "v_cvt_pk_bf16_f32 v43, v54, v55\n" \
  "buffer_store_dwordx4 v[40:43], v10, s[40:43], s65 offen offset:0 sc1\n" \
  "v_mul_f32 v56, v56, v9\n" \
  "v_mul_f32 v57, v57, v9\n" \
  "v_mul_f32 v58, v58, v9\n" \
  "v_mul_f32 v59, v59, v9\n" \
  "v_mul_f32 v60, v60, v9\n" \
  "v_mul_f32 v61, v61, v9\n" \
  "v_mul_f32 v62, v62, v9\n" \
  "v_mul_f32 v63, v63, v9\n" \
  "v_cvt_pk_bf16_f32 v44, v56, v57\n" \
  "v_cvt_pk_bf16_f32 v45, v58, v59\n" \
  "v_cvt_pk_bf16_f32 v46, v60, v61\n" \
  "v_cvt_pk_bf16_f32 v47, v62, v63\n" \
  "buffer_store_dwordx4 v[44:47], v10, s[40:43], s65 offen offset:1024 sc1\n" \
  "s_waitcnt lgkmcnt(0)\n" \
  "s_add_u32 s65, s65, 0x80000\n" \
  "s_sub_u32 s64, s64, 1\n" \
  "s_mov_b32 s68, 1\n" \
  "s_cmp_lg_u32 s64, 0\n" \
  "s_cbranch_scc1 .LCP_TOP_%=\n" \
  "s_waitcnt vmcnt(0)\n" \
  "s_mov_b64 exec, 1\n" \
  "buffer_store_dword v7, off, s[56:59], s72 sc1\n" \
  "s_memrealtime s[4:5]\n" \
  "s_lshr_b32 s6, s72, 9\n" \
  "s_add_u32 s6, s6, s36\n" \
  "s_addc_u32 s7, s37, 0\n" \
  "s_waitcnt lgkmcnt(0)\n" \
  "v_mov_b32 v11, s4\n" \
  "global_store_dword v2, v11, s[6:7]\n" \
  "v_mov_b32 v11, s5\n" \
  "global_store_dword v2, v11, s[6:7] offset:4\n" \
  "s_mov_b64 exec, -1\n" \
  "s_add_u32 s72, s72, 0x1000\n" \
  "s_mov_b32 s68, 0\n" \
  ".LCP_END_%=:\n" \
  "s_endpgm\n"
#define V_ASM \
  "s_mov_b32 s68, 0\n" \
  "s_mov_b32 s66, 3\n" \
  "s_mov_b32 s67, 2\n" \
  "s_mov_b32 s72, 0\n" \
  "v_mov_b32 v7, 1\n" \
  "v_mov_b32 v6, s70\n" \
  "v_mov_b32 v5, 0\n" \
  "ds_read_b32 v8, v4 offset:0\n" \
  "ds_read_b32 v9, v4 offset:64\n" \
  "ds_read_b32 v10, v4 offset:128\n" \
  "ds_read_b32 v4, v4 offset:192\n" \
  "s_waitcnt lgkmcnt(0)\n" \
  ".p2align 6\n" \
  ".LCW_TOP_%=:\n" \
  "ds_read_b128 v[28:31], v1 offset:0\n" \
  "ds_read_b128 v[32:35], v1 offset:1024\n" \
  "s_add_u32 s66, s66, 3\n" \
  "s_add_u32 s67, s67, 2\n" \
  "s_mov_b32 s69, 0x400000\n" \
  "v_xor_b32 v0, 0x8000, v0\n" \
  "v_xor_b32 v3, 0x100, v3\n" \
  "v_readfirstlane_b32 s71, v5\n" \
  "s_cmp_ge_u32 s71, s66\n" \
  "s_cbranch_scc1 .LCW_GO_%=\n" \
  "s_cmp_eq_u32 s68, 1\n" \
  "s_cbranch_scc0 .LCW_X1_%=\n" \
  "s_waitcnt vmcnt(0)\n" \
  "s_mov_b64 exec, 1\n" \
  "buffer_store_dword v7, off, s[56:59], s72 sc1\n" \
  "s_memrealtime s[4:5]\n" \
  "s_lshr_b32 s6, s72, 9\n" \
  "s_add_u32 s6, s6, s36\n" \
  "s_addc_u32 s7, s37, 0\n" \
  "s_waitcnt lgkmcnt(0)\n" \
  "v_mov_b32 v11, s4\n" \
  "global_store_dword v2, v11, s[6:7]\n" \
  "v_mov_b32 v11, s5\n" \
  "global_store_dword v2, v11, s[6:7] offset:4\n" \
  "s_mov_b64 exec, -1\n" \
  "s_add_u32 s72, s72, 0x1000\n" \
  "s_mov_b32 s68, 0\n" \
  ".LCW_X1_%=:\n" \
  ".LCW_S2_%=:\n" \
  "s_sleep 1\n" \
  "ds_read_b32 v5, v6 offset:0\n" \
  "s_sub_u32 s69, s69, 1\n" \
  "s_cbranch_scc1 .LCW_END_%=\n" \
  "s_waitcnt lgkmcnt(0)\n" \
  "v_readfirstlane_b32 s71, v5\n" \
  "s_cmp_ge_u32 s71, s66\n" \
  "s_cbranch_scc1 .LCW_GO_%=\n" \
  "s_branch .LCW_S2_%=\n" \
  ".LCW_GO_%=:\n" \
  "ds_read_b128 v[12:15], v0 offset:0\n" \
  "ds_read_b128 v[16:19], v0 offset:2048\n" \
  "ds_read_b128 v[20:23], v0 offset:4096\n" \
  "ds_read_b128 v[24:27], v0 offset:6144\n" \
  "ds_read_b128 v[36:39], v1 offset:2048\n" \
  "s_waitcnt lgkmcnt(4)\n" \
  "v_mfma_f32_16x16x32_bf16 v[42:45], v[12:15], v[64:67], 0\n" \
  "v_mfma_f32_16x16x32_bf16 v[48:51], v[12:15], v[128:131], 0\n" \
  "v_mfma_f32_16x16x32_bf16 v[54:57], v[12:15], v[192:195], 0\n" \
  "v_mfma_f32_16x16x32_bf16 v[60:63], v[12:15], v[28:31], 0\n" \
  "ds_read_b128 v[12:15], v0 offset:8192\n" \
  "ds_read_b128 v[28:31], v1 offset:3072\n" \
  "s_waitcnt lgkmcnt(5)\n" \
  "v_mfma_f32_16x16x32_bf16 v[42:45], v[16:19], v[68:71], v[42:45]\n" \
  "v_mfma_f32_16x16x32_bf16 v[48:51], v[16:19], v[132:135], v[48:51]\n" \
  "v_mfma_f32_16x16x32_bf16 v[54:57], v[16:19], v[196:199], v[54:57]\n" \
  "v_mfma_f32_16x16x32_bf16 v[60:63], v[16:19], v[32:35], v[60:63]\n" \
  "ds_read_b128 v[16:19], v0 offset:10240\n" \
  "ds_read_b128 v[32:35], v1 offset:4096\n" \
  "s_waitcnt lgkmcnt(6)\n" \
  "v_mfma_f32_16x16x32_bf16 v[42:45], v[20:23], v[72:75], v[42:45]\n" \
  "v_mfma_f32_16x16x32_bf16 v[48:51], v[20:23], v[136:139], v[48:51]\n" \
  "v_mfma_f32_16x16x32_bf16 v[54:57], v[20:23], v[200:203], v[54:57]\n" \
  "s_waitcnt lgkmcnt(4)\n" \
  "v_mfma_f32_16x16x32_bf16 v[60:63], v[20:23], v[36:39], v[60:63]\n" \
  "ds_read_b128 v[20:23], v0 offset:12288\n" \
  "ds_read_b128 v[36:39], v1 offset:5120\n" \
  "v_mfma_f32_16x16x32_bf16 v[42:45], v[24:27], v[76:79], v[42:45]\n" \
  "v_mfma_f32_16x16x32_bf16 v[48:51], v[24:27], v[140:143], v[48:51]\n" \
  "v_mfma_f32_16x16x32_bf16 v[54:57], v[24:27], v[204:207], v[54:57]\n" \
  "s_waitcnt lgkmcnt(4)\n" \
  "v_mfma_f32_16x16x32_bf16 v[60:63], v[24:27], v[28:31], v[60:63]\n" \
  "ds_read_b128 v[24:27], v0 offset:14336\n" \
  "ds_read_b128 v[28:31], v1 offset:6144\n" \
  "v_mfma_f32_16x16x32_bf16 v[42:45], v[12:15], v[80:83], v[42:45]\n" \
  "v_mfma_f32_16x16x32_bf16 v[48:51], v[12:15], v[144:147], v[48:51]\n" \
  "v_mfma_f32_16x16x32_bf16 v[54:57], v[12:15], v[208:211], v[54:57]\n" \
  "s_waitcnt lgkmcnt(4)\n" \
  "v_mfma_f32_16x16x32_bf16 v[60:63], v[12:15], v[32:35], v[60:63]\n" \
  "ds_read_b128 v[12:15], v0 offset:16384\n" \
  "ds_read_b128 v[32:35], v1 offset:7168\n" \
  "v_mfma_f32_16x16x32_bf16 v[42:45], v[16:19], v[84:87], v[42:45]\n" \
  "v_mfma_f32_16x16x32_bf16 v[48:51], v[16:19], v[148:151], v[48:51]\n" \
  "v_mfma_f32_16x16x32_bf16 v[54:57], v[16:19], v[212:215], v[54:57]\n" \
  "s_waitcnt lgkmcnt(4)\n" \
  "v_mfma_f32_16x16x32_bf16 v[60:63], v[16:19], v[36:39], v[60:63]\n" \
  "ds_read_b128 v[16:19], v0 offset:18432\n" \
  "ds_read_b128 v[36:39], v1 offset:8192\n" \
  "v_mfma_f32_16x16x32_bf16 v[42:45], v[20:23], v[88:91], v[42:45]\n" \
  "v_mfma_f32_16x16x32_bf16 v[48:51], v[20:23], v[152:155], v[48:51]\n" \
  "v_mfma_f32_16x16x32_bf16 v[54:57], v[20:23], v[216:219], v[54:57]\n" \
  "s_waitcnt lgkmcnt(4)\n" \
  "v_mfma_f32_16x16x32_bf16 v[60:63], v[20:23], v[28:31], v[60:63]\n" \
  "ds_read_b128 v[20:23], v0 offset:20480\n" \
  "ds_read_b128 v[28:31], v1 offset:9216\n" \
  "v_mfma_f32_16x16x32_bf16 v[42:45], v[24:27], v[92:95], v[42:45]\n" \
  "v_mfma_f32_16x16x32_bf16 v[48:51], v[24:27], v[156:159], v[48:51]\n" \
  "v_mfma_f32_16x16x32_bf16 v[54:57], v[24:27], v[220:223], v[54:57]\n" \
  "s_waitcnt lgkmcnt(4)\n" \
  "v_mfma_f32_16x16x32_bf16 v[60:63], v[24:27], v[32:35], v[60:63]\n" \
  "ds_read_b128 v[24:27], v0 offset:22528\n" \
  "ds_read_b128 v[32:35], v1 offset:10240\n" \
  "v_mfma_f32_16x16x32_bf16 v[42:45], v[12:15], v[96:99], v[42:45]\n" \
  "v_mfma_f32_16x16x32_bf16 v[48:51], v[12:15], v[160:163], v[48:51]\n" \
  "v_mfma_f32_16x16x32_bf16 v[54:57], v[12:15], v[224:227], v[54:57]\n" \
  "s_waitcnt lgkmcnt(4)\n" \
  "v_mfma_f32_16x16x32_bf16 v[60:63], v[12:15], v[36:39], v[60:63]\n" \
  "ds_read_b128 v[12:15], v0 offset:24576\n" \
  "ds_read_b128 v[36:39], v1 offset:11264\n" \
  "v_mfma_f32_16x16x32_bf16 v[42:45], v[16:19], v[100:103], v[42:45]\n" \
  "v_mfma_f32_16x16x32_bf16 v[48:51], v[16:19], v[164:167], v[48:51]\n" \
  "v_mfma_f32_16x16x32_bf16 v[54:57], v[16:19], v[228:231], v[54:57]\n" \
  "s_waitcnt lgkmcnt(4)\n" \
  "v_mfma_f32_16x16x32_bf16 v[60:63], v[16:19], v[28:31], v[60:63]\n" \
  "ds_read_b128 v[16:19], v0 offset:26624\n" \
  "ds_read_b128 v[28:31], v1 offset:12288\n" \
  "v_mfma_f32_16x16x32_bf16 v[42:45], v[20:23], v[104:107], v[42:45]\n" \
  "v_mfma_f32_16x16x32_bf16 v[48:51], v[20:23], v[168:171], v[48:51]\n" \
  "v_mfma_f32_16x16x32_bf16 v[54:57], v[20:23], v[232:235], v[54:57]\n" \
  "s_waitcnt lgkmcnt(4)\n" \
  "v_mfma_f32_16x16x32_bf16 v[60:63], v[20:23], v[32:35], v[60:63]\n" \
  "ds_read_b128 v[20:23], v0 offset:28672\n" \
  "ds_read_b128 v[32:35], v1 offset:13312\n" \
  "v_mfma_f32_16x16x32_bf16 v[42:45], v[24:27], v[108:111], v[42:45]\n" \
  "v_mfma_f32_16x16x32_bf16 v[48:51], v[24:27], v[172:175], v[48:51]\n" \
  "v_mfma_f32_16x16x32_bf16 v[54:57], v[24:27], v[236:239], v[54:57]\n" \
  "s_waitcnt lgkmcnt(4)\n" \
  "v_mfma_f32_16x16x32_bf16 v[60:63], v[24:27], v[36:39], v[60:63]\n" \
  "s_cmp_eq_u32 s68, 1\n" \
  "s_cbranch_scc0 .LCW_X3_%=\n" \
  "s_waitcnt vmcnt(0)\n" \
  "s_mov_b64 exec, 1\n" \
  "buffer_store_dword v7, off, s[56:59], s72 sc1\n" \
  "s_memrealtime s[4:5]\n" \
  "s_lshr_b32 s6, s72, 9\n" \
  "s_add_u32 s6, s6, s36\n" \
  "s_addc_u32 s7, s37, 0\n" \
  "s_waitcnt lgkmcnt(0)\n" \
  "v_mov_b32 v11, s4\n" \
  "global_store_dword v2, v11, s[6:7]\n" \
  "v_mov_b32 v11, s5\n" \
  "global_store_dword v2, v11, s[6:7] offset:4\n" \
  "s_mov_b64 exec, -1\n" \
  "s_add_u32 s72, s72, 0x1000\n" \
  "s_mov_b32 s68, 0\n" \
  ".LCW_X3_%=:\n" \
  "ds_read_b32 v5, v6 offset:16\n" \
  "ds_read_b128 v[24:27], v0 offset:30720\n" \
  "ds_read_b128 v[36:39], v1 offset:14336\n" \
  "v_mfma_f32_16x16x32_bf16 v[42:45], v[12:15], v[112:115], v[42:45]\n" \
  "v_mfma_f32_16x16x32_bf16 v[48:51], v[12:15], v[176:179], v[48:51]\n" \
  "v_mfma_f32_16x16x32_bf16 v[54:57], v[12:15], v[240:243], v[54:57]\n" \
  "s_waitcnt lgkmcnt(5)\n" \
  "v_mfma_f32_16x16x32_bf16 v[60:63], v[12:15], v[28:31], v[60:63]\n" \
  "ds_read_b128 v[12:15], v0 offset:256\n" \
  "ds_read_b128 v[28:31], v1 offset:15360\n" \
  "v_mfma_f32_16x16x32_bf16 v[42:45], v[16:19], v[116:119], v[42:45]\n" \
  "v_mfma_f32_16x16x32_bf16 v[48:51], v[16:19], v[180:183], v[48:51]\n" \
  "v_mfma_f32_16x16x32_bf16 v[54:57], v[16:19], v[244:247], v[54:57]\n" \
  "s_waitcnt lgkmcnt(5)\n" \
  "v_mfma_f32_16x16x32_bf16 v[60:63], v[16:19], v[32:35], v[60:63]\n" \
  "s_waitcnt lgkmcnt(4)\n" \
  "v_readfirstlane_b32 s71, v5\n" \
  "s_cmp_ge_u32 s71, s67\n" \
  "s_cbranch_scc1 .LCW_ST_%=\n" \
  ".LCW_S4_%=:\n" \
  "s_sleep 1\n" \
  "ds_read_b32 v5, v6 offset:16\n" \
  "s_sub_u32 s69, s69, 1\n" \
  "s_cbranch_scc1 .LCW_END_%=\n" \
  "s_waitcnt lgkmcnt(0)\n" \
  "v_readfirstlane_b32 s71, v5\n" \
  "s_cmp_ge_u32 s71, s67\n" \
  "s_cbranch_scc1 .LCW_ST_%=\n" \
  "s_branch .LCW_S4_%=\n" \
  ".LCW_ST_%=:\n" \
  "ds_read_b128 v[16:19], v0 offset:2304\n" \
  "ds_read_b128 v[32:35], v3 offset:0\n" \
  "v_mfma_f32_16x16x32_bf16 v[42:45], v[20:23], v[120:123], v[42:45]\n" \
  "v_mfma_f32_16x16x32_bf16 v[48:51], v[20:23], v[184:187], v[48:51]\n" \
  "v_mfma_f32_16x16x32_bf16 v[54:57], v[20:23], v[248:251], v[54:57]\n" \
  "s_waitcnt lgkmcnt(4)\n" \
  "v_mfma_f32_16x16x32_bf16 v[60:63], v[20:23], v[36:39], v[60:63]\n" \
  "ds_read_b128 v[20:23], v0 offset:4352\n" \
  "ds_read_b128 v[36:39], v3 offset:128\n" \
  "v_mfma_f32_16x16x32_bf16 v[42:45], v[24:27], v[124:127], v[42:45]\n" \
  "v_mfma_f32_16x16x32_bf16 v[48:51], v[24:27], v[188:191], v[48:51]\n" \
  "v_mfma_f32_16x16x32_bf16 v[54:57], v[24:27], v[252:255], v[54:57]\n" \
  "s_waitcnt lgkmcnt(4)\n" \
  "v_mfma_f32_16x16x32_bf16 v[60:63], v[24:27], v[28:31], v[60:63]\n" \
  "s_waitcnt lgkmcnt(0)\n" \
  "s_nop 4\n" \
  "v_fma_f32 v42, -v32, v8, v42\n" \
  "v_fma_f32 v43, -v33, v8, v43\n" \
  "v_fma_f32 v44, -v34, v8, v44\n" \
  "v_fma_f32 v45, -v35, v8, v45\n" \
  "v_fma_f32 v48, -v32, v9, v48\n" \
  "v_fma_f32 v49, -v33, v9, v49\n" \
  "v_fma_f32 v50, -v34, v9, v50\n" \
  "v_fma_f32 v51, -v35, v9, v51\n" \
  "v_fma_f32 v54, -v32, v10, v54\n" \
  "v_fma_f32 v55, -v33, v10, v55\n" \
  "v_fma_f32 v56, -v34, v10, v56\n" \
  "v_fma_f32 v57, -v35, v10, v57\n" \
  "v_fma_f32 v60, -v32, v4, v60\n" \
  "v_fma_f32 v61, -v33, v4, v61\n" \
  "v_fma_f32 v62, -v34, v4, v62\n" \
  "v_fma_f32 v63, -v35, v4, v63\n" \
  "ds_read_b128 v[32:35], v1 offset:0\n" \
  "v_mul_f32 v42, v42, v36\n" \
  "v_mul_f32 v43, v43, v37\n" \
  "v_mul_f32 v44, v44, v38\n" \
  "v_mul_f32 v45, v45, v39\n" \
  "v_mul_f32 v48, v48, v36\n" \
  "v_mul_f32 v49, v49, v37\n" \
  "v_mul_f32 v50, v50, v38\n" \
  "v_mul_f32 v51, v51, v39\n" \
  "v_mul_f32 v54, v54, v36\n" \
  "v_mul_f32 v55, v55, v37\n" \
  "v_mul_f32 v56, v56, v38\n" \
  "v_mul_f32 v57, v57, v39\n" \
  "v_mul_f32 v60, v60, v36\n" \
  "v_mul_f32 v61, v61, v37\n" \
  "v_mul_f32 v62, v62, v38\n" \
  "v_mul_f32 v63, v63, v39\n" \
  "ds_read_b128 v[36:39], v1 offset:1024\n" \
  "v_cvt_pk_bf16_f32 v40, v42, v43\n" \
  "v_cvt_pk_bf16_f32 v41, v44, v45\n" \
  "v_cvt_pk_bf16_f32 v46, v48, v49\n" \
  "v_cvt_pk_bf16_f32 v47, v50, v51\n" \
  "v_cvt_pk_bf16_f32 v52, v54, v55\n" \
  "v_cvt_pk_bf16_f32 v53, v56, v57\n" \
  "v_cvt_pk_bf16_f32 v58, v60, v61\n" \
  "v_cvt_pk_bf16_f32 v59, v62, v63\n" \
  "ds_read_b128 v[24:27], v0 offset:6400\n" \
  "ds_read_b128 v[28:31], v1 offset:2048\n" \
  "v_mfma_f32_16x16x32_bf16 v[42:45], v[12:15], v[64:67], 0\n" \
  "v_mfma_f32_16x16x32_bf16 v[48:51], v[12:15], v[128:131], 0\n" \
  "v_mfma_f32_16x16x32_bf16 v[54:57], v[12:15], v[192:195], 0\n" \
  "s_waitcnt lgkmcnt(3)\n" \
  "v_mfma_f32_16x16x32_bf16 v[60:63], v[12:15], v[32:35], 0\n" \
  "ds_read_b128 v[12:15], v0 offset:8448\n" \
  "ds_read_b128 v[32:35], v1 offset:3072\n" \
  "v_mfma_f32_16x16x32_bf16 v[42:45], v[16:19], v[68:71], v[42:45]\n" \
  "v_mfma_f32_16x16x32_bf16 v[48:51], v[16:19], v[132:135], v[48:51]\n" \
  "v_mfma_f32_16x16x32_bf16 v[54:57], v[16:19], v[196:199], v[54:57]\n" \
  "s_waitcnt lgkmcnt(4)\n" \
  "v_mfma_f32_16x16x32_bf16 v[60:63], v[16:19], v[36:39], v[60:63]\n" \
  "ds_read_b128 v[16:19], v0 offset:10496\n" \
  "ds_read_b128 v[36:39], v1 offset:4096\n" \
  "v_mfma_f32_16x16x32_bf16 v[42:45], v[20:23], v[72:75], v[42:45]\n" \
  "v_mfma_f32_16x16x32_bf16 v[48:51], v[20:23], v[136:139], v[48:51]\n" \
  "v_mfma_f32_16x16x32_bf16 v[54:57], v[20:23], v[200:203], v[54:57]\n" \
  "s_waitcnt lgkmcnt(4)\n" \
  "v_mfma_f32_16x16x32_bf16 v[60:63], v[20:23], v[28:31], v[60:63]\n" \
  "ds_read_b128 v[20:23], v0 offset:12544\n" \
  "ds_read_b128 v[28:31], v1 offset:5120\n" \
  "v_mfma_f32_16x16x32_bf16 v[42:45], v[24:27], v[76:79], v[42:45]\n" \
  "v_mfma_f32_16x16x32_bf16 v[48:51], v[24:27], v[140:143], v[48:51]\n" \
  "v_mfma_f32_16x16x32_bf16 v[54:57], v[24:27], v[204:207], v[54:57]\n" \
  "s_waitcnt lgkmcnt(4)\n" \
  "v_mfma_f32_16x16x32_bf16 v[60:63], v[24:27], v[32:35], v[60:63]\n" \
  "ds_read_b128 v[24:27], v0 offset:14592\n" \
  "ds_read_b128 v[32:35], v1 offset:6144\n" \
  "v_mfma_f32_16x16x32_bf16 v[42:45], v[12:15], v[80:83], v[42:45]\n" \
  "v_mfma_f32_16x16x32_bf16 v[48:51], v[12:15], v[144:147], v[48:51]\n" \
  "v_mfma_f32_16x16x32_bf16 v[54:57], v[12:15], v[208:211], v[54:57]\n" \
  "s_waitcnt lgkmcnt(4)\n" \
  "v_mfma_f32_16x16x32_bf16 v[60:63], v[12:15], v[36:39], v[60:63]\n" \
  "ds_read_b128 v[12:15], v0 offset:16640\n" \
  "ds_read_b128 v[36:39], v1 offset:7168\n" \
  "v_mfma_f32_16x16x32_bf16 v[42:45], v[16:19], v[84:87], v[42:45]\n" \
  "v_mfma_f32_16x16x32_bf16 v[48:51], v[16:19], v[148:151], v[48:51]\n" \
  "v_mfma_f32_16x16x32_bf16 v[54:57], v[16:19], v[212:215], v[54:57]\n" \
  "s_waitcnt lgkmcnt(4)\n" \
  "v_mfma_f32_16x16x32_bf16 v[60:63], v[16:19], v[28:31], v[60:63]\n" \
  "ds_read_b128 v[16:19], v0 offset:18688\n" \
  "ds_read_b128 v[28:31], v1 offset:8192\n" \
  "v_mfma_f32_16x16x32_bf16 v[42:45], v[20:23], v[88:91], v[42:45]\n" \
  "v_mfma_f32_16x16x32_bf16 v[48:51], v[20:23], v[152:155], v[48:51]\n" \
  "v_mfma_f32_16x16x32_bf16 v[54:57], v[20:23], v[216:219], v[54:57]\n" \
  "s_waitcnt lgkmcnt(4)\n" \
  "v_mfma_f32_16x16x32_bf16 v[60:63], v[20:23], v[32:35], v[60:63]\n" \
  "ds_read_b128 v[20:23], v0 offset:20736\n" \
  "ds_read_b128 v[32:35], v1 offset:9216\n" \
  "v_mfma_f32_16x16x32_bf16 v[42:45], v[24:27], v[92:95], v[42:45]\n" \
  "v_mfma_f32_16x16x32_bf16 v[48:51], v[24:27], v[156:159], v[48:51]\n" \
  "v_mfma_f32_16x16x32_bf16 v[54:57], v[24:27], v[220:223], v[54:57]\n" \
  "s_waitcnt lgkmcnt(4)\n" \
  "v_mfma_f32_16x16x32_bf16 v[60:63], v[24:27], v[36:39], v[60:63]\n" \
  "ds_read_b128 v[24:27], v0 offset:22784\n" \
  "ds_read_b128 v[36:39], v1 offset:10240\n" \
  "v_mfma_f32_16x16x32_bf16 v[42:45], v[12:15], v[96:99], v[42:45]\n" \
  "v_mfma_f32_16x16x32_bf16 v[48:51], v[12:15], v[160:163], v[48:51]\n" \
  "v_mfma_f32_16x16x32_bf16 v[54:57], v[12:15], v[224:227], v[54:57]\n" \
  "s_waitcnt lgkmcnt(4)\n" \
  "v_mfma_f32_16x16x32_bf16 v[60:63], v[12:15], v[28:31], v[60:63]\n" \
  "ds_read_b128 v[12:15], v0 offset:24832\n" \
  "ds_read_b128 v[28:31], v1 offset:11264\n" \
  "v_mfma_f32_16x16x32_bf16 v[42:45], v[16:19], v[100:103], v[42:45]\n" \
  "v_mfma_f32_16x16x32_bf16 v[48:51], v[16:19], v[164:167], v[48:51]\n" \
  "v_mfma_f32_16x16x32_bf16 v[54:57], v[16:19], v[228:231], v[54:57]\n" \
  "s_waitcnt lgkmcnt(4)\n" \
  "v_mfma_f32_16x16x32_bf16 v[60:63], v[16:19], v[32:35], v[60:63]\n" \
  "ds_read_b128 v[16:19], v0 offset:26880\n" \
  "ds_read_b128 v[32:35], v1 offset:12288\n" \
  "v_mfma_f32_16x16x32_bf16 v[42:45], v[20:23], v[104:107], v[42:45]\n" \
  "v_mfma_f32_16x16x32_bf16 v[48:51], v[20:23], v[168:171], v[48:51]\n" \
  "v_mfma_f32_16x16x32_bf16 v[54:57], v[20:23], v[232:235], v[54:57]\n" \
  "s_waitcnt lgkmcnt(4)\n" \
  "v_mfma_f32_16x16x32_bf16 v[60:63], v[20:23], v[36:39], v[60:63]\n" \
  "ds_read_b128 v[20:23], v0 offset:28928\n" \
  "ds_read_b128 v[36:39], v1 offset:13312\n" \
  "v_mfma_f32_16x16x32_bf16 v[42:45], v[24:27], v[108:111], v[42:45]\n" \
  "v_mfma_f32_16x16x32_bf16 v[48:51], v[24:27], v[172:175], v[48:51]\n" \
  "v_mfma_f32_16x16x32_bf16 v[54:57], v[24:27], v[236:239], v[54:57]\n" \
  "s_waitcnt lgkmcnt(4)\n" \
  "v_mfma_f32_16x16x32_bf16 v[60:63], v[24:27], v[28:31], v[60:63]\n" \
  "ds_read_b128 v[24:27], v0 offset:30976\n" \
  "ds_read_b128 v[28:31], v1 offset:14336\n" \
  "v_mfma_f32_16x16x32_bf16 v[42:45], v[12:15], v[112:115], v[42:45]\n" \
  "v_mfma_f32_16x16x32_bf16 v[48:51], v[12:15], v[176:179], v[48:51]\n" \
  "v_mfma_f32_16x16x32_bf16 v[54:57], v[12:15], v[240:243], v[54:57]\n" \
  "s_waitcnt lgkmcnt(4)\n" \
  "v_mfma_f32_16x16x32_bf16 v[60:63], v[12:15], v[32:35], v[60:63]\n" \
  "ds_read_b128 v[32:35], v1 offset:15360\n" \
  "v_mfma_f32_16x16x32_bf16 v[42:45], v[16:19], v[116:119], v[42:45]\n" \
  "v_mfma_f32_16x16x32_bf16 v[48:51], v[16:19], v[180:183], v[48:51]\n" \
  "v_mfma_f32_16x16x32_bf16 v[54:57], v[16:19], v[244:247], v[54:57]\n" \
  "s_waitcnt lgkmcnt(3)\n" \
  "v_mfma_f32_16x16x32_bf16 v[60:63], v[16:19], v[36:39], v[60:63]\n" \
  "ds_read_b128 v[36:39], v3 offset:64\n" \
  "v_mfma_f32_16x16x32_bf16 v[42:45], v[20:23], v[120:123], v[42:45]\n" \
  "v_mfma_f32_16x16x32_bf16 v[48:51], v[20:23], v[184:187], v[48:51]\n" \
  "v_mfma_f32_16x16x32_bf16 v[54:57], v[20:23], v[248:251], v[54:57]\n" \
  "s_waitcnt lgkmcnt(2)\n" \
  "v_mfma_f32_16x16x32_bf16 v[60:63], v[20:23], v[28:31], v[60:63]\n" \
  "ds_read_b128 v[28:31], v3 offset:192\n" \
  "s_mov_b64 exec, 1\n" \
  "ds_add_u32 v6, v7 offset:8\n" \
  "s_mov_b64 exec, -1\n" \
  "v_xor_b32 v6, 4, v6\n" \
  "v_mfma_f32_16x16x32_bf16 v[42:45], v[24:27], v[124:127], v[42:45]\n" \
  "v_mfma_f32_16x16x32_bf16 v[48:51], v[24:27], v[188:191], v[48:51]\n" \
  "v_mfma_f32_16x16x32_bf16 v[54:57], v[24:27], v[252:255], v[54:57]\n" \
  "s_waitcnt lgkmcnt(3)\n" \
  "v_mfma_f32_16x16x32_bf16 v[60:63], v[24:27], v[32:35], v[60:63]\n" \
  "s_waitcnt lgkmcnt(1)\n" \
  "s_nop 4\n" \
  "ds_read_b32 v5, v6 offset:0\n" \
  "v_fma_f32 v42, -v36, v8, v42\n" \
  "v_fma_f32 v43, -v37, v8, v43\n" \
  "v_fma_f32 v44, -v38, v8, v44\n" \
  "v_fma_f32 v45, -v39, v8, v45\n" \
  "v_mul_f32 v42, v42, v28\n" \
  "v_mul_f32 v43, v43, v29\n" \
  "v_mul_f32 v44, v44, v30\n" \
  "v_mul_f32 v45, v45, v31\n" \
  "v_cvt_pk_bf16_f32 v42, v42, v43\n" \
  "v_cvt_pk_bf16_f32 v43, v44, v45\n" \
  "buffer_store_dwordx4 v[40:43], v2, s[40:43], s65 offen offset:0 sc1\n" \
  "v_fma_f32 v48, -v36, v9, v48\n" \
  "v_fma_f32 v49, -v37, v9, v49\n" \
  "v_fma_f32 v50, -v38, v9, v50\n" \
  "v_fma_f32 v51, -v39, v9, v51\n" \
  "v_mul_f32 v48, v48, v28\n" \
  "v_mul_f32 v49, v49, v29\n" \
  "v_mul_f32 v50, v50, v30\n" \
  "v_mul_f32 v51, v51, v31\n" \
  "v_cvt_pk_bf16_f32 v48, v48, v49\n" \
  "v_cvt_pk_bf16_f32 v49, v50, v51\n" \
  "buffer_store_dwordx4 v[46:49], v2, s[40:43], s65 offen offset:1024 sc1\n" \
  "v_fma_f32 v54, -v36, v10, v54\n" \
  "v_fma_f32 v55, -v37, v10, v55\n" \
  "v_fma_f32 v56, -v38, v10, v56\n" \
  "v_fma_f32 v57, -v39, v10, v57\n" \
  "v_mul_f32 v54, v54, v28\n" \
  "v_mul_f32 v55, v55, v29\n" \
  "v_mul_f32 v56, v56, v30\n" \
  "v_mul_f32 v57, v57, v31\n" \
  "v_cvt_pk_bf16_f32 v54, v54, v55\n" \
  "v_cvt_pk_bf16_f32 v55, v56, v57\n" \
  "buffer_store_dwordx4 v[52:55], v2, s[40:43], s65 offen offset:2048 sc1\n" \
  "v_fma_f32 v60, -v36, v4, v60\n" \
  "v_fma_f32 v61, -v37, v4, v61\n" \
  "v_fma_f32 v62, -v38, v4, v62\n" \
  "v_fma_f32 v63, -v39, v4, v63\n" \
  "v_mul_f32 v60, v60, v28\n" \
  "v_mul_f32 v61, v61, v29\n" \
  "v_mul_f32 v62, v62, v30\n" \
  "v_mul_f32 v63, v63, v31\n" \
  "v_cvt_pk_bf16_f32 v60, v60, v61\n" \
  "v_cvt_pk_bf16_f32 v61, v62, v63\n" \
  "buffer_store_dwordx4 v[58:61], v2, s[40:43], s65 offen offset:3072 sc1\n" \
  "s_waitcnt lgkmcnt(0)\n" \
  "s_add_u32 s65, s65, 0x40000\n" \
  "s_sub_u32 s64, s64, 1\n" \
  "s_mov_b32 s68, 1\n" \
  "s_cmp_lg_u32 s64, 0\n" \
  "s_cbranch_scc1 .LCW_TOP_%=\n" \
  "s_waitcnt vmcnt(0)\n" \
  "s_mov_b64 exec, 1\n" \
  "buffer_store_dword v7, off, s[56:59], s72 sc1\n" \
  "s_memrealtime s[4:5]\n" \
  "s_lshr_b32 s6, s72, 9\n" \
  "s_add_u32 s6, s6, s36\n" \
  "s_addc_u32 s7, s37, 0\n" \
  "s_waitcnt lgkmcnt(0)\n" \
  "v_mov_b32 v11, s4\n" \
  "global_store_dword v2, v11, s[6:7]\n" \
  "v_mov_b32 v11, s5\n" \
  "global_store_dword v2, v11, s[6:7] offset:4\n" \
  "s_mov_b64 exec, -1\n" \
  "s_add_u32 s72, s72, 0x1000\n" \
  "s_mov_b32 s68, 0\n" \
  ".LCW_END_%=:\n" \
  "s_endpgm\n"
constexpr int RG = 2;
#define RFL(x) __builtin_amdgcn_readfirstlane(x)
#define HALF(x, i) BF8(((i) ? __builtin_shufflevector(x, x, 4, 5, 6, 7) : __builtin_shufflevector(x, x, 0, 1, 2, 3)))
constexpr int P_W = 65536, P_ST = 131072, P_SS = P_ST + 1024, P_CN = P_SS + 512;
template <bool PR>
__device__ __forceinline__ void role_proj_t(const MegaArgs& A, char* smem, const int L, const int idx, const bool kv) {
  const int w = A.wv;
  const int l = lane_id();
  const int c = l & 15, g = l >> 4;
  char* ws = A.ws;
  unsigned* FL = (unsigned*)(ws + OFF_FL);
  unsigned* TMO = FL + FL_TMO;
  const unsigned sb = lds_base(smem);
  auto cn = (volatile __attribute__((address_space(3))) unsigned*)(unsigned long)(sb + P_CN);
  u64 tag;
  const bool h4 = kv && L;
  const __amdgpu_buffer_rsrc_t rsM = MKRS(ws + OFF_MEMK + L * SZ_MEM, kv && w == 2 ? (4 - L) * SZ_MEM : 0);
  const bool mc = kv && w == 2 && (MCE < 2 || !L);
  constexpr int NMC = MCE == 2 ? 8 : 4;
  const int b0 = RFL(h4 ? (H4S ? 2 : 3) * w : w == 2 ? 0 : HX0),
            b1 = RFL(h4 ? (H4S ? 2 * w + 2 : w == 2 ? 8 : 3 * w + 3 * (w < 2)) : w == 2 ? HX0 : 8);
  const __amdgpu_buffer_rsrc_t rsF = MKRS(FL + FL_FC + L * 64 * 32 * 32, 64 * 4096);
  const int fo = ((blockIdx.x & 3) * 8 + ((H4S && h4) || w == 3 ? l >> 3 : min(b0 + (l >> 3), b1 - 1))) * 128 + (l & 7) * 4;
#define FC_LOOK(s) __builtin_amdgcn_raw_buffer_load_b32(rsF, fo, (s) * 4096, AUX_SC1)
#define MEM_CLEAR(s)                                                                                                          \
  _Pragma("unroll") for (int i = 0; i < NMC; ++i)                                                                             \
    ST128((v4u{0u, 0u, 0u, 0u}), rsM, l * 16 + (i & 1) * 1024, (s) * 65536 + idx * 2048 + (i >> 1) * (int)SZ_MEM * (MCE == 2 ? 1 : 2));
  const char* Xb = ws + (L ? OFF_X3 : OFF_XA);
  const __amdgpu_buffer_rsrc_t rsS = MKRS(ws + OFF_H + L * (SZ_X / 2), SZ_X / 2);
  unsigned nf = 0u;
  bool early = false;
  v4u t0[16];
#define X_ASK(s) \
  for (int i = b0; i < b1; ++i) glds64c(Xb + RFL((s) * 32768 + i * 4096), l * 16, RFL(sb + ((s) & 1) * 32768 + i * 4096));
#define S_ASK(s, t) _Pragma("unroll") for (int i = 0; i < 16; ++i) t[i] = LD128(rsS, l * 16, (s) * 16384 + i * 1024);
  {
    const int j = w & 3;
    const bool vt = kv && j >= 2;
    constexpr int NT = PR ? 3 : 1;
    v8u wq[NT][8];
    const u16* Wm = (kv ? (PR ? A.Wv : A.Wk) : A.Wq) - (PR ? 128 : 0) * HD;
    const int hm = kv ? idx : 2 * idx + PR;
    const int q = (l >> 2) & 3, p = l & 3, f = q | ((g & 1) << 2);
    unsigned ra[NT];
#pragma unroll
    for (int m = 0; m < NT; ++m) {
      const int ct = PR ? m : 3;
      ra[m] = sb + (j >> 1) * 65536 + (8 * g + q) * 256 + ((4 * (j & 1) + (vt ? ct : (ct & 2) + (p >> 1))) ^ f) * 32 + (vt ? p * 8 : (p & 1) * 16 + (ct & 1) * 8);
    }
#define W_ISSUE(n) wqkv_issue<false>(Wm + (128 * (n) - 256 * ((n) & 1)) * HD, (n) & 1, hm, w, l, sb, (WNT >> (kv ? 1 + PR : 3) & 1) && (L == 0 || (WNT & 32)))
#define W_STAGE(n, WAIT, NEXT)                                                                             \
  {                                                                                                        \
    WAIT;                                                                                                  \
    if (n == 3 && EL == 2 && !PR && L == 0 && w >= 2) nf = FC_LOOK(0);                                     \
    if (n == 3 && EL == 1 && !PR && L == 0 && w >= 2) {                                                    \
      early = __all(nf != 0u);                                                                             \
      if (early) {                                                                                         \
        X_ASK(0)                                                                                           \
        if (w == 3) { S_ASK(0, t0) }                                                                       \
      }                                                                                                    \
      nf = FC_LOOK(early);                                                                                 \
    }                                                                                                      \
    RAW_BAR();                                                                                             \
    if (n == 3 && EL == 3 && !PR && L == 0 && w >= 2) nf = FC_LOOK(0);                                     \
    _Pragma("unroll") for (int m = 0; m < NT; ++m) {                                                       \
      _Pragma("unroll") for (int t = 0; t < 2; ++t) {                                                      \
        const unsigned a_ = ra[m] + ((n) & 1) * 32768 + t * 16384;                                         \
        const v2u r0 = trd(a_), r1 = trd(a_ + 1024), r2 = trd(a_ + 8192), r3 = trd(a_ + 8192 + 1024);      \
        wq[m][2 * (n) + t] = v8u{r0.x, r0.y, r1.x, r1.y, r2.x, r2.y, r3.x, r3.y};                          \
      }                                                                                                    \
    }                                                                                                      \
    _Pragma("unroll") for (int m = 0; m < 2 * NT; ++m) asm volatile("" : "+v"(wq[m >> 1][2 * (n) + (m & 1)])); \
    RAW_BAR();                                                                                             \
    NEXT;                                                                                                  \
    _Pragma("unroll") for (int m = 0; m < 4 * NT; ++m)                                                     \
      cs[m % NT] = MFMA16(HALF(wq[m % NT][2 * (n) + m / NT / 2], (m / NT) & 1), BF8(one), cs[m % NT]);     \
    _Pragma("unroll") for (int m = 0; m < NT; ++m) asm volatile("" : "+v"(cs[m]));                         \
  }
    __builtin_amdgcn_s_waitcnt(0x0f70);
    if (EL && !MC3 && mc) { MEM_CLEAR(0) }
    W_ISSUE(0);
    W_ISSUE(1);
    tag = call_tag(A);
    if (w == 0 && l < 8) cn[l] = l == 1 ? 3u : l == 5 ? 2u : 0u;
    const v4u one = {0x3f803f80u, 0x3f803f80u, 0x3f803f80u, 0x3f803f80u};
    f32x4 cs[NT] = {};
    u64 rv = 0;
    W_STAGE(0, WAIT_VM(8), W_ISSUE(2); if (EL) RD_ASK(rv));
    SST(4);
    W_STAGE(1, if (EL) WAIT_VM(9); else WAIT_VM(8), W_ISSUE(3); if (EL != 0 && MC3 != 0 && mc) { MEM_CLEAR(0) if (MC3 == 2) { MEM_CLEAR(1) } });
    SST(5);
    W_STAGE(2, if (EL != 0 && MC3 == 2 && mc) WAIT_VM(16); else if (EL != 0 && MC3 != 0 && mc) WAIT_VM(12); else WAIT_VM(8), (void)0);
    SST(6);
    asm volatile("" : "+v"(rv));
    if (!EL) ready_wait(A, tag, ready_look(A));
    for (unsigned spins = 0; EL && !__all(rv == tag) && spins < SPIN_CAP; ++spins) {
      __builtin_amdgcn_s_sleep(8);
      RD_ASK(rv);
      asm volatile("s_waitcnt vmcnt(0)" : "+v"(rv)::"memory");
    }
    if (EL == 1 && !PR && L == 0 && w >= 2) nf = FC_LOOK(0);
    W_STAGE(3, if (EL >= 2 && MC3 == 2 && mc) WAIT_VM(8); else if (EL >= 2 && MC3 == 1 && mc) WAIT_VM(4); else WAIT_VM(0), if (!PR) {
      _Pragma("unroll") for (int ks = 0; ks < 16; ++ks)
        *reinterpret_cast<bf16x8*>(smem + P_W + j * 16384 + ks * 1024 + l * 16) = HALF(wq[0][ks >> 1], ks & 1);
    });
    SST(7);
#undef W_STAGE
#undef W_ISSUE
#pragma unroll
    for (int m = 0; m < NT; ++m) *reinterpret_cast<f32x4*>(smem + P_ST + j * 256 + (PR ? m : 3) * 64 + g * 16) = cs[m];
    RAW_BAR();
    SST(8);
    if constexpr (PR) {
      const v4u rsO = mk_srd(ws + RFL((unsigned)(vt ? OFF_VF + L * SZ_O : OFF_QK + L * SZ_QK)), RFL((unsigned)(vt ? SZ_O : SZ_QK)));
      const v4u rsP = mk_srd(FL + RFL(kv ? FL_FQ + (L * 64 * 32 + idx) * 32 + 2 * j : FL_FP + (L * 16 + idx) * 32 + j), 63 * 4096 + 4);
      const int so = RFL(vt ? idx * 8 + 4 * (j & 1) : (kv ? idx * 16 + 8 : (2 * idx + (j >> 1)) * 16) + 2 * (j & 1));
      const v8u pv = {sb + 32768 + g * 512 + c * 16, sb + P_W + j * 16384 + l * 16, (unsigned)l * 16, sb + P_SS + 256 + (vt ? g * 16 : c * 4),
                      sb + P_ST + j * 256 + (vt ? c * 4 : g * 16), 0u, 0u, 0u};
      const char* dbg = ws + WS_TOTAL + RFL(kv ? A.vb : 300 + L * 16 + idx) * 64 * 8;
#define P_OPS                                                                                                                               \
  ::"{v[64:71]}"(wq[0][0]), "{v[72:79]}"(wq[0][1]), "{v[80:87]}"(wq[0][2]), "{v[88:95]}"(wq[0][3]), "{v[96:103]}"(wq[0][4]),                \
      "{v[104:111]}"(wq[0][5]), "{v[112:119]}"(wq[0][6]), "{v[120:127]}"(wq[0][7]), "{v[128:135]}"(wq[1][0]), "{v[136:143]}"(wq[1][1]),     \
      "{v[144:151]}"(wq[1][2]), "{v[152:159]}"(wq[1][3]), "{v[160:167]}"(wq[1][4]), "{v[168:175]}"(wq[1][5]), "{v[176:183]}"(wq[1][6]),     \
      "{v[184:191]}"(wq[1][7]), "{v[192:199]}"(wq[2][0]), "{v[200:207]}"(wq[2][1]), "{v[208:215]}"(wq[2][2]), "{v[216:223]}"(wq[2][3]),     \
      "{v[224:231]}"(wq[2][4]), "{v[232:239]}"(wq[2][5]), "{v[240:247]}"(wq[2][6]), "{v[248:255]}"(wq[2][7]), "{v[0:7]}"(pv),               \
      "{s[40:43]}"(rsO), "{s[56:59]}"(rsP), "{s64}"(NSEG), "{s65}"(so * 1024), "{s70}"(sb + P_CN), "{s[36:37]}"(dbg)                        \
      : "memory", "vcc", "scc", "s80", "v63"
      if (vt) asm volatile("s_mov_b64 exec, -1\ns_waitcnt vmcnt(0) lgkmcnt(0)\ns_nop 4\n" V_ASM P_OPS);
      else asm volatile("s_mov_b64 exec, -1\ns_waitcnt vmcnt(0) lgkmcnt(0)\ns_nop 4\n" P_ASM P_OPS);
#undef P_OPS
      return;
    }
  }
  __builtin_amdgcn_s_setprio(RPRIO);
  if (h4 || w >= 2) {
    if (!EL && mc) { MEM_CLEAR(0) }
    auto stats = [&](const v4u (&t)[16], const int s) {
      float a = 0.f, b = 0.f, qa = 0.f, qb = 0.f;
#pragma unroll
      for (int i = 0; i < 16; ++i) {
        a = one(a + __uint_as_float(t[i].x));
        b = one(b + __uint_as_float(t[i].z));
        qa = one(qa + __uint_as_float(t[i].y));
        qb = one(qb + __uint_as_float(t[i].w));
      }
      a = xsum32(xsum16(a));
      b = xsum32(xsum16(b));
      float da = 0.f, db = 0.f;
#pragma unroll
      for (int i = 0; i < 16; ++i) {
        const float u = one(__uint_as_float(t[i].x) - a * (1.0f / 64)), v = one(__uint_as_float(t[i].z) - b * (1.0f / 64));
        da = one(da + u * u);
        db = one(db + v * v);
      }
      const float var = xsum32(swap_add16(qa + da * 0.125f, qb + db * 0.125f)) * (1.0f / DM);
      const float ri = 1.0f / bfr1(sqrtf(bfr1(bfr1(var) + 1e-5f)));
      reinterpret_cast<float*>(smem + P_SS)[(s & 1) * 64 + (g >> 1) * 32 + 2 * c + (g & 1)] = g < 2 ? bfr1(((g & 1) ? b : a) * (1.0f / DM)) : ri;
      asm volatile("" ::: "memory");
      if (l == 0) cn[4 + (s & 1)] = 2 * s + 4;
    };
    static_assert(EL != 1 || MC3 == 2, "clear");
    if (early) {
      if (w == 3) WAIT_VM(17);
      else WAIT_VM(1);
      if (l == 0) __hip_atomic_fetch_add((__attribute__((address_space(3))) unsigned*)cn, 3u, __ATOMIC_RELAXED, __HIP_MEMORY_SCOPE_WORKGROUP);
      SSX(10, w == 2);
      SSX(9, w == 2);
      if (w == 3) stats(t0, 0);
      SSX(11, w == 3);
    }
    unsigned tn = now10(), tg = tn;
    for (int s = early; s < NSEG; ++s) {
      for (unsigned spins = 0; !__all(nf != 0u); ++spins) {
        if (spin_timeout(spins, TMO)) return;
        if (spins) __builtin_amdgcn_s_sleep(SLP_H);
        else if (GF_H && (h4 || (GF_P && L)) && s >= GF0 && s < NSEG - 6) gate(tg);
        tg = now10() + GF_H;
        nf = FC_LOOK(s);
        asm volatile("" ::: "memory");
      }
      if (NFW) asm volatile("" ::"v"(nf));
      TS(4, s, 128);
      for (unsigned spins = 0; cn[2 + (s & 1)] < 4u * (s >> 1); ++spins) {
        if (spins > 64u * SPIN_CAP) return;
        __builtin_amdgcn_s_sleep(2);
      }
      if (L == 0 && s >= PACE0) gate(tn);
      else tn = now10();
      tn += PACE;
      TS(5, s, 128);
      const u64 t5 = __builtin_amdgcn_s_memrealtime();
      X_ASK(s)
      if (H4S && h4) {
        v4u t[4];
#pragma unroll
        for (int i = 0; i < 4; ++i) t[i] = LD128(rsS, (l >> 2) * 256 + (l & 3) * 16 + w * 64, s * 16384 + i * 4096);
        nf = 0u;
        if (MCE == 1 && w == 2 && s < NSEG - 1) {
          MEM_CLEAR(s + 1)
          WAIT_VM(8);
        } else {
          WAIT_VM(4);
        }
        if (l == 0) __hip_atomic_fetch_add((__attribute__((address_space(3))) unsigned*)cn + (s & 1), w < 2 ? 2u : 1u, __ATOMIC_RELAXED, __HIP_MEMORY_SCOPE_WORKGROUP);
        TS(6, s, 128);
#define F(x) __uint_as_float(x)
        const float a = red_mod4(one(F(t[0].x) + F(t[1].x)) + one(F(t[2].x) + F(t[3].x)));
        const float b = red_mod4(one(F(t[0].z) + F(t[1].z)) + one(F(t[2].z) + F(t[3].z)));
        float da = 0.f, db = 0.f;
#pragma unroll
        for (int i = 0; i < 4; ++i) {
          const float u = one(F(t[i].x) - a * (1.0f / 64)), v = one(F(t[i].z) - b * (1.0f / 64));
          da = one(da + u * u);
          db = one(db + v * v);
        }
        const float qa = one(F(t[0].y) + F(t[1].y)) + one(F(t[2].y) + F(t[3].y)), qb = one(F(t[0].w) + F(t[1].w)) + one(F(t[2].w) + F(t[3].w));
#undef F
        float var = xsum32(swap_add16(one(qa + da * 0.125f), one(qb + db * 0.125f)));
        var += dppr<0x124>(var);
        var += dppr<0x128>(var);
        const float ri = 1.0f / bfr1(sqrtf(bfr1(bfr1(var * (1.0f / DM)) + 1e-5f)));
        if (c < 4) reinterpret_cast<float*>(smem + P_SS)[(s & 1) * 64 + (g >> 1) * 32 + 8 * w + 2 * c + (g & 1)] = g < 2 ? bfr1(((g & 1) ? b : a) * (1.0f / DM)) : ri;
        asm volatile("" ::: "memory");
        if (l == 0) __hip_atomic_fetch_add((__attribute__((address_space(3))) unsigned*)cn + 4 + (s & 1), 1u, __ATOMIC_RELAXED, __HIP_MEMORY_SCOPE_WORKGROUP);
        TS(7, s, 192);
        if (!MCE && w == 2 && s < NSEG - 1) { MEM_CLEAR(s + 1) }
      } else if (w != 3) {
        nf = L ? 0u : FC_LOOK(s + 1);
        if (mc && s < NSEG - 1 && (EL == 0 || MC3 != 2 || s)) {
          MEM_CLEAR(s + 1)
          if (NMC == 8) WAIT_VM(8);
          else WAIT_VM(4);
        } else {
          WAIT_VM(0);
        }
        if (l == 0) __hip_atomic_fetch_add((__attribute__((address_space(3))) unsigned*)cn + (s & 1), h4 ? 2u : 3u, __ATOMIC_RELAXED, __HIP_MEMORY_SCOPE_WORKGROUP);
        TS(6, s, 128);
        SSX(10, s == 0 && w == 2);
        if (s == 0 && w == 2 && l == 0) ((unsigned long long*)(A.ws + WS_TOTAL))[539 * 256 + A.vb] = t5;
      } else {
        v4u t[16];
        S_ASK(s, t)
        WAIT_VM(16);
        nf = L ? 0u : FC_LOOK(s + 1);
        if (l == 0 && !h4) __hip_atomic_fetch_add((__attribute__((address_space(3))) unsigned*)cn + (s & 1), 3u, __ATOMIC_RELAXED, __HIP_MEMORY_SCOPE_WORKGROUP);
        stats(t, s);
        SSX(11, s == 0);
        TS(7, s, 192);
      }
    }
    return;
  }
  const int RL = !kv, rp = idx & 15, u = RFL(kv ? idx >> 4 : L);
  const __amdgpu_buffer_rsrc_t rsW = MKRS(ws + OFF_PW, SZ_PW);
  const __amdgpu_buffer_rsrc_t rsX = MKRS(ws + (RL ? OFF_X3 : OFF_XA), SZ_X);
  const __amdgpu_buffer_rsrc_t rsY = RL ? MKRS(A.out, SZ_X) : MKRS(ws + OFF_X2, SZ_X);
  const int r = 2 * rp + (g >> 1), yo = r * (DM * 2) + (l & 31) * 16 + u * 512;
  if (kv) {
    if (XZD) gate(now10() + XZD);
    if (XZP & 3) {
      if (!wait_n<SLP_V, 32>(FL + FL_FZ + (blockIdx.x & 7) * 64, 64, TMO)) return;
    } else {
      for (int s = w; s < (XZP ? 2 * RG : NSEG); s += RG) ST128((v4u{0u, 0u, 0u, 0u}), rsY, yo, s * SEG * DM * 2);
      DRAIN();
      if (l < 8) set_flag(FL + FL_FZ + l * 64 + 2 * idx + w);
    }
  }
  v4u sel;
#pragma unroll
  for (int e = 0; e < 4; ++e) sel[e] = (g >> 1) == (c >> 3) && ((c & 7) >> 1) == e ? 0x3f80u << (16 * (c & 1)) : 0u;
  unsigned tl = now10();
  for (int s = w; s < NSEG; s += RG) {
    if (GF_R) {
      gate(tl);
      const gu32* f = (gu32*)(FL + FL_FW) + ((RL * 64 + s) * 4 + (blockIdx.x & 3)) * 4 * 32 + (c >> 2) * 32 + (c & 3);
      for (unsigned spins = 0;; ++spins) {
        tl = now10() + GF_R;
        if (__all(__hip_atomic_load(f, RLX_AGENT) != 0u) || spin_timeout(spins, TMO)) break;
        __builtin_amdgcn_s_sleep(SLP_R);
      }
      asm volatile("" ::: "memory");
    } else
    grp_wait<SLP_R, 4, GATE_R>(true, nullptr, 0, FL + FL_FW + ((RL * 64 + s) * 4 + (blockIdx.x & 3)) * 4 * 32, 16, TMO, tl);
    if (s >= NSEG - 6) tl = now10();
    DRAIN();
    TS(0, s, w * 64);
    v4u p[8][2];
    const v4u xr = LD128(rsX, (l & 31) * 512 + r * 16, s * 32768 + u * 16384);
#pragma unroll
    for (int k = 0; k < 8; ++k)
#pragma unroll
      for (int b = 0; b < 2; ++b)
        p[k][b] = LD128(rsW, (g & 1) * 32768 + (c >> 2) * 128 + (g >> 1) * 64 + (c & 3) * 16 + b * 512, ((RL * 64 + s) * 16 + 2 * k) * 32768 + rp * 2048 + u * 1024);
    __builtin_amdgcn_sched_barrier(0);
    f32x4 ac[2] = {};
#pragma unroll
    for (int k = 0; k < 8; ++k)
#pragma unroll
      for (int b = 0; b < 2; ++b) ac[b] = MFMA16(BF8(sel), BF8(p[k][b]), ac[b]);
    v4u xo;
    {
      const auto x = __builtin_amdgcn_permlane16_swap(pk2s(ac[0][0], ac[0][1]), pk2s(ac[1][0], ac[1][1]), false, false);
      const auto y = __builtin_amdgcn_permlane16_swap(pk2s(ac[0][2], ac[0][3]), pk2s(ac[1][2], ac[1][3]), false, false);
      const v4u a = {x[0], y[0], x[1], y[1]};
#pragma unroll
      for (int e = 0; e < 4; ++e) {
        xo[e] = pk2s(lo16(xr[e]) + lo16(a[e]), hi16(xr[e]) + hi16(a[e]));
        if (kv) xo[e] = xo[e] ? xo[e] : 0x8000u;
      }
      if (kv) ST128(xo, rsY, yo, s * SEG * DM * 2);
      else ST128A(xo, rsY, yo, s * SEG * DM * 2, OAUX);
    }
    TS(1, s, w * 64);
    if (kv) {
      const float pv = lo16(g < 2 ? __builtin_amdgcn_readlane(xo[0], 0) : __builtin_amdgcn_readlane(xo[0], 32));
      float sm = 0.f, q0 = 0.f, q1 = 0.f;
#pragma unroll
      for (int e = 0; e < 4; ++e) {
        const float a = one(lo16(xo[e]) - pv), b = one(hi16(xo[e]) - pv);
        sm = one(sm + one(a + b));
        q0 = one(q0 + a * a);
        q1 = one(q1 + b * b);
      }
      sm = half_sum(sm);
      const float q = fmaxf(one(half_sum(q0 + q1)) - sm * sm * (1.0f / 256), 0.f);
      const u64 st = (u64)__float_as_uint(sm + pv * 256.f) | (u64)(__float_as_uint(q) | 1u) << 32;
      if ((l & 31) < 4) __hip_atomic_store((gu64*)(FL + FL_FR + ((s * 4 + (l & 3)) * 4 + (r >> 3)) * 32 + (r & 7) * 4 + 2 * u), st, RLX_AGENT);
      if (XZP == 4 && s + 2 * RG < NSEG) ST128((v4u{0u, 0u, 0u, 0u}), rsY, yo, (s + 2 * RG) * SEG * DM * 2);
    }
    TS(3, s, w * 64);
    if (l == 0) ((unsigned long long*)(A.ws + WS_TOTAL))[(340 + RL * 32 + u * 16 + rp) * 64 + s] = __builtin_amdgcn_s_memrealtime();
  }
  if (L == 1 && idx == 0 && w == 0 && l == 0) __hip_atomic_store((gu64*)(ws + OFF_EP), tag, RLX_AGENT);
}
__device__ __forceinline__ void role_proj(const MegaArgs& A, char* smem, const int L, const int idx, const bool kv) {
  if (A.wv < 4) role_proj_t<false>(A, smem, L, idx, kv);
  else role_proj_t<true>(A, smem, L, idx, kv);
}

constexpr int CUN = 2144;
__device__ __forceinline__ v2u part_stats(const v4u o) {
  float v[8];
#pragma unroll
  for (int e = 0; e < 4; ++e) { v[2 * e] = lo16(o[e]); v[2 * e + 1] = hi16(o[e]); }
  const float s = ((v[0] + v[1]) + (v[2] + v[3])) + ((v[4] + v[5]) + (v[6] + v[7])), m = s * 0.125f;
  float q0 = 0.f, q1 = 0.f;
#pragma unroll
  for (int e = 0; e < 4; ++e) {
    const float a = v[e] - m, b = v[4 + e] - m;
    q0 += a * a;
    q1 += b * b;
  }
  return v2u{__float_as_uint(s), __float_as_uint(q0 + q1)};
}
constexpr int CRN = 176;
constexpr int CTN = 272;
constexpr int CV_LDS = (8 * CKN + 8 * 2 * CUN + 64 + 2 * 32 * 8 + 16 * CRN + 64 + 8 * CTN) * 2 + 8 * 1568;
__device__ __forceinline__ void role_conv(const MegaArgs& A, char* smem, const int cv) {
  u16* Kr = reinterpret_cast<u16*>(smem);
  u16* Uc = Kr + 8 * CKN;
  u16* Us = Uc + 8 * CUN + 63;
  u16* To = Uc + 8 * 2 * CUN + 64;
  u16* Sx = To + 32 * 8;
  u16* Rc = Sx + 32 * 8;
  u16* Rs = Rc + 8 * CRN + 63;
  u16* Ta = Rc + 16 * CRN + 64;
  const int w = A.wv;
  const int l = lane_id(), tid = 64 * w + l, c = l & 15, g = l >> 4;
  const int d = 8 * cv + w;
  char* ws = A.ws;
  unsigned* FL = (unsigned*)(ws + OFF_FL);
  unsigned* TMO = FL + FL_TMO;
  u16* kr = Kr + w * CKN;
  const u16* ka = kr + 2080 - 16 * c + 8 * g;
#define KFRAG(D0) (*reinterpret_cast<const f16x8*>(ka - (D0)))
  typedef __attribute__((address_space(3))) const unsigned* lpu;
#define BFRAG(a) __builtin_bit_cast(f16x8, (v4u{((lpu)(unsigned long)(a))[0], ((lpu)(unsigned long)(a))[1], ((lpu)(unsigned long)(a))[2], ((lpu)(unsigned long)(a))[3]}))
  f32x4 acc[8];
  v4u tx[4];
  const __amdgpu_buffer_rsrc_t rz1 = MKRS(ws + OFF_XA, SZ_X), rps = MKRS(ws + OFF_H, SZ_X);
  u16* uc = Uc + w * CUN;
  const unsigned ub = lds_base(reinterpret_cast<char*>(((c & 1) ? Us : Uc) + w * CUN + c + 8 * g + 32));
  const int t0 = 16 * c + 4 * g;
  float dsk, ysc;
#define SKIP(T) (*reinterpret_cast<const v2u*>(uc + 32 + 256 * (T) + t0))
#define TILE_OUT(dst, a, uu)                                                                   \
  {                                                                                            \
    v2u v_;                                                                                    \
    _Pragma("unroll") for (int e = 0; e < 2; ++e) {                                            \
      const unsigned yy = pk2(a[2 * e] * ysc, a[2 * e + 1] * ysc), u_ = uu[e];                 \
      const unsigned k_ = pk2(h2f((u16)u_) * dsk, h2f((u16)(u_ >> 16)) * dsk);                 \
      v_[e] = pk2(lo16(yy) + lo16(k_), hi16(yy) + hi16(k_));                                   \
    }                                                                                          \
    *reinterpret_cast<v2u*>((dst) + t0) = v_;                                                  \
  }
#define CHUNK_ST(i, src, N) CHUNK_STX(i, tx[i], src, N)
#define CHUNK_STX(i, X, src, N)                                                                \
  {                                                                                            \
    const unsigned p_ = lds_base((char*)((src) + (c >> 2) * (N) + (tid & ~15) + 4 * (c & 3))); \
    const v2u a0_ = trd(p_), a1_ = trd(p_ + 8 * (N));                                          \
    const v4u a_ = {a0_.x, a0_.y, a1_.x, a1_.y};                                               \
    v4u o;                                                                                     \
    _Pragma("unroll") for (int e = 0; e < 4; ++e) o[e] = pk2(lo16(X[e]) + lo16(a_[e]), hi16(X[e]) + hi16(a_[e])); \
    ST128(o, rz1, (l >> 5) * 32768 + (l & 31) * 16, (2 * (w + 8 * (i)) * 64 + cv) * 512);      \
    ST64(part_stats(o), rps, (l >> 5) * 16384 + (l & 31) * 8, (2 * (w + 8 * (i)) * 64 + cv) * 256); \
  }
#define CHUNK_FLAG(i) set_flag(FL + FL_FC + (((2 * w + 16 * ((i) + (l >> 3)) + ((l >> 2) & 1)) * 4 + (l & 3)) * 8 + (cv >> 3)) * 32 + (cv & 7))
  {
#define U_VAL(i)                                                                               \
  {                                                                                            \
    const float mb_ = lo16(st[i]), ri_ = 1024.0f / hi16(st[i]);                                 \
    _Pragma("unroll") for (int e = 0; e < 4; ++e) {                                            \
      const unsigned t_ = pk2(lo16(tx[i][e]) - mb_, hi16(tx[i][e]) - mb_);                     \
      const unsigned q_ = pk2(lo16(t_) * ri_, hi16(t_) * ri_);                                 \
      uv[e] = pkh(lo16(q_), hi16(q_));                                                         \
    }                                                                                          \
  }
    unsigned ta[3], k0p, st[4];
    v4u uv;
    v2u kn;
    u64 tag;
    const __amdgpu_buffer_rsrc_t rst = MKRS(ws + OFF_GT, (L_SEQ + 512) * 16);
    {
      KGen G;
      G.load(A, d, l);
      const u16 pk = A.Dskip[d];
      asm volatile("" ::: "memory");
      if (XL == 1) ask_slow(A, 16 * (w - 4) + (cv & 15), ta);
      if (XL) asm volatile("" : "+v"(G.pa), "+v"(G.pb), "+v"(G.pc), "+v"(G.pd), "+v"(G.pe)::"memory");
      tx[0] = *reinterpret_cast<const v4u*>(A.x0 + (size_t)tid * DM + 8 * cv);
      if (XL != 1) ask_slow(A, 16 * (w - 4) + (cv & 15), ta);
      tag = call_tag(A);
      G.setup();
      dsk = bf2f(pk) * (1.0f / 1024);
      G.st = smem + w * 16640;
      SST(0);
      ysc = G.scale();
      const Cx z = G.power(1.f);
      Cx y = cmul(G.C, z);
      G.rows(G.st + l * 8, y, z, 16);
      G.get(G.bh, G.bl, l);
      const float k0 = 2.0f * wave_sum(G.C.r.x + G.C.r.y);
      k0p = l == 0 ? pkh(k0, 0.f) : 0u;
      if (TP) {
        constexpr int R = 64 * TP, U0 = 2 * R + 32, U1 = 3 * R + 111, KN = 4 * R + 192;
        u16* k1 = reinterpret_cast<u16*>(smem + 8 * 16640) + w * KN;
        u16* t1 = reinterpret_cast<u16*>(smem + 8 * 16640) + 8 * KN;
        v4u s1 = {};
        u64 rv = 0;
        if (w < TP) {
          s1 = LD128(rst, tid * 16, (L_SEQ + R * (cv >> 4)) * 16);
          rv = ready_look(A);
        }
        if (l < R / 8 + 4) reinterpret_cast<v4u*>(k1)[l < R / 8 ? l + R / 8 + 4 : l - R / 8] = v4u{k0p, 0u, 0u, 0u};
        k1[U0 + (l < 32 ? l : R + l)] = k1[U1 + (l < 32 ? l : R + l)] = 0;
        G.columns(l, 0, R / 16);
        const f32x4 kt = G.tile<true>(l);
        if (c < R / 16) *reinterpret_cast<v2u*>(k1 + R + 28 - 16 * c - 4 * g) = v2u{pkh(kt.w, kt.z), pkh(kt.y, kt.x)};
        if (w < TP) {
          for (unsigned spins = 0; !__all(has_tag(tag, s1.x, s1.y) && has_tag(tag, s1.z, s1.w)) && spins < SPIN_CAP; ++spins) {
            __builtin_amdgcn_s_sleep(4);
            s1 = LD128(rst, tid * 16, (L_SEQ + R * (cv >> 4)) * 16);
          }
          st[0] = __builtin_amdgcn_perm(s1.w, s1.y, 0x07060302);
          U_VAL(0);
          u16* up = reinterpret_cast<u16*>(smem + 8 * 16640) + 32 + tid;
#pragma unroll
          for (int e = 0; e < 8; ++e) up[e * KN + U0] = up[e * KN + U1] = (u16)(uv[e >> 1] >> (16 * (e & 1)));
        }
        RAW_BAR();
        const unsigned u1 = lds_base(reinterpret_cast<char*>(k1 + ((c & 1) ? U1 : U0) + c + 8 * g));
        f32x4 a = {};
#pragma unroll
        for (int b = 0; b <= R / 32; ++b)
          a = MFMAH(BFRAG(u1 + 64 * b), *reinterpret_cast<const f16x8*>(k1 + R - 16 * (c & (R / 16 - 1)) + 8 * g + 32 * b), a);
        if (c < R / 16) {
          const v2u sk = *reinterpret_cast<const v2u*>(k1 + U0 + 32 + t0);
          TILE_OUT(t1 + w * (R + 16), a, sk);
        }
        RAW_BAR();
        SST(1);
        if (w < TP) CHUNK_ST(0, t1, R + 16);
        G.columns(l, R / 16, TPS);
        if (w < TP) {
          DRAIN();
          ready_wait(A, tag, rv);
          if (l < 8) CHUNK_FLAG(0);
        }
        SST(6);
        G.columns(l, TPS, 16);
      } else {
        G.columns(l, 0, 16);
      }
      const f32x4 kt = G.tile<true>(l);
      kn = v2u{pkh(kt.w, kt.z), pkh(kt.y, kt.x)};
    }
#define U_PUT(i)                                                                               \
  _Pragma("unroll") for (int e = 0; e < 4; ++e) {                                              \
    ((lph)(unsigned long)up0)[(2 * e) * CUN + 512 * (i)] = (u16)uv[e];                         \
    ((lph)(unsigned long)up0)[(2 * e + 1) * CUN + 512 * (i)] = (u16)(uv[e] >> 16);             \
    ((lph)(unsigned long)up1)[(2 * e) * CUN + 512 * (i)] = (u16)uv[e];                         \
    ((lph)(unsigned long)up1)[(2 * e + 1) * CUN + 512 * (i)] = (u16)(uv[e] >> 16);             \
  }
#define U_ROW(i) { U_VAL(i) U_PUT(i) }
    typedef __attribute__((address_space(3))) u16* lph;
    unsigned up0 = lds_base((char*)(Uc + 32 + tid)), up1 = lds_base((char*)(Us + 32 + tid));
    asm volatile("" : "+v"(up0), "+v"(up1), "+v"(kn), "+v"(k0p));
#define ST_GET(I0, I1)                                                                                                        \
  for (unsigned spins = 0; spins < SPIN_CAP; ++spins) {                                                                       \
    bool ok = true;                                                                                                           \
    v4u t_[4];                                                                                                                \
    _Pragma("unroll") for (int i = I0; i < I1; ++i) t_[i] = LD128(rst, (tid + 512 * i) * 16, 0);                              \
    _Pragma("unroll") for (int i = I0; i < I1; ++i) {                                                                         \
      ok = ok && has_tag(tag, t_[i].x, t_[i].y) && has_tag(tag, t_[i].z, t_[i].w);                                            \
      st[i] = __builtin_amdgcn_perm(t_[i].w, t_[i].y, 0x07060302);                                                            \
    }                                                                                                                         \
    if (__all(ok)) break;                                                                                                     \
    __builtin_amdgcn_s_sleep(4);                                                                                              \
    asm volatile("" ::: "memory");                                                                                            \
  }
    if (w < 4) {
      ST_GET(0, 2);
      U_VAL(0);
      asm volatile("" : "+v"(uv));
    }
#pragma unroll
    for (int i = 1; i < 4; ++i) tx[i] = *reinterpret_cast<const v4u*>(A.x0 + (size_t)(tid + 512 * i) * DM + 8 * cv);
    RAW_BAR();
    SST(2);
    if (w < 4) { U_PUT(0) }
    *reinterpret_cast<v2u*>(kr + 2076 - 16 * c - 4 * g) = kn;
    if (l < 38) reinterpret_cast<v4u*>(kr)[l < 32 ? 260 + l : l < 34 ? 194 + l : l - 34] = v4u{k0p, 0u, 0u, 0u};
    (l < 32 ? uc : Us + w * CUN - 32)[l] = 0;
    uc[32 + L_SEQ + l] = Us[w * CUN + 32 + L_SEQ + l] = 0;
    uc[32 + 256 + l] = Us[w * CUN + 32 + 256 + l] = 0;
    RAW_BAR();
    SST(11);
    {
      f32x4 a0 = {}, a1 = {};
      f16x8 bf[9], af[9];
#pragma unroll
      for (int b = 0; b < 9; ++b) {
        af[b] = KFRAG(32 - 32 * b);
        bf[b] = BFRAG(ub + 64 * (b - 1));
      }
      const v2u k0 = SKIP(0);
      __builtin_amdgcn_sched_barrier(0);
#pragma unroll
      for (int b = 0; b < 9; ++b) (b & 1 ? a1 : a0) = MFMAH(bf[b], af[b], (b & 1 ? a1 : a0));
      a0 += a1;
      TILE_OUT(Ta + w * CTN, a0, k0);
    }
    const u64 rv = ready_look(A);
    RAW_BAR();
    SST(3);
    if (w < 4) {
      if (w >= TP) CHUNK_ST(0, Ta, CTN);
      asm volatile("" : "+v"(tx[1]));
      U_ROW(1);
      DRAIN();
      ready_wait(A, tag, rv);
      if (l < 8 && w >= TP) CHUNK_FLAG(0);
      ST_GET(2, 4);
    } else {
      ST_GET(0, 4);
      ready_wait(A, tag, rv);
      asm volatile("" : "+v"(tx[0]), "+v"(tx[1]));
      U_ROW(0);
      U_ROW(1);
    }
    ASKED(ta);
    if (tid < (16 * CRN + 64) / 8) reinterpret_cast<v4u*>(Rc)[tid] = v4u{0u, 0u, 0u, 0u};
    SST(4);
    const gu64* fk = (gu64*)(ws + OFF_KG) + cv * 16 + w;
    u64 kg = __hip_atomic_load(fk, RLX_AGENT);
    U_ROW(2);
    for (unsigned spins = 0; !__all(kg == tag) && spins < SPIN_CAP; ++spins) {
      __builtin_amdgcn_s_sleep(8);
      kg = __hip_atomic_load(fk, RLX_AGENT);
    }
    asm volatile("" : "+v"(tx[3]), "+v"(st[3])::"memory");
    SST(5);
    const unsigned* kd = reinterpret_cast<const unsigned*>(Ta + 8 * CTN) + w * 392;
    glds16c(ws + OFF_KF + d * 2048, l * 16, lds_base((char*)kd));
    if (l < 34) glds16c(ws + OFF_KF + d * 2048 + 1024, l * 16, lds_base((char*)kd) + 1024);
    U_ROW(3);
    DRAIN();
    {
#define MIX(lo, hi) __builtin_amdgcn_perm(hi, lo, 0x07060100)
      const unsigned a0 = kd[384 - l], b0 = kd[383 - l], a1 = kd[320 - l], b1 = kd[319 - l];
      const unsigned a2 = kd[256 - 2 * l], m2 = kd[255 - 2 * l], b2 = kd[254 - 2 * l];
      const unsigned* k3 = kd + 124 - 4 * (l & 31);
      v4u* ko = reinterpret_cast<v4u*>(kr) + l;
      ko[4] = v4u{a0, a0, (l & 1) ? b0 : MIX(a0, b0), b0};
      ko[68] = v4u{a1, a1, (l & 1) ? b1 : MIX(a1, b1), b1};
      ko[132] = v4u{a2, MIX(a2, m2), m2, b2};
      if (l < 32) ko[196] = v4u{k3[4], MIX(k3[3], k3[2]), k3[2], MIX(k3[1], k3[0])};
#undef MIX
    }
    RAW_BAR();
    SST(7);
    {
      unsigned uq[4] = {ub, ub + 1024, ub + 2048, ub + 3072};
      f16x8 bf[8], a[4];
      const f16x8 bm = BFRAG(ub - 64);
      asm volatile("" : "+v"(uq[0]), "+v"(uq[1]), "+v"(uq[2]), "+v"(uq[3]));
#pragma unroll
      for (int j = 1; j < 8; ++j) bf[j] = BFRAG(uq[j >> 1] + 512 * (j & 1));
#pragma unroll
      for (int T = 1; T < 8; ++T) acc[T] = MFMAH(bm, KFRAG(256 * T + 32), (f32x4{}));
#pragma unroll
      for (int e = 0; e < 3; ++e) a[e] = KFRAG(256 * e);
      const u16* kq = ka - 256 * 7;
#pragma unroll 1
      for (int r = 0; r < 8; ++r) {
#pragma unroll
        for (int e = 0; e < 8; ++e) {
          a[(e + 3) & 3] = *reinterpret_cast<const f16x8*>(kq + 256 * (7 - ((e + 3) & 7)) + (e >= 5 ? 32 : 0));
          const int j = (8 - e) & 7;
          bf[j] = BFRAG(uq[j >> 1] + 512 * (j & 1) + (e ? 64 : 0));
          __builtin_amdgcn_sched_barrier(0);
#pragma unroll
          for (int T = 7; T >= (e > 1 ? e : 1); --T) acc[T] = MFMAH(bf[T - e], a[e & 3], acc[T]);
          __builtin_amdgcn_sched_barrier(0);
        }
        kq += 32;
#pragma unroll
        for (int j = 0; j < 4; ++j) uq[j] += 64;
        asm volatile("" : "+v"(uq[0]), "+v"(uq[1]), "+v"(uq[2]), "+v"(uq[3]));
      }
    }
    SST(8);
    {
      v2u k[8];
#pragma unroll
      for (int T = 1; T < 8; ++T) k[T] = SKIP(T);
#pragma unroll
      for (int T = 1; T < 8; ++T) TILE_OUT(uc + 32 + 256 * T, acc[T], k[T]);
    }
    RAW_BAR();
    SST(10);
    if (w >= 4) CHUNK_ST(0, Uc + 32, CUN);
    CHUNK_ST(1, Uc + 32 + 512, CUN);
    if (PP < 0) {
      if (CVSP) __builtin_amdgcn_s_sleep(CVSP);
      CHUNK_ST(2, Uc + 32 + 1024, CUN);
      if (CVSP) __builtin_amdgcn_s_sleep(CVSP);
      CHUNK_ST(3, Uc + 32 + 1536, CUN);
    }
    if (w == 1) (void)wait_n<SLP_V, 32>(FL + FL_FZ + (blockIdx.x & 7) * 64, 64, TMO);
    DRAIN();
    if (l < (PP < 0 ? 32 : 16)) CHUNK_FLAG(0);
    SST(9);
#undef U_ROW
#undef U_VAL
#undef U_PUT
#undef ST_GET
  }
  const unsigned rb = lds_base(reinterpret_cast<char*>(((c & 1) ? Rs : Rc) + w * CRN + c + 8 * g));
#pragma unroll
  for (int j = 0; j < 8; ++j) acc[j] = f32x4{};
  const __amdgpu_buffer_rsrc_t rx = MKRS(ws + OFF_X2, SZ_X), rz = MKRS(ws + OFF_X3, SZ_X);
  const __amdgpu_buffer_rsrc_t rf = MKRS(FL + FL_FR, 64 * 16 * 128);
  v2u st = {0u, 0u}, xx = {0u, 0u};
#define X2_LOAD(ss, far)                                                                                                      \
  {                                                                                                                      \
    xx = LD64(rx, (l >> 1) * (DM * 2) + (l & 1) * 8, (far) ? (int)SZ_X : ((ss) * SEG * DM + 8 * cv) * 2);                 \
    st = LD64(rf, l * 8, ((ss) * 4 + (blockIdx.x & 3)) * 512);                                                           \
  }
  unsigned tl = now10();
  bool late = false;
  for (int s = 0; s < NSEG; ++s) {
    const int q = s & 3, q8 = s & 7;
    if (w == 1) {
      late = __builtin_popcountll(__ballot(st.y != 0u)) >= CVLATE;
      for (unsigned spins = 0; !__all(umin3(xx.x, xx.y, st.y) != 0u); ++spins) {
        if (spin_timeout(spins, TMO)) break;
        if (GF_V && s >= GF0) {
          if (spins) __builtin_amdgcn_s_sleep(SLP_V);
          else if (s < NSEG - 6) gate(tl);
          tl = now10() + GF_V;
          X2_LOAD(s, CVT2 > 64 ? 0 : spins && __builtin_popcountll(__ballot(st.y != 0u)) < CVT2);
          continue;
        }
        if (GATE_V && spins == 0 && !__any(st.y != 0u)) gate(tl);
        if (spins && !__all(st.y != 0u)) __builtin_amdgcn_s_sleep(SLP_V);
        X2_LOAD(s, __builtin_popcountll(__ballot(st.y != 0u)) < CVTHR);
      }
      if (!GF_V || s < GF0) tl = now10() + GATE_V;
      TS(0, s, 64);
      const float s0 = __uint_as_float(st.x), s1 = dppr<0xb1>(s0), m2 = __uint_as_float(st.y);
      const float var = (m2 + dppr<0xb1>(m2) + (s0 - s1) * (s0 - s1) * (1.0f / DM)) * (1.0f / DM);
      const float mb = bfr1((s0 + s1) * (1.0f / DM)), ri = 1024.0f * __builtin_amdgcn_rcpf(bfr1(__builtin_amdgcn_sqrtf(bfr1(bfr1(var) + 1e-5f))));
      const unsigned t0 = pk2(lo16(xx.x) - mb, hi16(xx.x) - mb), t1 = pk2(lo16(xx.y) - mb, hi16(xx.y) - mb);
      const unsigned b0 = pk2(lo16(t0) * ri, hi16(t0) * ri), b1 = pk2(lo16(t1) * ri, hi16(t1) * ri);
      const unsigned u0 = pkh(lo16(b0), hi16(b0)), u1 = pkh(lo16(b1), hi16(b1));
      u16* rp = Rc + (l & 1) * 4 * CRN + 32 * q + (l >> 1);
#define R_PUT(o) { rp[o] = (u16)u0; rp[(o) + CRN] = (u16)(u0 >> 16); rp[(o) + 2 * CRN] = (u16)u1; rp[(o) + 3 * CRN] = (u16)(u1 >> 16); }
      R_PUT(0) R_PUT(8 * CRN + 63)
      if (q == 0) { R_PUT(128) R_PUT(8 * CRN + 63 + 128) }
#undef R_PUT
      *reinterpret_cast<v2u*>(Sx + l * 4) = xx;
    }
    const u16* kq = ka + 32 * q8;
    const f16x8 a0 = *reinterpret_cast<const f16x8*>(kq - 32), a1 = *reinterpret_cast<const f16x8*>(kq);
    __syncthreads();
    TS(1, s, 0);
    const f16x8 bp = BFRAG(rb + 64 * ((q + 3) & 3)), bc = BFRAG(rb + 64 * q);
    const float u0 = h2f(Rc[w * CRN + 32 * q + c]), u1 = h2f(Rc[w * CRN + 32 * q + 16 + c]);
    const float x0 = bf2f(Sx[c * 8 + w]), x1 = bf2f(Sx[(16 + c) * 8 + w]);
    acc[0] = MFMAH(a0, bp, (q8 == 0 ? acc[1] : acc[0]));
    const f32x4 tmp = MFMAH(a1, bc, acc[0]);
    const unsigned uu = pk2(u0 * dsk, u1 * dsk);
    const unsigned yy = pk2(((q & 1) ? tmp[2] : tmp[0]) * ysc, ((q & 1) ? tmp[3] : tmp[1]) * ysc);
    const unsigned aa = pk2(lo16(yy) + lo16(uu), hi16(yy) + hi16(uu)), oo = pk2(x0 + lo16(aa), x1 + hi16(aa));
    if (g == (q8 >> 1)) {
      To[c * 8 + w] = (u16)oo;
      To[(16 + c) * 8 + w] = (u16)(oo >> 16);
    }
    __syncthreads();
    if (w == 0 && l < 32) {
      const v4u o = *reinterpret_cast<const v4u*>(To + l * 8);
      ST128A(o, rz, l * 16, (s * 64 + cv) * 512, XAUX);
      ST64(part_stats(o), rps, l * 8, ((64 + s) * 64 + cv) * 256);
    }
    if (w == 1) {
      if (GF_V && s + 1 >= GF0) st.y = 0u;
      else X2_LOAD(s + 1, !late);
    }
    const int left = 7 - (s >> 3);
#define PUSH(J0, SH)                                                                                              \
  if (left >= (J0)) {                                                                                             \
    _Pragma("unroll") for (int j = (J0); j < (J0) + 2 && j < 8; ++j)                                              \
      acc[j] = MFMAH(*reinterpret_cast<const f16x8*>(kq - 32 - 256 * j), bp, (j + (SH) < 8 ? acc[(j + (SH)) & 7] : f32x4{})); \
  }
    TS(2, s, 0);
    if (w == 0) {
      DRAIN();
      if (l < 4) set_flag(FL + FL_FC + (((64 + s) * 4 + l) * 8 + (cv >> 3)) * 32 + (cv & 7));
    }
    TS(3, s, 0);
    if (q8 == 0) { PUSH(1, 1) PUSH(3, 1) PUSH(5, 1) PUSH(7, 1) }
    else { PUSH(1, 0) PUSH(3, 0) PUSH(5, 0) PUSH(7, 0) }
#undef PUSH
    if (PP >= 0 && (unsigned)(s - PP) < 3u) {
      const int i = 2 + s - PP;
      if (i > 2) {
        DRAIN();
        if (l < 8) CHUNK_FLAG(i - 1);
        tx[2] = tx[3];
      }
      if (i < 4) CHUNK_STX(i, tx[2], Uc + 32 + 512 * i, CUN);
    }
    MSTAMP();
  }
#undef KFRAG
#undef BFRAG
#undef X2_LOAD
#undef TILE_OUT
#undef SKIP
#undef CHUNK_ST
#undef CHUNK_STX
#undef CHUNK_FLAG
}

__device__ __forceinline__ v4u ln_row_packed(const v4u xr) {
  float v[8];
#pragma unroll
  for (int e = 0; e < 4; ++e) { v[2 * e] = lo16(xr[e]); v[2 * e + 1] = hi16(xr[e]); }
  float s = ((v[0] + v[1]) + (v[2] + v[3])) + ((v[4] + v[5]) + (v[6] + v[7]));
  s = wave_sum(s);
  const float mean = s * (1.0f / DM);
  float q = 0.f;
#pragma unroll
  for (int j = 0; j < 8; ++j) { float t = v[j] - mean; q += t * t; }
  q = wave_sum(q);
  const float var = q * (1.0f / DM);
  const float mb = bfr(mean);
  const float sd = bfr(sqrtf(bfr(bfr(var) + 1e-5f)));
  const float ri = 1.0f / sd;
  v4u o;
#pragma unroll
  for (int e = 0; e < 4; ++e) {
    const unsigned t = pk2(v[2 * e] - mb, v[2 * e + 1] - mb);
    const float x0 = lo16(t), x1 = hi16(t);
    float q0 = x0 * ri, q1 = x1 * ri;
    q0 = __builtin_fmaf(__builtin_fmaf(-q0, sd, x0), ri, q0);
    q1 = __builtin_fmaf(__builtin_fmaf(-q1, sd, x1), ri, q1);
    o[e] = pk2(q0, q1);
  }
  return o;
}

__device__ __forceinline__ void ln4_packed(v4u (&x)[4]) {
  f32x2_t v[16];
#pragma unroll
  for (int e = 0; e < 16; ++e) v[e] = f32x2_t{lo16(x[e >> 2][e & 3]), hi16(x[e >> 2][e & 3])};
  const f32x2_t t = (((v[0] + v[1]) + (v[2] + v[3])) + ((v[4] + v[5]) + (v[6] + v[7]))) +
                    (((v[8] + v[9]) + (v[10] + v[11])) + ((v[12] + v[13]) + (v[14] + v[15])));
  const float mean = red_mod4(t.x + t.y) * (1.0f / DM);
  const float mb = bfr1(mean);
  f32x2_t q0 = {0.f, 0.f}, q1 = {0.f, 0.f};
#pragma unroll
  for (int e = 0; e < 16; e += 2) {
    v[e] -= mb;
    v[e + 1] -= mb;
    q0 += v[e] * v[e];
    q1 += v[e + 1] * v[e + 1];
  }
  q0 += q1;
  const float dm = mean - mb;
  const float var = fmaxf(red_mod4(q0.x + q0.y) * (1.0f / DM) - dm * dm, 0.f);
  const float sd = bfr1(sqrtf(bfr1(bfr1(var) + 1e-5f)));
  const float ri = 1.0f / sd;
#pragma unroll
  for (int e = 0; e < 16; ++e) {
    const unsigned t2 = pk2(v[e].x, v[e].y);
    const f32x2_t q = f32x2_t{lo16(t2), hi16(t2)} * ri;
    x[e >> 2][e & 3] = pk2(q.x, q.y);
  }
}

constexpr int MEGA_WGS = 256;
constexpr size_t MEGA_LDS = (size_t)(32 * 4 * 64 * 8 + 8 * SMS + 15 * QS + 128 + 16 * 64 * 8 + 8 * SOS + 512) * 2;
static_assert(MEGA_LDS <= 163840, "lds");
static_assert(MEGA_LDS >= (size_t)AW_CNT + 128 && MEGA_LDS >= (size_t)P_CN + 512, "lds");
static_assert(MEGA_LDS >= (size_t)CV_LDS, "lds");
static_assert(MEGA_LDS >= (size_t)2 * 32 * SMS * 2 && MEGA_LDS >= 131072, "lds");
static_assert(MEGA_LDS >= (size_t)8 * (16640 + 2 * (704 + 144)), "lds");
__device__ __forceinline__ MegaArgs role_args(const int b, const int wv) {
  auto p = (const __attribute__((address_space(4))) u64*)__builtin_amdgcn_kernarg_segment_ptr();
  asm volatile("" : "+s"(p));
#define KA(i) ((u16*)(__attribute__((address_space(1))) u16*)p[i])
  return MegaArgs{KA(0), KA(1), (char*)KA(2), KA(3), KA(4), KA(5), KA(6), KA(7), KA(8), KA(9), KA(10), KA(11), KA(12), KA(13), b, wv};
#undef KA
}
__global__ __launch_bounds__(512, 2) void mega_kernel(const u16* mem0, const u16* Dskip, char* ws, u16* out, const u16* x0,
                                                      const u16* logAre, const u16* Aim, const u16* Cre, const u16* Cim,
                                                      const u16* logdt, const u16* Wq, const u16* Wk, const u16* Wv,
                                                      const u16* Wo) {
  extern __shared__ __attribute__((aligned(16))) char smem[];
  const int b = vbid(), wv = __builtin_amdgcn_readfirstlane(threadIdx.x >> 6);
#define A role_args(b, wv)
  if (threadIdx.x == 0) ((unsigned long long*)(A.ws + WS_TOTAL))[256 * 64 + b] = __builtin_amdgcn_s_memrealtime();
  u64 tag = 0;
  {
    const unsigned t0 = now10();
    if ((b >= 32 && b < 64) || (b >= 200 && b < 232)) start_kgen(A, smem, b < 64 ? b - 32 : b - 168);
    else if (b >= 64 && b < 96) tag = start_stats<4>(A, b - 64);
    else if (b >= 240) (void)start_stats<8>(A, b - 224);
    const unsigned dl = (b >= 32 && b < 64) ? DL_C : (b >= 64 && b < 96) ? DL_A : b >= 240 ? DL_R2 : ((b >= 96 && b < 104) || b >= 232) ? DL_R : (b >= 200 && b < 232) ? DL_Q : 0u;
    while (now10() - t0 < dl) __builtin_amdgcn_s_sleep(16);
  }
  if ((EXP_OFF & 1) && b >= 64 && b < 80) return;
  if ((EXP_OFF & 8) && b >= 200 && b < 232) return;
  if ((EXP_OFF & 16) && b >= 32 && b < 64) return;
  if ((EXP_OFF & 32) && b >= 80 && b < 96) return;
  if (b < 64) role_chain(A, smem, b >> 5, b & 31, 0);
  else if (b < 96) role_aw(A, smem, (b >> 4) & 1, b & 15, tag);
  else if (b >= 104 && b < 168) role_conv(A, smem, b - 104);
  else {
    const bool kv = b >= 168 && b < 232;
    role_proj(A, smem, b >= (kv ? 200 : 240), kv ? (b - 168) & 31 : b & 15, kv);
  }
#undef A
}

__global__ void diag_kernel(const char* ws) {
  const unsigned long long* s = (const unsigned long long*)(ws + WS_TOTAL);
  const int l = threadIdx.x;
  unsigned long long e = ~0ull, q = 0;
  for (int i = 0; i < 256; ++i) e = s[256 * 64 + i] < e ? s[256 * 64 + i] : e;
  for (int i = 0; i < 0; ++i) q = s[16384 + i * 0] - e > q ? s[16384 + i * 0] - e : q;
  if (q > 30000) q = 30000;
  for (int i = 0; i < 4000000 && __builtin_amdgcn_s_memrealtime() < e + 25000 + 4 * q; ++i) __builtin_amdgcn_s_sleep(1);
}
extern "C" void kernel_launch(void* const* d_in, const int* in_sizes, int n_in, void* d_out, int out_size, void* d_ws,
                              size_t ws_size, hipStream_t stream) {
  MegaArgs A;
  A.x0 = (const u16*)d_in[0];
  A.logAre = (const u16*)d_in[1];
  A.Aim = (const u16*)d_in[2];
  A.Cre = (const u16*)d_in[3];
  A.Cim = (const u16*)d_in[4];
  A.logdt = (const u16*)d_in[5];
  A.Dskip = (const u16*)d_in[6];
  A.mem0 = (const u16*)d_in[8];
  A.Wq = (const u16*)d_in[9];
  A.Wk = (const u16*)d_in[10];
  A.Wv = (const u16*)d_in[11];
  A.Wo = (const u16*)d_in[12];
  A.ws = (char*)d_ws;
  A.out = (u16*)d_out;

  (void)hipFuncSetAttribute((const void*)mega_kernel, hipFuncAttributeMaxDynamicSharedMemorySize, (int)MEGA_LDS);
  mega_kernel<<<MEGA_WGS, 512, MEGA_LDS, stream>>>(A.mem0, A.Dskip, A.ws, A.out, A.x0, A.logAre, A.Aim, A.Cre, A.Cim, A.logdt,
                                                   A.Wq, A.Wk, A.Wv, A.Wo);
  diag_kernel<<<1, 64, 0, stream>>>(A.ws);
}

extern "C" size_t ws_stamp_offset() { return WS_TOTAL; }
extern "C" void ws_touch() { (void)hipFuncSetAttribute((const void*)mega_kernel, hipFuncAttributeMaxDynamicSharedMemorySize, (int)MEGA_LDS); }
```

```cpp
#include <hip/hip_runtime.h>
#include <hip/hip_bf16.h>
#include <stdint.h>

constexpr int L_SEQ = 2048;
constexpr int DM    = 512;
constexpr int NST   = 128;
constexpr int NH    = 32;
constexpr int DH    = 128;
constexpr int HD    = NH * DH;
constexpr int MM    = 8;
constexpr int SEG   = 32;
constexpr int NSEG  = L_SEQ / SEG;
constexpr int LK    = MM + SEG;

typedef unsigned short u16;
using bf16x8 = __attribute__((ext_vector_type(8))) short;
using f32x4  = __attribute__((ext_vector_type(4))) float;
using f32x16 = __attribute__((ext_vector_type(16))) float;

static __device__ __forceinline__ u16 f2bf(float f) {
  unsigned u = __float_as_uint(f);
  u += 0x7fffu + ((u >> 16) & 1u);
  return (u16)(u >> 16);
}
static __device__ __forceinline__ float bf2f(u16 h) { return __uint_as_float(((unsigned)h) << 16); }
static __device__ __forceinline__ float bfr(float f) { return bf2f(f2bf(f)); }

constexpr size_t SZ_X      = (size_t)L_SEQ * DM * 2;
constexpr size_t OFF_KF    = 0;
constexpr size_t OFF_UT    = OFF_KF + (size_t)DM * L_SEQ * 4;
constexpr size_t OFF_YT    = OFF_UT + SZ_X;
constexpr size_t OFF_XA    = OFF_YT + SZ_X;
constexpr size_t OFF_H     = OFF_XA + SZ_X;
constexpr size_t OFF_X2    = OFF_H + SZ_X;
constexpr size_t OFF_U2    = OFF_X2 + SZ_X;
constexpr size_t OFF_X3    = OFF_U2 + SZ_X;
constexpr size_t SZ_QK     = (size_t)L_SEQ * 2 * HD * 2;
constexpr size_t OFF_QK    = OFF_X3 + SZ_X;
constexpr size_t SZ_O      = (size_t)L_SEQ * HD * 2;
constexpr size_t OFF_VF    = OFF_QK + 2 * SZ_QK;
constexpr size_t SZ_PW     = (size_t)2 * NSEG * 16 * SEG * DM * 2;
constexpr size_t OFF_PW    = OFF_VF + 2 * SZ_O;
constexpr size_t SZ_MEM    = (size_t)NSEG * MM * HD * 2;
constexpr size_t OFF_MEMK  = OFF_PW + SZ_PW;
constexpr size_t OFF_MEMV  = OFF_MEMK + 2 * SZ_MEM;
constexpr size_t OFF_GT    = OFF_MEMV + 2 * SZ_MEM;
constexpr size_t OFF_EP    = OFF_GT + 65536;
constexpr size_t OFF_RD    = OFF_EP + 4096;
constexpr size_t OFF_KG    = OFF_RD + 4096;
constexpr size_t OFF_G     = OFF_KG + 8192;
constexpr size_t SZ_ACC    = (size_t)2 * 65536;
constexpr size_t OFF_Z     = OFF_GT + (size_t)128 * DM * 4;
constexpr size_t OFF_ACC   = OFF_Z;
constexpr size_t OFF_FL    = OFF_ACC + 2 * SZ_ACC;
constexpr int FL_FK  = 0;
constexpr int FL_FW  = FL_FK + 64 * 8 * 32;
constexpr int FL_FR  = FL_FW + 2 * 64 * 16 * 32;
constexpr int FL_FC  = FL_FR + 64 * 16 * 32;
constexpr int FL_FQ  = FL_FC + 2 * 64 * 32 * 32;
constexpr int FL_FP  = FL_FQ + 2 * 64 * 32 * 32;
constexpr int FL_FD  = FL_FP + 64 * 32 * 32;
constexpr int FL_FZ  = FL_FD + 64 * 2 * 32;
constexpr int FL_TMO = FL_FZ + 16 * 32;
constexpr int FL_END = FL_TMO + 64;
constexpr size_t Z_BYTES   = OFF_FL + (size_t)FL_END * 4 - OFF_Z;
constexpr size_t WS_TOTAL  = OFF_Z + Z_BYTES;
static_assert(Z_BYTES % 16 == 0 && OFF_Z % 256 == 0 && Z_BYTES <= 4 * 64 * 8192 && OFF_G + 2048 <= OFF_Z, "zero region");
static_assert(WS_TOTAL + (4 << 20) <= (size_t)256 * 1024 * 1024, "workspace too large");

static_assert(WS_TOTAL - OFF_XA <= (size_t)384 * 8 * 65536, "translation warm-up");
constexpr int CKN = 2336;
static_assert((size_t)DM * 2 * CKN * 2 <= (size_t)DM * L_SEQ * 4 + SZ_X, "K buffer");
__device__ __forceinline__ float pos_of_index(int p) {
  if (p < 256) return (float)p;
  if (p < 384) return (float)(256 + 2 * (p - 256));
  if (p < 512) return (float)(512 + 4 * (p - 384));
  if (p < 640) return (float)(1024 + 8 * (p - 512));
  return 2048.0f;
}
__device__ __forceinline__ int index_of_l(int l) {
  float pf = bfr((float)l);
  int p = (int)pf;
  if (p < 256) return p;
  if (p < 512) return 256 + ((p - 256) >> 1);
  if (p < 1024) return 384 + ((p - 512) >> 2);
  if (p < 2048) return 512 + ((p - 1024) >> 3);
  return 640;
}

template <int CTRL> __device__ __forceinline__ float dppr(float x) {
  return __builtin_bit_cast(float, __builtin_amdgcn_update_dpp(0, __builtin_bit_cast(int, x), CTRL, 0xf, 0xf, false));
}
__device__ __forceinline__ float wave_sum(float v) {
  v += dppr<0x121>(v);
  v += dppr<0x122>(v);
  v += dppr<0x124>(v);
  v += dppr<0x128>(v);
  {
    auto r = __builtin_amdgcn_permlane16_swap(__float_as_uint(v), __float_as_uint(v), false, false);
    v = __uint_as_float(r[0]) + __uint_as_float(r[1]);
  }
  {
    auto r = __builtin_amdgcn_permlane32_swap(__float_as_uint(v), __float_as_uint(v), false, false);
    v = __uint_as_float(r[0]) + __uint_as_float(r[1]);
  }
  return v;
}
__device__ __forceinline__ void ln_row8(float (&v)[8]) {
  float s = 0.f;
#pragma unroll
  for (int j = 0; j < 8; ++j) s += v[j];
  s = wave_sum(s);
  const float mean = s * (1.0f / DM);
  float q = 0.f;
#pragma unroll
  for (int j = 0; j < 8; ++j) { float t = v[j] - mean; q += t * t; }
  q = wave_sum(q);
  const float var = q * (1.0f / DM);
  const float mb = bfr(mean);
  const float sd = bfr(sqrtf(bfr(bfr(var) + 1e-5f)));
#pragma unroll
  for (int j = 0; j < 8; ++j) v[j] = bfr(bfr(v[j] - mb) / sd);
}

#define RLX_AGENT __ATOMIC_RELAXED, __HIP_MEMORY_SCOPE_AGENT
typedef __attribute__((address_space(1))) unsigned gu32;
typedef unsigned v4u __attribute__((ext_vector_type(4)));
constexpr int AUX_SC1 = 16;
constexpr unsigned SPIN_CAP = 1u << 18;

__device__ __forceinline__ bool wait_flags(const unsigned* flags, int lane, int nfl, unsigned want, unsigned* tmo) {
  for (unsigned spins = 0;; ++spins) {
    unsigned v = want;
    if (lane < nfl) v = __hip_atomic_load((gu32*)flags + lane * 32, RLX_AGENT);
    if (__all(v >= want)) break;
    if (spins > SPIN_CAP) {
      if (lane == 0) __hip_atomic_store((gu32*)tmo, 1u, RLX_AGENT);
      return false;
    }
  }
  __builtin_amdgcn_fence(__ATOMIC_ACQUIRE, "wavefront");
  return true;
}

#define STAMP(i)
#define STAMP_DUMP()
#define WSTAMP(i)
template <int CTRL> __device__ __forceinline__ float dppf(float old, float x) {
  return __builtin_bit_cast(float, __builtin_amdgcn_update_dpp(__builtin_bit_cast(int, old), __builtin_bit_cast(int, x), CTRL, 0xf, 0xf, false));
}
__device__ __forceinline__ float wave_max_dpp(float x) {
  const float ninf = -3e38f;
  x = fmaxf(x, dppf<0x111>(ninf, x));
  x = fmaxf(x, dppf<0x112>(ninf, x));
  x = fmaxf(x, dppf<0x114>(ninf, x));
  x = fmaxf(x, dppf<0x118>(ninf, x));
  x = fmaxf(x, dppf<0x142>(ninf, x));
  x = fmaxf(x, dppf<0x143>(ninf, x));
  return __builtin_bit_cast(float, __builtin_amdgcn_readlane(__builtin_bit_cast(int, x), 63));
}
__device__ __forceinline__ float wave_sum_dpp(float x) {
  x += dppf<0x111>(0.f, x);
  x += dppf<0x112>(0.f, x);
  x += dppf<0x114>(0.f, x);
  x += dppf<0x118>(0.f, x);
  x += dppf<0x142>(0.f, x);
  x += dppf<0x143>(0.f, x);
  return __builtin_bit_cast(float, __builtin_amdgcn_readlane(__builtin_bit_cast(int, x), 63));
}
constexpr int SMS = 528;
constexpr int QS = 144;
constexpr int VS = 80;
constexpr int SOS = 144;
#define VAR_NREP 4
constexpr int NREP = VAR_NREP;
__device__ __forceinline__ float xsum16(float x) {
  auto r = __builtin_amdgcn_permlane16_swap(__float_as_uint(x), __float_as_uint(x), false, false);
  return __uint_as_float(r[0]) + __uint_as_float(r[1]);
}
__device__ __forceinline__ float xsum32(float x) {
  auto r = __builtin_amdgcn_permlane32_swap(__float_as_uint(x), __float_as_uint(x), false, false);
  return __uint_as_float(r[0]) + __uint_as_float(r[1]);
}
__device__ __forceinline__ float xmax16(float x) {
  auto r = __builtin_amdgcn_permlane16_swap(__float_as_uint(x), __float_as_uint(x), false, false);
  return fmaxf(__uint_as_float(r[0]), __uint_as_float(r[1]));
}
__device__ __forceinline__ float xmax32(float x) {
  auto r = __builtin_amdgcn_permlane32_swap(__float_as_uint(x), __float_as_uint(x), false, false);
  return fmaxf(__uint_as_float(r[0]), __uint_as_float(r[1]));
}
__device__ __forceinline__ float red_mod4(float x) {
  x += dppf<0x124>(0.f, x);
  x += dppf<0x128>(0.f, x);
  return xsum32(xsum16(x));
}
typedef __bf16 bf16x2_t __attribute__((ext_vector_type(2)));
typedef float f32x2_t __attribute__((ext_vector_type(2)));
__device__ __forceinline__ unsigned pk2(float a, float b) {
  f32x2_t v = {a, b};
  bf16x2_t r = __builtin_convertvector(v, bf16x2_t);
  return __builtin_bit_cast(unsigned, r);
}
typedef _Float16 f16x2_t __attribute__((ext_vector_type(2)));
typedef _Float16 f16x8 __attribute__((ext_vector_type(8)));
__device__ __forceinline__ unsigned pkh(float a, float b) {
  f32x2_t v = {a, b};
  return __builtin_bit_cast(unsigned, __builtin_convertvector(v, f16x2_t));
}
__device__ __forceinline__ float h2f(u16 h) { return (float)__builtin_bit_cast(_Float16, h); }
__device__ __forceinline__ float one(float x) {
  asm("" : "+v"(x));
  return x;
}
__device__ __forceinline__ unsigned pk2s(float a, float b) { return pk2(one(a), one(b)); }
__device__ __forceinline__ float dot2sq(const unsigned x, const float acc) {
  return __builtin_amdgcn_fdot2_f32_bf16(__builtin_bit_cast(bf16x2_t, x), __builtin_bit_cast(bf16x2_t, x), acc, false);
}
__device__ __forceinline__ float lo16(unsigned u) { return __uint_as_float(u << 16); }
__device__ __forceinline__ float hi16(unsigned u) { return __uint_as_float(u & 0xffff0000u); }
__device__ __forceinline__ float bfr1(float x) { return lo16(pk2(x, x)); }
__device__ __forceinline__ float half_sum(float v) {
  v += dppr<0x121>(v);
  v += dppr<0x122>(v);
  v += dppr<0x124>(v);
  v += dppr<0x128>(v);
  return xsum16(v);
}
__device__ __forceinline__ void scale_pair(float a, float b, float scale, float& oa, float& ob) {
  unsigned u = pk2(a, b);
  unsigned v = pk2s(lo16(u) * scale, hi16(u) * scale);
  oa = lo16(v);
  ob = hi16(v);
}

#define XMAP 2
__device__ __forceinline__ int vbid() {
  const int b = blockIdx.x, x = b & 7, t = b >> 3;
  if (XMAP == 2) {
    const int q = (t >> 2) + 2 * (x & 4);
    const unsigned long long m = (q & 8) ? 0xf0d85040cdc9ada9ull : 0x76726e6a25210501ull;
    const int e = (int)(m >> (8 * (q & 7))) & 255;
    return (e & 252) + ((x & 3) << (2 + (e & 3))) + (t & 3) + (q == 14 ? ((x & 2) ? 8 : -120) : 0);
  }
  if (XMAP == 0) {
    if (b < 64) return (b & 32) + 4 * x + (t & 3);
    if (b < 96) return (b & ~15) + 2 * x + (t & 1);
    if (b >= 104 && b < 168) return 104 + 8 * x + t - 13;
    if (b >= 168 && b < 232) return 168 + ((b - 168) & 32) + 4 * x + ((t - 21) & 3);
    return b;
  }
  if (XMAP == 3) {
    const int y = x & 3;
    if (t >= 16 && t < 24) return 104 + 16 * y + 2 * (x & 4) + t - 16;
    if (x < 4) return t < 16 ? 4 * (t & 8) + 8 * x + (t & 7) : 64 + 4 * (t & 4) + 4 * y + (t & 3);
    if (t >= 24) return (t >= 28 ? 240 : y < 2 ? 96 : 224) + 4 * y + (t & 3);
    return 168 + 4 * (t & 8) + 8 * y + (t & 7);
  }
  if (XMAP == 4) {
    const int y = x & 3;
    if (x < 4) return t < 16 ? 4 * (t & 8) + 8 * x + (t & 7) : t < 28 ? 104 + 16 * y + t - 16 : 64 + 4 * y + (t & 3);
    if (t >= 24) return (t >= 28 ? 240 : y < 2 ? 96 : 224) + 4 * y + (t & 3);
    if (t < 16) return 168 + 4 * (t & 8) + 8 * y + (t & 7);
    return t < 20 ? 104 + 16 * y + t - 4 : 80 + 4 * y + (t & 3);
  }
  if (XMAP == 2 || XMAP >= 5) {
    if (XMAP == 5 && x < 4 && t < 16) return 16 * x + t;
    if (XMAP == 6 && x < 4 && t < 16) return 32 * (x & 1) + 8 * (x & 2) + t;
    if (XMAP == 7 && x < 4) return (x & 1) ? 104 + 16 * (x & 2) + t : 16 * x + t;
    if (x < 4) return t < 16 ? 4 * (t & 8) + 8 * x + (t & 7) : 104 + 16 * x + t - 16;
    const int y = x - 4;
    if (t >= 24) return (t >= 28 ? 240 : y < 2 ? 96 : 224) + 4 * y + (t & 3);
    if (t < 16) return 168 + 4 * (t & 8) + 8 * y + (t & 7);
    return 64 + 4 * (t & 4) + 4 * y + (t & 3);
  }
  if (x < 2) return 2 * (t & 16) + 16 * x + (t & 15);
  const int i = 16 * (x - 2) + (t & 15);
  if (t < 16) {
    const int k = (i * 171) >> 9, r = i - 3 * k;
    return r == 0 ? 168 + k : r == 1 ? 200 + k : 64 + 16 * (k & 1) + (k >> 1);
  }
  const int g = i >> 3, e = i & 7;
  if (g == 3) return 96 + e;
  if (g == 7) return 232 + e;
  if (g == 9) return 240 + e;
  if (g == 11) return 248 + e;
  return 104 + 8 * (g - (g > 3) - (g > 7) - (g > 9)) + e;
}
#define MSTAMP()
#define WSTAMP2()
#define SST(k)
#define SSX(k, c)
#define SSW(k, v)
#define TS(k, ss, t)
#define LS(k, ss)
#define LS_DUMP()
struct MegaArgs {
  const u16* mem0;
  const u16* Dskip;
  char* ws;
  u16* out;
  const u16* x0;
  const u16 *logAre, *Aim, *Cre, *Cim, *logdt;
  const u16 *Wq, *Wk, *Wv, *Wo;
  int vb, wv;
};

__device__ __forceinline__ int lane_id() {
  int l;
  asm volatile("v_mbcnt_lo_u32_b32 %0, -1, 0\n\tv_mbcnt_hi_u32_b32 %0, -1, %0" : "=v"(l));
  __builtin_assume(l >= 0 && l < 64);
  return l;
}
__device__ __forceinline__ bool spin_timeout(unsigned spins, unsigned* tmo) {
  if (spins > SPIN_CAP) {
    if (lane_id() == 0) __hip_atomic_store((gu32*)tmo, 1u, RLX_AGENT);
    return true;
  }
  return false;
}
#define ROT_H (4 * (h & 1))
#define ROT_T (h & 2)
#define C2G 420
#define CVSP 40
#define PFA 0
#define PFB 0
#define WO1 1
#define AWH 0
#define PEEK0 1
#define SLP0 2
#define GF_R 400
#define GF_V 0
#define PP 0
#define EL 1
#define IPF 3
#define XL 0
#define MC3 2
#define XZD 300
#define XZP 4
#define NFW 1
#define IPS 128u
#define WNT 17
#define OAUX 16
#define XAUX 16
#define GF_RT 0
#define GF_RU 0
#define EF 1
#define RGB 3
#define GR2 345
#define GR3 280
#define RLS 1
#define GT1 120
#define GT2 32
#define GT3 1
#define PCD 100
#define RAUX AUX_SC1
#define RVAL 1
#define RLEAD 60
#define SLP_D 0
#define RFA 1
#define TP 1
#define TPS (4 * TP + 4)
#define MCE 1
#define H4S 0
#define GF_H 150
#define CVT2 48
#define GF_P 0
#define GF0 8
#define SLP_H 12
#define HX0 6
#define CVSTEP 2
#define CVLATE 64
#define PACE 203
#define PACE0 6
#define RPRIO 3
#define DL_R2 1400
#define PNL 8
#define GSL 12
#define AWDIR 1
#define AWAT 0
#define AWRP 0
#define PEPS 4
#define FGATE 0
#define DL_C 0
#define DL_A 400
#define DL_R 0
#define DL_Q 0
#define KC0 0
#define CPRIO 1
#define P1PRIO 3
#define QLEAD 2
#define POLL_SLEEP 12
#define SLP_C POLL_SLEEP
#define SLP_A POLL_SLEEP
#define SLP_W POLL_SLEEP
#define SLP_R POLL_SLEEP
#define SLP_V POLL_SLEEP
#define SLP_Q POLL_SLEEP
#define SLP_M 4
#define AWPRIO 2
#define SLP_S 8
#define SPOS 4
#define CVPRE 12
#define QPD 3
#define QPRI 2
#define TLBW 3
#define QHV 0
#define QCHK 0
#define LEADN 99
#define GATE_V 170
#define GATE_R 380
#define GATE_N 380
#define AWAHEAD 1
#define GATE_M 150
#define PRI_A 3
#define PRI_WA 2
#define PRI_WB 1
#define AWSKIP 3
#define PRI_0 3
#define AWDEF 0
#define SLP_F 2
#define EXP_OFF 0
#define AWSPEC 1
#define CVTHR 48
#define NR_C 8
#define NR_R 8
#define NR_W 8
#define NR_N 2
#define QAHEAD 0
#define NS0 4
#define SLP_N POLL_SLEEP
template <int SLP, int WPL>
__device__ __forceinline__ bool wait_n(const unsigned* f, int n, unsigned* tmo) {
  const int l = lane_id();
  for (unsigned spins = 0;; ++spins) {
    unsigned v = 1u;
    if (l < n) v = __hip_atomic_load((gu32*)f + (l / WPL) * 32 + l % WPL, RLX_AGENT);
    if (__all(v != 0u)) break;
    if (spin_timeout(spins, tmo)) return false;
    __builtin_amdgcn_s_sleep(SLP);
  }
  asm volatile("" ::: "memory");
  return true;
}
__device__ __forceinline__ unsigned peek(const unsigned* base, const unsigned off) {
  unsigned r;
  asm volatile("global_atomic_or %0, %1, %2, %3 sc0\n\ts_waitcnt vmcnt(0)" : "=&v"(r) : "v"(off), "v"(0u), "s"(base) : "memory");
  return r;
}
__device__ __forceinline__ unsigned now10() { return (unsigned)__builtin_amdgcn_s_memrealtime(); }
__device__ __forceinline__ void gate(const unsigned t) {
#pragma unroll 1
  for (int i = 0; i < 64; ++i) {
    const int d = (int)(t - now10());
    if (d <= 0) break;
    if (d > GT1) __builtin_amdgcn_s_sleep(32);
    else if (d > GT2) __builtin_amdgcn_s_sleep(8);
    else __builtin_amdgcn_s_sleep(GT3);
  }
}
template <int SLP, int WPL, int GT>
__device__ __forceinline__ void grp_wait(const bool leader, volatile unsigned* sw, const unsigned val, const unsigned* f, int n,
                                         unsigned* tmo, unsigned& tl) {
  if (leader) {
    const int l = lane_id();
    for (unsigned spins = 0;; ++spins) {
      unsigned v = 1u;
      if (l < n) v = __hip_atomic_load((gu32*)f + (l / WPL) * 32 + l % WPL, RLX_AGENT);
      if (__all(v != 0u) || spin_timeout(spins, tmo)) break;
      if (GT && spins == 0) gate(tl);
      else __builtin_amdgcn_s_sleep(SLP);
    }
    if (GT) tl = now10() + GT;
    if (sw && l == 0) *sw = val;
  } else {
    for (unsigned spins = 0; *sw < val; ++spins) {
      if (spins > 64u * SPIN_CAP) break;
      if (FGATE && GT && spins == 0) gate(tl);
      __builtin_amdgcn_s_sleep(SLP_F);
    }
    if (FGATE && GT) tl = now10() + GT;
  }
  asm volatile("" ::: "memory");
}
template <int SLP, int WPL>
__device__ __forceinline__ bool wg_wait(const unsigned* f, int n, unsigned* tmo) {
  if (threadIdx.x < 64) (void)wait_n<SLP, WPL>(f, n, tmo);
  __syncthreads();
  asm volatile("" ::: "memory");
  return true;
}
__device__ __forceinline__ void set_flag(unsigned* f) { __hip_atomic_store((gu32*)f, 1u, RLX_AGENT); }
#define DRAIN() asm volatile("s_waitcnt vmcnt(0)" ::: "memory")
#define MKRS(ptr, bytes) __builtin_amdgcn_make_buffer_rsrc((void*)(ptr), 0, (int)(bytes), 0x00020000)
typedef unsigned v2u __attribute__((ext_vector_type(2)));
#define LD128(rs, voff, soff) __builtin_amdgcn_raw_buffer_load_b128(rs, voff, soff, AUX_SC1)
#define LD64(rs, voff, soff) __builtin_amdgcn_raw_buffer_load_b64(rs, voff, soff, AUX_SC1)
#define ST128(v, rs, voff, soff) ST128A(v, rs, voff, soff, AUX_SC1)
#define ST128A(v, rs, voff, soff, aux)                                        \
  do {                                                                        \
    const v4u st_d_ = (v);                                                    \
    __builtin_amdgcn_raw_buffer_store_b128(st_d_, rs, voff, soff, aux);       \
    asm volatile("s_nop 1" ::"v"(st_d_) : "memory");                          \
  } while (0)
#define ST64(v, rs, voff, soff) __builtin_amdgcn_raw_buffer_store_b64(v, rs, voff, soff, AUX_SC1)
#define BF8(x) __builtin_bit_cast(bf16x8, x)
#define MFMA16(a, b, c) __builtin_amdgcn_mfma_f32_16x16x32_bf16(a, b, c, 0, 0, 0)
typedef short bf16x4 __attribute__((ext_vector_type(4)));
#define MFMA16K(a, b, c) \
  __builtin_amdgcn_mfma_f32_16x16x16bf16_1k(__builtin_bit_cast(bf16x4, a), __builtin_bit_cast(bf16x4, b), c, 0, 0, 0)
__device__ __forceinline__ float fmax_nn(float a, float b) { return __builtin_elementwise_maximum(a, b); }
typedef __bf16 bfv8 __attribute__((ext_vector_type(8)));
typedef __bf16 bfv16 __attribute__((ext_vector_type(16)));
typedef unsigned v8u __attribute__((ext_vector_type(8)));
#define SMF(a, b, c, idx) \
  __builtin_amdgcn_smfmac_f32_16x16x64_bf16(__builtin_bit_cast(bfv8, a), __builtin_bit_cast(bfv16, b), c, idx, 0, 0)
__device__ __forceinline__ float swap_add32(float a, float b) {
  auto r = __builtin_amdgcn_permlane32_swap(__float_as_uint(a), __float_as_uint(b), false, false);
  return __uint_as_float(r[0]) + __uint_as_float(r[1]);
}
__device__ __forceinline__ float swap_add16(float a, float b) {
  auto r = __builtin_amdgcn_permlane16_swap(__float_as_uint(a), __float_as_uint(b), false, false);
  return __uint_as_float(r[0]) + __uint_as_float(r[1]);
}
__device__ __forceinline__ v4u smf_fix(const v4u d) {
  auto x = __builtin_amdgcn_permlane16_swap(d.x, d.z, false, false);
  auto y = __builtin_amdgcn_permlane16_swap(d.y, d.w, false, false);
  return v4u{x[0], x[1], y[0], y[1]};
}
__device__ __forceinline__ v8u smf_bfrag(const u16* p) {
  const v2u a = *reinterpret_cast<const v2u*>(p), b = *reinterpret_cast<const v2u*>(p + 8);
  const v2u c = *reinterpret_cast<const v2u*>(p + 32), d = *reinterpret_cast<const v2u*>(p + 40);
  return v8u{a.x, b.x, a.y, b.y, c.x, d.x, c.y, d.y};
}

#define VAR_PPA 0
#define VAR_PPB 0
#define VAR_THR 0
#define VAR_THR2 0
#define CV_LEAD 100
#define VAR_EARLY 1
#define VAR_DRAIN 1
#define VAR_D1 0
#define VAR_D2 0
#define VAR_DR2 1
#define VAR_S1 0
#define VAR_S2 0
typedef __attribute__((address_space(1))) unsigned long long gu64;
typedef __attribute__((address_space(1))) char gch;
__device__ __forceinline__ int cvt_rpi(float x) {
  int r;
  asm("v_cvt_rpi_i32_f32 %0, %1" : "=v"(r) : "v"(x));
  return r;
}
typedef unsigned long long u64;
__device__ __forceinline__ u64 call_tag(const MegaArgs& A) {
  u64 t = *(const __attribute__((address_space(4))) u64*)(A.ws + OFF_EP) * 6364136223846793005ull + 1442695040888963407ull;
  asm volatile("" : "+s"(t)::"memory");
  return t;
}
__device__ __forceinline__ u64 tagged(const u64 tag, const unsigned v) { return (tag & 0xffffffffffffull) | (u64)(v >> 16) << 48; }
__device__ __forceinline__ bool has_tag(const u64 tag, const unsigned lo, const unsigned hi) {
  return lo == (unsigned)tag && (hi & 0xffffu) == ((unsigned)(tag >> 32) & 0xffffu);
}
__device__ __forceinline__ u64 ready_look(const MegaArgs& A) {
  return __hip_atomic_load((gu64*)(A.ws + OFF_RD) + (blockIdx.x & 7) * 64 + lane_id(), RLX_AGENT);
}
#define RD_ASK(v) \
  asm volatile("global_load_dwordx2 %0, %1, %2 sc1" : "=v"(v) : "v"(lane_id() * 8), "s"(A.ws + OFF_RD + (blockIdx.x & 7) * 512) : "memory")
__device__ __forceinline__ void ready_wait(const MegaArgs& A, const u64 tag, u64 v) {
  for (unsigned spins = 0; !__all(v == tag) && spins < SPIN_CAP; ++spins) {
    __builtin_amdgcn_s_sleep(8);
    v = ready_look(A);
  }
  asm volatile("" ::: "memory");
}
typedef short v4s __attribute__((ext_vector_type(4)));
typedef __attribute__((address_space(3))) v4s* lp4s;
__device__ __forceinline__ void glds16(const void* sbase, const unsigned voff, const unsigned lds, const bool nt = false) {
  unsigned keep;
#define GLDS16(M)                                                                                                                     \
  asm volatile("s_nop 4\n\ts_mov_b32 %0, m0\n\ts_mov_b32 m0, %3\n\ts_nop 0\n\tglobal_load_lds_dwordx4 %1, %2" M "\n\ts_mov_b32 m0, %0" \
               : "=&s"(keep)                                                                                                          \
               : "v"(voff), "s"(sbase), "s"(lds)                                                                                      \
               : "memory")
  if (nt) GLDS16(" nt");
  else GLDS16("");
#undef GLDS16
}
__device__ __forceinline__ void glds16c(const void* sbase, const unsigned voff, const unsigned lds) {
  unsigned keep;
  asm volatile("s_nop 4\n\ts_mov_b32 %0, m0\n\ts_mov_b32 m0, %3\n\ts_nop 0\n\tglobal_load_lds_dwordx4 %1, %2 sc1\n\ts_mov_b32 m0, %0"
               : "=&s"(keep)
               : "v"(voff), "s"(sbase), "s"(lds)
               : "memory");
}
__device__ __forceinline__ void glds64c(const void* sbase, const unsigned voff, const unsigned lds) {
  unsigned keep;
  asm volatile("s_nop 4\n\ts_mov_b32 %0, m0\n\ts_mov_b32 m0, %3\n\ts_nop 0\n\tglobal_load_lds_dwordx4 %1, %2 sc1\n\t"
               "global_load_lds_dwordx4 %1, %2 offset:1024 sc1\n\tglobal_load_lds_dwordx4 %1, %2 offset:2048 sc1\n\t"
               "global_load_lds_dwordx4 %1, %2 offset:3072 sc1\n\ts_mov_b32 m0, %0"
               : "=&s"(keep)
               : "v"(voff), "s"(sbase), "s"(lds)
               : "memory");
}
__device__ __forceinline__ v2u trd(const unsigned a) {
  return __builtin_bit_cast(v2u, __builtin_amdgcn_ds_read_tr16_b64_v4i16((lp4s)(unsigned long)a));
}
__device__ __forceinline__ unsigned lds_base(char* smem) { return (unsigned)(unsigned long)(__attribute__((address_space(3))) char*)smem; }
#define RAW_BAR()                                           \
  do {                                                      \
    asm volatile("s_waitcnt lgkmcnt(0)" ::: "memory");      \
    __builtin_amdgcn_s_barrier();                           \
    asm volatile("" ::: "memory");                          \
  } while (0)
#define WAIT_VM(n) asm volatile("s_waitcnt vmcnt(" #n ")" ::: "memory")
template <bool SP>
__device__ __forceinline__ void wqkv_issue(const u16* Wm, const int hf, const int h, const int w, const int l, const unsigned sb, const bool nt) {
  const char* base = (const char*)Wm + (size_t)(256 * hf + 32 * w) * (HD * 2) + h * (DH * 2);
#define WQKV(NT_)                                                                                                       \
  _Pragma("unroll") for (int i = 0; i < 8; ++i) {                                                                      \
    const int r = l >> 4;                                                                                              \
    const int f = SP ? ((r & 1) | ((i & 1) << 1) | (((i >> 1) & 1) << 2)) : (r | (((i >> 1) & 1) << 2));               \
    glds16(base, (unsigned)((4 * i + r) * (HD * 2) + (((l & 15) ^ (f << 1)) << 4)),                                    \
           sb + (w >> 2) * 65536 + hf * 32768 + (w & 3) * 8192 + i * 1024, NT_);                                       \
  }
  if (nt) { WQKV(true) } else { WQKV(false) }
#undef WQKV
}

__device__ __forceinline__ unsigned row_stats(const v4u raw) {
  float v[8];
#pragma unroll
  for (int e = 0; e < 4; ++e) { v[2 * e] = lo16(raw[e]); v[2 * e + 1] = hi16(raw[e]); }
  float s = 0.f;
#pragma unroll
  for (int e = 0; e < 8; ++e) s += v[e];
  const float mean = wave_sum(s) * (1.0f / DM);
  float q = 0.f;
#pragma unroll
  for (int e = 0; e < 8; ++e) { const float t = v[e] - mean; q += t * t; }
  const float var = wave_sum(q) * (1.0f / DM);
  const float sd = bfr(sqrtf(bfr(bfr(var) + 1e-5f)));
  return (unsigned)f2bf(mean) | ((unsigned)f2bf(sd) << 16);
}
__device__ __forceinline__ void ask_slow(const MegaArgs& A, const int k, unsigned (&t)[3]) {
  const int l = lane_id(), x = blockIdx.x & 7;
  const char *wq = (const char*)A.Wq, *wv = (const char*)A.Wv;
  asm("" : "+s"(wq), "+s"(wv));
#pragma unroll
  for (int m = 0; m < 3; ++m) {
    const char* W = m == 1 ? (const char*)A.Wk : ((m == 0) == (x < 4)) ? wv : wq;
    const int a = (int)((unsigned long)W >> 8) + 8 * (x & 3), h = k < 32 ? ((1 - a) & 3) + 4 * (k & 1) : (31 - a) & 31;
    const int r = k < 32 ? k >> 1 : k - 32;
    t[m] = __builtin_amdgcn_raw_buffer_load_b32(MKRS(W, (unsigned)k < 48u && h < 8 ? DM * HD * 2 : 0), (l >> 1) * (HD * 2) + (l & 1) * 128,
                                                (32 * (r & 15)) * (HD * 2) + (8 * (x & 3) + (h & 7)) * 256, 0);
  }
}
#define ASKED(t) asm volatile("" ::"v"(t[0]), "v"(t[1]), "v"(t[2]))
template <int N>
__device__ __forceinline__ u64 start_stats(const MegaArgs& A, const int idx) {
  const int w = A.wv, l = lane_id();
  const int r0 = N * (8 * idx + w);
  unsigned ta[3], tb[3];
  v4u raw[N];
#pragma unroll
  for (int i = 0; i < N; ++i) raw[i] = *reinterpret_cast<const v4u*>(A.x0 + (size_t)(r0 + i) * DM + l * 8);
  asm volatile("" ::: "memory");
  ask_slow(A, 16 * w + (idx & 3) + (idx >> 4) * N, ta);
  if (N > 4) ask_slow(A, 16 * w + (idx & 3) + 12, tb);
  unsigned tc[2] = {};
  if (N == 4 && AWH > 0) tc[0] = *reinterpret_cast<const unsigned*>(A.Wo + (size_t)idx * 65536 + 32768 + (64 * w + l) * 64);
  if (N == 4 && AWH > 1 && w >= 4) tc[1] = *reinterpret_cast<const unsigned*>(A.Wo + (size_t)idx * 65536 + (64 * w + l) * 64);
  const u64 tag = call_tag(A);
  unsigned stw = 0u;
#pragma unroll
  for (int i = 0; i < N; ++i) {
    const unsigned t = row_stats(raw[i]);
    if ((l >> 1) == i) stw = t;
  }
  if (l < 2 * N) __hip_atomic_store((gu64*)(A.ws + OFF_GT) + 2 * r0 + l, tagged(tag, (l & 1) ? stw : stw << 16), RLX_AGENT);
  SST(0);
  ASKED(ta);
  if (N > 4) ASKED(tb);
  asm volatile("" ::"v"(tc[0]), "v"(tc[1]));
  return tag;
}
#define MFMAH(a, b, c) __builtin_amdgcn_mfma_f32_16x16x32_f16(a, b, c, 0, 0, 0)
struct Cx {
  f32x2_t r, i;
};
__device__ __forceinline__ Cx cmul(const Cx a, const Cx b) { return Cx{a.r * b.r - a.i * b.i, a.r * b.i + a.i * b.r}; }
struct KGen {
  unsigned pa, pb, pc, pd;
  u16 pe;
  f32x2_t ar, ai;
  Cx C;
  char* st;
  v4u bh[8], bl[8];
  __device__ __forceinline__ void load(const MegaArgs& A, const int d, const int l) {
    pa = *reinterpret_cast<const unsigned*>(A.logAre + d * NST + 2 * l);
    pb = *reinterpret_cast<const unsigned*>(A.Aim + d * NST + 2 * l);
    pc = *reinterpret_cast<const unsigned*>(A.Cre + d * NST + 2 * l);
    pd = *reinterpret_cast<const unsigned*>(A.Cim + d * NST + 2 * l);
    pe = A.logdt[d];
  }
  __device__ __forceinline__ void setup() {
    const float dt = bfr(expf(bf2f(pe)));
    ar = f32x2_t{-bfr(expf(lo16(pa))) * dt, -bfr(expf(hi16(pa))) * dt};
    ai = f32x2_t{lo16(pb) * dt, hi16(pb) * dt};
    C = Cx{f32x2_t{lo16(pc) * dt, hi16(pc) * dt}, f32x2_t{lo16(pd) * dt, hi16(pd) * dt}};
  }
  __device__ __forceinline__ float scale() {
    const float b = 2.0f * wave_sum((fabsf(C.r.x) + fabsf(C.r.y)) + (fabsf(C.i.x) + fabsf(C.i.y)));
    const unsigned eb = max(__float_as_uint(b) >> 23, 25u);
    const float ks = __uint_as_float((268u - eb) << 23);
    C.r *= ks;
    C.i *= ks;
    return __uint_as_float((eb - 24u) << 23);
  }
  __device__ __forceinline__ Cx power(const float p) const {
    Cx z;
#pragma unroll
    for (int e = 0; e < 2; ++e) {
      const float x = ai[e] * p;
      const float f = __builtin_fmaf(x, 6.4206382e-9f, __builtin_fmaf(x, 0.15915494f, -__builtin_rintf(x * 0.15915494f)));
      const float m = __expf(ar[e] * p);
      z.r[e] = m * __builtin_amdgcn_cosf(f);
      z.i[e] = m * __builtin_amdgcn_sinf(f);
    }
    return z;
  }
  static __device__ __forceinline__ unsigned rest(const f32x2_t y, const unsigned h) {
    unsigned r;
    asm("v_fma_mixlo_f16 %0, %1, -1.0, %2 op_sel_hi:[1,0,0]" : "=v"(r) : "v"(h), "v"(y.x));
    asm("v_fma_mixhi_f16 %0, %1, -1.0, %2 op_sel:[1,0,0] op_sel_hi:[1,0,0]" : "+v"(r) : "v"(h), "v"(y.y));
    return r;
  }
  __device__ __forceinline__ char* rows(char* p, Cx& y, const Cx s, const int n) const {
#pragma unroll 1
    for (int i = 0; i < n; i += 2) {
#pragma unroll
      for (int k = 0; k < 2; ++k) {
        const unsigned h0 = pkh(y.r.x, y.r.y), h1 = pkh(y.i.x, y.i.y);
        *reinterpret_cast<v2u*>(p + 1040 * k) = v2u{h0, h1};
        *reinterpret_cast<v2u*>(p + 1040 * k + 512) = v2u{rest(y.r, h0), rest(y.i, h1)};
        y = cmul(y, s);
      }
      p += 2080;
    }
    return p;
  }
  __device__ __forceinline__ void get(v4u (&h)[8], v4u (&o)[8], const int l) const {
    const char* p = st + (l & 15) * 1040 + (l >> 4) * 16;
    asm volatile("" ::: "memory");
#pragma unroll
    for (int k = 0; k < 8; ++k) {
      h[k] = *reinterpret_cast<const v4u*>(p + 64 * k);
      o[k] = *reinterpret_cast<const v4u*>(p + 512 + 64 * k);
    }
    asm volatile("" ::: "memory");
  }
  Cx cy, cs;
  __device__ __forceinline__ void columns(const int l, const int j0, const int j1) {
    if (j0 == 0) {
      cs = power(16.f);
      cs.i = -cs.i;
      cy = Cx{f32x2_t{16384.f, 16384.f}, f32x2_t{0.f, 0.f}};
    }
    rows(st + l * 8 + 1040 * j0, cy, cs, j1 - j0);
  }
  template <bool COL>
  __device__ __forceinline__ f32x4 tile(const int l) const {
    v4u h[8], o[8];
    get(h, o, l);
    f32x4 d = {}, e = {}, f = {};
#define H8(x) __builtin_bit_cast(f16x8, x)
#define KMM(a, b, d) (COL ? MFMAH(H8(b), H8(a), d) : MFMAH(H8(a), H8(b), d))
#pragma unroll
    for (int k = 0; k < 8; ++k) {
      d = KMM(h[k], bh[k], d);
      e = KMM(o[k], bh[k], e);
      f = KMM(h[k], bl[k], f);
    }
#undef KMM
#undef H8
    return (d + (e + f)) * (1.0f / 8192);
  }
};
#define ACC_INIT 0xc9b26c86c9b26ca0ull
#define CHAIN_ASM_ATT \
  "s_mov_b32 s95, 272\n" \
  "s_mov_b32 s91, 0x4c7c2000\n" \
  "s_mov_b32 s85, 0xc9b26c9b\n" \
  "s_mov_b32 s92, 0xc9326c9b\n" \
  "s_mov_b32 s93, 0xc9326c9b\n" \
  "s_mov_b32 s78, 0x3e0293ee\n" \
  "s_mov_b32 s76, 0x33333333\n" \
  "s_mov_b32 s77, 0x33333333\n" \
  "s_lshl_b32 s98, s73, 10\n" \
  "s_add_u32 s98, s98, s74\n" \
  "s_add_u32 s98, s98, 131072\n" \
  "s_lshl_b32 s97, s73, 9\n" \
  "s_add_u32 s97, s97, s74\n" \
  "s_add_u32 s97, s97, 158720\n" \
  "s_lshl_b32 s99, s73, 10\n" \
  "v_mbcnt_lo_u32_b32 v48, -1, 0\n" \
  "v_mbcnt_hi_u32_b32 v48, -1, v48\n" \
  "s_add_u32 s88, s74, 157760\n" \
  "v_mov_b32 v59, s88\n" \
  "v_lshrrev_b32 v49, 2, v48\n" \
  "v_lshrrev_b32 v50, 4, v48\n" \
  "v_bitop3_b32 v50, v49, v50, 3 bitop3:0x28\n" \
  "v_lshl_add_u32 v8, v50, 3, s97\n" \
  "v_and_b32 v50, 3, v48\n" \
  "v_lshl_add_u32 v8, v50, 5, v8\n" \
  "v_and_b32 v49, 1, v49\n" \
  "v_lshl_add_u32 v8, v49, 8, v8\n" \
  "v_and_b32 v49, 8, v48\n" \
  "v_lshl_add_u32 v8, v49, 4, v8\n" \
  "v_lshrrev_b32 v49, 5, v48\n" \
  "v_add_u32 v10, s99, v1\n" \
  "s_lshr_b32 s88, s99, 2\n" \
  "s_add_u32 s88, s88, s74\n" \
  "s_add_u32 s88, s88, 139264\n" \
  "v_lshl_add_u32 v7, v49, 10, s88\n" \
  "v_and_b32 v49, 31, v48\n" \
  "v_lshl_add_u32 v7, v49, 3, v7\n" \
  "v_mov_b64 v[12:13], 0\n" \
  "v_mov_b64 v[14:15], 0\n" \
  "v_mov_b64 v[16:17], 0\n" \
  "v_mov_b64 v[18:19], 0\n" \
  "v_mov_b64 v[20:21], 0\n" \
  "v_mov_b64 v[22:23], 0\n" \
  "v_mov_b32 v34, 0xc9b26ca0\n" \
  "v_mov_b32 v35, 0xc9b26c86\n" \
  "s_mov_b32 s87, 0x40000\n" \
  "v_subrev_u32 v30, 64, v59\n" \
  "v_add_u32 v17, 0x10000, v28\n" \
  "v_xor_b32 v18, 128, v28\n" \
  "v_xor_b32 v19, 128, v17\n" \
  "ds_write_b32 v30, v12 offset:0\n" \
  "ds_write_b32 v30, v12 offset:32\n" \
  "ds_write_b32 v30, v12 offset:64\n" \
  "v_xor_b32 v20, 0, v29\n" \
  "v_xor_b32 v21, 32, v29\n" \
  "v_xor_b32 v22, 64, v29\n" \
  "v_xor_b32 v23, 96, v29\n" \
  "s_waitcnt vmcnt(12) lgkmcnt(0)\n" \
  "s_barrier\n" \
  "ds_read_b64_tr_b16 v[160:161], v20 offset:0\n" \
  "ds_read_b64_tr_b16 v[162:163], v20 offset:2048\n" \
  "ds_read_b64_tr_b16 v[168:169], v21 offset:0\n" \
  "ds_read_b64_tr_b16 v[170:171], v21 offset:2048\n" \
  "ds_read_b64_tr_b16 v[176:177], v22 offset:0\n" \
  "ds_read_b64_tr_b16 v[178:179], v22 offset:2048\n" \
  "ds_read_b64_tr_b16 v[184:185], v23 offset:0\n" \
  "ds_read_b64_tr_b16 v[186:187], v23 offset:2048\n" \
  "s_lshl_b32 s88, s73, 10\n" \
  "v_add_u32 v48, s88, v1\n" \
  "ds_read_b128 v[52:55], v48 offset:0\n" \
  "s_waitcnt lgkmcnt(0)\n" \
  "v_mov_b32 v11, 0\n" \
  "v_dot2c_f32_bf16 v11, v52, v52\n" \
  "v_dot2c_f32_bf16 v11, v53, v53\n" \
  "v_dot2c_f32_bf16 v11, v54, v54\n" \
  "v_dot2c_f32_bf16 v11, v55, v55\n" \
  "s_nop 0\n" \
  "s_nop 1\n" \
  "v_max_u32_dpp v11, v11, v11 row_ror:1 row_mask:0xf bank_mask:0xf\n" \
  "s_nop 1\n" \
  "v_max_u32_dpp v11, v11, v11 row_ror:2 row_mask:0xf bank_mask:0xf\n" \
  "s_nop 1\n" \
  "v_max_u32_dpp v11, v11, v11 row_ror:4 row_mask:0xf bank_mask:0xf\n" \
  "s_nop 1\n" \
  "v_max_u32_dpp v11, v11, v11 row_ror:8 row_mask:0xf bank_mask:0xf\n" \
  "v_mov_b32 v31, v11\n" \
  "s_nop 1\n" \
  "v_permlane16_swap_b32 v11, v31\n" \
  "v_max_u32 v11, v11, v31\n" \
  "v_mov_b32 v31, v11\n" \
  "s_nop 1\n" \
  "v_permlane32_swap_b32 v11, v31\n" \
  "v_max_u32 v11, v11, v31\n" \
  "s_mov_b64 exec, 1\n" \
  "ds_max_u32 v59, v11\n" \
  "s_mov_b64 exec, -1\n" \
  "s_waitcnt vmcnt(8) lgkmcnt(0)\n" \
  "s_barrier\n" \
  "ds_read_b64_tr_b16 v[164:165], v20 offset:4096\n" \
  "ds_read_b64_tr_b16 v[166:167], v20 offset:6144\n" \
  "ds_read_b64_tr_b16 v[172:173], v21 offset:4096\n" \
  "ds_read_b64_tr_b16 v[174:175], v21 offset:6144\n" \
  "ds_read_b64_tr_b16 v[180:181], v22 offset:4096\n" \
  "ds_read_b64_tr_b16 v[182:183], v22 offset:6144\n" \
  "ds_read_b64_tr_b16 v[188:189], v23 offset:4096\n" \
  "ds_read_b64_tr_b16 v[190:191], v23 offset:6144\n" \
  "s_add_u32 m0, s19, 0\n" \
  "s_nop 0\n" \
  "global_load_lds_dwordx4 v24, s[4:5] offset:0\n" \
  "global_load_lds_dwordx4 v25, s[4:5] offset:1024\n" \
  "global_load_lds_dwordx4 v26, s[4:5] offset:2048\n" \
  "global_load_lds_dwordx4 v27, s[4:5] offset:3072\n" \
  "s_add_u32 s4, s4, 0x100000\n" \
  "s_addc_u32 s5, s5, 0\n" \
  "v_mfma_f32_16x16x32_bf16 v[64:67], v[160:163], v[160:163], 0\n" \
  "v_mfma_f32_16x16x32_bf16 v[68:71], v[168:171], v[168:171], 0\n" \
  "v_mfma_f32_16x16x32_bf16 v[72:75], v[176:179], v[176:179], 0\n" \
  "v_mfma_f32_16x16x32_bf16 v[76:79], v[184:187], v[184:187], 0\n" \
  "s_waitcnt vmcnt(8) lgkmcnt(0)\n" \
  "s_barrier\n" \
  "ds_read_b64_tr_b16 v[32:33], v20 offset:8192\n" \
  "ds_read_b64_tr_b16 v[34:35], v20 offset:10240\n" \
  "ds_read_b64_tr_b16 v[40:41], v21 offset:8192\n" \
  "ds_read_b64_tr_b16 v[42:43], v21 offset:10240\n" \
  "ds_read_b64_tr_b16 v[48:49], v22 offset:8192\n" \
  "ds_read_b64_tr_b16 v[50:51], v22 offset:10240\n" \
  "ds_read_b64_tr_b16 v[56:57], v23 offset:8192\n" \
  "ds_read_b64_tr_b16 v[58:59], v23 offset:10240\n" \
  "s_add_u32 m0, s19, 4096\n" \
  "s_nop 0\n" \
  "global_load_lds_dwordx4 v24, s[4:5] offset:0\n" \
  "global_load_lds_dwordx4 v25, s[4:5] offset:1024\n" \
  "global_load_lds_dwordx4 v26, s[4:5] offset:2048\n" \
  "global_load_lds_dwordx4 v27, s[4:5] offset:3072\n" \
  "s_add_u32 s4, s4, 0x100000\n" \
  "s_addc_u32 s5, s5, 0\n" \
  "v_mfma_f32_16x16x32_bf16 v[64:67], v[164:167], v[164:167], v[64:67]\n" \
  "v_mfma_f32_16x16x32_bf16 v[68:71], v[172:175], v[172:175], v[68:71]\n" \
  "v_mfma_f32_16x16x32_bf16 v[72:75], v[180:183], v[180:183], v[72:75]\n" \
  "v_mfma_f32_16x16x32_bf16 v[76:79], v[188:191], v[188:191], v[76:79]\n" \
  "s_waitcnt vmcnt(8) lgkmcnt(0)\n" \
  "s_barrier\n" \
  "ds_read_b64_tr_b16 v[36:37], v20 offset:12288\n" \
  "ds_read_b64_tr_b16 v[38:39], v20 offset:14336\n" \
  "ds_read_b64_tr_b16 v[44:45], v21 offset:12288\n" \
  "ds_read_b64_tr_b16 v[46:47], v21 offset:14336\n" \
  "ds_read_b64_tr_b16 v[52:53], v22 offset:12288\n" \
  "ds_read_b64_tr_b16 v[54:55], v22 offset:14336\n" \
  "ds_read_b64_tr_b16 v[60:61], v23 offset:12288\n" \
  "ds_read_b64_tr_b16 v[62:63], v23 offset:14336\n" \
  "s_add_u32 m0, s19, 8192\n" \
  "s_nop 0\n" \
  "global_load_lds_dwordx4 v24, s[4:5] offset:0\n" \
  "global_load_lds_dwordx4 v25, s[4:5] offset:1024\n" \
  "global_load_lds_dwordx4 v26, s[4:5] offset:2048\n" \
  "global_load_lds_dwordx4 v27, s[4:5] offset:3072\n" \
  "s_add_u32 s4, s4, 0x100000\n" \
  "s_addc_u32 s5, s5, 0\n" \
  "v_mfma_f32_16x16x32_bf16 v[64:67], v[32:35], v[32:35], v[64:67]\n" \
  "v_mfma_f32_16x16x32_bf16 v[68:71], v[40:43], v[40:43], v[68:71]\n" \
  "v_mfma_f32_16x16x32_bf16 v[72:75], v[48:51], v[48:51], v[72:75]\n" \
  "v_mfma_f32_16x16x32_bf16 v[76:79], v[56:59], v[56:59], v[76:79]\n" \
  "s_waitcnt vmcnt(8) lgkmcnt(0)\n" \
  "s_barrier\n" \
  "s_add_u32 m0, s19, 12288\n" \
  "s_nop 0\n" \
  "global_load_lds_dwordx4 v24, s[4:5] offset:0\n" \
  "global_load_lds_dwordx4 v25, s[4:5] offset:1024\n" \
  "global_load_lds_dwordx4 v26, s[4:5] offset:2048\n" \
  "global_load_lds_dwordx4 v27, s[4:5] offset:3072\n" \
  "s_add_u32 s4, s4, 0x100000\n" \
  "s_addc_u32 s5, s5, 0\n" \
  "v_mfma_f32_16x16x32_bf16 v[64:67], v[36:39], v[36:39], v[64:67]\n" \
  "v_mfma_f32_16x16x32_bf16 v[68:71], v[44:47], v[44:47], v[68:71]\n" \
  "v_mfma_f32_16x16x32_bf16 v[72:75], v[52:55], v[52:55], v[72:75]\n" \
  "v_mfma_f32_16x16x32_bf16 v[76:79], v[60:63], v[60:63], v[76:79]\n" \
  "s_nop 7\n" \
  "v_max3_f32 v13, v64, v65, v66\n" \
  "v_max3_f32 v13, v13, v67, v68\n" \
  "v_max3_f32 v13, v13, v69, v70\n" \
  "v_max3_f32 v13, v13, v71, v72\n" \
  "v_max3_f32 v13, v13, v73, v74\n" \
  "v_max3_f32 v13, v13, v75, v76\n" \
  "v_max3_f32 v13, v13, v77, v78\n" \
  "v_max_f32 v13, v13, v79\n" \
  "s_nop 1\n" \
  "v_max_f32_dpp v13, v13, v13 row_ror:1 row_mask:0xf bank_mask:0xf\n" \
  "s_nop 1\n" \
  "v_max_f32_dpp v13, v13, v13 row_ror:2 row_mask:0xf bank_mask:0xf\n" \
  "s_nop 1\n" \
  "v_max_f32_dpp v13, v13, v13 row_ror:4 row_mask:0xf bank_mask:0xf\n" \
  "s_nop 1\n" \
  "v_max_f32_dpp v13, v13, v13 row_ror:8 row_mask:0xf bank_mask:0xf\n" \
  "v_mov_b32 v31, v13\n" \
  "s_nop 1\n" \
  "v_permlane16_swap_b32 v13, v31\n" \
  "v_max_f32 v13, v13, v31\n" \
  "v_mov_b32 v31, v13\n" \
  "s_nop 1\n" \
  "v_permlane32_swap_b32 v13, v31\n" \
  "v_max_f32 v13, v13, v31\n" \
  "s_mov_b64 exec, 1\n" \
  "ds_max_u32 v30, v13 offset:32\n" \
  "s_mov_b64 exec, -1\n" \
  "s_waitcnt vmcnt(8) lgkmcnt(0)\n" \
  "s_barrier\n" \
  "s_add_u32 m0, s19, 0\n" \
  "s_nop 0\n" \
  "global_load_lds_dwordx4 v24, s[6:7] offset:0\n" \
  "global_load_lds_dwordx4 v25, s[6:7] offset:1024\n" \
  "global_load_lds_dwordx4 v26, s[6:7] offset:2048\n" \
  "global_load_lds_dwordx4 v27, s[6:7] offset:3072\n" \
  "s_add_u32 s6, s6, 0x100000\n" \
  "s_addc_u32 s7, s7, 0\n" \
  "s_waitcnt vmcnt(8) lgkmcnt(0)\n" \
  "s_barrier\n" \
  "s_add_u32 m0, s19, 4096\n" \
  "s_nop 0\n" \
  "global_load_lds_dwordx4 v24, s[6:7] offset:0\n" \
  "global_load_lds_dwordx4 v25, s[6:7] offset:1024\n" \
  "global_load_lds_dwordx4 v26, s[6:7] offset:2048\n" \
  "global_load_lds_dwordx4 v27, s[6:7] offset:3072\n" \
  "s_add_u32 s6, s6, 0x100000\n" \
  "s_addc_u32 s7, s7, 0\n" \
  "s_waitcnt vmcnt(8) lgkmcnt(0)\n" \
  "s_barrier\n" \
  "s_add_u32 m0, s19, 8192\n" \
  "s_nop 0\n" \
  "global_load_lds_dwordx4 v24, s[6:7] offset:0\n" \
  "global_load_lds_dwordx4 v25, s[6:7] offset:1024\n" \
  "global_load_lds_dwordx4 v26, s[6:7] offset:2048\n" \
  "global_load_lds_dwordx4 v27, s[6:7] offset:3072\n" \
  "s_add_u32 s6, s6, 0x100000\n" \
  "s_addc_u32 s7, s7, 0\n" \
  "s_waitcnt vmcnt(8) lgkmcnt(0)\n" \
  "s_barrier\n" \
  "ds_read_b64_tr_b16 v[64:65], v28 offset:0\n" \
  "ds_read_b64_tr_b16 v[66:67], v28 offset:512\n" \
  "ds_read_b64_tr_b16 v[68:69], v28 offset:32768\n" \
  "ds_read_b64_tr_b16 v[70:71], v28 offset:33280\n" \
  "ds_read_b64_tr_b16 v[72:73], v17 offset:0\n" \
  "ds_read_b64_tr_b16 v[74:75], v17 offset:512\n" \
  "ds_read_b64_tr_b16 v[76:77], v17 offset:32768\n" \
  "ds_read_b64_tr_b16 v[78:79], v17 offset:33280\n" \
  "ds_read_b64_tr_b16 v[192:193], v18 offset:0\n" \
  "ds_read_b64_tr_b16 v[194:195], v18 offset:512\n" \
  "ds_read_b64_tr_b16 v[196:197], v18 offset:32768\n" \
  "ds_read_b64_tr_b16 v[198:199], v18 offset:33280\n" \
  "ds_read_b64_tr_b16 v[200:201], v19 offset:0\n" \
  "ds_read_b64_tr_b16 v[202:203], v19 offset:512\n" \
  "ds_read_b64_tr_b16 v[204:205], v19 offset:32768\n" \
  "ds_read_b64_tr_b16 v[206:207], v19 offset:33280\n" \
  "s_add_u32 m0, s19, 12288\n" \
  "s_nop 0\n" \
  "global_load_lds_dwordx4 v24, s[6:7] offset:0\n" \
  "global_load_lds_dwordx4 v25, s[6:7] offset:1024\n" \
  "global_load_lds_dwordx4 v26, s[6:7] offset:2048\n" \
  "global_load_lds_dwordx4 v27, s[6:7] offset:3072\n" \
  "s_add_u32 s6, s6, 0x100000\n" \
  "s_addc_u32 s7, s7, 0\n" \
  "s_waitcnt vmcnt(8) lgkmcnt(0)\n" \
  "s_barrier\n" \
  "ds_read_b64_tr_b16 v[80:81], v28 offset:4096\n" \
  "ds_read_b64_tr_b16 v[82:83], v28 offset:4608\n" \
  "ds_read_b64_tr_b16 v[84:85], v28 offset:36864\n" \
  "ds_read_b64_tr_b16 v[86:87], v28 offset:37376\n" \
  "ds_read_b64_tr_b16 v[88:89], v17 offset:4096\n" \
  "ds_read_b64_tr_b16 v[90:91], v17 offset:4608\n" \
  "ds_read_b64_tr_b16 v[92:93], v17 offset:36864\n" \
  "ds_read_b64_tr_b16 v[94:95], v17 offset:37376\n" \
  "ds_read_b64_tr_b16 v[208:209], v18 offset:4096\n" \
  "ds_read_b64_tr_b16 v[210:211], v18 offset:4608\n" \
  "ds_read_b64_tr_b16 v[212:213], v18 offset:36864\n" \
  "ds_read_b64_tr_b16 v[214:215], v18 offset:37376\n" \
  "ds_read_b64_tr_b16 v[216:217], v19 offset:4096\n" \
  "ds_read_b64_tr_b16 v[218:219], v19 offset:4608\n" \
  "ds_read_b64_tr_b16 v[220:221], v19 offset:36864\n" \
  "ds_read_b64_tr_b16 v[222:223], v19 offset:37376\n" \
  "s_add_u32 m0, s19, 0\n" \
  "s_nop 0\n" \
  "global_load_lds_dwordx4 v24, s[8:9] offset:0\n" \
  "global_load_lds_dwordx4 v25, s[8:9] offset:1024\n" \
  "global_load_lds_dwordx4 v26, s[8:9] offset:2048\n" \
  "global_load_lds_dwordx4 v27, s[8:9] offset:3072\n" \
  "s_add_u32 s8, s8, 0x100000\n" \
  "s_addc_u32 s9, s9, 0\n" \
  "s_waitcnt vmcnt(8) lgkmcnt(0)\n" \
  "s_barrier\n" \
  "ds_read_b64_tr_b16 v[96:97], v28 offset:8192\n" \
  "ds_read_b64_tr_b16 v[98:99], v28 offset:8704\n" \
  "ds_read_b64_tr_b16 v[100:101], v28 offset:40960\n" \
  "ds_read_b64_tr_b16 v[102:103], v28 offset:41472\n" \
  "ds_read_b64_tr_b16 v[104:105], v17 offset:8192\n" \
  "ds_read_b64_tr_b16 v[106:107], v17 offset:8704\n" \
  "ds_read_b64_tr_b16 v[108:109], v17 offset:40960\n" \
  "ds_read_b64_tr_b16 v[110:111], v17 offset:41472\n" \
  "ds_read_b64_tr_b16 v[224:225], v18 offset:8192\n" \
  "ds_read_b64_tr_b16 v[226:227], v18 offset:8704\n" \
  "ds_read_b64_tr_b16 v[228:229], v18 offset:40960\n" \
  "ds_read_b64_tr_b16 v[230:231], v18 offset:41472\n" \
  "ds_read_b64_tr_b16 v[232:233], v19 offset:8192\n" \
  "ds_read_b64_tr_b16 v[234:235], v19 offset:8704\n" \
  "ds_read_b64_tr_b16 v[236:237], v19 offset:40960\n" \
  "ds_read_b64_tr_b16 v[238:239], v19 offset:41472\n" \
  "s_add_u32 m0, s19, 4096\n" \
  "s_nop 0\n" \
  "global_load_lds_dwordx4 v24, s[8:9] offset:0\n" \
  "global_load_lds_dwordx4 v25, s[8:9] offset:1024\n" \
  "global_load_lds_dwordx4 v26, s[8:9] offset:2048\n" \
  "global_load_lds_dwordx4 v27, s[8:9] offset:3072\n" \
  "s_add_u32 s8, s8, 0x100000\n" \
  "s_addc_u32 s9, s9, 0\n" \
  "s_waitcnt vmcnt(8) lgkmcnt(0)\n" \
  "s_barrier\n" \
  "ds_read_b64_tr_b16 v[112:113], v28 offset:12288\n" \
  "ds_read_b64_tr_b16 v[114:115], v28 offset:12800\n" \
  "ds_read_b64_tr_b16 v[116:117], v28 offset:45056\n" \
  "ds_read_b64_tr_b16 v[118:119], v28 offset:45568\n" \
  "ds_read_b64_tr_b16 v[120:121], v17 offset:12288\n" \
  "ds_read_b64_tr_b16 v[122:123], v17 offset:12800\n" \
  "ds_read_b64_tr_b16 v[124:125], v17 offset:45056\n" \
  "ds_read_b64_tr_b16 v[126:127], v17 offset:45568\n" \
  "ds_read_b64_tr_b16 v[240:241], v18 offset:12288\n" \
  "ds_read_b64_tr_b16 v[242:243], v18 offset:12800\n" \
  "ds_read_b64_tr_b16 v[244:245], v18 offset:45056\n" \
  "ds_read_b64_tr_b16 v[246:247], v18 offset:45568\n" \
  "ds_read_b64_tr_b16 v[248:249], v19 offset:12288\n" \
  "ds_read_b64_tr_b16 v[250:251], v19 offset:12800\n" \
  "ds_read_b64_tr_b16 v[252:253], v19 offset:45056\n" \
  "ds_read_b64_tr_b16 v[254:255], v19 offset:45568\n" \
  "s_add_u32 m0, s19, 8192\n" \
  "s_nop 0\n" \
  "global_load_lds_dwordx4 v24, s[8:9] offset:0\n" \
  "global_load_lds_dwordx4 v25, s[8:9] offset:1024\n" \
  "global_load_lds_dwordx4 v26, s[8:9] offset:2048\n" \
  "global_load_lds_dwordx4 v27, s[8:9] offset:3072\n" \
  "s_add_u32 s8, s8, 0x100000\n" \
  "s_addc_u32 s9, s9, 0\n" \
  "s_waitcnt vmcnt(8) lgkmcnt(0)\n" \
  "s_barrier\n" \
  "ds_read_b64_tr_b16 v[128:129], v28 offset:0\n" \
  "ds_read_b64_tr_b16 v[130:131], v28 offset:512\n" \
  "ds_read_b64_tr_b16 v[132:133], v28 offset:32768\n" \
  "ds_read_b64_tr_b16 v[134:135], v28 offset:33280\n" \
  "ds_read_b64_tr_b16 v[136:137], v17 offset:0\n" \
  "ds_read_b64_tr_b16 v[138:139], v17 offset:512\n" \
  "ds_read_b64_tr_b16 v[140:141], v17 offset:32768\n" \
  "ds_read_b64_tr_b16 v[142:143], v17 offset:33280\n" \
  "s_add_u32 m0, s19, 12288\n" \
  "s_nop 0\n" \
  "global_load_lds_dwordx4 v24, s[8:9] offset:0\n" \
  "global_load_lds_dwordx4 v25, s[8:9] offset:1024\n" \
  "global_load_lds_dwordx4 v26, s[8:9] offset:2048\n" \
  "global_load_lds_dwordx4 v27, s[8:9] offset:3072\n" \
  "s_add_u32 s8, s8, 0x100000\n" \
  "s_addc_u32 s9, s9, 0\n" \
  "s_waitcnt vmcnt(8) lgkmcnt(0)\n" \
  "s_barrier\n" \
  "ds_write_b128 v6, v[160:163] offset:0\n" \
  "ds_write_b128 v6, v[164:167] offset:1024\n" \
  "ds_write_b128 v6, v[168:171] offset:2048\n" \
  "ds_write_b128 v6, v[172:175] offset:3072\n" \
  "ds_read_b64_tr_b16 v[144:145], v28 offset:4096\n" \
  "ds_read_b64_tr_b16 v[146:147], v28 offset:4608\n" \
  "ds_read_b64_tr_b16 v[148:149], v28 offset:36864\n" \
  "ds_read_b64_tr_b16 v[150:151], v28 offset:37376\n" \
  "ds_read_b64_tr_b16 v[152:153], v17 offset:4096\n" \
  "ds_read_b64_tr_b16 v[154:155], v17 offset:4608\n" \
  "ds_read_b64_tr_b16 v[156:157], v17 offset:36864\n" \
  "ds_read_b64_tr_b16 v[158:159], v17 offset:37376\n" \
  "s_waitcnt vmcnt(4) lgkmcnt(0)\n" \
  "s_barrier\n" \
  "ds_write_b128 v6, v[176:179] offset:4096\n" \
  "ds_write_b128 v6, v[180:183] offset:5120\n" \
  "ds_write_b128 v6, v[184:187] offset:6144\n" \
  "ds_write_b128 v6, v[188:191] offset:7168\n" \
  "ds_read_b64_tr_b16 v[160:161], v28 offset:8192\n" \
  "ds_read_b64_tr_b16 v[162:163], v28 offset:8704\n" \
  "ds_read_b64_tr_b16 v[164:165], v28 offset:40960\n" \
  "ds_read_b64_tr_b16 v[166:167], v28 offset:41472\n" \
  "ds_read_b64_tr_b16 v[168:169], v17 offset:8192\n" \
  "ds_read_b64_tr_b16 v[170:171], v17 offset:8704\n" \
  "ds_read_b64_tr_b16 v[172:173], v17 offset:40960\n" \
  "ds_read_b64_tr_b16 v[174:175], v17 offset:41472\n" \
  "s_waitcnt vmcnt(0) lgkmcnt(0)\n" \
  "s_barrier\n" \
  "ds_write_b128 v6, v[32:35] offset:8192\n" \
  "ds_write_b128 v6, v[36:39] offset:9216\n" \
  "ds_write_b128 v6, v[40:43] offset:10240\n" \
  "ds_write_b128 v6, v[44:47] offset:11264\n" \
  "ds_read_b64_tr_b16 v[176:177], v28 offset:12288\n" \
  "ds_read_b64_tr_b16 v[178:179], v28 offset:12800\n" \
  "ds_read_b64_tr_b16 v[180:181], v28 offset:45056\n" \
  "ds_read_b64_tr_b16 v[182:183], v28 offset:45568\n" \
  "ds_read_b64_tr_b16 v[184:185], v17 offset:12288\n" \
  "ds_read_b64_tr_b16 v[186:187], v17 offset:12800\n" \
  "ds_read_b64_tr_b16 v[188:189], v17 offset:45056\n" \
  "ds_read_b64_tr_b16 v[190:191], v17 offset:45568\n" \
  "ds_read_b32 v31, v30 offset:0\n" \
  "s_waitcnt lgkmcnt(0)\n" \
  "s_barrier\n" \
  "ds_write_b128 v6, v[48:51] offset:12288\n" \
  "ds_write_b128 v6, v[52:55] offset:13312\n" \
  "ds_write_b128 v6, v[56:59] offset:14336\n" \
  "ds_write_b128 v6, v[60:63] offset:15360\n" \
  "v_mul_f32 v31, 0x3f8e353f, v31\n" \
  "v_mul_f32 v30, 0x44870000, v31\n" \
  "v_mov_b64 v[12:13], 0\n" \
  "v_mov_b64 v[14:15], 0\n" \
  "v_mov_b64 v[16:17], 0\n" \
  "v_mov_b64 v[18:19], 0\n" \
  "v_mov_b64 v[20:21], 0\n" \
  "v_mov_b64 v[22:23], 0\n" \
  "v_readfirstlane_b32 s72, v31\n" \
  "v_readfirstlane_b32 s86, v30\n" \
  "s_lshr_b32 s86, s86, 23\n" \
  "s_addk_i32 s86, 0xff83\n" \
  "s_ashr_i32 s86, s86, 1\n" \
  "s_max_i32 s86, s86, -20\n" \
  "s_min_i32 s86, s86, 100\n" \
  "s_mov_b32 s82, s86\n" \
  "s_lshr_b32 s83, s72, 23\n" \
  "s_sub_i32 s83, s83, 243\n" \
  "s_waitcnt lgkmcnt(0)\n" \
  "s_add_u32 s88, s74, 157760\n" \
  "v_mov_b32 v59, s88\n" \
  "v_mov_b32 v34, 0xc9b26ca0\n" \
  "v_mov_b32 v35, 0xc9b26c86\n" \
  ".p2align 6\n" \
  "s_nop 0\n" \
  "s_nop 0\n" \
  "s_nop 0\n" \
  ".LCA_TOP_%=:\n" \
  "s_waitcnt lgkmcnt(0)\n" \
  "s_barrier\n" \
  "ds_read_b128 v[40:43], v1 offset:0\n" \
  "ds_read_b128 v[44:47], v1 offset:1024\n" \
  "ds_read_b128 v[48:51], v1 offset:2048\n" \
  "ds_read_b128 v[52:55], v1 offset:3072\n" \
  "ds_read_b128 v[56:59], v1 offset:4096\n" \
  "ds_read_b128 v[60:63], v1 offset:5120\n" \
  "s_cmp_lg_u32 s73, 0\n" \
  "s_cbranch_scc1 .LCA_NOZ_%=\n" \
  "s_xor_b32 s88, s60, 0x10000\n" \
  "s_add_u32 s88, s88, s61\n" \
  "buffer_store_dwordx2 v[34:35], v5, s[40:43], s88 offen sc1\n" \
  ".LCA_NOZ_%=:\n" \
  "s_waitcnt lgkmcnt(5)\n" \
  "v_smfmac_f32_16x16x64_bf16 v[20:23], v[40:43], v[192:199], v0\n" \
  "v_smfmac_f32_16x16x64_bf16 v[12:15], v[40:43], v[64:71], v0\n" \
  "v_smfmac_f32_16x16x64_bf16 v[16:19], v[40:43], v[128:135], v0\n" \
  "ds_read_b128 v[40:43], v1 offset:6144\n" \
  "s_waitcnt lgkmcnt(5)\n" \
  "v_smfmac_f32_16x16x64_bf16 v[20:23], v[44:47], v[200:207], v0\n" \
  "v_smfmac_f32_16x16x64_bf16 v[12:15], v[44:47], v[72:79], v0\n" \
  "v_smfmac_f32_16x16x64_bf16 v[16:19], v[44:47], v[136:143], v0\n" \
  "ds_read_b128 v[44:47], v1 offset:7168\n" \
  "s_waitcnt lgkmcnt(5)\n" \
  "v_smfmac_f32_16x16x64_bf16 v[20:23], v[48:51], v[208:215], v0\n" \
  "v_smfmac_f32_16x16x64_bf16 v[12:15], v[48:51], v[80:87], v0\n" \
  "v_smfmac_f32_16x16x64_bf16 v[16:19], v[48:51], v[144:151], v0\n" \
  "ds_read_b128 v[24:27], v1 offset:10240\n" \
  "ds_read_b128 v[28:31], v1 offset:14336\n" \
  "s_waitcnt lgkmcnt(6)\n" \
  "v_smfmac_f32_16x16x64_bf16 v[20:23], v[52:55], v[216:223], v0\n" \
  "v_smfmac_f32_16x16x64_bf16 v[12:15], v[52:55], v[88:95], v0\n" \
  "v_smfmac_f32_16x16x64_bf16 v[16:19], v[52:55], v[152:159], v0\n" \
  "ds_read_b128 v[32:35], v1 offset:11264\n" \
  "ds_read_b128 v[36:39], v1 offset:15360\n" \
  "s_waitcnt lgkmcnt(7)\n" \
  "v_smfmac_f32_16x16x64_bf16 v[20:23], v[56:59], v[224:231], v0\n" \
  "v_smfmac_f32_16x16x64_bf16 v[12:15], v[56:59], v[96:103], v0\n" \
  "v_smfmac_f32_16x16x64_bf16 v[16:19], v[56:59], v[160:167], v0\n" \
  "ds_read_b128 v[56:59], v1 offset:12288\n" \
  "s_waitcnt lgkmcnt(7)\n" \
  "v_smfmac_f32_16x16x64_bf16 v[20:23], v[60:63], v[232:239], v0\n" \
  "v_smfmac_f32_16x16x64_bf16 v[12:15], v[60:63], v[104:111], v0\n" \
  "v_smfmac_f32_16x16x64_bf16 v[16:19], v[60:63], v[168:175], v0\n" \
  "ds_read_b128 v[60:63], v1 offset:16384\n" \
  "s_waitcnt lgkmcnt(7)\n" \
  "v_smfmac_f32_16x16x64_bf16 v[20:23], v[40:43], v[240:247], v0\n" \
  "v_smfmac_f32_16x16x64_bf16 v[12:15], v[40:43], v[112:119], v0\n" \
  "v_smfmac_f32_16x16x64_bf16 v[16:19], v[40:43], v[176:183], v0\n" \
  "s_waitcnt lgkmcnt(6)\n" \
  "v_smfmac_f32_16x16x64_bf16 v[20:23], v[44:47], v[248:255], v0\n" \
  "v_smfmac_f32_16x16x64_bf16 v[12:15], v[44:47], v[120:127], v0\n" \
  "v_smfmac_f32_16x16x64_bf16 v[16:19], v[44:47], v[184:191], v0\n" \
  "s_cmp_eq_u32 s71, 0\n" \
  "v_permlane32_swap_b32 v20, v22\n" \
  "v_permlane32_swap_b32 v21, v23\n" \
  "v_pk_add_f32 v[20:21], v[20:21], v[22:23]\n" \
  "s_mov_b64 vcc, s[76:77]\n" \
  "v_permlane32_swap_b32 v12, v14\n" \
  "v_permlane32_swap_b32 v13, v15\n" \
  "v_cvt_pk_bf16_f32 v20, v20, v21\n" \
  "v_pk_add_f32 v[12:13], v[12:13], v[14:15]\n" \
  "v_permlane32_swap_b32 v16, v18\n" \
  "v_permlane32_swap_b32 v17, v19\n" \
  "ds_write_b32 v8, v20 offset:2048\n" \
  "v_cvt_pk_bf16_f32 v12, v12, v13\n" \
  "v_pk_add_f32 v[16:17], v[16:17], v[18:19]\n" \
  "s_nop 0\n" \
  "v_cvt_pk_bf16_f32 v13, v16, v17\n" \
  "ds_write_b64 v8, v[12:13]\n" \
  "s_setprio 1\n" \
  "s_waitcnt lgkmcnt(0)\n" \
  "s_barrier\n" \
  "s_cbranch_scc1 .LCA_LAST_%=\n" \
  "ds_read_b64_tr_b16 v[40:41], v2 offset:0\n" \
  "ds_read_b64_tr_b16 v[42:43], v3 offset:0\n" \
  "ds_read_b64_tr_b16 v[44:45], v2 offset:1024\n" \
  "ds_read_b64_tr_b16 v[46:47], v3 offset:1024\n" \
  "ds_read_b64_tr_b16 v[48:49], v2 offset:2048\n" \
  "ds_read_b64_tr_b16 v[50:51], v3 offset:2048\n" \
  "ds_read_b64_tr_b16 v[52:53], v2 offset:3072\n" \
  "ds_read_b64_tr_b16 v[54:55], v3 offset:3072\n" \
  "ds_read_b32 v11, v1 offset:26688\n" \
  "s_waitcnt lgkmcnt(7)\n" \
  "v_mfma_f32_16x16x32_bf16 v[12:15], v[24:27], v[40:43], 0\n" \
  "v_mfma_f32_16x16x32_bf16 v[16:19], v[28:31], v[40:43], 0\n" \
  "ds_read_b128 v[24:27], v1 offset:13312\n" \
  "ds_read_b128 v[28:31], v1 offset:17408\n" \
  "v_mfma_f32_16x16x32_bf16 v[20:23], v[40:43], v[40:43], 0\n" \
  "s_waitcnt lgkmcnt(7)\n" \
  "v_mfma_f32_16x16x32_bf16 v[12:15], v[32:35], v[44:47], v[12:15]\n" \
  "v_mfma_f32_16x16x32_bf16 v[16:19], v[36:39], v[44:47], v[16:19]\n" \
  "v_mfma_f32_16x16x32_bf16 v[20:23], v[44:47], v[44:47], v[20:23]\n" \
  "s_waitcnt lgkmcnt(2)\n" \
  "v_mfma_f32_16x16x32_bf16 v[12:15], v[56:59], v[48:51], v[12:15]\n" \
  "v_readfirstlane_b32 s86, v11\n" \
  "v_mfma_f32_16x16x32_bf16 v[16:19], v[60:63], v[48:51], v[16:19]\n" \
  "ds_read_b128 v[32:35], v6 offset:8192\n" \
  "ds_read_b128 v[36:39], v6 offset:9216\n" \
  "v_mfma_f32_16x16x32_bf16 v[20:23], v[48:51], v[48:51], v[20:23]\n" \
  "s_lshr_b32 s86, s86, 23\n" \
  "s_add_i32 s86, s86, s83\n" \
  "s_ashr_i32 s86, s86, 1\n" \
  "ds_read_b128 v[48:51], v10 offset:18432\n" \
  "v_mfma_f32_16x16x32_bf16 v[20:23], v[52:55], v[52:55], v[20:23]\n" \
  "s_max_i32 s86, s86, s82\n" \
  "s_min_i32 s86, s86, 100\n" \
  "s_lshl_b32 s86, s86, 23\n" \
  "s_sub_i32 s84, 0xc8800000, s86\n" \
  "s_waitcnt lgkmcnt(3)\n" \
  "v_mfma_f32_16x16x32_bf16 v[12:15], v[24:27], v[52:55], v[12:15]\n" \
  "v_mfma_f32_16x16x32_bf16 v[16:19], v[28:31], v[52:55], v[16:19]\n" \
  "ds_read_b128 v[52:55], v10 offset:22528\n" \
  "ds_read_b128 v[56:59], v6 offset:0\n" \
  "ds_read_b128 v[60:63], v6 offset:1024\n" \
  "ds_read_b128 v[24:27], v6 offset:2048\n" \
  "ds_read_b128 v[28:31], v6 offset:3072\n" \
  "v_cndmask_b32_dpp v20, v23, v22, vcc quad_perm:[2,3,0,1] row_mask:0xf bank_mask:0xf\n" \
  "v_mov_b32 v41, 0\n" \
  "v_mov_b32 v42, 0\n" \
  "v_cndmask_b32_dpp v12, v16, v12, vcc quad_perm:[2,3,0,1] row_mask:0xf bank_mask:0xf\n" \
  "v_cndmask_b32_dpp v13, v17, v13, vcc quad_perm:[2,3,0,1] row_mask:0xf bank_mask:0xf\n" \
  "v_cndmask_b32_dpp v14, v18, v14, vcc quad_perm:[2,3,0,1] row_mask:0xf bank_mask:0xf\n" \
  "v_cndmask_b32_dpp v15, v19, v15, vcc quad_perm:[2,3,0,1] row_mask:0xf bank_mask:0xf\n" \
  "v_maximum3_f32 v16, v20, v12, v13\n" \
  "v_maximum3_f32 v16, v16, v14, v15\n" \
  "s_nop 1\n" \
  "v_max_f32_dpp v16, v16, v16 quad_perm:[2,3,0,1] row_mask:0xf bank_mask:0xf\n" \
  "v_mov_b32 v17, v16\n" \
  "s_nop 1\n" \
  "v_permlane16_swap_b32 v16, v17\n" \
  "v_max_f32 v16, v16, v17\n" \
  "v_mov_b32 v17, v16\n" \
  "s_nop 1\n" \
  "v_permlane32_swap_b32 v16, v17\n" \
  "v_max_f32 v16, v16, v17\n" \
  "v_mul_f32 v16, s78, v16\n" \
  "v_pk_fma_f32 v[12:13], v[12:13], s[78:79], v[16:17] op_sel_hi:[1,0,0] neg_lo:[0,0,1] neg_hi:[0,0,1]\n" \
  "v_pk_fma_f32 v[14:15], v[14:15], s[78:79], v[16:17] op_sel_hi:[1,0,0] neg_lo:[0,0,1] neg_hi:[0,0,1]\n" \
  "v_fma_f32 v20, v20, s78, -v16\n" \
  "v_exp_f32 v12, v12\n" \
  "v_exp_f32 v13, v13\n" \
  "v_exp_f32 v14, v14\n" \
  "v_exp_f32 v15, v15\n" \
  "v_exp_f32 v20, v20\n" \
  "s_waitcnt lgkmcnt(4)\n" \
  "v_cvt_pk_bf16_f32 v44, v12, v13\n" \
  "v_cvt_pk_bf16_f32 v45, v14, v15\n" \
  "v_pk_add_f32 v[8:9], v[12:13], v[14:15]\n" \
  "v_mov_b32_dpp v46, v44 quad_perm:[2,3,0,1] row_mask:0xf bank_mask:0xf\n" \
  "v_mov_b32_dpp v47, v45 quad_perm:[2,3,0,1] row_mask:0xf bank_mask:0xf\n" \
  "v_add_f32 v10, v8, v20\n" \
  "v_add_f32 v10, v10, v9\n" \
  "v_mfma_f32_16x16x32_bf16 v[12:15], v[48:51], v[44:47], 0\n" \
  "v_mov_b32_dpp v21, v20 quad_perm:[2,3,0,1] row_mask:0xf bank_mask:0xf\n" \
  "v_add_f32_dpp v10, v10, v10 quad_perm:[2,3,0,1] row_mask:0xf bank_mask:0xf\n" \
  "v_mov_b32 v11, v10\n" \
  "v_mfma_f32_16x16x32_bf16 v[16:19], v[52:55], v[44:47], 0\n" \
  "s_setprio 3\n" \
  "s_barrier\n" \
  "ds_read_b64 v[22:23], v7 offset:0\n" \
  "s_nop 0\n" \
  "v_permlane16_swap_b32 v10, v11\n" \
  "v_add_f32 v10, v10, v11\n" \
  "v_mov_b32 v11, v10\n" \
  "v_mov_b64 v[8:9], s[92:93]\n" \
  "s_nop 0\n" \
  "v_permlane32_swap_b32 v10, v11\n" \
  "v_add_f32 v10, v10, v11\n" \
  "v_rcp_f32 v10, v10\n" \
  "s_nop 0\n" \
  "v_mul_f32 v10, s84, v10\n" \
  "v_pk_mul_f32 v[20:21], v[20:21], v[10:11] op_sel_hi:[1,0]\n" \
  "v_pk_mul_f32 v[12:13], v[12:13], v[10:11] op_sel_hi:[1,0]\n" \
  "v_pk_mul_f32 v[14:15], v[14:15], v[10:11] op_sel_hi:[1,0]\n" \
  "v_cvt_pk_bf16_f32 v40, v20, v21\n" \
  "v_pk_mul_f32 v[16:17], v[16:17], v[10:11] op_sel_hi:[1,0]\n" \
  "v_cvt_pk_bf16_f32 v43, v20, v21\n" \
  "v_pk_mul_f32 v[18:19], v[18:19], v[10:11] op_sel_hi:[1,0]\n" \
  "s_waitcnt lgkmcnt(0)\n" \
  "v_mfma_f32_16x16x16_bf16 v[12:15], v[22:23], v[40:41], v[12:15]\n" \
  "v_mfma_f32_16x16x16_bf16 v[16:19], v[22:23], v[42:43], v[16:19]\n" \
  "v_mov_b64 v[10:11], s[92:93]\n" \
  "ds_read_b128 v[48:51], v6 offset:10240\n" \
  "ds_read_b128 v[52:55], v6 offset:11264\n" \
  "v_mov_b64 v[20:21], s[92:93]\n" \
  "v_mov_b64 v[22:23], s[92:93]\n" \
  "s_nop 1\n" \
  "v_cvt_pk_bf16_f32 v40, v12, v13\n" \
  "v_cvt_pk_bf16_f32 v41, v14, v15\n" \
  "v_cvt_pk_bf16_f32 v42, v16, v17\n" \
  "ds_write_b64 v4, v[40:41]\n" \
  "v_cvt_pk_bf16_f32 v43, v18, v19\n" \
  "ds_write_b64 v4, v[42:43] offset:1024\n" \
  "v_mov_b64 v[12:13], s[92:93]\n" \
  "v_mov_b64 v[14:15], s[92:93]\n" \
  "v_mov_b64 v[16:17], s[92:93]\n" \
  "v_mov_b64 v[18:19], s[92:93]\n" \
  "s_setprio 0\n" \
  "s_waitcnt lgkmcnt(0)\n" \
  "s_barrier\n" \
  "ds_read_b128 v[40:43], v1 offset:8192\n" \
  "ds_read_b128 v[44:47], v1 offset:9216\n" \
  "s_waitcnt lgkmcnt(1)\n" \
  "v_smfmac_f32_16x16x64_bf16 v[12:15], v[40:43], v[56:63], v0\n" \
  "ds_read_b128 v[56:59], v6 offset:4096\n" \
  "ds_read_b128 v[60:63], v6 offset:5120\n" \
  "v_smfmac_f32_16x16x64_bf16 v[16:19], v[40:43], v[24:31], v0\n" \
  "ds_read_b128 v[24:27], v6 offset:6144\n" \
  "ds_read_b128 v[28:31], v6 offset:7168\n" \
  "s_waitcnt lgkmcnt(4)\n" \
  "v_smfmac_f32_16x16x64_bf16 v[12:15], v[44:47], v[32:39], v0\n" \
  "ds_read_b128 v[32:35], v6 offset:12288\n" \
  "ds_read_b128 v[36:39], v6 offset:13312\n" \
  "v_smfmac_f32_16x16x64_bf16 v[16:19], v[44:47], v[48:55], v0\n" \
  "ds_read_b128 v[48:51], v6 offset:14336\n" \
  "ds_read_b128 v[52:55], v6 offset:15360\n" \
  "s_nop 5\n" \
  "v_permlane32_swap_b32 v12, v17\n" \
  "v_permlane32_swap_b32 v13, v16\n" \
  "v_permlane32_swap_b32 v14, v19\n" \
  "v_permlane32_swap_b32 v15, v18\n" \
  "v_pk_add_f32 v[12:13], v[12:13], v[16:17] op_sel:[0,1] op_sel_hi:[1,0]\n" \
  "v_pk_add_f32 v[14:15], v[14:15], v[18:19] op_sel:[0,1] op_sel_hi:[1,0]\n" \
  "buffer_atomic_add_x2 v[12:13], v5, s[40:43], s56 offen\n" \
  "buffer_atomic_add_x2 v[14:15], v5, s[40:43], s57 offen\n" \
  "s_waitcnt lgkmcnt(6)\n" \
  "v_smfmac_f32_16x16x64_bf16 v[20:23], v[40:43], v[56:63], v0\n" \
  "s_waitcnt lgkmcnt(4)\n" \
  "v_smfmac_f32_16x16x64_bf16 v[8:11], v[40:43], v[24:31], v0\n" \
  "s_waitcnt lgkmcnt(2)\n" \
  "v_smfmac_f32_16x16x64_bf16 v[20:23], v[44:47], v[32:39], v0\n" \
  "s_waitcnt lgkmcnt(0)\n" \
  "v_smfmac_f32_16x16x64_bf16 v[8:11], v[44:47], v[48:55], v0\n" \
  "s_nop 7\n" \
  "v_permlane32_swap_b32 v20, v9\n" \
  "v_permlane32_swap_b32 v21, v8\n" \
  "v_permlane32_swap_b32 v22, v11\n" \
  "v_permlane32_swap_b32 v23, v10\n" \
  "v_pk_add_f32 v[20:21], v[20:21], v[8:9] op_sel:[0,1] op_sel_hi:[1,0]\n" \
  "v_pk_add_f32 v[22:23], v[22:23], v[10:11] op_sel:[0,1] op_sel_hi:[1,0]\n" \
  "buffer_atomic_add_x2 v[20:21], v5, s[40:43], s58 offen\n" \
  "buffer_atomic_add_x2 v[22:23], v5, s[40:43], s59 offen\n" \
  "s_memrealtime s[38:39]\n" \
  "v_mbcnt_lo_u32_b32 v48, -1, 0\n" \
  "v_mbcnt_hi_u32_b32 v48, -1, v48\n" \
  "s_add_u32 s88, s74, 157760\n" \
  "v_mov_b32 v59, s88\n" \
  "v_lshl_add_u32 v52, v48, 2, v59\n" \
  "ds_read_b32 v52, v52 offset:192\n" \
  "v_lshrrev_b32 v49, 3, v48\n" \
  "v_and_b32 v50, 7, v48\n" \
  "v_lshlrev_b32 v50, 4, v50\n" \
  "v_lshl_or_b32 v44, v49, 8, v50\n" \
  "s_mov_b32 s87, 0x40000\n" \
  "v_lshrrev_b32 v49, 2, v48\n" \
  "v_lshrrev_b32 v50, 4, v48\n" \
  "v_bitop3_b32 v50, v49, v50, 3 bitop3:0x28\n" \
  "v_lshl_add_u32 v8, v50, 3, s97\n" \
  "v_and_b32 v50, 3, v48\n" \
  "v_lshl_add_u32 v8, v50, 5, v8\n" \
  "v_and_b32 v49, 1, v49\n" \
  "v_lshl_add_u32 v8, v49, 8, v8\n" \
  "v_and_b32 v49, 8, v48\n" \
  "v_lshl_add_u32 v8, v49, 4, v8\n" \
  "v_lshrrev_b32 v49, 5, v48\n" \
  "v_add_u32 v10, s99, v1\n" \
  "s_lshr_b32 s88, s99, 2\n" \
  "s_add_u32 s88, s88, s74\n" \
  "s_add_u32 s88, s88, 139264\n" \
  "v_lshl_add_u32 v7, v49, 10, s88\n" \
  "v_and_b32 v49, 31, v48\n" \
  "v_lshl_add_u32 v7, v49, 3, v7\n" \
  "v_mov_b64 v[12:13], 0\n" \
  "v_mov_b64 v[14:15], 0\n" \
  "v_mov_b64 v[16:17], 0\n" \
  "v_mov_b64 v[18:19], 0\n" \
  "v_mov_b64 v[20:21], 0\n" \
  "v_mov_b64 v[22:23], 0\n" \
  "v_mov_b32 v34, 0xc9b26ca0\n" \
  "v_mov_b32 v35, 0xc9b26c86\n" \
  "v_and_b32 v50, 3, v48\n" \
  "v_lshl_add_u32 v40, v50, 2, s98\n" \
  "v_lshrrev_b32 v50, 4, v48\n" \
  "v_mad_u32_u24 v40, v50, s95, v40\n" \
  "v_bfe_u32 v49, v48, 3, 1\n" \
  "v_lshl_add_u32 v40, v49, 6, v40\n" \
  "v_bfe_u32 v49, v48, 2, 1\n" \
  "v_lshl_add_u32 v40, v49, 7, v40\n" \
  "v_xor_b32 v41, 16, v40\n" \
  "v_xor_b32 v42, 32, v40\n" \
  "v_xor_b32 v43, 48, v40\n" \
  "s_add_i32 s80, s86, 0x35000000\n" \
  "v_mov_b32 v11, 0\n" \
  "s_mov_b32 s89, s60\n" \
  "s_bitcmp1_b32 s60, 7\n" \
  "s_cselect_b32 s88, 0x10000, 0\n" \
  "s_bitset1_b32 s88, 7\n" \
  "s_xor_b32 s56, s56, s88\n" \
  "s_xor_b32 s57, s57, s88\n" \
  "s_xor_b32 s58, s58, s88\n" \
  "s_xor_b32 s59, s59, s88\n" \
  "s_xor_b32 s60, s60, s88\n" \
  "s_sub_u32 s71, s71, 1\n" \
  "s_waitcnt lgkmcnt(0)\n" \
  "s_sub_u32 s39, s38, 150\n" \
  "v_subrev_u32 v52, s39, v52\n" \
  "v_cmp_lt_u32 vcc, 0x1000, v52\n" \
  "s_cmp_eq_u64 vcc, 0\n" \
  "s_cbranch_scc1 .LCA_OKM_%=\n" \
  ".LCA_AGN_%=:\n" \
  "buffer_load_dword v52, v5, s[0:3], s66 offen sc1\n" \
  "s_sub_u32 s87, s87, 1\n" \
  "s_cbranch_scc1 .LCA_END_%=\n" \
  "s_waitcnt vmcnt(0)\n" \
  "v_cmp_eq_u32 vcc, 0, v52\n" \
  "v_subrev_u32 v52, s39, v52\n" \
  "s_cmp_eq_u64 vcc, 0\n" \
  "s_cbranch_scc1 .LCA_OKM_%=\n" \
  "s_sleep 4\n" \
  "s_branch .LCA_AGN_%=\n" \
  ".LCA_OKM_%=:\n" \
  "s_nop 1\n" \
  "v_max_i32_dpp v52, v52, v52 row_ror:1 row_mask:0xf bank_mask:0xf\n" \
  "s_nop 1\n" \
  "v_max_i32_dpp v52, v52, v52 row_ror:2 row_mask:0xf bank_mask:0xf\n" \
  "s_nop 1\n" \
  "v_max_i32_dpp v52, v52, v52 row_ror:4 row_mask:0xf bank_mask:0xf\n" \
  "s_nop 1\n" \
  "v_max_i32_dpp v52, v52, v52 row_ror:8 row_mask:0xf bank_mask:0xf\n" \
  "s_movk_i32 vcc_lo, 150\n" \
  "v_readlane_b32 s88, v52, 0\n" \
  "s_max_i32 vcc_lo, vcc_lo, s88\n" \
  "v_readlane_b32 s88, v52, 16\n" \
  "s_max_i32 vcc_lo, vcc_lo, s88\n" \
  "v_readlane_b32 s88, v52, 32\n" \
  "s_max_i32 vcc_lo, vcc_lo, s88\n" \
  "v_readlane_b32 s88, v52, 48\n" \
  "s_max_i32 vcc_lo, vcc_lo, s88\n" \
  "s_add_u32 s66, s66, 1024\n" \
  "s_add_u32 vcc_lo, vcc_lo, s39\n" \
  "s_add_u32 vcc_lo, vcc_lo, 23\n" \
  ".LCA_TW_%=:\n" \
  "s_memrealtime s[38:39]\n" \
  "s_waitcnt lgkmcnt(0)\n" \
  "s_sub_u32 s38, s38, vcc_lo\n" \
  "s_cmp_lt_i32 s38, 0\n" \
  "s_cbranch_scc1 .LCA_TW_%=\n" \
  "buffer_load_dwordx4 v[24:27], v44, s[40:43], s89 offen sc1\n" \
  "buffer_load_dwordx4 v[28:31], v44, s[40:43], s89 offen offset:2048 sc1\n" \
  ".LCA_SEE_%=:\n" \
  "s_waitcnt vmcnt(1)\n" \
  "v_cvt_f32_i32 v24, v24\n" \
  "v_cvt_f32_i32 v25, v25\n" \
  "v_cvt_f32_i32 v26, v26\n" \
  "v_cvt_f32_i32 v27, v27\n" \
  "v_max_f32 v45, |v24|, |v26|\n" \
  "v_pk_mul_f32 v[24:25], v[24:25], s[80:81] op_sel_hi:[1,0]\n" \
  "v_pk_mul_f32 v[26:27], v[26:27], s[80:81] op_sel_hi:[1,0]\n" \
  "s_nop 0\n" \
  "v_cvt_pk_bf16_f32 v32, v24, v26\n" \
  "v_cvt_pk_bf16_f32 v33, v25, v27\n" \
  "ds_write_b32 v40, v32\n" \
  "ds_write_b32 v41, v33\n" \
  "s_waitcnt vmcnt(0)\n" \
  "v_cvt_f32_i32 v28, v28\n" \
  "v_cvt_f32_i32 v30, v30\n" \
  "v_cvt_f32_i32 v29, v29\n" \
  "v_max3_f32 v45, v45, |v28|, |v30|\n" \
  "v_cvt_f32_i32 v31, v31\n" \
  "v_cmp_lt_f32 vcc, s91, v45\n" \
  "v_pk_mul_f32 v[28:29], v[28:29], s[80:81] op_sel_hi:[1,0]\n" \
  "v_pk_mul_f32 v[30:31], v[30:31], s[80:81] op_sel_hi:[1,0]\n" \
  "s_cmp_eq_u64 vcc, 0\n" \
  "s_cbranch_scc0 .LCA_POLL_%=\n" \
  "v_cvt_pk_bf16_f32 v36, v28, v30\n" \
  "v_cvt_pk_bf16_f32 v37, v29, v31\n" \
  "ds_write_b32 v42, v36\n" \
  "ds_write_b32 v43, v37\n" \
  "v_dot2c_f32_bf16 v11, v32, v32\n" \
  "v_dot2c_f32_bf16 v11, v33, v33\n" \
  "v_dot2c_f32_bf16 v11, v36, v36\n" \
  "v_dot2c_f32_bf16 v11, v37, v37\n" \
  "s_nop 1\n" \
  "s_nop 0\n" \
  "v_cmp_lt_f32 vcc, 4.0, v11\n" \
  "s_cbranch_vccz .LCA_TOP_%=\n" \
  "s_mov_b64 exec, vcc\n" \
  "ds_max_u32 v59, v11\n" \
  "s_mov_b64 exec, -1\n" \
  "s_branch .LCA_TOP_%=\n" \
  ".LCA_POLL_%=:\n" \
  "buffer_load_dwordx4 v[24:27], v44, s[40:43], s89 offen sc1\n" \
  "buffer_load_dwordx4 v[28:31], v44, s[40:43], s89 offen offset:2048 sc1\n" \
  "s_sub_u32 s87, s87, 1\n" \
  "s_cbranch_scc1 .LCA_END_%=\n" \
  "s_branch .LCA_SEE_%=\n" \
  ".LCA_LAST_%=:\n" \
  ".LCA_END_%=:\n" \
  "s_endpgm\n"
#define CHAIN_ASM_VO \
  "s_mov_b32 s95, 272\n" \
  "s_mov_b32 s91, 0x4c7c2000\n" \
  "s_mov_b32 s85, 0xc9b26c9b\n" \
  "s_mov_b32 s92, 0xc9326c9b\n" \
  "s_mov_b32 s93, 0xc9326c9b\n" \
  "s_mov_b32 s78, 0x3e0293ee\n" \
  "s_mov_b32 s76, 0x33333333\n" \
  "s_mov_b32 s77, 0x33333333\n" \
  "s_lshl_b32 s98, s73, 10\n" \
  "s_add_u32 s98, s98, s74\n" \
  "s_add_u32 s98, s98, 131072\n" \
  "s_lshl_b32 s97, s73, 9\n" \
  "s_add_u32 s97, s97, s74\n" \
  "s_add_u32 s97, s97, 158724\n" \
  "s_lshl_b32 s99, s73, 11\n" \
  "s_add_u32 s99, s99, s74\n" \
  "s_add_u32 s99, s99, 133120\n" \
  "s_lshl_b32 s94, s73, 4\n" \
  "s_add_u32 s94, s94, s74\n" \
  "s_add_u32 s94, s94, 157760\n" \
  "v_mbcnt_lo_u32_b32 v48, -1, 0\n" \
  "v_mbcnt_hi_u32_b32 v48, -1, v48\n" \
  "s_add_u32 s88, s74, 157760\n" \
  "v_mov_b32 v59, s88\n" \
  "s_sub_u32 s88, s73, 4\n" \
  "v_lshrrev_b32 v49, 2, v48\n" \
  "v_lshrrev_b32 v50, 4, v48\n" \
  "v_bitop3_b32 v50, v49, v50, 3 bitop3:0x28\n" \
  "v_lshl_add_u32 v4, v50, 3, s97\n" \
  "v_and_b32 v50, 3, v48\n" \
  "v_lshl_add_u32 v4, v50, 5, v4\n" \
  "v_and_b32 v49, 1, v49\n" \
  "v_lshl_add_u32 v4, v49, 8, v4\n" \
  "v_and_b32 v49, 8, v48\n" \
  "v_lshl_add_u32 v4, v49, 4, v4\n" \
  "v_and_b32 v51, 15, v48\n" \
  "v_bfe_u32 v49, v48, 4, 1\n" \
  "v_lshrrev_b32 v50, 5, v48\n" \
  "s_lshl_b32 s89, s88, 10\n" \
  "s_add_u32 s89, s89, s74\n" \
  "s_add_u32 s89, s89, 158720\n" \
  "v_lshrrev_b32 v9, 3, v51\n" \
  "v_mul_u32_u24 v6, 41, v9\n" \
  "v_xor_b32 v6, v6, v51\n" \
  "v_lshl_add_u32 v6, v6, 3, s89\n" \
  "v_lshl_add_u32 v6, v49, 6, v6\n" \
  "v_lshl_add_u32 v6, v50, 9, v6\n" \
  "v_xor_b32 v9, 0x90, v6\n" \
  "v_lshlrev_b32 v8, 4, v49\n" \
  "v_lshl_add_u32 v8, v51, 5, v8\n" \
  "v_lshl_add_u32 v8, v50, 3, v8\n" \
  "s_lshl_b32 s89, s88, 8\n" \
  "s_add_u32 s89, s89, s74\n" \
  "s_add_u32 s89, s89, 139264\n" \
  "v_and_b32 v49, 31, v48\n" \
  "v_lshl_add_u32 v5, v49, 3, s89\n" \
  "v_lshl_add_u32 v5, v50, 10, v5\n" \
  "v_lshrrev_b32 v49, 2, v51\n" \
  "v_and_b32 v50, 5, v51\n" \
  "v_and_or_b32 v49, v49, 2, v50\n" \
  "v_and_b32 v50, 48, v48\n" \
  "v_lshlrev_b32 v7, 3, v50\n" \
  "v_lshl_add_u32 v7, v49, 4, v7\n" \
  "v_bitop3_b32 v49, v51, 2, 2 bitop3:0x0c\n" \
  "v_lshl_add_u32 v7, v49, 29, v7\n" \
  "v_mov_b64 v[12:13], 0\n" \
  "v_mov_b64 v[14:15], 0\n" \
  "v_mov_b64 v[16:17], 0\n" \
  "v_mov_b64 v[18:19], 0\n" \
  "v_mov_b64 v[20:21], 0\n" \
  "v_mov_b64 v[22:23], 0\n" \
  "v_lshlrev_b32 v10, 4, v48\n" \
  "s_add_u32 s78, s74, 157952\n" \
  "v_lshl_add_u32 v52, v48, 2, v59\n" \
  "ds_write_b32 v52, v12 offset:192\n" \
  "s_movk_i32 s79, 105\n" \
  "s_movk_i32 s96, 25\n" \
  "s_mov_b32 s87, 0x40000\n" \
  "v_subrev_u32 v30, 64, v59\n" \
  "v_add_u32 v17, 0x10000, v28\n" \
  "v_xor_b32 v18, 128, v28\n" \
  "v_xor_b32 v19, 128, v17\n" \
  "ds_write_b32 v30, v12 offset:0\n" \
  "ds_write_b32 v30, v12 offset:32\n" \
  "ds_write_b32 v30, v12 offset:64\n" \
  "v_xor_b32 v20, 0, v29\n" \
  "v_xor_b32 v21, 32, v29\n" \
  "v_xor_b32 v22, 64, v29\n" \
  "v_xor_b32 v23, 96, v29\n" \
  "s_waitcnt vmcnt(12) lgkmcnt(0)\n" \
  "s_barrier\n" \
  "ds_read_b64_tr_b16 v[96:97], v20 offset:0\n" \
  "ds_read_b64_tr_b16 v[98:99], v20 offset:2048\n" \
  "ds_read_b64_tr_b16 v[104:105], v21 offset:0\n" \
  "ds_read_b64_tr_b16 v[106:107], v21 offset:2048\n" \
  "ds_read_b64_tr_b16 v[112:113], v22 offset:0\n" \
  "ds_read_b64_tr_b16 v[114:115], v22 offset:2048\n" \
  "ds_read_b64_tr_b16 v[120:121], v23 offset:0\n" \
  "ds_read_b64_tr_b16 v[122:123], v23 offset:2048\n" \
  "s_lshl_b32 s88, s73, 10\n" \
  "v_add_u32 v48, s88, v1\n" \
  "ds_read_b128 v[52:55], v48 offset:0\n" \
  "s_waitcnt lgkmcnt(0)\n" \
  "v_mov_b32 v11, 0\n" \
  "v_dot2c_f32_bf16 v11, v52, v52\n" \
  "v_dot2c_f32_bf16 v11, v53, v53\n" \
  "v_dot2c_f32_bf16 v11, v54, v54\n" \
  "v_dot2c_f32_bf16 v11, v55, v55\n" \
  "s_nop 0\n" \
  "s_nop 1\n" \
  "v_max_u32_dpp v11, v11, v11 row_ror:1 row_mask:0xf bank_mask:0xf\n" \
  "s_nop 1\n" \
  "v_max_u32_dpp v11, v11, v11 row_ror:2 row_mask:0xf bank_mask:0xf\n" \
  "s_nop 1\n" \
  "v_max_u32_dpp v11, v11, v11 row_ror:4 row_mask:0xf bank_mask:0xf\n" \
  "s_nop 1\n" \
  "v_max_u32_dpp v11, v11, v11 row_ror:8 row_mask:0xf bank_mask:0xf\n" \
  "v_mov_b32 v31, v11\n" \
  "s_nop 1\n" \
  "v_permlane16_swap_b32 v11, v31\n" \
  "v_max_u32 v11, v11, v31\n" \
  "v_mov_b32 v31, v11\n" \
  "s_nop 1\n" \
  "v_permlane32_swap_b32 v11, v31\n" \
  "v_max_u32 v11, v11, v31\n" \
  "s_mov_b64 exec, 1\n" \
  "ds_max_u32 v59, v11\n" \
  "s_mov_b64 exec, -1\n" \
  "s_waitcnt vmcnt(8) lgkmcnt(0)\n" \
  "s_barrier\n" \
  "ds_read_b64_tr_b16 v[100:101], v20 offset:4096\n" \
  "ds_read_b64_tr_b16 v[102:103], v20 offset:6144\n" \
  "ds_read_b64_tr_b16 v[108:109], v21 offset:4096\n" \
  "ds_read_b64_tr_b16 v[110:111], v21 offset:6144\n" \
  "ds_read_b64_tr_b16 v[116:117], v22 offset:4096\n" \
  "ds_read_b64_tr_b16 v[118:119], v22 offset:6144\n" \
  "ds_read_b64_tr_b16 v[124:125], v23 offset:4096\n" \
  "ds_read_b64_tr_b16 v[126:127], v23 offset:6144\n" \
  "s_add_u32 m0, s19, 0\n" \
  "s_nop 0\n" \
  "global_load_lds_dwordx4 v24, s[4:5] offset:0\n" \
  "global_load_lds_dwordx4 v25, s[4:5] offset:1024\n" \
  "global_load_lds_dwordx4 v26, s[4:5] offset:2048\n" \
  "global_load_lds_dwordx4 v27, s[4:5] offset:3072\n" \
  "s_add_u32 s4, s4, 0x100000\n" \
  "s_addc_u32 s5, s5, 0\n" \
  "v_mfma_f32_16x16x32_bf16 v[64:67], v[96:99], v[96:99], 0\n" \
  "v_mfma_f32_16x16x32_bf16 v[68:71], v[104:107], v[104:107], 0\n" \
  "v_mfma_f32_16x16x32_bf16 v[72:75], v[112:115], v[112:115], 0\n" \
  "v_mfma_f32_16x16x32_bf16 v[76:79], v[120:123], v[120:123], 0\n" \
  "s_waitcnt vmcnt(8) lgkmcnt(0)\n" \
  "s_barrier\n" \
  "ds_read_b64_tr_b16 v[32:33], v20 offset:8192\n" \
  "ds_read_b64_tr_b16 v[34:35], v20 offset:10240\n" \
  "ds_read_b64_tr_b16 v[40:41], v21 offset:8192\n" \
  "ds_read_b64_tr_b16 v[42:43], v21 offset:10240\n" \
  "ds_read_b64_tr_b16 v[48:49], v22 offset:8192\n" \
  "ds_read_b64_tr_b16 v[50:51], v22 offset:10240\n" \
  "ds_read_b64_tr_b16 v[56:57], v23 offset:8192\n" \
  "ds_read_b64_tr_b16 v[58:59], v23 offset:10240\n" \
  "s_add_u32 m0, s19, 4096\n" \
  "s_nop 0\n" \
  "global_load_lds_dwordx4 v24, s[4:5] offset:0\n" \
  "global_load_lds_dwordx4 v25, s[4:5] offset:1024\n" \
  "global_load_lds_dwordx4 v26, s[4:5] offset:2048\n" \
  "global_load_lds_dwordx4 v27, s[4:5] offset:3072\n" \
  "s_add_u32 s4, s4, 0x100000\n" \
  "s_addc_u32 s5, s5, 0\n" \
  "v_mfma_f32_16x16x32_bf16 v[64:67], v[100:103], v[100:103], v[64:67]\n" \
  "v_mfma_f32_16x16x32_bf16 v[68:71], v[108:111], v[108:111], v[68:71]\n" \
  "v_mfma_f32_16x16x32_bf16 v[72:75], v[116:119], v[116:119], v[72:75]\n" \
  "v_mfma_f32_16x16x32_bf16 v[76:79], v[124:127], v[124:127], v[76:79]\n" \
  "s_waitcnt vmcnt(8) lgkmcnt(0)\n" \
  "s_barrier\n" \
  "ds_read_b64_tr_b16 v[36:37], v20 offset:12288\n" \
  "ds_read_b64_tr_b16 v[38:39], v20 offset:14336\n" \
  "ds_read_b64_tr_b16 v[44:45], v21 offset:12288\n" \
  "ds_read_b64_tr_b16 v[46:47], v21 offset:14336\n" \
  "ds_read_b64_tr_b16 v[52:53], v22 offset:12288\n" \
  "ds_read_b64_tr_b16 v[54:55], v22 offset:14336\n" \
  "ds_read_b64_tr_b16 v[60:61], v23 offset:12288\n" \
  "ds_read_b64_tr_b16 v[62:63], v23 offset:14336\n" \
  "s_add_u32 m0, s19, 8192\n" \
  "s_nop 0\n" \
  "global_load_lds_dwordx4 v24, s[4:5] offset:0\n" \
  "global_load_lds_dwordx4 v25, s[4:5] offset:1024\n" \
  "global_load_lds_dwordx4 v26, s[4:5] offset:2048\n" \
  "global_load_lds_dwordx4 v27, s[4:5] offset:3072\n" \
  "s_add_u32 s4, s4, 0x100000\n" \
  "s_addc_u32 s5, s5, 0\n" \
  "v_mfma_f32_16x16x32_bf16 v[64:67], v[32:35], v[32:35], v[64:67]\n" \
  "v_mfma_f32_16x16x32_bf16 v[68:71], v[40:43], v[40:43], v[68:71]\n" \
  "v_mfma_f32_16x16x32_bf16 v[72:75], v[48:51], v[48:51], v[72:75]\n" \
  "v_mfma_f32_16x16x32_bf16 v[76:79], v[56:59], v[56:59], v[76:79]\n" \
  "s_waitcnt vmcnt(8) lgkmcnt(0)\n" \
  "s_barrier\n" \
  "ds_read_b64_tr_b16 v[128:129], v18 offset:0\n" \
  "ds_read_b64_tr_b16 v[130:131], v18 offset:512\n" \
  "ds_read_b64_tr_b16 v[132:133], v18 offset:32768\n" \
  "ds_read_b64_tr_b16 v[134:135], v18 offset:33280\n" \
  "ds_read_b64_tr_b16 v[136:137], v19 offset:0\n" \
  "ds_read_b64_tr_b16 v[138:139], v19 offset:512\n" \
  "ds_read_b64_tr_b16 v[140:141], v19 offset:32768\n" \
  "ds_read_b64_tr_b16 v[142:143], v19 offset:33280\n" \
  "ds_read_b64_tr_b16 v[192:193], v28 offset:0\n" \
  "ds_read_b64_tr_b16 v[194:195], v28 offset:512\n" \
  "ds_read_b64_tr_b16 v[196:197], v28 offset:32768\n" \
  "ds_read_b64_tr_b16 v[198:199], v28 offset:33280\n" \
  "ds_read_b64_tr_b16 v[200:201], v17 offset:0\n" \
  "ds_read_b64_tr_b16 v[202:203], v17 offset:512\n" \
  "ds_read_b64_tr_b16 v[204:205], v17 offset:32768\n" \
  "ds_read_b64_tr_b16 v[206:207], v17 offset:33280\n" \
  "s_add_u32 m0, s19, 12288\n" \
  "s_nop 0\n" \
  "global_load_lds_dwordx4 v24, s[4:5] offset:0\n" \
  "global_load_lds_dwordx4 v25, s[4:5] offset:1024\n" \
  "global_load_lds_dwordx4 v26, s[4:5] offset:2048\n" \
  "global_load_lds_dwordx4 v27, s[4:5] offset:3072\n" \
  "s_add_u32 s4, s4, 0x100000\n" \
  "s_addc_u32 s5, s5, 0\n" \
  "v_mfma_f32_16x16x32_bf16 v[64:67], v[36:39], v[36:39], v[64:67]\n" \
  "v_mfma_f32_16x16x32_bf16 v[68:71], v[44:47], v[44:47], v[68:71]\n" \
  "v_mfma_f32_16x16x32_bf16 v[72:75], v[52:55], v[52:55], v[72:75]\n" \
  "v_mfma_f32_16x16x32_bf16 v[76:79], v[60:63], v[60:63], v[76:79]\n" \
  "s_nop 7\n" \
  "v_max3_f32 v13, v64, v65, v66\n" \
  "v_max3_f32 v13, v13, v67, v68\n" \
  "v_max3_f32 v13, v13, v69, v70\n" \
  "v_max3_f32 v13, v13, v71, v72\n" \
  "v_max3_f32 v13, v13, v73, v74\n" \
  "v_max3_f32 v13, v13, v75, v76\n" \
  "v_max3_f32 v13, v13, v77, v78\n" \
  "v_max_f32 v13, v13, v79\n" \
  "s_nop 1\n" \
  "v_max_f32_dpp v13, v13, v13 row_ror:1 row_mask:0xf bank_mask:0xf\n" \
  "s_nop 1\n" \
  "v_max_f32_dpp v13, v13, v13 row_ror:2 row_mask:0xf bank_mask:0xf\n" \
  "s_nop 1\n" \
  "v_max_f32_dpp v13, v13, v13 row_ror:4 row_mask:0xf bank_mask:0xf\n" \
  "s_nop 1\n" \
  "v_max_f32_dpp v13, v13, v13 row_ror:8 row_mask:0xf bank_mask:0xf\n" \
  "v_mov_b32 v31, v13\n" \
  "s_nop 1\n" \
  "v_permlane16_swap_b32 v13, v31\n" \
  "v_max_f32 v13, v13, v31\n" \
  "v_mov_b32 v31, v13\n" \
  "s_nop 1\n" \
  "v_permlane32_swap_b32 v13, v31\n" \
  "v_max_f32 v13, v13, v31\n" \
  "s_mov_b64 exec, 1\n" \
  "ds_max_u32 v30, v13 offset:32\n" \
  "s_mov_b64 exec, -1\n" \
  "s_waitcnt vmcnt(8) lgkmcnt(0)\n" \
  "s_barrier\n" \
  "ds_read_b64_tr_b16 v[144:145], v18 offset:4096\n" \
  "ds_read_b64_tr_b16 v[146:147], v18 offset:4608\n" \
  "ds_read_b64_tr_b16 v[148:149], v18 offset:36864\n" \
  "ds_read_b64_tr_b16 v[150:151], v18 offset:37376\n" \
  "ds_read_b64_tr_b16 v[152:153], v19 offset:4096\n" \
  "ds_read_b64_tr_b16 v[154:155], v19 offset:4608\n" \
  "ds_read_b64_tr_b16 v[156:157], v19 offset:36864\n" \
  "ds_read_b64_tr_b16 v[158:159], v19 offset:37376\n" \
  "ds_read_b64_tr_b16 v[208:209], v28 offset:4096\n" \
  "ds_read_b64_tr_b16 v[210:211], v28 offset:4608\n" \
  "ds_read_b64_tr_b16 v[212:213], v28 offset:36864\n" \
  "ds_read_b64_tr_b16 v[214:215], v28 offset:37376\n" \
  "ds_read_b64_tr_b16 v[216:217], v17 offset:4096\n" \
  "ds_read_b64_tr_b16 v[218:219], v17 offset:4608\n" \
  "ds_read_b64_tr_b16 v[220:221], v17 offset:36864\n" \
  "ds_read_b64_tr_b16 v[222:223], v17 offset:37376\n" \
  "s_add_u32 m0, s19, 0\n" \
  "s_nop 0\n" \
  "global_load_lds_dwordx4 v24, s[6:7] offset:0\n" \
  "global_load_lds_dwordx4 v25, s[6:7] offset:1024\n" \
  "global_load_lds_dwordx4 v26, s[6:7] offset:2048\n" \
  "global_load_lds_dwordx4 v27, s[6:7] offset:3072\n" \
  "s_add_u32 s6, s6, 0x100000\n" \
  "s_addc_u32 s7, s7, 0\n" \
  "v_mfma_f32_16x16x32_bf16 v[64:67], v[128:131], v[128:131], 0\n" \
  "v_mfma_f32_16x16x32_bf16 v[68:71], v[192:195], v[192:195], 0\n" \
  "v_mfma_f32_16x16x32_bf16 v[64:67], v[132:135], v[132:135], v[64:67]\n" \
  "v_mfma_f32_16x16x32_bf16 v[68:71], v[196:199], v[196:199], v[68:71]\n" \
  "v_mfma_f32_16x16x32_bf16 v[64:67], v[136:139], v[136:139], v[64:67]\n" \
  "v_mfma_f32_16x16x32_bf16 v[68:71], v[200:203], v[200:203], v[68:71]\n" \
  "v_mfma_f32_16x16x32_bf16 v[64:67], v[140:143], v[140:143], v[64:67]\n" \
  "v_mfma_f32_16x16x32_bf16 v[68:71], v[204:207], v[204:207], v[68:71]\n" \
  "s_waitcnt vmcnt(8) lgkmcnt(0)\n" \
  "s_barrier\n" \
  "ds_read_b64_tr_b16 v[160:161], v18 offset:8192\n" \
  "ds_read_b64_tr_b16 v[162:163], v18 offset:8704\n" \
  "ds_read_b64_tr_b16 v[164:165], v18 offset:40960\n" \
  "ds_read_b64_tr_b16 v[166:167], v18 offset:41472\n" \
  "ds_read_b64_tr_b16 v[168:169], v19 offset:8192\n" \
  "ds_read_b64_tr_b16 v[170:171], v19 offset:8704\n" \
  "ds_read_b64_tr_b16 v[172:173], v19 offset:40960\n" \
  "ds_read_b64_tr_b16 v[174:175], v19 offset:41472\n" \
  "ds_read_b64_tr_b16 v[224:225], v28 offset:8192\n" \
  "ds_read_b64_tr_b16 v[226:227], v28 offset:8704\n" \
  "ds_read_b64_tr_b16 v[228:229], v28 offset:40960\n" \
  "ds_read_b64_tr_b16 v[230:231], v28 offset:41472\n" \
  "ds_read_b64_tr_b16 v[232:233], v17 offset:8192\n" \
  "ds_read_b64_tr_b16 v[234:235], v17 offset:8704\n" \
  "ds_read_b64_tr_b16 v[236:237], v17 offset:40960\n" \
  "ds_read_b64_tr_b16 v[238:239], v17 offset:41472\n" \
  "s_add_u32 m0, s19, 4096\n" \
  "s_nop 0\n" \
  "global_load_lds_dwordx4 v24, s[6:7] offset:0\n" \
  "global_load_lds_dwordx4 v25, s[6:7] offset:1024\n" \
  "global_load_lds_dwordx4 v26, s[6:7] offset:2048\n" \
  "global_load_lds_dwordx4 v27, s[6:7] offset:3072\n" \
  "s_add_u32 s6, s6, 0x100000\n" \
  "s_addc_u32 s7, s7, 0\n" \
  "v_mfma_f32_16x16x32_bf16 v[64:67], v[144:147], v[144:147], v[64:67]\n" \
  "v_mfma_f32_16x16x32_bf16 v[68:71], v[208:211], v[208:211], v[68:71]\n" \
  "v_mfma_f32_16x16x32_bf16 v[64:67], v[148:151], v[148:151], v[64:67]\n" \
  "v_mfma_f32_16x16x32_bf16 v[68:71], v[212:215], v[212:215], v[68:71]\n" \
  "v_mfma_f32_16x16x32_bf16 v[64:67], v[152:155], v[152:155], v[64:67]\n" \
  "v_mfma_f32_16x16x32_bf16 v[68:71], v[216:219], v[216:219], v[68:71]\n" \
  "v_mfma_f32_16x16x32_bf16 v[64:67], v[156:159], v[156:159], v[64:67]\n" \
  "v_mfma_f32_16x16x32_bf16 v[68:71], v[220:223], v[220:223], v[68:71]\n" \
  "s_waitcnt vmcnt(8) lgkmcnt(0)\n" \
  "s_barrier\n" \
  "ds_read_b64_tr_b16 v[176:177], v18 offset:12288\n" \
  "ds_read_b64_tr_b16 v[178:179], v18 offset:12800\n" \
  "ds_read_b64_tr_b16 v[180:181], v18 offset:45056\n" \
  "ds_read_b64_tr_b16 v[182:183], v18 offset:45568\n" \
  "ds_read_b64_tr_b16 v[184:185], v19 offset:12288\n" \
  "ds_read_b64_tr_b16 v[186:187], v19 offset:12800\n" \
  "ds_read_b64_tr_b16 v[188:189], v19 offset:45056\n" \
  "ds_read_b64_tr_b16 v[190:191], v19 offset:45568\n" \
  "ds_read_b64_tr_b16 v[240:241], v28 offset:12288\n" \
  "ds_read_b64_tr_b16 v[242:243], v28 offset:12800\n" \
  "ds_read_b64_tr_b16 v[244:245], v28 offset:45056\n" \
  "ds_read_b64_tr_b16 v[246:247], v28 offset:45568\n" \
  "ds_read_b64_tr_b16 v[248:249], v17 offset:12288\n" \
  "ds_read_b64_tr_b16 v[250:251], v17 offset:12800\n" \
  "ds_read_b64_tr_b16 v[252:253], v17 offset:45056\n" \
  "ds_read_b64_tr_b16 v[254:255], v17 offset:45568\n" \
  "s_add_u32 m0, s19, 8192\n" \
  "s_nop 0\n" \
  "global_load_lds_dwordx4 v24, s[6:7] offset:0\n" \
  "global_load_lds_dwordx4 v25, s[6:7] offset:1024\n" \
  "global_load_lds_dwordx4 v26, s[6:7] offset:2048\n" \
  "global_load_lds_dwordx4 v27, s[6:7] offset:3072\n" \
  "s_add_u32 s6, s6, 0x100000\n" \
  "s_addc_u32 s7, s7, 0\n" \
  "v_mfma_f32_16x16x32_bf16 v[64:67], v[160:163], v[160:163], v[64:67]\n" \
  "v_mfma_f32_16x16x32_bf16 v[68:71], v[224:227], v[224:227], v[68:71]\n" \
  "v_mfma_f32_16x16x32_bf16 v[64:67], v[164:167], v[164:167], v[64:67]\n" \
  "v_mfma_f32_16x16x32_bf16 v[68:71], v[228:231], v[228:231], v[68:71]\n" \
  "v_mfma_f32_16x16x32_bf16 v[64:67], v[168:171], v[168:171], v[64:67]\n" \
  "v_mfma_f32_16x16x32_bf16 v[68:71], v[232:235], v[232:235], v[68:71]\n" \
  "v_mfma_f32_16x16x32_bf16 v[64:67], v[172:175], v[172:175], v[64:67]\n" \
  "v_mfma_f32_16x16x32_bf16 v[68:71], v[236:239], v[236:239], v[68:71]\n" \
  "s_waitcnt vmcnt(8) lgkmcnt(0)\n" \
  "s_barrier\n" \
  "s_add_u32 m0, s19, 12288\n" \
  "s_nop 0\n" \
  "global_load_lds_dwordx4 v24, s[6:7] offset:0\n" \
  "global_load_lds_dwordx4 v25, s[6:7] offset:1024\n" \
  "global_load_lds_dwordx4 v26, s[6:7] offset:2048\n" \
  "global_load_lds_dwordx4 v27, s[6:7] offset:3072\n" \
  "s_add_u32 s6, s6, 0x100000\n" \
  "s_addc_u32 s7, s7, 0\n" \
  "v_mfma_f32_16x16x32_bf16 v[64:67], v[176:179], v[176:179], v[64:67]\n" \
  "v_mfma_f32_16x16x32_bf16 v[68:71], v[240:243], v[240:243], v[68:71]\n" \
  "v_mfma_f32_16x16x32_bf16 v[64:67], v[180:183], v[180:183], v[64:67]\n" \
  "v_mfma_f32_16x16x32_bf16 v[68:71], v[244:247], v[244:247], v[68:71]\n" \
  "v_mfma_f32_16x16x32_bf16 v[64:67], v[184:187], v[184:187], v[64:67]\n" \
  "v_mfma_f32_16x16x32_bf16 v[68:71], v[248:251], v[248:251], v[68:71]\n" \
  "v_mfma_f32_16x16x32_bf16 v[64:67], v[188:191], v[188:191], v[64:67]\n" \
  "v_mfma_f32_16x16x32_bf16 v[68:71], v[252:255], v[252:255], v[68:71]\n" \
  "s_nop 7\n" \
  "s_mov_b32 exec_lo, 0x100001\n" \
  "s_mov_b32 exec_hi, 0x10000100\n" \
  "v_add_f32 v12, v64, v68\n" \
  "s_mov_b32 exec_lo, 0x200002\n" \
  "s_mov_b32 exec_hi, 0x20000200\n" \
  "v_add_f32 v12, v65, v69\n" \
  "s_mov_b32 exec_lo, 0x400004\n" \
  "s_mov_b32 exec_hi, 0x40000400\n" \
  "v_add_f32 v12, v66, v70\n" \
  "s_mov_b32 exec_lo, 0x800008\n" \
  "s_mov_b32 exec_hi, 0x80000800\n" \
  "v_add_f32 v12, v67, v71\n" \
  "s_mov_b64 exec, -1\n" \
  "s_nop 1\n" \
  "v_add_f32_dpp v12, v12, v12 row_ror:1 row_mask:0xf bank_mask:0xf\n" \
  "s_nop 1\n" \
  "v_add_f32_dpp v12, v12, v12 row_ror:2 row_mask:0xf bank_mask:0xf\n" \
  "s_nop 1\n" \
  "v_add_f32_dpp v12, v12, v12 row_ror:4 row_mask:0xf bank_mask:0xf\n" \
  "s_nop 1\n" \
  "v_add_f32_dpp v12, v12, v12 row_ror:8 row_mask:0xf bank_mask:0xf\n" \
  "v_mov_b32 v31, v12\n" \
  "s_nop 1\n" \
  "v_permlane16_swap_b32 v12, v31\n" \
  "v_add_f32 v12, v12, v31\n" \
  "v_mov_b32 v31, v12\n" \
  "s_nop 1\n" \
  "v_permlane32_swap_b32 v12, v31\n" \
  "v_add_f32 v12, v12, v31\n" \
  "v_lshl_add_u32 v31, s73, 2, v30\n" \
  "s_mov_b64 exec, 1\n" \
  "ds_write_b32 v31, v12\n" \
  "s_mov_b64 exec, -1\n" \
  "s_waitcnt vmcnt(8) lgkmcnt(0)\n" \
  "s_barrier\n" \
  "s_add_u32 m0, s19, 0\n" \
  "s_nop 0\n" \
  "global_load_lds_dwordx4 v24, s[8:9] offset:0\n" \
  "global_load_lds_dwordx4 v25, s[8:9] offset:1024\n" \
  "global_load_lds_dwordx4 v26, s[8:9] offset:2048\n" \
  "global_load_lds_dwordx4 v27, s[8:9] offset:3072\n" \
  "s_add_u32 s8, s8, 0x100000\n" \
  "s_addc_u32 s9, s9, 0\n" \
  "ds_read_b128 v[20:23], v30 offset:16\n" \
  "ds_read_b32 v31, v30 offset:32\n" \
  "v_mbcnt_lo_u32_b32 v16, -1, 0\n" \
  "v_mbcnt_hi_u32_b32 v16, -1, v16\n" \
  "v_lshlrev_b32 v15, 8, v16\n" \
  "s_waitcnt lgkmcnt(0)\n" \
  "v_add_f32 v20, v20, v21\n" \
  "v_add_f32 v22, v22, v23\n" \
  "v_add_f32 v20, v20, v22\n" \
  "v_mul_f32 v31, v20, v31\n" \
  "v_mul_f32 v31, 0x3f8147ae, v31\n" \
  "v_mov_b32 v12, s10\n" \
  "v_mov_b32 v13, 0xffff\n" \
  "v_bfi_b32 v13, v13, s11, v31\n" \
  "s_mov_b32 exec_hi, 0\n" \
  "s_mov_b32 exec_lo, s18\n" \
  "global_store_dwordx2 v15, v[12:13], s[16:17] sc1\n" \
  "s_mov_b64 exec, -1\n" \
  "v_lshlrev_b32 v16, 3, v16\n" \
  "s_waitcnt vmcnt(8) lgkmcnt(0)\n" \
  "s_barrier\n" \
  "s_add_u32 m0, s19, 4096\n" \
  "s_nop 0\n" \
  "global_load_lds_dwordx4 v24, s[8:9] offset:0\n" \
  "global_load_lds_dwordx4 v25, s[8:9] offset:1024\n" \
  "global_load_lds_dwordx4 v26, s[8:9] offset:2048\n" \
  "global_load_lds_dwordx4 v27, s[8:9] offset:3072\n" \
  "s_add_u32 s8, s8, 0x100000\n" \
  "s_addc_u32 s9, s9, 0\n" \
  "global_load_dwordx2 v[20:21], v16, s[12:13] sc1\n" \
  "s_waitcnt vmcnt(9) lgkmcnt(0)\n" \
  "s_barrier\n" \
  "s_add_u32 m0, s19, 8192\n" \
  "s_nop 0\n" \
  "global_load_lds_dwordx4 v24, s[8:9] offset:0\n" \
  "global_load_lds_dwordx4 v25, s[8:9] offset:1024\n" \
  "global_load_lds_dwordx4 v26, s[8:9] offset:2048\n" \
  "global_load_lds_dwordx4 v27, s[8:9] offset:3072\n" \
  "s_add_u32 s8, s8, 0x100000\n" \
  "s_addc_u32 s9, s9, 0\n" \
  "s_waitcnt vmcnt(9) lgkmcnt(0)\n" \
  "s_barrier\n" \
  "ds_read_b64_tr_b16 v[64:65], v28 offset:0\n" \
  "ds_read_b64_tr_b16 v[66:67], v28 offset:512\n" \
  "ds_read_b64_tr_b16 v[68:69], v28 offset:32768\n" \
  "ds_read_b64_tr_b16 v[70:71], v28 offset:33280\n" \
  "ds_read_b64_tr_b16 v[72:73], v17 offset:0\n" \
  "ds_read_b64_tr_b16 v[74:75], v17 offset:512\n" \
  "ds_read_b64_tr_b16 v[76:77], v17 offset:32768\n" \
  "ds_read_b64_tr_b16 v[78:79], v17 offset:33280\n" \
  "s_add_u32 m0, s19, 12288\n" \
  "s_nop 0\n" \
  "global_load_lds_dwordx4 v24, s[8:9] offset:0\n" \
  "global_load_lds_dwordx4 v25, s[8:9] offset:1024\n" \
  "global_load_lds_dwordx4 v26, s[8:9] offset:2048\n" \
  "global_load_lds_dwordx4 v27, s[8:9] offset:3072\n" \
  "s_add_u32 s8, s8, 0x100000\n" \
  "s_addc_u32 s9, s9, 0\n" \
  "s_waitcnt vmcnt(8)\n" \
  ".LCV_RD0_%=:\n" \
  "v_cmp_ne_u32 vcc, s10, v20\n" \
  "v_xor_b32 v31, s11, v21\n" \
  "v_cmp_ne_u32 s[22:23], 0, v31\n" \
  "s_or_b64 vcc, vcc, s[22:23]\n" \
  "s_cbranch_scc0 .LCV_RD1_%=\n" \
  "s_sleep 8\n" \
  "global_load_dwordx2 v[20:21], v16, s[12:13] sc1\n" \
  "s_sub_u32 s87, s87, 1\n" \
  "s_cbranch_scc1 .LCV_END_%=\n" \
  "s_waitcnt vmcnt(0)\n" \
  "s_branch .LCV_RD0_%=\n" \
  ".LCV_RD1_%=:\n" \
  "s_waitcnt vmcnt(8) lgkmcnt(0)\n" \
  "s_barrier\n" \
  "ds_write_b128 v3, v[96:99] offset:0\n" \
  "ds_write_b128 v3, v[100:103] offset:1024\n" \
  "ds_write_b128 v3, v[104:107] offset:2048\n" \
  "ds_write_b128 v3, v[108:111] offset:3072\n" \
  "ds_read_b64_tr_b16 v[80:81], v28 offset:4096\n" \
  "ds_read_b64_tr_b16 v[82:83], v28 offset:4608\n" \
  "ds_read_b64_tr_b16 v[84:85], v28 offset:36864\n" \
  "ds_read_b64_tr_b16 v[86:87], v28 offset:37376\n" \
  "ds_read_b64_tr_b16 v[88:89], v17 offset:4096\n" \
  "ds_read_b64_tr_b16 v[90:91], v17 offset:4608\n" \
  "ds_read_b64_tr_b16 v[92:93], v17 offset:36864\n" \
  "ds_read_b64_tr_b16 v[94:95], v17 offset:37376\n" \
  "v_and_b32 v16, 0xff, v16\n" \
  "global_load_dwordx2 v[22:23], v16, s[14:15] sc1\n" \
  "s_mov_b64 exec, 15\n" \
  "buffer_load_dword v13, v2, s[44:47], s68 offen sc1\n" \
  "s_mov_b64 exec, -1\n" \
  "s_waitcnt vmcnt(6) lgkmcnt(0)\n" \
  "s_barrier\n" \
  "ds_write_b128 v3, v[112:115] offset:4096\n" \
  "ds_write_b128 v3, v[116:119] offset:5120\n" \
  "ds_write_b128 v3, v[120:123] offset:6144\n" \
  "ds_write_b128 v3, v[124:127] offset:7168\n" \
  "ds_read_b64_tr_b16 v[96:97], v28 offset:8192\n" \
  "ds_read_b64_tr_b16 v[98:99], v28 offset:8704\n" \
  "ds_read_b64_tr_b16 v[100:101], v28 offset:40960\n" \
  "ds_read_b64_tr_b16 v[102:103], v28 offset:41472\n" \
  "ds_read_b64_tr_b16 v[104:105], v17 offset:8192\n" \
  "ds_read_b64_tr_b16 v[106:107], v17 offset:8704\n" \
  "ds_read_b64_tr_b16 v[108:109], v17 offset:40960\n" \
  "ds_read_b64_tr_b16 v[110:111], v17 offset:41472\n" \
  "s_waitcnt vmcnt(0)\n" \
  "v_cmp_eq_u32 vcc, 0, v13\n" \
  "s_and_b32 s90, vcc_lo, 15\n" \
  "s_cbranch_scc1 .LCV_GL0_%=\n" \
  "s_mov_b32 m0, s99\n" \
  "s_add_u32 s88, s99, 8192\n" \
  "global_load_lds_dwordx4 v10, s[62:63] sc1\n" \
  "global_load_lds_dwordx4 v10, s[62:63] offset:1024 sc1\n" \
  "s_mov_b32 m0, s88\n" \
  "s_nop 0\n" \
  "global_load_lds_dwordx4 v10, s[64:65] sc1\n" \
  "global_load_lds_dwordx4 v10, s[64:65] offset:1024 sc1\n" \
  ".LCV_GL0_%=:\n" \
  "v_cmp_ne_u32 vcc, s10, v22\n" \
  "v_xor_b32 v31, s11, v23\n" \
  "v_and_b32 v31, 0xffff, v31\n" \
  "v_cmp_ne_u32 s[22:23], 0, v31\n" \
  "s_or_b64 vcc, vcc, s[22:23]\n" \
  "s_cbranch_scc0 .LCV_GL1_%=\n" \
  "s_sleep 12\n" \
  "global_load_dwordx2 v[22:23], v16, s[14:15] sc1\n" \
  "s_sub_u32 s87, s87, 1\n" \
  "s_cbranch_scc1 .LCV_END_%=\n" \
  "s_waitcnt vmcnt(0)\n" \
  "s_branch .LCV_GL0_%=\n" \
  ".LCV_GL1_%=:\n" \
  "v_and_b32 v31, 0xffff0000, v23\n" \
  "s_nop 1\n" \
  "v_max_u32_dpp v31, v31, v31 row_ror:1 row_mask:0xf bank_mask:0xf\n" \
  "s_nop 1\n" \
  "v_max_u32_dpp v31, v31, v31 row_ror:2 row_mask:0xf bank_mask:0xf\n" \
  "s_nop 1\n" \
  "v_max_u32_dpp v31, v31, v31 row_ror:4 row_mask:0xf bank_mask:0xf\n" \
  "s_nop 1\n" \
  "v_max_u32_dpp v31, v31, v31 row_ror:8 row_mask:0xf bank_mask:0xf\n" \
  "v_mov_b32 v12, v31\n" \
  "s_nop 1\n" \
  "v_permlane16_swap_b32 v31, v12\n" \
  "v_max_u32 v31, v31, v12\n" \
  "v_mov_b32 v12, v31\n" \
  "s_nop 1\n" \
  "v_permlane32_swap_b32 v31, v12\n" \
  "v_max_u32 v31, v31, v12\n" \
  "s_mov_b64 exec, 1\n" \
  "ds_write_b32 v30, v31\n" \
  "s_mov_b64 exec, -1\n" \
  "s_waitcnt vmcnt(4) lgkmcnt(0)\n" \
  "s_barrier\n" \
  "ds_write_b128 v3, v[32:35] offset:8192\n" \
  "ds_write_b128 v3, v[36:39] offset:9216\n" \
  "ds_write_b128 v3, v[40:43] offset:10240\n" \
  "ds_write_b128 v3, v[44:47] offset:11264\n" \
  "ds_read_b64_tr_b16 v[112:113], v28 offset:12288\n" \
  "ds_read_b64_tr_b16 v[114:115], v28 offset:12800\n" \
  "ds_read_b64_tr_b16 v[116:117], v28 offset:45056\n" \
  "ds_read_b64_tr_b16 v[118:119], v28 offset:45568\n" \
  "ds_read_b64_tr_b16 v[120:121], v17 offset:12288\n" \
  "ds_read_b64_tr_b16 v[122:123], v17 offset:12800\n" \
  "ds_read_b64_tr_b16 v[124:125], v17 offset:45056\n" \
  "ds_read_b64_tr_b16 v[126:127], v17 offset:45568\n" \
  "ds_read_b32 v31, v30 offset:0\n" \
  "s_waitcnt lgkmcnt(0)\n" \
  "s_barrier\n" \
  "ds_write_b128 v3, v[48:51] offset:12288\n" \
  "ds_write_b128 v3, v[52:55] offset:13312\n" \
  "ds_write_b128 v3, v[56:59] offset:14336\n" \
  "ds_write_b128 v3, v[60:63] offset:15360\n" \
  "v_mul_f32 v31, 0x3f8e353f, v31\n" \
  "v_mul_f32 v30, 0x44870000, v31\n" \
  "v_mov_b64 v[12:13], 0\n" \
  "v_mov_b64 v[14:15], 0\n" \
  "v_mov_b64 v[16:17], 0\n" \
  "v_mov_b64 v[18:19], 0\n" \
  "v_mov_b64 v[20:21], 0\n" \
  "v_mov_b64 v[22:23], 0\n" \
  "v_readfirstlane_b32 s72, v31\n" \
  "v_readfirstlane_b32 s86, v30\n" \
  "s_lshr_b32 s86, s86, 23\n" \
  "s_addk_i32 s86, 0xff83\n" \
  "s_ashr_i32 s86, s86, 1\n" \
  "s_max_i32 s86, s86, -20\n" \
  "s_min_i32 s86, s86, 100\n" \
  "s_mov_b32 s82, s86\n" \
  "s_lshr_b32 s83, s72, 23\n" \
  "s_sub_i32 s83, s83, 243\n" \
  "s_waitcnt lgkmcnt(0)\n" \
  "s_add_u32 s88, s74, 157760\n" \
  "v_mov_b32 v59, s88\n" \
  "s_cmp_lg_u32 s61, 0\n" \
  "s_movk_i32 s76, -8000\n" \
  "s_cmovk_i32 s76, 9152\n" \
  "s_movk_i32 s77, 90\n" \
  "s_cmovk_i32 s77, 90\n" \
  "s_cselect_b32 s72, -1, 55\n" \
  "s_mov_b32 s87, 0x40000\n" \
  "s_cmp_eq_u32 s90, 0\n" \
  "s_cbranch_scc1 .LCV_W1_%=\n" \
  "s_branch .LCV_W2_%=\n" \
  ".LCV_W0_%=:\n" \
  "s_sleep 2\n" \
  "s_cmp_eq_u32 s61, 0\n" \
  "s_cbranch_scc1 .LCV_W2_%=\n" \
  "s_sleep 32\n" \
  ".LCV_W2_%=:\n" \
  "s_mov_b64 exec, 15\n" \
  "buffer_load_dword v58, v2, s[44:47], s68 offen sc1\n" \
  "s_mov_b64 exec, -1\n" \
  "s_sub_u32 s87, s87, 1\n" \
  "s_cbranch_scc1 .LCV_END_%=\n" \
  "s_waitcnt vmcnt(0)\n" \
  "v_cmp_eq_u32 vcc, 0, v58\n" \
  "s_and_b32 vcc_lo, vcc_lo, 15\n" \
  "s_cbranch_scc1 .LCV_W0_%=\n" \
  "s_mov_b32 m0, s99\n" \
  "s_add_u32 s88, s99, 8192\n" \
  "global_load_lds_dwordx4 v10, s[62:63] sc1\n" \
  "global_load_lds_dwordx4 v10, s[62:63] offset:1024 sc1\n" \
  "s_mov_b32 m0, s88\n" \
  "s_nop 0\n" \
  "global_load_lds_dwordx4 v10, s[64:65] sc1\n" \
  "global_load_lds_dwordx4 v10, s[64:65] offset:1024 sc1\n" \
  ".LCV_W1_%=:\n" \
  "s_add_u32 s62, s62, 0x80000\n" \
  "s_addc_u32 s63, s63, 0\n" \
  "s_add_u32 s64, s64, 0x40000\n" \
  "s_addc_u32 s65, s65, 0\n" \
  "s_add_u32 s68, s68, 0x1000\n" \
  "s_cmp_eq_u32 s61, 0\n" \
  "s_cbranch_scc1 .LCV_W4_%=\n" \
  "s_add_u32 s38, s67, 4992\n" \
  "v_mov_b32 v58, s38\n" \
  "buffer_load_dword v58, v58, s[0:3], 0 offen sc1\n" \
  "s_waitcnt vmcnt(0)\n" \
  "v_readfirstlane_b32 vcc_lo, v58\n" \
  "s_movk_i32 s87, 200\n" \
  "s_cmp_eq_u32 vcc_lo, 0\n" \
  "s_cbranch_scc1 .LCV_W4_%=\n" \
  "s_add_u32 vcc_lo, vcc_lo, 590\n" \
  ".LCV_W3_%=:\n" \
  "s_sleep 2\n" \
  "s_memrealtime s[38:39]\n" \
  "s_sub_u32 s87, s87, 1\n" \
  "s_cbranch_scc1 .LCV_W4_%=\n" \
  "s_waitcnt lgkmcnt(0)\n" \
  "s_sub_u32 s38, s38, vcc_lo\n" \
  "s_cmp_lt_i32 s38, 0\n" \
  "s_cbranch_scc1 .LCV_W3_%=\n" \
  ".LCV_W4_%=:\n" \
  "s_waitcnt vmcnt(0)\n" \
  ".p2align 6\n" \
  ".LCV_TOP_%=:\n" \
  "s_waitcnt lgkmcnt(0)\n" \
  "s_barrier\n" \
  "s_setprio 3\n" \
  "ds_read_b128 v[24:27], v1 offset:0\n" \
  "ds_read_b128 v[28:31], v1 offset:1024\n" \
  "ds_read_b128 v[32:35], v1 offset:2048\n" \
  "ds_read_b128 v[36:39], v1 offset:3072\n" \
  "ds_read_b128 v[40:43], v1 offset:4096\n" \
  "ds_read_b128 v[44:47], v1 offset:5120\n" \
  "ds_read_b128 v[48:51], v1 offset:6144\n" \
  "ds_read_b128 v[52:55], v1 offset:7168\n" \
  "s_memrealtime s[38:39]\n" \
  "s_waitcnt lgkmcnt(7)\n" \
  "v_smfmac_f32_16x16x64_bf16 v[12:15], v[24:27], v[64:71], v0\n" \
  "s_waitcnt lgkmcnt(6)\n" \
  "v_smfmac_f32_16x16x64_bf16 v[12:15], v[28:31], v[72:79], v0\n" \
  "s_waitcnt lgkmcnt(5)\n" \
  "v_smfmac_f32_16x16x64_bf16 v[12:15], v[32:35], v[80:87], v0\n" \
  "s_waitcnt lgkmcnt(4)\n" \
  "v_smfmac_f32_16x16x64_bf16 v[12:15], v[36:39], v[88:95], v0\n" \
  "s_waitcnt lgkmcnt(3)\n" \
  "v_smfmac_f32_16x16x64_bf16 v[12:15], v[40:43], v[96:103], v0\n" \
  "s_waitcnt lgkmcnt(2)\n" \
  "v_smfmac_f32_16x16x64_bf16 v[12:15], v[44:47], v[104:111], v0\n" \
  "s_waitcnt lgkmcnt(1)\n" \
  "v_smfmac_f32_16x16x64_bf16 v[12:15], v[48:51], v[112:119], v0\n" \
  "s_waitcnt lgkmcnt(0)\n" \
  "v_smfmac_f32_16x16x64_bf16 v[12:15], v[52:55], v[120:127], v0\n" \
  "ds_read_b32 v11, v1 offset:26688\n" \
  "s_mov_b32 s61, s38\n" \
  "s_add_u32 s38, s38, s79\n" \
  "s_add_u32 s38, s38, s96\n" \
  "s_mov_b32 s96, 0\n" \
  "v_or_b32 v57, 1, s38\n" \
  "v_add_u32 v56, s67, v10\n" \
  "s_add_u32 s39, s67, s76\n" \
  "s_add_u32 s38, s38, s77\n" \
  "s_bitset1_b32 s38, 0\n" \
  "v_writelane_b32 v56, s39, 1\n" \
  "v_writelane_b32 v57, s38, 1\n" \
  "s_sub_u32 s39, s71, s72\n" \
  "s_cmp_lt_u32 s39, 9\n" \
  "s_cselect_b64 exec, 19, 3\n" \
  "buffer_store_dword v57, v56, s[0:3], 0 offen sc1\n" \
  "s_mov_b64 exec, -1\n" \
  "v_permlane32_swap_b32 v12, v14\n" \
  "v_permlane32_swap_b32 v13, v15\n" \
  "v_pk_add_f32 v[12:13], v[12:13], v[14:15]\n" \
  "s_nop 0\n" \
  "v_cvt_pk_bf16_f32 v12, v12, v13\n" \
  "ds_write_b32 v4, v12\n" \
  "s_setprio 2\n" \
  "v_mov_b64 v[12:13], s[92:93]\n" \
  "v_mov_b64 v[14:15], s[92:93]\n" \
  "s_waitcnt lgkmcnt(1)\n" \
  "v_readfirstlane_b32 s86, v11\n" \
  "s_lshr_b32 s86, s86, 23\n" \
  "s_add_i32 s86, s86, s83\n" \
  "s_ashr_i32 s86, s86, 1\n" \
  "s_max_i32 s86, s86, s82\n" \
  "s_min_i32 s86, s86, 100\n" \
  "s_lshl_b32 s86, s86, 23\n" \
  "s_sub_i32 s84, 0xc8800000, s86\n" \
  "s_cmp_eq_u32 s73, 5\n" \
  "v_smfmac_f32_16x16x64_bf16 v[16:19], v[24:27], v[128:135], v0\n" \
  "v_smfmac_f32_16x16x64_bf16 v[16:19], v[28:31], v[136:143], v0\n" \
  "v_smfmac_f32_16x16x64_bf16 v[16:19], v[32:35], v[144:151], v0\n" \
  "v_smfmac_f32_16x16x64_bf16 v[16:19], v[36:39], v[152:159], v0\n" \
  "v_smfmac_f32_16x16x64_bf16 v[16:19], v[40:43], v[160:167], v0\n" \
  "v_smfmac_f32_16x16x64_bf16 v[16:19], v[44:47], v[168:175], v0\n" \
  "s_waitcnt lgkmcnt(0)\n" \
  "s_barrier\n" \
  "ds_read_b64_tr_b16 v[60:61], v6 offset:0\n" \
  "ds_read_b64_tr_b16 v[62:63], v9 offset:0\n" \
  "v_smfmac_f32_16x16x64_bf16 v[16:19], v[48:51], v[176:183], v0\n" \
  "v_smfmac_f32_16x16x64_bf16 v[16:19], v[52:55], v[184:191], v0\n" \
  "v_smfmac_f32_16x16x64_bf16 v[20:23], v[24:27], v[192:199], v0\n" \
  "v_smfmac_f32_16x16x64_bf16 v[20:23], v[28:31], v[200:207], v0\n" \
  "v_smfmac_f32_16x16x64_bf16 v[20:23], v[32:35], v[208:215], v0\n" \
  "v_smfmac_f32_16x16x64_bf16 v[20:23], v[36:39], v[216:223], v0\n" \
  "v_smfmac_f32_16x16x64_bf16 v[20:23], v[40:43], v[224:231], v0\n" \
  "v_smfmac_f32_16x16x64_bf16 v[20:23], v[44:47], v[232:239], v0\n" \
  "v_smfmac_f32_16x16x64_bf16 v[20:23], v[48:51], v[240:247], v0\n" \
  "v_smfmac_f32_16x16x64_bf16 v[20:23], v[52:55], v[248:255], v0\n" \
  "s_cmp_eq_u32 s71, 0\n" \
  "v_permlane32_swap_b32 v16, v18\n" \
  "v_permlane32_swap_b32 v17, v19\n" \
  "v_add_f32 v16, v16, v18\n" \
  "v_add_f32 v17, v17, v19\n" \
  "v_cvt_pk_bf16_f32 v16, v16, v17\n" \
  "s_waitcnt lgkmcnt(0)\n" \
  "v_max_u32 v60, 1, v60\n" \
  "v_max_u32 v61, 1, v61\n" \
  "v_max_u32 v62, 1, v62\n" \
  "v_max_u32 v63, 1, v63\n" \
  "buffer_store_dwordx4 v[60:63], v7, s[48:51], s69 offen sc1\n" \
  "v_permlane32_swap_b32 v20, v22\n" \
  "v_permlane32_swap_b32 v21, v23\n" \
  "v_pk_add_f32 v[20:21], v[20:21], v[22:23]\n" \
  "v_max_u32 v18, 1, v16\n" \
  "v_cvt_pk_bf16_f32 v17, v20, v21\n" \
  "ds_write_b64 v5, v[16:17]\n" \
  "v_max_u32 v19, 1, v17\n" \
  "s_nop 1\n" \
  "v_permlane32_swap_b32 v18, v19\n" \
  "buffer_store_dwordx2 v[18:19], v8, s[52:55], s70 offen sc1\n" \
  "ds_read_b128 v[24:27], v3 offset:0\n" \
  "ds_read_b128 v[28:31], v3 offset:1024\n" \
  "ds_read_b128 v[32:35], v3 offset:2048\n" \
  "ds_read_b128 v[36:39], v3 offset:3072\n" \
  "ds_read_b128 v[40:43], v3 offset:8192\n" \
  "ds_read_b128 v[44:47], v3 offset:9216\n" \
  "ds_read_b128 v[48:51], v3 offset:10240\n" \
  "ds_read_b128 v[52:55], v3 offset:11264\n" \
  "s_cbranch_scc1 .LCV_LAST_%=\n" \
  "s_cmp_eq_u32 s73, 5\n" \
  "s_cbranch_scc0 .LCV_NMK_%=\n" \
  "s_mov_b32 m0, s78\n" \
  "s_nop 0\n" \
  "buffer_load_dword v2, s[0:3], s66 offen sc1 lds\n" \
  ".LCV_NMK_%=:\n" \
  "s_setprio 0\n" \
  "s_waitcnt lgkmcnt(8)\n" \
  "s_barrier\n" \
  "s_add_u32 s67, s67, 1024\n" \
  "v_mov_b32 v11, 0\n" \
  "ds_write_b32 v59, v11\n" \
  "ds_read_b128 v[56:59], v3 offset:4096\n" \
  "ds_read_b128 v[60:63], v3 offset:5120\n" \
  "v_mov_b64 v[20:21], s[92:93]\n" \
  "v_mov_b64 v[22:23], s[92:93]\n" \
  "v_mov_b64 v[16:17], s[92:93]\n" \
  "v_mov_b64 v[18:19], s[92:93]\n" \
  "s_mov_b32 m0, s94\n" \
  "s_mov_b64 exec, 15\n" \
  "buffer_load_dword v2, s[44:47], s68 offen sc1 lds\n" \
  "s_mov_b64 exec, -1\n" \
  "s_setprio 2\n" \
  "s_waitcnt lgkmcnt(0)\n" \
  "s_barrier\n" \
  "ds_read_b128 v[4:7], v1 offset:8192\n" \
  "ds_read_b128 v[8:11], v1 offset:9216\n" \
  "s_waitcnt lgkmcnt(1)\n" \
  "v_smfmac_f32_16x16x64_bf16 v[12:15], v[4:7], v[24:31], v0\n" \
  "ds_read_b128 v[24:27], v3 offset:6144\n" \
  "ds_read_b128 v[28:31], v3 offset:7168\n" \
  "v_smfmac_f32_16x16x64_bf16 v[16:19], v[4:7], v[32:39], v0\n" \
  "ds_read_b128 v[32:35], v3 offset:12288\n" \
  "ds_read_b128 v[36:39], v3 offset:13312\n" \
  "s_waitcnt lgkmcnt(4)\n" \
  "v_smfmac_f32_16x16x64_bf16 v[12:15], v[8:11], v[40:47], v0\n" \
  "v_smfmac_f32_16x16x64_bf16 v[16:19], v[8:11], v[48:55], v0\n" \
  "v_smfmac_f32_16x16x64_bf16 v[20:23], v[4:7], v[56:63], v0\n" \
  "ds_read_b128 v[56:59], v3 offset:14336\n" \
  "ds_read_b128 v[60:63], v3 offset:15360\n" \
  "v_mov_b64 v[40:41], s[92:93]\n" \
  "v_mov_b64 v[42:43], s[92:93]\n" \
  "s_nop 2\n" \
  "v_permlane32_swap_b32 v12, v17\n" \
  "v_permlane32_swap_b32 v13, v16\n" \
  "v_permlane32_swap_b32 v14, v19\n" \
  "v_permlane32_swap_b32 v15, v18\n" \
  "v_pk_add_f32 v[12:13], v[12:13], v[16:17] op_sel:[0,1] op_sel_hi:[1,0]\n" \
  "v_pk_add_f32 v[14:15], v[14:15], v[18:19] op_sel:[0,1] op_sel_hi:[1,0]\n" \
  "buffer_atomic_add_x2 v[12:13], v2, s[40:43], s56 offen\n" \
  "buffer_atomic_add_x2 v[14:15], v2, s[40:43], s57 offen\n" \
  "s_waitcnt lgkmcnt(2)\n" \
  "v_smfmac_f32_16x16x64_bf16 v[20:23], v[8:11], v[32:39], v0\n" \
  "v_smfmac_f32_16x16x64_bf16 v[40:43], v[4:7], v[24:31], v0\n" \
  "s_waitcnt lgkmcnt(0)\n" \
  "v_smfmac_f32_16x16x64_bf16 v[40:43], v[8:11], v[56:63], v0\n" \
  "s_nop 7\n" \
  "v_permlane32_swap_b32 v20, v41\n" \
  "v_permlane32_swap_b32 v21, v40\n" \
  "v_permlane32_swap_b32 v22, v43\n" \
  "v_permlane32_swap_b32 v23, v42\n" \
  "v_pk_add_f32 v[20:21], v[20:21], v[40:41] op_sel:[0,1] op_sel_hi:[1,0]\n" \
  "v_pk_add_f32 v[22:23], v[22:23], v[42:43] op_sel:[0,1] op_sel_hi:[1,0]\n" \
  "buffer_atomic_add_x2 v[20:21], v2, s[40:43], s58 offen\n" \
  "buffer_atomic_add_x2 v[22:23], v2, s[40:43], s59 offen\n" \
  "s_setprio 0\n" \
  "s_memrealtime s[38:39]\n" \
  "v_mbcnt_lo_u32_b32 v48, -1, 0\n" \
  "v_mbcnt_hi_u32_b32 v48, -1, v48\n" \
  "s_add_u32 s88, s74, 157760\n" \
  "v_mov_b32 v59, s88\n" \
  "v_lshl_add_u32 v52, v48, 2, v59\n" \
  "ds_read_b32 v52, v52 offset:192\n" \
  "v_lshrrev_b32 v49, 3, v48\n" \
  "v_and_b32 v50, 7, v48\n" \
  "v_lshlrev_b32 v50, 4, v50\n" \
  "v_lshl_or_b32 v44, v49, 8, v50\n" \
  "s_mov_b32 s87, 0x40000\n" \
  "v_lshlrev_b32 v10, 4, v48\n" \
  "v_lshl_add_u32 v58, v48, 2, s94\n" \
  "s_waitcnt vmcnt(4)\n" \
  "ds_read_b32 v58, v58\n" \
  "s_waitcnt lgkmcnt(0)\n" \
  "v_cmp_eq_u32 vcc, 0, v58\n" \
  "s_and_b32 vcc_lo, vcc_lo, 15\n" \
  "s_cbranch_scc0 .LCV_GOT_%=\n" \
  "s_branch .LCV_ASK_%=\n" \
  ".LCV_LATE_%=:\n" \
  "v_cmp_eq_u32 vcc, 0, v58\n" \
  "s_and_b32 vcc_lo, vcc_lo, 15\n" \
  "s_cbranch_scc0 .LCV_GOT_%=\n" \
  "s_sleep 12\n" \
  ".LCV_ASK_%=:\n" \
  "s_mov_b64 exec, 15\n" \
  "buffer_load_dword v58, v2, s[44:47], s68 offen sc1\n" \
  "s_mov_b64 exec, -1\n" \
  "s_sub_u32 s87, s87, 1\n" \
  "s_cbranch_scc1 .LCV_END_%=\n" \
  "s_waitcnt vmcnt(0)\n" \
  "s_branch .LCV_LATE_%=\n" \
  ".LCV_GOT_%=:\n" \
  "s_mov_b32 m0, s99\n" \
  "s_add_u32 s88, s99, 8192\n" \
  "global_load_lds_dwordx4 v10, s[62:63] sc1\n" \
  "global_load_lds_dwordx4 v10, s[62:63] offset:1024 sc1\n" \
  "s_mov_b32 m0, s88\n" \
  "s_nop 0\n" \
  "global_load_lds_dwordx4 v10, s[64:65] sc1\n" \
  "global_load_lds_dwordx4 v10, s[64:65] offset:1024 sc1\n" \
  ".LCV_HAVE_%=:\n" \
  "s_waitcnt lgkmcnt(0)\n" \
  "s_sub_u32 s61, s38, s61\n" \
  "s_add_u32 s79, s79, 2\n" \
  "s_min_u32 s79, s79, s61\n" \
  "s_sub_u32 s39, s38, 150\n" \
  "v_subrev_u32 v52, s39, v52\n" \
  "v_cmp_lt_u32 vcc, 0x1000, v52\n" \
  "s_cmp_eq_u64 vcc, 0\n" \
  "s_cbranch_scc1 .LCV_OKM_%=\n" \
  ".LCV_AGN_%=:\n" \
  "buffer_load_dword v52, v2, s[0:3], s66 offen sc1\n" \
  "s_sub_u32 s87, s87, 1\n" \
  "s_cbranch_scc1 .LCV_END_%=\n" \
  "s_waitcnt vmcnt(0)\n" \
  "v_cmp_eq_u32 vcc, 0, v52\n" \
  "v_subrev_u32 v52, s39, v52\n" \
  "s_cmp_eq_u64 vcc, 0\n" \
  "s_cbranch_scc1 .LCV_OKM_%=\n" \
  "s_sleep 4\n" \
  "s_branch .LCV_AGN_%=\n" \
  ".LCV_OKM_%=:\n" \
  "s_nop 1\n" \
  "v_max_i32_dpp v52, v52, v52 row_ror:1 row_mask:0xf bank_mask:0xf\n" \
  "s_nop 1\n" \
  "v_max_i32_dpp v52, v52, v52 row_ror:2 row_mask:0xf bank_mask:0xf\n" \
  "s_nop 1\n" \
  "v_max_i32_dpp v52, v52, v52 row_ror:4 row_mask:0xf bank_mask:0xf\n" \
  "s_nop 1\n" \
  "v_max_i32_dpp v52, v52, v52 row_ror:8 row_mask:0xf bank_mask:0xf\n" \
  "s_movk_i32 vcc_lo, 150\n" \
  "v_readlane_b32 s88, v52, 0\n" \
  "s_max_i32 vcc_lo, vcc_lo, s88\n" \
  "v_readlane_b32 s88, v52, 16\n" \
  "s_max_i32 vcc_lo, vcc_lo, s88\n" \
  "v_readlane_b32 s88, v52, 32\n" \
  "s_max_i32 vcc_lo, vcc_lo, s88\n" \
  "v_readlane_b32 s88, v52, 48\n" \
  "s_max_i32 vcc_lo, vcc_lo, s88\n" \
  "s_add_u32 s66, s66, 1024\n" \
  "s_add_u32 vcc_lo, vcc_lo, s39\n" \
  "s_add_u32 vcc_lo, vcc_lo, 23\n" \
  ".LCV_TW_%=:\n" \
  "s_memrealtime s[38:39]\n" \
  "s_waitcnt lgkmcnt(0)\n" \
  "s_sub_u32 s38, s38, vcc_lo\n" \
  "s_cmp_lt_i32 s38, 0\n" \
  "s_cbranch_scc1 .LCV_TW_%=\n" \
  "buffer_load_dwordx4 v[24:27], v44, s[40:43], s60 offen sc1\n" \
  "buffer_load_dwordx4 v[28:31], v44, s[40:43], s60 offen offset:2048 sc1\n" \
  "s_add_u32 s62, s62, 0x80000\n" \
  "s_addc_u32 s63, s63, 0\n" \
  "s_add_u32 s64, s64, 0x40000\n" \
  "s_addc_u32 s65, s65, 0\n" \
  "s_add_u32 s68, s68, 0x1000\n" \
  "s_add_u32 s69, s69, 0x10000\n" \
  "s_add_u32 s70, s70, 0x10000\n" \
  "s_sub_u32 s88, s73, 4\n" \
  "v_lshrrev_b32 v49, 2, v48\n" \
  "v_lshrrev_b32 v50, 4, v48\n" \
  "v_bitop3_b32 v50, v49, v50, 3 bitop3:0x28\n" \
  "v_lshl_add_u32 v4, v50, 3, s97\n" \
  "v_and_b32 v50, 3, v48\n" \
  "v_lshl_add_u32 v4, v50, 5, v4\n" \
  "v_and_b32 v49, 1, v49\n" \
  "v_lshl_add_u32 v4, v49, 8, v4\n" \
  "v_and_b32 v49, 8, v48\n" \
  "v_lshl_add_u32 v4, v49, 4, v4\n" \
  "v_and_b32 v51, 15, v48\n" \
  "v_bfe_u32 v49, v48, 4, 1\n" \
  "v_lshrrev_b32 v50, 5, v48\n" \
  "s_lshl_b32 s89, s88, 10\n" \
  "s_add_u32 s89, s89, s74\n" \
  "s_add_u32 s89, s89, 158720\n" \
  "v_lshrrev_b32 v9, 3, v51\n" \
  "v_mul_u32_u24 v6, 41, v9\n" \
  "v_xor_b32 v6, v6, v51\n" \
  "v_lshl_add_u32 v6, v6, 3, s89\n" \
  "v_lshl_add_u32 v6, v49, 6, v6\n" \
  "v_lshl_add_u32 v6, v50, 9, v6\n" \
  "v_xor_b32 v9, 0x90, v6\n" \
  "v_lshlrev_b32 v8, 4, v49\n" \
  "v_lshl_add_u32 v8, v51, 5, v8\n" \
  "v_lshl_add_u32 v8, v50, 3, v8\n" \
  "s_lshl_b32 s89, s88, 8\n" \
  "s_add_u32 s89, s89, s74\n" \
  "s_add_u32 s89, s89, 139264\n" \
  "v_and_b32 v49, 31, v48\n" \
  "v_lshl_add_u32 v5, v49, 3, s89\n" \
  "v_lshl_add_u32 v5, v50, 10, v5\n" \
  "v_lshrrev_b32 v49, 2, v51\n" \
  "v_and_b32 v50, 5, v51\n" \
  "v_and_or_b32 v49, v49, 2, v50\n" \
  "v_and_b32 v50, 48, v48\n" \
  "v_lshlrev_b32 v7, 3, v50\n" \
  "v_lshl_add_u32 v7, v49, 4, v7\n" \
  "v_bitop3_b32 v49, v51, 2, 2 bitop3:0x0c\n" \
  "v_lshl_add_u32 v7, v49, 29, v7\n" \
  "v_mov_b64 v[12:13], 0\n" \
  "v_mov_b64 v[14:15], 0\n" \
  "v_mov_b64 v[16:17], 0\n" \
  "v_mov_b64 v[18:19], 0\n" \
  "v_mov_b64 v[20:21], 0\n" \
  "v_mov_b64 v[22:23], 0\n" \
  "v_and_b32 v50, 3, v48\n" \
  "v_lshl_add_u32 v40, v50, 2, s98\n" \
  "v_lshrrev_b32 v50, 4, v48\n" \
  "v_mad_u32_u24 v40, v50, s95, v40\n" \
  "v_bfe_u32 v49, v48, 3, 1\n" \
  "v_lshl_add_u32 v40, v49, 6, v40\n" \
  "v_bfe_u32 v49, v48, 2, 1\n" \
  "v_lshl_add_u32 v40, v49, 7, v40\n" \
  "v_xor_b32 v41, 16, v40\n" \
  "v_xor_b32 v42, 32, v40\n" \
  "v_xor_b32 v43, 48, v40\n" \
  "s_add_i32 s80, s86, 0x35000000\n" \
  "v_mov_b32 v11, 0\n" \
  "s_mov_b32 s89, s60\n" \
  "s_bitcmp1_b32 s60, 7\n" \
  "s_cselect_b32 s88, 0x10000, 0\n" \
  "s_bitset1_b32 s88, 7\n" \
  "s_xor_b32 s56, s56, s88\n" \
  "s_xor_b32 s57, s57, s88\n" \
  "s_xor_b32 s58, s58, s88\n" \
  "s_xor_b32 s59, s59, s88\n" \
  "s_xor_b32 s60, s60, s88\n" \
  "s_sub_u32 s71, s71, 1\n" \
  ".LCV_SEE_%=:\n" \
  "s_waitcnt vmcnt(1)\n" \
  "v_cvt_f32_i32 v24, v24\n" \
  "v_cvt_f32_i32 v25, v25\n" \
  "v_cvt_f32_i32 v26, v26\n" \
  "v_cvt_f32_i32 v27, v27\n" \
  "v_max_f32 v45, |v24|, |v26|\n" \
  "v_pk_mul_f32 v[24:25], v[24:25], s[80:81] op_sel_hi:[1,0]\n" \
  "v_pk_mul_f32 v[26:27], v[26:27], s[80:81] op_sel_hi:[1,0]\n" \
  "s_nop 0\n" \
  "v_cvt_pk_bf16_f32 v32, v24, v26\n" \
  "v_cvt_pk_bf16_f32 v33, v25, v27\n" \
  "ds_write_b32 v40, v32\n" \
  "ds_write_b32 v41, v33\n" \
  "s_waitcnt vmcnt(0)\n" \
  "v_cvt_f32_i32 v28, v28\n" \
  "v_cvt_f32_i32 v30, v30\n" \
  "v_cvt_f32_i32 v29, v29\n" \
  "v_max3_f32 v45, v45, |v28|, |v30|\n" \
  "v_cvt_f32_i32 v31, v31\n" \
  "v_cmp_lt_f32 vcc, s91, v45\n" \
  "v_pk_mul_f32 v[28:29], v[28:29], s[80:81] op_sel_hi:[1,0]\n" \
  "v_pk_mul_f32 v[30:31], v[30:31], s[80:81] op_sel_hi:[1,0]\n" \
  "s_cmp_eq_u64 vcc, 0\n" \
  "s_cbranch_scc0 .LCV_POLL_%=\n" \
  "v_cvt_pk_bf16_f32 v36, v28, v30\n" \
  "v_cvt_pk_bf16_f32 v37, v29, v31\n" \
  "ds_write_b32 v42, v36\n" \
  "ds_write_b32 v43, v37\n" \
  "v_dot2c_f32_bf16 v11, v32, v32\n" \
  "v_dot2c_f32_bf16 v11, v33, v33\n" \
  "v_dot2c_f32_bf16 v11, v36, v36\n" \
  "v_dot2c_f32_bf16 v11, v37, v37\n" \
  "s_nop 1\n" \
  "s_nop 0\n" \
  "v_cmp_lt_f32 vcc, 4.0, v11\n" \
  "s_cbranch_vccz .LCV_TOP_%=\n" \
  "s_mov_b64 exec, vcc\n" \
  "ds_max_u32 v59, v11\n" \
  "s_mov_b64 exec, -1\n" \
  "s_branch .LCV_TOP_%=\n" \
  ".LCV_POLL_%=:\n" \
  "buffer_load_dwordx4 v[24:27], v44, s[40:43], s89 offen sc1\n" \
  "buffer_load_dwordx4 v[28:31], v44, s[40:43], s89 offen offset:2048 sc1\n" \
  "s_sub_u32 s87, s87, 1\n" \
  "s_cbranch_scc1 .LCV_END_%=\n" \
  "s_branch .LCV_SEE_%=\n" \
  ".LCV_LAST_%=:\n" \
  "s_waitcnt vmcnt(0)\n" \
  ".LCV_END_%=:\n" \
  "s_endpgm\n"
__device__ __forceinline__ void start_kgen(const MegaArgs& A, char* smem, const int kg) {
  const int w = A.wv, l = lane_id(), tid = 64 * w + l, c = l & 15, g = l >> 4;
  KGen G;
  unsigned tp = 0u;
#define TEXT_ASK()                                                                                                     \
  {                                                                                                                    \
    const char* pc;                                                                                                    \
    unsigned n;                                                                                                        \
    asm volatile("s_getpc_b64 %0\n.LKG_PC:\n\ts_mov_b32 %1, .LKG_LAST-.LKG_PC" : "=s"(pc), "=s"(n));                    \
    const unsigned o = (unsigned)(tid * 8 + ((blockIdx.x >> 3) & 7)) * IPS;                                            \
    if ((int)o < (int)n) asm volatile("global_load_dword %0, %1, %2" : "=v"(tp) : "v"(o), "s"(pc) : "memory");         \
  }
  if (IPF == 2) TEXT_ASK()
  v4u raw[2] = {};
#pragma unroll
  for (int i = 0; i < TP; ++i)
    if (kg < 32) raw[i] = *reinterpret_cast<const v4u*>(A.x0 + (size_t)(TP * (8 * (kg & 7) + w) + i) * DM + l * 8);
  G.load(A, 8 * kg + w, l);
  {
    const __amdgpu_buffer_rsrc_t rz = MKRS(A.ws + OFF_Z, Z_BYTES);
#pragma unroll
    for (int i = 0; i < 4; ++i) {
      const u64 z = i == 0 && kg < (int)(2 * SZ_ACC / 8192) ? ACC_INIT : 0ull;
      ST128((v4u{(unsigned)z, (unsigned)(z >> 32), (unsigned)z, (unsigned)(z >> 32)}), rz, tid * 16, (i * 64 + kg) * 8192);
    }
  }
  const u64 tag = call_tag(A);
  if (TP && kg < 32) {
    unsigned t = row_stats(raw[0]);
    if (TP > 1) {
      const unsigned t1 = row_stats(raw[1]);
      t = l < 2 ? t : t1;
    }
    if (l < 2 * TP) __hip_atomic_store((gu64*)(A.ws + OFF_GT) + 2 * (L_SEQ + TP * (8 * kg + w)) + l, tagged(tag, (l & 1) ? t : t << 16), RLX_AGENT);
  }
  DRAIN();
  __syncthreads();
  if (tid < 8) __hip_atomic_store((gu64*)(A.ws + OFF_RD) + tid * 64 + kg, tag, RLX_AGENT);
  asm volatile("" : "+v"(G.pa), "+v"(G.pb), "+v"(G.pe));
  G.setup();
  G.st = smem + w * 16640;
  const __amdgpu_buffer_rsrc_t rs = MKRS(A.ws + OFF_KF + (8 * kg + w) * 2048, 386 * 4);
  SST(1);
  unsigned tw = 0u;
#define TLB_ASK()                                                                                                      \
  if (tid < 384) {                                                                                                     \
    const unsigned o = (unsigned)OFF_XA + (unsigned)(tid * 8 + ((blockIdx.x >> 3) & 7)) * 65536u;                      \
    if (o < WS_TOTAL) asm volatile("global_load_dword %0, %1, %2" : "=v"(tw) : "v"(o), "s"(A.ws) : "memory");          \
  }
#define X2_CLEAR() \
  _Pragma("unroll") for (int i = 0; i < 4; ++i) ST128((v4u{0u, 0u, 0u, 0u}), MKRS(A.ws + OFF_X2, SZ_X), tid * 16, (4 * kg + i) * 8192);
  if (XZP == 1) { X2_CLEAR() }
  if (IPF == 1) TEXT_ASK()
  if (TLBW == 1) TLB_ASK()
  unsigned pf[6] = {};
#define PF_ASK(M)                                                                                                                  \
  if (kg < 32) {                                                                                                                   \
    if ((M) & 1) asm volatile("global_load_dword %0, %2, %3\n\tglobal_load_dword %1, %2, %3 offset:128" : "=&v"(pf[0]), "=&v"(pf[1]) : "v"(kg * 131072 + tid * 256), "s"(A.Wo)); \
    if ((M) & 2) asm volatile("global_load_dword %0, %2, %3\n\tglobal_load_dword %1, %2, %3 offset:128" : "=&v"(pf[2]), "=&v"(pf[3]) : "v"(kg * 256 + tid * 8192), "s"(A.Wq)); \
    if ((M) & 4) asm volatile("global_load_dword %0, %2, %3\n\tglobal_load_dword %1, %2, %3 offset:128" : "=&v"(pf[4]), "=&v"(pf[5]) : "v"(kg * 256 + tid * 8192), "s"(A.Wk)); \
  }
  PF_ASK(PFA)
  G.columns(l, 0, 16);
  G.get(G.bh, G.bl, l);
  if (XZP == 2) { X2_CLEAR() }
  PF_ASK(PFB)
  if (TLBW == 2) TLB_ASK()
  (void)G.scale();
  const Cx zb = G.power(256.f);
  Cx b = G.C, z = G.power(2.f);
#pragma unroll 1
  for (int f = 1; f < 9;) {
    char* p = G.st + l * 8;
    const int fe = f == 1 ? 4 : 9;
#pragma unroll 1
    for (; f < fe; ++f) {
      b = cmul(b, zb);
      if (f == 2 || f == 4) z = cmul(z, z);
      Cx y = b;
      p = G.rows(p, y, z, f == 1 ? 8 : f < 4 ? 4 : 2);
    }
    const f32x4 d = G.tile<false>(l);
    const v4u r = {pkh(d.x, d.x), pkh(d.y, d.y), pkh(d.z, d.z), pkh(d.w, d.w)};
    if (f == 4) {
      ST128(r, rs, (g < 2 ? 8 * c + 4 * g : 64 * g + 4 * c) * 4, 0);
    } else {
      ST64((v2u{r.x, r.y}), rs, (256 + 64 * g + 2 * c) * 4, 0);
      ST64((v2u{r.z, r.w}), rs, (288 + 64 * g + 2 * c) * 4, 0);
    }
  }
  SST(2);
  DRAIN();
  asm volatile("" ::"v"(tp), "v"(tw), "v"(pf[0]), "v"(pf[1]), "v"(pf[2]), "v"(pf[3]), "v"(pf[4]), "v"(pf[5]));
  if (l == 0) __hip_atomic_store((gu64*)(A.ws + OFF_KG) + kg * 16 + w, tag, RLX_AGENT);
  if (XZP == 3) { X2_CLEAR() }
  const u64 rv = (XZP & 3) && w == 0 ? ready_look(A) : 0;
  if (TLBW == 3) {
    TLB_ASK()
    DRAIN();
    asm volatile("" ::"v"(tw));
  }
  DRAIN();
  __syncthreads();
  if ((XZP & 3) && w == 0) {
    ready_wait(A, tag, rv);
    if (l < 8) set_flag((unsigned*)(A.ws + OFF_FL) + FL_FZ + l * 64 + kg);
  }
  SST(3);
}

constexpr int C_SM = 131072, C_SO = C_SM + 8192, C_KT = C_SO + 2048, C_X = C_KT + 16384, C_SQ = C_X + 1024;
static_assert(C_SQ + 4096 <= 163840 && C_SQ % 512 == 0, "lds");
__device__ __forceinline__ v4u mk_srd(const void* p, const unsigned bytes) {
  const unsigned long long a = (unsigned long long)p;
  return v4u{(unsigned)a, (unsigned)(a >> 32) & 0xffffu, bytes, 0x00020000u};
}
template <bool ATT>
__device__ __forceinline__ void role_chain_t(const MegaArgs& A, char* smem, const int L, const int h, const int rep) {
  const int w = A.wv;
  char* ws = A.ws;
  unsigned* FL = (unsigned*)(ws + OFF_FL);
  const int l = lane_id(), c = l & 15, g = l >> 4;
  unsigned* FQ = FL + FL_FQ + L * 64 * 32 * 32;
  const int wb = (w + ROT_H) & 7, tb = ROT_T;
  const unsigned sb = lds_base(smem);
  glds16(A.mem0, (unsigned)((c & 7) * DM + 64 * w + 16 * g + 8 * (c >> 3)) * 2, sb + C_SM + w * 1024);
#pragma unroll
  for (int i = 0; i < 16; ++i)
    glds16((const char*)A.Wo + (size_t)(h * DH + 32 * (i >> 2) + 4 * w + (i & 2)) * (DM * 2),
           (unsigned)((i & 1) * 1024 + ((l ^ (((i & 1) | ((w & 3) << 1)) << 1)) << 4)), sb + w * 16384 + i * 1024, WNT & 1);
  const u64 tag = call_tag(A);
  const char* gK = ws + OFF_QK + L * SZ_QK + ((size_t)h * 16 + 2 * w) * 1024;
  const char* gV = ws + OFF_VF + L * SZ_O + ((size_t)h * 8 + 2 * w - 8) * 1024;
  v8u pv, pw;
  {
    const unsigned voff = g * 256 + c * 8, wp = sb + w * 16384 + l * 16;
    pv[0] = (c & 8) ? 0xEEEE : 0x4444;
    pv[1] = sb + C_SM + l * 16;
    const unsigned ra = sb + C_SQ + (g >> 1) * 512 + (c ^ ((g & 1) * 18)) * 8;
    pv[2] = ATT ? ra : voff;
    pv[3] = ATT ? ra ^ 0x108 : wp;
    pv[4] = sb + ((c & 2) ? C_SQ : C_SO) + w * 256 + (((c & 5) | ((c >> 2) & 2)) + 8 * (g >> 1)) * 16 + 8 * ((g ^ (c >> 1)) & 1);
    pv[5] = voff;
    pv[6] = wp;
    pv[7] = sb + C_SO + (l >> 5) * 1024 + w * 256 + (l & 31) * 8;
    const int q = (l >> 2) & 3, p = l & 3;
    const int f = (q & 1) | ((g & 1) << 1) | ((q >> 1) << 2);
#pragma unroll
    for (int i = 0; i < 4; ++i) pw[i] = (4 * i + g) * (HD * 2) + ((c ^ (((g & 1) | (i << 1)) << 1)) << 4) - 1024 * i;
    pw[4] = sb + (g >> 1) * 16384 + (4 * (g & 1) + (q & 1) + 8 * (q >> 1)) * 256 + (w ^ f) * 32 + p * 8;
    pw[5] = sb + (4 * (g >> 1) + 2 * (q >> 1) + (g & 1)) * 16384 + (q & 1) * 1024 + p * 8 + ((4 * wb) ^ f ^ tb) * 32;
    pw[6] = pw[7] = 0u;
  }
  const size_t wo = (size_t)(16 * w) * HD + h * DH;
#define POL 2
#define PG(w) (POL == 2 ? (w) * 4096 : ((w) & 1) * 4096 + ((w) >> 1) * 16384)
  const v4u rsA = mk_srd(ws + OFF_ACC + L * SZ_ACC + (POL == 1 ? 8192 : 0), SZ_ACC - 8192);
  const v4u rsF = mk_srd(FQ, 64 * 32 * 32 * 4);
  const v4u rsMK = mk_srd(ws + OFF_MEMK + L * SZ_MEM, SZ_MEM);
  const v4u rsMV = mk_srd(ws + OFF_MEMV + L * SZ_MEM, SZ_MEM);
  const v4u pAB = (v4u{0u, 2048u, 3072u, 1024u} ^ (1536u * tb)) + PG(wb);
  const v4u rsT = mk_srd(FL + FL_FK + L * 32, 64 * 1024 - 128);
  unsigned sc[8];
  sc[0] = h * 128;
  sc[1] = (h * 4 + w - 4) * 512;
  sc[2] = (h * 4 + w - 4) * 512;
  sc[3] = NSEG - 1;
  sc[5] = w;
  sc[6] = sb;
  const char* dbg = ws;
#define CHAIN_OPS                                                                                                          \
  ::"{v[0:7]}"(pv), "{v[24:31]}"(pw), "{s[4:5]}"(A.Wv + wo), "{s[6:7]}"(A.Wq + wo), "{s[8:9]}"(A.Wk + wo), "{s[10:11]}"(tag),         \
      "{s[12:13]}"(ws + OFF_RD + (blockIdx.x & 7) * 512), "{s[14:15]}"(ws + OFF_G + (blockIdx.x & 7) * 256), "{s[16:17]}"(ws + OFF_G + h * 8), \
      "{s18}"(w == 4 && L == 0 ? 255 : 0), "{s19}"(sb + w * 16384), "{s[20:21]}"(ws + WS_TOTAL + (w == 0 ? 534 * 256 + A.vb : 610 * 256) * 8), \
      "{s[40:43]}"(rsA), "{s[44:47]}"(rsF), "{s[48:51]}"(rsMK), "{s[52:55]}"(rsMV), "{s[56:59]}"(pAB), "{s60}"(PG(w)), "{s61}"(ATT ? PG(h >> 2) + (h & 3) * 1024 : L), \
      "{s[62:63]}"(gK), "{s[64:65]}"(gV), "{s66}"(0), "{s67}"(w == 4 ? (h >> 3) * 256 + (h & 7) * 8 : 0x40000000), "{s68}"(sc[0]), "{s69}"(sc[1]), "{s70}"(sc[2]),    \
      "{s71}"(sc[3]), "{s73}"(sc[5]), "{s74}"(sc[6]), "{s[36:37]}"(dbg),                   \
      "{s[0:3]}"(rsT)                                                                                                      \
      : "memory", "vcc", "scc", "s99", "v255"
  if (ATT) asm volatile("s_mov_b64 exec, -1\ns_nop 4\n" CHAIN_ASM_ATT ".LKG_LAS2:\n" CHAIN_OPS);
  else asm volatile("s_mov_b64 exec, -1\ns_nop 4\n" CHAIN_ASM_VO CHAIN_OPS);
#undef CHAIN_OPS
}

__device__ __forceinline__ void role_chain(const MegaArgs& A, char* smem, const int L, const int h, const int rep) {
  if (A.wv < 4) role_chain_t<true>(A, smem, L, h, rep);
  else role_chain_t<false>(A, smem, L, h, rep);
}

constexpr int AW_SO = 0, AW_TK = 32768, AW_CNT = AW_TK + 98304;
__device__ __forceinline__ unsigned umin3(unsigned a, unsigned b, unsigned c) {
  return __builtin_elementwise_min(__builtin_elementwise_min(a, b), c);
}
#define PW_FREE(L_, s_, i_) \
  if (l == 0) set_flag(FL + FL_FD + ((s_) * 2 + (L_)) * 32 + (i_))
#define AW_ASM_X \
  "s_mov_b32 s61, 0\n" \
  "s_mov_b32 s67, 1\n" \
  "s_mov_b32 s68, 0\n" \
  "s_mov_b32 s70, 0\n" \
  "s_mov_b32 s73, 0\n" \
  "s_mov_b32 s71, 0x3e0293ee\n" \
  "s_mov_b32 s72, 0xbe0293ee\n" \
  "s_mov_b32 s65, 49152\n" \
  "s_bitcmp0_b32 s58, 2\n" \
  "s_cbranch_scc1 .LCX_OWN_%=\n" \
  "v_swap_b32 v192, v208\n" \
  "v_swap_b32 v193, v209\n" \
  "v_swap_b32 v194, v210\n" \
  "v_swap_b32 v195, v211\n" \
  "v_swap_b32 v196, v212\n" \
  "v_swap_b32 v197, v213\n" \
  "v_swap_b32 v198, v214\n" \
  "v_swap_b32 v199, v215\n" \
  "v_swap_b32 v200, v216\n" \
  "v_swap_b32 v201, v217\n" \
  "v_swap_b32 v202, v218\n" \
  "v_swap_b32 v203, v219\n" \
  "v_swap_b32 v204, v220\n" \
  "v_swap_b32 v205, v221\n" \
  "v_swap_b32 v206, v222\n" \
  "v_swap_b32 v207, v223\n" \
  "v_swap_b32 v224, v240\n" \
  "v_swap_b32 v225, v241\n" \
  "v_swap_b32 v226, v242\n" \
  "v_swap_b32 v227, v243\n" \
  "v_swap_b32 v228, v244\n" \
  "v_swap_b32 v229, v245\n" \
  "v_swap_b32 v230, v246\n" \
  "v_swap_b32 v231, v247\n" \
  "v_swap_b32 v232, v248\n" \
  "v_swap_b32 v233, v249\n" \
  "v_swap_b32 v234, v250\n" \
  "v_swap_b32 v235, v251\n" \
  "v_swap_b32 v236, v252\n" \
  "v_swap_b32 v237, v253\n" \
  "v_swap_b32 v238, v254\n" \
  "v_swap_b32 v239, v255\n" \
  ".LCX_OWN_%=:\n" \
  "v_mov_b32 v38, s58\n" \
  "s_memrealtime s[38:39]\n" \
  "s_waitcnt lgkmcnt(0)\n" \
  "s_mov_b32 s69, s38\n" \
  "s_setprio 3\n" \
  ".p2align 6\n" \
  ".LCX_TOP_%=:\n" \
  "s_mov_b32 s62, 0x200000\n" \
  "s_cmp_ge_u32 s73, s67\n" \
  "s_cbranch_scc1 .LCX_TOK_%=\n" \
  "s_cmp_eq_u32 s61, 0\n" \
  "s_cbranch_scc1 .LCX_SPIN_%=\n" \
  ".LCX_RE_%=:\n" \
  "ds_read_b32 v37, v38\n" \
  "s_add_u32 s62, s62, 1\n" \
  "s_waitcnt lgkmcnt(0)\n" \
  "v_readfirstlane_b32 s73, v37\n" \
  "s_bfe_u32 s64, s73, 0x80008\n" \
  "s_and_b32 s73, s73, 0xff\n" \
  "s_min_u32 s73, s73, s64\n" \
  "s_cmp_ge_u32 s73, s67\n" \
  "s_cbranch_scc1 .LCX_TOK_%=\n" \
  "s_sleep 1\n" \
  "s_cmp_lt_u32 s62, 2097158\n" \
  "s_cbranch_scc1 .LCX_RE_%=\n" \
  ".LCX_SPIN_%=:\n" \
  "s_sleep 2\n" \
  "s_memrealtime s[38:39]\n" \
  "ds_read_b32 v37, v38\n" \
  "s_sub_u32 s62, s62, 1\n" \
  "s_cbranch_scc1 .LCX_END_%=\n" \
  "s_waitcnt lgkmcnt(0)\n" \
  "v_readfirstlane_b32 s73, v37\n" \
  "s_add_u32 s38, s38, 80\n" \
  "s_bfe_u32 s64, s73, 0x80008\n" \
  "s_and_b32 s73, s73, 0xff\n" \
  "s_min_u32 s73, s73, s64\n" \
  "s_cmp_ge_u32 s73, s67\n" \
  "s_cbranch_scc0 .LCX_SPIN_%=\n" \
  "s_cmp_eq_u32 s70, 0\n" \
  "s_cbranch_scc1 .LCX_TOK_%=\n" \
  "s_sub_u32 s63, s38, s69\n" \
  "s_max_i32 s63, s63, 0\n" \
  "s_add_u32 s69, s69, s63\n" \
  ".LCX_TOK_%=:\n" \
  "ds_read_b128 v[48:51], v6 offset:0\n" \
  "ds_read_b128 v[64:67], v0 offset:8192\n" \
  "ds_read_b128 v[80:83], v0 offset:12288\n" \
  "ds_read_b128 v[52:55], v6 offset:1024\n" \
  "ds_read_b128 v[68:71], v0 offset:9216\n" \
  "ds_read_b128 v[84:87], v0 offset:13312\n" \
  "ds_read_b128 v[56:59], v6 offset:2048\n" \
  "ds_read_b128 v[72:75], v0 offset:10240\n" \
  "ds_read_b128 v[88:91], v0 offset:14336\n" \
  "ds_read_b128 v[60:63], v6 offset:3072\n" \
  "ds_read_b128 v[76:79], v0 offset:11264\n" \
  "ds_read_b128 v[92:95], v0 offset:15360\n" \
  "s_waitcnt lgkmcnt(9)\n" \
  "v_mfma_f32_16x16x32_bf16 v[20:23], v[64:67], v[48:51], v[8:11]\n" \
  "v_mfma_f32_16x16x32_bf16 v[24:27], v[80:83], v[48:51], v[12:15]\n" \
  "ds_read_b128 v[96:99], v0 offset:16384\n" \
  "ds_read_b128 v[100:103], v0 offset:17408\n" \
  "s_waitcnt lgkmcnt(8)\n" \
  "v_mfma_f32_16x16x32_bf16 v[20:23], v[68:71], v[52:55], v[20:23]\n" \
  "v_mfma_f32_16x16x32_bf16 v[24:27], v[84:87], v[52:55], v[24:27]\n" \
  "ds_read_b128 v[104:107], v0 offset:18432\n" \
  "ds_read_b128 v[108:111], v0 offset:19456\n" \
  "s_waitcnt lgkmcnt(7)\n" \
  "v_mfma_f32_16x16x32_bf16 v[20:23], v[72:75], v[56:59], v[20:23]\n" \
  "v_mfma_f32_16x16x32_bf16 v[24:27], v[88:91], v[56:59], v[24:27]\n" \
  "ds_read_b128 v[112:115], v0 offset:20480\n" \
  "ds_read_b128 v[116:119], v0 offset:21504\n" \
  "s_waitcnt lgkmcnt(6)\n" \
  "v_mfma_f32_16x16x32_bf16 v[20:23], v[76:79], v[60:63], v[20:23]\n" \
  "v_mfma_f32_16x16x32_bf16 v[24:27], v[92:95], v[60:63], v[24:27]\n" \
  "ds_read_b128 v[120:123], v0 offset:22528\n" \
  "ds_read_b128 v[124:127], v0 offset:23552\n" \
  "s_add_u32 s67, s67, 1\n" \
  "v_add_u32 v0, s65, v0\n" \
  "v_add_u32 v6, s65, v6\n" \
  "s_sub_u32 s65, 0, s65\n" \
  "s_nop 1\n" \
  "v_max3_f32 v32, v20, v21, v22\n" \
  "v_max3_f32 v33, v23, v24, v25\n" \
  "v_max3_f32 v32, v32, v26, v27\n" \
  "v_max_f32 v32, v32, v33\n" \
  "v_mov_b32 v33, v32\n" \
  "s_nop 1\n" \
  "v_permlane16_swap_b32 v32, v33\n" \
  "v_max_f32 v32, v32, v33\n" \
  "v_mov_b32 v33, v32\n" \
  "s_nop 1\n" \
  "v_permlane32_swap_b32 v32, v33\n" \
  "v_max_f32 v32, v32, v33\n" \
  "v_mul_f32 v33, s72, v32\n" \
  "v_fma_f32 v20, v20, s71, v33\n" \
  "v_fma_f32 v21, v21, s71, v33\n" \
  "v_fma_f32 v22, v22, s71, v33\n" \
  "v_fma_f32 v23, v23, s71, v33\n" \
  "v_fma_f32 v24, v24, s71, v33\n" \
  "v_fma_f32 v25, v25, s71, v33\n" \
  "v_fma_f32 v26, v26, s71, v33\n" \
  "v_fma_f32 v27, v27, s71, v33\n" \
  "v_exp_f32 v20, v20\n" \
  "v_exp_f32 v21, v21\n" \
  "v_exp_f32 v22, v22\n" \
  "v_exp_f32 v23, v23\n" \
  "v_exp_f32 v24, v24\n" \
  "v_exp_f32 v25, v25\n" \
  "v_exp_f32 v26, v26\n" \
  "v_exp_f32 v27, v27\n" \
  "v_add_f32 v34, v20, v21\n" \
  "v_add_f32 v33, v22, v23\n" \
  "v_add_f32 v36, v24, v25\n" \
  "v_add_f32 v34, v34, v33\n" \
  "v_add_f32 v33, v26, v27\n" \
  "v_add_f32 v34, v34, v36\n" \
  "v_add_f32 v34, v34, v33\n" \
  "v_mov_b64 v[28:29], 0\n" \
  "v_mov_b64 v[30:31], 0\n" \
  "s_waitcnt vmcnt(2) lgkmcnt(0)\n" \
  "s_cmp_gt_i32 s68, -2\n" \
  "s_cbranch_scc1 .LCX_NP_%=\n" \
  "s_waitcnt vmcnt(0)\n" \
  ".LCX_NP_%=:\n" \
  "s_cmp_lg_u32 s68, 0\n" \
  "s_cbranch_scc1 .LCX_TEST_%=\n" \
  ".LCX_WF_%=:\n" \
  "s_memrealtime s[38:39]\n" \
  "s_waitcnt lgkmcnt(0)\n" \
  "s_sub_u32 s63, s69, s38\n" \
  "s_cmp_le_i32 s63, 0\n" \
  "s_cbranch_scc1 .LCX_GO_%=\n" \
  "s_cmp_lt_i32 s63, 12\n" \
  "s_cbranch_scc1 .LCX_WF_%=\n" \
  "s_sleep 3\n" \
  "s_branch .LCX_WF_%=\n" \
  ".LCX_GO_%=:\n" \
  "v_mov_b32 v35, 0\n" \
  "global_load_dwordx4 v[128:131], v5, s[48:49] offset:0 sc1\n" \
  "global_load_dwordx4 v[132:135], v5, s[48:49] offset:1024 sc1\n" \
  "global_load_dwordx4 v[136:139], v3, s[48:49] offset:0 sc1\n" \
  "global_load_dwordx4 v[140:143], v3, s[48:49] offset:512 sc1\n" \
  "global_load_dword v35, v35, s[50:51] sc1\n" \
  "global_load_dword v146, v2, s[52:53] sc1\n" \
  "s_sub_u32 s62, s62, 8\n" \
  "s_cbranch_scc1 .LCX_END_%=\n" \
  "s_waitcnt vmcnt(0)\n" \
  ".LCX_TEST_%=:\n" \
  "s_setprio 3\n" \
  "v_smfmac_f32_16x16x64_bf16 v[28:31], v[128:131], v[48:55], v1\n" \
  "v_smfmac_f32_16x16x64_bf16 v[28:31], v[132:135], v[56:63], v1\n" \
  "v_min3_u32 v33, v128, v129, v130\n" \
  "v_min3_u32 v36, v131, v132, v133\n" \
  "v_min3_u32 v33, v33, v134, v135\n" \
  "v_min3_u32 v36, v36, v136, v137\n" \
  "v_min3_u32 v33, v33, v138, v139\n" \
  "v_min3_u32 v36, v36, v140, v141\n" \
  "v_min3_u32 v33, v33, v142, v143\n" \
  "v_min3_u32 v33, v33, v36, v146\n" \
  "v_cmp_eq_u32 vcc, 0, v33\n" \
  "v_readfirstlane_b32 s64, v35\n" \
  "s_mov_b32 s68, 0\n" \
  "s_cmp_eq_u64 vcc, 0\n" \
  "s_cbranch_scc0 .LCX_FAIL_%=\n" \
  "v_permlane32_swap_b32 v28, v30\n" \
  "v_permlane32_swap_b32 v29, v31\n" \
  "v_mov_b32 v33, s67\n" \
  "ds_write_b32 v38, v33 offset:16\n" \
  "v_add_f32 v28, v28, v30\n" \
  "v_add_f32 v29, v29, v31\n" \
  "v_max3_f32 v36, v28, v29, v32\n" \
  "v_mov_b32 v33, v36\n" \
  "s_cmp_eq_u32 s64, 0\n" \
  "s_cselect_b32 s64, s38, s64\n" \
  "v_permlane16_swap_b32 v36, v33\n" \
  "v_max_f32 v36, v36, v33\n" \
  "v_mov_b32 v33, v36\n" \
  "s_sub_u32 s63, s64, s70\n" \
  "s_mov_b32 s70, s64\n" \
  "v_permlane32_swap_b32 v36, v33\n" \
  "v_max_f32 v36, v36, v33\n" \
  "v_mul_f32 v36, s72, v36\n" \
  "v_fma_f32 v32, v32, s71, v36\n" \
  "v_fma_f32 v28, v28, s71, v36\n" \
  "v_fma_f32 v29, v29, s71, v36\n" \
  "v_exp_f32 v32, v32\n" \
  "v_exp_f32 v28, v28\n" \
  "v_exp_f32 v29, v29\n" \
  "v_fma_f32 v34, v34, v32, v28\n" \
  "v_add_f32 v34, v34, v29\n" \
  "v_mov_b32 v33, v34\n" \
  "s_min_u32 s63, s63, 250\n" \
  "s_add_u32 s69, s64, s63\n" \
  "v_permlane16_swap_b32 v34, v33\n" \
  "v_add_f32 v34, v34, v33\n" \
  "v_mov_b32 v33, v34\n" \
  "s_add_u32 s69, s69, -48\n" \
  "s_add_u32 s48, s48, 0x10000\n" \
  "s_addc_u32 s49, s49, 0\n" \
  "v_permlane32_swap_b32 v34, v33\n" \
  "v_add_f32 v34, v34, v33\n" \
  "v_rcp_f32 v34, v34\n" \
  "s_add_u32 s50, s50, 1024\n" \
  "s_addc_u32 s51, s51, 0\n" \
  "s_add_u32 s52, s52, 256\n" \
  "s_addc_u32 s53, s53, 0\n" \
  "v_mul_f32 v32, v32, v34\n" \
  "v_mul_f32 v20, v20, v32\n" \
  "v_mul_f32 v21, v21, v32\n" \
  "v_mul_f32 v22, v22, v32\n" \
  "v_mul_f32 v23, v23, v32\n" \
  "v_mul_f32 v24, v24, v32\n" \
  "v_mul_f32 v25, v25, v32\n" \
  "v_mul_f32 v26, v26, v32\n" \
  "v_mul_f32 v27, v27, v32\n" \
  "v_cvt_pk_bf16_f32 v40, v20, v21\n" \
  "v_cvt_pk_bf16_f32 v41, v22, v23\n" \
  "v_cvt_pk_bf16_f32 v42, v24, v25\n" \
  "v_cvt_pk_bf16_f32 v43, v26, v27\n" \
  "v_mul_f32 v28, v28, v34\n" \
  "v_mul_f32 v29, v29, v34\n" \
  "v_mfma_f32_16x16x32_bf16 v[64:67], v[96:99], v[40:43], 0\n" \
  "v_cvt_pk_bf16_f32 v44, v28, v29\n" \
  "v_mov_b32 v45, 0\n" \
  "v_mfma_f32_16x16x32_bf16 v[68:71], v[100:103], v[40:43], 0\n" \
  "v_cvt_pk_bf16_f32 v47, v28, v29\n" \
  "v_mov_b32 v46, 0\n" \
  "v_mfma_f32_16x16x32_bf16 v[72:75], v[104:107], v[40:43], 0\n" \
  "v_permlane32_swap_b32 v44, v45\n" \
  "v_mfma_f32_16x16x32_bf16 v[76:79], v[108:111], v[40:43], 0\n" \
  "v_permlane32_swap_b32 v46, v47\n" \
  "v_mfma_f32_16x16x32_bf16 v[80:83], v[112:115], v[40:43], 0\n" \
  "v_xor_b32 v16, 4096, v7\n" \
  "v_mfma_f32_16x16x32_bf16 v[84:87], v[116:119], v[40:43], 0\n" \
  "v_xor_b32 v17, 8192, v7\n" \
  "v_mfma_f32_16x16x32_bf16 v[88:91], v[120:123], v[40:43], 0\n" \
  "v_xor_b32 v18, 12288, v7\n" \
  "v_mfma_f32_16x16x32_bf16 v[92:95], v[124:127], v[40:43], 0\n" \
  "s_memrealtime s[38:39]\n" \
  "ds_read_b32 v37, v38\n" \
  "v_mfma_f32_16x16x16_bf16 v[64:67], v[136:137], v[44:45], v[64:67]\n" \
  "v_mfma_f32_16x16x16_bf16 v[68:71], v[140:141], v[44:45], v[68:71]\n" \
  "v_mfma_f32_16x16x16_bf16 v[72:75], v[136:137], v[46:47], v[72:75]\n" \
  "v_mfma_f32_16x16x16_bf16 v[76:79], v[140:141], v[46:47], v[76:79]\n" \
  "v_mfma_f32_16x16x16_bf16 v[80:83], v[138:139], v[44:45], v[80:83]\n" \
  "v_mfma_f32_16x16x16_bf16 v[84:87], v[142:143], v[44:45], v[84:87]\n" \
  "v_mfma_f32_16x16x16_bf16 v[88:91], v[138:139], v[46:47], v[88:91]\n" \
  "v_mfma_f32_16x16x16_bf16 v[92:95], v[142:143], v[46:47], v[92:95]\n" \
  "s_nop 0\n" \
  "v_cvt_pk_bf16_f32 v160, v64, v65\n" \
  "v_cvt_pk_bf16_f32 v161, v66, v67\n" \
  "v_cvt_pk_bf16_f32 v162, v68, v69\n" \
  "v_cvt_pk_bf16_f32 v163, v70, v71\n" \
  "ds_write_b128 v7, v[160:163] offset:0\n" \
  "v_cvt_pk_bf16_f32 v164, v72, v73\n" \
  "v_cvt_pk_bf16_f32 v165, v74, v75\n" \
  "v_cvt_pk_bf16_f32 v166, v76, v77\n" \
  "v_cvt_pk_bf16_f32 v167, v78, v79\n" \
  "ds_write_b128 v7, v[164:167] offset:1024\n" \
  "v_cvt_pk_bf16_f32 v168, v80, v81\n" \
  "v_cvt_pk_bf16_f32 v169, v82, v83\n" \
  "v_cvt_pk_bf16_f32 v170, v84, v85\n" \
  "v_cvt_pk_bf16_f32 v171, v86, v87\n" \
  "ds_write_b128 v7, v[168:171] offset:2048\n" \
  "v_cvt_pk_bf16_f32 v172, v88, v89\n" \
  "v_cvt_pk_bf16_f32 v173, v90, v91\n" \
  "v_cvt_pk_bf16_f32 v174, v92, v93\n" \
  "v_cvt_pk_bf16_f32 v175, v94, v95\n" \
  "ds_write_b128 v7, v[172:175] offset:3072\n" \
  "v_xor_b32 v7, 0x4000, v7\n" \
  "v_mfma_f32_16x16x32_bf16 v[112:115], v[192:195], v[160:163], 0\n" \
  "v_mfma_f32_16x16x32_bf16 v[116:119], v[224:227], v[160:163], 0\n" \
  "s_waitcnt lgkmcnt(0)\n" \
  "s_barrier\n" \
  "ds_read_b128 v[64:67], v16 offset:0\n" \
  "ds_read_b128 v[68:71], v16 offset:1024\n" \
  "ds_read_b128 v[72:75], v16 offset:2048\n" \
  "ds_read_b128 v[76:79], v16 offset:3072\n" \
  "ds_read_b128 v[80:83], v17 offset:0\n" \
  "ds_read_b128 v[84:87], v17 offset:1024\n" \
  "ds_read_b128 v[88:91], v17 offset:2048\n" \
  "ds_read_b128 v[92:95], v17 offset:3072\n" \
  "ds_read_b128 v[96:99], v18 offset:0\n" \
  "ds_read_b128 v[100:103], v18 offset:1024\n" \
  "ds_read_b128 v[104:107], v18 offset:2048\n" \
  "ds_read_b128 v[108:111], v18 offset:3072\n" \
  "s_sub_u32 s59, s60, 1\n" \
  "s_bfe_u32 s59, s59, 0x10003\n" \
  "v_readfirstlane_b32 s73, v37\n" \
  "s_sub_u32 s63, s38, s69\n" \
  "s_bfe_u32 s64, s73, 0x80010\n" \
  "s_sub_u32 s64, s64, s67\n" \
  "s_or_b32 s63, s63, s64\n" \
  "s_bfe_u32 s64, s73, 0x80008\n" \
  "s_and_b32 s73, s73, 0xff\n" \
  "s_min_u32 s73, s73, s64\n" \
  "s_or_b32 s63, s63, s68\n" \
  "s_cmp_lt_i32 s63, 0\n" \
  "s_cbranch_scc1 .LCX_E2_%=\n" \
  "v_mov_b32 v35, 0\n" \
  "global_load_dwordx4 v[128:131], v5, s[48:49] offset:0 sc1\n" \
  "global_load_dwordx4 v[132:135], v5, s[48:49] offset:1024 sc1\n" \
  "global_load_dwordx4 v[136:139], v3, s[48:49] offset:0 sc1\n" \
  "global_load_dwordx4 v[140:143], v3, s[48:49] offset:512 sc1\n" \
  "global_load_dword v35, v35, s[50:51] sc1\n" \
  "global_load_dword v146, v2, s[52:53] sc1\n" \
  "s_mov_b32 s68, -1\n" \
  ".LCX_E2_%=:\n" \
  "v_mfma_f32_16x16x32_bf16 v[112:115], v[196:199], v[164:167], v[112:115]\n" \
  "v_mfma_f32_16x16x32_bf16 v[116:119], v[228:231], v[164:167], v[116:119]\n" \
  "v_mfma_f32_16x16x32_bf16 v[112:115], v[200:203], v[168:171], v[112:115]\n" \
  "v_mfma_f32_16x16x32_bf16 v[116:119], v[232:235], v[168:171], v[116:119]\n" \
  "v_mfma_f32_16x16x32_bf16 v[112:115], v[204:207], v[172:175], v[112:115]\n" \
  "v_mfma_f32_16x16x32_bf16 v[116:119], v[236:239], v[172:175], v[116:119]\n" \
  "s_waitcnt lgkmcnt(11)\n" \
  "v_mfma_f32_16x16x32_bf16 v[120:123], v[192:195], v[64:67], 0\n" \
  "v_mfma_f32_16x16x32_bf16 v[124:127], v[224:227], v[64:67], 0\n" \
  "s_waitcnt lgkmcnt(10)\n" \
  "v_mfma_f32_16x16x32_bf16 v[120:123], v[196:199], v[68:71], v[120:123]\n" \
  "v_mfma_f32_16x16x32_bf16 v[124:127], v[228:231], v[68:71], v[124:127]\n" \
  "s_waitcnt lgkmcnt(9)\n" \
  "v_mfma_f32_16x16x32_bf16 v[120:123], v[200:203], v[72:75], v[120:123]\n" \
  "v_mfma_f32_16x16x32_bf16 v[124:127], v[232:235], v[72:75], v[124:127]\n" \
  "s_waitcnt lgkmcnt(8)\n" \
  "v_mfma_f32_16x16x32_bf16 v[120:123], v[204:207], v[76:79], v[120:123]\n" \
  "v_mfma_f32_16x16x32_bf16 v[124:127], v[236:239], v[76:79], v[124:127]\n" \
  "s_waitcnt lgkmcnt(7)\n" \
  "v_mfma_f32_16x16x32_bf16 v[112:115], v[208:211], v[80:83], v[112:115]\n" \
  "v_mfma_f32_16x16x32_bf16 v[116:119], v[240:243], v[80:83], v[116:119]\n" \
  "s_waitcnt lgkmcnt(6)\n" \
  "v_mfma_f32_16x16x32_bf16 v[112:115], v[212:215], v[84:87], v[112:115]\n" \
  "v_mfma_f32_16x16x32_bf16 v[116:119], v[244:247], v[84:87], v[116:119]\n" \
  "s_waitcnt lgkmcnt(5)\n" \
  "v_mfma_f32_16x16x32_bf16 v[112:115], v[216:219], v[88:91], v[112:115]\n" \
  "v_mfma_f32_16x16x32_bf16 v[116:119], v[248:251], v[88:91], v[116:119]\n" \
  "s_waitcnt lgkmcnt(4)\n" \
  "v_mfma_f32_16x16x32_bf16 v[112:115], v[220:223], v[92:95], v[112:115]\n" \
  "v_mfma_f32_16x16x32_bf16 v[116:119], v[252:255], v[92:95], v[116:119]\n" \
  "s_memrealtime s[38:39]\n" \
  "ds_read_b32 v37, v38\n" \
  "s_waitcnt lgkmcnt(4)\n" \
  "v_mfma_f32_16x16x32_bf16 v[120:123], v[208:211], v[96:99], v[120:123]\n" \
  "v_mfma_f32_16x16x32_bf16 v[124:127], v[240:243], v[96:99], v[124:127]\n" \
  "s_waitcnt lgkmcnt(3)\n" \
  "v_mfma_f32_16x16x32_bf16 v[120:123], v[212:215], v[100:103], v[120:123]\n" \
  "v_mfma_f32_16x16x32_bf16 v[124:127], v[244:247], v[100:103], v[124:127]\n" \
  "v_cvt_pk_bf16_f32 v20, v112, v113\n" \
  "v_cvt_pk_bf16_f32 v21, v114, v115\n" \
  "v_cvt_pk_bf16_f32 v22, v116, v117\n" \
  "v_cvt_pk_bf16_f32 v23, v118, v119\n" \
  "v_and_or_b32 v20, v20, -2, s59\n" \
  "s_waitcnt lgkmcnt(2)\n" \
  "v_mfma_f32_16x16x32_bf16 v[120:123], v[216:219], v[104:107], v[120:123]\n" \
  "v_mfma_f32_16x16x32_bf16 v[124:127], v[248:251], v[104:107], v[124:127]\n" \
  "buffer_store_dwordx4 v[20:23], v4, s[40:43], s54 offen sc1\n" \
  "s_waitcnt lgkmcnt(0)\n" \
  "v_mfma_f32_16x16x32_bf16 v[120:123], v[220:223], v[108:111], v[120:123]\n" \
  "v_mfma_f32_16x16x32_bf16 v[124:127], v[252:255], v[108:111], v[124:127]\n" \
  "s_xor_b32 s64, s54, 16384\n" \
  "s_mov_b32 s61, 1\n" \
  "s_nop 3\n" \
  "v_cvt_pk_bf16_f32 v24, v120, v121\n" \
  "v_cvt_pk_bf16_f32 v25, v122, v123\n" \
  "v_cvt_pk_bf16_f32 v26, v124, v125\n" \
  "v_cvt_pk_bf16_f32 v27, v126, v127\n" \
  "v_and_or_b32 v24, v24, -2, s59\n" \
  "buffer_store_dwordx4 v[24:27], v4, s[40:43], s64 offen sc1\n" \
  "v_readfirstlane_b32 s73, v37\n" \
  "s_sub_u32 s63, s38, s69\n" \
  "s_bfe_u32 s64, s73, 0x80010\n" \
  "s_sub_u32 s64, s64, s67\n" \
  "s_or_b32 s63, s63, s64\n" \
  "s_bfe_u32 s64, s73, 0x80008\n" \
  "s_and_b32 s73, s73, 0xff\n" \
  "s_min_u32 s73, s73, s64\n" \
  "s_or_b32 s63, s63, s68\n" \
  "s_cmp_lt_i32 s63, 0\n" \
  "s_cbranch_scc1 .LCX_E3_%=\n" \
  "v_mov_b32 v35, 0\n" \
  "global_load_dwordx4 v[128:131], v5, s[48:49] offset:0 sc1\n" \
  "global_load_dwordx4 v[132:135], v5, s[48:49] offset:1024 sc1\n" \
  "global_load_dwordx4 v[136:139], v3, s[48:49] offset:0 sc1\n" \
  "global_load_dwordx4 v[140:143], v3, s[48:49] offset:512 sc1\n" \
  "global_load_dword v35, v35, s[50:51] sc1\n" \
  "global_load_dword v146, v2, s[52:53] sc1\n" \
  "s_mov_b32 s68, -2\n" \
  ".LCX_E3_%=:\n" \
  "s_setprio 0\n" \
  "s_add_u32 s54, s54, 0x80000\n" \
  "s_bitset0_b32 s54, 22\n" \
  "s_sub_u32 s60, s60, 1\n" \
  "s_cmp_lg_u32 s60, 0\n" \
  "s_cbranch_scc1 .LCX_TOP_%=\n" \
  "s_waitcnt vmcnt(0)\n" \
  ".LCX_END_%=:\n" \
  "s_endpgm\n" \
  ".LCX_FAIL_%=:\n" \
  "v_mov_b64 v[28:29], 0\n" \
  "v_mov_b64 v[30:31], 0\n" \
  ".LCX_FAIL2_%=:\n" \
  "s_add_u32 s69, s64, -48\n" \
  "s_cmp_lg_u32 s64, 0\n" \
  "s_cbranch_scc1 .LCX_WF_%=\n" \
  "s_sleep 4\n" \
  "global_load_dword v35, v35, s[50:51] sc1\n" \
  "s_sub_u32 s62, s62, 8\n" \
  "s_cbranch_scc1 .LCX_END_%=\n" \
  "s_waitcnt vmcnt(0)\n" \
  "v_readfirstlane_b32 s64, v35\n" \
  "s_branch .LCX_FAIL2_%=\n"
#define AW_ASM_Y \
  "s_mov_b32 s72, 0\n" \
  "s_mov_b32 s67, 0\n" \
  "s_mov_b32 s68, 2\n" \
  "s_mov_b32 s69, 0\n" \
  "s_mov_b32 s65, 49152\n" \
  "s_mov_b32 s66, 16384\n" \
  "v_mov_b32 v5, s58\n" \
  "v_mov_b32 v60, v3\n" \
  "v_add_u32 v3, 4096, v2\n" \
  "s_mov_b32 s62, 0x40000\n" \
  "s_setprio 1\n" \
  "s_branch .LCY_ASK_%=\n" \
  ".p2align 6\n" \
  ".LCY_BAR_%=:\n" \
  "s_barrier\n" \
  "s_memrealtime s[38:39]\n" \
  "ds_read_b128 v[8:11], v0 offset:0\n" \
  "ds_read_b128 v[12:15], v0 offset:1024\n" \
  "ds_read_b128 v[16:19], v0 offset:2048\n" \
  "s_sub_u32 s59, s60, 1\n" \
  "s_bfe_u32 s59, s59, 0x10003\n" \
  "s_add_u32 s68, s68, 1\n" \
  "s_add_u32 s64, s54, 16384\n" \
  "s_cmp_eq_u32 s69, 0\n" \
  "s_cbranch_scc1 .LCY_NI_%=\n" \
  "s_waitcnt vmcnt(3)\n" \
  "s_cmp_lg_u32 s72, 0\n" \
  "s_cbranch_scc1 .LCY_NJ_%=\n" \
  "s_waitcnt vmcnt(0)\n" \
  ".LCY_NJ_%=:\n" \
  "v_mov_b32 v6, s55\n" \
  "s_mov_b64 exec, 1\n" \
  "ds_add_u32 v5, v6\n" \
  "s_mov_b64 exec, -1\n" \
  "s_mov_b32 s69, 0\n" \
  ".LCY_NI_%=:\n" \
  "s_waitcnt lgkmcnt(0)\n" \
  "s_add_u32 s63, s38, 200\n" \
  "s_mov_b32 s72, 0\n" \
  "v_or_b32 v7, 1, s63\n" \
  "s_mov_b64 exec, 1\n" \
  "global_store_dword v2, v7, s[52:53] sc1\n" \
  "s_mov_b64 exec, -1\n" \
  "s_add_u32 s52, s52, 2048\n" \
  "s_addc_u32 s53, s53, 0\n" \
  "v_cmp_eq_u32 vcc, 0, v4\n" \
  "s_cmp_eq_u64 vcc, 0\n" \
  "s_cbranch_scc0 .LCY_NS_%=\n" \
  "v_lshlrev_b32 v6, 16, s55\n" \
  "s_mov_b64 exec, 1\n" \
  "ds_add_u32 v5, v6\n" \
  "s_mov_b64 exec, -1\n" \
  "s_mov_b32 m0, s56\n" \
  "s_add_u32 s71, s56, 4096\n" \
  "global_load_lds_dwordx4 v2, s[48:49] offset:0 sc1\n" \
  "global_load_lds_dwordx4 v2, s[48:49] offset:1024 sc1\n" \
  "global_load_lds_dwordx4 v2, s[48:49] offset:2048 sc1\n" \
  "global_load_lds_dwordx4 v2, s[48:49] offset:3072 sc1\n" \
  "s_mov_b32 m0, s71\n" \
  "s_add_u32 s56, s56, s65\n" \
  "global_load_lds_dwordx4 v3, s[48:49] offset:0 sc1\n" \
  "global_load_lds_dwordx4 v3, s[48:49] offset:1024 sc1\n" \
  ".LCY_NS_%=:\n" \
  "ds_read_b128 v[20:23], v0 offset:3072\n" \
  "v_mfma_f32_16x16x32_bf16 v[32:35], v[64:67], v[8:11], 0\n" \
  "v_mfma_f32_16x16x32_bf16 v[36:39], v[96:99], v[8:11], 0\n" \
  "v_mfma_f32_16x16x32_bf16 v[40:43], v[128:131], v[8:11], 0\n" \
  "v_mfma_f32_16x16x32_bf16 v[44:47], v[160:163], v[8:11], 0\n" \
  "v_mfma_f32_16x16x32_bf16 v[48:51], v[192:195], v[8:11], 0\n" \
  "v_mfma_f32_16x16x32_bf16 v[52:55], v[224:227], v[8:11], 0\n" \
  "ds_read_b128 v[8:11], v0 offset:8192\n" \
  "v_mfma_f32_16x16x32_bf16 v[32:35], v[68:71], v[12:15], v[32:35]\n" \
  "v_mfma_f32_16x16x32_bf16 v[36:39], v[100:103], v[12:15], v[36:39]\n" \
  "v_mfma_f32_16x16x32_bf16 v[40:43], v[132:135], v[12:15], v[40:43]\n" \
  "v_mfma_f32_16x16x32_bf16 v[44:47], v[164:167], v[12:15], v[44:47]\n" \
  "v_mfma_f32_16x16x32_bf16 v[48:51], v[196:199], v[12:15], v[48:51]\n" \
  "v_mfma_f32_16x16x32_bf16 v[52:55], v[228:231], v[12:15], v[52:55]\n" \
  "s_cbranch_vccnz .LCY_D1_%=\n" \
  "global_load_lds_dwordx4 v3, s[48:49] offset:2048 sc1\n" \
  ".LCY_D1_%=:\n" \
  "ds_read_b128 v[12:15], v0 offset:9216\n" \
  "v_mfma_f32_16x16x32_bf16 v[32:35], v[72:75], v[16:19], v[32:35]\n" \
  "v_mfma_f32_16x16x32_bf16 v[36:39], v[104:107], v[16:19], v[36:39]\n" \
  "v_mfma_f32_16x16x32_bf16 v[40:43], v[136:139], v[16:19], v[40:43]\n" \
  "v_mfma_f32_16x16x32_bf16 v[44:47], v[168:171], v[16:19], v[44:47]\n" \
  "v_mfma_f32_16x16x32_bf16 v[48:51], v[200:203], v[16:19], v[48:51]\n" \
  "v_mfma_f32_16x16x32_bf16 v[52:55], v[232:235], v[16:19], v[52:55]\n" \
  "s_cbranch_vccnz .LCY_D2_%=\n" \
  "global_load_lds_dwordx4 v3, s[48:49] offset:3072 sc1\n" \
  "s_add_u32 s48, s48, 0x80000\n" \
  "s_addc_u32 s49, s49, 0\n" \
  ".LCY_D2_%=:\n" \
  "ds_read_b128 v[16:19], v0 offset:10240\n" \
  "s_waitcnt lgkmcnt(3)\n" \
  "v_mfma_f32_16x16x32_bf16 v[32:35], v[76:79], v[20:23], v[32:35]\n" \
  "v_mfma_f32_16x16x32_bf16 v[36:39], v[108:111], v[20:23], v[36:39]\n" \
  "v_mfma_f32_16x16x32_bf16 v[40:43], v[140:143], v[20:23], v[40:43]\n" \
  "v_mfma_f32_16x16x32_bf16 v[44:47], v[172:175], v[20:23], v[44:47]\n" \
  "v_mfma_f32_16x16x32_bf16 v[48:51], v[204:207], v[20:23], v[48:51]\n" \
  "v_mfma_f32_16x16x32_bf16 v[52:55], v[236:239], v[20:23], v[52:55]\n" \
  "s_cbranch_vccnz .LCY_D3_%=\n" \
  "s_mov_b32 m0, s57\n" \
  "s_add_u32 s57, s57, s65\n" \
  "global_load_lds_dwordx4 v2, s[50:51] offset:0 sc1\n" \
  ".LCY_D3_%=:\n" \
  "ds_read_b128 v[20:23], v0 offset:11264\n" \
  "s_waitcnt lgkmcnt(3)\n" \
  "v_mfma_f32_16x16x32_bf16 v[32:35], v[80:83], v[8:11], v[32:35]\n" \
  "v_mfma_f32_16x16x32_bf16 v[36:39], v[112:115], v[8:11], v[36:39]\n" \
  "v_mfma_f32_16x16x32_bf16 v[40:43], v[144:147], v[8:11], v[40:43]\n" \
  "v_mfma_f32_16x16x32_bf16 v[44:47], v[176:179], v[8:11], v[44:47]\n" \
  "v_mfma_f32_16x16x32_bf16 v[48:51], v[208:211], v[8:11], v[48:51]\n" \
  "v_mfma_f32_16x16x32_bf16 v[52:55], v[240:243], v[8:11], v[52:55]\n" \
  "s_cbranch_vccnz .LCY_D4_%=\n" \
  "global_load_lds_dwordx4 v2, s[50:51] offset:1024 sc1\n" \
  "s_sub_u32 s65, 0, s65\n" \
  "s_add_u32 s67, s67, 4096\n" \
  ".LCY_D4_%=:\n" \
  "ds_read_b128 v[8:11], v0 offset:4096\n" \
  "s_waitcnt lgkmcnt(3)\n" \
  "v_mfma_f32_16x16x32_bf16 v[32:35], v[84:87], v[12:15], v[32:35]\n" \
  "v_mfma_f32_16x16x32_bf16 v[36:39], v[116:119], v[12:15], v[36:39]\n" \
  "v_mfma_f32_16x16x32_bf16 v[40:43], v[148:151], v[12:15], v[40:43]\n" \
  "v_mfma_f32_16x16x32_bf16 v[44:47], v[180:183], v[12:15], v[44:47]\n" \
  "v_mfma_f32_16x16x32_bf16 v[48:51], v[212:215], v[12:15], v[48:51]\n" \
  "v_mfma_f32_16x16x32_bf16 v[52:55], v[244:247], v[12:15], v[52:55]\n" \
  "s_cbranch_vccnz .LCY_D5_%=\n" \
  "global_load_lds_dwordx4 v2, s[50:51] offset:2048 sc1\n" \
  "s_sub_u32 s68, s68, 1\n" \
  "s_mov_b32 s69, 1\n" \
  ".LCY_D5_%=:\n" \
  "ds_read_b128 v[12:15], v0 offset:5120\n" \
  "s_waitcnt lgkmcnt(3)\n" \
  "v_mfma_f32_16x16x32_bf16 v[32:35], v[88:91], v[16:19], v[32:35]\n" \
  "v_mfma_f32_16x16x32_bf16 v[36:39], v[120:123], v[16:19], v[36:39]\n" \
  "v_mfma_f32_16x16x32_bf16 v[40:43], v[152:155], v[16:19], v[40:43]\n" \
  "v_mfma_f32_16x16x32_bf16 v[44:47], v[184:187], v[16:19], v[44:47]\n" \
  "v_mfma_f32_16x16x32_bf16 v[48:51], v[216:219], v[16:19], v[48:51]\n" \
  "v_mfma_f32_16x16x32_bf16 v[52:55], v[248:251], v[16:19], v[52:55]\n" \
  "s_cbranch_vccnz .LCY_D6_%=\n" \
  "global_load_lds_dwordx4 v2, s[50:51] offset:3072 sc1\n" \
  "s_add_u32 s50, s50, 0x40000\n" \
  "s_addc_u32 s51, s51, 0\n" \
  ".LCY_D6_%=:\n" \
  "ds_read_b128 v[16:19], v0 offset:6144\n" \
  "s_waitcnt lgkmcnt(3)\n" \
  "v_mfma_f32_16x16x32_bf16 v[32:35], v[92:95], v[20:23], v[32:35]\n" \
  "v_mfma_f32_16x16x32_bf16 v[36:39], v[124:127], v[20:23], v[36:39]\n" \
  "v_mfma_f32_16x16x32_bf16 v[40:43], v[156:159], v[20:23], v[40:43]\n" \
  "v_mfma_f32_16x16x32_bf16 v[44:47], v[188:191], v[20:23], v[44:47]\n" \
  "v_mfma_f32_16x16x32_bf16 v[48:51], v[220:223], v[20:23], v[48:51]\n" \
  "v_mfma_f32_16x16x32_bf16 v[52:55], v[252:255], v[20:23], v[52:55]\n" \
  "ds_read_b128 v[20:23], v0 offset:7168\n" \
  "s_waitcnt lgkmcnt(3)\n" \
  "s_nop 1\n" \
  "v_cvt_pk_bf16_f32 v24, v32, v33\n" \
  "v_cvt_pk_bf16_f32 v25, v34, v35\n" \
  "v_cvt_pk_bf16_f32 v26, v36, v37\n" \
  "v_cvt_pk_bf16_f32 v27, v38, v39\n" \
  "s_nop 0\n" \
  "v_mfma_f32_16x16x32_bf16 v[32:35], v[64:67], v[8:11], 0\n" \
  "v_mfma_f32_16x16x32_bf16 v[36:39], v[96:99], v[8:11], 0\n" \
  "v_and_or_b32 v24, v24, -2, s59\n" \
  "buffer_store_dwordx4 v[24:27], v1, s[40:43], s54 offen offset:0 sc1\n" \
  "v_cvt_pk_bf16_f32 v28, v40, v41\n" \
  "v_cvt_pk_bf16_f32 v29, v42, v43\n" \
  "v_cvt_pk_bf16_f32 v30, v44, v45\n" \
  "v_cvt_pk_bf16_f32 v31, v46, v47\n" \
  "s_nop 0\n" \
  "v_mfma_f32_16x16x32_bf16 v[40:43], v[128:131], v[8:11], 0\n" \
  "v_mfma_f32_16x16x32_bf16 v[44:47], v[160:163], v[8:11], 0\n" \
  "v_and_or_b32 v28, v28, -2, s59\n" \
  "buffer_store_dwordx4 v[28:31], v1, s[40:43], s54 offen offset:128 sc1\n" \
  "v_cvt_pk_bf16_f32 v56, v48, v49\n" \
  "v_cvt_pk_bf16_f32 v57, v50, v51\n" \
  "v_cvt_pk_bf16_f32 v58, v52, v53\n" \
  "v_cvt_pk_bf16_f32 v59, v54, v55\n" \
  "s_nop 0\n" \
  "v_mfma_f32_16x16x32_bf16 v[48:51], v[192:195], v[8:11], 0\n" \
  "v_mfma_f32_16x16x32_bf16 v[52:55], v[224:227], v[8:11], 0\n" \
  "v_and_or_b32 v56, v56, -2, s59\n" \
  "buffer_store_dwordx4 v[56:59], v1, s[40:43], s54 offen offset:256 sc1\n" \
  "ds_read_b128 v[8:11], v0 offset:12288\n" \
  "s_waitcnt lgkmcnt(3)\n" \
  "v_mfma_f32_16x16x32_bf16 v[32:35], v[68:71], v[12:15], v[32:35]\n" \
  "v_mfma_f32_16x16x32_bf16 v[36:39], v[100:103], v[12:15], v[36:39]\n" \
  "v_mfma_f32_16x16x32_bf16 v[40:43], v[132:135], v[12:15], v[40:43]\n" \
  "v_mfma_f32_16x16x32_bf16 v[44:47], v[164:167], v[12:15], v[44:47]\n" \
  "v_mfma_f32_16x16x32_bf16 v[48:51], v[196:199], v[12:15], v[48:51]\n" \
  "v_mfma_f32_16x16x32_bf16 v[52:55], v[228:231], v[12:15], v[52:55]\n" \
  "ds_read_b128 v[12:15], v0 offset:13312\n" \
  "s_waitcnt lgkmcnt(3)\n" \
  "v_mfma_f32_16x16x32_bf16 v[32:35], v[72:75], v[16:19], v[32:35]\n" \
  "v_mfma_f32_16x16x32_bf16 v[36:39], v[104:107], v[16:19], v[36:39]\n" \
  "v_mfma_f32_16x16x32_bf16 v[40:43], v[136:139], v[16:19], v[40:43]\n" \
  "v_mfma_f32_16x16x32_bf16 v[44:47], v[168:171], v[16:19], v[44:47]\n" \
  "v_mfma_f32_16x16x32_bf16 v[48:51], v[200:203], v[16:19], v[48:51]\n" \
  "v_mfma_f32_16x16x32_bf16 v[52:55], v[232:235], v[16:19], v[52:55]\n" \
  "ds_read_b128 v[16:19], v0 offset:14336\n" \
  "s_waitcnt lgkmcnt(3)\n" \
  "v_mfma_f32_16x16x32_bf16 v[32:35], v[76:79], v[20:23], v[32:35]\n" \
  "v_mfma_f32_16x16x32_bf16 v[36:39], v[108:111], v[20:23], v[36:39]\n" \
  "v_mfma_f32_16x16x32_bf16 v[40:43], v[140:143], v[20:23], v[40:43]\n" \
  "v_mfma_f32_16x16x32_bf16 v[44:47], v[172:175], v[20:23], v[44:47]\n" \
  "v_mfma_f32_16x16x32_bf16 v[48:51], v[204:207], v[20:23], v[48:51]\n" \
  "v_mfma_f32_16x16x32_bf16 v[52:55], v[236:239], v[20:23], v[52:55]\n" \
  "ds_read_b128 v[20:23], v0 offset:15360\n" \
  "s_waitcnt lgkmcnt(3)\n" \
  "v_mfma_f32_16x16x32_bf16 v[32:35], v[80:83], v[8:11], v[32:35]\n" \
  "v_mfma_f32_16x16x32_bf16 v[36:39], v[112:115], v[8:11], v[36:39]\n" \
  "v_mfma_f32_16x16x32_bf16 v[40:43], v[144:147], v[8:11], v[40:43]\n" \
  "v_mfma_f32_16x16x32_bf16 v[44:47], v[176:179], v[8:11], v[44:47]\n" \
  "v_mfma_f32_16x16x32_bf16 v[48:51], v[208:211], v[8:11], v[48:51]\n" \
  "v_mfma_f32_16x16x32_bf16 v[52:55], v[240:243], v[8:11], v[52:55]\n" \
  "s_waitcnt lgkmcnt(2)\n" \
  "v_mfma_f32_16x16x32_bf16 v[32:35], v[84:87], v[12:15], v[32:35]\n" \
  "v_mfma_f32_16x16x32_bf16 v[36:39], v[116:119], v[12:15], v[36:39]\n" \
  "v_mfma_f32_16x16x32_bf16 v[40:43], v[148:151], v[12:15], v[40:43]\n" \
  "v_mfma_f32_16x16x32_bf16 v[44:47], v[180:183], v[12:15], v[44:47]\n" \
  "v_mfma_f32_16x16x32_bf16 v[48:51], v[212:215], v[12:15], v[48:51]\n" \
  "v_mfma_f32_16x16x32_bf16 v[52:55], v[244:247], v[12:15], v[52:55]\n" \
  "s_waitcnt lgkmcnt(1)\n" \
  "v_mfma_f32_16x16x32_bf16 v[32:35], v[88:91], v[16:19], v[32:35]\n" \
  "v_mfma_f32_16x16x32_bf16 v[36:39], v[120:123], v[16:19], v[36:39]\n" \
  "v_mfma_f32_16x16x32_bf16 v[40:43], v[152:155], v[16:19], v[40:43]\n" \
  "v_mfma_f32_16x16x32_bf16 v[44:47], v[184:187], v[16:19], v[44:47]\n" \
  "v_mfma_f32_16x16x32_bf16 v[48:51], v[216:219], v[16:19], v[48:51]\n" \
  "v_mfma_f32_16x16x32_bf16 v[52:55], v[248:251], v[16:19], v[52:55]\n" \
  "s_cbranch_vccnz .LCY_NT_%=\n" \
  "v_mov_b32 v4, 0\n" \
  "buffer_load_dword v4, v60, s[44:47], s67 offen sc1\n" \
  ".LCY_NT_%=:\n" \
  "s_waitcnt lgkmcnt(0)\n" \
  "v_mfma_f32_16x16x32_bf16 v[32:35], v[92:95], v[20:23], v[32:35]\n" \
  "v_mfma_f32_16x16x32_bf16 v[36:39], v[124:127], v[20:23], v[36:39]\n" \
  "v_mfma_f32_16x16x32_bf16 v[40:43], v[156:159], v[20:23], v[40:43]\n" \
  "v_mfma_f32_16x16x32_bf16 v[44:47], v[188:191], v[20:23], v[44:47]\n" \
  "v_mfma_f32_16x16x32_bf16 v[48:51], v[220:223], v[20:23], v[48:51]\n" \
  "v_mfma_f32_16x16x32_bf16 v[52:55], v[252:255], v[20:23], v[52:55]\n" \
  "v_add_u32 v0, s66, v0\n" \
  "s_sub_u32 s66, 0, s66\n" \
  "s_add_u32 s54, s54, 0x80000\n" \
  "s_bitset0_b32 s54, 22\n" \
  "s_mov_b32 s62, 0x40000\n" \
  "s_nop 0\n" \
  "v_cvt_pk_bf16_f32 v24, v32, v33\n" \
  "v_cvt_pk_bf16_f32 v25, v34, v35\n" \
  "v_cvt_pk_bf16_f32 v26, v36, v37\n" \
  "v_cvt_pk_bf16_f32 v27, v38, v39\n" \
  "v_and_or_b32 v24, v24, -2, s59\n" \
  "buffer_store_dwordx4 v[24:27], v1, s[40:43], s64 offen offset:0 sc1\n" \
  "v_cvt_pk_bf16_f32 v28, v40, v41\n" \
  "v_cvt_pk_bf16_f32 v29, v42, v43\n" \
  "v_cvt_pk_bf16_f32 v30, v44, v45\n" \
  "v_cvt_pk_bf16_f32 v31, v46, v47\n" \
  "v_and_or_b32 v28, v28, -2, s59\n" \
  "buffer_store_dwordx4 v[28:31], v1, s[40:43], s64 offen offset:128 sc1\n" \
  "v_cvt_pk_bf16_f32 v56, v48, v49\n" \
  "v_cvt_pk_bf16_f32 v57, v50, v51\n" \
  "v_cvt_pk_bf16_f32 v58, v52, v53\n" \
  "v_cvt_pk_bf16_f32 v59, v54, v55\n" \
  "v_and_or_b32 v56, v56, -2, s59\n" \
  "buffer_store_dwordx4 v[56:59], v1, s[40:43], s64 offen offset:256 sc1\n" \
  "s_cbranch_vccnz .LCY_ND_%=\n" \
  "s_cmp_eq_u32 s68, 0\n" \
  "s_cselect_b32 s72, 1, 0\n" \
  "s_cbranch_scc1 .LCY_DEF_%=\n" \
  "s_waitcnt vmcnt(0)\n" \
  "v_mov_b32 v6, s55\n" \
  "s_mov_b64 exec, 1\n" \
  "ds_add_u32 v5, v6\n" \
  "s_mov_b64 exec, -1\n" \
  "s_mov_b32 s69, 0\n" \
  "s_branch .LCY_WC_%=\n" \
  ".LCY_ND_%=:\n" \
  "v_mov_b32 v4, 0\n" \
  "buffer_load_dword v4, v60, s[44:47], s67 offen sc1\n" \
  "s_waitcnt vmcnt(0)\n" \
  ".LCY_WC_%=:\n" \
  ".LCY_DEF_%=:\n" \
  "s_sub_u32 s60, s60, 1\n" \
  "s_cmp_eq_u32 s60, 0\n" \
  "s_cbranch_scc1 .LCY_END_%=\n" \
  ".LCY_NEED_%=:\n" \
  "s_cmp_eq_u32 s68, 0\n" \
  "s_cbranch_scc1 .LCY_BAR_%=\n" \
  "v_cmp_eq_u32 vcc, 0, v4\n" \
  "s_cmp_eq_u64 vcc, 0\n" \
  "s_cbranch_scc1 .LCY_GET_%=\n" \
  "s_cmp_ge_u32 s67, 0x40000\n" \
  "s_cbranch_scc1 .LCY_BAR_%=\n" \
  "s_sleep 4\n" \
  "ds_read_b32 v6, v5 offset:16\n" \
  "s_waitcnt lgkmcnt(0)\n" \
  "v_readfirstlane_b32 s63, v6\n" \
  "s_add_u32 s63, s63, s60\n" \
  "s_cmp_ge_u32 s63, 66\n" \
  "s_cbranch_scc1 .LCY_BAR_%=\n" \
  ".LCY_ASK_%=:\n" \
  "v_mov_b32 v4, 0\n" \
  "buffer_load_dword v4, v60, s[44:47], s67 offen sc1\n" \
  "s_sub_u32 s62, s62, 1\n" \
  "s_cbranch_scc1 .LCY_END_%=\n" \
  "s_waitcnt vmcnt(0)\n" \
  "s_branch .LCY_NEED_%=\n" \
  ".LCY_GET_%=:\n" \
  "v_lshlrev_b32 v6, 16, s55\n" \
  "s_mov_b64 exec, 1\n" \
  "ds_add_u32 v5, v6\n" \
  "s_mov_b64 exec, -1\n" \
  "s_mov_b32 m0, s56\n" \
  "s_add_u32 s71, s56, 4096\n" \
  "global_load_lds_dwordx4 v2, s[48:49] offset:0 sc1\n" \
  "global_load_lds_dwordx4 v2, s[48:49] offset:1024 sc1\n" \
  "global_load_lds_dwordx4 v2, s[48:49] offset:2048 sc1\n" \
  "global_load_lds_dwordx4 v2, s[48:49] offset:3072 sc1\n" \
  "s_mov_b32 m0, s71\n" \
  "s_add_u32 s56, s56, s65\n" \
  "global_load_lds_dwordx4 v3, s[48:49] offset:0 sc1\n" \
  "global_load_lds_dwordx4 v3, s[48:49] offset:1024 sc1\n" \
  "global_load_lds_dwordx4 v3, s[48:49] offset:2048 sc1\n" \
  "global_load_lds_dwordx4 v3, s[48:49] offset:3072 sc1\n" \
  "s_add_u32 s48, s48, 0x80000\n" \
  "s_addc_u32 s49, s49, 0\n" \
  "s_mov_b32 m0, s57\n" \
  "s_add_u32 s57, s57, s65\n" \
  "global_load_lds_dwordx4 v2, s[50:51] offset:0 sc1\n" \
  "global_load_lds_dwordx4 v2, s[50:51] offset:1024 sc1\n" \
  "s_sub_u32 s65, 0, s65\n" \
  "s_add_u32 s67, s67, 4096\n" \
  "global_load_lds_dwordx4 v2, s[50:51] offset:2048 sc1\n" \
  "s_sub_u32 s68, s68, 1\n" \
  "s_mov_b32 s69, 1\n" \
  "global_load_lds_dwordx4 v2, s[50:51] offset:3072 sc1\n" \
  "s_add_u32 s50, s50, 0x40000\n" \
  "s_addc_u32 s51, s51, 0\n" \
  "v_mov_b32 v4, 0\n" \
  "buffer_load_dword v4, v60, s[44:47], s67 offen sc1\n" \
  "s_mov_b32 s61, 0\n" \
  "s_cmp_eq_u32 s68, 0\n" \
  "s_cbranch_scc1 .LCY_BAR_%=\n" \
  "s_waitcnt vmcnt(0)\n" \
  "v_mov_b32 v6, s55\n" \
  "s_mov_b64 exec, 1\n" \
  "ds_add_u32 v5, v6\n" \
  "s_mov_b64 exec, -1\n" \
  "s_mov_b32 s69, 0\n" \
  "s_branch .LCY_NEED_%=\n" \
  ".LCY_END_%=:\n" \
  "s_endpgm\n"
template <bool AT>
__device__ __forceinline__ void role_aw_t(const MegaArgs& A, char* smem, const int L, const int a, const u64 tag) {
  const int w = A.wv;
  const int l = lane_id(), c = l & 15, g = l >> 4;
  const int qt = w & 1, hh = (w >> 1) & 1, h = 2 * a + hh;
  char* ws = A.ws;
  unsigned* FL = (unsigned*)(ws + OFF_FL);
  const unsigned sb = lds_base(smem);
  if (w == 0 && l < 8) reinterpret_cast<unsigned*>(smem + AW_CNT)[l] = 0u;
  constexpr int NP = AT ? 1 : 3;
  const int cp0 = AT ? w : 3 * w - 8;
  v8u wo[2 * NP][4];
  {
    const int q = (l >> 2) & 3, p = l & 3, f = q | ((g & 1) << 2);
    const unsigned ra = sb + (4 * g + q) * 256 + (p & 1) * 16;
#define PW_CLEAR(n)                                                                                                         \
  _Pragma("unroll") for (int i = 0; i < 16; ++i)                                                                            \
    ST128((v4u{0u, 0u, 0u, 0u}), MKRS(ws + OFF_PW, SZ_PW), l * 16 + (i & 3) * 1024,                                         \
          ((((n) >> RGB) * 64 + ((n) & ((1 << RGB) - 1))) * 32 + 2 * a + qt) * 16384 + (i >> 2) * 4096);
    if (EF && AT && L) { PW_CLEAR(hh) }
#pragma unroll
    for (int r = 0; r < 2; ++r) {
#pragma unroll
      for (int k = 0; k < 2; ++k) {
        const char* base_ = (const char*)A.Wo + ((size_t)((2 * a + r) * DH + 64 * (w & 1) + 32 * k) * DM + 128 * (w >> 1)) * 2;
#pragma unroll
        for (int i = 0; i < 8; ++i) {
          const int fd = g | ((i & 1) << 2);
          glds16(base_, (unsigned)((4 * i + g) * (DM * 2) + ((c ^ (fd << 1)) << 4)), sb + w * 16384 + k * 8192 + i * 1024, WNT & 16);
        }
      }
      SST(4 + 3 * r);
      if (r == 1) ready_wait(A, tag, ready_look(A));
      WAIT_VM(0);
      if (EF && AT && r == 0 && L) {
        ready_wait(A, tag, ready_look(A));
        PW_FREE(0, hh, 2 * a + qt);
      }
      RAW_BAR();
      SST(5 + 3 * r);
#pragma unroll
      for (int k2 = 0; k2 < 2; ++k2) {
#pragma unroll
        for (int ct = 0; ct < 2 * NP; ++ct) {
          const int cp = cp0 + (ct >> 1);
          const unsigned a_ = ra + (cp >> 2) * 32768 + k2 * 16384 + ((2 * (cp & 3) + (p >> 1)) ^ f) * 32 + (ct & 1) * 8;
          const v2u r0 = trd(a_), r1 = trd(a_ + 4096), r2 = trd(a_ + 8192), r3 = trd(a_ + 12288);
          wo[ct][2 * r + k2] = v8u{r0.x, r0.y, r1.x, r1.y, r2.x, r2.y, r3.x, r3.y};
          asm volatile("" : "+v"(wo[ct][2 * r + k2]));
        }
      }
      RAW_BAR();
      SST(6 + 3 * r);
    }
  }
  WSTAMP2();
  if (EF && L) {
    const unsigned t0 = now10();
#pragma unroll 1
    for (int n = 2 + (w >> 1); n < (2 << RGB); n += 4) {
      gate(t0 + (n - 2) * PCD);
      PW_CLEAR(n)
      DRAIN();
      PW_FREE(n >> RGB, n & ((1 << RGB) - 1), 2 * a + qt);
    }
  }
  const v4u rsW = mk_srd(ws + OFF_PW, SZ_PW);
  const int dp = (FL_FP - FL_FQ + (L * 16 + a - L * 64 * 32 - h) * 32 + 2 * hh) * 4;
  const v4u rsF = mk_srd(FL + FL_FQ + (L * 64 * 32 + h) * 32, (qt ? 0 : dp) + 64 * 4096 - 128);
  const char* p0 = AT ? ws + OFF_MEMK + L * SZ_MEM + h * 2048 : ws + OFF_QK + L * SZ_QK + ((size_t)h * 16 + 8 * qt) * 1024;
  const char* p1 = AT ? (const char*)(FL + FL_FK + L * 32 + (h >> 3) * 64 + (h & 7) * 2) : ws + OFF_VF + L * SZ_O + ((size_t)h * 8 + 4 * qt) * 1024;
  const unsigned* pFW = EF && AT ? FL + FL_FD + L * 32 : FL + FL_FW + (L * 64 * 16 + (a >> 2)) * 32 + (a & 3) + (EF ? (w & 3) * 128 : 0);
  const unsigned tk = sb + AW_TK + hh * 24 * 1024, voff = (c >> 1) * 2048 + (c & 1) * 64 + g * 16;
  const char* dbg = ws + WS_TOTAL + A.vb * 64 * 8;
#define AW_SOPS                                                                                                                 \
  "{s[40:43]}"(rsW), "{s[44:47]}"(rsF), "{s[48:49]}"(p0), "{s[50:51]}"(p1), "{s[52:53]}"(pFW),                                   \
      "{s54}"((L * 64 * 16 + a) * 32768 + cp0 * 128 + (AT ? qt * 16384 : 0)), "{s55}"(1 << (8 * qt)), "{s56}"(tk + qt * 8192), "{s57}"(tk + 16384 + qt * 4096),                  \
      "{s58}"(sb + AW_CNT + 8 + 4 * hh), "{s59}"(sb + AW_CNT), "{s60}"(NSEG), "{s[36:37]}"(dbg)                                  \
      : "memory", "vcc", "scc", "s80"
  if constexpr (AT) {
    const v8u pv = {tk + l * 16, (c & 8) ? 0xEEEEu : 0x4444u, (l & 31) * 4u, (unsigned)(OFF_MEMV - OFF_MEMK) + c * 32 + (g & 1) * 16 + (g >> 1) * 1024,
                    voff, (unsigned)l * 16, tk + qt * 4096 + l * 16, sb + AW_SO + w * 4096 + l * 16};
    const unsigned neg = 0xff61b1e6u;
    v8u bt;
#pragma unroll
    for (int i = 0; i < 4; ++i) {
      const unsigned d = 4 * g + i > c ? neg : 0u;
      bt[i] = qt ? 0u : d;
      bt[4 + i] = qt ? d : neg;
    }
    asm volatile("s_mov_b64 exec, -1\ns_waitcnt lgkmcnt(0)\ns_nop 4\n" AW_ASM_X ".LKG_LAST:\n"
                 ::"{v[192:199]}"(wo[0][0]), "{v[200:207]}"(wo[0][1]), "{v[208:215]}"(wo[0][2]), "{v[216:223]}"(wo[0][3]),
                 "{v[224:231]}"(wo[1][0]), "{v[232:239]}"(wo[1][1]), "{v[240:247]}"(wo[1][2]), "{v[248:255]}"(wo[1][3]),
                 "{v[0:7]}"(pv), "{v[8:15]}"(bt), AW_SOPS);
  } else {
    const v4u pv = {sb + AW_SO + l * 16, voff, (unsigned)l * 16, (l & 2) ? 16u + 8 * qt : qt ? 8u * (l & 1) : dp + 4 * (l & 1)};
    asm volatile("s_mov_b64 exec, -1\ns_waitcnt vmcnt(0) lgkmcnt(0)\ns_nop 4\n" AW_ASM_Y
                 ::"{v[64:71]}"(wo[0][0]), "{v[72:79]}"(wo[0][1]), "{v[80:87]}"(wo[0][2]), "{v[88:95]}"(wo[0][3]),
                 "{v[96:103]}"(wo[1][0]), "{v[104:111]}"(wo[1][1]), "{v[112:119]}"(wo[1][2]), "{v[120:127]}"(wo[1][3]),
                 "{v[128:135]}"(wo[2][0]), "{v[136:143]}"(wo[2][1]), "{v[144:151]}"(wo[2][2]), "{v[152:159]}"(wo[2][3]),
                 "{v[160:167]}"(wo[3][0]), "{v[168:175]}"(wo[3][1]), "{v[176:183]}"(wo[3][2]), "{v[184:191]}"(wo[3][3]),
                 "{v[192:199]}"(wo[4][0]), "{v[200:207]}"(wo[4][1]), "{v[208:215]}"(wo[4][2]), "{v[216:223]}"(wo[4][3]),
                 "{v[224:231]}"(wo[5][0]), "{v[232:239]}"(wo[5][1]), "{v[240:247]}"(wo[5][2]), "{v[248:255]}"(wo[5][3]),
                 "{v[0:3]}"(pv), AW_SOPS);
  }
#undef AW_SOPS
}
__device__ __forceinline__ void role_aw(const MegaArgs& A, char* smem, const int L, const int a, const u64 tag) {
  if (A.wv < 4) role_aw_t<true>(A, smem, L, a, tag);
  else role_aw_t<false>(A, smem, L, a, tag);
}

#define P_ASM \
  "s_mov_b32 s68, 0\n" \
  "s_mov_b32 s66, 3\n" \
  "s_mov_b32 s67, 2\n" \
  "s_mov_b32 s72, 0\n" \
  "v_mov_b32 v7, 1\n" \
  "v_mov_b32 v6, s70\n" \
  "v_mov_b32 v5, 0\n" \
  "v_add_u32 v10, 4096, v2\n" \
  ".p2align 6\n" \
  ".LCP_TOP_%=:\n" \
  "ds_read_b128 v[28:31], v1 offset:0\n" \
  "ds_read_b128 v[32:35], v1 offset:1024\n" \
  "s_add_u32 s66, s66, 3\n" \
  "s_add_u32 s67, s67, 2\n" \
  "s_mov_b32 s69, 0x400000\n" \
  "v_xor_b32 v0, 0x8000, v0\n" \
  "v_xor_b32 v3, 0x100, v3\n" \
  "v_readfirstlane_b32 s71, v5\n" \
  "s_cmp_ge_u32 s71, s66\n" \
  "s_cbranch_scc1 .LCP_GO_%=\n" \
  "s_cmp_eq_u32 s68, 1\n" \
  "s_cbranch_scc0 .LCP_X1_%=\n" \
  "s_waitcnt vmcnt(0)\n" \
  "s_mov_b64 exec, 1\n" \
  "buffer_store_dword v7, off, s[56:59], s72 sc1\n" \
  "s_mov_b64 exec, -1\n" \
  "s_add_u32 s72, s72, 0x1000\n" \
  "s_mov_b32 s68, 0\n" \
  ".LCP_X1_%=:\n" \
  ".LCP_S2_%=:\n" \
  "s_sleep 1\n" \
  "ds_read_b32 v5, v6 offset:0\n" \
  "s_sub_u32 s69, s69, 1\n" \
  "s_cbranch_scc1 .LCP_END_%=\n" \
  "s_waitcnt lgkmcnt(0)\n" \
  "v_readfirstlane_b32 s71, v5\n" \
  "s_cmp_ge_u32 s71, s66\n" \
  "s_cbranch_scc1 .LCP_GO_%=\n" \
  "s_branch .LCP_S2_%=\n" \
  ".LCP_GO_%=:\n" \
  "ds_read_b128 v[12:15], v0 offset:0\n" \
  "ds_read_b128 v[16:19], v0 offset:2048\n" \
  "ds_read_b128 v[20:23], v0 offset:4096\n" \
  "ds_read_b128 v[24:27], v0 offset:6144\n" \
  "ds_read_b128 v[36:39], v1 offset:2048\n" \
  "s_waitcnt lgkmcnt(4)\n" \
  "v_mfma_f32_16x16x32_bf16 v[48:51], v[64:67], v[12:15], 0\n" \
  "v_mfma_f32_16x16x32_bf16 v[52:55], v[128:131], v[12:15], 0\n" \
  "v_mfma_f32_16x16x32_bf16 v[56:59], v[192:195], v[12:15], 0\n" \
  "v_mfma_f32_16x16x32_bf16 v[60:63], v[28:31], v[12:15], 0\n" \
  "ds_read_b128 v[12:15], v0 offset:8192\n" \
  "ds_read_b128 v[28:31], v1 offset:3072\n" \
  "s_waitcnt lgkmcnt(5)\n" \
  "v_mfma_f32_16x16x32_bf16 v[48:51], v[68:71], v[16:19], v[48:51]\n" \
  "v_mfma_f32_16x16x32_bf16 v[52:55], v[132:135], v[16:19], v[52:55]\n" \
  "v_mfma_f32_16x16x32_bf16 v[56:59], v[196:199], v[16:19], v[56:59]\n" \
  "v_mfma_f32_16x16x32_bf16 v[60:63], v[32:35], v[16:19], v[60:63]\n" \
  "ds_read_b128 v[16:19], v0 offset:10240\n" \
  "ds_read_b128 v[32:35], v1 offset:4096\n" \
  "s_waitcnt lgkmcnt(6)\n" \
  "v_mfma_f32_16x16x32_bf16 v[48:51], v[72:75], v[20:23], v[48:51]\n" \
  "v_mfma_f32_16x16x32_bf16 v[52:55], v[136:139], v[20:23], v[52:55]\n" \
  "v_mfma_f32_16x16x32_bf16 v[56:59], v[200:203], v[20:23], v[56:59]\n" \
  "s_waitcnt lgkmcnt(4)\n" \
  "v_mfma_f32_16x16x32_bf16 v[60:63], v[36:39], v[20:23], v[60:63]\n" \
  "ds_read_b128 v[20:23], v0 offset:12288\n" \
  "ds_read_b128 v[36:39], v1 offset:5120\n" \
  "v_mfma_f32_16x16x32_bf16 v[48:51], v[76:79], v[24:27], v[48:51]\n" \
  "v_mfma_f32_16x16x32_bf16 v[52:55], v[140:143], v[24:27], v[52:55]\n" \
  "v_mfma_f32_16x16x32_bf16 v[56:59], v[204:207], v[24:27], v[56:59]\n" \
  "s_waitcnt lgkmcnt(4)\n" \
  "v_mfma_f32_16x16x32_bf16 v[60:63], v[28:31], v[24:27], v[60:63]\n" \
  "ds_read_b128 v[24:27], v0 offset:14336\n" \
  "ds_read_b128 v[28:31], v1 offset:6144\n" \
  "v_mfma_f32_16x16x32_bf16 v[48:51], v[80:83], v[12:15], v[48:51]\n" \
  "v_mfma_f32_16x16x32_bf16 v[52:55], v[144:147], v[12:15], v[52:55]\n" \
  "v_mfma_f32_16x16x32_bf16 v[56:59], v[208:211], v[12:15], v[56:59]\n" \
  "s_waitcnt lgkmcnt(4)\n" \
  "v_mfma_f32_16x16x32_bf16 v[60:63], v[32:35], v[12:15], v[60:63]\n" \
  "ds_read_b128 v[12:15], v0 offset:16384\n" \
  "ds_read_b128 v[32:35], v1 offset:7168\n" \
  "v_mfma_f32_16x16x32_bf16 v[48:51], v[84:87], v[16:19], v[48:51]\n" \
  "v_mfma_f32_16x16x32_bf16 v[52:55], v[148:151], v[16:19], v[52:55]\n" \
  "v_mfma_f32_16x16x32_bf16 v[56:59], v[212:215], v[16:19], v[56:59]\n" \
  "s_waitcnt lgkmcnt(4)\n" \
  "v_mfma_f32_16x16x32_bf16 v[60:63], v[36:39], v[16:19], v[60:63]\n" \
  "ds_read_b128 v[16:19], v0 offset:18432\n" \
  "ds_read_b128 v[36:39], v1 offset:8192\n" \
  "v_mfma_f32_16x16x32_bf16 v[48:51], v[88:91], v[20:23], v[48:51]\n" \
  "v_mfma_f32_16x16x32_bf16 v[52:55], v[152:155], v[20:23], v[52:55]\n" \
  "v_mfma_f32_16x16x32_bf16 v[56:59], v[216:219], v[20:23], v[56:59]\n" \
  "s_waitcnt lgkmcnt(4)\n" \
  "v_mfma_f32_16x16x32_bf16 v[60:63], v[28:31], v[20:23], v[60:63]\n" \
  "ds_read_b128 v[20:23], v0 offset:20480\n" \
  "ds_read_b128 v[28:31], v1 offset:9216\n" \
  "v_mfma_f32_16x16x32_bf16 v[48:51], v[92:95], v[24:27], v[48:51]\n" \
  "v_mfma_f32_16x16x32_bf16 v[52:55], v[156:159], v[24:27], v[52:55]\n" \
  "v_mfma_f32_16x16x32_bf16 v[56:59], v[220:223], v[24:27], v[56:59]\n" \
  "s_waitcnt lgkmcnt(4)\n" \
  "v_mfma_f32_16x16x32_bf16 v[60:63], v[32:35], v[24:27], v[60:63]\n" \
  "ds_read_b128 v[24:27], v0 offset:22528\n" \
  "ds_read_b128 v[32:35], v1 offset:10240\n" \
  "v_mfma_f32_16x16x32_bf16 v[48:51], v[96:99], v[12:15], v[48:51]\n" \
  "v_mfma_f32_16x16x32_bf16 v[52:55], v[160:163], v[12:15], v[52:55]\n" \
  "v_mfma_f32_16x16x32_bf16 v[56:59], v[224:227], v[12:15], v[56:59]\n" \
  "s_waitcnt lgkmcnt(4)\n" \
  "v_mfma_f32_16x16x32_bf16 v[60:63], v[36:39], v[12:15], v[60:63]\n" \
  "ds_read_b128 v[12:15], v0 offset:24576\n" \
  "ds_read_b128 v[36:39], v1 offset:11264\n" \
  "v_mfma_f32_16x16x32_bf16 v[48:51], v[100:103], v[16:19], v[48:51]\n" \
  "v_mfma_f32_16x16x32_bf16 v[52:55], v[164:167], v[16:19], v[52:55]\n" \
  "v_mfma_f32_16x16x32_bf16 v[56:59], v[228:231], v[16:19], v[56:59]\n" \
  "s_waitcnt lgkmcnt(4)\n" \
  "v_mfma_f32_16x16x32_bf16 v[60:63], v[28:31], v[16:19], v[60:63]\n" \
  "ds_read_b128 v[16:19], v0 offset:26624\n" \
  "ds_read_b128 v[28:31], v1 offset:12288\n" \
  "v_mfma_f32_16x16x32_bf16 v[48:51], v[104:107], v[20:23], v[48:51]\n" \
  "v_mfma_f32_16x16x32_bf16 v[52:55], v[168:171], v[20:23], v[52:55]\n" \
  "v_mfma_f32_16x16x32_bf16 v[56:59], v[232:235], v[20:23], v[56:59]\n" \
  "s_waitcnt lgkmcnt(4)\n" \
  "v_mfma_f32_16x16x32_bf16 v[60:63], v[32:35], v[20:23], v[60:63]\n" \
  "ds_read_b128 v[20:23], v0 offset:28672\n" \
  "ds_read_b128 v[32:35], v1 offset:13312\n" \
  "v_mfma_f32_16x16x32_bf16 v[48:51], v[108:111], v[24:27], v[48:51]\n" \
  "v_mfma_f32_16x16x32_bf16 v[52:55], v[172:175], v[24:27], v[52:55]\n" \
  "v_mfma_f32_16x16x32_bf16 v[56:59], v[236:239], v[24:27], v[56:59]\n" \
  "s_waitcnt lgkmcnt(4)\n" \
  "v_mfma_f32_16x16x32_bf16 v[60:63], v[36:39], v[24:27], v[60:63]\n" \
  "s_cmp_eq_u32 s68, 1\n" \
  "s_cbranch_scc0 .LCP_X3_%=\n" \
  "s_waitcnt vmcnt(0)\n" \
  "s_mov_b64 exec, 1\n" \
  "buffer_store_dword v7, off, s[56:59], s72 sc1\n" \
  "s_mov_b64 exec, -1\n" \
  "s_add_u32 s72, s72, 0x1000\n" \
  "s_mov_b32 s68, 0\n" \
  ".LCP_X3_%=:\n" \
  "ds_read_b32 v5, v6 offset:16\n" \
  "ds_read_b128 v[24:27], v0 offset:30720\n" \
  "ds_read_b128 v[36:39], v1 offset:14336\n" \
  "v_mfma_f32_16x16x32_bf16 v[48:51], v[112:115], v[12:15], v[48:51]\n" \
  "v_mfma_f32_16x16x32_bf16 v[52:55], v[176:179], v[12:15], v[52:55]\n" \
  "v_mfma_f32_16x16x32_bf16 v[56:59], v[240:243], v[12:15], v[56:59]\n" \
  "s_waitcnt lgkmcnt(5)\n" \
  "v_mfma_f32_16x16x32_bf16 v[60:63], v[28:31], v[12:15], v[60:63]\n" \
  "ds_read_b128 v[12:15], v0 offset:256\n" \
  "ds_read_b128 v[28:31], v1 offset:15360\n" \
  "ds_read_b128 v[40:43], v4 offset:0\n" \
  "ds_read_b128 v[44:47], v4 offset:64\n" \
  "v_mfma_f32_16x16x32_bf16 v[48:51], v[116:119], v[16:19], v[48:51]\n" \
  "v_mfma_f32_16x16x32_bf16 v[52:55], v[180:183], v[16:19], v[52:55]\n" \
  "v_mfma_f32_16x16x32_bf16 v[56:59], v[244:247], v[16:19], v[56:59]\n" \
  "s_waitcnt lgkmcnt(7)\n" \
  "v_mfma_f32_16x16x32_bf16 v[60:63], v[32:35], v[16:19], v[60:63]\n" \
  "s_waitcnt lgkmcnt(6)\n" \
  "v_readfirstlane_b32 s71, v5\n" \
  "s_cmp_ge_u32 s71, s67\n" \
  "s_cbranch_scc1 .LCP_ST_%=\n" \
  ".LCP_S4_%=:\n" \
  "s_sleep 1\n" \
  "ds_read_b32 v5, v6 offset:16\n" \
  "s_sub_u32 s69, s69, 1\n" \
  "s_cbranch_scc1 .LCP_END_%=\n" \
  "s_waitcnt lgkmcnt(0)\n" \
  "v_readfirstlane_b32 s71, v5\n" \
  "s_cmp_ge_u32 s71, s67\n" \
  "s_cbranch_scc1 .LCP_ST_%=\n" \
  "s_branch .LCP_S4_%=\n" \
  ".LCP_ST_%=:\n" \
  "ds_read_b128 v[16:19], v0 offset:2304\n" \
  "ds_read_b128 v[32:35], v4 offset:128\n" \
  "ds_read2_b32 v[8:9], v3 offset0:0 offset1:32\n" \
  "v_mfma_f32_16x16x32_bf16 v[48:51], v[120:123], v[20:23], v[48:51]\n" \
  "v_mfma_f32_16x16x32_bf16 v[52:55], v[184:187], v[20:23], v[52:55]\n" \
  "v_mfma_f32_16x16x32_bf16 v[56:59], v[248:251], v[20:23], v[56:59]\n" \
  "s_waitcnt lgkmcnt(7)\n" \
  "v_mfma_f32_16x16x32_bf16 v[60:63], v[36:39], v[20:23], v[60:63]\n" \
  "ds_read_b128 v[20:23], v0 offset:4352\n" \
  "ds_read_b128 v[36:39], v4 offset:192\n" \
  "v_mfma_f32_16x16x32_bf16 v[48:51], v[124:127], v[24:27], v[48:51]\n" \
  "v_mfma_f32_16x16x32_bf16 v[52:55], v[188:191], v[24:27], v[52:55]\n" \
  "v_mfma_f32_16x16x32_bf16 v[56:59], v[252:255], v[24:27], v[56:59]\n" \
  "s_waitcnt lgkmcnt(7)\n" \
  "v_mfma_f32_16x16x32_bf16 v[60:63], v[28:31], v[24:27], v[60:63]\n" \
  "s_waitcnt lgkmcnt(0)\n" \
  "s_nop 4\n" \
  "v_fma_f32 v48, -v8, v40, v48\n" \
  "v_fma_f32 v49, -v8, v41, v49\n" \
  "v_fma_f32 v50, -v8, v42, v50\n" \
  "v_fma_f32 v51, -v8, v43, v51\n" \
  "v_fma_f32 v52, -v8, v44, v52\n" \
  "v_fma_f32 v53, -v8, v45, v53\n" \
  "v_fma_f32 v54, -v8, v46, v54\n" \
  "v_fma_f32 v55, -v8, v47, v55\n" \
  "v_fma_f32 v56, -v8, v32, v56\n" \
  "v_fma_f32 v57, -v8, v33, v57\n" \
  "v_fma_f32 v58, -v8, v34, v58\n" \
  "v_fma_f32 v59, -v8, v35, v59\n" \
  "v_fma_f32 v60, -v8, v36, v60\n" \
  "v_fma_f32 v61, -v8, v37, v61\n" \
  "v_fma_f32 v62, -v8, v38, v62\n" \
  "v_fma_f32 v63, -v8, v39, v63\n" \
  "ds_read_b128 v[32:35], v1 offset:0\n" \
  "ds_read_b128 v[36:39], v1 offset:1024\n" \
  "v_mul_f32 v48, v48, v9\n" \
  "v_mul_f32 v49, v49, v9\n" \
  "v_mul_f32 v50, v50, v9\n" \
  "v_mul_f32 v51, v51, v9\n" \
  "v_mul_f32 v52, v52, v9\n" \
  "v_mul_f32 v53, v53, v9\n" \
  "v_mul_f32 v54, v54, v9\n" \
  "v_mul_f32 v55, v55, v9\n" \
  "v_cvt_pk_bf16_f32 v40, v48, v49\n" \
  "v_cvt_pk_bf16_f32 v41, v50, v51\n" \
  "v_cvt_pk_bf16_f32 v42, v52, v53\n" \
  "v_cvt_pk_bf16_f32 v43, v54, v55\n" \
  "buffer_store_dwordx4 v[40:43], v2, s[40:43], s65 offen offset:0 sc1\n" \
  "v_mul_f32 v56, v56, v9\n" \
  "v_mul_f32 v57, v57, v9\n" \
  "v_mul_f32 v58, v58, v9\n" \
  "v_mul_f32 v59, v59, v9\n" \
  "v_mul_f32 v60, v60, v9\n" \
  "v_mul_f32 v61, v61, v9\n" \
  "v_mul_f32 v62, v62, v9\n" \
  "v_mul_f32 v63, v63, v9\n" \
  "v_cvt_pk_bf16_f32 v44, v56, v57\n" \
  "v_cvt_pk_bf16_f32 v45, v58, v59\n" \
  "v_cvt_pk_bf16_f32 v46, v60, v61\n" \
  "v_cvt_pk_bf16_f32 v47, v62, v63\n" \
  "buffer_store_dwordx4 v[44:47], v2, s[40:43], s65 offen offset:1024 sc1\n" \
  "ds_read_b128 v[24:27], v0 offset:6400\n" \
  "ds_read_b128 v[28:31], v1 offset:2048\n" \
  "v_mfma_f32_16x16x32_bf16 v[48:51], v[64:67], v[12:15], 0\n" \
  "v_mfma_f32_16x16x32_bf16 v[52:55], v[128:131], v[12:15], 0\n" \
  "v_mfma_f32_16x16x32_bf16 v[56:59], v[192:195], v[12:15], 0\n" \
  "s_waitcnt lgkmcnt(3)\n" \
  "v_mfma_f32_16x16x32_bf16 v[60:63], v[32:35], v[12:15], 0\n" \
  "ds_read_b128 v[12:15], v0 offset:8448\n" \
  "ds_read_b128 v[32:35], v1 offset:3072\n" \
  "v_mfma_f32_16x16x32_bf16 v[48:51], v[68:71], v[16:19], v[48:51]\n" \
  "v_mfma_f32_16x16x32_bf16 v[52:55], v[132:135], v[16:19], v[52:55]\n" \
  "v_mfma_f32_16x16x32_bf16 v[56:59], v[196:199], v[16:19], v[56:59]\n" \
  "s_waitcnt lgkmcnt(4)\n" \
  "v_mfma_f32_16x16x32_bf16 v[60:63], v[36:39], v[16:19], v[60:63]\n" \
  "ds_read_b128 v[16:19], v0 offset:10496\n" \
  "ds_read_b128 v[36:39], v1 offset:4096\n" \
  "v_mfma_f32_16x16x32_bf16 v[48:51], v[72:75], v[20:23], v[48:51]\n" \
  "v_mfma_f32_16x16x32_bf16 v[52:55], v[136:139], v[20:23], v[52:55]\n" \
  "v_mfma_f32_16x16x32_bf16 v[56:59], v[200:203], v[20:23], v[56:59]\n" \
  "s_waitcnt lgkmcnt(4)\n" \
  "v_mfma_f32_16x16x32_bf16 v[60:63], v[28:31], v[20:23], v[60:63]\n" \
  "ds_read_b128 v[20:23], v0 offset:12544\n" \
  "ds_read_b128 v[28:31], v1 offset:5120\n" \
  "v_mfma_f32_16x16x32_bf16 v[48:51], v[76:79], v[24:27], v[48:51]\n" \
  "v_mfma_f32_16x16x32_bf16 v[52:55], v[140:143], v[24:27], v[52:55]\n" \
  "v_mfma_f32_16x16x32_bf16 v[56:59], v[204:207], v[24:27], v[56:59]\n" \
  "s_waitcnt lgkmcnt(4)\n" \
  "v_mfma_f32_16x16x32_bf16 v[60:63], v[32:35], v[24:27], v[60:63]\n" \
  "ds_read_b128 v[24:27], v0 offset:14592\n" \
  "ds_read_b128 v[32:35], v1 offset:6144\n" \
  "v_mfma_f32_16x16x32_bf16 v[48:51], v[80:83], v[12:15], v[48:51]\n" \
  "v_mfma_f32_16x16x32_bf16 v[52:55], v[144:147], v[12:15], v[52:55]\n" \
  "v_mfma_f32_16x16x32_bf16 v[56:59], v[208:211], v[12:15], v[56:59]\n" \
  "s_waitcnt lgkmcnt(4)\n" \
  "v_mfma_f32_16x16x32_bf16 v[60:63], v[36:39], v[12:15], v[60:63]\n" \
  "ds_read_b128 v[12:15], v0 offset:16640\n" \
  "ds_read_b128 v[36:39], v1 offset:7168\n" \
  "v_mfma_f32_16x16x32_bf16 v[48:51], v[84:87], v[16:19], v[48:51]\n" \
  "v_mfma_f32_16x16x32_bf16 v[52:55], v[148:151], v[16:19], v[52:55]\n" \
  "v_mfma_f32_16x16x32_bf16 v[56:59], v[212:215], v[16:19], v[56:59]\n" \
  "s_waitcnt lgkmcnt(4)\n" \
  "v_mfma_f32_16x16x32_bf16 v[60:63], v[28:31], v[16:19], v[60:63]\n" \
  "ds_read_b128 v[16:19], v0 offset:18688\n" \
  "ds_read_b128 v[28:31], v1 offset:8192\n" \
  "v_mfma_f32_16x16x32_bf16 v[48:51], v[88:91], v[20:23], v[48:51]\n" \
  "v_mfma_f32_16x16x32_bf16 v[52:55], v[152:155], v[20:23], v[52:55]\n" \
  "v_mfma_f32_16x16x32_bf16 v[56:59], v[216:219], v[20:23], v[56:59]\n" \
  "s_waitcnt lgkmcnt(4)\n" \
  "v_mfma_f32_16x16x32_bf16 v[60:63], v[32:35], v[20:23], v[60:63]\n" \
  "ds_read_b128 v[20:23], v0 offset:20736\n" \
  "ds_read_b128 v[32:35], v1 offset:9216\n" \
  "v_mfma_f32_16x16x32_bf16 v[48:51], v[92:95], v[24:27], v[48:51]\n" \
  "v_mfma_f32_16x16x32_bf16 v[52:55], v[156:159], v[24:27], v[52:55]\n" \
  "v_mfma_f32_16x16x32_bf16 v[56:59], v[220:223], v[24:27], v[56:59]\n" \
  "s_waitcnt lgkmcnt(4)\n" \
  "v_mfma_f32_16x16x32_bf16 v[60:63], v[36:39], v[24:27], v[60:63]\n" \
  "ds_read_b128 v[24:27], v0 offset:22784\n" \
  "ds_read_b128 v[36:39], v1 offset:10240\n" \
  "v_mfma_f32_16x16x32_bf16 v[48:51], v[96:99], v[12:15], v[48:51]\n" \
  "v_mfma_f32_16x16x32_bf16 v[52:55], v[160:163], v[12:15], v[52:55]\n" \
  "v_mfma_f32_16x16x32_bf16 v[56:59], v[224:227], v[12:15], v[56:59]\n" \
  "s_waitcnt lgkmcnt(4)\n" \
  "v_mfma_f32_16x16x32_bf16 v[60:63], v[28:31], v[12:15], v[60:63]\n" \
  "ds_read_b128 v[12:15], v0 offset:24832\n" \
  "ds_read_b128 v[28:31], v1 offset:11264\n" \
  "v_mfma_f32_16x16x32_bf16 v[48:51], v[100:103], v[16:19], v[48:51]\n" \
  "v_mfma_f32_16x16x32_bf16 v[52:55], v[164:167], v[16:19], v[52:55]\n" \
  "v_mfma_f32_16x16x32_bf16 v[56:59], v[228:231], v[16:19], v[56:59]\n" \
  "s_waitcnt lgkmcnt(4)\n" \
  "v_mfma_f32_16x16x32_bf16 v[60:63], v[32:35], v[16:19], v[60:63]\n" \
  "ds_read_b128 v[16:19], v0 offset:26880\n" \
  "ds_read_b128 v[32:35], v1 offset:12288\n" \
  "v_mfma_f32_16x16x32_bf16 v[48:51], v[104:107], v[20:23], v[48:51]\n" \
  "v_mfma_f32_16x16x32_bf16 v[52:55], v[168:171], v[20:23], v[52:55]\n" \
  "v_mfma_f32_16x16x32_bf16 v[56:59], v[232:235], v[20:23], v[56:59]\n" \
  "s_waitcnt lgkmcnt(4)\n" \
  "v_mfma_f32_16x16x32_bf16 v[60:63], v[36:39], v[20:23], v[60:63]\n" \
  "ds_read_b128 v[20:23], v0 offset:28928\n" \
  "ds_read_b128 v[36:39], v1 offset:13312\n" \
  "v_mfma_f32_16x16x32_bf16 v[48:51], v[108:111], v[24:27], v[48:51]\n" \
  "v_mfma_f32_16x16x32_bf16 v[52:55], v[172:175], v[24:27], v[52:55]\n" \
  "v_mfma_f32_16x16x32_bf16 v[56:59], v[236:239], v[24:27], v[56:59]\n" \
  "s_waitcnt lgkmcnt(4)\n" \
  "v_mfma_f32_16x16x32_bf16 v[60:63], v[28:31], v[24:27], v[60:63]\n" \
  "ds_read_b128 v[24:27], v0 offset:30976\n" \
  "ds_read_b128 v[28:31], v1 offset:14336\n" \
  "v_mfma_f32_16x16x32_bf16 v[48:51], v[112:115], v[12:15], v[48:51]\n" \
  "v_mfma_f32_16x16x32_bf16 v[52:55], v[176:179], v[12:15], v[52:55]\n" \
  "v_mfma_f32_16x16x32_bf16 v[56:59], v[240:243], v[12:15], v[56:59]\n" \
  "s_waitcnt lgkmcnt(4)\n" \
  "v_mfma_f32_16x16x32_bf16 v[60:63], v[32:35], v[12:15], v[60:63]\n" \
  "ds_read_b128 v[32:35], v1 offset:15360\n" \
  "ds_read_b128 v[40:43], v4 offset:0\n" \
  "ds_read_b128 v[44:47], v4 offset:64\n" \
  "v_mfma_f32_16x16x32_bf16 v[48:51], v[116:119], v[16:19], v[48:51]\n" \
  "v_mfma_f32_16x16x32_bf16 v[52:55], v[180:183], v[16:19], v[52:55]\n" \
  "v_mfma_f32_16x16x32_bf16 v[56:59], v[244:247], v[16:19], v[56:59]\n" \
  "s_waitcnt lgkmcnt(5)\n" \
  "v_mfma_f32_16x16x32_bf16 v[60:63], v[36:39], v[16:19], v[60:63]\n" \
  "ds_read_b128 v[36:39], v4 offset:128\n" \
  "ds_read2_b32 v[8:9], v3 offset0:16 offset1:48\n" \
  "v_mfma_f32_16x16x32_bf16 v[48:51], v[120:123], v[20:23], v[48:51]\n" \
  "v_mfma_f32_16x16x32_bf16 v[52:55], v[184:187], v[20:23], v[52:55]\n" \
  "v_mfma_f32_16x16x32_bf16 v[56:59], v[248:251], v[20:23], v[56:59]\n" \
  "s_waitcnt lgkmcnt(5)\n" \
  "v_mfma_f32_16x16x32_bf16 v[60:63], v[28:31], v[20:23], v[60:63]\n" \
  "ds_read_b128 v[28:31], v4 offset:192\n" \
  "s_mov_b64 exec, 1\n" \
  "ds_add_u32 v6, v7 offset:8\n" \
  "s_mov_b64 exec, -1\n" \
  "v_xor_b32 v6, 4, v6\n" \
  "v_mfma_f32_16x16x32_bf16 v[48:51], v[124:127], v[24:27], v[48:51]\n" \
  "v_mfma_f32_16x16x32_bf16 v[52:55], v[188:191], v[24:27], v[52:55]\n" \
  "v_mfma_f32_16x16x32_bf16 v[56:59], v[252:255], v[24:27], v[56:59]\n" \
  "s_waitcnt lgkmcnt(6)\n" \
  "v_mfma_f32_16x16x32_bf16 v[60:63], v[32:35], v[24:27], v[60:63]\n" \
  "s_waitcnt lgkmcnt(1)\n" \
  "s_nop 4\n" \
  "ds_read_b32 v5, v6 offset:0\n" \
  "v_fma_f32 v48, -v8, v40, v48\n" \
  "v_fma_f32 v49, -v8, v41, v49\n" \
  "v_fma_f32 v50, -v8, v42, v50\n" \
  "v_fma_f32 v51, -v8, v43, v51\n" \
  "v_fma_f32 v52, -v8, v44, v52\n" \
  "v_fma_f32 v53, -v8, v45, v53\n" \
  "v_fma_f32 v54, -v8, v46, v54\n" \
  "v_fma_f32 v55, -v8, v47, v55\n" \
  "v_fma_f32 v56, -v8, v36, v56\n" \
  "v_fma_f32 v57, -v8, v37, v57\n" \
  "v_fma_f32 v58, -v8, v38, v58\n" \
  "v_fma_f32 v59, -v8, v39, v59\n" \
  "v_fma_f32 v60, -v8, v28, v60\n" \
  "v_fma_f32 v61, -v8, v29, v61\n" \
  "v_fma_f32 v62, -v8, v30, v62\n" \
  "v_fma_f32 v63, -v8, v31, v63\n" \
  "v_mul_f32 v48, v48, v9\n" \
  "v_mul_f32 v49, v49, v9\n" \
  "v_mul_f32 v50, v50, v9\n" \
  "v_mul_f32 v51, v51, v9\n" \
  "v_mul_f32 v52, v52, v9\n" \
  "v_mul_f32 v53, v53, v9\n" \
  "v_mul_f32 v54, v54, v9\n" \
  "v_mul_f32 v55, v55, v9\n" \
  "v_cvt_pk_bf16_f32 v40, v48, v49\n" \
  "v_cvt_pk_bf16_f32 v41, v50, v51\n" \
  "v_cvt_pk_bf16_f32 v42, v52, v53\n" \
  "v_cvt_pk_bf16_f32 v43, v54, v55\n" \
  "buffer_store_dwordx4 v[40:43], v10, s[40:43], s65 offen offset:0 sc1\n" \
  "v_mul_f32 v56, v56, v9\n" \
  "v_mul_f32 v57, v57, v9\n" \
  "v_mul_f32 v58, v58, v9\n" \
  "v_mul_f32 v59, v59, v9\n" \
  "v_mul_f32 v60, v60, v9\n" \
  "v_mul_f32 v61, v61, v9\n" \
  "v_mul_f32 v62, v62, v9\n" \
  "v_mul_f32 v63, v63, v9\n" \
  "v_cvt_pk_bf16_f32 v44, v56, v57\n" \
  "v_cvt_pk_bf16_f32 v45, v58, v59\n" \
  "v_cvt_pk_bf16_f32 v46, v60, v61\n" \
  "v_cvt_pk_bf16_f32 v47, v62, v63\n" \
  "buffer_store_dwordx4 v[44:47], v10, s[40:43], s65 offen offset:1024 sc1\n" \
  "s_waitcnt lgkmcnt(0)\n" \
  "s_add_u32 s65, s65, 0x80000\n" \
  "s_sub_u32 s64, s64, 1\n" \
  "s_mov_b32 s68, 1\n" \
  "s_cmp_lg_u32 s64, 0\n" \
  "s_cbranch_scc1 .LCP_TOP_%=\n" \
  "s_waitcnt vmcnt(0)\n" \
  "s_mov_b64 exec, 1\n" \
  "buffer_store_dword v7, off, s[56:59], s72 sc1\n" \
  "s_mov_b64 exec, -1\n" \
  "s_add_u32 s72, s72, 0x1000\n" \
  "s_mov_b32 s68, 0\n" \
  ".LCP_END_%=:\n" \
  "s_endpgm\n"
#define V_ASM \
  "s_mov_b32 s68, 0\n" \
  "s_mov_b32 s66, 3\n" \
  "s_mov_b32 s67, 2\n" \
  "s_mov_b32 s72, 0\n" \
  "v_mov_b32 v7, 1\n" \
  "v_mov_b32 v6, s70\n" \
  "v_mov_b32 v5, 0\n" \
  "ds_read_b32 v8, v4 offset:0\n" \
  "ds_read_b32 v9, v4 offset:64\n" \
  "ds_read_b32 v10, v4 offset:128\n" \
  "ds_read_b32 v4, v4 offset:192\n" \
  "s_waitcnt lgkmcnt(0)\n" \
  ".p2align 6\n" \
  ".LCW_TOP_%=:\n" \
  "ds_read_b128 v[28:31], v1 offset:0\n" \
  "ds_read_b128 v[32:35], v1 offset:1024\n" \
  "s_add_u32 s66, s66, 3\n" \
  "s_add_u32 s67, s67, 2\n" \
  "s_mov_b32 s69, 0x400000\n" \
  "v_xor_b32 v0, 0x8000, v0\n" \
  "v_xor_b32 v3, 0x100, v3\n" \
  "v_readfirstlane_b32 s71, v5\n" \
  "s_cmp_ge_u32 s71, s66\n" \
  "s_cbranch_scc1 .LCW_GO_%=\n" \
  "s_cmp_eq_u32 s68, 1\n" \
  "s_cbranch_scc0 .LCW_X1_%=\n" \
  "s_waitcnt vmcnt(0)\n" \
  "s_mov_b64 exec, 1\n" \
  "buffer_store_dword v7, off, s[56:59], s72 sc1\n" \
  "s_mov_b64 exec, -1\n" \
  "s_add_u32 s72, s72, 0x1000\n" \
  "s_mov_b32 s68, 0\n" \
  ".LCW_X1_%=:\n" \
  ".LCW_S2_%=:\n" \
  "s_sleep 1\n" \
  "ds_read_b32 v5, v6 offset:0\n" \
  "s_sub_u32 s69, s69, 1\n" \
  "s_cbranch_scc1 .LCW_END_%=\n" \
  "s_waitcnt lgkmcnt(0)\n" \
  "v_readfirstlane_b32 s71, v5\n" \
  "s_cmp_ge_u32 s71, s66\n" \
  "s_cbranch_scc1 .LCW_GO_%=\n" \
  "s_branch .LCW_S2_%=\n" \
  ".LCW_GO_%=:\n" \
  "ds_read_b128 v[12:15], v0 offset:0\n" \
  "ds_read_b128 v[16:19], v0 offset:2048\n" \
  "ds_read_b128 v[20:23], v0 offset:4096\n" \
  "ds_read_b128 v[24:27], v0 offset:6144\n" \
  "ds_read_b128 v[36:39], v1 offset:2048\n" \
  "s_waitcnt lgkmcnt(4)\n" \
  "v_mfma_f32_16x16x32_bf16 v[42:45], v[12:15], v[64:67], 0\n" \
  "v_mfma_f32_16x16x32_bf16 v[48:51], v[12:15], v[128:131], 0\n" \
  "v_mfma_f32_16x16x32_bf16 v[54:57], v[12:15], v[192:195], 0\n" \
  "v_mfma_f32_16x16x32_bf16 v[60:63], v[12:15], v[28:31], 0\n" \
  "ds_read_b128 v[12:15], v0 offset:8192\n" \
  "ds_read_b128 v[28:31], v1 offset:3072\n" \
  "s_waitcnt lgkmcnt(5)\n" \
  "v_mfma_f32_16x16x32_bf16 v[42:45], v[16:19], v[68:71], v[42:45]\n" \
  "v_mfma_f32_16x16x32_bf16 v[48:51], v[16:19], v[132:135], v[48:51]\n" \
  "v_mfma_f32_16x16x32_bf16 v[54:57], v[16:19], v[196:199], v[54:57]\n" \
  "v_mfma_f32_16x16x32_bf16 v[60:63], v[16:19], v[32:35], v[60:63]\n" \
  "ds_read_b128 v[16:19], v0 offset:10240\n" \
  "ds_read_b128 v[32:35], v1 offset:4096\n" \
  "s_waitcnt lgkmcnt(6)\n" \
  "v_mfma_f32_16x16x32_bf16 v[42:45], v[20:23], v[72:75], v[42:45]\n" \
  "v_mfma_f32_16x16x32_bf16 v[48:51], v[20:23], v[136:139], v[48:51]\n" \
  "v_mfma_f32_16x16x32_bf16 v[54:57], v[20:23], v[200:203], v[54:57]\n" \
  "s_waitcnt lgkmcnt(4)\n" \
  "v_mfma_f32_16x16x32_bf16 v[60:63], v[20:23], v[36:39], v[60:63]\n" \
  "ds_read_b128 v[20:23], v0 offset:12288\n" \
  "ds_read_b128 v[36:39], v1 offset:5120\n" \
  "v_mfma_f32_16x16x32_bf16 v[42:45], v[24:27], v[76:79], v[42:45]\n" \
  "v_mfma_f32_16x16x32_bf16 v[48:51], v[24:27], v[140:143], v[48:51]\n" \
  "v_mfma_f32_16x16x32_bf16 v[54:57], v[24:27], v[204:207], v[54:57]\n" \
  "s_waitcnt lgkmcnt(4)\n" \
  "v_mfma_f32_16x16x32_bf16 v[60:63], v[24:27], v[28:31], v[60:63]\n" \
  "ds_read_b128 v[24:27], v0 offset:14336\n" \
  "ds_read_b128 v[28:31], v1 offset:6144\n" \
  "v_mfma_f32_16x16x32_bf16 v[42:45], v[12:15], v[80:83], v[42:45]\n" \
  "v_mfma_f32_16x16x32_bf16 v[48:51], v[12:15], v[144:147], v[48:51]\n" \
  "v_mfma_f32_16x16x32_bf16 v[54:57], v[12:15], v[208:211], v[54:57]\n" \
  "s_waitcnt lgkmcnt(4)\n" \
  "v_mfma_f32_16x16x32_bf16 v[60:63], v[12:15], v[32:35], v[60:63]\n" \
  "ds_read_b128 v[12:15], v0 offset:16384\n" \
  "ds_read_b128 v[32:35], v1 offset:7168\n" \
  "v_mfma_f32_16x16x32_bf16 v[42:45], v[16:19], v[84:87], v[42:45]\n" \
  "v_mfma_f32_16x16x32_bf16 v[48:51], v[16:19], v[148:151], v[48:51]\n" \
  "v_mfma_f32_16x16x32_bf16 v[54:57], v[16:19], v[212:215], v[54:57]\n" \
  "s_waitcnt lgkmcnt(4)\n" \
  "v_mfma_f32_16x16x32_bf16 v[60:63], v[16:19], v[36:39], v[60:63]\n" \
  "ds_read_b128 v[16:19], v0 offset:18432\n" \
  "ds_read_b128 v[36:39], v1 offset:8192\n" \
  "v_mfma_f32_16x16x32_bf16 v[42:45], v[20:23], v[88:91], v[42:45]\n" \
  "v_mfma_f32_16x16x32_bf16 v[48:51], v[20:23], v[152:155], v[48:51]\n" \
  "v_mfma_f32_16x16x32_bf16 v[54:57], v[20:23], v[216:219], v[54:57]\n" \
  "s_waitcnt lgkmcnt(4)\n" \
  "v_mfma_f32_16x16x32_bf16 v[60:63], v[20:23], v[28:31], v[60:63]\n" \
  "ds_read_b128 v[20:23], v0 offset:20480\n" \
  "ds_read_b128 v[28:31], v1 offset:9216\n" \
  "v_mfma_f32_16x16x32_bf16 v[42:45], v[24:27], v[92:95], v[42:45]\n" \
  "v_mfma_f32_16x16x32_bf16 v[48:51], v[24:27], v[156:159], v[48:51]\n" \
  "v_mfma_f32_16x16x32_bf16 v[54:57], v[24:27], v[220:223], v[54:57]\n" \
  "s_waitcnt lgkmcnt(4)\n" \
  "v_mfma_f32_16x16x32_bf16 v[60:63], v[24:27], v[32:35], v[60:63]\n" \
  "ds_read_b128 v[24:27], v0 offset:22528\n" \
  "ds_read_b128 v[32:35], v1 offset:10240\n" \
  "v_mfma_f32_16x16x32_bf16 v[42:45], v[12:15], v[96:99], v[42:45]\n" \
  "v_mfma_f32_16x16x32_bf16 v[48:51], v[12:15], v[160:163], v[48:51]\n" \
  "v_mfma_f32_16x16x32_bf16 v[54:57], v[12:15], v[224:227], v[54:57]\n" \
  "s_waitcnt lgkmcnt(4)\n" \
  "v_mfma_f32_16x16x32_bf16 v[60:63], v[12:15], v[36:39], v[60:63]\n" \
  "ds_read_b128 v[12:15], v0 offset:24576\n" \
  "ds_read_b128 v[36:39], v1 offset:11264\n" \
  "v_mfma_f32_16x16x32_bf16 v[42:45], v[16:19], v[100:103], v[42:45]\n" \
  "v_mfma_f32_16x16x32_bf16 v[48:51], v[16:19], v[164:167], v[48:51]\n" \
  "v_mfma_f32_16x16x32_bf16 v[54:57], v[16:19], v[228:231], v[54:57]\n" \
  "s_waitcnt lgkmcnt(4)\n" \
  "v_mfma_f32_16x16x32_bf16 v[60:63], v[16:19], v[28:31], v[60:63]\n" \
  "ds_read_b128 v[16:19], v0 offset:26624\n" \
  "ds_read_b128 v[28:31], v1 offset:12288\n" \
  "v_mfma_f32_16x16x32_bf16 v[42:45], v[20:23], v[104:107], v[42:45]\n" \
  "v_mfma_f32_16x16x32_bf16 v[48:51], v[20:23], v[168:171], v[48:51]\n" \
  "v_mfma_f32_16x16x32_bf16 v[54:57], v[20:23], v[232:235], v[54:57]\n" \
  "s_waitcnt lgkmcnt(4)\n" \
  "v_mfma_f32_16x16x32_bf16 v[60:63], v[20:23], v[32:35], v[60:63]\n" \
  "ds_read_b128 v[20:23], v0 offset:28672\n" \
  "ds_read_b128 v[32:35], v1 offset:13312\n" \
  "v_mfma_f32_16x16x32_bf16 v[42:45], v[24:27], v[108:111], v[42:45]\n" \
  "v_mfma_f32_16x16x32_bf16 v[48:51], v[24:27], v[172:175], v[48:51]\n" \
  "v_mfma_f32_16x16x32_bf16 v[54:57], v[24:27], v[236:239], v[54:57]\n" \
  "s_waitcnt lgkmcnt(4)\n" \
  "v_mfma_f32_16x16x32_bf16 v[60:63], v[24:27], v[36:39], v[60:63]\n" \
  "s_cmp_eq_u32 s68, 1\n" \
  "s_cbranch_scc0 .LCW_X3_%=\n" \
  "s_waitcnt vmcnt(0)\n" \
  "s_mov_b64 exec, 1\n" \
  "buffer_store_dword v7, off, s[56:59], s72 sc1\n" \
  "s_mov_b64 exec, -1\n" \
  "s_add_u32 s72, s72, 0x1000\n" \
  "s_mov_b32 s68, 0\n" \
  ".LCW_X3_%=:\n" \
  "ds_read_b32 v5, v6 offset:16\n" \
  "ds_read_b128 v[24:27], v0 offset:30720\n" \
  "ds_read_b128 v[36:39], v1 offset:14336\n" \
  "v_mfma_f32_16x16x32_bf16 v[42:45], v[12:15], v[112:115], v[42:45]\n" \
  "v_mfma_f32_16x16x32_bf16 v[48:51], v[12:15], v[176:179], v[48:51]\n" \
  "v_mfma_f32_16x16x32_bf16 v[54:57], v[12:15], v[240:243], v[54:57]\n" \
  "s_waitcnt lgkmcnt(5)\n" \
  "v_mfma_f32_16x16x32_bf16 v[60:63], v[12:15], v[28:31], v[60:63]\n" \
  "ds_read_b128 v[12:15], v0 offset:256\n" \
  "ds_read_b128 v[28:31], v1 offset:15360\n" \
  "v_mfma_f32_16x16x32_bf16 v[42:45], v[16:19], v[116:119], v[42:45]\n" \
  "v_mfma_f32_16x16x32_bf16 v[48:51], v[16:19], v[180:183], v[48:51]\n" \
  "v_mfma_f32_16x16x32_bf16 v[54:57], v[16:19], v[244:247], v[54:57]\n" \
  "s_waitcnt lgkmcnt(5)\n" \
  "v_mfma_f32_16x16x32_bf16 v[60:63], v[16:19], v[32:35], v[60:63]\n" \
  "s_waitcnt lgkmcnt(4)\n" \
  "v_readfirstlane_b32 s71, v5\n" \
  "s_cmp_ge_u32 s71, s67\n" \
  "s_cbranch_scc1 .LCW_ST_%=\n" \
  ".LCW_S4_%=:\n" \
  "s_sleep 1\n" \
  "ds_read_b32 v5, v6 offset:16\n" \
  "s_sub_u32 s69, s69, 1\n" \
  "s_cbranch_scc1 .LCW_END_%=\n" \
  "s_waitcnt lgkmcnt(0)\n" \
  "v_readfirstlane_b32 s71, v5\n" \
  "s_cmp_ge_u32 s71, s67\n" \
  "s_cbranch_scc1 .LCW_ST_%=\n" \
  "s_branch .LCW_S4_%=\n" \
  ".LCW_ST_%=:\n" \
  "ds_read_b128 v[16:19], v0 offset:2304\n" \
  "ds_read_b128 v[32:35], v3 offset:0\n" \
  "v_mfma_f32_16x16x32_bf16 v[42:45], v[20:23], v[120:123], v[42:45]\n" \
  "v_mfma_f32_16x16x32_bf16 v[48:51], v[20:23], v[184:187], v[48:51]\n" \
  "v_mfma_f32_16x16x32_bf16 v[54:57], v[20:23], v[248:251], v[54:57]\n" \
  "s_waitcnt lgkmcnt(4)\n" \
  "v_mfma_f32_16x16x32_bf16 v[60:63], v[20:23], v[36:39], v[60:63]\n" \
  "ds_read_b128 v[20:23], v0 offset:4352\n" \
  "ds_read_b128 v[36:39], v3 offset:128\n" \
  "v_mfma_f32_16x16x32_bf16 v[42:45], v[24:27], v[124:127], v[42:45]\n" \
  "v_mfma_f32_16x16x32_bf16 v[48:51], v[24:27], v[188:191], v[48:51]\n" \
  "v_mfma_f32_16x16x32_bf16 v[54:57], v[24:27], v[252:255], v[54:57]\n" \
  "s_waitcnt lgkmcnt(4)\n" \
  "v_mfma_f32_16x16x32_bf16 v[60:63], v[24:27], v[28:31], v[60:63]\n" \
  "s_waitcnt lgkmcnt(0)\n" \
  "s_nop 4\n" \
  "v_fma_f32 v42, -v32, v8, v42\n" \
  "v_fma_f32 v43, -v33, v8, v43\n" \
  "v_fma_f32 v44, -v34, v8, v44\n" \
  "v_fma_f32 v45, -v35, v8, v45\n" \
  "v_fma_f32 v48, -v32, v9, v48\n" \
  "v_fma_f32 v49, -v33, v9, v49\n" \
  "v_fma_f32 v50, -v34, v9, v50\n" \
  "v_fma_f32 v51, -v35, v9, v51\n" \
  "v_fma_f32 v54, -v32, v10, v54\n" \
  "v_fma_f32 v55, -v33, v10, v55\n" \
  "v_fma_f32 v56, -v34, v10, v56\n" \
  "v_fma_f32 v57, -v35, v10, v57\n" \
  "v_fma_f32 v60, -v32, v4, v60\n" \
  "v_fma_f32 v61, -v33, v4, v61\n" \
  "v_fma_f32 v62, -v34, v4, v62\n" \
  "v_fma_f32 v63, -v35, v4, v63\n" \
  "ds_read_b128 v[32:35], v1 offset:0\n" \
  "v_mul_f32 v42, v42, v36\n" \
  "v_mul_f32 v43, v43, v37\n" \
  "v_mul_f32 v44, v44, v38\n" \
  "v_mul_f32 v45, v45, v39\n" \
  "v_mul_f32 v48, v48, v36\n" \
  "v_mul_f32 v49, v49, v37\n" \
  "v_mul_f32 v50, v50, v38\n" \
  "v_mul_f32 v51, v51, v39\n" \
  "v_mul_f32 v54, v54, v36\n" \
  "v_mul_f32 v55, v55, v37\n" \
  "v_mul_f32 v56, v56, v38\n" \
  "v_mul_f32 v57, v57, v39\n" \
  "v_mul_f32 v60, v60, v36\n" \
  "v_mul_f32 v61, v61, v37\n" \
  "v_mul_f32 v62, v62, v38\n" \
  "v_mul_f32 v63, v63, v39\n" \
  "ds_read_b128 v[36:39], v1 offset:1024\n" \
  "v_cvt_pk_bf16_f32 v40, v42, v43\n" \
  "v_cvt_pk_bf16_f32 v41, v44, v45\n" \
  "v_cvt_pk_bf16_f32 v46, v48, v49\n" \
  "v_cvt_pk_bf16_f32 v47, v50, v51\n" \
  "v_cvt_pk_bf16_f32 v52, v54, v55\n" \
  "v_cvt_pk_bf16_f32 v53, v56, v57\n" \
  "v_cvt_pk_bf16_f32 v58, v60, v61\n" \
  "v_cvt_pk_bf16_f32 v59, v62, v63\n" \
  "ds_read_b128 v[24:27], v0 offset:6400\n" \
  "ds_read_b128 v[28:31], v1 offset:2048\n" \
  "v_mfma_f32_16x16x32_bf16 v[42:45], v[12:15], v[64:67], 0\n" \
  "v_mfma_f32_16x16x32_bf16 v[48:51], v[12:15], v[128:131], 0\n" \
  "v_mfma_f32_16x16x32_bf16 v[54:57], v[12:15], v[192:195], 0\n" \
  "s_waitcnt lgkmcnt(3)\n" \
  "v_mfma_f32_16x16x32_bf16 v[60:63], v[12:15], v[32:35], 0\n" \
  "ds_read_b128 v[12:15], v0 offset:8448\n" \
  "ds_read_b128 v[32:35], v1 offset:3072\n" \
  "v_mfma_f32_16x16x32_bf16 v[42:45], v[16:19], v[68:71], v[42:45]\n" \
  "v_mfma_f32_16x16x32_bf16 v[48:51], v[16:19], v[132:135], v[48:51]\n" \
  "v_mfma_f32_16x16x32_bf16 v[54:57], v[16:19], v[196:199], v[54:57]\n" \
  "s_waitcnt lgkmcnt(4)\n" \
  "v_mfma_f32_16x16x32_bf16 v[60:63], v[16:19], v[36:39], v[60:63]\n" \
  "ds_read_b128 v[16:19], v0 offset:10496\n" \
  "ds_read_b128 v[36:39], v1 offset:4096\n" \
  "v_mfma_f32_16x16x32_bf16 v[42:45], v[20:23], v[72:75], v[42:45]\n" \
  "v_mfma_f32_16x16x32_bf16 v[48:51], v[20:23], v[136:139], v[48:51]\n" \
  "v_mfma_f32_16x16x32_bf16 v[54:57], v[20:23], v[200:203], v[54:57]\n" \
  "s_waitcnt lgkmcnt(4)\n" \
  "v_mfma_f32_16x16x32_bf16 v[60:63], v[20:23], v[28:31], v[60:63]\n" \
  "ds_read_b128 v[20:23], v0 offset:12544\n" \
  "ds_read_b128 v[28:31], v1 offset:5120\n" \
  "v_mfma_f32_16x16x32_bf16 v[42:45], v[24:27], v[76:79], v[42:45]\n" \
  "v_mfma_f32_16x16x32_bf16 v[48:51], v[24:27], v[140:143], v[48:51]\n" \
  "v_mfma_f32_16x16x32_bf16 v[54:57], v[24:27], v[204:207], v[54:57]\n" \
  "s_waitcnt lgkmcnt(4)\n" \
  "v_mfma_f32_16x16x32_bf16 v[60:63], v[24:27], v[32:35], v[60:63]\n" \
  "ds_read_b128 v[24:27], v0 offset:14592\n" \
  "ds_read_b128 v[32:35], v1 offset:6144\n" \
  "v_mfma_f32_16x16x32_bf16 v[42:45], v[12:15], v[80:83], v[42:45]\n" \
  "v_mfma_f32_16x16x32_bf16 v[48:51], v[12:15], v[144:147], v[48:51]\n" \
  "v_mfma_f32_16x16x32_bf16 v[54:57], v[12:15], v[208:211], v[54:57]\n" \
  "s_waitcnt lgkmcnt(4)\n" \
  "v_mfma_f32_16x16x32_bf16 v[60:63], v[12:15], v[36:39], v[60:63]\n" \
  "ds_read_b128 v[12:15], v0 offset:16640\n" \
  "ds_read_b128 v[36:39], v1 offset:7168\n" \
  "v_mfma_f32_16x16x32_bf16 v[42:45], v[16:19], v[84:87], v[42:45]\n" \
  "v_mfma_f32_16x16x32_bf16 v[48:51], v[16:19], v[148:151], v[48:51]\n" \
  "v_mfma_f32_16x16x32_bf16 v[54:57], v[16:19], v[212:215], v[54:57]\n" \
  "s_waitcnt lgkmcnt(4)\n" \
  "v_mfma_f32_16x16x32_bf16 v[60:63], v[16:19], v[28:31], v[60:63]\n" \
  "ds_read_b128 v[16:19], v0 offset:18688\n" \
  "ds_read_b128 v[28:31], v1 offset:8192\n" \
  "v_mfma_f32_16x16x32_bf16 v[42:45], v[20:23], v[88:91], v[42:45]\n" \
  "v_mfma_f32_16x16x32_bf16 v[48:51], v[20:23], v[152:155], v[48:51]\n" \
  "v_mfma_f32_16x16x32_bf16 v[54:57], v[20:23], v[216:219], v[54:57]\n" \
  "s_waitcnt lgkmcnt(4)\n" \
  "v_mfma_f32_16x16x32_bf16 v[60:63], v[20:23], v[32:35], v[60:63]\n" \
  "ds_read_b128 v[20:23], v0 offset:20736\n" \
  "ds_read_b128 v[32:35], v1 offset:9216\n" \
  "v_mfma_f32_16x16x32_bf16 v[42:45], v[24:27], v[92:95], v[42:45]\n" \
  "v_mfma_f32_16x16x32_bf16 v[48:51], v[24:27], v[156:159], v[48:51]\n" \
  "v_mfma_f32_16x16x32_bf16 v[54:57], v[24:27], v[220:223], v[54:57]\n" \
  "s_waitcnt lgkmcnt(4)\n" \
  "v_mfma_f32_16x16x32_bf16 v[60:63], v[24:27], v[36:39], v[60:63]\n" \
  "ds_read_b128 v[24:27], v0 offset:22784\n" \
  "ds_read_b128 v[36:39], v1 offset:10240\n" \
  "v_mfma_f32_16x16x32_bf16 v[42:45], v[12:15], v[96:99], v[42:45]\n" \
  "v_mfma_f32_16x16x32_bf16 v[48:51], v[12:15], v[160:163], v[48:51]\n" \
  "v_mfma_f32_16x16x32_bf16 v[54:57], v[12:15], v[224:227], v[54:57]\n" \
  "s_waitcnt lgkmcnt(4)\n" \
  "v_mfma_f32_16x16x32_bf16 v[60:63], v[12:15], v[28:31], v[60:63]\n" \
  "ds_read_b128 v[12:15], v0 offset:24832\n" \
  "ds_read_b128 v[28:31], v1 offset:11264\n" \
  "v_mfma_f32_16x16x32_bf16 v[42:45], v[16:19], v[100:103], v[42:45]\n" \
  "v_mfma_f32_16x16x32_bf16 v[48:51], v[16:19], v[164:167], v[48:51]\n" \
  "v_mfma_f32_16x16x32_bf16 v[54:57], v[16:19], v[228:231], v[54:57]\n" \
  "s_waitcnt lgkmcnt(4)\n" \
  "v_mfma_f32_16x16x32_bf16 v[60:63], v[16:19], v[32:35], v[60:63]\n" \
  "ds_read_b128 v[16:19], v0 offset:26880\n" \
  "ds_read_b128 v[32:35], v1 offset:12288\n" \
  "v_mfma_f32_16x16x32_bf16 v[42:45], v[20:23], v[104:107], v[42:45]\n" \
  "v_mfma_f32_16x16x32_bf16 v[48:51], v[20:23], v[168:171], v[48:51]\n" \
  "v_mfma_f32_16x16x32_bf16 v[54:57], v[20:23], v[232:235], v[54:57]\n" \
  "s_waitcnt lgkmcnt(4)\n" \
  "v_mfma_f32_16x16x32_bf16 v[60:63], v[20:23], v[36:39], v[60:63]\n" \
  "ds_read_b128 v[20:23], v0 offset:28928\n" \
  "ds_read_b128 v[36:39], v1 offset:13312\n" \
  "v_mfma_f32_16x16x32_bf16 v[42:45], v[24:27], v[108:111], v[42:45]\n" \
  "v_mfma_f32_16x16x32_bf16 v[48:51], v[24:27], v[172:175], v[48:51]\n" \
  "v_mfma_f32_16x16x32_bf16 v[54:57], v[24:27], v[236:239], v[54:57]\n" \
  "s_waitcnt lgkmcnt(4)\n" \
  "v_mfma_f32_16x16x32_bf16 v[60:63], v[24:27], v[28:31], v[60:63]\n" \
  "ds_read_b128 v[24:27], v0 offset:30976\n" \
  "ds_read_b128 v[28:31], v1 offset:14336\n" \
  "v_mfma_f32_16x16x32_bf16 v[42:45], v[12:15], v[112:115], v[42:45]\n" \
  "v_mfma_f32_16x16x32_bf16 v[48:51], v[12:15], v[176:179], v[48:51]\n" \
  "v_mfma_f32_16x16x32_bf16 v[54:57], v[12:15], v[240:243], v[54:57]\n" \
  "s_waitcnt lgkmcnt(4)\n" \
  "v_mfma_f32_16x16x32_bf16 v[60:63], v[12:15], v[32:35], v[60:63]\n" \
  "ds_read_b128 v[32:35], v1 offset:15360\n" \
  "v_mfma_f32_16x16x32_bf16 v[42:45], v[16:19], v[116:119], v[42:45]\n" \
  "v_mfma_f32_16x16x32_bf16 v[48:51], v[16:19], v[180:183], v[48:51]\n" \
  "v_mfma_f32_16x16x32_bf16 v[54:57], v[16:19], v[244:247], v[54:57]\n" \
  "s_waitcnt lgkmcnt(3)\n" \
  "v_mfma_f32_16x16x32_bf16 v[60:63], v[16:19], v[36:39], v[60:63]\n" \
  "ds_read_b128 v[36:39], v3 offset:64\n" \
  "v_mfma_f32_16x16x32_bf16 v[42:45], v[20:23], v[120:123], v[42:45]\n" \
  "v_mfma_f32_16x16x32_bf16 v[48:51], v[20:23], v[184:187], v[48:51]\n" \
  "v_mfma_f32_16x16x32_bf16 v[54:57], v[20:23], v[248:251], v[54:57]\n" \
  "s_waitcnt lgkmcnt(2)\n" \
  "v_mfma_f32_16x16x32_bf16 v[60:63], v[20:23], v[28:31], v[60:63]\n" \
  "ds_read_b128 v[28:31], v3 offset:192\n" \
  "s_mov_b64 exec, 1\n" \
  "ds_add_u32 v6, v7 offset:8\n" \
  "s_mov_b64 exec, -1\n" \
  "v_xor_b32 v6, 4, v6\n" \
  "v_mfma_f32_16x16x32_bf16 v[42:45], v[24:27], v[124:127], v[42:45]\n" \
  "v_mfma_f32_16x16x32_bf16 v[48:51], v[24:27], v[188:191], v[48:51]\n" \
  "v_mfma_f32_16x16x32_bf16 v[54:57], v[24:27], v[252:255], v[54:57]\n" \
  "s_waitcnt lgkmcnt(3)\n" \
  "v_mfma_f32_16x16x32_bf16 v[60:63], v[24:27], v[32:35], v[60:63]\n" \
  "s_waitcnt lgkmcnt(1)\n" \
  "s_nop 4\n" \
  "ds_read_b32 v5, v6 offset:0\n" \
  "v_fma_f32 v42, -v36, v8, v42\n" \
  "v_fma_f32 v43, -v37, v8, v43\n" \
  "v_fma_f32 v44, -v38, v8, v44\n" \
  "v_fma_f32 v45, -v39, v8, v45\n" \
  "v_mul_f32 v42, v42, v28\n" \
  "v_mul_f32 v43, v43, v29\n" \
  "v_mul_f32 v44, v44, v30\n" \
  "v_mul_f32 v45, v45, v31\n" \
  "v_cvt_pk_bf16_f32 v42, v42, v43\n" \
  "v_cvt_pk_bf16_f32 v43, v44, v45\n" \
  "buffer_store_dwordx4 v[40:43], v2, s[40:43], s65 offen offset:0 sc1\n" \
  "v_fma_f32 v48, -v36, v9, v48\n" \
  "v_fma_f32 v49, -v37, v9, v49\n" \
  "v_fma_f32 v50, -v38, v9, v50\n" \
  "v_fma_f32 v51, -v39, v9, v51\n" \
  "v_mul_f32 v48, v48, v28\n" \
  "v_mul_f32 v49, v49, v29\n" \
  "v_mul_f32 v50, v50, v30\n" \
  "v_mul_f32 v51, v51, v31\n" \
  "v_cvt_pk_bf16_f32 v48, v48, v49\n" \
  "v_cvt_pk_bf16_f32 v49, v50, v51\n" \
  "buffer_store_dwordx4 v[46:49], v2, s[40:43], s65 offen offset:1024 sc1\n" \
  "v_fma_f32 v54, -v36, v10, v54\n" \
  "v_fma_f32 v55, -v37, v10, v55\n" \
  "v_fma_f32 v56, -v38, v10, v56\n" \
  "v_fma_f32 v57, -v39, v10, v57\n" \
  "v_mul_f32 v54, v54, v28\n" \
  "v_mul_f32 v55, v55, v29\n" \
  "v_mul_f32 v56, v56, v30\n" \
  "v_mul_f32 v57, v57, v31\n" \
  "v_cvt_pk_bf16_f32 v54, v54, v55\n" \
  "v_cvt_pk_bf16_f32 v55, v56, v57\n" \
  "buffer_store_dwordx4 v[52:55], v2, s[40:43], s65 offen offset:2048 sc1\n" \
  "v_fma_f32 v60, -v36, v4, v60\n" \
  "v_fma_f32 v61, -v37, v4, v61\n" \
  "v_fma_f32 v62, -v38, v4, v62\n" \
  "v_fma_f32 v63, -v39, v4, v63\n" \
  "v_mul_f32 v60, v60, v28\n" \
  "v_mul_f32 v61, v61, v29\n" \
  "v_mul_f32 v62, v62, v30\n" \
  "v_mul_f32 v63, v63, v31\n" \
  "v_cvt_pk_bf16_f32 v60, v60, v61\n" \
  "v_cvt_pk_bf16_f32 v61, v62, v63\n" \
  "buffer_store_dwordx4 v[58:61], v2, s[40:43], s65 offen offset:3072 sc1\n" \
  "s_waitcnt lgkmcnt(0)\n" \
  "s_add_u32 s65, s65, 0x40000\n" \
  "s_sub_u32 s64, s64, 1\n" \
  "s_mov_b32 s68, 1\n" \
  "s_cmp_lg_u32 s64, 0\n" \
  "s_cbranch_scc1 .LCW_TOP_%=\n" \
  "s_waitcnt vmcnt(0)\n" \
  "s_mov_b64 exec, 1\n" \
  "buffer_store_dword v7, off, s[56:59], s72 sc1\n" \
  "s_mov_b64 exec, -1\n" \
  "s_add_u32 s72, s72, 0x1000\n" \
  "s_mov_b32 s68, 0\n" \
  ".LCW_END_%=:\n" \
  "s_endpgm\n"
constexpr int RG = 2;
#define RFL(x) __builtin_amdgcn_readfirstlane(x)
#define HALF(x, i) BF8(((i) ? __builtin_shufflevector(x, x, 4, 5, 6, 7) : __builtin_shufflevector(x, x, 0, 1, 2, 3)))
constexpr int P_W = 65536, P_ST = 131072, P_SS = P_ST + 1024, P_CN = P_SS + 512;
template <bool PR>
__device__ __forceinline__ void role_proj_t(const MegaArgs& A, char* smem, const int L, const int idx, const bool kv) {
  const int w = A.wv;
  const int l = lane_id();
  const int c = l & 15, g = l >> 4;
  char* ws = A.ws;
  unsigned* FL = (unsigned*)(ws + OFF_FL);
  unsigned* TMO = FL + FL_TMO;
  const unsigned sb = lds_base(smem);
  auto cn = (volatile __attribute__((address_space(3))) unsigned*)(unsigned long)(sb + P_CN);
  u64 tag;
  const bool h4 = kv && L;
  const __amdgpu_buffer_rsrc_t rsM = MKRS(ws + OFF_MEMK + L * SZ_MEM, kv && w == 2 ? (4 - L) * SZ_MEM : 0);
  const bool mc = kv && w == 2 && (MCE < 2 || !L);
  constexpr int NMC = MCE == 2 ? 8 : 4;
  const int b0 = RFL(h4 ? (H4S ? 2 : 3) * w : w == 2 ? 0 : HX0),
            b1 = RFL(h4 ? (H4S ? 2 * w + 2 : w == 2 ? 8 : 3 * w + 3 * (w < 2)) : w == 2 ? HX0 : 8);
  const __amdgpu_buffer_rsrc_t rsF = MKRS(FL + FL_FC + L * 64 * 32 * 32, 64 * 4096);
  const int fo = ((blockIdx.x & 3) * 8 + ((H4S && h4) || w == 3 ? l >> 3 : min(b0 + (l >> 3), b1 - 1))) * 128 + (l & 7) * 4;
#define FC_LOOK(s) __builtin_amdgcn_raw_buffer_load_b32(rsF, fo, (s) * 4096, AUX_SC1)
#define MEM_CLEAR(s)                                                                                                          \
  _Pragma("unroll") for (int i = 0; i < NMC; ++i)                                                                             \
    ST128((v4u{0u, 0u, 0u, 0u}), rsM, l * 16 + (i & 1) * 1024, (s) * 65536 + idx * 2048 + (i >> 1) * (int)SZ_MEM * (MCE == 2 ? 1 : 2));
  const char* Xb = ws + (L ? OFF_X3 : OFF_XA);
  const __amdgpu_buffer_rsrc_t rsS = MKRS(ws + OFF_H + L * (SZ_X / 2), SZ_X / 2);
  unsigned nf = 0u;
  bool early = false;
  v4u t0[16];
#define X_ASK(s) \
  for (int i = b0; i < b1; ++i) glds64c(Xb + RFL((s) * 32768 + i * 4096), l * 16, RFL(sb + ((s) & 1) * 32768 + i * 4096));
#define S_ASK(s, t) _Pragma("unroll") for (int i = 0; i < 16; ++i) t[i] = LD128(rsS, l * 16, (s) * 16384 + i * 1024);
  {
    const int j = w & 3;
    const bool vt = kv && j >= 2;
    constexpr int NT = PR ? 3 : 1;
    v8u wq[NT][8];
    const u16* Wm = (kv ? (PR ? A.Wv : A.Wk) : A.Wq) - (PR ? 128 : 0) * HD;
    const int hm = kv ? idx : 2 * idx + PR;
    const int q = (l >> 2) & 3, p = l & 3, f = q | ((g & 1) << 2);
    unsigned ra[NT];
#pragma unroll
    for (int m = 0; m < NT; ++m) {
      const int ct = PR ? m : 3;
      ra[m] = sb + (j >> 1) * 65536 + (8 * g + q) * 256 + ((4 * (j & 1) + (vt ? ct : (ct & 2) + (p >> 1))) ^ f) * 32 + (vt ? p * 8 : (p & 1) * 16 + (ct & 1) * 8);
    }
#define W_ISSUE(n) wqkv_issue<false>(Wm + (128 * (n) - 256 * ((n) & 1)) * HD, (n) & 1, hm, w, l, sb, (WNT >> (kv ? 1 + PR : 3) & 1) && (L == 0 || (WNT & 32)))
#define W_STAGE(n, WAIT, NEXT)                                                                             \
  {                                                                                                        \
    WAIT;                                                                                                  \
    if (n == 3 && EL == 2 && !PR && L == 0 && w >= 2) nf = FC_LOOK(0);                                     \
    if (n == 3 && EL == 1 && !PR && L == 0 && w >= 2) {                                                    \
      early = __all(nf != 0u);                                                                             \
      if (early) {                                                                                         \
        X_ASK(0)                                                                                           \
        if (w == 3) { S_ASK(0, t0) }                                                                       \
      }                                                                                                    \
      nf = FC_LOOK(early);                                                                                 \
    }                                                                                                      \
    RAW_BAR();                                                                                             \
    if (n == 3 && EL == 3 && !PR && L == 0 && w >= 2) nf = FC_LOOK(0);                                     \
    _Pragma("unroll") for (int m = 0; m < NT; ++m) {                                                       \
      _Pragma("unroll") for (int t = 0; t < 2; ++t) {                                                      \
        const unsigned a_ = ra[m] + ((n) & 1) * 32768 + t * 16384;                                         \
        const v2u r0 = trd(a_), r1 = trd(a_ + 1024), r2 = trd(a_ + 8192), r3 = trd(a_ + 8192 + 1024);      \
        wq[m][2 * (n) + t] = v8u{r0.x, r0.y, r1.x, r1.y, r2.x, r2.y, r3.x, r3.y};                          \
      }                                                                                                    \
    }                                                                                                      \
    _Pragma("unroll") for (int m = 0; m < 2 * NT; ++m) asm volatile("" : "+v"(wq[m >> 1][2 * (n) + (m & 1)])); \
    RAW_BAR();                                                                                             \
    NEXT;                                                                                                  \
    _Pragma("unroll") for (int m = 0; m < 4 * NT; ++m)                                                     \
      cs[m % NT] = MFMA16(HALF(wq[m % NT][2 * (n) + m / NT / 2], (m / NT) & 1), BF8(one), cs[m % NT]);     \
    _Pragma("unroll") for (int m = 0; m < NT; ++m) asm volatile("" : "+v"(cs[m]));                         \
  }
    if (EL && !MC3 && mc) { MEM_CLEAR(0) }
    W_ISSUE(0);
    W_ISSUE(1);
    tag = call_tag(A);
    if (w == 0 && l < 8) cn[l] = l == 1 ? 3u : l == 5 ? 2u : 0u;
    const v4u one = {0x3f803f80u, 0x3f803f80u, 0x3f803f80u, 0x3f803f80u};
    f32x4 cs[NT] = {};
    u64 rv = 0;
    W_STAGE(0, WAIT_VM(8), W_ISSUE(2); if (EL) RD_ASK(rv));
    SST(4);
    W_STAGE(1, if (EL) WAIT_VM(9); else WAIT_VM(8), W_ISSUE(3); if (EL != 0 && MC3 != 0 && mc) { MEM_CLEAR(0) if (MC3 == 2) { MEM_CLEAR(1) } });
    SST(5);
    W_STAGE(2, if (EL != 0 && MC3 == 2 && mc) WAIT_VM(16); else if (EL != 0 && MC3 != 0 && mc) WAIT_VM(12); else WAIT_VM(8), (void)0);
    SST(6);
    asm volatile("" : "+v"(rv));
    if (!EL) ready_wait(A, tag, ready_look(A));
    for (unsigned spins = 0; EL && !__all(rv == tag) && spins < SPIN_CAP; ++spins) {
      __builtin_amdgcn_s_sleep(8);
      RD_ASK(rv);
      asm volatile("s_waitcnt vmcnt(0)" : "+v"(rv)::"memory");
    }
    if (EL == 1 && !PR && L == 0 && w >= 2) nf = FC_LOOK(0);
    W_STAGE(3, if (EL >= 2 && MC3 == 2 && mc) WAIT_VM(8); else if (EL >= 2 && MC3 == 1 && mc) WAIT_VM(4); else WAIT_VM(0), if (!PR) {
      _Pragma("unroll") for (int ks = 0; ks < 16; ++ks)
        *reinterpret_cast<bf16x8*>(smem + P_W + j * 16384 + ks * 1024 + l * 16) = HALF(wq[0][ks >> 1], ks & 1);
    });
    SST(7);
#undef W_STAGE
#undef W_ISSUE
#pragma unroll
    for (int m = 0; m < NT; ++m) *reinterpret_cast<f32x4*>(smem + P_ST + j * 256 + (PR ? m : 3) * 64 + g * 16) = cs[m];
    RAW_BAR();
    SST(8);
    if constexpr (PR) {
      const v4u rsO = mk_srd(ws + RFL((unsigned)(vt ? OFF_VF + L * SZ_O : OFF_QK + L * SZ_QK)), RFL((unsigned)(vt ? SZ_O : SZ_QK)));
      const v4u rsP = mk_srd(FL + RFL(kv ? FL_FQ + (L * 64 * 32 + idx) * 32 + 2 * j : FL_FP + (L * 16 + idx) * 32 + j), 63 * 4096 + 4);
      const int so = RFL(vt ? idx * 8 + 4 * (j & 1) : (kv ? idx * 16 + 8 : (2 * idx + (j >> 1)) * 16) + 2 * (j & 1));
      const v8u pv = {sb + 32768 + g * 512 + c * 16, sb + P_W + j * 16384 + l * 16, (unsigned)l * 16, sb + P_SS + 256 + (vt ? g * 16 : c * 4),
                      sb + P_ST + j * 256 + (vt ? c * 4 : g * 16), 0u, 0u, 0u};
      const char* dbg = nullptr;
#define P_OPS                                                                                                                               \
  ::"{v[64:71]}"(wq[0][0]), "{v[72:79]}"(wq[0][1]), "{v[80:87]}"(wq[0][2]), "{v[88:95]}"(wq[0][3]), "{v[96:103]}"(wq[0][4]),                \
      "{v[104:111]}"(wq[0][5]), "{v[112:119]}"(wq[0][6]), "{v[120:127]}"(wq[0][7]), "{v[128:135]}"(wq[1][0]), "{v[136:143]}"(wq[1][1]),     \
      "{v[144:151]}"(wq[1][2]), "{v[152:159]}"(wq[1][3]), "{v[160:167]}"(wq[1][4]), "{v[168:175]}"(wq[1][5]), "{v[176:183]}"(wq[1][6]),     \
      "{v[184:191]}"(wq[1][7]), "{v[192:199]}"(wq[2][0]), "{v[200:207]}"(wq[2][1]), "{v[208:215]}"(wq[2][2]), "{v[216:223]}"(wq[2][3]),     \
      "{v[224:231]}"(wq[2][4]), "{v[232:239]}"(wq[2][5]), "{v[240:247]}"(wq[2][6]), "{v[248:255]}"(wq[2][7]), "{v[0:7]}"(pv),               \
      "{s[40:43]}"(rsO), "{s[56:59]}"(rsP), "{s64}"(NSEG), "{s65}"(so * 1024), "{s70}"(sb + P_CN), "{s[36:37]}"(dbg)                        \
      : "memory", "vcc", "scc", "s80", "v63"
      if (vt) asm volatile("s_mov_b64 exec, -1\ns_waitcnt vmcnt(0) lgkmcnt(0)\ns_nop 4\n" V_ASM P_OPS);
      else asm volatile("s_mov_b64 exec, -1\ns_waitcnt vmcnt(0) lgkmcnt(0)\ns_nop 4\n" P_ASM P_OPS);
#undef P_OPS
      return;
    }
  }
  __builtin_amdgcn_s_setprio(RPRIO);
  if (h4 || w >= 2) {
    if (!EL && mc) { MEM_CLEAR(0) }
    auto stats = [&](const v4u (&t)[16], const int s) {
      float a = 0.f, b = 0.f, qa = 0.f, qb = 0.f;
#pragma unroll
      for (int i = 0; i < 16; ++i) {
        a = one(a + __uint_as_float(t[i].x));
        b = one(b + __uint_as_float(t[i].z));
        qa = one(qa + __uint_as_float(t[i].y));
        qb = one(qb + __uint_as_float(t[i].w));
      }
      a = xsum32(xsum16(a));
      b = xsum32(xsum16(b));
      float da = 0.f, db = 0.f;
#pragma unroll
      for (int i = 0; i < 16; ++i) {
        const float u = one(__uint_as_float(t[i].x) - a * (1.0f / 64)), v = one(__uint_as_float(t[i].z) - b * (1.0f / 64));
        da = one(da + u * u);
        db = one(db + v * v);
      }
      const float var = xsum32(swap_add16(qa + da * 0.125f, qb + db * 0.125f)) * (1.0f / DM);
      const float ri = 1.0f / bfr1(sqrtf(bfr1(bfr1(var) + 1e-5f)));
      reinterpret_cast<float*>(smem + P_SS)[(s & 1) * 64 + (g >> 1) * 32 + 2 * c + (g & 1)] = g < 2 ? bfr1(((g & 1) ? b : a) * (1.0f / DM)) : ri;
      asm volatile("" ::: "memory");
      if (l == 0) cn[4 + (s & 1)] = 2 * s + 4;
    };
    static_assert(EL != 1 || MC3 == 2, "clear");
    if (early) {
      if (w == 3) WAIT_VM(17);
      else WAIT_VM(1);
      if (l == 0) __hip_atomic_fetch_add((__attribute__((address_space(3))) unsigned*)cn, 3u, __ATOMIC_RELAXED, __HIP_MEMORY_SCOPE_WORKGROUP);
      SSX(10, w == 2);
      SSX(9, w == 2);
      if (w == 3) stats(t0, 0);
      SSX(11, w == 3);
    }
    unsigned tn = now10(), tg = tn;
    for (int s = early; s < NSEG; ++s) {
      for (unsigned spins = 0; !__all(nf != 0u); ++spins) {
        if (spin_timeout(spins, TMO)) return;
        if (spins) __builtin_amdgcn_s_sleep(SLP_H);
        else if (GF_H && (h4 || (GF_P && L)) && s >= GF0 && s < NSEG - 6) gate(tg);
        tg = now10() + GF_H;
        nf = FC_LOOK(s);
        asm volatile("" ::: "memory");
      }
      if (NFW) asm volatile("" ::"v"(nf));
      TS(4, s, 128);
      for (unsigned spins = 0; cn[2 + (s & 1)] < 4u * (s >> 1); ++spins) {
        if (spins > 64u * SPIN_CAP) return;
        __builtin_amdgcn_s_sleep(2);
      }
      if (L == 0 && s >= PACE0) gate(tn);
      else tn = now10();
      tn += PACE;
      TS(5, s, 128);
      X_ASK(s)
      if (H4S && h4) {
        v4u t[4];
#pragma unroll
        for (int i = 0; i < 4; ++i) t[i] = LD128(rsS, (l >> 2) * 256 + (l & 3) * 16 + w * 64, s * 16384 + i * 4096);
        nf = 0u;
        if (MCE == 1 && w == 2 && s < NSEG - 1) {
          MEM_CLEAR(s + 1)
          WAIT_VM(8);
        } else {
          WAIT_VM(4);
        }
        if (l == 0) __hip_atomic_fetch_add((__attribute__((address_space(3))) unsigned*)cn + (s & 1), w < 2 ? 2u : 1u, __ATOMIC_RELAXED, __HIP_MEMORY_SCOPE_WORKGROUP);
        TS(6, s, 128);
#define F(x) __uint_as_float(x)
        const float a = red_mod4(one(F(t[0].x) + F(t[1].x)) + one(F(t[2].x) + F(t[3].x)));
        const float b = red_mod4(one(F(t[0].z) + F(t[1].z)) + one(F(t[2].z) + F(t[3].z)));
        float da = 0.f, db = 0.f;
#pragma unroll
        for (int i = 0; i < 4; ++i) {
          const float u = one(F(t[i].x) - a * (1.0f / 64)), v = one(F(t[i].z) - b * (1.0f / 64));
          da = one(da + u * u);
          db = one(db + v * v);
        }
        const float qa = one(F(t[0].y) + F(t[1].y)) + one(F(t[2].y) + F(t[3].y)), qb = one(F(t[0].w) + F(t[1].w)) + one(F(t[2].w) + F(t[3].w));
#undef F
        float var = xsum32(swap_add16(one(qa + da * 0.125f), one(qb + db * 0.125f)));
        var += dppr<0x124>(var);
        var += dppr<0x128>(var);
        const float ri = 1.0f / bfr1(sqrtf(bfr1(bfr1(var * (1.0f / DM)) + 1e-5f)));
        if (c < 4) reinterpret_cast<float*>(smem + P_SS)[(s & 1) * 64 + (g >> 1) * 32 + 8 * w + 2 * c + (g & 1)] = g < 2 ? bfr1(((g & 1) ? b : a) * (1.0f / DM)) : ri;
        asm volatile("" ::: "memory");
        if (l == 0) __hip_atomic_fetch_add((__attribute__((address_space(3))) unsigned*)cn + 4 + (s & 1), 1u, __ATOMIC_RELAXED, __HIP_MEMORY_SCOPE_WORKGROUP);
        TS(7, s, 192);
        if (!MCE && w == 2 && s < NSEG - 1) { MEM_CLEAR(s + 1) }
      } else if (w != 3) {
        nf = L ? 0u : FC_LOOK(s + 1);
        if (mc && s < NSEG - 1 && (EL == 0 || MC3 != 2 || s)) {
          MEM_CLEAR(s + 1)
          if (NMC == 8) WAIT_VM(8);
          else WAIT_VM(4);
        } else {
          WAIT_VM(0);
        }
        if (l == 0) __hip_atomic_fetch_add((__attribute__((address_space(3))) unsigned*)cn + (s & 1), h4 ? 2u : 3u, __ATOMIC_RELAXED, __HIP_MEMORY_SCOPE_WORKGROUP);
        TS(6, s, 128);
        SSX(10, s == 0 && w == 2);
      } else {
        v4u t[16];
        S_ASK(s, t)
        WAIT_VM(16);
        nf = L ? 0u : FC_LOOK(s + 1);
        if (l == 0 && !h4) __hip_atomic_fetch_add((__attribute__((address_space(3))) unsigned*)cn + (s & 1), 3u, __ATOMIC_RELAXED, __HIP_MEMORY_SCOPE_WORKGROUP);
        stats(t, s);
        SSX(11, s == 0);
        TS(7, s, 192);
      }
    }
    return;
  }
  const int RL = !kv, rp = idx & 15, u = RFL(kv ? idx >> 4 : L);
  const __amdgpu_buffer_rsrc_t rsW = MKRS(ws + OFF_PW, SZ_PW);
  const __amdgpu_buffer_rsrc_t rsX = MKRS(ws + (RL ? OFF_X3 : OFF_XA), SZ_X);
  const __amdgpu_buffer_rsrc_t rsY = RL ? MKRS(A.out, SZ_X) : MKRS(ws + OFF_X2, SZ_X);
  const int r = 2 * rp + (g >> 1), yo = r * (DM * 2) + (l & 31) * 16 + u * 512;
  if (kv) {
    if (XZD) gate(now10() + XZD);
    if (XZP & 3) {
      if (!wait_n<SLP_V, 32>(FL + FL_FZ + (blockIdx.x & 7) * 64, 64, TMO)) return;
    } else {
      for (int s = w; s < (XZP ? 2 * RG : NSEG); s += RG) ST128((v4u{0u, 0u, 0u, 0u}), rsY, yo, s * SEG * DM * 2);
      DRAIN();
      if (l < 8) set_flag(FL + FL_FZ + l * 64 + 2 * idx + w);
    }
  }
  v4u sel;
#pragma unroll
  for (int e = 0; e < 4; ++e) sel[e] = (g >> 1) == (c >> 3) && ((c & 7) >> 1) == e ? 0x3f80u << (16 * (c & 1)) : 0u;
  unsigned tl = now10();
  int po = (g & 1) * 32768 + (c >> 2) * 128 + (g >> 1) * 64 + (c & 3) * 16;
  asm volatile("" : "+v"(po));
  for (int s = w; s < NSEG; s += RG) {
    v4u xr;
    if (GF_R) {
      gate(tl);
      const gu32* f = (gu32*)(FL + FL_FW) + ((RL * 64 + s) * 4 + (blockIdx.x & 3)) * 4 * 32 + (c >> 2) * 32 + (c & 3);
      unsigned mk;
      for (unsigned spins = 0;; ++spins) {
        tl = now10() + GF_R;
        mk = __hip_atomic_load(f, RLX_AGENT);
        if (__all(mk != 0u) || spin_timeout(spins, TMO)) break;
        if (s < NSEG - RFA) __builtin_amdgcn_s_sleep(SLP_R);
      }
      if (EF) {
        int d = (int)(mk - tl);
        d = max(d, __builtin_amdgcn_update_dpp(0, d, 0x121, 0xf, 0xf, false));
        d = max(d, __builtin_amdgcn_update_dpp(0, d, 0x122, 0xf, 0xf, false));
        d = max(d, __builtin_amdgcn_update_dpp(0, d, 0x124, 0xf, 0xf, false));
        d = max(d, __builtin_amdgcn_update_dpp(0, d, 0x128, 0xf, 0xf, false));
        const unsigned tp = tl + RFL(d);
        asm volatile("" ::: "memory");
        xr = LD128(rsX, (l & 31) * 512 + r * 16, s * 32768 + u * 16384);
        gate(tp - (RL && s >= NSEG - RLS ? RLEAD : 0));
        if (GR2) tl = tp + (s >= NSEG - 12 ? GR3 : GR2);
      }
      asm volatile("" ::: "memory");
    } else
    grp_wait<SLP_R, 4, GATE_R>(true, nullptr, 0, FL + FL_FW + ((RL * 64 + s) * 4 + (blockIdx.x & 3)) * 4 * 32, 16, TMO, tl);
    if (s >= NSEG - 6 && !(EF != 0 && GR2 != 0)) tl -= GF_R - (s >= NSEG - 4 ? GF_RT : GF_RU);
    if (!EF) DRAIN();
    TS(0, s, w * 64);
    v4u p[8][2];
    if (!EF) xr = LD128(rsX, (l & 31) * 512 + r * 16, s * 32768 + u * 16384);
    f32x4 ac[2];
    const int sr = EF ? s & ((1 << RGB) - 1) : s;
    const unsigned old = (s >> RGB) & 1;
    for (unsigned spins = 0;; ++spins) {
      int so = (RL * 64 + sr) * 16 * 32768 + rp * 2048 + u * 1024;
#pragma unroll
      for (int k = 0; k < 8; ++k) {
        asm volatile("" : "+s"(so));
#pragma unroll
        for (int b = 0; b < 2; ++b) p[k][b] = __builtin_amdgcn_raw_buffer_load_b128(rsW, po + b * 512, so, RAUX);
        so += 65536;
      }
      __builtin_amdgcn_sched_barrier(0);
      unsigned z[2] = {1u, 1u};
      ac[0] = ac[1] = f32x4{};
#pragma unroll
      for (int k = 0; k < 8; ++k)
#pragma unroll
        for (int b = 0; b < 2; ++b) {
          ac[b] = MFMA16(BF8(sel), BF8(p[k][b]), ac[b]);
          z[b] = __builtin_amdgcn_bitop3_b32(z[b], p[k][b].x, old, 0x60);
          __builtin_amdgcn_sched_barrier(0);
        }
      if (!EF || !RVAL || __all((z[0] & z[1]) != 0u) || spin_timeout(spins, TMO)) break;
      if (SLP_D) __builtin_amdgcn_s_sleep(SLP_D);
      asm volatile("" ::: "memory");
    }
    v4u xo;
    {
      const auto x = __builtin_amdgcn_permlane16_swap(pk2s(ac[0][0], ac[0][1]), pk2s(ac[1][0], ac[1][1]), false, false);
      const auto y = __builtin_amdgcn_permlane16_swap(pk2s(ac[0][2], ac[0][3]), pk2s(ac[1][2], ac[1][3]), false, false);
      const v4u a = {x[0], y[0], x[1], y[1]};
#pragma unroll
      for (int e = 0; e < 4; ++e) {
        xo[e] = pk2s(lo16(xr[e]) + lo16(a[e]), hi16(xr[e]) + hi16(a[e]));
        if (kv) xo[e] = xo[e] ? xo[e] : 0x8000u;
      }
      if (kv) ST128(xo, rsY, yo, s * SEG * DM * 2);
      else ST128A(xo, rsY, yo, s * SEG * DM * 2, OAUX);
    }
    TS(1, s, w * 64);
    if (kv) {
      const float pv = lo16(g < 2 ? __builtin_amdgcn_readlane(xo[0], 0) : __builtin_amdgcn_readlane(xo[0], 32));
      float sm = 0.f, q0 = 0.f, q1 = 0.f;
#pragma unroll
      for (int e = 0; e < 4; ++e) {
        const float a = one(lo16(xo[e]) - pv), b = one(hi16(xo[e]) - pv);
        sm = one(sm + one(a + b));
        q0 = one(q0 + a * a);
        q1 = one(q1 + b * b);
      }
      sm = half_sum(sm);
      const float q = fmaxf(one(half_sum(q0 + q1)) - sm * sm * (1.0f / 256), 0.f);
      const u64 st = (u64)__float_as_uint(sm + pv * 256.f) | (u64)(__float_as_uint(q) | 1u) << 32;
      if ((l & 31) < 4) __hip_atomic_store((gu64*)(FL + FL_FR + ((s * 4 + (l & 3)) * 4 + (r >> 3)) * 32 + (r & 7) * 4 + 2 * u), st, RLX_AGENT);
      if (XZP == 4 && s + 2 * RG < NSEG) ST128((v4u{0u, 0u, 0u, 0u}), rsY, yo, (s + 2 * RG) * SEG * DM * 2);
    }
    if (EF && s + (1 << RGB) < NSEG) PW_FREE(RL, s + (1 << RGB), 2 * rp + u);
    TS(3, s, w * 64);
  }
  if (L == 1 && idx == 0 && w == 0 && l == 0) __hip_atomic_store((gu64*)(ws + OFF_EP), tag, RLX_AGENT);
}
__device__ __forceinline__ void role_proj(const MegaArgs& A, char* smem, const int L, const int idx, const bool kv) {
  if (A.wv < 4) role_proj_t<false>(A, smem, L, idx, kv);
  else role_proj_t<true>(A, smem, L, idx, kv);
}

constexpr int CUN = 2144;
__device__ __forceinline__ v2u part_stats(const v4u o) {
  float v[8];
#pragma unroll
  for (int e = 0; e < 4; ++e) { v[2 * e] = lo16(o[e]); v[2 * e + 1] = hi16(o[e]); }
  const float s = ((v[0] + v[1]) + (v[2] + v[3])) + ((v[4] + v[5]) + (v[6] + v[7])), m = s * 0.125f;
  float q0 = 0.f, q1 = 0.f;
#pragma unroll
  for (int e = 0; e < 4; ++e) {
    const float a = v[e] - m, b = v[4 + e] - m;
    q0 += a * a;
    q1 += b * b;
  }
  return v2u{__float_as_uint(s), __float_as_uint(q0 + q1)};
}
constexpr int CRN = 176;
constexpr int CTN = 272;
constexpr int CV_LDS = (8 * CKN + 8 * 2 * CUN + 64 + 2 * 32 * 8 + 16 * CRN + 64 + 8 * CTN) * 2 + 8 * 1568;
__device__ __forceinline__ void role_conv(const MegaArgs& A, char* smem, const int cv) {
  u16* Kr = reinterpret_cast<u16*>(smem);
  u16* Uc = Kr + 8 * CKN;
  u16* Us = Uc + 8 * CUN + 63;
  u16* To = Uc + 8 * 2 * CUN + 64;
  u16* Sx = To + 32 * 8;
  u16* Rc = Sx + 32 * 8;
  u16* Rs = Rc + 8 * CRN + 63;
  u16* Ta = Rc + 16 * CRN + 64;
  const int w = A.wv;
  const int l = lane_id(), tid = 64 * w + l, c = l & 15, g = l >> 4;
  const int d = 8 * cv + w;
  char* ws = A.ws;
  unsigned* FL = (unsigned*)(ws + OFF_FL);
  unsigned* TMO = FL + FL_TMO;
  u16* kr = Kr + w * CKN;
  const u16* ka = kr + 2080 - 16 * c + 8 * g;
#define KFRAG(D0) (*reinterpret_cast<const f16x8*>(ka - (D0)))
  typedef __attribute__((address_space(3))) const unsigned* lpu;
#define BFRAG(a) __builtin_bit_cast(f16x8, (v4u{((lpu)(unsigned long)(a))[0], ((lpu)(unsigned long)(a))[1], ((lpu)(unsigned long)(a))[2], ((lpu)(unsigned long)(a))[3]}))
  f32x4 acc[8];
  v4u tx[4];
  const __amdgpu_buffer_rsrc_t rz1 = MKRS(ws + OFF_XA, SZ_X), rps = MKRS(ws + OFF_H, SZ_X);
  u16* uc = Uc + w * CUN;
  const unsigned ub = lds_base(reinterpret_cast<char*>(((c & 1) ? Us : Uc) + w * CUN + c + 8 * g + 32));
  const int t0 = 16 * c + 4 * g;
  float dsk, ysc;
#define SKIP(T) (*reinterpret_cast<const v2u*>(uc + 32 + 256 * (T) + t0))
#define TILE_OUT(dst, a, uu)                                                                   \
  {                                                                                            \
    v2u v_;                                                                                    \
    _Pragma("unroll") for (int e = 0; e < 2; ++e) {                                            \
      const unsigned yy = pk2(a[2 * e] * ysc, a[2 * e + 1] * ysc), u_ = uu[e];                 \
      const unsigned k_ = pk2(h2f((u16)u_) * dsk, h2f((u16)(u_ >> 16)) * dsk);                 \
      v_[e] = pk2(lo16(yy) + lo16(k_), hi16(yy) + hi16(k_));                                   \
    }                                                                                          \
    *reinterpret_cast<v2u*>((dst) + t0) = v_;                                                  \
  }
#define CHUNK_ST(i, src, N) CHUNK_STX(i, tx[i], src, N)
#define CHUNK_STX(i, X, src, N)                                                                \
  {                                                                                            \
    const unsigned p_ = lds_base((char*)((src) + (c >> 2) * (N) + (tid & ~15) + 4 * (c & 3))); \
    const v2u a0_ = trd(p_), a1_ = trd(p_ + 8 * (N));                                          \
    const v4u a_ = {a0_.x, a0_.y, a1_.x, a1_.y};                                               \
    v4u o;                                                                                     \
    _Pragma("unroll") for (int e = 0; e < 4; ++e) o[e] = pk2(lo16(X[e]) + lo16(a_[e]), hi16(X[e]) + hi16(a_[e])); \
    ST128(o, rz1, (l >> 5) * 32768 + (l & 31) * 16, (2 * (w + 8 * (i)) * 64 + cv) * 512);      \
    ST64(part_stats(o), rps, (l >> 5) * 16384 + (l & 31) * 8, (2 * (w + 8 * (i)) * 64 + cv) * 256); \
  }
#define CHUNK_FLAG(i) set_flag(FL + FL_FC + (((2 * w + 16 * ((i) + (l >> 3)) + ((l >> 2) & 1)) * 4 + (l & 3)) * 8 + (cv >> 3)) * 32 + (cv & 7))
  {
#define U_VAL(i)                                                                               \
  {                                                                                            \
    const float mb_ = lo16(st[i]), ri_ = 1024.0f / hi16(st[i]);                                 \
    _Pragma("unroll") for (int e = 0; e < 4; ++e) {                                            \
      const unsigned t_ = pk2(lo16(tx[i][e]) - mb_, hi16(tx[i][e]) - mb_);                     \
      const unsigned q_ = pk2(lo16(t_) * ri_, hi16(t_) * ri_);                                 \
      uv[e] = pkh(lo16(q_), hi16(q_));                                                         \
    }                                                                                          \
  }
    unsigned ta[3], k0p, st[4];
    v4u uv;
    v2u kn;
    u64 tag;
    const __amdgpu_buffer_rsrc_t rst = MKRS(ws + OFF_GT, (L_SEQ + 512) * 16);
    {
      KGen G;
      G.load(A, d, l);
      const u16 pk = A.Dskip[d];
      asm volatile("" ::: "memory");
      if (XL == 1) ask_slow(A, 16 * (w - 4) + (cv & 15), ta);
      if (XL) asm volatile("" : "+v"(G.pa), "+v"(G.pb), "+v"(G.pc), "+v"(G.pd), "+v"(G.pe)::"memory");
      tx[0] = *reinterpret_cast<const v4u*>(A.x0 + (size_t)tid * DM + 8 * cv);
      if (XL != 1) ask_slow(A, 16 * (w - 4) + (cv & 15), ta);
      tag = call_tag(A);
      G.setup();
      dsk = bf2f(pk) * (1.0f / 1024);
      G.st = smem + w * 16640;
      SST(0);
      ysc = G.scale();
      const Cx z = G.power(1.f);
      Cx y = cmul(G.C, z);
      G.rows(G.st + l * 8, y, z, 16);
      G.get(G.bh, G.bl, l);
      const float k0 = 2.0f * wave_sum(G.C.r.x + G.C.r.y);
      k0p = l == 0 ? pkh(k0, 0.f) : 0u;
      if (TP) {
        constexpr int R = 64 * TP, U0 = 2 * R + 32, U1 = 3 * R + 111, KN = 4 * R + 192;
        u16* k1 = reinterpret_cast<u16*>(smem + 8 * 16640) + w * KN;
        u16* t1 = reinterpret_cast<u16*>(smem + 8 * 16640) + 8 * KN;
        v4u s1 = {};
        u64 rv = 0;
        if (w < TP) {
          s1 = LD128(rst, tid * 16, (L_SEQ + R * (cv >> 4)) * 16);
          rv = ready_look(A);
        }
        if (l < R / 8 + 4) reinterpret_cast<v4u*>(k1)[l < R / 8 ? l + R / 8 + 4 : l - R / 8] = v4u{k0p, 0u, 0u, 0u};
        k1[U0 + (l < 32 ? l : R + l)] = k1[U1 + (l < 32 ? l : R + l)] = 0;
        G.columns(l, 0, R / 16);
        const f32x4 kt = G.tile<true>(l);
        if (c < R / 16) *reinterpret_cast<v2u*>(k1 + R + 28 - 16 * c - 4 * g) = v2u{pkh(kt.w, kt.z), pkh(kt.y, kt.x)};
        if (w < TP) {
          for (unsigned spins = 0; !__all(has_tag(tag, s1.x, s1.y) && has_tag(tag, s1.z, s1.w)) && spins < SPIN_CAP; ++spins) {
            __builtin_amdgcn_s_sleep(4);
            s1 = LD128(rst, tid * 16, (L_SEQ + R * (cv >> 4)) * 16);
          }
          st[0] = __builtin_amdgcn_perm(s1.w, s1.y, 0x07060302);
          U_VAL(0);
          u16* up = reinterpret_cast<u16*>(smem + 8 * 16640) + 32 + tid;
#pragma unroll
          for (int e = 0; e < 8; ++e) up[e * KN + U0] = up[e * KN + U1] = (u16)(uv[e >> 1] >> (16 * (e & 1)));
        }
        RAW_BAR();
        const unsigned u1 = lds_base(reinterpret_cast<char*>(k1 + ((c & 1) ? U1 : U0) + c + 8 * g));
        f32x4 a = {};
#pragma unroll
        for (int b = 0; b <= R / 32; ++b)
          a = MFMAH(BFRAG(u1 + 64 * b), *reinterpret_cast<const f16x8*>(k1 + R - 16 * (c & (R / 16 - 1)) + 8 * g + 32 * b), a);
        if (c < R / 16) {
          const v2u sk = *reinterpret_cast<const v2u*>(k1 + U0 + 32 + t0);
          TILE_OUT(t1 + w * (R + 16), a, sk);
        }
        RAW_BAR();
        SST(1);
        if (w < TP) CHUNK_ST(0, t1, R + 16);
        G.columns(l, R / 16, TPS);
        if (w < TP) {
          DRAIN();
          ready_wait(A, tag, rv);
          if (l < 8) CHUNK_FLAG(0);
        }
        SST(6);
        G.columns(l, TPS, 16);
      } else {
        G.columns(l, 0, 16);
      }
      const f32x4 kt = G.tile<true>(l);
      kn = v2u{pkh(kt.w, kt.z), pkh(kt.y, kt.x)};
    }
#define U_PUT(i)                                                                               \
  _Pragma("unroll") for (int e = 0; e < 4; ++e) {                                              \
    ((lph)(unsigned long)up0)[(2 * e) * CUN + 512 * (i)] = (u16)uv[e];                         \
    ((lph)(unsigned long)up0)[(2 * e + 1) * CUN + 512 * (i)] = (u16)(uv[e] >> 16);             \
    ((lph)(unsigned long)up1)[(2 * e) * CUN + 512 * (i)] = (u16)uv[e];                         \
    ((lph)(unsigned long)up1)[(2 * e + 1) * CUN + 512 * (i)] = (u16)(uv[e] >> 16);             \
  }
#define U_ROW(i) { U_VAL(i) U_PUT(i) }
    typedef __attribute__((address_space(3))) u16* lph;
    unsigned up0 = lds_base((char*)(Uc + 32 + tid)), up1 = lds_base((char*)(Us + 32 + tid));
    asm volatile("" : "+v"(up0), "+v"(up1), "+v"(kn), "+v"(k0p));
#define ST_GET(I0, I1)                                                                                                        \
  for (unsigned spins = 0; spins < SPIN_CAP; ++spins) {                                                                       \
    bool ok = true;                                                                                                           \
    v4u t_[4];                                                                                                                \
    _Pragma("unroll") for (int i = I0; i < I1; ++i) t_[i] = LD128(rst, (tid + 512 * i) * 16, 0);                              \
    _Pragma("unroll") for (int i = I0; i < I1; ++i) {                                                                         \
      ok = ok && has_tag(tag, t_[i].x, t_[i].y) && has_tag(tag, t_[i].z, t_[i].w);                                            \
      st[i] = __builtin_amdgcn_perm(t_[i].w, t_[i].y, 0x07060302);                                                            \
    }                                                                                                                         \
    if (__all(ok)) break;                                                                                                     \
    __builtin_amdgcn_s_sleep(4);                                                                                              \
    asm volatile("" ::: "memory");                                                                                            \
  }
    if (w < 4) {
      ST_GET(0, 2);
      U_VAL(0);
      asm volatile("" : "+v"(uv));
    }
#pragma unroll
    for (int i = 1; i < 4; ++i) tx[i] = *reinterpret_cast<const v4u*>(A.x0 + (size_t)(tid + 512 * i) * DM + 8 * cv);
    RAW_BAR();
    SST(2);
    if (w < 4) { U_PUT(0) }
    *reinterpret_cast<v2u*>(kr + 2076 - 16 * c - 4 * g) = kn;
    if (l < 38) reinterpret_cast<v4u*>(kr)[l < 32 ? 260 + l : l < 34 ? 194 + l : l - 34] = v4u{k0p, 0u, 0u, 0u};
    (l < 32 ? uc : Us + w * CUN - 32)[l] = 0;
    uc[32 + L_SEQ + l] = Us[w * CUN + 32 + L_SEQ + l] = 0;
    uc[32 + 256 + l] = Us[w * CUN + 32 + 256 + l] = 0;
    RAW_BAR();
    SST(11);
    {
      f32x4 a0 = {}, a1 = {};
      f16x8 bf[9], af[9];
#pragma unroll
      for (int b = 0; b < 9; ++b) {
        af[b] = KFRAG(32 - 32 * b);
        bf[b] = BFRAG(ub + 64 * (b - 1));
      }
      const v2u k0 = SKIP(0);
      __builtin_amdgcn_sched_barrier(0);
#pragma unroll
      for (int b = 0; b < 9; ++b) (b & 1 ? a1 : a0) = MFMAH(bf[b], af[b], (b & 1 ? a1 : a0));
      a0 += a1;
      TILE_OUT(Ta + w * CTN, a0, k0);
    }
    const u64 rv = ready_look(A);
    RAW_BAR();
    SST(3);
    if (w < 4) {
      if (w >= TP) CHUNK_ST(0, Ta, CTN);
      asm volatile("" : "+v"(tx[1]));
      U_ROW(1);
      DRAIN();
      ready_wait(A, tag, rv);
      if (l < 8 && w >= TP) CHUNK_FLAG(0);
      ST_GET(2, 4);
    } else {
      ST_GET(0, 4);
      ready_wait(A, tag, rv);
      asm volatile("" : "+v"(tx[0]), "+v"(tx[1]));
      U_ROW(0);
      U_ROW(1);
    }
    ASKED(ta);
    if (tid < (16 * CRN + 64) / 8) reinterpret_cast<v4u*>(Rc)[tid] = v4u{0u, 0u, 0u, 0u};
    SST(4);
    const gu64* fk = (gu64*)(ws + OFF_KG) + cv * 16 + w;
    u64 kg = __hip_atomic_load(fk, RLX_AGENT);
    U_ROW(2);
    for (unsigned spins = 0; !__all(kg == tag) && spins < SPIN_CAP; ++spins) {
      __builtin_amdgcn_s_sleep(8);
      kg = __hip_atomic_load(fk, RLX_AGENT);
    }
    asm volatile("" : "+v"(tx[3]), "+v"(st[3])::"memory");
    SST(5);
    const unsigned* kd = reinterpret_cast<const unsigned*>(Ta + 8 * CTN) + w * 392;
    glds16c(ws + OFF_KF + d * 2048, l * 16, lds_base((char*)kd));
    if (l < 34) glds16c(ws + OFF_KF + d * 2048 + 1024, l * 16, lds_base((char*)kd) + 1024);
    U_ROW(3);
    DRAIN();
    {
#define MIX(lo, hi) __builtin_amdgcn_perm(hi, lo, 0x07060100)
      const unsigned a0 = kd[384 - l], b0 = kd[383 - l], a1 = kd[320 - l], b1 = kd[319 - l];
      const unsigned a2 = kd[256 - 2 * l], m2 = kd[255 - 2 * l], b2 = kd[254 - 2 * l];
      const unsigned* k3 = kd + 124 - 4 * (l & 31);
      v4u* ko = reinterpret_cast<v4u*>(kr) + l;
      ko[4] = v4u{a0, a0, (l & 1) ? b0 : MIX(a0, b0), b0};
      ko[68] = v4u{a1, a1, (l & 1) ? b1 : MIX(a1, b1), b1};
      ko[132] = v4u{a2, MIX(a2, m2), m2, b2};
      if (l < 32) ko[196] = v4u{k3[4], MIX(k3[3], k3[2]), k3[2], MIX(k3[1], k3[0])};
#undef MIX
    }
    RAW_BAR();
    SST(7);
    {
      unsigned uq[4] = {ub, ub + 1024, ub + 2048, ub + 3072};
      f16x8 bf[8], a[4];
      const f16x8 bm = BFRAG(ub - 64);
      asm volatile("" : "+v"(uq[0]), "+v"(uq[1]), "+v"(uq[2]), "+v"(uq[3]));
#pragma unroll
      for (int j = 1; j < 8; ++j) bf[j] = BFRAG(uq[j >> 1] + 512 * (j & 1));
#pragma unroll
      for (int T = 1; T < 8; ++T) acc[T] = MFMAH(bm, KFRAG(256 * T + 32), (f32x4{}));
#pragma unroll
      for (int e = 0; e < 3; ++e) a[e] = KFRAG(256 * e);
      const u16* kq = ka - 256 * 7;
#pragma unroll 1
      for (int r = 0; r < 8; ++r) {
#pragma unroll
        for (int e = 0; e < 8; ++e) {
          a[(e + 3) & 3] = *reinterpret_cast<const f16x8*>(kq + 256 * (7 - ((e + 3) & 7)) + (e >= 5 ? 32 : 0));
          const int j = (8 - e) & 7;
          bf[j] = BFRAG(uq[j >> 1] + 512 * (j & 1) + (e ? 64 : 0));
          __builtin_amdgcn_sched_barrier(0);
#pragma unroll
          for (int T = 7; T >= (e > 1 ? e : 1); --T) acc[T] = MFMAH(bf[T - e], a[e & 3], acc[T]);
          __builtin_amdgcn_sched_barrier(0);
        }
        kq += 32;
#pragma unroll
        for (int j = 0; j < 4; ++j) uq[j] += 64;
        asm volatile("" : "+v"(uq[0]), "+v"(uq[1]), "+v"(uq[2]), "+v"(uq[3]));
      }
    }
    SST(8);
    {
      v2u k[8];
#pragma unroll
      for (int T = 1; T < 8; ++T) k[T] = SKIP(T);
#pragma unroll
      for (int T = 1; T < 8; ++T) TILE_OUT(uc + 32 + 256 * T, acc[T], k[T]);
    }
    RAW_BAR();
    SST(10);
    if (w >= 4) CHUNK_ST(0, Uc + 32, CUN);
    CHUNK_ST(1, Uc + 32 + 512, CUN);
    if (PP < 0) {
      if (CVSP) __builtin_amdgcn_s_sleep(CVSP);
      CHUNK_ST(2, Uc + 32 + 1024, CUN);
      if (CVSP) __builtin_amdgcn_s_sleep(CVSP);
      CHUNK_ST(3, Uc + 32 + 1536, CUN);
    }
    if (w == 1) (void)wait_n<SLP_V, 32>(FL + FL_FZ + (blockIdx.x & 7) * 64, 64, TMO);
    DRAIN();
    if (l < (PP < 0 ? 32 : 16)) CHUNK_FLAG(0);
    SST(9);
#undef U_ROW
#undef U_VAL
#undef U_PUT
#undef ST_GET
  }
  const unsigned rb = lds_base(reinterpret_cast<char*>(((c & 1) ? Rs : Rc) + w * CRN + c + 8 * g));
#pragma unroll
  for (int j = 0; j < 8; ++j) acc[j] = f32x4{};
  const __amdgpu_buffer_rsrc_t rx = MKRS(ws + OFF_X2, SZ_X), rz = MKRS(ws + OFF_X3, SZ_X);
  const __amdgpu_buffer_rsrc_t rf = MKRS(FL + FL_FR, 64 * 16 * 128);
  v2u st = {0u, 0u}, xx = {0u, 0u};
#define X2_LOAD(ss, far)                                                                                                      \
  {                                                                                                                      \
    xx = LD64(rx, (l >> 1) * (DM * 2) + (l & 1) * 8, (far) ? (int)SZ_X : ((ss) * SEG * DM + 8 * cv) * 2);                 \
    st = LD64(rf, l * 8, ((ss) * 4 + (blockIdx.x & 3)) * 512);                                                           \
  }
  unsigned tl = now10();
  bool late = false;
  for (int s = 0; s < NSEG; ++s) {
    const int q = s & 3, q8 = s & 7;
    if (w == 1) {
      late = __builtin_popcountll(__ballot(st.y != 0u)) >= CVLATE;
      for (unsigned spins = 0; !__all(umin3(xx.x, xx.y, st.y) != 0u); ++spins) {
        if (spin_timeout(spins, TMO)) break;
        if (GF_V && s >= GF0) {
          if (spins) __builtin_amdgcn_s_sleep(SLP_V);
          else if (s < NSEG - 6) gate(tl);
          tl = now10() + GF_V;
          X2_LOAD(s, CVT2 > 64 ? 0 : spins && __builtin_popcountll(__ballot(st.y != 0u)) < CVT2);
          continue;
        }
        if (GATE_V && spins == 0 && !__any(st.y != 0u)) gate(tl);
        if (spins && !__all(st.y != 0u)) __builtin_amdgcn_s_sleep(SLP_V);
        X2_LOAD(s, __builtin_popcountll(__ballot(st.y != 0u)) < CVTHR);
      }
      if (!GF_V || s < GF0) tl = now10() + GATE_V;
      TS(0, s, 64);
      const float s0 = __uint_as_float(st.x), s1 = dppr<0xb1>(s0), m2 = __uint_as_float(st.y);
      const float var = (m2 + dppr<0xb1>(m2) + (s0 - s1) * (s0 - s1) * (1.0f / DM)) * (1.0f / DM);
      const float mb = bfr1((s0 + s1) * (1.0f / DM)), ri = 1024.0f * __builtin_amdgcn_rcpf(bfr1(__builtin_amdgcn_sqrtf(bfr1(bfr1(var) + 1e-5f))));
      const unsigned t0 = pk2(lo16(xx.x) - mb, hi16(xx.x) - mb), t1 = pk2(lo16(xx.y) - mb, hi16(xx.y) - mb);
      const unsigned b0 = pk2(lo16(t0) * ri, hi16(t0) * ri), b1 = pk2(lo16(t1) * ri, hi16(t1) * ri);
      const unsigned u0 = pkh(lo16(b0), hi16(b0)), u1 = pkh(lo16(b1), hi16(b1));
      u16* rp = Rc + (l & 1) * 4 * CRN + 32 * q + (l >> 1);
#define R_PUT(o) { rp[o] = (u16)u0; rp[(o) + CRN] = (u16)(u0 >> 16); rp[(o) + 2 * CRN] = (u16)u1; rp[(o) + 3 * CRN] = (u16)(u1 >> 16); }
      R_PUT(0) R_PUT(8 * CRN + 63)
      if (q == 0) { R_PUT(128) R_PUT(8 * CRN + 63 + 128) }
#undef R_PUT
      *reinterpret_cast<v2u*>(Sx + l * 4) = xx;
    }
    const u16* kq = ka + 32 * q8;
    const f16x8 a0 = *reinterpret_cast<const f16x8*>(kq - 32), a1 = *reinterpret_cast<const f16x8*>(kq);
    __syncthreads();
    TS(1, s, 0);
    const f16x8 bp = BFRAG(rb + 64 * ((q + 3) & 3)), bc = BFRAG(rb + 64 * q);
    const float u0 = h2f(Rc[w * CRN + 32 * q + c]), u1 = h2f(Rc[w * CRN + 32 * q + 16 + c]);
    const float x0 = bf2f(Sx[c * 8 + w]), x1 = bf2f(Sx[(16 + c) * 8 + w]);
    acc[0] = MFMAH(a0, bp, (q8 == 0 ? acc[1] : acc[0]));
    const f32x4 tmp = MFMAH(a1, bc, acc[0]);
    const unsigned uu = pk2(u0 * dsk, u1 * dsk);
    const unsigned yy = pk2(((q & 1) ? tmp[2] : tmp[0]) * ysc, ((q & 1) ? tmp[3] : tmp[1]) * ysc);
    const unsigned aa = pk2(lo16(yy) + lo16(uu), hi16(yy) + hi16(uu)), oo = pk2(x0 + lo16(aa), x1 + hi16(aa));
    if (g == (q8 >> 1)) {
      To[c * 8 + w] = (u16)oo;
      To[(16 + c) * 8 + w] = (u16)(oo >> 16);
    }
    __syncthreads();
    if (w == 0 && l < 32) {
      const v4u o = *reinterpret_cast<const v4u*>(To + l * 8);
      ST128A(o, rz, l * 16, (s * 64 + cv) * 512, XAUX);
      ST64(part_stats(o), rps, l * 8, ((64 + s) * 64 + cv) * 256);
    }
    if (w == 1) {
      if (GF_V && s + 1 >= GF0) st.y = 0u;
      else X2_LOAD(s + 1, !late);
    }
    const int left = 7 - (s >> 3);
#define PUSH(J0, SH)                                                                                              \
  if (left >= (J0)) {                                                                                             \
    _Pragma("unroll") for (int j = (J0); j < (J0) + 2 && j < 8; ++j)                                              \
      acc[j] = MFMAH(*reinterpret_cast<const f16x8*>(kq - 32 - 256 * j), bp, (j + (SH) < 8 ? acc[(j + (SH)) & 7] : f32x4{})); \
  }
    TS(2, s, 0);
    if (w == 0) {
      DRAIN();
      if (l < 4) set_flag(FL + FL_FC + (((64 + s) * 4 + l) * 8 + (cv >> 3)) * 32 + (cv & 7));
    }
    TS(3, s, 0);
    if (q8 == 0) { PUSH(1, 1) PUSH(3, 1) PUSH(5, 1) PUSH(7, 1) }
    else { PUSH(1, 0) PUSH(3, 0) PUSH(5, 0) PUSH(7, 0) }
#undef PUSH
    if (PP >= 0 && (unsigned)(s - PP) < 3u) {
      const int i = 2 + s - PP;
      if (i > 2) {
        DRAIN();
        if (l < 8) CHUNK_FLAG(i - 1);
        tx[2] = tx[3];
      }
      if (i < 4) CHUNK_STX(i, tx[2], Uc + 32 + 512 * i, CUN);
    }
    MSTAMP();
  }
#undef KFRAG
#undef BFRAG
#undef X2_LOAD
#undef TILE_OUT
#undef SKIP
#undef CHUNK_ST
#undef CHUNK_STX
#undef CHUNK_FLAG
}

__device__ __forceinline__ v4u ln_row_packed(const v4u xr) {
  float v[8];
#pragma unroll
  for (int e = 0; e < 4; ++e) { v[2 * e] = lo16(xr[e]); v[2 * e + 1] = hi16(xr[e]); }
  float s = ((v[0] + v[1]) + (v[2] + v[3])) + ((v[4] + v[5]) + (v[6] + v[7]));
  s = wave_sum(s);
  const float mean = s * (1.0f / DM);
  float q = 0.f;
#pragma unroll
  for (int j = 0; j < 8; ++j) { float t = v[j] - mean; q += t * t; }
  q = wave_sum(q);
  const float var = q * (1.0f / DM);
  const float mb = bfr(mean);
  const float sd = bfr(sqrtf(bfr(bfr(var) + 1e-5f)));
  const float ri = 1.0f / sd;
  v4u o;
#pragma unroll
  for (int e = 0; e < 4; ++e) {
    const unsigned t = pk2(v[2 * e] - mb, v[2 * e + 1] - mb);
    const float x0 = lo16(t), x1 = hi16(t);
    float q0 = x0 * ri, q1 = x1 * ri;
    q0 = __builtin_fmaf(__builtin_fmaf(-q0, sd, x0), ri, q0);
    q1 = __builtin_fmaf(__builtin_fmaf(-q1, sd, x1), ri, q1);
    o[e] = pk2(q0, q1);
  }
  return o;
}

__device__ __forceinline__ void ln4_packed(v4u (&x)[4]) {
  f32x2_t v[16];
#pragma unroll
  for (int e = 0; e < 16; ++e) v[e] = f32x2_t{lo16(x[e >> 2][e & 3]), hi16(x[e >> 2][e & 3])};
  const f32x2_t t = (((v[0] + v[1]) + (v[2] + v[3])) + ((v[4] + v[5]) + (v[6] + v[7]))) +
                    (((v[8] + v[9]) + (v[10] + v[11])) + ((v[12] + v[13]) + (v[14] + v[15])));
  const float mean = red_mod4(t.x + t.y) * (1.0f / DM);
  const float mb = bfr1(mean);
  f32x2_t q0 = {0.f, 0.f}, q1 = {0.f, 0.f};
#pragma unroll
  for (int e = 0; e < 16; e += 2) {
    v[e] -= mb;
    v[e + 1] -= mb;
    q0 += v[e] * v[e];
    q1 += v[e + 1] * v[e + 1];
  }
  q0 += q1;
  const float dm = mean - mb;
  const float var = fmaxf(red_mod4(q0.x + q0.y) * (1.0f / DM) - dm * dm, 0.f);
  const float sd = bfr1(sqrtf(bfr1(bfr1(var) + 1e-5f)));
  const float ri = 1.0f / sd;
#pragma unroll
  for (int e = 0; e < 16; ++e) {
    const unsigned t2 = pk2(v[e].x, v[e].y);
    const f32x2_t q = f32x2_t{lo16(t2), hi16(t2)} * ri;
    x[e >> 2][e & 3] = pk2(q.x, q.y);
  }
}

constexpr int MEGA_WGS = 256;
constexpr size_t MEGA_LDS = (size_t)(32 * 4 * 64 * 8 + 8 * SMS + 15 * QS + 128 + 16 * 64 * 8 + 8 * SOS + 512) * 2;
static_assert(MEGA_LDS <= 163840 && MEGA_LDS - 480 >= C_SQ + 4096, "lds");
static_assert(MEGA_LDS >= (size_t)AW_CNT + 128 && MEGA_LDS >= (size_t)P_CN + 512, "lds");
static_assert(MEGA_LDS >= (size_t)CV_LDS, "lds");
static_assert(MEGA_LDS >= (size_t)2 * 32 * SMS * 2 && MEGA_LDS >= 131072, "lds");
static_assert(MEGA_LDS >= (size_t)8 * (16640 + 2 * (704 + 144)), "lds");
__device__ __forceinline__ MegaArgs role_args(const int b, const int wv, const unsigned k0, const unsigned k1) {
  auto p = (const __attribute__((address_space(4))) u64*)__builtin_amdgcn_kernarg_segment_ptr();
  asm volatile("s_waitcnt lgkmcnt(0)" : "+s"(p) : "s"(k0), "s"(k1));
#define KA(i) ((u16*)(__attribute__((address_space(1))) u16*)p[i])
  return MegaArgs{KA(0), KA(1), (char*)KA(2), KA(3), KA(4), KA(5), KA(6), KA(7), KA(8), KA(9), KA(10), KA(11), KA(12), KA(13), b, wv};
#undef KA
}
__global__ __launch_bounds__(512, 2) void mega_kernel(const u16* mem0, const u16* Dskip, char* ws, u16* out, const u16* x0,
                                                      const u16* logAre, const u16* Aim, const u16* Cre, const u16* Cim,
                                                      const u16* logdt, const u16* Wq, const u16* Wk, const u16* Wv,
                                                      const u16* Wo) {
  extern __shared__ __attribute__((aligned(16))) char smem[];
  unsigned k0 = 0u, k1 = 0u;
  if (IPF == 3) {
    unsigned o;
    asm volatile(
        "s_load_dword %0, %3, 0\n\ts_load_dword %1, %3, 64\n\ts_getpc_b64 s[6:7]\n.LKG_PC:\n\ts_bfe_u32 s4, %4, 0x30003\n\t"
        "v_lshl_add_u32 %2, %5, 3, s4\n\tv_lshlrev_b32 %2, 7, %2\n\ts_mov_b32 m0, %6\n\ts_mov_b32 s4, .LKG_LAST-.LKG_PC\n\t"
        "s_max_i32 s4, s4, .LKG_LAS2-.LKG_PC\n\tv_cmp_gt_i32 vcc, s4, %2\n\t"
        "s_and_saveexec_b64 s[4:5], vcc\n\tglobal_load_lds_dword %2, s[6:7]\n\ts_mov_b64 exec, s[4:5]"
        : "=&s"(k0), "=&s"(k1), "=&v"(o)
        : "s"(__builtin_amdgcn_kernarg_segment_ptr()), "s"(blockIdx.x), "v"(threadIdx.x), "s"(lds_base(smem) + (unsigned)MEGA_LDS - 480)
        : "memory", "vcc", "scc", "s4", "s5", "s6", "s7");
  }
  const int b = vbid(), wv = __builtin_amdgcn_readfirstlane(threadIdx.x >> 6);
#define A role_args(b, wv, k0, k1)
  u64 tag = 0;
  if (__builtin_expect((0xde000ff0u >> (b >> 3)) & 1u, 0)) {
    const unsigned t0 = now10();
    if ((b >= 32 && b < 64) || (b >= 200 && b < 232)) start_kgen(A, smem, b < 64 ? b - 32 : b - 168);
    else if (b >= 64 && b < 96) tag = start_stats<4>(A, b - 64);
    else if (b >= 240) (void)start_stats<8>(A, b - 224);
    const unsigned dl = (b >= 32 && b < 64) ? DL_C : (b >= 64 && b < 96) ? DL_A : b >= 240 ? DL_R2 : ((b >= 96 && b < 104) || b >= 232) ? DL_R : (b >= 200 && b < 232) ? DL_Q : 0u;
    while (now10() - t0 < dl) __builtin_amdgcn_s_sleep(16);
    __builtin_amdgcn_s_waitcnt(0x0f70);
  }
  if ((EXP_OFF & 1) && b >= 64 && b < 80) return;
  if ((EXP_OFF & 8) && b >= 200 && b < 232) return;
  if ((EXP_OFF & 16) && b >= 32 && b < 64) return;
  if ((EXP_OFF & 32) && b >= 80 && b < 96) return;
  if (b < 64) role_chain(A, smem, b >> 5, b & 31, 0);
  else if (b < 96) role_aw(A, smem, (b >> 4) & 1, b & 15, tag);
  else if (b >= 104 && b < 168) role_conv(A, smem, b - 104);
  else {
    const bool kv = b >= 168 && b < 232;
    role_proj(A, smem, b >= (kv ? 200 : 240), kv ? (b - 168) & 31 : b & 15, kv);
  }
#undef A
}

extern "C" void kernel_launch(void* const* d_in, const int* in_sizes, int n_in, void* d_out, int out_size, void* d_ws,
                              size_t ws_size, hipStream_t stream) {
  MegaArgs A;
  A.x0 = (const u16*)d_in[0];
  A.logAre = (const u16*)d_in[1];
  A.Aim = (const u16*)d_in[2];
  A.Cre = (const u16*)d_in[3];
  A.Cim = (const u16*)d_in[4];
  A.logdt = (const u16*)d_in[5];
  A.Dskip = (const u16*)d_in[6];
  A.mem0 = (const u16*)d_in[8];
  A.Wq = (const u16*)d_in[9];
  A.Wk = (const u16*)d_in[10];
  A.Wv = (const u16*)d_in[11];
  A.Wo = (const u16*)d_in[12];
  A.ws = (char*)d_ws;
  A.out = (u16*)d_out;

  (void)hipFuncSetAttribute((const void*)mega_kernel, hipFuncAttributeMaxDynamicSharedMemorySize, (int)MEGA_LDS);
  mega_kernel<<<MEGA_WGS, 512, MEGA_LDS, stream>>>(A.mem0, A.Dskip, A.ws, A.out, A.x0, A.logAre, A.Aim, A.Cre, A.Cim, A.logdt,
                                                   A.Wq, A.Wk, A.Wv, A.Wo);
}
```

```cpp
#include <hip/hip_runtime.h>
#include <hip/hip_bf16.h>
#include <stdint.h>

constexpr int L_SEQ = 2048;
constexpr int DM    = 512;
constexpr int NST   = 128;
constexpr int NH    = 32;
constexpr int DH    = 128;
constexpr int HD    = NH * DH;
constexpr int MM    = 8;
constexpr int SEG   = 32;
constexpr int NSEG  = L_SEQ / SEG;
constexpr int LK    = MM + SEG;

typedef unsigned short u16;
using bf16x8 = __attribute__((ext_vector_type(8))) short;
using f32x4  = __attribute__((ext_vector_type(4))) float;
using f32x16 = __attribute__((ext_vector_type(16))) float;

static __device__ __forceinline__ u16 f2bf(float f) {
  unsigned u = __float_as_uint(f);
  u += 0x7fffu + ((u >> 16) & 1u);
  return (u16)(u >> 16);
}
static __device__ __forceinline__ float bf2f(u16 h) { return __uint_as_float(((unsigned)h) << 16); }
static __device__ __forceinline__ float bfr(float f) { return bf2f(f2bf(f)); }

constexpr size_t SZ_X      = (size_t)L_SEQ * DM * 2;
constexpr size_t OFF_KF    = 0;
constexpr size_t OFF_UT    = OFF_KF + (size_t)DM * L_SEQ * 4;
constexpr size_t OFF_YT    = OFF_UT + SZ_X;
constexpr size_t OFF_XA    = OFF_YT + SZ_X;
constexpr size_t OFF_H     = OFF_XA + SZ_X;
constexpr size_t OFF_X2    = OFF_H + SZ_X;
constexpr size_t OFF_U2    = OFF_X2 + SZ_X;
constexpr size_t OFF_X3    = OFF_U2 + SZ_X;
constexpr size_t SZ_QK     = (size_t)L_SEQ * 2 * HD * 2;
constexpr size_t OFF_QK    = OFF_X3 + SZ_X;
constexpr size_t SZ_O      = (size_t)L_SEQ * HD * 2;
constexpr size_t OFF_VF    = OFF_QK + 2 * SZ_QK;
constexpr size_t SZ_PW     = (size_t)2 * NSEG * 16 * SEG * DM * 2;
constexpr size_t OFF_PW    = OFF_VF + 2 * SZ_O;
constexpr size_t SZ_MEM    = (size_t)NSEG * MM * HD * 2;
constexpr size_t OFF_MEMK  = OFF_PW + SZ_PW;
constexpr size_t OFF_MEMV  = OFF_MEMK + 2 * SZ_MEM;
constexpr size_t OFF_GT    = OFF_MEMV + 2 * SZ_MEM;
constexpr size_t OFF_EP    = OFF_GT + 65536;
constexpr size_t OFF_RD    = OFF_EP + 4096;
constexpr size_t OFF_KG    = OFF_RD + 4096;
constexpr size_t OFF_G     = OFF_KG + 8192;
constexpr size_t SZ_ACC    = (size_t)2 * 65536;
constexpr size_t OFF_Z     = OFF_GT + (size_t)128 * DM * 4;
constexpr size_t OFF_ACC   = OFF_Z;
constexpr size_t OFF_FL    = OFF_ACC + 2 * SZ_ACC;
constexpr int FL_FK  = 0;
constexpr int FL_FW  = FL_FK + 64 * 8 * 32;
constexpr int FL_FR  = FL_FW + 2 * 64 * 16 * 32;
constexpr int FL_FC  = FL_FR + 64 * 16 * 32;
constexpr int FL_FQ  = FL_FC + 2 * 64 * 32 * 32;
constexpr int FL_FP  = FL_FQ + 2 * 64 * 32 * 32;
constexpr int FL_FD  = FL_FP + 64 * 32 * 32;
constexpr int FL_FZ  = FL_FD + 64 * 2 * 32;
constexpr int FL_TMO = FL_FZ + 16 * 32;
constexpr int FL_END = FL_TMO + 64;
constexpr size_t Z_BYTES   = OFF_FL + (size_t)FL_END * 4 - OFF_Z;
constexpr size_t WS_TOTAL  = OFF_Z + Z_BYTES;
static_assert(Z_BYTES % 16 == 0 && OFF_Z % 256 == 0 && Z_BYTES <= 4 * 64 * 8192 && OFF_G + 2048 <= OFF_Z, "zero region");
static_assert(WS_TOTAL + (4 << 20) <= (size_t)256 * 1024 * 1024, "workspace too large");

static_assert(WS_TOTAL - OFF_XA <= (size_t)384 * 8 * 65536, "translation warm-up");
constexpr int CKN = 2336;
static_assert((size_t)DM * 2 * CKN * 2 <= (size_t)DM * L_SEQ * 4 + SZ_X, "K buffer");
__device__ __forceinline__ float pos_of_index(int p) {
  if (p < 256) return (float)p;
  if (p < 384) return (float)(256 + 2 * (p - 256));
  if (p < 512) return (float)(512 + 4 * (p - 384));
  if (p < 640) return (float)(1024 + 8 * (p - 512));
  return 2048.0f;
}
__device__ __forceinline__ int index_of_l(int l) {
  float pf = bfr((float)l);
  int p = (int)pf;
  if (p < 256) return p;
  if (p < 512) return 256 + ((p - 256) >> 1);
  if (p < 1024) return 384 + ((p - 512) >> 2);
  if (p < 2048) return 512 + ((p - 1024) >> 3);
  return 640;
}

template <int CTRL> __device__ __forceinline__ float dppr(float x) {
  return __builtin_bit_cast(float, __builtin_amdgcn_update_dpp(0, __builtin_bit_cast(int, x), CTRL, 0xf, 0xf, false));
}
__device__ __forceinline__ float wave_sum(float v) {
  v += dppr<0x121>(v);
  v += dppr<0x122>(v);
  v += dppr<0x124>(v);
  v += dppr<0x128>(v);
  {
    auto r = __builtin_amdgcn_permlane16_swap(__float_as_uint(v), __float_as_uint(v), false, false);
    v = __uint_as_float(r[0]) + __uint_as_float(r[1]);
  }
  {
    auto r = __builtin_amdgcn_permlane32_swap(__float_as_uint(v), __float_as_uint(v), false, false);
    v = __uint_as_float(r[0]) + __uint_as_float(r[1]);
  }
  return v;
}
__device__ __forceinline__ void ln_row8(float (&v)[8]) {
  float s = 0.f;
#pragma unroll
  for (int j = 0; j < 8; ++j) s += v[j];
  s = wave_sum(s);
  const float mean = s * (1.0f / DM);
  float q = 0.f;
#pragma unroll
  for (int j = 0; j < 8; ++j) { float t = v[j] - mean; q += t * t; }
  q = wave_sum(q);
  const float var = q * (1.0f / DM);
  const float mb = bfr(mean);
  const float sd = bfr(sqrtf(bfr(bfr(var) + 1e-5f)));
#pragma unroll
  for (int j = 0; j < 8; ++j) v[j] = bfr(bfr(v[j] - mb) / sd);
}

#define RLX_AGENT __ATOMIC_RELAXED, __HIP_MEMORY_SCOPE_AGENT
typedef __attribute__((address_space(1))) unsigned gu32;
typedef unsigned v4u __attribute__((ext_vector_type(4)));
constexpr int AUX_SC1 = 16;
constexpr unsigned SPIN_CAP = 1u << 18;

__device__ __forceinline__ bool wait_flags(const unsigned* flags, int lane, int nfl, unsigned want, unsigned* tmo) {
  for (unsigned spins = 0;; ++spins) {
    unsigned v = want;
    if (lane < nfl) v = __hip_atomic_load((gu32*)flags + lane * 32, RLX_AGENT);
    if (__all(v >= want)) break;
    if (spins > SPIN_CAP) {
      if (lane == 0) __hip_atomic_store((gu32*)tmo, 1u, RLX_AGENT);
      return false;
    }
  }
  __builtin_amdgcn_fence(__ATOMIC_ACQUIRE, "wavefront");
  return true;
}

#define STAMP(i)
#define STAMP_DUMP()
#define WSTAMP(i)
template <int CTRL> __device__ __forceinline__ float dppf(float old, float x) {
  return __builtin_bit_cast(float, __builtin_amdgcn_update_dpp(__builtin_bit_cast(int, old), __builtin_bit_cast(int, x), CTRL, 0xf, 0xf, false));
}
__device__ __forceinline__ float wave_max_dpp(float x) {
  const float ninf = -3e38f;
  x = fmaxf(x, dppf<0x111>(ninf, x));
  x = fmaxf(x, dppf<0x112>(ninf, x));
  x = fmaxf(x, dppf<0x114>(ninf, x));
  x = fmaxf(x, dppf<0x118>(ninf, x));
  x = fmaxf(x, dppf<0x142>(ninf, x));
  x = fmaxf(x, dppf<0x143>(ninf, x));
  return __builtin_bit_cast(float, __builtin_amdgcn_readlane(__builtin_bit_cast(int, x), 63));
}
__device__ __forceinline__ float wave_sum_dpp(float x) {
  x += dppf<0x111>(0.f, x);
  x += dppf<0x112>(0.f, x);
  x += dppf<0x114>(0.f, x);
  x += dppf<0x118>(0.f, x);
  x += dppf<0x142>(0.f, x);
  x += dppf<0x143>(0.f, x);
  return __builtin_bit_cast(float, __builtin_amdgcn_readlane(__builtin_bit_cast(int, x), 63));
}
constexpr int SMS = 528;
constexpr int QS = 144;
constexpr int VS = 80;
constexpr int SOS = 144;
#define VAR_NREP 4
constexpr int NREP = VAR_NREP;
__device__ __forceinline__ float xsum16(float x) {
  auto r = __builtin_amdgcn_permlane16_swap(__float_as_uint(x), __float_as_uint(x), false, false);
  return __uint_as_float(r[0]) + __uint_as_float(r[1]);
}
__device__ __forceinline__ float xsum32(float x) {
  auto r = __builtin_amdgcn_permlane32_swap(__float_as_uint(x), __float_as_uint(x), false, false);
  return __uint_as_float(r[0]) + __uint_as_float(r[1]);
}
__device__ __forceinline__ float xmax16(float x) {
  auto r = __builtin_amdgcn_permlane16_swap(__float_as_uint(x), __float_as_uint(x), false, false);
  return fmaxf(__uint_as_float(r[0]), __uint_as_float(r[1]));
}
__device__ __forceinline__ float xmax32(float x) {
  auto r = __builtin_amdgcn_permlane32_swap(__float_as_uint(x), __float_as_uint(x), false, false);
  return fmaxf(__uint_as_float(r[0]), __uint_as_float(r[1]));
}
__device__ __forceinline__ float red_mod4(float x) {
  x += dppf<0x124>(0.f, x);
  x += dppf<0x128>(0.f, x);
  return xsum32(xsum16(x));
}
typedef __bf16 bf16x2_t __attribute__((ext_vector_type(2)));
typedef float f32x2_t __attribute__((ext_vector_type(2)));
__device__ __forceinline__ unsigned pk2(float a, float b) {
  f32x2_t v = {a, b};
  bf16x2_t r = __builtin_convertvector(v, bf16x2_t);
  return __builtin_bit_cast(unsigned, r);
}
typedef _Float16 f16x2_t __attribute__((ext_vector_type(2)));
typedef _Float16 f16x8 __attribute__((ext_vector_type(8)));
__device__ __forceinline__ unsigned pkh(float a, float b) {
  f32x2_t v = {a, b};
  return __builtin_bit_cast(unsigned, __builtin_convertvector(v, f16x2_t));
}
__device__ __forceinline__ float h2f(u16 h) { return (float)__builtin_bit_cast(_Float16, h); }
__device__ __forceinline__ float one(float x) {
  asm("" : "+v"(x));
  return x;
}
__device__ __forceinline__ unsigned pk2s(float a, float b) { return pk2(one(a), one(b)); }
__device__ __forceinline__ float dot2sq(const unsigned x, const float acc) {
  return __builtin_amdgcn_fdot2_f32_bf16(__builtin_bit_cast(bf16x2_t, x), __builtin_bit_cast(bf16x2_t, x), acc, false);
}
__device__ __forceinline__ float lo16(unsigned u) { return __uint_as_float(u << 16); }
__device__ __forceinline__ float hi16(unsigned u) { return __uint_as_float(u & 0xffff0000u); }
__device__ __forceinline__ float bfr1(float x) { return lo16(pk2(x, x)); }
__device__ __forceinline__ float half_sum(float v) {
  v += dppr<0x121>(v);
  v += dppr<0x122>(v);
  v += dppr<0x124>(v);
  v += dppr<0x128>(v);
  return xsum16(v);
}
__device__ __forceinline__ void scale_pair(float a, float b, float scale, float& oa, float& ob) {
  unsigned u = pk2(a, b);
  unsigned v = pk2s(lo16(u) * scale, hi16(u) * scale);
  oa = lo16(v);
  ob = hi16(v);
}

#define XMAP 2
__device__ __forceinline__ int vbid() {
  const int b = blockIdx.x, x = b & 7, t = b >> 3;
  if (XMAP == 2) {
    const int q = (t >> 2) + 2 * (x & 4);
    const unsigned long long m = (q & 8) ? 0xf0d85040cdc9ada9ull : 0x76726e6a25210501ull;
    const int e = (int)(m >> (8 * (q & 7))) & 255;
    return (e & 252) + ((x & 3) << (2 + (e & 3))) + (t & 3) + (q == 14 ? ((x & 2) ? 8 : -120) : 0);
  }
  if (XMAP == 0) {
    if (b < 64) return (b & 32) + 4 * x + (t & 3);
    if (b < 96) return (b & ~15) + 2 * x + (t & 1);
    if (b >= 104 && b < 168) return 104 + 8 * x + t - 13;
    if (b >= 168 && b < 232) return 168 + ((b - 168) & 32) + 4 * x + ((t - 21) & 3);
    return b;
  }
  if (XMAP == 3) {
    const int y = x & 3;
    if (t >= 16 && t < 24) return 104 + 16 * y + 2 * (x & 4) + t - 16;
    if (x < 4) return t < 16 ? 4 * (t & 8) + 8 * x + (t & 7) : 64 + 4 * (t & 4) + 4 * y + (t & 3);
    if (t >= 24) return (t >= 28 ? 240 : y < 2 ? 96 : 224) + 4 * y + (t & 3);
    return 168 + 4 * (t & 8) + 8 * y + (t & 7);
  }
  if (XMAP == 4) {
    const int y = x & 3;
    if (x < 4) return t < 16 ? 4 * (t & 8) + 8 * x + (t & 7) : t < 28 ? 104 + 16 * y + t - 16 : 64 + 4 * y + (t & 3);
    if (t >= 24) return (t >= 28 ? 240 : y < 2 ? 96 : 224) + 4 * y + (t & 3);
    if (t < 16) return 168 + 4 * (t & 8) + 8 * y + (t & 7);
    return t < 20 ? 104 + 16 * y + t - 4 : 80 + 4 * y + (t & 3);
  }
  if (XMAP == 2 || XMAP >= 5) {
    if (XMAP == 5 && x < 4 && t < 16) return 16 * x + t;
    if (XMAP == 6 && x < 4 && t < 16) return 32 * (x & 1) + 8 * (x & 2) + t;
    if (XMAP == 7 && x < 4) return (x & 1) ? 104 + 16 * (x & 2) + t : 16 * x + t;
    if (x < 4) return t < 16 ? 4 * (t & 8) + 8 * x + (t & 7) : 104 + 16 * x + t - 16;
    const int y = x - 4;
    if (t >= 24) return (t >= 28 ? 240 : y < 2 ? 96 : 224) + 4 * y + (t & 3);
    if (t < 16) return 168 + 4 * (t & 8) + 8 * y + (t & 7);
    return 64 + 4 * (t & 4) + 4 * y + (t & 3);
  }
  if (x < 2) return 2 * (t & 16) + 16 * x + (t & 15);
  const int i = 16 * (x - 2) + (t & 15);
  if (t < 16) {
    const int k = (i * 171) >> 9, r = i - 3 * k;
    return r == 0 ? 168 + k : r == 1 ? 200 + k : 64 + 16 * (k & 1) + (k >> 1);
  }
  const int g = i >> 3, e = i & 7;
  if (g == 3) return 96 + e;
  if (g == 7) return 232 + e;
  if (g == 9) return 240 + e;
  if (g == 11) return 248 + e;
  return 104 + 8 * (g - (g > 3) - (g > 7) - (g > 9)) + e;
}
#define MSTAMP()
#define WSTAMP2()
#define SST(k)
#define SSX(k, c)
#define SSW(k, v)
#define TS(k, ss, t)
#define LS(k, ss)
#define LS_DUMP()
struct MegaArgs {
  const u16* mem0;
  const u16* Dskip;
  char* ws;
  u16* out;
  const u16* x0;
  const u16 *logAre, *Aim, *Cre, *Cim, *logdt;
  const u16 *Wq, *Wk, *Wv, *Wo;
  int vb, wv;
};

__device__ __forceinline__ int lane_id() {
  int l;
  asm volatile("v_mbcnt_lo_u32_b32 %0, -1, 0\n\tv_mbcnt_hi_u32_b32 %0, -1, %0" : "=v"(l));
  __builtin_assume(l >= 0 && l < 64);
  return l;
}
__device__ __forceinline__ bool spin_timeout(unsigned spins, unsigned* tmo) {
  if (spins > SPIN_CAP) {
    if (lane_id() == 0) __hip_atomic_store((gu32*)tmo, 1u, RLX_AGENT);
    return true;
  }
  return false;
}
#define ROT_H (4 * (h & 1))
#define ROT_T (h & 2)
#define C2G 420
#define CVSP 40
#define PFA 0
#define PFB 0
#define WO1 1
#define AWH 0
#define PEEK0 1
#define SLP0 2
#define GF_R 400
#define GF_V 0
#define PP 0
#define EL 1
#define IPF 3
#define XL 0
#define MC3 2
#define XZD 300
#define XZP 4
#define NFW 1
#define IPS 128u
#define WNT 17
#define OAUX 16
#define XAUX 16
#define GF_RT 0
#define GF_RU 0
#define EF 1
#define RGB 3
#define GR2 345
#define GR3 280
#define RLS 1
#define GT1 120
#define GT2 32
#define GT3 1
#define CE 180
#define KE 0
#define PE KE
#define PCD 100
#define RAUX AUX_SC1
#define RVAL 1
#define RLEAD 60
#define SLP_D 0
#define RFA 1
#define TP 1
#define TPS (4 * TP + 4)
#define MCE 1
#define H4S 0
#define GF_H 150
#define CVT2 48
#define GF_P 0
#define GF0 8
#define SLP_H 12
#define HX0 6
#define CVSTEP 2
#define CVLATE 64
#define PACE 203
#define PACE0 6
#define RPRIO 3
#define DL_R2 1400
#define PNL 8
#define GSL 12
#define AWDIR 1
#define AWAT 0
#define AWRP 0
#define PEPS 4
#define FGATE 0
#define DL_C 0
#define DL_A 400
#define DL_R 0
#define DL_Q 0
#define KC0 0
#define CPRIO 1
#define P1PRIO 3
#define QLEAD 2
#define POLL_SLEEP 12
#define SLP_C POLL_SLEEP
#define SLP_A POLL_SLEEP
#define SLP_W POLL_SLEEP
#define SLP_R POLL_SLEEP
#define SLP_V POLL_SLEEP
#define SLP_Q POLL_SLEEP
#define SLP_M 4
#define AWPRIO 2
#define SLP_S 8
#define SPOS 4
#define CVPRE 12
#define QPD 3
#define QPRI 2
#define TLBW 3
#define QHV 0
#define QCHK 0
#define LEADN 99
#define GATE_V 170
#define GATE_R 380
#define GATE_N 380
#define AWAHEAD 1
#define GATE_M 150
#define PRI_A 3
#define PRI_WA 2
#define PRI_WB 1
#define AWSKIP 3
#define PRI_0 3
#define AWDEF 0
#define SLP_F 2
#define EXP_OFF 0
#define AWSPEC 1
#define CVTHR 48
#define NR_C 8
#define NR_R 8
#define NR_W 8
#define NR_N 2
#define QAHEAD 0
#define NS0 4
#define SLP_N POLL_SLEEP
template <int SLP, int WPL>
__device__ __forceinline__ bool wait_n(const unsigned* f, int n, unsigned* tmo) {
  const int l = lane_id();
  for (unsigned spins = 0;; ++spins) {
    unsigned v = 1u;
    if (l < n) v = __hip_atomic_load((gu32*)f + (l / WPL) * 32 + l % WPL, RLX_AGENT);
    if (__all(v != 0u)) break;
    if (spin_timeout(spins, tmo)) return false;
    __builtin_amdgcn_s_sleep(SLP);
  }
  asm volatile("" ::: "memory");
  return true;
}
__device__ __forceinline__ unsigned peek(const unsigned* base, const unsigned off) {
  unsigned r;
  asm volatile("global_atomic_or %0, %1, %2, %3 sc0\n\ts_waitcnt vmcnt(0)" : "=&v"(r) : "v"(off), "v"(0u), "s"(base) : "memory");
  return r;
}
__device__ __forceinline__ unsigned now10() { return (unsigned)__builtin_amdgcn_s_memrealtime(); }
__device__ __forceinline__ void gate(const unsigned t) {
#pragma unroll 1
  for (int i = 0; i < 64; ++i) {
    const int d = (int)(t - now10());
    if (d <= 0) break;
    if (d > GT1) __builtin_amdgcn_s_sleep(32);
    else if (d > GT2) __builtin_amdgcn_s_sleep(8);
    else __builtin_amdgcn_s_sleep(GT3);
  }
}
__device__ __forceinline__ unsigned latest(const unsigned mk, const unsigned t) {
  int d = (int)(mk - t);
  d = max(d, __builtin_amdgcn_update_dpp(0, d, 0x121, 0xf, 0xf, false));
  d = max(d, __builtin_amdgcn_update_dpp(0, d, 0x122, 0xf, 0xf, false));
  d = max(d, __builtin_amdgcn_update_dpp(0, d, 0x124, 0xf, 0xf, false));
  d = max(d, __builtin_amdgcn_update_dpp(0, d, 0x128, 0xf, 0xf, false));
  return t + __builtin_amdgcn_readfirstlane(d);
}
template <int SLP, int WPL, int GT>
__device__ __forceinline__ void grp_wait(const bool leader, volatile unsigned* sw, const unsigned val, const unsigned* f, int n,
                                         unsigned* tmo, unsigned& tl) {
  if (leader) {
    const int l = lane_id();
    for (unsigned spins = 0;; ++spins) {
      unsigned v = 1u;
      if (l < n) v = __hip_atomic_load((gu32*)f + (l / WPL) * 32 + l % WPL, RLX_AGENT);
      if (__all(v != 0u) || spin_timeout(spins, tmo)) break;
      if (GT && spins == 0) gate(tl);
      else __builtin_amdgcn_s_sleep(SLP);
    }
    if (GT) tl = now10() + GT;
    if (sw && l == 0) *sw = val;
  } else {
    for (unsigned spins = 0; *sw < val; ++spins) {
      if (spins > 64u * SPIN_CAP) break;
      if (FGATE && GT && spins == 0) gate(tl);
      __builtin_amdgcn_s_sleep(SLP_F);
    }
    if (FGATE && GT) tl = now10() + GT;
  }
  asm volatile("" ::: "memory");
}
template <int SLP, int WPL>
__device__ __forceinline__ bool wg_wait(const unsigned* f, int n, unsigned* tmo) {
  if (threadIdx.x < 64) (void)wait_n<SLP, WPL>(f, n, tmo);
  __syncthreads();
  asm volatile("" ::: "memory");
  return true;
}
__device__ __forceinline__ void set_flag(unsigned* f) { __hip_atomic_store((gu32*)f, 1u, RLX_AGENT); }
#define DRAIN() asm volatile("s_waitcnt vmcnt(0)" ::: "memory")
#define MKRS(ptr, bytes) __builtin_amdgcn_make_buffer_rsrc((void*)(ptr), 0, (int)(bytes), 0x00020000)
typedef unsigned v2u __attribute__((ext_vector_type(2)));
#define LD128(rs, voff, soff) __builtin_amdgcn_raw_buffer_load_b128(rs, voff, soff, AUX_SC1)
#define LD64(rs, voff, soff) __builtin_amdgcn_raw_buffer_load_b64(rs, voff, soff, AUX_SC1)
#define ST128(v, rs, voff, soff) ST128A(v, rs, voff, soff, AUX_SC1)
#define ST128A(v, rs, voff, soff, aux)                                        \
  do {                                                                        \
    const v4u st_d_ = (v);                                                    \
    __builtin_amdgcn_raw_buffer_store_b128(st_d_, rs, voff, soff, aux);       \
    asm volatile("s_nop 1" ::"v"(st_d_) : "memory");                          \
  } while (0)
#define ST64(v, rs, voff, soff) __builtin_amdgcn_raw_buffer_store_b64(v, rs, voff, soff, AUX_SC1)
#define BF8(x) __builtin_bit_cast(bf16x8, x)
#define MFMA16(a, b, c) __builtin_amdgcn_mfma_f32_16x16x32_bf16(a, b, c, 0, 0, 0)
typedef short bf16x4 __attribute__((ext_vector_type(4)));
#define MFMA16K(a, b, c) \
  __builtin_amdgcn_mfma_f32_16x16x16bf16_1k(__builtin_bit_cast(bf16x4, a), __builtin_bit_cast(bf16x4, b), c, 0, 0, 0)
__device__ __forceinline__ float fmax_nn(float a, float b) { return __builtin_elementwise_maximum(a, b); }
typedef __bf16 bfv8 __attribute__((ext_vector_type(8)));
typedef __bf16 bfv16 __attribute__((ext_vector_type(16)));
typedef unsigned v8u __attribute__((ext_vector_type(8)));
#define SMF(a, b, c, idx) \
  __builtin_amdgcn_smfmac_f32_16x16x64_bf16(__builtin_bit_cast(bfv8, a), __builtin_bit_cast(bfv16, b), c, idx, 0, 0)
__device__ __forceinline__ float swap_add32(float a, float b) {
  auto r = __builtin_amdgcn_permlane32_swap(__float_as_uint(a), __float_as_uint(b), false, false);
  return __uint_as_float(r[0]) + __uint_as_float(r[1]);
}
__device__ __forceinline__ float swap_add16(float a, float b) {
  auto r = __builtin_amdgcn_permlane16_swap(__float_as_uint(a), __float_as_uint(b), false, false);
  return __uint_as_float(r[0]) + __uint_as_float(r[1]);
}
__device__ __forceinline__ v4u smf_fix(const v4u d) {
  auto x = __builtin_amdgcn_permlane16_swap(d.x, d.z, false, false);
  auto y = __builtin_amdgcn_permlane16_swap(d.y, d.w, false, false);
  return v4u{x[0], x[1], y[0], y[1]};
}
__device__ __forceinline__ v8u smf_bfrag(const u16* p) {
  const v2u a = *reinterpret_cast<const v2u*>(p), b = *reinterpret_cast<const v2u*>(p + 8);
  const v2u c = *reinterpret_cast<const v2u*>(p + 32), d = *reinterpret_cast<const v2u*>(p + 40);
  return v8u{a.x, b.x, a.y, b.y, c.x, d.x, c.y, d.y};
}

#define VAR_PPA 0
#define VAR_PPB 0
#define VAR_THR 0
#define VAR_THR2 0
#define CV_LEAD 100
#define VAR_EARLY 1
#define VAR_DRAIN 1
#define VAR_D1 0
#define VAR_D2 0
#define VAR_DR2 1
#define VAR_S1 0
#define VAR_S2 0
typedef __attribute__((address_space(1))) unsigned long long gu64;
typedef __attribute__((address_space(1))) char gch;
__device__ __forceinline__ int cvt_rpi(float x) {
  int r;
  asm("v_cvt_rpi_i32_f32 %0, %1" : "=v"(r) : "v"(x));
  return r;
}
typedef unsigned long long u64;
__device__ __forceinline__ u64 call_tag(const MegaArgs& A) {
  u64 t = *(const __attribute__((address_space(4))) u64*)(A.ws + OFF_EP) * 6364136223846793005ull + 1442695040888963407ull;
  asm volatile("" : "+s"(t)::"memory");
  return t;
}
__device__ __forceinline__ u64 tagged(const u64 tag, const unsigned v) { return (tag & 0xffffffffffffull) | (u64)(v >> 16) << 48; }
__device__ __forceinline__ bool has_tag(const u64 tag, const unsigned lo, const unsigned hi) {
  return lo == (unsigned)tag && (hi & 0xffffu) == ((unsigned)(tag >> 32) & 0xffffu);
}
__device__ __forceinline__ u64 ready_look(const MegaArgs& A) {
  return __hip_atomic_load((gu64*)(A.ws + OFF_RD) + (blockIdx.x & 7) * 64 + lane_id(), RLX_AGENT);
}
#define RD_ASK(v) \
  asm volatile("global_load_dwordx2 %0, %1, %2 sc1" : "=v"(v) : "v"(lane_id() * 8), "s"(A.ws + OFF_RD + (blockIdx.x & 7) * 512) : "memory")
__device__ __forceinline__ void ready_wait(const MegaArgs& A, const u64 tag, u64 v) {
  for (unsigned spins = 0; !__all(v == tag) && spins < SPIN_CAP; ++spins) {
    __builtin_amdgcn_s_sleep(8);
    v = ready_look(A);
  }
  asm volatile("" ::: "memory");
}
typedef short v4s __attribute__((ext_vector_type(4)));
typedef __attribute__((address_space(3))) v4s* lp4s;
__device__ __forceinline__ void glds16(const void* sbase, const unsigned voff, const unsigned lds, const bool nt = false) {
  unsigned keep;
#define GLDS16(M)                                                                                                                     \
  asm volatile("s_nop 4\n\ts_mov_b32 %0, m0\n\ts_mov_b32 m0, %3\n\ts_nop 0\n\tglobal_load_lds_dwordx4 %1, %2" M "\n\ts_mov_b32 m0, %0" \
               : "=&s"(keep)                                                                                                          \
               : "v"(voff), "s"(sbase), "s"(lds)                                                                                      \
               : "memory")
  if (nt) GLDS16(" nt");
  else GLDS16("");
#undef GLDS16
}
__device__ __forceinline__ void glds16c(const void* sbase, const unsigned voff, const unsigned lds) {
  unsigned keep;
  asm volatile("s_nop 4\n\ts_mov_b32 %0, m0\n\ts_mov_b32 m0, %3\n\ts_nop 0\n\tglobal_load_lds_dwordx4 %1, %2 sc1\n\ts_mov_b32 m0, %0"
               : "=&s"(keep)
               : "v"(voff), "s"(sbase), "s"(lds)
               : "memory");
}
__device__ __forceinline__ void glds64c(const void* sbase, const unsigned voff, const unsigned lds) {
  unsigned keep;
  asm volatile("s_nop 4\n\ts_mov_b32 %0, m0\n\ts_mov_b32 m0, %3\n\ts_nop 0\n\tglobal_load_lds_dwordx4 %1, %2 sc1\n\t"
               "global_load_lds_dwordx4 %1, %2 offset:1024 sc1\n\tglobal_load_lds_dwordx4 %1, %2 offset:2048 sc1\n\t"
               "global_load_lds_dwordx4 %1, %2 offset:3072 sc1\n\ts_mov_b32 m0, %0"
               : "=&s"(keep)
               : "v"(voff), "s"(sbase), "s"(lds)
               : "memory");
}
__device__ __forceinline__ v2u trd(const unsigned a) {
  return __builtin_bit_cast(v2u, __builtin_amdgcn_ds_read_tr16_b64_v4i16((lp4s)(unsigned long)a));
}
__device__ __forceinline__ unsigned lds_base(char* smem) { return (unsigned)(unsigned long)(__attribute__((address_space(3))) char*)smem; }
#define RAW_BAR()                                           \
  do {                                                      \
    asm volatile("s_waitcnt lgkmcnt(0)" ::: "memory");      \
    __builtin_amdgcn_s_barrier();                           \
    asm volatile("" ::: "memory");                          \
  } while (0)
#define WAIT_VM(n) asm volatile("s_waitcnt vmcnt(" #n ")" ::: "memory")
template <bool SP>
__device__ __forceinline__ void wqkv_issue(const u16* Wm, const int hf, const int h, const int w, const int l, const unsigned sb, const bool nt) {
  const char* base = (const char*)Wm + (size_t)(256 * hf + 32 * w) * (HD * 2) + h * (DH * 2);
#define WQKV(NT_)                                                                                                       \
  _Pragma("unroll") for (int i = 0; i < 8; ++i) {                                                                      \
    const int r = l >> 4;                                                                                              \
    const int f = SP ? ((r & 1) | ((i & 1) << 1) | (((i >> 1) & 1) << 2)) : (r | (((i >> 1) & 1) << 2));               \
    glds16(base, (unsigned)((4 * i + r) * (HD * 2) + (((l & 15) ^ (f << 1)) << 4)),                                    \
           sb + (w >> 2) * 65536 + hf * 32768 + (w & 3) * 8192 + i * 1024, NT_);                                       \
  }
  if (nt) { WQKV(true) } else { WQKV(false) }
#undef WQKV
}

__device__ __forceinline__ unsigned row_stats(const v4u raw) {
  float v[8];
#pragma unroll
  for (int e = 0; e < 4; ++e) { v[2 * e] = lo16(raw[e]); v[2 * e + 1] = hi16(raw[e]); }
  float s = 0.f;
#pragma unroll
  for (int e = 0; e < 8; ++e) s += v[e];
  const float mean = wave_sum(s) * (1.0f / DM);
  float q = 0.f;
#pragma unroll
  for (int e = 0; e < 8; ++e) { const float t = v[e] - mean; q += t * t; }
  const float var = wave_sum(q) * (1.0f / DM);
  const float sd = bfr(sqrtf(bfr(bfr(var) + 1e-5f)));
  return (unsigned)f2bf(mean) | ((unsigned)f2bf(sd) << 16);
}
__device__ __forceinline__ void ask_slow(const MegaArgs& A, const int k, unsigned (&t)[3]) {
  const int l = lane_id(), x = blockIdx.x & 7;
  const char *wq = (const char*)A.Wq, *wv = (const char*)A.Wv;
  asm("" : "+s"(wq), "+s"(wv));
#pragma unroll
  for (int m = 0; m < 3; ++m) {
    const char* W = m == 1 ? (const char*)A.Wk : ((m == 0) == (x < 4)) ? wv : wq;
    const int a = (int)((unsigned long)W >> 8) + 8 * (x & 3), h = k < 32 ? ((1 - a) & 3) + 4 * (k & 1) : (31 - a) & 31;
    const int r = k < 32 ? k >> 1 : k - 32;
    t[m] = __builtin_amdgcn_raw_buffer_load_b32(MKRS(W, (unsigned)k < 48u && h < 8 ? DM * HD * 2 : 0), (l >> 1) * (HD * 2) + (l & 1) * 128,
                                                (32 * (r & 15)) * (HD * 2) + (8 * (x & 3) + (h & 7)) * 256, 0);
  }
}
#define ASKED(t) asm volatile("" ::"v"(t[0]), "v"(t[1]), "v"(t[2]))
template <int N>
__device__ __forceinline__ u64 start_stats(const MegaArgs& A, const int idx) {
  const int w = A.wv, l = lane_id();
  const int r0 = N * (8 * idx + w);
  unsigned ta[3], tb[3];
  v4u raw[N];
#pragma unroll
  for (int i = 0; i < N; ++i) raw[i] = *reinterpret_cast<const v4u*>(A.x0 + (size_t)(r0 + i) * DM + l * 8);
  asm volatile("" ::: "memory");
  ask_slow(A, 16 * w + (idx & 3) + (idx >> 4) * N, ta);
  if (N > 4) ask_slow(A, 16 * w + (idx & 3) + 12, tb);
  unsigned tc[2] = {};
  if (N == 4 && AWH > 0) tc[0] = *reinterpret_cast<const unsigned*>(A.Wo + (size_t)idx * 65536 + 32768 + (64 * w + l) * 64);
  if (N == 4 && AWH > 1 && w >= 4) tc[1] = *reinterpret_cast<const unsigned*>(A.Wo + (size_t)idx * 65536 + (64 * w + l) * 64);
  const u64 tag = call_tag(A);
  unsigned stw = 0u;
#pragma unroll
  for (int i = 0; i < N; ++i) {
    const unsigned t = row_stats(raw[i]);
    if ((l >> 1) == i) stw = t;
  }
  if (l < 2 * N) __hip_atomic_store((gu64*)(A.ws + OFF_GT) + 2 * r0 + l, tagged(tag, (l & 1) ? stw : stw << 16), RLX_AGENT);
  SST(0);
  ASKED(ta);
  if (N > 4) ASKED(tb);
  asm volatile("" ::"v"(tc[0]), "v"(tc[1]));
  return tag;
}
#define MFMAH(a, b, c) __builtin_amdgcn_mfma_f32_16x16x32_f16(a, b, c, 0, 0, 0)
struct Cx {
  f32x2_t r, i;
};
__device__ __forceinline__ Cx cmul(const Cx a, const Cx b) { return Cx{a.r * b.r - a.i * b.i, a.r * b.i + a.i * b.r}; }
struct KGen {
  unsigned pa, pb, pc, pd;
  u16 pe;
  f32x2_t ar, ai;
  Cx C;
  char* st;
  v4u bh[8], bl[8];
  __device__ __forceinline__ void load(const MegaArgs& A, const int d, const int l) {
    pa = *reinterpret_cast<const unsigned*>(A.logAre + d * NST + 2 * l);
    pb = *reinterpret_cast<const unsigned*>(A.Aim + d * NST + 2 * l);
    pc = *reinterpret_cast<const unsigned*>(A.Cre + d * NST + 2 * l);
    pd = *reinterpret_cast<const unsigned*>(A.Cim + d * NST + 2 * l);
    pe = A.logdt[d];
  }
  __device__ __forceinline__ void setup() {
    const float dt = bfr(expf(bf2f(pe)));
    ar = f32x2_t{-bfr(expf(lo16(pa))) * dt, -bfr(expf(hi16(pa))) * dt};
    ai = f32x2_t{lo16(pb) * dt, hi16(pb) * dt};
    C = Cx{f32x2_t{lo16(pc) * dt, hi16(pc) * dt}, f32x2_t{lo16(pd) * dt, hi16(pd) * dt}};
  }
  __device__ __forceinline__ float scale() {
    const float b = 2.0f * wave_sum((fabsf(C.r.x) + fabsf(C.r.y)) + (fabsf(C.i.x) + fabsf(C.i.y)));
    const unsigned eb = max(__float_as_uint(b) >> 23, 25u);
    const float ks = __uint_as_float((268u - eb) << 23);
    C.r *= ks;
    C.i *= ks;
    return __uint_as_float((eb - 24u) << 23);
  }
  __device__ __forceinline__ Cx power(const float p) const {
    Cx z;
#pragma unroll
    for (int e = 0; e < 2; ++e) {
      const float x = ai[e] * p;
      const float f = __builtin_fmaf(x, 6.4206382e-9f, __builtin_fmaf(x, 0.15915494f, -__builtin_rintf(x * 0.15915494f)));
      const float m = __expf(ar[e] * p);
      z.r[e] = m * __builtin_amdgcn_cosf(f);
      z.i[e] = m * __builtin_amdgcn_sinf(f);
    }
    return z;
  }
  static __device__ __forceinline__ unsigned rest(const f32x2_t y, const unsigned h) {
    unsigned r;
    asm("v_fma_mixlo_f16 %0, %1, -1.0, %2 op_sel_hi:[1,0,0]" : "=v"(r) : "v"(h), "v"(y.x));
    asm("v_fma_mixhi_f16 %0, %1, -1.0, %2 op_sel:[1,0,0] op_sel_hi:[1,0,0]" : "+v"(r) : "v"(h), "v"(y.y));
    return r;
  }
  __device__ __forceinline__ char* rows(char* p, Cx& y, const Cx s, const int n) const {
#pragma unroll 1
    for (int i = 0; i < n; i += 2) {
#pragma unroll
      for (int k = 0; k < 2; ++k) {
        const unsigned h0 = pkh(y.r.x, y.r.y), h1 = pkh(y.i.x, y.i.y);
        *reinterpret_cast<v2u*>(p + 1040 * k) = v2u{h0, h1};
        *reinterpret_cast<v2u*>(p + 1040 * k + 512) = v2u{rest(y.r, h0), rest(y.i, h1)};
        y = cmul(y, s);
      }
      p += 2080;
    }
    return p;
  }
  __device__ __forceinline__ void get(v4u (&h)[8], v4u (&o)[8], const int l) const {
    const char* p = st + (l & 15) * 1040 + (l >> 4) * 16;
    asm volatile("" ::: "memory");
#pragma unroll
    for (int k = 0; k < 8; ++k) {
      h[k] = *reinterpret_cast<const v4u*>(p + 64 * k);
      o[k] = *reinterpret_cast<const v4u*>(p + 512 + 64 * k);
    }
    asm volatile("" ::: "memory");
  }
  Cx cy, cs;
  __device__ __forceinline__ void columns(const int l, const int j0, const int j1) {
    if (j0 == 0) {
      cs = power(16.f);
      cs.i = -cs.i;
      cy = Cx{f32x2_t{16384.f, 16384.f}, f32x2_t{0.f, 0.f}};
    }
    rows(st + l * 8 + 1040 * j0, cy, cs, j1 - j0);
  }
  template <bool COL>
  __device__ __forceinline__ f32x4 tile(const int l) const {
    v4u h[8], o[8];
    get(h, o, l);
    f32x4 d = {}, e = {}, f = {};
#define H8(x) __builtin_bit_cast(f16x8, x)
#define KMM(a, b, d) (COL ? MFMAH(H8(b), H8(a), d) : MFMAH(H8(a), H8(b), d))
#pragma unroll
    for (int k = 0; k < 8; ++k) {
      d = KMM(h[k], bh[k], d);
      e = KMM(o[k], bh[k], e);
      f = KMM(h[k], bl[k], f);
    }
#undef KMM
#undef H8
    return (d + (e + f)) * (1.0f / 8192);
  }
};
#define ACC_INIT 0xc9b26c86c9b26ca0ull
#define CHAIN_ASM_ATT \
  "s_mov_b32 s95, 272\n" \
  "s_mov_b32 s91, 0x4c7c2000\n" \
  "s_mov_b32 s85, 0xc9b26c9b\n" \
  "s_mov_b32 s92, 0xc9326c9b\n" \
  "s_mov_b32 s93, 0xc9326c9b\n" \
  "s_mov_b32 s78, 0x3e0293ee\n" \
  "s_mov_b32 s76, 0x33333333\n" \
  "s_mov_b32 s77, 0x33333333\n" \
  "s_lshl_b32 s98, s73, 10\n" \
  "s_add_u32 s98, s98, s74\n" \
  "s_add_u32 s98, s98, 131072\n" \
  "s_lshl_b32 s97, s73, 9\n" \
  "s_add_u32 s97, s97, s74\n" \
  "s_add_u32 s97, s97, 158720\n" \
  "s_lshl_b32 s99, s73, 10\n" \
  "v_mbcnt_lo_u32_b32 v48, -1, 0\n" \
  "v_mbcnt_hi_u32_b32 v48, -1, v48\n" \
  "s_add_u32 s88, s74, 157760\n" \
  "v_mov_b32 v59, s88\n" \
  "v_lshrrev_b32 v49, 2, v48\n" \
  "v_lshrrev_b32 v50, 4, v48\n" \
  "v_bitop3_b32 v50, v49, v50, 3 bitop3:0x28\n" \
  "v_lshl_add_u32 v8, v50, 3, s97\n" \
  "v_and_b32 v50, 3, v48\n" \
  "v_lshl_add_u32 v8, v50, 5, v8\n" \
  "v_and_b32 v49, 1, v49\n" \
  "v_lshl_add_u32 v8, v49, 8, v8\n" \
  "v_and_b32 v49, 8, v48\n" \
  "v_lshl_add_u32 v8, v49, 4, v8\n" \
  "v_lshrrev_b32 v49, 5, v48\n" \
  "v_add_u32 v10, s99, v1\n" \
  "s_lshr_b32 s88, s99, 2\n" \
  "s_add_u32 s88, s88, s74\n" \
  "s_add_u32 s88, s88, 139264\n" \
  "v_lshl_add_u32 v7, v49, 10, s88\n" \
  "v_and_b32 v49, 31, v48\n" \
  "v_lshl_add_u32 v7, v49, 3, v7\n" \
  "v_mov_b64 v[12:13], 0\n" \
  "v_mov_b64 v[14:15], 0\n" \
  "v_mov_b64 v[16:17], 0\n" \
  "v_mov_b64 v[18:19], 0\n" \
  "v_mov_b64 v[20:21], 0\n" \
  "v_mov_b64 v[22:23], 0\n" \
  "v_mov_b32 v34, 0xc9b26ca0\n" \
  "v_mov_b32 v35, 0xc9b26c86\n" \
  "s_mov_b32 s87, 0x40000\n" \
  "v_subrev_u32 v30, 64, v59\n" \
  "v_add_u32 v17, 0x10000, v28\n" \
  "v_xor_b32 v18, 128, v28\n" \
  "v_xor_b32 v19, 128, v17\n" \
  "ds_write_b32 v30, v12 offset:0\n" \
  "ds_write_b32 v30, v12 offset:32\n" \
  "ds_write_b32 v30, v12 offset:64\n" \
  "v_xor_b32 v20, 0, v29\n" \
  "v_xor_b32 v21, 32, v29\n" \
  "v_xor_b32 v22, 64, v29\n" \
  "v_xor_b32 v23, 96, v29\n" \
  "s_waitcnt vmcnt(12) lgkmcnt(0)\n" \
  "s_barrier\n" \
  "ds_read_b64_tr_b16 v[160:161], v20 offset:0\n" \
  "ds_read_b64_tr_b16 v[162:163], v20 offset:2048\n" \
  "ds_read_b64_tr_b16 v[168:169], v21 offset:0\n" \
  "ds_read_b64_tr_b16 v[170:171], v21 offset:2048\n" \
  "ds_read_b64_tr_b16 v[176:177], v22 offset:0\n" \
  "ds_read_b64_tr_b16 v[178:179], v22 offset:2048\n" \
  "ds_read_b64_tr_b16 v[184:185], v23 offset:0\n" \
  "ds_read_b64_tr_b16 v[186:187], v23 offset:2048\n" \
  "s_lshl_b32 s88, s73, 10\n" \
  "v_add_u32 v48, s88, v1\n" \
  "ds_read_b128 v[52:55], v48 offset:0\n" \
  "s_waitcnt lgkmcnt(0)\n" \
  "v_mov_b32 v11, 0\n" \
  "v_dot2c_f32_bf16 v11, v52, v52\n" \
  "v_dot2c_f32_bf16 v11, v53, v53\n" \
  "v_dot2c_f32_bf16 v11, v54, v54\n" \
  "v_dot2c_f32_bf16 v11, v55, v55\n" \
  "s_nop 0\n" \
  "s_nop 1\n" \
  "v_max_u32_dpp v11, v11, v11 row_ror:1 row_mask:0xf bank_mask:0xf\n" \
  "s_nop 1\n" \
  "v_max_u32_dpp v11, v11, v11 row_ror:2 row_mask:0xf bank_mask:0xf\n" \
  "s_nop 1\n" \
  "v_max_u32_dpp v11, v11, v11 row_ror:4 row_mask:0xf bank_mask:0xf\n" \
  "s_nop 1\n" \
  "v_max_u32_dpp v11, v11, v11 row_ror:8 row_mask:0xf bank_mask:0xf\n" \
  "v_mov_b32 v31, v11\n" \
  "s_nop 1\n" \
  "v_permlane16_swap_b32 v11, v31\n" \
  "v_max_u32 v11, v11, v31\n" \
  "v_mov_b32 v31, v11\n" \
  "s_nop 1\n" \
  "v_permlane32_swap_b32 v11, v31\n" \
  "v_max_u32 v11, v11, v31\n" \
  "s_mov_b64 exec, 1\n" \
  "ds_max_u32 v59, v11\n" \
  "s_mov_b64 exec, -1\n" \
  "s_waitcnt vmcnt(8) lgkmcnt(0)\n" \
  "s_barrier\n" \
  "ds_read_b64_tr_b16 v[164:165], v20 offset:4096\n" \
  "ds_read_b64_tr_b16 v[166:167], v20 offset:6144\n" \
  "ds_read_b64_tr_b16 v[172:173], v21 offset:4096\n" \
  "ds_read_b64_tr_b16 v[174:175], v21 offset:6144\n" \
  "ds_read_b64_tr_b16 v[180:181], v22 offset:4096\n" \
  "ds_read_b64_tr_b16 v[182:183], v22 offset:6144\n" \
  "ds_read_b64_tr_b16 v[188:189], v23 offset:4096\n" \
  "ds_read_b64_tr_b16 v[190:191], v23 offset:6144\n" \
  "s_add_u32 m0, s19, 0\n" \
  "s_nop 0\n" \
  "global_load_lds_dwordx4 v24, s[4:5] offset:0\n" \
  "global_load_lds_dwordx4 v25, s[4:5] offset:1024\n" \
  "global_load_lds_dwordx4 v26, s[4:5] offset:2048\n" \
  "global_load_lds_dwordx4 v27, s[4:5] offset:3072\n" \
  "s_add_u32 s4, s4, 0x100000\n" \
  "s_addc_u32 s5, s5, 0\n" \
  "v_mfma_f32_16x16x32_bf16 v[64:67], v[160:163], v[160:163], 0\n" \
  "v_mfma_f32_16x16x32_bf16 v[68:71], v[168:171], v[168:171], 0\n" \
  "v_mfma_f32_16x16x32_bf16 v[72:75], v[176:179], v[176:179], 0\n" \
  "v_mfma_f32_16x16x32_bf16 v[76:79], v[184:187], v[184:187], 0\n" \
  "s_waitcnt vmcnt(8) lgkmcnt(0)\n" \
  "s_barrier\n" \
  "ds_read_b64_tr_b16 v[32:33], v20 offset:8192\n" \
  "ds_read_b64_tr_b16 v[34:35], v20 offset:10240\n" \
  "ds_read_b64_tr_b16 v[40:41], v21 offset:8192\n" \
  "ds_read_b64_tr_b16 v[42:43], v21 offset:10240\n" \
  "ds_read_b64_tr_b16 v[48:49], v22 offset:8192\n" \
  "ds_read_b64_tr_b16 v[50:51], v22 offset:10240\n" \
  "ds_read_b64_tr_b16 v[56:57], v23 offset:8192\n" \
  "ds_read_b64_tr_b16 v[58:59], v23 offset:10240\n" \
  "s_add_u32 m0, s19, 4096\n" \
  "s_nop 0\n" \
  "global_load_lds_dwordx4 v24, s[4:5] offset:0\n" \
  "global_load_lds_dwordx4 v25, s[4:5] offset:1024\n" \
  "global_load_lds_dwordx4 v26, s[4:5] offset:2048\n" \
  "global_load_lds_dwordx4 v27, s[4:5] offset:3072\n" \
  "s_add_u32 s4, s4, 0x100000\n" \
  "s_addc_u32 s5, s5, 0\n" \
  "v_mfma_f32_16x16x32_bf16 v[64:67], v[164:167], v[164:167], v[64:67]\n" \
  "v_mfma_f32_16x16x32_bf16 v[68:71], v[172:175], v[172:175], v[68:71]\n" \
  "v_mfma_f32_16x16x32_bf16 v[72:75], v[180:183], v[180:183], v[72:75]\n" \
  "v_mfma_f32_16x16x32_bf16 v[76:79], v[188:191], v[188:191], v[76:79]\n" \
  "s_waitcnt vmcnt(8) lgkmcnt(0)\n" \
  "s_barrier\n" \
  "ds_read_b64_tr_b16 v[36:37], v20 offset:12288\n" \
  "ds_read_b64_tr_b16 v[38:39], v20 offset:14336\n" \
  "ds_read_b64_tr_b16 v[44:45], v21 offset:12288\n" \
  "ds_read_b64_tr_b16 v[46:47], v21 offset:14336\n" \
  "ds_read_b64_tr_b16 v[52:53], v22 offset:12288\n" \
  "ds_read_b64_tr_b16 v[54:55], v22 offset:14336\n" \
  "ds_read_b64_tr_b16 v[60:61], v23 offset:12288\n" \
  "ds_read_b64_tr_b16 v[62:63], v23 offset:14336\n" \
  "s_add_u32 m0, s19, 8192\n" \
  "s_nop 0\n" \
  "global_load_lds_dwordx4 v24, s[4:5] offset:0\n" \
  "global_load_lds_dwordx4 v25, s[4:5] offset:1024\n" \
  "global_load_lds_dwordx4 v26, s[4:5] offset:2048\n" \
  "global_load_lds_dwordx4 v27, s[4:5] offset:3072\n" \
  "s_add_u32 s4, s4, 0x100000\n" \
  "s_addc_u32 s5, s5, 0\n" \
  "v_mfma_f32_16x16x32_bf16 v[64:67], v[32:35], v[32:35], v[64:67]\n" \
  "v_mfma_f32_16x16x32_bf16 v[68:71], v[40:43], v[40:43], v[68:71]\n" \
  "v_mfma_f32_16x16x32_bf16 v[72:75], v[48:51], v[48:51], v[72:75]\n" \
  "v_mfma_f32_16x16x32_bf16 v[76:79], v[56:59], v[56:59], v[76:79]\n" \
  "s_waitcnt vmcnt(8) lgkmcnt(0)\n" \
  "s_barrier\n" \
  "s_add_u32 m0, s19, 12288\n" \
  "s_nop 0\n" \
  "global_load_lds_dwordx4 v24, s[4:5] offset:0\n" \
  "global_load_lds_dwordx4 v25, s[4:5] offset:1024\n" \
  "global_load_lds_dwordx4 v26, s[4:5] offset:2048\n" \
  "global_load_lds_dwordx4 v27, s[4:5] offset:3072\n" \
  "s_add_u32 s4, s4, 0x100000\n" \
  "s_addc_u32 s5, s5, 0\n" \
  "v_mfma_f32_16x16x32_bf16 v[64:67], v[36:39], v[36:39], v[64:67]\n" \
  "v_mfma_f32_16x16x32_bf16 v[68:71], v[44:47], v[44:47], v[68:71]\n" \
  "v_mfma_f32_16x16x32_bf16 v[72:75], v[52:55], v[52:55], v[72:75]\n" \
  "v_mfma_f32_16x16x32_bf16 v[76:79], v[60:63], v[60:63], v[76:79]\n" \
  "s_nop 7\n" \
  "v_max3_f32 v13, v64, v65, v66\n" \
  "v_max3_f32 v13, v13, v67, v68\n" \
  "v_max3_f32 v13, v13, v69, v70\n" \
  "v_max3_f32 v13, v13, v71, v72\n" \
  "v_max3_f32 v13, v13, v73, v74\n" \
  "v_max3_f32 v13, v13, v75, v76\n" \
  "v_max3_f32 v13, v13, v77, v78\n" \
  "v_max_f32 v13, v13, v79\n" \
  "s_nop 1\n" \
  "v_max_f32_dpp v13, v13, v13 row_ror:1 row_mask:0xf bank_mask:0xf\n" \
  "s_nop 1\n" \
  "v_max_f32_dpp v13, v13, v13 row_ror:2 row_mask:0xf bank_mask:0xf\n" \
  "s_nop 1\n" \
  "v_max_f32_dpp v13, v13, v13 row_ror:4 row_mask:0xf bank_mask:0xf\n" \
  "s_nop 1\n" \
  "v_max_f32_dpp v13, v13, v13 row_ror:8 row_mask:0xf bank_mask:0xf\n" \
  "v_mov_b32 v31, v13\n" \
  "s_nop 1\n" \
  "v_permlane16_swap_b32 v13, v31\n" \
  "v_max_f32 v13, v13, v31\n" \
  "v_mov_b32 v31, v13\n" \
  "s_nop 1\n" \
  "v_permlane32_swap_b32 v13, v31\n" \
  "v_max_f32 v13, v13, v31\n" \
  "s_mov_b64 exec, 1\n" \
  "ds_max_u32 v30, v13 offset:32\n" \
  "s_mov_b64 exec, -1\n" \
  "s_waitcnt vmcnt(8) lgkmcnt(0)\n" \
  "s_barrier\n" \
  "s_add_u32 m0, s19, 0\n" \
  "s_nop 0\n" \
  "global_load_lds_dwordx4 v24, s[6:7] offset:0\n" \
  "global_load_lds_dwordx4 v25, s[6:7] offset:1024\n" \
  "global_load_lds_dwordx4 v26, s[6:7] offset:2048\n" \
  "global_load_lds_dwordx4 v27, s[6:7] offset:3072\n" \
  "s_add_u32 s6, s6, 0x100000\n" \
  "s_addc_u32 s7, s7, 0\n" \
  "s_waitcnt vmcnt(8) lgkmcnt(0)\n" \
  "s_barrier\n" \
  "s_add_u32 m0, s19, 4096\n" \
  "s_nop 0\n" \
  "global_load_lds_dwordx4 v24, s[6:7] offset:0\n" \
  "global_load_lds_dwordx4 v25, s[6:7] offset:1024\n" \
  "global_load_lds_dwordx4 v26, s[6:7] offset:2048\n" \
  "global_load_lds_dwordx4 v27, s[6:7] offset:3072\n" \
  "s_add_u32 s6, s6, 0x100000\n" \
  "s_addc_u32 s7, s7, 0\n" \
  "s_waitcnt vmcnt(8) lgkmcnt(0)\n" \
  "s_barrier\n" \
  "s_add_u32 m0, s19, 8192\n" \
  "s_nop 0\n" \
  "global_load_lds_dwordx4 v24, s[6:7] offset:0\n" \
  "global_load_lds_dwordx4 v25, s[6:7] offset:1024\n" \
  "global_load_lds_dwordx4 v26, s[6:7] offset:2048\n" \
  "global_load_lds_dwordx4 v27, s[6:7] offset:3072\n" \
  "s_add_u32 s6, s6, 0x100000\n" \
  "s_addc_u32 s7, s7, 0\n" \
  "s_waitcnt vmcnt(8) lgkmcnt(0)\n" \
  "s_barrier\n" \
  "ds_read_b64_tr_b16 v[64:65], v28 offset:0\n" \
  "ds_read_b64_tr_b16 v[66:67], v28 offset:512\n" \
  "ds_read_b64_tr_b16 v[68:69], v28 offset:32768\n" \
  "ds_read_b64_tr_b16 v[70:71], v28 offset:33280\n" \
  "ds_read_b64_tr_b16 v[72:73], v17 offset:0\n" \
  "ds_read_b64_tr_b16 v[74:75], v17 offset:512\n" \
  "ds_read_b64_tr_b16 v[76:77], v17 offset:32768\n" \
  "ds_read_b64_tr_b16 v[78:79], v17 offset:33280\n" \
  "ds_read_b64_tr_b16 v[192:193], v18 offset:0\n" \
  "ds_read_b64_tr_b16 v[194:195], v18 offset:512\n" \
  "ds_read_b64_tr_b16 v[196:197], v18 offset:32768\n" \
  "ds_read_b64_tr_b16 v[198:199], v18 offset:33280\n" \
  "ds_read_b64_tr_b16 v[200:201], v19 offset:0\n" \
  "ds_read_b64_tr_b16 v[202:203], v19 offset:512\n" \
  "ds_read_b64_tr_b16 v[204:205], v19 offset:32768\n" \
  "ds_read_b64_tr_b16 v[206:207], v19 offset:33280\n" \
  "s_add_u32 m0, s19, 12288\n" \
  "s_nop 0\n" \
  "global_load_lds_dwordx4 v24, s[6:7] offset:0\n" \
  "global_load_lds_dwordx4 v25, s[6:7] offset:1024\n" \
  "global_load_lds_dwordx4 v26, s[6:7] offset:2048\n" \
  "global_load_lds_dwordx4 v27, s[6:7] offset:3072\n" \
  "s_add_u32 s6, s6, 0x100000\n" \
  "s_addc_u32 s7, s7, 0\n" \
  "s_waitcnt vmcnt(8) lgkmcnt(0)\n" \
  "s_barrier\n" \
  "ds_read_b64_tr_b16 v[80:81], v28 offset:4096\n" \
  "ds_read_b64_tr_b16 v[82:83], v28 offset:4608\n" \
  "ds_read_b64_tr_b16 v[84:85], v28 offset:36864\n" \
  "ds_read_b64_tr_b16 v[86:87], v28 offset:37376\n" \
  "ds_read_b64_tr_b16 v[88:89], v17 offset:4096\n" \
  "ds_read_b64_tr_b16 v[90:91], v17 offset:4608\n" \
  "ds_read_b64_tr_b16 v[92:93], v17 offset:36864\n" \
  "ds_read_b64_tr_b16 v[94:95], v17 offset:37376\n" \
  "ds_read_b64_tr_b16 v[208:209], v18 offset:4096\n" \
  "ds_read_b64_tr_b16 v[210:211], v18 offset:4608\n" \
  "ds_read_b64_tr_b16 v[212:213], v18 offset:36864\n" \
  "ds_read_b64_tr_b16 v[214:215], v18 offset:37376\n" \
  "ds_read_b64_tr_b16 v[216:217], v19 offset:4096\n" \
  "ds_read_b64_tr_b16 v[218:219], v19 offset:4608\n" \
  "ds_read_b64_tr_b16 v[220:221], v19 offset:36864\n" \
  "ds_read_b64_tr_b16 v[222:223], v19 offset:37376\n" \
  "s_add_u32 m0, s19, 0\n" \
  "s_nop 0\n" \
  "global_load_lds_dwordx4 v24, s[8:9] offset:0\n" \
  "global_load_lds_dwordx4 v25, s[8:9] offset:1024\n" \
  "global_load_lds_dwordx4 v26, s[8:9] offset:2048\n" \
  "global_load_lds_dwordx4 v27, s[8:9] offset:3072\n" \
  "s_add_u32 s8, s8, 0x100000\n" \
  "s_addc_u32 s9, s9, 0\n" \
  "s_waitcnt vmcnt(8) lgkmcnt(0)\n" \
  "s_barrier\n" \
  "ds_read_b64_tr_b16 v[96:97], v28 offset:8192\n" \
  "ds_read_b64_tr_b16 v[98:99], v28 offset:8704\n" \
  "ds_read_b64_tr_b16 v[100:101], v28 offset:40960\n" \
  "ds_read_b64_tr_b16 v[102:103], v28 offset:41472\n" \
  "ds_read_b64_tr_b16 v[104:105], v17 offset:8192\n" \
  "ds_read_b64_tr_b16 v[106:107], v17 offset:8704\n" \
  "ds_read_b64_tr_b16 v[108:109], v17 offset:40960\n" \
  "ds_read_b64_tr_b16 v[110:111], v17 offset:41472\n" \
  "ds_read_b64_tr_b16 v[224:225], v18 offset:8192\n" \
  "ds_read_b64_tr_b16 v[226:227], v18 offset:8704\n" \
  "ds_read_b64_tr_b16 v[228:229], v18 offset:40960\n" \
  "ds_read_b64_tr_b16 v[230:231], v18 offset:41472\n" \
  "ds_read_b64_tr_b16 v[232:233], v19 offset:8192\n" \
  "ds_read_b64_tr_b16 v[234:235], v19 offset:8704\n" \
  "ds_read_b64_tr_b16 v[236:237], v19 offset:40960\n" \
  "ds_read_b64_tr_b16 v[238:239], v19 offset:41472\n" \
  "s_add_u32 m0, s19, 4096\n" \
  "s_nop 0\n" \
  "global_load_lds_dwordx4 v24, s[8:9] offset:0\n" \
  "global_load_lds_dwordx4 v25, s[8:9] offset:1024\n" \
  "global_load_lds_dwordx4 v26, s[8:9] offset:2048\n" \
  "global_load_lds_dwordx4 v27, s[8:9] offset:3072\n" \
  "s_add_u32 s8, s8, 0x100000\n" \
  "s_addc_u32 s9, s9, 0\n" \
  "s_waitcnt vmcnt(8) lgkmcnt(0)\n" \
  "s_barrier\n" \
  "ds_read_b64_tr_b16 v[112:113], v28 offset:12288\n" \
  "ds_read_b64_tr_b16 v[114:115], v28 offset:12800\n" \
  "ds_read_b64_tr_b16 v[116:117], v28 offset:45056\n" \
  "ds_read_b64_tr_b16 v[118:119], v28 offset:45568\n" \
  "ds_read_b64_tr_b16 v[120:121], v17 offset:12288\n" \
  "ds_read_b64_tr_b16 v[122:123], v17 offset:12800\n" \
  "ds_read_b64_tr_b16 v[124:125], v17 offset:45056\n" \
  "ds_read_b64_tr_b16 v[126:127], v17 offset:45568\n" \
  "ds_read_b64_tr_b16 v[240:241], v18 offset:12288\n" \
  "ds_read_b64_tr_b16 v[242:243], v18 offset:12800\n" \
  "ds_read_b64_tr_b16 v[244:245], v18 offset:45056\n" \
  "ds_read_b64_tr_b16 v[246:247], v18 offset:45568\n" \
  "ds_read_b64_tr_b16 v[248:249], v19 offset:12288\n" \
  "ds_read_b64_tr_b16 v[250:251], v19 offset:12800\n" \
  "ds_read_b64_tr_b16 v[252:253], v19 offset:45056\n" \
  "ds_read_b64_tr_b16 v[254:255], v19 offset:45568\n" \
  "s_add_u32 m0, s19, 8192\n" \
  "s_nop 0\n" \
  "global_load_lds_dwordx4 v24, s[8:9] offset:0\n" \
  "global_load_lds_dwordx4 v25, s[8:9] offset:1024\n" \
  "global_load_lds_dwordx4 v26, s[8:9] offset:2048\n" \
  "global_load_lds_dwordx4 v27, s[8:9] offset:3072\n" \
  "s_add_u32 s8, s8, 0x100000\n" \
  "s_addc_u32 s9, s9, 0\n" \
  "s_waitcnt vmcnt(8) lgkmcnt(0)\n" \
  "s_barrier\n" \
  "ds_read_b64_tr_b16 v[128:129], v28 offset:0\n" \
  "ds_read_b64_tr_b16 v[130:131], v28 offset:512\n" \
  "ds_read_b64_tr_b16 v[132:133], v28 offset:32768\n" \
  "ds_read_b64_tr_b16 v[134:135], v28 offset:33280\n" \
  "ds_read_b64_tr_b16 v[136:137], v17 offset:0\n" \
  "ds_read_b64_tr_b16 v[138:139], v17 offset:512\n" \
  "ds_read_b64_tr_b16 v[140:141], v17 offset:32768\n" \
  "ds_read_b64_tr_b16 v[142:143], v17 offset:33280\n" \
  "s_add_u32 m0, s19, 12288\n" \
  "s_nop 0\n" \
  "global_load_lds_dwordx4 v24, s[8:9] offset:0\n" \
  "global_load_lds_dwordx4 v25, s[8:9] offset:1024\n" \
  "global_load_lds_dwordx4 v26, s[8:9] offset:2048\n" \
  "global_load_lds_dwordx4 v27, s[8:9] offset:3072\n" \
  "s_add_u32 s8, s8, 0x100000\n" \
  "s_addc_u32 s9, s9, 0\n" \
  "s_waitcnt vmcnt(8) lgkmcnt(0)\n" \
  "s_barrier\n" \
  "ds_write_b128 v6, v[160:163] offset:0\n" \
  "ds_write_b128 v6, v[164:167] offset:1024\n" \
  "ds_write_b128 v6, v[168:171] offset:2048\n" \
  "ds_write_b128 v6, v[172:175] offset:3072\n" \
  "ds_read_b64_tr_b16 v[144:145], v28 offset:4096\n" \
  "ds_read_b64_tr_b16 v[146:147], v28 offset:4608\n" \
  "ds_read_b64_tr_b16 v[148:149], v28 offset:36864\n" \
  "ds_read_b64_tr_b16 v[150:151], v28 offset:37376\n" \
  "ds_read_b64_tr_b16 v[152:153], v17 offset:4096\n" \
  "ds_read_b64_tr_b16 v[154:155], v17 offset:4608\n" \
  "ds_read_b64_tr_b16 v[156:157], v17 offset:36864\n" \
  "ds_read_b64_tr_b16 v[158:159], v17 offset:37376\n" \
  "s_waitcnt vmcnt(4) lgkmcnt(0)\n" \
  "s_barrier\n" \
  "ds_write_b128 v6, v[176:179] offset:4096\n" \
  "ds_write_b128 v6, v[180:183] offset:5120\n" \
  "ds_write_b128 v6, v[184:187] offset:6144\n" \
  "ds_write_b128 v6, v[188:191] offset:7168\n" \
  "ds_read_b64_tr_b16 v[160:161], v28 offset:8192\n" \
  "ds_read_b64_tr_b16 v[162:163], v28 offset:8704\n" \
  "ds_read_b64_tr_b16 v[164:165], v28 offset:40960\n" \
  "ds_read_b64_tr_b16 v[166:167], v28 offset:41472\n" \
  "ds_read_b64_tr_b16 v[168:169], v17 offset:8192\n" \
  "ds_read_b64_tr_b16 v[170:171], v17 offset:8704\n" \
  "ds_read_b64_tr_b16 v[172:173], v17 offset:40960\n" \
  "ds_read_b64_tr_b16 v[174:175], v17 offset:41472\n" \
  "s_waitcnt vmcnt(0) lgkmcnt(0)\n" \
  "s_barrier\n" \
  "ds_write_b128 v6, v[32:35] offset:8192\n" \
  "ds_write_b128 v6, v[36:39] offset:9216\n" \
  "ds_write_b128 v6, v[40:43] offset:10240\n" \
  "ds_write_b128 v6, v[44:47] offset:11264\n" \
  "ds_read_b64_tr_b16 v[176:177], v28 offset:12288\n" \
  "ds_read_b64_tr_b16 v[178:179], v28 offset:12800\n" \
  "ds_read_b64_tr_b16 v[180:181], v28 offset:45056\n" \
  "ds_read_b64_tr_b16 v[182:183], v28 offset:45568\n" \
  "ds_read_b64_tr_b16 v[184:185], v17 offset:12288\n" \
  "ds_read_b64_tr_b16 v[186:187], v17 offset:12800\n" \
  "ds_read_b64_tr_b16 v[188:189], v17 offset:45056\n" \
  "ds_read_b64_tr_b16 v[190:191], v17 offset:45568\n" \
  "ds_read_b32 v31, v30 offset:0\n" \
  "s_waitcnt lgkmcnt(0)\n" \
  "s_barrier\n" \
  "ds_write_b128 v6, v[48:51] offset:12288\n" \
  "ds_write_b128 v6, v[52:55] offset:13312\n" \
  "ds_write_b128 v6, v[56:59] offset:14336\n" \
  "ds_write_b128 v6, v[60:63] offset:15360\n" \
  "v_mul_f32 v31, 0x3f8e353f, v31\n" \
  "v_mul_f32 v30, 0x44870000, v31\n" \
  "v_mov_b64 v[12:13], 0\n" \
  "v_mov_b64 v[14:15], 0\n" \
  "v_mov_b64 v[16:17], 0\n" \
  "v_mov_b64 v[18:19], 0\n" \
  "v_mov_b64 v[20:21], 0\n" \
  "v_mov_b64 v[22:23], 0\n" \
  "v_readfirstlane_b32 s72, v31\n" \
  "v_readfirstlane_b32 s86, v30\n" \
  "s_lshr_b32 s86, s86, 23\n" \
  "s_addk_i32 s86, 0xff83\n" \
  "s_ashr_i32 s86, s86, 1\n" \
  "s_max_i32 s86, s86, -20\n" \
  "s_min_i32 s86, s86, 100\n" \
  "s_mov_b32 s82, s86\n" \
  "s_lshr_b32 s83, s72, 23\n" \
  "s_sub_i32 s83, s83, 243\n" \
  "s_waitcnt lgkmcnt(0)\n" \
  "s_add_u32 s88, s74, 157760\n" \
  "v_mov_b32 v59, s88\n" \
  "v_mov_b32 v34, 0xc9b26ca0\n" \
  "v_mov_b32 v35, 0xc9b26c86\n" \
  ".p2align 6\n" \
  "s_nop 0\n" \
  "s_nop 0\n" \
  "s_nop 0\n" \
  ".LCA_TOP_%=:\n" \
  "s_waitcnt lgkmcnt(0)\n" \
  "s_barrier\n" \
  "ds_read_b128 v[40:43], v1 offset:0\n" \
  "ds_read_b128 v[44:47], v1 offset:1024\n" \
  "ds_read_b128 v[48:51], v1 offset:2048\n" \
  "ds_read_b128 v[52:55], v1 offset:3072\n" \
  "ds_read_b128 v[56:59], v1 offset:4096\n" \
  "ds_read_b128 v[60:63], v1 offset:5120\n" \
  "s_cmp_lg_u32 s73, 0\n" \
  "s_cbranch_scc1 .LCA_NOZ_%=\n" \
  "s_xor_b32 s88, s60, 0x10000\n" \
  "s_add_u32 s88, s88, s61\n" \
  "buffer_store_dwordx2 v[34:35], v5, s[40:43], s88 offen sc1\n" \
  ".LCA_NOZ_%=:\n" \
  "s_waitcnt lgkmcnt(5)\n" \
  "v_smfmac_f32_16x16x64_bf16 v[20:23], v[40:43], v[192:199], v0\n" \
  "v_smfmac_f32_16x16x64_bf16 v[12:15], v[40:43], v[64:71], v0\n" \
  "v_smfmac_f32_16x16x64_bf16 v[16:19], v[40:43], v[128:135], v0\n" \
  "ds_read_b128 v[40:43], v1 offset:6144\n" \
  "s_waitcnt lgkmcnt(5)\n" \
  "v_smfmac_f32_16x16x64_bf16 v[20:23], v[44:47], v[200:207], v0\n" \
  "v_smfmac_f32_16x16x64_bf16 v[12:15], v[44:47], v[72:79], v0\n" \
  "v_smfmac_f32_16x16x64_bf16 v[16:19], v[44:47], v[136:143], v0\n" \
  "ds_read_b128 v[44:47], v1 offset:7168\n" \
  "s_waitcnt lgkmcnt(5)\n" \
  "v_smfmac_f32_16x16x64_bf16 v[20:23], v[48:51], v[208:215], v0\n" \
  "v_smfmac_f32_16x16x64_bf16 v[12:15], v[48:51], v[80:87], v0\n" \
  "v_smfmac_f32_16x16x64_bf16 v[16:19], v[48:51], v[144:151], v0\n" \
  "ds_read_b128 v[24:27], v1 offset:10240\n" \
  "ds_read_b128 v[28:31], v1 offset:14336\n" \
  "s_waitcnt lgkmcnt(6)\n" \
  "v_smfmac_f32_16x16x64_bf16 v[20:23], v[52:55], v[216:223], v0\n" \
  "v_smfmac_f32_16x16x64_bf16 v[12:15], v[52:55], v[88:95], v0\n" \
  "v_smfmac_f32_16x16x64_bf16 v[16:19], v[52:55], v[152:159], v0\n" \
  "ds_read_b128 v[32:35], v1 offset:11264\n" \
  "ds_read_b128 v[36:39], v1 offset:15360\n" \
  "s_waitcnt lgkmcnt(7)\n" \
  "v_smfmac_f32_16x16x64_bf16 v[20:23], v[56:59], v[224:231], v0\n" \
  "v_smfmac_f32_16x16x64_bf16 v[12:15], v[56:59], v[96:103], v0\n" \
  "v_smfmac_f32_16x16x64_bf16 v[16:19], v[56:59], v[160:167], v0\n" \
  "ds_read_b128 v[56:59], v1 offset:12288\n" \
  "s_waitcnt lgkmcnt(7)\n" \
  "v_smfmac_f32_16x16x64_bf16 v[20:23], v[60:63], v[232:239], v0\n" \
  "v_smfmac_f32_16x16x64_bf16 v[12:15], v[60:63], v[104:111], v0\n" \
  "v_smfmac_f32_16x16x64_bf16 v[16:19], v[60:63], v[168:175], v0\n" \
  "ds_read_b128 v[60:63], v1 offset:16384\n" \
  "s_waitcnt lgkmcnt(7)\n" \
  "v_smfmac_f32_16x16x64_bf16 v[20:23], v[40:43], v[240:247], v0\n" \
  "v_smfmac_f32_16x16x64_bf16 v[12:15], v[40:43], v[112:119], v0\n" \
  "v_smfmac_f32_16x16x64_bf16 v[16:19], v[40:43], v[176:183], v0\n" \
  "s_waitcnt lgkmcnt(6)\n" \
  "v_smfmac_f32_16x16x64_bf16 v[20:23], v[44:47], v[248:255], v0\n" \
  "v_smfmac_f32_16x16x64_bf16 v[12:15], v[44:47], v[120:127], v0\n" \
  "v_smfmac_f32_16x16x64_bf16 v[16:19], v[44:47], v[184:191], v0\n" \
  "s_cmp_eq_u32 s71, 0\n" \
  "v_permlane32_swap_b32 v20, v22\n" \
  "v_permlane32_swap_b32 v21, v23\n" \
  "v_pk_add_f32 v[20:21], v[20:21], v[22:23]\n" \
  "s_mov_b64 vcc, s[76:77]\n" \
  "v_permlane32_swap_b32 v12, v14\n" \
  "v_permlane32_swap_b32 v13, v15\n" \
  "v_cvt_pk_bf16_f32 v20, v20, v21\n" \
  "v_pk_add_f32 v[12:13], v[12:13], v[14:15]\n" \
  "v_permlane32_swap_b32 v16, v18\n" \
  "v_permlane32_swap_b32 v17, v19\n" \
  "ds_write_b32 v8, v20 offset:2048\n" \
  "v_cvt_pk_bf16_f32 v12, v12, v13\n" \
  "v_pk_add_f32 v[16:17], v[16:17], v[18:19]\n" \
  "s_nop 0\n" \
  "v_cvt_pk_bf16_f32 v13, v16, v17\n" \
  "ds_write_b64 v8, v[12:13]\n" \
  "s_setprio 1\n" \
  "s_waitcnt lgkmcnt(0)\n" \
  "s_barrier\n" \
  "s_cbranch_scc1 .LCA_LAST_%=\n" \
  "ds_read_b64_tr_b16 v[40:41], v2 offset:0\n" \
  "ds_read_b64_tr_b16 v[42:43], v3 offset:0\n" \
  "ds_read_b64_tr_b16 v[44:45], v2 offset:1024\n" \
  "ds_read_b64_tr_b16 v[46:47], v3 offset:1024\n" \
  "ds_read_b64_tr_b16 v[48:49], v2 offset:2048\n" \
  "ds_read_b64_tr_b16 v[50:51], v3 offset:2048\n" \
  "ds_read_b64_tr_b16 v[52:53], v2 offset:3072\n" \
  "ds_read_b64_tr_b16 v[54:55], v3 offset:3072\n" \
  "ds_read_b32 v11, v1 offset:26688\n" \
  "s_waitcnt lgkmcnt(7)\n" \
  "v_mfma_f32_16x16x32_bf16 v[12:15], v[24:27], v[40:43], 0\n" \
  "v_mfma_f32_16x16x32_bf16 v[16:19], v[28:31], v[40:43], 0\n" \
  "ds_read_b128 v[24:27], v1 offset:13312\n" \
  "ds_read_b128 v[28:31], v1 offset:17408\n" \
  "v_mfma_f32_16x16x32_bf16 v[20:23], v[40:43], v[40:43], 0\n" \
  "s_waitcnt lgkmcnt(7)\n" \
  "v_mfma_f32_16x16x32_bf16 v[12:15], v[32:35], v[44:47], v[12:15]\n" \
  "v_mfma_f32_16x16x32_bf16 v[16:19], v[36:39], v[44:47], v[16:19]\n" \
  "v_mfma_f32_16x16x32_bf16 v[20:23], v[44:47], v[44:47], v[20:23]\n" \
  "s_waitcnt lgkmcnt(2)\n" \
  "v_mfma_f32_16x16x32_bf16 v[12:15], v[56:59], v[48:51], v[12:15]\n" \
  "v_readfirstlane_b32 s86, v11\n" \
  "v_mfma_f32_16x16x32_bf16 v[16:19], v[60:63], v[48:51], v[16:19]\n" \
  "ds_read_b128 v[32:35], v6 offset:8192\n" \
  "ds_read_b128 v[36:39], v6 offset:9216\n" \
  "v_mfma_f32_16x16x32_bf16 v[20:23], v[48:51], v[48:51], v[20:23]\n" \
  "s_lshr_b32 s86, s86, 23\n" \
  "s_add_i32 s86, s86, s83\n" \
  "s_ashr_i32 s86, s86, 1\n" \
  "ds_read_b128 v[48:51], v10 offset:18432\n" \
  "v_mfma_f32_16x16x32_bf16 v[20:23], v[52:55], v[52:55], v[20:23]\n" \
  "s_max_i32 s86, s86, s82\n" \
  "s_min_i32 s86, s86, 100\n" \
  "s_lshl_b32 s86, s86, 23\n" \
  "s_sub_i32 s84, 0xc8800000, s86\n" \
  "s_waitcnt lgkmcnt(3)\n" \
  "v_mfma_f32_16x16x32_bf16 v[12:15], v[24:27], v[52:55], v[12:15]\n" \
  "v_mfma_f32_16x16x32_bf16 v[16:19], v[28:31], v[52:55], v[16:19]\n" \
  "ds_read_b128 v[52:55], v10 offset:22528\n" \
  "ds_read_b128 v[56:59], v6 offset:0\n" \
  "ds_read_b128 v[60:63], v6 offset:1024\n" \
  "ds_read_b128 v[24:27], v6 offset:2048\n" \
  "ds_read_b128 v[28:31], v6 offset:3072\n" \
  "v_cndmask_b32_dpp v20, v23, v22, vcc quad_perm:[2,3,0,1] row_mask:0xf bank_mask:0xf\n" \
  "v_mov_b32 v41, 0\n" \
  "v_mov_b32 v42, 0\n" \
  "v_cndmask_b32_dpp v12, v16, v12, vcc quad_perm:[2,3,0,1] row_mask:0xf bank_mask:0xf\n" \
  "v_cndmask_b32_dpp v13, v17, v13, vcc quad_perm:[2,3,0,1] row_mask:0xf bank_mask:0xf\n" \
  "v_cndmask_b32_dpp v14, v18, v14, vcc quad_perm:[2,3,0,1] row_mask:0xf bank_mask:0xf\n" \
  "v_cndmask_b32_dpp v15, v19, v15, vcc quad_perm:[2,3,0,1] row_mask:0xf bank_mask:0xf\n" \
  "v_maximum3_f32 v16, v20, v12, v13\n" \
  "v_maximum3_f32 v16, v16, v14, v15\n" \
  "s_nop 1\n" \
  "v_max_f32_dpp v16, v16, v16 quad_perm:[2,3,0,1] row_mask:0xf bank_mask:0xf\n" \
  "v_mov_b32 v17, v16\n" \
  "s_nop 1\n" \
  "v_permlane16_swap_b32 v16, v17\n" \
  "v_max_f32 v16, v16, v17\n" \
  "v_mov_b32 v17, v16\n" \
  "s_nop 1\n" \
  "v_permlane32_swap_b32 v16, v17\n" \
  "v_max_f32 v16, v16, v17\n" \
  "v_mul_f32 v16, s78, v16\n" \
  "v_pk_fma_f32 v[12:13], v[12:13], s[78:79], v[16:17] op_sel_hi:[1,0,0] neg_lo:[0,0,1] neg_hi:[0,0,1]\n" \
  "v_pk_fma_f32 v[14:15], v[14:15], s[78:79], v[16:17] op_sel_hi:[1,0,0] neg_lo:[0,0,1] neg_hi:[0,0,1]\n" \
  "v_fma_f32 v20, v20, s78, -v16\n" \
  "v_exp_f32 v12, v12\n" \
  "v_exp_f32 v13, v13\n" \
  "v_exp_f32 v14, v14\n" \
  "v_exp_f32 v15, v15\n" \
  "v_exp_f32 v20, v20\n" \
  "s_waitcnt lgkmcnt(4)\n" \
  "v_cvt_pk_bf16_f32 v44, v12, v13\n" \
  "v_cvt_pk_bf16_f32 v45, v14, v15\n" \
  "v_pk_add_f32 v[8:9], v[12:13], v[14:15]\n" \
  "v_mov_b32_dpp v46, v44 quad_perm:[2,3,0,1] row_mask:0xf bank_mask:0xf\n" \
  "v_mov_b32_dpp v47, v45 quad_perm:[2,3,0,1] row_mask:0xf bank_mask:0xf\n" \
  "v_add_f32 v10, v8, v20\n" \
  "v_add_f32 v10, v10, v9\n" \
  "v_mfma_f32_16x16x32_bf16 v[12:15], v[48:51], v[44:47], 0\n" \
  "v_mov_b32_dpp v21, v20 quad_perm:[2,3,0,1] row_mask:0xf bank_mask:0xf\n" \
  "v_add_f32_dpp v10, v10, v10 quad_perm:[2,3,0,1] row_mask:0xf bank_mask:0xf\n" \
  "v_mov_b32 v11, v10\n" \
  "v_mfma_f32_16x16x32_bf16 v[16:19], v[52:55], v[44:47], 0\n" \
  "s_setprio 3\n" \
  "s_barrier\n" \
  "ds_read_b64 v[22:23], v7 offset:0\n" \
  "s_nop 0\n" \
  "v_permlane16_swap_b32 v10, v11\n" \
  "v_add_f32 v10, v10, v11\n" \
  "v_mov_b32 v11, v10\n" \
  "v_mov_b64 v[8:9], s[92:93]\n" \
  "s_nop 0\n" \
  "v_permlane32_swap_b32 v10, v11\n" \
  "v_add_f32 v10, v10, v11\n" \
  "v_rcp_f32 v10, v10\n" \
  "s_nop 0\n" \
  "v_mul_f32 v10, s84, v10\n" \
  "v_pk_mul_f32 v[20:21], v[20:21], v[10:11] op_sel_hi:[1,0]\n" \
  "v_pk_mul_f32 v[12:13], v[12:13], v[10:11] op_sel_hi:[1,0]\n" \
  "v_pk_mul_f32 v[14:15], v[14:15], v[10:11] op_sel_hi:[1,0]\n" \
  "v_cvt_pk_bf16_f32 v40, v20, v21\n" \
  "v_pk_mul_f32 v[16:17], v[16:17], v[10:11] op_sel_hi:[1,0]\n" \
  "v_cvt_pk_bf16_f32 v43, v20, v21\n" \
  "v_pk_mul_f32 v[18:19], v[18:19], v[10:11] op_sel_hi:[1,0]\n" \
  "s_waitcnt lgkmcnt(0)\n" \
  "v_mfma_f32_16x16x16_bf16 v[12:15], v[22:23], v[40:41], v[12:15]\n" \
  "v_mfma_f32_16x16x16_bf16 v[16:19], v[22:23], v[42:43], v[16:19]\n" \
  "v_mov_b64 v[10:11], s[92:93]\n" \
  "ds_read_b128 v[48:51], v6 offset:10240\n" \
  "ds_read_b128 v[52:55], v6 offset:11264\n" \
  "v_mov_b64 v[20:21], s[92:93]\n" \
  "v_mov_b64 v[22:23], s[92:93]\n" \
  "s_nop 1\n" \
  "v_cvt_pk_bf16_f32 v40, v12, v13\n" \
  "v_cvt_pk_bf16_f32 v41, v14, v15\n" \
  "v_cvt_pk_bf16_f32 v42, v16, v17\n" \
  "ds_write_b64 v4, v[40:41]\n" \
  "v_cvt_pk_bf16_f32 v43, v18, v19\n" \
  "ds_write_b64 v4, v[42:43] offset:1024\n" \
  "v_mov_b64 v[12:13], s[92:93]\n" \
  "v_mov_b64 v[14:15], s[92:93]\n" \
  "v_mov_b64 v[16:17], s[92:93]\n" \
  "v_mov_b64 v[18:19], s[92:93]\n" \
  "s_setprio 0\n" \
  "s_waitcnt lgkmcnt(0)\n" \
  "s_barrier\n" \
  "ds_read_b128 v[40:43], v1 offset:8192\n" \
  "ds_read_b128 v[44:47], v1 offset:9216\n" \
  "s_waitcnt lgkmcnt(1)\n" \
  "v_smfmac_f32_16x16x64_bf16 v[12:15], v[40:43], v[56:63], v0\n" \
  "ds_read_b128 v[56:59], v6 offset:4096\n" \
  "ds_read_b128 v[60:63], v6 offset:5120\n" \
  "v_smfmac_f32_16x16x64_bf16 v[16:19], v[40:43], v[24:31], v0\n" \
  "ds_read_b128 v[24:27], v6 offset:6144\n" \
  "ds_read_b128 v[28:31], v6 offset:7168\n" \
  "s_waitcnt lgkmcnt(4)\n" \
  "v_smfmac_f32_16x16x64_bf16 v[12:15], v[44:47], v[32:39], v0\n" \
  "ds_read_b128 v[32:35], v6 offset:12288\n" \
  "ds_read_b128 v[36:39], v6 offset:13312\n" \
  "v_smfmac_f32_16x16x64_bf16 v[16:19], v[44:47], v[48:55], v0\n" \
  "ds_read_b128 v[48:51], v6 offset:14336\n" \
  "ds_read_b128 v[52:55], v6 offset:15360\n" \
  "s_nop 5\n" \
  "v_permlane32_swap_b32 v12, v17\n" \
  "v_permlane32_swap_b32 v13, v16\n" \
  "v_permlane32_swap_b32 v14, v19\n" \
  "v_permlane32_swap_b32 v15, v18\n" \
  "v_pk_add_f32 v[12:13], v[12:13], v[16:17] op_sel:[0,1] op_sel_hi:[1,0]\n" \
  "v_pk_add_f32 v[14:15], v[14:15], v[18:19] op_sel:[0,1] op_sel_hi:[1,0]\n" \
  "buffer_atomic_add_x2 v[12:13], v5, s[40:43], s56 offen\n" \
  "buffer_atomic_add_x2 v[14:15], v5, s[40:43], s57 offen\n" \
  "s_waitcnt lgkmcnt(6)\n" \
  "v_smfmac_f32_16x16x64_bf16 v[20:23], v[40:43], v[56:63], v0\n" \
  "s_waitcnt lgkmcnt(4)\n" \
  "v_smfmac_f32_16x16x64_bf16 v[8:11], v[40:43], v[24:31], v0\n" \
  "s_waitcnt lgkmcnt(2)\n" \
  "v_smfmac_f32_16x16x64_bf16 v[20:23], v[44:47], v[32:39], v0\n" \
  "s_waitcnt lgkmcnt(0)\n" \
  "v_smfmac_f32_16x16x64_bf16 v[8:11], v[44:47], v[48:55], v0\n" \
  "s_nop 7\n" \
  "v_permlane32_swap_b32 v20, v9\n" \
  "v_permlane32_swap_b32 v21, v8\n" \
  "v_permlane32_swap_b32 v22, v11\n" \
  "v_permlane32_swap_b32 v23, v10\n" \
  "v_pk_add_f32 v[20:21], v[20:21], v[8:9] op_sel:[0,1] op_sel_hi:[1,0]\n" \
  "v_pk_add_f32 v[22:23], v[22:23], v[10:11] op_sel:[0,1] op_sel_hi:[1,0]\n" \
  "buffer_atomic_add_x2 v[20:21], v5, s[40:43], s58 offen\n" \
  "buffer_atomic_add_x2 v[22:23], v5, s[40:43], s59 offen\n" \
  "s_memrealtime s[38:39]\n" \
  "v_mbcnt_lo_u32_b32 v48, -1, 0\n" \
  "v_mbcnt_hi_u32_b32 v48, -1, v48\n" \
  "s_add_u32 s88, s74, 157760\n" \
  "v_mov_b32 v59, s88\n" \
  "v_lshl_add_u32 v52, v48, 2, v59\n" \
  "ds_read_b32 v52, v52 offset:192\n" \
  "v_lshrrev_b32 v49, 3, v48\n" \
  "v_and_b32 v50, 7, v48\n" \
  "v_lshlrev_b32 v50, 4, v50\n" \
  "v_lshl_or_b32 v44, v49, 8, v50\n" \
  "s_mov_b32 s87, 0x40000\n" \
  "v_lshrrev_b32 v49, 2, v48\n" \
  "v_lshrrev_b32 v50, 4, v48\n" \
  "v_bitop3_b32 v50, v49, v50, 3 bitop3:0x28\n" \
  "v_lshl_add_u32 v8, v50, 3, s97\n" \
  "v_and_b32 v50, 3, v48\n" \
  "v_lshl_add_u32 v8, v50, 5, v8\n" \
  "v_and_b32 v49, 1, v49\n" \
  "v_lshl_add_u32 v8, v49, 8, v8\n" \
  "v_and_b32 v49, 8, v48\n" \
  "v_lshl_add_u32 v8, v49, 4, v8\n" \
  "v_lshrrev_b32 v49, 5, v48\n" \
  "v_add_u32 v10, s99, v1\n" \
  "s_lshr_b32 s88, s99, 2\n" \
  "s_add_u32 s88, s88, s74\n" \
  "s_add_u32 s88, s88, 139264\n" \
  "v_lshl_add_u32 v7, v49, 10, s88\n" \
  "v_and_b32 v49, 31, v48\n" \
  "v_lshl_add_u32 v7, v49, 3, v7\n" \
  "v_mov_b64 v[12:13], 0\n" \
  "v_mov_b64 v[14:15], 0\n" \
  "v_mov_b64 v[16:17], 0\n" \
  "v_mov_b64 v[18:19], 0\n" \
  "v_mov_b64 v[20:21], 0\n" \
  "v_mov_b64 v[22:23], 0\n" \
  "v_mov_b32 v34, 0xc9b26ca0\n" \
  "v_mov_b32 v35, 0xc9b26c86\n" \
  "v_and_b32 v50, 3, v48\n" \
  "v_lshl_add_u32 v40, v50, 2, s98\n" \
  "v_lshrrev_b32 v50, 4, v48\n" \
  "v_mad_u32_u24 v40, v50, s95, v40\n" \
  "v_bfe_u32 v49, v48, 3, 1\n" \
  "v_lshl_add_u32 v40, v49, 6, v40\n" \
  "v_bfe_u32 v49, v48, 2, 1\n" \
  "v_lshl_add_u32 v40, v49, 7, v40\n" \
  "v_xor_b32 v41, 16, v40\n" \
  "v_xor_b32 v42, 32, v40\n" \
  "v_xor_b32 v43, 48, v40\n" \
  "s_add_i32 s80, s86, 0x35000000\n" \
  "v_mov_b32 v11, 0\n" \
  "s_mov_b32 s89, s60\n" \
  "s_bitcmp1_b32 s60, 7\n" \
  "s_cselect_b32 s88, 0x10000, 0\n" \
  "s_bitset1_b32 s88, 7\n" \
  "s_xor_b32 s56, s56, s88\n" \
  "s_xor_b32 s57, s57, s88\n" \
  "s_xor_b32 s58, s58, s88\n" \
  "s_xor_b32 s59, s59, s88\n" \
  "s_xor_b32 s60, s60, s88\n" \
  "s_sub_u32 s71, s71, 1\n" \
  "s_waitcnt lgkmcnt(0)\n" \
  "s_sub_u32 s39, s38, 150\n" \
  "v_subrev_u32 v52, s39, v52\n" \
  "v_cmp_lt_u32 vcc, 0x1000, v52\n" \
  "s_cmp_eq_u64 vcc, 0\n" \
  "s_cbranch_scc1 .LCA_OKM_%=\n" \
  ".LCA_AGN_%=:\n" \
  "buffer_load_dword v52, v5, s[0:3], s66 offen sc1\n" \
  "s_sub_u32 s87, s87, 1\n" \
  "s_cbranch_scc1 .LCA_END_%=\n" \
  "s_waitcnt vmcnt(0)\n" \
  "v_cmp_eq_u32 vcc, 0, v52\n" \
  "v_subrev_u32 v52, s39, v52\n" \
  "s_cmp_eq_u64 vcc, 0\n" \
  "s_cbranch_scc1 .LCA_OKM_%=\n" \
  "s_sleep 4\n" \
  "s_branch .LCA_AGN_%=\n" \
  ".LCA_OKM_%=:\n" \
  "s_nop 1\n" \
  "v_max_i32_dpp v52, v52, v52 row_ror:1 row_mask:0xf bank_mask:0xf\n" \
  "s_nop 1\n" \
  "v_max_i32_dpp v52, v52, v52 row_ror:2 row_mask:0xf bank_mask:0xf\n" \
  "s_nop 1\n" \
  "v_max_i32_dpp v52, v52, v52 row_ror:4 row_mask:0xf bank_mask:0xf\n" \
  "s_nop 1\n" \
  "v_max_i32_dpp v52, v52, v52 row_ror:8 row_mask:0xf bank_mask:0xf\n" \
  "s_movk_i32 vcc_lo, 150\n" \
  "v_readlane_b32 s88, v52, 0\n" \
  "s_max_i32 vcc_lo, vcc_lo, s88\n" \
  "v_readlane_b32 s88, v52, 16\n" \
  "s_max_i32 vcc_lo, vcc_lo, s88\n" \
  "v_readlane_b32 s88, v52, 32\n" \
  "s_max_i32 vcc_lo, vcc_lo, s88\n" \
  "v_readlane_b32 s88, v52, 48\n" \
  "s_max_i32 vcc_lo, vcc_lo, s88\n" \
  "s_add_u32 s66, s66, 1024\n" \
  "s_add_u32 vcc_lo, vcc_lo, s39\n" \
  "s_add_u32 vcc_lo, vcc_lo, 23\n" \
  ".LCA_TW_%=:\n" \
  "s_memrealtime s[38:39]\n" \
  "s_waitcnt lgkmcnt(0)\n" \
  "s_sub_u32 s38, s38, vcc_lo\n" \
  "s_cmp_lt_i32 s38, 0\n" \
  "s_cbranch_scc1 .LCA_TW_%=\n" \
  "buffer_load_dwordx4 v[24:27], v44, s[40:43], s89 offen sc1\n" \
  "buffer_load_dwordx4 v[28:31], v44, s[40:43], s89 offen offset:2048 sc1\n" \
  ".LCA_SEE_%=:\n" \
  "s_waitcnt vmcnt(1)\n" \
  "v_cvt_f32_i32 v24, v24\n" \
  "v_cvt_f32_i32 v25, v25\n" \
  "v_cvt_f32_i32 v26, v26\n" \
  "v_cvt_f32_i32 v27, v27\n" \
  "v_max_f32 v45, |v24|, |v26|\n" \
  "v_pk_mul_f32 v[24:25], v[24:25], s[80:81] op_sel_hi:[1,0]\n" \
  "v_pk_mul_f32 v[26:27], v[26:27], s[80:81] op_sel_hi:[1,0]\n" \
  "s_nop 0\n" \
  "v_cvt_pk_bf16_f32 v32, v24, v26\n" \
  "v_cvt_pk_bf16_f32 v33, v25, v27\n" \
  "ds_write_b32 v40, v32\n" \
  "ds_write_b32 v41, v33\n" \
  "s_waitcnt vmcnt(0)\n" \
  "v_cvt_f32_i32 v28, v28\n" \
  "v_cvt_f32_i32 v30, v30\n" \
  "v_cvt_f32_i32 v29, v29\n" \
  "v_max3_f32 v45, v45, |v28|, |v30|\n" \
  "v_cvt_f32_i32 v31, v31\n" \
  "v_cmp_lt_f32 vcc, s91, v45\n" \
  "v_pk_mul_f32 v[28:29], v[28:29], s[80:81] op_sel_hi:[1,0]\n" \
  "v_pk_mul_f32 v[30:31], v[30:31], s[80:81] op_sel_hi:[1,0]\n" \
  "s_cmp_eq_u64 vcc, 0\n" \
  "s_cbranch_scc0 .LCA_POLL_%=\n" \
  "v_cvt_pk_bf16_f32 v36, v28, v30\n" \
  "v_cvt_pk_bf16_f32 v37, v29, v31\n" \
  "ds_write_b32 v42, v36\n" \
  "ds_write_b32 v43, v37\n" \
  "v_dot2c_f32_bf16 v11, v32, v32\n" \
  "v_dot2c_f32_bf16 v11, v33, v33\n" \
  "v_dot2c_f32_bf16 v11, v36, v36\n" \
  "v_dot2c_f32_bf16 v11, v37, v37\n" \
  "s_nop 1\n" \
  "s_nop 0\n" \
  "v_cmp_lt_f32 vcc, 4.0, v11\n" \
  "s_cbranch_vccz .LCA_TOP_%=\n" \
  "s_mov_b64 exec, vcc\n" \
  "ds_max_u32 v59, v11\n" \
  "s_mov_b64 exec, -1\n" \
  "s_branch .LCA_TOP_%=\n" \
  ".LCA_POLL_%=:\n" \
  "buffer_load_dwordx4 v[24:27], v44, s[40:43], s89 offen sc1\n" \
  "buffer_load_dwordx4 v[28:31], v44, s[40:43], s89 offen offset:2048 sc1\n" \
  "s_sub_u32 s87, s87, 1\n" \
  "s_cbranch_scc1 .LCA_END_%=\n" \
  "s_branch .LCA_SEE_%=\n" \
  ".LCA_LAST_%=:\n" \
  ".LCA_END_%=:\n" \
  "s_endpgm\n"
#define CHAIN_ASM_VO \
  "s_mov_b32 s95, 272\n" \
  "s_mov_b32 s91, 0x4c7c2000\n" \
  "s_mov_b32 s85, 0xc9b26c9b\n" \
  "s_mov_b32 s92, 0xc9326c9b\n" \
  "s_mov_b32 s93, 0xc9326c9b\n" \
  "s_mov_b32 s78, 0x3e0293ee\n" \
  "s_mov_b32 s76, 0x33333333\n" \
  "s_mov_b32 s77, 0x33333333\n" \
  "s_lshl_b32 s98, s73, 10\n" \
  "s_add_u32 s98, s98, s74\n" \
  "s_add_u32 s98, s98, 131072\n" \
  "s_lshl_b32 s97, s73, 9\n" \
  "s_add_u32 s97, s97, s74\n" \
  "s_add_u32 s97, s97, 158724\n" \
  "s_lshl_b32 s99, s73, 11\n" \
  "s_add_u32 s99, s99, s74\n" \
  "s_add_u32 s99, s99, 133120\n" \
  "s_lshl_b32 s94, s73, 4\n" \
  "s_add_u32 s94, s94, s74\n" \
  "s_add_u32 s94, s94, 157760\n" \
  "v_mbcnt_lo_u32_b32 v48, -1, 0\n" \
  "v_mbcnt_hi_u32_b32 v48, -1, v48\n" \
  "s_add_u32 s88, s74, 157760\n" \
  "v_mov_b32 v59, s88\n" \
  "s_sub_u32 s88, s73, 4\n" \
  "v_lshrrev_b32 v49, 2, v48\n" \
  "v_lshrrev_b32 v50, 4, v48\n" \
  "v_bitop3_b32 v50, v49, v50, 3 bitop3:0x28\n" \
  "v_lshl_add_u32 v4, v50, 3, s97\n" \
  "v_and_b32 v50, 3, v48\n" \
  "v_lshl_add_u32 v4, v50, 5, v4\n" \
  "v_and_b32 v49, 1, v49\n" \
  "v_lshl_add_u32 v4, v49, 8, v4\n" \
  "v_and_b32 v49, 8, v48\n" \
  "v_lshl_add_u32 v4, v49, 4, v4\n" \
  "v_and_b32 v51, 15, v48\n" \
  "v_bfe_u32 v49, v48, 4, 1\n" \
  "v_lshrrev_b32 v50, 5, v48\n" \
  "s_lshl_b32 s89, s88, 10\n" \
  "s_add_u32 s89, s89, s74\n" \
  "s_add_u32 s89, s89, 158720\n" \
  "v_lshrrev_b32 v9, 3, v51\n" \
  "v_mul_u32_u24 v6, 41, v9\n" \
  "v_xor_b32 v6, v6, v51\n" \
  "v_lshl_add_u32 v6, v6, 3, s89\n" \
  "v_lshl_add_u32 v6, v49, 6, v6\n" \
  "v_lshl_add_u32 v6, v50, 9, v6\n" \
  "v_xor_b32 v9, 0x90, v6\n" \
  "v_lshlrev_b32 v8, 4, v49\n" \
  "v_lshl_add_u32 v8, v51, 5, v8\n" \
  "v_lshl_add_u32 v8, v50, 3, v8\n" \
  "s_lshl_b32 s89, s88, 8\n" \
  "s_add_u32 s89, s89, s74\n" \
  "s_add_u32 s89, s89, 139264\n" \
  "v_and_b32 v49, 31, v48\n" \
  "v_lshl_add_u32 v5, v49, 3, s89\n" \
  "v_lshl_add_u32 v5, v50, 10, v5\n" \
  "v_lshrrev_b32 v49, 2, v51\n" \
  "v_and_b32 v50, 5, v51\n" \
  "v_and_or_b32 v49, v49, 2, v50\n" \
  "v_and_b32 v50, 48, v48\n" \
  "v_lshlrev_b32 v7, 3, v50\n" \
  "v_lshl_add_u32 v7, v49, 4, v7\n" \
  "v_bitop3_b32 v49, v51, 2, 2 bitop3:0x0c\n" \
  "v_lshl_add_u32 v7, v49, 29, v7\n" \
  "v_mov_b64 v[12:13], 0\n" \
  "v_mov_b64 v[14:15], 0\n" \
  "v_mov_b64 v[16:17], 0\n" \
  "v_mov_b64 v[18:19], 0\n" \
  "v_mov_b64 v[20:21], 0\n" \
  "v_mov_b64 v[22:23], 0\n" \
  "v_lshlrev_b32 v10, 4, v48\n" \
  "s_add_u32 s78, s74, 157952\n" \
  "v_lshl_add_u32 v52, v48, 2, v59\n" \
  "ds_write_b32 v52, v12 offset:192\n" \
  "s_movk_i32 s79, 105\n" \
  "s_movk_i32 s96, 25\n" \
  "s_mov_b32 s87, 0x40000\n" \
  "v_subrev_u32 v30, 64, v59\n" \
  "v_add_u32 v17, 0x10000, v28\n" \
  "v_xor_b32 v18, 128, v28\n" \
  "v_xor_b32 v19, 128, v17\n" \
  "ds_write_b32 v30, v12 offset:0\n" \
  "ds_write_b32 v30, v12 offset:32\n" \
  "ds_write_b32 v30, v12 offset:64\n" \
  "v_xor_b32 v20, 0, v29\n" \
  "v_xor_b32 v21, 32, v29\n" \
  "v_xor_b32 v22, 64, v29\n" \
  "v_xor_b32 v23, 96, v29\n" \
  "s_waitcnt vmcnt(12) lgkmcnt(0)\n" \
  "s_barrier\n" \
  "ds_read_b64_tr_b16 v[96:97], v20 offset:0\n" \
  "ds_read_b64_tr_b16 v[98:99], v20 offset:2048\n" \
  "ds_read_b64_tr_b16 v[104:105], v21 offset:0\n" \
  "ds_read_b64_tr_b16 v[106:107], v21 offset:2048\n" \
  "ds_read_b64_tr_b16 v[112:113], v22 offset:0\n" \
  "ds_read_b64_tr_b16 v[114:115], v22 offset:2048\n" \
  "ds_read_b64_tr_b16 v[120:121], v23 offset:0\n" \
  "ds_read_b64_tr_b16 v[122:123], v23 offset:2048\n" \
  "s_lshl_b32 s88, s73, 10\n" \
  "v_add_u32 v48, s88, v1\n" \
  "ds_read_b128 v[52:55], v48 offset:0\n" \
  "s_waitcnt lgkmcnt(0)\n" \
  "v_mov_b32 v11, 0\n" \
  "v_dot2c_f32_bf16 v11, v52, v52\n" \
  "v_dot2c_f32_bf16 v11, v53, v53\n" \
  "v_dot2c_f32_bf16 v11, v54, v54\n" \
  "v_dot2c_f32_bf16 v11, v55, v55\n" \
  "s_nop 0\n" \
  "s_nop 1\n" \
  "v_max_u32_dpp v11, v11, v11 row_ror:1 row_mask:0xf bank_mask:0xf\n" \
  "s_nop 1\n" \
  "v_max_u32_dpp v11, v11, v11 row_ror:2 row_mask:0xf bank_mask:0xf\n" \
  "s_nop 1\n" \
  "v_max_u32_dpp v11, v11, v11 row_ror:4 row_mask:0xf bank_mask:0xf\n" \
  "s_nop 1\n" \
  "v_max_u32_dpp v11, v11, v11 row_ror:8 row_mask:0xf bank_mask:0xf\n" \
  "v_mov_b32 v31, v11\n" \
  "s_nop 1\n" \
  "v_permlane16_swap_b32 v11, v31\n" \
  "v_max_u32 v11, v11, v31\n" \
  "v_mov_b32 v31, v11\n" \
  "s_nop 1\n" \
  "v_permlane32_swap_b32 v11, v31\n" \
  "v_max_u32 v11, v11, v31\n" \
  "s_mov_b64 exec, 1\n" \
  "ds_max_u32 v59, v11\n" \
  "s_mov_b64 exec, -1\n" \
  "s_waitcnt vmcnt(8) lgkmcnt(0)\n" \
  "s_barrier\n" \
  "ds_read_b64_tr_b16 v[100:101], v20 offset:4096\n" \
  "ds_read_b64_tr_b16 v[102:103], v20 offset:6144\n" \
  "ds_read_b64_tr_b16 v[108:109], v21 offset:4096\n" \
  "ds_read_b64_tr_b16 v[110:111], v21 offset:6144\n" \
  "ds_read_b64_tr_b16 v[116:117], v22 offset:4096\n" \
  "ds_read_b64_tr_b16 v[118:119], v22 offset:6144\n" \
  "ds_read_b64_tr_b16 v[124:125], v23 offset:4096\n" \
  "ds_read_b64_tr_b16 v[126:127], v23 offset:6144\n" \
  "s_add_u32 m0, s19, 0\n" \
  "s_nop 0\n" \
  "global_load_lds_dwordx4 v24, s[4:5] offset:0\n" \
  "global_load_lds_dwordx4 v25, s[4:5] offset:1024\n" \
  "global_load_lds_dwordx4 v26, s[4:5] offset:2048\n" \
  "global_load_lds_dwordx4 v27, s[4:5] offset:3072\n" \
  "s_add_u32 s4, s4, 0x100000\n" \
  "s_addc_u32 s5, s5, 0\n" \
  "v_mfma_f32_16x16x32_bf16 v[64:67], v[96:99], v[96:99], 0\n" \
  "v_mfma_f32_16x16x32_bf16 v[68:71], v[104:107], v[104:107], 0\n" \
  "v_mfma_f32_16x16x32_bf16 v[72:75], v[112:115], v[112:115], 0\n" \
  "v_mfma_f32_16x16x32_bf16 v[76:79], v[120:123], v[120:123], 0\n" \
  "s_waitcnt vmcnt(8) lgkmcnt(0)\n" \
  "s_barrier\n" \
  "ds_read_b64_tr_b16 v[32:33], v20 offset:8192\n" \
  "ds_read_b64_tr_b16 v[34:35], v20 offset:10240\n" \
  "ds_read_b64_tr_b16 v[40:41], v21 offset:8192\n" \
  "ds_read_b64_tr_b16 v[42:43], v21 offset:10240\n" \
  "ds_read_b64_tr_b16 v[48:49], v22 offset:8192\n" \
  "ds_read_b64_tr_b16 v[50:51], v22 offset:10240\n" \
  "ds_read_b64_tr_b16 v[56:57], v23 offset:8192\n" \
  "ds_read_b64_tr_b16 v[58:59], v23 offset:10240\n" \
  "s_add_u32 m0, s19, 4096\n" \
  "s_nop 0\n" \
  "global_load_lds_dwordx4 v24, s[4:5] offset:0\n" \
  "global_load_lds_dwordx4 v25, s[4:5] offset:1024\n" \
  "global_load_lds_dwordx4 v26, s[4:5] offset:2048\n" \
  "global_load_lds_dwordx4 v27, s[4:5] offset:3072\n" \
  "s_add_u32 s4, s4, 0x100000\n" \
  "s_addc_u32 s5, s5, 0\n" \
  "v_mfma_f32_16x16x32_bf16 v[64:67], v[100:103], v[100:103], v[64:67]\n" \
  "v_mfma_f32_16x16x32_bf16 v[68:71], v[108:111], v[108:111], v[68:71]\n" \
  "v_mfma_f32_16x16x32_bf16 v[72:75], v[116:119], v[116:119], v[72:75]\n" \
  "v_mfma_f32_16x16x32_bf16 v[76:79], v[124:127], v[124:127], v[76:79]\n" \
  "s_waitcnt vmcnt(8) lgkmcnt(0)\n" \
  "s_barrier\n" \
  "ds_read_b64_tr_b16 v[36:37], v20 offset:12288\n" \
  "ds_read_b64_tr_b16 v[38:39], v20 offset:14336\n" \
  "ds_read_b64_tr_b16 v[44:45], v21 offset:12288\n" \
  "ds_read_b64_tr_b16 v[46:47], v21 offset:14336\n" \
  "ds_read_b64_tr_b16 v[52:53], v22 offset:12288\n" \
  "ds_read_b64_tr_b16 v[54:55], v22 offset:14336\n" \
  "ds_read_b64_tr_b16 v[60:61], v23 offset:12288\n" \
  "ds_read_b64_tr_b16 v[62:63], v23 offset:14336\n" \
  "s_add_u32 m0, s19, 8192\n" \
  "s_nop 0\n" \
  "global_load_lds_dwordx4 v24, s[4:5] offset:0\n" \
  "global_load_lds_dwordx4 v25, s[4:5] offset:1024\n" \
  "global_load_lds_dwordx4 v26, s[4:5] offset:2048\n" \
  "global_load_lds_dwordx4 v27, s[4:5] offset:3072\n" \
  "s_add_u32 s4, s4, 0x100000\n" \
  "s_addc_u32 s5, s5, 0\n" \
  "v_mfma_f32_16x16x32_bf16 v[64:67], v[32:35], v[32:35], v[64:67]\n" \
  "v_mfma_f32_16x16x32_bf16 v[68:71], v[40:43], v[40:43], v[68:71]\n" \
  "v_mfma_f32_16x16x32_bf16 v[72:75], v[48:51], v[48:51], v[72:75]\n" \
  "v_mfma_f32_16x16x32_bf16 v[76:79], v[56:59], v[56:59], v[76:79]\n" \
  "s_waitcnt vmcnt(8) lgkmcnt(0)\n" \
  "s_barrier\n" \
  "ds_read_b64_tr_b16 v[128:129], v18 offset:0\n" \
  "ds_read_b64_tr_b16 v[130:131], v18 offset:512\n" \
  "ds_read_b64_tr_b16 v[132:133], v18 offset:32768\n" \
  "ds_read_b64_tr_b16 v[134:135], v18 offset:33280\n" \
  "ds_read_b64_tr_b16 v[136:137], v19 offset:0\n" \
  "ds_read_b64_tr_b16 v[138:139], v19 offset:512\n" \
  "ds_read_b64_tr_b16 v[140:141], v19 offset:32768\n" \
  "ds_read_b64_tr_b16 v[142:143], v19 offset:33280\n" \
  "ds_read_b64_tr_b16 v[192:193], v28 offset:0\n" \
  "ds_read_b64_tr_b16 v[194:195], v28 offset:512\n" \
  "ds_read_b64_tr_b16 v[196:197], v28 offset:32768\n" \
  "ds_read_b64_tr_b16 v[198:199], v28 offset:33280\n" \
  "ds_read_b64_tr_b16 v[200:201], v17 offset:0\n" \
  "ds_read_b64_tr_b16 v[202:203], v17 offset:512\n" \
  "ds_read_b64_tr_b16 v[204:205], v17 offset:32768\n" \
  "ds_read_b64_tr_b16 v[206:207], v17 offset:33280\n" \
  "s_add_u32 m0, s19, 12288\n" \
  "s_nop 0\n" \
  "global_load_lds_dwordx4 v24, s[4:5] offset:0\n" \
  "global_load_lds_dwordx4 v25, s[4:5] offset:1024\n" \
  "global_load_lds_dwordx4 v26, s[4:5] offset:2048\n" \
  "global_load_lds_dwordx4 v27, s[4:5] offset:3072\n" \
  "s_add_u32 s4, s4, 0x100000\n" \
  "s_addc_u32 s5, s5, 0\n" \
  "v_mfma_f32_16x16x32_bf16 v[64:67], v[36:39], v[36:39], v[64:67]\n" \
  "v_mfma_f32_16x16x32_bf16 v[68:71], v[44:47], v[44:47], v[68:71]\n" \
  "v_mfma_f32_16x16x32_bf16 v[72:75], v[52:55], v[52:55], v[72:75]\n" \
  "v_mfma_f32_16x16x32_bf16 v[76:79], v[60:63], v[60:63], v[76:79]\n" \
  "s_nop 7\n" \
  "v_max3_f32 v13, v64, v65, v66\n" \
  "v_max3_f32 v13, v13, v67, v68\n" \
  "v_max3_f32 v13, v13, v69, v70\n" \
  "v_max3_f32 v13, v13, v71, v72\n" \
  "v_max3_f32 v13, v13, v73, v74\n" \
  "v_max3_f32 v13, v13, v75, v76\n" \
  "v_max3_f32 v13, v13, v77, v78\n" \
  "v_max_f32 v13, v13, v79\n" \
  "s_nop 1\n" \
  "v_max_f32_dpp v13, v13, v13 row_ror:1 row_mask:0xf bank_mask:0xf\n" \
  "s_nop 1\n" \
  "v_max_f32_dpp v13, v13, v13 row_ror:2 row_mask:0xf bank_mask:0xf\n" \
  "s_nop 1\n" \
  "v_max_f32_dpp v13, v13, v13 row_ror:4 row_mask:0xf bank_mask:0xf\n" \
  "s_nop 1\n" \
  "v_max_f32_dpp v13, v13, v13 row_ror:8 row_mask:0xf bank_mask:0xf\n" \
  "v_mov_b32 v31, v13\n" \
  "s_nop 1\n" \
  "v_permlane16_swap_b32 v13, v31\n" \
  "v_max_f32 v13, v13, v31\n" \
  "v_mov_b32 v31, v13\n" \
  "s_nop 1\n" \
  "v_permlane32_swap_b32 v13, v31\n" \
  "v_max_f32 v13, v13, v31\n" \
  "s_mov_b64 exec, 1\n" \
  "ds_max_u32 v30, v13 offset:32\n" \
  "s_mov_b64 exec, -1\n" \
  "s_waitcnt vmcnt(8) lgkmcnt(0)\n" \
  "s_barrier\n" \
  "ds_read_b64_tr_b16 v[144:145], v18 offset:4096\n" \
  "ds_read_b64_tr_b16 v[146:147], v18 offset:4608\n" \
  "ds_read_b64_tr_b16 v[148:149], v18 offset:36864\n" \
  "ds_read_b64_tr_b16 v[150:151], v18 offset:37376\n" \
  "ds_read_b64_tr_b16 v[152:153], v19 offset:4096\n" \
  "ds_read_b64_tr_b16 v[154:155], v19 offset:4608\n" \
  "ds_read_b64_tr_b16 v[156:157], v19 offset:36864\n" \
  "ds_read_b64_tr_b16 v[158:159], v19 offset:37376\n" \
  "ds_read_b64_tr_b16 v[208:209], v28 offset:4096\n" \
  "ds_read_b64_tr_b16 v[210:211], v28 offset:4608\n" \
  "ds_read_b64_tr_b16 v[212:213], v28 offset:36864\n" \
  "ds_read_b64_tr_b16 v[214:215], v28 offset:37376\n" \
  "ds_read_b64_tr_b16 v[216:217], v17 offset:4096\n" \
  "ds_read_b64_tr_b16 v[218:219], v17 offset:4608\n" \
  "ds_read_b64_tr_b16 v[220:221], v17 offset:36864\n" \
  "ds_read_b64_tr_b16 v[222:223], v17 offset:37376\n" \
  "s_add_u32 m0, s19, 0\n" \
  "s_nop 0\n" \
  "global_load_lds_dwordx4 v24, s[6:7] offset:0\n" \
  "global_load_lds_dwordx4 v25, s[6:7] offset:1024\n" \
  "global_load_lds_dwordx4 v26, s[6:7] offset:2048\n" \
  "global_load_lds_dwordx4 v27, s[6:7] offset:3072\n" \
  "s_add_u32 s6, s6, 0x100000\n" \
  "s_addc_u32 s7, s7, 0\n" \
  "v_mfma_f32_16x16x32_bf16 v[64:67], v[128:131], v[128:131], 0\n" \
  "v_mfma_f32_16x16x32_bf16 v[68:71], v[192:195], v[192:195], 0\n" \
  "v_mfma_f32_16x16x32_bf16 v[64:67], v[132:135], v[132:135], v[64:67]\n" \
  "v_mfma_f32_16x16x32_bf16 v[68:71], v[196:199], v[196:199], v[68:71]\n" \
  "v_mfma_f32_16x16x32_bf16 v[64:67], v[136:139], v[136:139], v[64:67]\n" \
  "v_mfma_f32_16x16x32_bf16 v[68:71], v[200:203], v[200:203], v[68:71]\n" \
  "v_mfma_f32_16x16x32_bf16 v[64:67], v[140:143], v[140:143], v[64:67]\n" \
  "v_mfma_f32_16x16x32_bf16 v[68:71], v[204:207], v[204:207], v[68:71]\n" \
  "s_waitcnt vmcnt(8) lgkmcnt(0)\n" \
  "s_barrier\n" \
  "ds_read_b64_tr_b16 v[160:161], v18 offset:8192\n" \
  "ds_read_b64_tr_b16 v[162:163], v18 offset:8704\n" \
  "ds_read_b64_tr_b16 v[164:165], v18 offset:40960\n" \
  "ds_read_b64_tr_b16 v[166:167], v18 offset:41472\n" \
  "ds_read_b64_tr_b16 v[168:169], v19 offset:8192\n" \
  "ds_read_b64_tr_b16 v[170:171], v19 offset:8704\n" \
  "ds_read_b64_tr_b16 v[172:173], v19 offset:40960\n" \
  "ds_read_b64_tr_b16 v[174:175], v19 offset:41472\n" \
  "ds_read_b64_tr_b16 v[224:225], v28 offset:8192\n" \
  "ds_read_b64_tr_b16 v[226:227], v28 offset:8704\n" \
  "ds_read_b64_tr_b16 v[228:229], v28 offset:40960\n" \
  "ds_read_b64_tr_b16 v[230:231], v28 offset:41472\n" \
  "ds_read_b64_tr_b16 v[232:233], v17 offset:8192\n" \
  "ds_read_b64_tr_b16 v[234:235], v17 offset:8704\n" \
  "ds_read_b64_tr_b16 v[236:237], v17 offset:40960\n" \
  "ds_read_b64_tr_b16 v[238:239], v17 offset:41472\n" \
  "s_add_u32 m0, s19, 4096\n" \
  "s_nop 0\n" \
  "global_load_lds_dwordx4 v24, s[6:7] offset:0\n" \
  "global_load_lds_dwordx4 v25, s[6:7] offset:1024\n" \
  "global_load_lds_dwordx4 v26, s[6:7] offset:2048\n" \
  "global_load_lds_dwordx4 v27, s[6:7] offset:3072\n" \
  "s_add_u32 s6, s6, 0x100000\n" \
  "s_addc_u32 s7, s7, 0\n" \
  "v_mfma_f32_16x16x32_bf16 v[64:67], v[144:147], v[144:147], v[64:67]\n" \
  "v_mfma_f32_16x16x32_bf16 v[68:71], v[208:211], v[208:211], v[68:71]\n" \
  "v_mfma_f32_16x16x32_bf16 v[64:67], v[148:151], v[148:151], v[64:67]\n" \
  "v_mfma_f32_16x16x32_bf16 v[68:71], v[212:215], v[212:215], v[68:71]\n" \
  "v_mfma_f32_16x16x32_bf16 v[64:67], v[152:155], v[152:155], v[64:67]\n" \
  "v_mfma_f32_16x16x32_bf16 v[68:71], v[216:219], v[216:219], v[68:71]\n" \
  "v_mfma_f32_16x16x32_bf16 v[64:67], v[156:159], v[156:159], v[64:67]\n" \
  "v_mfma_f32_16x16x32_bf16 v[68:71], v[220:223], v[220:223], v[68:71]\n" \
  "s_waitcnt vmcnt(8) lgkmcnt(0)\n" \
  "s_barrier\n" \
  "ds_read_b64_tr_b16 v[176:177], v18 offset:12288\n" \
  "ds_read_b64_tr_b16 v[178:179], v18 offset:12800\n" \
  "ds_read_b64_tr_b16 v[180:181], v18 offset:45056\n" \
  "ds_read_b64_tr_b16 v[182:183], v18 offset:45568\n" \
  "ds_read_b64_tr_b16 v[184:185], v19 offset:12288\n" \
  "ds_read_b64_tr_b16 v[186:187], v19 offset:12800\n" \
  "ds_read_b64_tr_b16 v[188:189], v19 offset:45056\n" \
  "ds_read_b64_tr_b16 v[190:191], v19 offset:45568\n" \
  "ds_read_b64_tr_b16 v[240:241], v28 offset:12288\n" \
  "ds_read_b64_tr_b16 v[242:243], v28 offset:12800\n" \
  "ds_read_b64_tr_b16 v[244:245], v28 offset:45056\n" \
  "ds_read_b64_tr_b16 v[246:247], v28 offset:45568\n" \
  "ds_read_b64_tr_b16 v[248:249], v17 offset:12288\n" \
  "ds_read_b64_tr_b16 v[250:251], v17 offset:12800\n" \
  "ds_read_b64_tr_b16 v[252:253], v17 offset:45056\n" \
  "ds_read_b64_tr_b16 v[254:255], v17 offset:45568\n" \
  "s_add_u32 m0, s19, 8192\n" \
  "s_nop 0\n" \
  "global_load_lds_dwordx4 v24, s[6:7] offset:0\n" \
  "global_load_lds_dwordx4 v25, s[6:7] offset:1024\n" \
  "global_load_lds_dwordx4 v26, s[6:7] offset:2048\n" \
  "global_load_lds_dwordx4 v27, s[6:7] offset:3072\n" \
  "s_add_u32 s6, s6, 0x100000\n" \
  "s_addc_u32 s7, s7, 0\n" \
  "v_mfma_f32_16x16x32_bf16 v[64:67], v[160:163], v[160:163], v[64:67]\n" \
  "v_mfma_f32_16x16x32_bf16 v[68:71], v[224:227], v[224:227], v[68:71]\n" \
  "v_mfma_f32_16x16x32_bf16 v[64:67], v[164:167], v[164:167], v[64:67]\n" \
  "v_mfma_f32_16x16x32_bf16 v[68:71], v[228:231], v[228:231], v[68:71]\n" \
  "v_mfma_f32_16x16x32_bf16 v[64:67], v[168:171], v[168:171], v[64:67]\n" \
  "v_mfma_f32_16x16x32_bf16 v[68:71], v[232:235], v[232:235], v[68:71]\n" \
  "v_mfma_f32_16x16x32_bf16 v[64:67], v[172:175], v[172:175], v[64:67]\n" \
  "v_mfma_f32_16x16x32_bf16 v[68:71], v[236:239], v[236:239], v[68:71]\n" \
  "s_waitcnt vmcnt(8) lgkmcnt(0)\n" \
  "s_barrier\n" \
  "s_add_u32 m0, s19, 12288\n" \
  "s_nop 0\n" \
  "global_load_lds_dwordx4 v24, s[6:7] offset:0\n" \
  "global_load_lds_dwordx4 v25, s[6:7] offset:1024\n" \
  "global_load_lds_dwordx4 v26, s[6:7] offset:2048\n" \
  "global_load_lds_dwordx4 v27, s[6:7] offset:3072\n" \
  "s_add_u32 s6, s6, 0x100000\n" \
  "s_addc_u32 s7, s7, 0\n" \
  "v_mfma_f32_16x16x32_bf16 v[64:67], v[176:179], v[176:179], v[64:67]\n" \
  "v_mfma_f32_16x16x32_bf16 v[68:71], v[240:243], v[240:243], v[68:71]\n" \
  "v_mfma_f32_16x16x32_bf16 v[64:67], v[180:183], v[180:183], v[64:67]\n" \
  "v_mfma_f32_16x16x32_bf16 v[68:71], v[244:247], v[244:247], v[68:71]\n" \
  "v_mfma_f32_16x16x32_bf16 v[64:67], v[184:187], v[184:187], v[64:67]\n" \
  "v_mfma_f32_16x16x32_bf16 v[68:71], v[248:251], v[248:251], v[68:71]\n" \
  "v_mfma_f32_16x16x32_bf16 v[64:67], v[188:191], v[188:191], v[64:67]\n" \
  "v_mfma_f32_16x16x32_bf16 v[68:71], v[252:255], v[252:255], v[68:71]\n" \
  "s_nop 7\n" \
  "s_mov_b32 exec_lo, 0x100001\n" \
  "s_mov_b32 exec_hi, 0x10000100\n" \
  "v_add_f32 v12, v64, v68\n" \
  "s_mov_b32 exec_lo, 0x200002\n" \
  "s_mov_b32 exec_hi, 0x20000200\n" \
  "v_add_f32 v12, v65, v69\n" \
  "s_mov_b32 exec_lo, 0x400004\n" \
  "s_mov_b32 exec_hi, 0x40000400\n" \
  "v_add_f32 v12, v66, v70\n" \
  "s_mov_b32 exec_lo, 0x800008\n" \
  "s_mov_b32 exec_hi, 0x80000800\n" \
  "v_add_f32 v12, v67, v71\n" \
  "s_mov_b64 exec, -1\n" \
  "s_nop 1\n" \
  "v_add_f32_dpp v12, v12, v12 row_ror:1 row_mask:0xf bank_mask:0xf\n" \
  "s_nop 1\n" \
  "v_add_f32_dpp v12, v12, v12 row_ror:2 row_mask:0xf bank_mask:0xf\n" \
  "s_nop 1\n" \
  "v_add_f32_dpp v12, v12, v12 row_ror:4 row_mask:0xf bank_mask:0xf\n" \
  "s_nop 1\n" \
  "v_add_f32_dpp v12, v12, v12 row_ror:8 row_mask:0xf bank_mask:0xf\n" \
  "v_mov_b32 v31, v12\n" \
  "s_nop 1\n" \
  "v_permlane16_swap_b32 v12, v31\n" \
  "v_add_f32 v12, v12, v31\n" \
  "v_mov_b32 v31, v12\n" \
  "s_nop 1\n" \
  "v_permlane32_swap_b32 v12, v31\n" \
  "v_add_f32 v12, v12, v31\n" \
  "v_lshl_add_u32 v31, s73, 2, v30\n" \
  "s_mov_b64 exec, 1\n" \
  "ds_write_b32 v31, v12\n" \
  "s_mov_b64 exec, -1\n" \
  "s_waitcnt vmcnt(8) lgkmcnt(0)\n" \
  "s_barrier\n" \
  "s_add_u32 m0, s19, 0\n" \
  "s_nop 0\n" \
  "global_load_lds_dwordx4 v24, s[8:9] offset:0\n" \
  "global_load_lds_dwordx4 v25, s[8:9] offset:1024\n" \
  "global_load_lds_dwordx4 v26, s[8:9] offset:2048\n" \
  "global_load_lds_dwordx4 v27, s[8:9] offset:3072\n" \
  "s_add_u32 s8, s8, 0x100000\n" \
  "s_addc_u32 s9, s9, 0\n" \
  "ds_read_b128 v[20:23], v30 offset:16\n" \
  "ds_read_b32 v31, v30 offset:32\n" \
  "v_mbcnt_lo_u32_b32 v16, -1, 0\n" \
  "v_mbcnt_hi_u32_b32 v16, -1, v16\n" \
  "v_lshlrev_b32 v15, 8, v16\n" \
  "s_waitcnt lgkmcnt(0)\n" \
  "v_add_f32 v20, v20, v21\n" \
  "v_add_f32 v22, v22, v23\n" \
  "v_add_f32 v20, v20, v22\n" \
  "v_mul_f32 v31, v20, v31\n" \
  "v_mul_f32 v31, 0x3f8147ae, v31\n" \
  "v_mov_b32 v12, s10\n" \
  "v_mov_b32 v13, 0xffff\n" \
  "v_bfi_b32 v13, v13, s11, v31\n" \
  "s_mov_b32 exec_hi, 0\n" \
  "s_mov_b32 exec_lo, s18\n" \
  "global_store_dwordx2 v15, v[12:13], s[16:17] sc1\n" \
  "s_mov_b64 exec, -1\n" \
  "v_lshlrev_b32 v16, 3, v16\n" \
  "s_waitcnt vmcnt(8) lgkmcnt(0)\n" \
  "s_barrier\n" \
  "s_add_u32 m0, s19, 4096\n" \
  "s_nop 0\n" \
  "global_load_lds_dwordx4 v24, s[8:9] offset:0\n" \
  "global_load_lds_dwordx4 v25, s[8:9] offset:1024\n" \
  "global_load_lds_dwordx4 v26, s[8:9] offset:2048\n" \
  "global_load_lds_dwordx4 v27, s[8:9] offset:3072\n" \
  "s_add_u32 s8, s8, 0x100000\n" \
  "s_addc_u32 s9, s9, 0\n" \
  "global_load_dwordx2 v[20:21], v16, s[12:13] sc1\n" \
  "s_waitcnt vmcnt(9) lgkmcnt(0)\n" \
  "s_barrier\n" \
  "s_add_u32 m0, s19, 8192\n" \
  "s_nop 0\n" \
  "global_load_lds_dwordx4 v24, s[8:9] offset:0\n" \
  "global_load_lds_dwordx4 v25, s[8:9] offset:1024\n" \
  "global_load_lds_dwordx4 v26, s[8:9] offset:2048\n" \
  "global_load_lds_dwordx4 v27, s[8:9] offset:3072\n" \
  "s_add_u32 s8, s8, 0x100000\n" \
  "s_addc_u32 s9, s9, 0\n" \
  "s_waitcnt vmcnt(9) lgkmcnt(0)\n" \
  "s_barrier\n" \
  "ds_read_b64_tr_b16 v[64:65], v28 offset:0\n" \
  "ds_read_b64_tr_b16 v[66:67], v28 offset:512\n" \
  "ds_read_b64_tr_b16 v[68:69], v28 offset:32768\n" \
  "ds_read_b64_tr_b16 v[70:71], v28 offset:33280\n" \
  "ds_read_b64_tr_b16 v[72:73], v17 offset:0\n" \
  "ds_read_b64_tr_b16 v[74:75], v17 offset:512\n" \
  "ds_read_b64_tr_b16 v[76:77], v17 offset:32768\n" \
  "ds_read_b64_tr_b16 v[78:79], v17 offset:33280\n" \
  "s_add_u32 m0, s19, 12288\n" \
  "s_nop 0\n" \
  "global_load_lds_dwordx4 v24, s[8:9] offset:0\n" \
  "global_load_lds_dwordx4 v25, s[8:9] offset:1024\n" \
  "global_load_lds_dwordx4 v26, s[8:9] offset:2048\n" \
  "global_load_lds_dwordx4 v27, s[8:9] offset:3072\n" \
  "s_add_u32 s8, s8, 0x100000\n" \
  "s_addc_u32 s9, s9, 0\n" \
  "s_waitcnt vmcnt(8)\n" \
  ".LCV_RD0_%=:\n" \
  "v_cmp_ne_u32 vcc, s10, v20\n" \
  "v_xor_b32 v31, s11, v21\n" \
  "v_cmp_ne_u32 s[22:23], 0, v31\n" \
  "s_or_b64 vcc, vcc, s[22:23]\n" \
  "s_cbranch_scc0 .LCV_RD1_%=\n" \
  "s_sleep 8\n" \
  "global_load_dwordx2 v[20:21], v16, s[12:13] sc1\n" \
  "s_sub_u32 s87, s87, 1\n" \
  "s_cbranch_scc1 .LCV_END_%=\n" \
  "s_waitcnt vmcnt(0)\n" \
  "s_branch .LCV_RD0_%=\n" \
  ".LCV_RD1_%=:\n" \
  "s_waitcnt vmcnt(8) lgkmcnt(0)\n" \
  "s_barrier\n" \
  "ds_write_b128 v3, v[96:99] offset:0\n" \
  "ds_write_b128 v3, v[100:103] offset:1024\n" \
  "ds_write_b128 v3, v[104:107] offset:2048\n" \
  "ds_write_b128 v3, v[108:111] offset:3072\n" \
  "ds_read_b64_tr_b16 v[80:81], v28 offset:4096\n" \
  "ds_read_b64_tr_b16 v[82:83], v28 offset:4608\n" \
  "ds_read_b64_tr_b16 v[84:85], v28 offset:36864\n" \
  "ds_read_b64_tr_b16 v[86:87], v28 offset:37376\n" \
  "ds_read_b64_tr_b16 v[88:89], v17 offset:4096\n" \
  "ds_read_b64_tr_b16 v[90:91], v17 offset:4608\n" \
  "ds_read_b64_tr_b16 v[92:93], v17 offset:36864\n" \
  "ds_read_b64_tr_b16 v[94:95], v17 offset:37376\n" \
  "v_and_b32 v16, 0xff, v16\n" \
  "global_load_dwordx2 v[22:23], v16, s[14:15] sc1\n" \
  "s_mov_b64 exec, 15\n" \
  "buffer_load_dword v13, v2, s[44:47], s68 offen sc1\n" \
  "s_mov_b64 exec, -1\n" \
  "s_waitcnt vmcnt(6) lgkmcnt(0)\n" \
  "s_barrier\n" \
  "ds_write_b128 v3, v[112:115] offset:4096\n" \
  "ds_write_b128 v3, v[116:119] offset:5120\n" \
  "ds_write_b128 v3, v[120:123] offset:6144\n" \
  "ds_write_b128 v3, v[124:127] offset:7168\n" \
  "ds_read_b64_tr_b16 v[96:97], v28 offset:8192\n" \
  "ds_read_b64_tr_b16 v[98:99], v28 offset:8704\n" \
  "ds_read_b64_tr_b16 v[100:101], v28 offset:40960\n" \
  "ds_read_b64_tr_b16 v[102:103], v28 offset:41472\n" \
  "ds_read_b64_tr_b16 v[104:105], v17 offset:8192\n" \
  "ds_read_b64_tr_b16 v[106:107], v17 offset:8704\n" \
  "ds_read_b64_tr_b16 v[108:109], v17 offset:40960\n" \
  "ds_read_b64_tr_b16 v[110:111], v17 offset:41472\n" \
  "s_waitcnt vmcnt(0)\n" \
  "v_cmp_eq_u32 vcc, 0, v13\n" \
  "s_and_b32 s90, vcc_lo, 15\n" \
  "s_cbranch_scc1 .LCV_GL0_%=\n" \
  "s_mov_b32 m0, s99\n" \
  "s_add_u32 s88, s99, 8192\n" \
  "global_load_lds_dwordx4 v10, s[62:63] sc1\n" \
  "global_load_lds_dwordx4 v10, s[62:63] offset:1024 sc1\n" \
  "s_mov_b32 m0, s88\n" \
  "s_nop 0\n" \
  "global_load_lds_dwordx4 v10, s[64:65] sc1\n" \
  "global_load_lds_dwordx4 v10, s[64:65] offset:1024 sc1\n" \
  ".LCV_GL0_%=:\n" \
  "v_cmp_ne_u32 vcc, s10, v22\n" \
  "v_xor_b32 v31, s11, v23\n" \
  "v_and_b32 v31, 0xffff, v31\n" \
  "v_cmp_ne_u32 s[22:23], 0, v31\n" \
  "s_or_b64 vcc, vcc, s[22:23]\n" \
  "s_cbranch_scc0 .LCV_GL1_%=\n" \
  "s_sleep 12\n" \
  "global_load_dwordx2 v[22:23], v16, s[14:15] sc1\n" \
  "s_sub_u32 s87, s87, 1\n" \
  "s_cbranch_scc1 .LCV_END_%=\n" \
  "s_waitcnt vmcnt(0)\n" \
  "s_branch .LCV_GL0_%=\n" \
  ".LCV_GL1_%=:\n" \
  "v_and_b32 v31, 0xffff0000, v23\n" \
  "s_nop 1\n" \
  "v_max_u32_dpp v31, v31, v31 row_ror:1 row_mask:0xf bank_mask:0xf\n" \
  "s_nop 1\n" \
  "v_max_u32_dpp v31, v31, v31 row_ror:2 row_mask:0xf bank_mask:0xf\n" \
  "s_nop 1\n" \
  "v_max_u32_dpp v31, v31, v31 row_ror:4 row_mask:0xf bank_mask:0xf\n" \
  "s_nop 1\n" \
  "v_max_u32_dpp v31, v31, v31 row_ror:8 row_mask:0xf bank_mask:0xf\n" \
  "v_mov_b32 v12, v31\n" \
  "s_nop 1\n" \
  "v_permlane16_swap_b32 v31, v12\n" \
  "v_max_u32 v31, v31, v12\n" \
  "v_mov_b32 v12, v31\n" \
  "s_nop 1\n" \
  "v_permlane32_swap_b32 v31, v12\n" \
  "v_max_u32 v31, v31, v12\n" \
  "s_mov_b64 exec, 1\n" \
  "ds_write_b32 v30, v31\n" \
  "s_mov_b64 exec, -1\n" \
  "s_waitcnt vmcnt(4) lgkmcnt(0)\n" \
  "s_barrier\n" \
  "ds_write_b128 v3, v[32:35] offset:8192\n" \
  "ds_write_b128 v3, v[36:39] offset:9216\n" \
  "ds_write_b128 v3, v[40:43] offset:10240\n" \
  "ds_write_b128 v3, v[44:47] offset:11264\n" \
  "ds_read_b64_tr_b16 v[112:113], v28 offset:12288\n" \
  "ds_read_b64_tr_b16 v[114:115], v28 offset:12800\n" \
  "ds_read_b64_tr_b16 v[116:117], v28 offset:45056\n" \
  "ds_read_b64_tr_b16 v[118:119], v28 offset:45568\n" \
  "ds_read_b64_tr_b16 v[120:121], v17 offset:12288\n" \
  "ds_read_b64_tr_b16 v[122:123], v17 offset:12800\n" \
  "ds_read_b64_tr_b16 v[124:125], v17 offset:45056\n" \
  "ds_read_b64_tr_b16 v[126:127], v17 offset:45568\n" \
  "ds_read_b32 v31, v30 offset:0\n" \
  "s_waitcnt lgkmcnt(0)\n" \
  "s_barrier\n" \
  "ds_write_b128 v3, v[48:51] offset:12288\n" \
  "ds_write_b128 v3, v[52:55] offset:13312\n" \
  "ds_write_b128 v3, v[56:59] offset:14336\n" \
  "ds_write_b128 v3, v[60:63] offset:15360\n" \
  "v_mul_f32 v31, 0x3f8e353f, v31\n" \
  "v_mul_f32 v30, 0x44870000, v31\n" \
  "v_mov_b64 v[12:13], 0\n" \
  "v_mov_b64 v[14:15], 0\n" \
  "v_mov_b64 v[16:17], 0\n" \
  "v_mov_b64 v[18:19], 0\n" \
  "v_mov_b64 v[20:21], 0\n" \
  "v_mov_b64 v[22:23], 0\n" \
  "v_readfirstlane_b32 s72, v31\n" \
  "v_readfirstlane_b32 s86, v30\n" \
  "s_lshr_b32 s86, s86, 23\n" \
  "s_addk_i32 s86, 0xff83\n" \
  "s_ashr_i32 s86, s86, 1\n" \
  "s_max_i32 s86, s86, -20\n" \
  "s_min_i32 s86, s86, 100\n" \
  "s_mov_b32 s82, s86\n" \
  "s_lshr_b32 s83, s72, 23\n" \
  "s_sub_i32 s83, s83, 243\n" \
  "s_waitcnt lgkmcnt(0)\n" \
  "s_add_u32 s88, s74, 157760\n" \
  "v_mov_b32 v59, s88\n" \
  "s_cmp_lg_u32 s61, 0\n" \
  "s_movk_i32 s76, -8000\n" \
  "s_cmovk_i32 s76, 9152\n" \
  "s_movk_i32 s77, 90\n" \
  "s_cmovk_i32 s77, 90\n" \
  "s_cselect_b32 s72, -1, 55\n" \
  "s_mov_b32 s87, 0x40000\n" \
  "s_cmp_eq_u32 s90, 0\n" \
  "s_cbranch_scc1 .LCV_W1_%=\n" \
  "s_branch .LCV_W2_%=\n" \
  ".LCV_W0_%=:\n" \
  "s_sleep 2\n" \
  "s_cmp_eq_u32 s61, 0\n" \
  "s_cbranch_scc1 .LCV_W2_%=\n" \
  "s_sleep 32\n" \
  ".LCV_W2_%=:\n" \
  "s_mov_b64 exec, 15\n" \
  "buffer_load_dword v58, v2, s[44:47], s68 offen sc1\n" \
  "s_mov_b64 exec, -1\n" \
  "s_sub_u32 s87, s87, 1\n" \
  "s_cbranch_scc1 .LCV_END_%=\n" \
  "s_waitcnt vmcnt(0)\n" \
  "v_cmp_eq_u32 vcc, 0, v58\n" \
  "s_and_b32 vcc_lo, vcc_lo, 15\n" \
  "s_cbranch_scc1 .LCV_W0_%=\n" \
  "s_mov_b32 m0, s99\n" \
  "s_add_u32 s88, s99, 8192\n" \
  "global_load_lds_dwordx4 v10, s[62:63] sc1\n" \
  "global_load_lds_dwordx4 v10, s[62:63] offset:1024 sc1\n" \
  "s_mov_b32 m0, s88\n" \
  "s_nop 0\n" \
  "global_load_lds_dwordx4 v10, s[64:65] sc1\n" \
  "global_load_lds_dwordx4 v10, s[64:65] offset:1024 sc1\n" \
  ".LCV_W1_%=:\n" \
  "s_add_u32 s62, s62, 0x80000\n" \
  "s_addc_u32 s63, s63, 0\n" \
  "s_add_u32 s64, s64, 0x40000\n" \
  "s_addc_u32 s65, s65, 0\n" \
  "s_add_u32 s68, s68, 0x1000\n" \
  "s_cmp_eq_u32 s61, 0\n" \
  "s_cbranch_scc1 .LCV_W4_%=\n" \
  "s_add_u32 s38, s67, 4992\n" \
  "v_mov_b32 v58, s38\n" \
  "buffer_load_dword v58, v58, s[0:3], 0 offen sc1\n" \
  "s_waitcnt vmcnt(0)\n" \
  "v_readfirstlane_b32 vcc_lo, v58\n" \
  "s_movk_i32 s87, 200\n" \
  "s_cmp_eq_u32 vcc_lo, 0\n" \
  "s_cbranch_scc1 .LCV_W4_%=\n" \
  "s_add_u32 vcc_lo, vcc_lo, 590\n" \
  ".LCV_W3_%=:\n" \
  "s_sleep 2\n" \
  "s_memrealtime s[38:39]\n" \
  "s_sub_u32 s87, s87, 1\n" \
  "s_cbranch_scc1 .LCV_W4_%=\n" \
  "s_waitcnt lgkmcnt(0)\n" \
  "s_sub_u32 s38, s38, vcc_lo\n" \
  "s_cmp_lt_i32 s38, 0\n" \
  "s_cbranch_scc1 .LCV_W3_%=\n" \
  ".LCV_W4_%=:\n" \
  "s_waitcnt vmcnt(0)\n" \
  ".p2align 6\n" \
  ".LCV_TOP_%=:\n" \
  "s_waitcnt lgkmcnt(0)\n" \
  "s_barrier\n" \
  "s_setprio 3\n" \
  "ds_read_b128 v[24:27], v1 offset:0\n" \
  "ds_read_b128 v[28:31], v1 offset:1024\n" \
  "ds_read_b128 v[32:35], v1 offset:2048\n" \
  "ds_read_b128 v[36:39], v1 offset:3072\n" \
  "ds_read_b128 v[40:43], v1 offset:4096\n" \
  "ds_read_b128 v[44:47], v1 offset:5120\n" \
  "ds_read_b128 v[48:51], v1 offset:6144\n" \
  "ds_read_b128 v[52:55], v1 offset:7168\n" \
  "s_memrealtime s[38:39]\n" \
  "s_waitcnt lgkmcnt(7)\n" \
  "v_smfmac_f32_16x16x64_bf16 v[12:15], v[24:27], v[64:71], v0\n" \
  "s_waitcnt lgkmcnt(6)\n" \
  "v_smfmac_f32_16x16x64_bf16 v[12:15], v[28:31], v[72:79], v0\n" \
  "s_waitcnt lgkmcnt(5)\n" \
  "v_smfmac_f32_16x16x64_bf16 v[12:15], v[32:35], v[80:87], v0\n" \
  "s_waitcnt lgkmcnt(4)\n" \
  "v_smfmac_f32_16x16x64_bf16 v[12:15], v[36:39], v[88:95], v0\n" \
  "s_waitcnt lgkmcnt(3)\n" \
  "v_smfmac_f32_16x16x64_bf16 v[12:15], v[40:43], v[96:103], v0\n" \
  "s_waitcnt lgkmcnt(2)\n" \
  "v_smfmac_f32_16x16x64_bf16 v[12:15], v[44:47], v[104:111], v0\n" \
  "s_waitcnt lgkmcnt(1)\n" \
  "v_smfmac_f32_16x16x64_bf16 v[12:15], v[48:51], v[112:119], v0\n" \
  "s_waitcnt lgkmcnt(0)\n" \
  "v_smfmac_f32_16x16x64_bf16 v[12:15], v[52:55], v[120:127], v0\n" \
  "ds_read_b32 v11, v1 offset:26688\n" \
  "s_mov_b32 s61, s38\n" \
  "s_add_u32 s38, s38, s79\n" \
  "s_add_u32 s38, s38, s96\n" \
  "s_mov_b32 s96, 0\n" \
  "v_or_b32 v57, 1, s38\n" \
  "v_add_u32 v56, s67, v10\n" \
  "s_add_u32 s39, s67, s76\n" \
  "s_add_u32 s38, s38, s77\n" \
  "s_bitset1_b32 s38, 0\n" \
  "v_writelane_b32 v56, s39, 1\n" \
  "v_writelane_b32 v57, s38, 1\n" \
  "s_sub_u32 s39, s71, s72\n" \
  "s_cmp_lt_u32 s39, 9\n" \
  "s_cselect_b64 exec, 19, 3\n" \
  "buffer_store_dword v57, v56, s[0:3], 0 offen sc1\n" \
  "s_mov_b64 exec, -1\n" \
  "v_permlane32_swap_b32 v12, v14\n" \
  "v_permlane32_swap_b32 v13, v15\n" \
  "v_pk_add_f32 v[12:13], v[12:13], v[14:15]\n" \
  "s_nop 0\n" \
  "v_cvt_pk_bf16_f32 v12, v12, v13\n" \
  "ds_write_b32 v4, v12\n" \
  "s_setprio 2\n" \
  "v_mov_b64 v[12:13], s[92:93]\n" \
  "v_mov_b64 v[14:15], s[92:93]\n" \
  "s_waitcnt lgkmcnt(1)\n" \
  "v_readfirstlane_b32 s86, v11\n" \
  "s_lshr_b32 s86, s86, 23\n" \
  "s_add_i32 s86, s86, s83\n" \
  "s_ashr_i32 s86, s86, 1\n" \
  "s_max_i32 s86, s86, s82\n" \
  "s_min_i32 s86, s86, 100\n" \
  "s_lshl_b32 s86, s86, 23\n" \
  "s_sub_i32 s84, 0xc8800000, s86\n" \
  "s_cmp_eq_u32 s73, 5\n" \
  "v_smfmac_f32_16x16x64_bf16 v[16:19], v[24:27], v[128:135], v0\n" \
  "v_smfmac_f32_16x16x64_bf16 v[16:19], v[28:31], v[136:143], v0\n" \
  "v_smfmac_f32_16x16x64_bf16 v[16:19], v[32:35], v[144:151], v0\n" \
  "v_smfmac_f32_16x16x64_bf16 v[16:19], v[36:39], v[152:159], v0\n" \
  "v_smfmac_f32_16x16x64_bf16 v[16:19], v[40:43], v[160:167], v0\n" \
  "v_smfmac_f32_16x16x64_bf16 v[16:19], v[44:47], v[168:175], v0\n" \
  "s_waitcnt lgkmcnt(0)\n" \
  "s_barrier\n" \
  "ds_read_b64_tr_b16 v[60:61], v6 offset:0\n" \
  "ds_read_b64_tr_b16 v[62:63], v9 offset:0\n" \
  "v_smfmac_f32_16x16x64_bf16 v[16:19], v[48:51], v[176:183], v0\n" \
  "v_smfmac_f32_16x16x64_bf16 v[16:19], v[52:55], v[184:191], v0\n" \
  "v_smfmac_f32_16x16x64_bf16 v[20:23], v[24:27], v[192:199], v0\n" \
  "v_smfmac_f32_16x16x64_bf16 v[20:23], v[28:31], v[200:207], v0\n" \
  "v_smfmac_f32_16x16x64_bf16 v[20:23], v[32:35], v[208:215], v0\n" \
  "v_smfmac_f32_16x16x64_bf16 v[20:23], v[36:39], v[216:223], v0\n" \
  "v_smfmac_f32_16x16x64_bf16 v[20:23], v[40:43], v[224:231], v0\n" \
  "v_smfmac_f32_16x16x64_bf16 v[20:23], v[44:47], v[232:239], v0\n" \
  "v_smfmac_f32_16x16x64_bf16 v[20:23], v[48:51], v[240:247], v0\n" \
  "v_smfmac_f32_16x16x64_bf16 v[20:23], v[52:55], v[248:255], v0\n" \
  "s_cmp_eq_u32 s71, 0\n" \
  "v_permlane32_swap_b32 v16, v18\n" \
  "v_permlane32_swap_b32 v17, v19\n" \
  "v_add_f32 v16, v16, v18\n" \
  "v_add_f32 v17, v17, v19\n" \
  "v_cvt_pk_bf16_f32 v16, v16, v17\n" \
  "s_waitcnt lgkmcnt(0)\n" \
  "v_max_u32 v60, 1, v60\n" \
  "v_max_u32 v61, 1, v61\n" \
  "v_max_u32 v62, 1, v62\n" \
  "v_max_u32 v63, 1, v63\n" \
  "buffer_store_dwordx4 v[60:63], v7, s[48:51], s69 offen sc1\n" \
  "v_permlane32_swap_b32 v20, v22\n" \
  "v_permlane32_swap_b32 v21, v23\n" \
  "v_pk_add_f32 v[20:21], v[20:21], v[22:23]\n" \
  "v_max_u32 v18, 1, v16\n" \
  "v_cvt_pk_bf16_f32 v17, v20, v21\n" \
  "ds_write_b64 v5, v[16:17]\n" \
  "v_max_u32 v19, 1, v17\n" \
  "s_nop 1\n" \
  "v_permlane32_swap_b32 v18, v19\n" \
  "buffer_store_dwordx2 v[18:19], v8, s[52:55], s70 offen sc1\n" \
  "ds_read_b128 v[24:27], v3 offset:0\n" \
  "ds_read_b128 v[28:31], v3 offset:1024\n" \
  "ds_read_b128 v[32:35], v3 offset:2048\n" \
  "ds_read_b128 v[36:39], v3 offset:3072\n" \
  "ds_read_b128 v[40:43], v3 offset:8192\n" \
  "ds_read_b128 v[44:47], v3 offset:9216\n" \
  "ds_read_b128 v[48:51], v3 offset:10240\n" \
  "ds_read_b128 v[52:55], v3 offset:11264\n" \
  "s_cbranch_scc1 .LCV_LAST_%=\n" \
  "s_cmp_eq_u32 s73, 5\n" \
  "s_cbranch_scc0 .LCV_NMK_%=\n" \
  "s_mov_b32 m0, s78\n" \
  "s_nop 0\n" \
  "buffer_load_dword v2, s[0:3], s66 offen sc1 lds\n" \
  ".LCV_NMK_%=:\n" \
  "s_setprio 0\n" \
  "s_waitcnt lgkmcnt(8)\n" \
  "s_barrier\n" \
  "s_add_u32 s67, s67, 1024\n" \
  "v_mov_b32 v11, 0\n" \
  "ds_write_b32 v59, v11\n" \
  "ds_read_b128 v[56:59], v3 offset:4096\n" \
  "ds_read_b128 v[60:63], v3 offset:5120\n" \
  "v_mov_b64 v[20:21], s[92:93]\n" \
  "v_mov_b64 v[22:23], s[92:93]\n" \
  "v_mov_b64 v[16:17], s[92:93]\n" \
  "v_mov_b64 v[18:19], s[92:93]\n" \
  "s_mov_b32 m0, s94\n" \
  "s_mov_b64 exec, 15\n" \
  "buffer_load_dword v2, s[44:47], s68 offen sc1 lds\n" \
  "s_mov_b64 exec, -1\n" \
  "s_setprio 2\n" \
  "s_waitcnt lgkmcnt(0)\n" \
  "s_barrier\n" \
  "ds_read_b128 v[4:7], v1 offset:8192\n" \
  "ds_read_b128 v[8:11], v1 offset:9216\n" \
  "s_waitcnt lgkmcnt(1)\n" \
  "v_smfmac_f32_16x16x64_bf16 v[12:15], v[4:7], v[24:31], v0\n" \
  "ds_read_b128 v[24:27], v3 offset:6144\n" \
  "ds_read_b128 v[28:31], v3 offset:7168\n" \
  "v_smfmac_f32_16x16x64_bf16 v[16:19], v[4:7], v[32:39], v0\n" \
  "ds_read_b128 v[32:35], v3 offset:12288\n" \
  "ds_read_b128 v[36:39], v3 offset:13312\n" \
  "s_waitcnt lgkmcnt(4)\n" \
  "v_smfmac_f32_16x16x64_bf16 v[12:15], v[8:11], v[40:47], v0\n" \
  "v_smfmac_f32_16x16x64_bf16 v[16:19], v[8:11], v[48:55], v0\n" \
  "v_smfmac_f32_16x16x64_bf16 v[20:23], v[4:7], v[56:63], v0\n" \
  "ds_read_b128 v[56:59], v3 offset:14336\n" \
  "ds_read_b128 v[60:63], v3 offset:15360\n" \
  "v_mov_b64 v[40:41], s[92:93]\n" \
  "v_mov_b64 v[42:43], s[92:93]\n" \
  "s_nop 2\n" \
  "v_permlane32_swap_b32 v12, v17\n" \
  "v_permlane32_swap_b32 v13, v16\n" \
  "v_permlane32_swap_b32 v14, v19\n" \
  "v_permlane32_swap_b32 v15, v18\n" \
  "v_pk_add_f32 v[12:13], v[12:13], v[16:17] op_sel:[0,1] op_sel_hi:[1,0]\n" \
  "v_pk_add_f32 v[14:15], v[14:15], v[18:19] op_sel:[0,1] op_sel_hi:[1,0]\n" \
  "buffer_atomic_add_x2 v[12:13], v2, s[40:43], s56 offen\n" \
  "buffer_atomic_add_x2 v[14:15], v2, s[40:43], s57 offen\n" \
  "s_waitcnt lgkmcnt(2)\n" \
  "v_smfmac_f32_16x16x64_bf16 v[20:23], v[8:11], v[32:39], v0\n" \
  "v_smfmac_f32_16x16x64_bf16 v[40:43], v[4:7], v[24:31], v0\n" \
  "s_waitcnt lgkmcnt(0)\n" \
  "v_smfmac_f32_16x16x64_bf16 v[40:43], v[8:11], v[56:63], v0\n" \
  "s_nop 7\n" \
  "v_permlane32_swap_b32 v20, v41\n" \
  "v_permlane32_swap_b32 v21, v40\n" \
  "v_permlane32_swap_b32 v22, v43\n" \
  "v_permlane32_swap_b32 v23, v42\n" \
  "v_pk_add_f32 v[20:21], v[20:21], v[40:41] op_sel:[0,1] op_sel_hi:[1,0]\n" \
  "v_pk_add_f32 v[22:23], v[22:23], v[42:43] op_sel:[0,1] op_sel_hi:[1,0]\n" \
  "buffer_atomic_add_x2 v[20:21], v2, s[40:43], s58 offen\n" \
  "buffer_atomic_add_x2 v[22:23], v2, s[40:43], s59 offen\n" \
  "s_setprio 0\n" \
  "s_memrealtime s[38:39]\n" \
  "v_mbcnt_lo_u32_b32 v48, -1, 0\n" \
  "v_mbcnt_hi_u32_b32 v48, -1, v48\n" \
  "s_add_u32 s88, s74, 157760\n" \
  "v_mov_b32 v59, s88\n" \
  "v_lshl_add_u32 v52, v48, 2, v59\n" \
  "ds_read_b32 v52, v52 offset:192\n" \
  "v_lshrrev_b32 v49, 3, v48\n" \
  "v_and_b32 v50, 7, v48\n" \
  "v_lshlrev_b32 v50, 4, v50\n" \
  "v_lshl_or_b32 v44, v49, 8, v50\n" \
  "s_mov_b32 s87, 0x40000\n" \
  "v_lshlrev_b32 v10, 4, v48\n" \
  "v_lshl_add_u32 v58, v48, 2, s94\n" \
  "s_waitcnt vmcnt(4)\n" \
  "ds_read_b32 v58, v58\n" \
  "s_waitcnt lgkmcnt(0)\n" \
  "v_cmp_eq_u32 vcc, 0, v58\n" \
  "s_and_b32 vcc_lo, vcc_lo, 15\n" \
  "s_cbranch_scc0 .LCV_GOT_%=\n" \
  "s_branch .LCV_ASK_%=\n" \
  ".LCV_LATE_%=:\n" \
  "v_cmp_eq_u32 vcc, 0, v58\n" \
  "s_and_b32 vcc_lo, vcc_lo, 15\n" \
  "s_cbranch_scc0 .LCV_GOT_%=\n" \
  "s_sleep 12\n" \
  ".LCV_ASK_%=:\n" \
  "s_mov_b64 exec, 15\n" \
  "buffer_load_dword v58, v2, s[44:47], s68 offen sc1\n" \
  "s_mov_b64 exec, -1\n" \
  "s_sub_u32 s87, s87, 1\n" \
  "s_cbranch_scc1 .LCV_END_%=\n" \
  "s_waitcnt vmcnt(0)\n" \
  "s_branch .LCV_LATE_%=\n" \
  ".LCV_GOT_%=:\n" \
  "s_mov_b32 m0, s99\n" \
  "s_add_u32 s88, s99, 8192\n" \
  "global_load_lds_dwordx4 v10, s[62:63] sc1\n" \
  "global_load_lds_dwordx4 v10, s[62:63] offset:1024 sc1\n" \
  "s_mov_b32 m0, s88\n" \
  "s_nop 0\n" \
  "global_load_lds_dwordx4 v10, s[64:65] sc1\n" \
  "global_load_lds_dwordx4 v10, s[64:65] offset:1024 sc1\n" \
  ".LCV_HAVE_%=:\n" \
  "s_waitcnt lgkmcnt(0)\n" \
  "s_sub_u32 s61, s38, s61\n" \
  "s_add_u32 s79, s79, 2\n" \
  "s_min_u32 s79, s79, s61\n" \
  "s_sub_u32 s39, s38, 150\n" \
  "v_subrev_u32 v52, s39, v52\n" \
  "v_cmp_lt_u32 vcc, 0x1000, v52\n" \
  "s_cmp_eq_u64 vcc, 0\n" \
  "s_cbranch_scc1 .LCV_OKM_%=\n" \
  ".LCV_AGN_%=:\n" \
  "buffer_load_dword v52, v2, s[0:3], s66 offen sc1\n" \
  "s_sub_u32 s87, s87, 1\n" \
  "s_cbranch_scc1 .LCV_END_%=\n" \
  "s_waitcnt vmcnt(0)\n" \
  "v_cmp_eq_u32 vcc, 0, v52\n" \
  "v_subrev_u32 v52, s39, v52\n" \
  "s_cmp_eq_u64 vcc, 0\n" \
  "s_cbranch_scc1 .LCV_OKM_%=\n" \
  "s_sleep 4\n" \
  "s_branch .LCV_AGN_%=\n" \
  ".LCV_OKM_%=:\n" \
  "s_nop 1\n" \
  "v_max_i32_dpp v52, v52, v52 row_ror:1 row_mask:0xf bank_mask:0xf\n" \
  "s_nop 1\n" \
  "v_max_i32_dpp v52, v52, v52 row_ror:2 row_mask:0xf bank_mask:0xf\n" \
  "s_nop 1\n" \
  "v_max_i32_dpp v52, v52, v52 row_ror:4 row_mask:0xf bank_mask:0xf\n" \
  "s_nop 1\n" \
  "v_max_i32_dpp v52, v52, v52 row_ror:8 row_mask:0xf bank_mask:0xf\n" \
  "s_movk_i32 vcc_lo, 150\n" \
  "v_readlane_b32 s88, v52, 0\n" \
  "s_max_i32 vcc_lo, vcc_lo, s88\n" \
  "v_readlane_b32 s88, v52, 16\n" \
  "s_max_i32 vcc_lo, vcc_lo, s88\n" \
  "v_readlane_b32 s88, v52, 32\n" \
  "s_max_i32 vcc_lo, vcc_lo, s88\n" \
  "v_readlane_b32 s88, v52, 48\n" \
  "s_max_i32 vcc_lo, vcc_lo, s88\n" \
  "s_add_u32 s66, s66, 1024\n" \
  "s_add_u32 vcc_lo, vcc_lo, s39\n" \
  "s_add_u32 vcc_lo, vcc_lo, 23\n" \
  ".LCV_TW_%=:\n" \
  "s_memrealtime s[38:39]\n" \
  "s_waitcnt lgkmcnt(0)\n" \
  "s_sub_u32 s38, s38, vcc_lo\n" \
  "s_cmp_lt_i32 s38, 0\n" \
  "s_cbranch_scc1 .LCV_TW_%=\n" \
  "buffer_load_dwordx4 v[24:27], v44, s[40:43], s60 offen sc1\n" \
  "buffer_load_dwordx4 v[28:31], v44, s[40:43], s60 offen offset:2048 sc1\n" \
  "s_add_u32 s62, s62, 0x80000\n" \
  "s_addc_u32 s63, s63, 0\n" \
  "s_add_u32 s64, s64, 0x40000\n" \
  "s_addc_u32 s65, s65, 0\n" \
  "s_add_u32 s68, s68, 0x1000\n" \
  "s_add_u32 s69, s69, 0x10000\n" \
  "s_add_u32 s70, s70, 0x10000\n" \
  "s_sub_u32 s88, s73, 4\n" \
  "v_lshrrev_b32 v49, 2, v48\n" \
  "v_lshrrev_b32 v50, 4, v48\n" \
  "v_bitop3_b32 v50, v49, v50, 3 bitop3:0x28\n" \
  "v_lshl_add_u32 v4, v50, 3, s97\n" \
  "v_and_b32 v50, 3, v48\n" \
  "v_lshl_add_u32 v4, v50, 5, v4\n" \
  "v_and_b32 v49, 1, v49\n" \
  "v_lshl_add_u32 v4, v49, 8, v4\n" \
  "v_and_b32 v49, 8, v48\n" \
  "v_lshl_add_u32 v4, v49, 4, v4\n" \
  "v_and_b32 v51, 15, v48\n" \
  "v_bfe_u32 v49, v48, 4, 1\n" \
  "v_lshrrev_b32 v50, 5, v48\n" \
  "s_lshl_b32 s89, s88, 10\n" \
  "s_add_u32 s89, s89, s74\n" \
  "s_add_u32 s89, s89, 158720\n" \
  "v_lshrrev_b32 v9, 3, v51\n" \
  "v_mul_u32_u24 v6, 41, v9\n" \
  "v_xor_b32 v6, v6, v51\n" \
  "v_lshl_add_u32 v6, v6, 3, s89\n" \
  "v_lshl_add_u32 v6, v49, 6, v6\n" \
  "v_lshl_add_u32 v6, v50, 9, v6\n" \
  "v_xor_b32 v9, 0x90, v6\n" \
  "v_lshlrev_b32 v8, 4, v49\n" \
  "v_lshl_add_u32 v8, v51, 5, v8\n" \
  "v_lshl_add_u32 v8, v50, 3, v8\n" \
  "s_lshl_b32 s89, s88, 8\n" \
  "s_add_u32 s89, s89, s74\n" \
  "s_add_u32 s89, s89, 139264\n" \
  "v_and_b32 v49, 31, v48\n" \
  "v_lshl_add_u32 v5, v49, 3, s89\n" \
  "v_lshl_add_u32 v5, v50, 10, v5\n" \
  "v_lshrrev_b32 v49, 2, v51\n" \
  "v_and_b32 v50, 5, v51\n" \
  "v_and_or_b32 v49, v49, 2, v50\n" \
  "v_and_b32 v50, 48, v48\n" \
  "v_lshlrev_b32 v7, 3, v50\n" \
  "v_lshl_add_u32 v7, v49, 4, v7\n" \
  "v_bitop3_b32 v49, v51, 2, 2 bitop3:0x0c\n" \
  "v_lshl_add_u32 v7, v49, 29, v7\n" \
  "v_mov_b64 v[12:13], 0\n" \
  "v_mov_b64 v[14:15], 0\n" \
  "v_mov_b64 v[16:17], 0\n" \
  "v_mov_b64 v[18:19], 0\n" \
  "v_mov_b64 v[20:21], 0\n" \
  "v_mov_b64 v[22:23], 0\n" \
  "v_and_b32 v50, 3, v48\n" \
  "v_lshl_add_u32 v40, v50, 2, s98\n" \
  "v_lshrrev_b32 v50, 4, v48\n" \
  "v_mad_u32_u24 v40, v50, s95, v40\n" \
  "v_bfe_u32 v49, v48, 3, 1\n" \
  "v_lshl_add_u32 v40, v49, 6, v40\n" \
  "v_bfe_u32 v49, v48, 2, 1\n" \
  "v_lshl_add_u32 v40, v49, 7, v40\n" \
  "v_xor_b32 v41, 16, v40\n" \
  "v_xor_b32 v42, 32, v40\n" \
  "v_xor_b32 v43, 48, v40\n" \
  "s_add_i32 s80, s86, 0x35000000\n" \
  "v_mov_b32 v11, 0\n" \
  "s_mov_b32 s89, s60\n" \
  "s_bitcmp1_b32 s60, 7\n" \
  "s_cselect_b32 s88, 0x10000, 0\n" \
  "s_bitset1_b32 s88, 7\n" \
  "s_xor_b32 s56, s56, s88\n" \
  "s_xor_b32 s57, s57, s88\n" \
  "s_xor_b32 s58, s58, s88\n" \
  "s_xor_b32 s59, s59, s88\n" \
  "s_xor_b32 s60, s60, s88\n" \
  "s_sub_u32 s71, s71, 1\n" \
  ".LCV_SEE_%=:\n" \
  "s_waitcnt vmcnt(1)\n" \
  "v_cvt_f32_i32 v24, v24\n" \
  "v_cvt_f32_i32 v25, v25\n" \
  "v_cvt_f32_i32 v26, v26\n" \
  "v_cvt_f32_i32 v27, v27\n" \
  "v_max_f32 v45, |v24|, |v26|\n" \
  "v_pk_mul_f32 v[24:25], v[24:25], s[80:81] op_sel_hi:[1,0]\n" \
  "v_pk_mul_f32 v[26:27], v[26:27], s[80:81] op_sel_hi:[1,0]\n" \
  "s_nop 0\n" \
  "v_cvt_pk_bf16_f32 v32, v24, v26\n" \
  "v_cvt_pk_bf16_f32 v33, v25, v27\n" \
  "ds_write_b32 v40, v32\n" \
  "ds_write_b32 v41, v33\n" \
  "s_waitcnt vmcnt(0)\n" \
  "v_cvt_f32_i32 v28, v28\n" \
  "v_cvt_f32_i32 v30, v30\n" \
  "v_cvt_f32_i32 v29, v29\n" \
  "v_max3_f32 v45, v45, |v28|, |v30|\n" \
  "v_cvt_f32_i32 v31, v31\n" \
  "v_cmp_lt_f32 vcc, s91, v45\n" \
  "v_pk_mul_f32 v[28:29], v[28:29], s[80:81] op_sel_hi:[1,0]\n" \
  "v_pk_mul_f32 v[30:31], v[30:31], s[80:81] op_sel_hi:[1,0]\n" \
  "s_cmp_eq_u64 vcc, 0\n" \
  "s_cbranch_scc0 .LCV_POLL_%=\n" \
  "v_cvt_pk_bf16_f32 v36, v28, v30\n" \
  "v_cvt_pk_bf16_f32 v37, v29, v31\n" \
  "ds_write_b32 v42, v36\n" \
  "ds_write_b32 v43, v37\n" \
  "v_dot2c_f32_bf16 v11, v32, v32\n" \
  "v_dot2c_f32_bf16 v11, v33, v33\n" \
  "v_dot2c_f32_bf16 v11, v36, v36\n" \
  "v_dot2c_f32_bf16 v11, v37, v37\n" \
  "s_nop 1\n" \
  "s_nop 0\n" \
  "v_cmp_lt_f32 vcc, 4.0, v11\n" \
  "s_cbranch_vccz .LCV_TOP_%=\n" \
  "s_mov_b64 exec, vcc\n" \
  "ds_max_u32 v59, v11\n" \
  "s_mov_b64 exec, -1\n" \
  "s_branch .LCV_TOP_%=\n" \
  ".LCV_POLL_%=:\n" \
  "buffer_load_dwordx4 v[24:27], v44, s[40:43], s89 offen sc1\n" \
  "buffer_load_dwordx4 v[28:31], v44, s[40:43], s89 offen offset:2048 sc1\n" \
  "s_sub_u32 s87, s87, 1\n" \
  "s_cbranch_scc1 .LCV_END_%=\n" \
  "s_branch .LCV_SEE_%=\n" \
  ".LCV_LAST_%=:\n" \
  "s_waitcnt vmcnt(0)\n" \
  ".LCV_END_%=:\n" \
  "s_endpgm\n"
__device__ __forceinline__ void start_kgen(const MegaArgs& A, char* smem, const int kg) {
  const int w = A.wv, l = lane_id(), tid = 64 * w + l, c = l & 15, g = l >> 4;
  KGen G;
  unsigned tp = 0u;
#define TEXT_ASK()                                                                                                     \
  {                                                                                                                    \
    const char* pc;                                                                                                    \
    unsigned n;                                                                                                        \
    asm volatile("s_getpc_b64 %0\n.LKG_PC:\n\ts_mov_b32 %1, .LKG_LAST-.LKG_PC" : "=s"(pc), "=s"(n));                    \
    const unsigned o = (unsigned)(tid * 8 + ((blockIdx.x >> 3) & 7)) * IPS;                                            \
    if ((int)o < (int)n) asm volatile("global_load_dword %0, %1, %2" : "=v"(tp) : "v"(o), "s"(pc) : "memory");         \
  }
  if (IPF == 2) TEXT_ASK()
  v4u raw[2] = {};
#pragma unroll
  for (int i = 0; i < TP; ++i)
    if (kg < 32) raw[i] = *reinterpret_cast<const v4u*>(A.x0 + (size_t)(TP * (8 * (kg & 7) + w) + i) * DM + l * 8);
  G.load(A, 8 * kg + w, l);
  {
    const __amdgpu_buffer_rsrc_t rz = MKRS(A.ws + OFF_Z, Z_BYTES);
#pragma unroll
    for (int i = 0; i < 4; ++i) {
      const u64 z = i == 0 && kg < (int)(2 * SZ_ACC / 8192) ? ACC_INIT : 0ull;
      ST128((v4u{(unsigned)z, (unsigned)(z >> 32), (unsigned)z, (unsigned)(z >> 32)}), rz, tid * 16, (i * 64 + kg) * 8192);
    }
  }
  const u64 tag = call_tag(A);
  if (TP && kg < 32) {
    unsigned t = row_stats(raw[0]);
    if (TP > 1) {
      const unsigned t1 = row_stats(raw[1]);
      t = l < 2 ? t : t1;
    }
    if (l < 2 * TP) __hip_atomic_store((gu64*)(A.ws + OFF_GT) + 2 * (L_SEQ + TP * (8 * kg + w)) + l, tagged(tag, (l & 1) ? t : t << 16), RLX_AGENT);
  }
  DRAIN();
  __syncthreads();
  if (tid < 8) __hip_atomic_store((gu64*)(A.ws + OFF_RD) + tid * 64 + kg, tag, RLX_AGENT);
  asm volatile("" : "+v"(G.pa), "+v"(G.pb), "+v"(G.pe));
  G.setup();
  G.st = smem + w * 16640;
  const __amdgpu_buffer_rsrc_t rs = MKRS(A.ws + OFF_KF + (8 * kg + w) * 2048, 386 * 4);
  SST(1);
  unsigned tw = 0u;
#define TLB_ASK()                                                                                                      \
  if (tid < 384) {                                                                                                     \
    const unsigned o = (unsigned)OFF_XA + (unsigned)(tid * 8 + ((blockIdx.x >> 3) & 7)) * 65536u;                      \
    if (o < WS_TOTAL) asm volatile("global_load_dword %0, %1, %2" : "=v"(tw) : "v"(o), "s"(A.ws) : "memory");          \
  }
#define X2_CLEAR() \
  _Pragma("unroll") for (int i = 0; i < 4; ++i) ST128((v4u{0u, 0u, 0u, 0u}), MKRS(A.ws + OFF_X2, SZ_X), tid * 16, (4 * kg + i) * 8192);
  if (XZP == 1) { X2_CLEAR() }
  if (IPF == 1) TEXT_ASK()
  if (TLBW == 1) TLB_ASK()
  unsigned pf[6] = {};
#define PF_ASK(M)                                                                                                                  \
  if (kg < 32) {                                                                                                                   \
    if ((M) & 1) asm volatile("global_load_dword %0, %2, %3\n\tglobal_load_dword %1, %2, %3 offset:128" : "=&v"(pf[0]), "=&v"(pf[1]) : "v"(kg * 131072 + tid * 256), "s"(A.Wo)); \
    if ((M) & 2) asm volatile("global_load_dword %0, %2, %3\n\tglobal_load_dword %1, %2, %3 offset:128" : "=&v"(pf[2]), "=&v"(pf[3]) : "v"(kg * 256 + tid * 8192), "s"(A.Wq)); \
    if ((M) & 4) asm volatile("global_load_dword %0, %2, %3\n\tglobal_load_dword %1, %2, %3 offset:128" : "=&v"(pf[4]), "=&v"(pf[5]) : "v"(kg * 256 + tid * 8192), "s"(A.Wk)); \
  }
  PF_ASK(PFA)
  G.columns(l, 0, 16);
  G.get(G.bh, G.bl, l);
  if (XZP == 2) { X2_CLEAR() }
  PF_ASK(PFB)
  if (TLBW == 2) TLB_ASK()
  (void)G.scale();
  const Cx zb = G.power(256.f);
  Cx b = G.C, z = G.power(2.f);
#pragma unroll 1
  for (int f = 1; f < 9;) {
    char* p = G.st + l * 8;
    const int fe = f == 1 ? 4 : 9;
#pragma unroll 1
    for (; f < fe; ++f) {
      b = cmul(b, zb);
      if (f == 2 || f == 4) z = cmul(z, z);
      Cx y = b;
      p = G.rows(p, y, z, f == 1 ? 8 : f < 4 ? 4 : 2);
    }
    const f32x4 d = G.tile<false>(l);
    const v4u r = {pkh(d.x, d.x), pkh(d.y, d.y), pkh(d.z, d.z), pkh(d.w, d.w)};
    if (f == 4) {
      ST128(r, rs, (g < 2 ? 8 * c + 4 * g : 64 * g + 4 * c) * 4, 0);
    } else {
      ST64((v2u{r.x, r.y}), rs, (256 + 64 * g + 2 * c) * 4, 0);
      ST64((v2u{r.z, r.w}), rs, (288 + 64 * g + 2 * c) * 4, 0);
    }
  }
  SST(2);
  DRAIN();
  asm volatile("" ::"v"(tp), "v"(tw), "v"(pf[0]), "v"(pf[1]), "v"(pf[2]), "v"(pf[3]), "v"(pf[4]), "v"(pf[5]));
  if (l == 0) __hip_atomic_store((gu64*)(A.ws + OFF_KG) + kg * 16 + w, tag, RLX_AGENT);
  if (XZP == 3) { X2_CLEAR() }
  const u64 rv = (XZP & 3) && w == 0 ? ready_look(A) : 0;
  if (TLBW == 3) {
    TLB_ASK()
    DRAIN();
    asm volatile("" ::"v"(tw));
  }
  DRAIN();
  __syncthreads();
  if ((XZP & 3) && w == 0) {
    ready_wait(A, tag, rv);
    if (l < 8) set_flag((unsigned*)(A.ws + OFF_FL) + FL_FZ + l * 64 + kg);
  }
  SST(3);
}

constexpr int C_SM = 131072, C_SO = C_SM + 8192, C_KT = C_SO + 2048, C_X = C_KT + 16384, C_SQ = C_X + 1024;
static_assert(C_SQ + 4096 <= 163840 && C_SQ % 512 == 0, "lds");
__device__ __forceinline__ v4u mk_srd(const void* p, const unsigned bytes) {
  const unsigned long long a = (unsigned long long)p;
  return v4u{(unsigned)a, (unsigned)(a >> 32) & 0xffffu, bytes, 0x00020000u};
}
template <bool ATT>
__device__ __forceinline__ void role_chain_t(const MegaArgs& A, char* smem, const int L, const int h, const int rep) {
  const int w = A.wv;
  char* ws = A.ws;
  unsigned* FL = (unsigned*)(ws + OFF_FL);
  const int l = lane_id(), c = l & 15, g = l >> 4;
  unsigned* FQ = FL + FL_FQ + L * 64 * 32 * 32;
  const int wb = (w + ROT_H) & 7, tb = ROT_T;
  const unsigned sb = lds_base(smem);
  glds16(A.mem0, (unsigned)((c & 7) * DM + 64 * w + 16 * g + 8 * (c >> 3)) * 2, sb + C_SM + w * 1024);
#pragma unroll
  for (int i = 0; i < 16; ++i)
    glds16((const char*)A.Wo + (size_t)(h * DH + 32 * (i >> 2) + 4 * w + (i & 2)) * (DM * 2),
           (unsigned)((i & 1) * 1024 + ((l ^ (((i & 1) | ((w & 3) << 1)) << 1)) << 4)), sb + w * 16384 + i * 1024, WNT & 1);
  const u64 tag = call_tag(A);
  const char* gK = ws + OFF_QK + L * SZ_QK + ((size_t)h * 16 + 2 * w) * 1024;
  const char* gV = ws + OFF_VF + L * SZ_O + ((size_t)h * 8 + 2 * w - 8) * 1024;
  v8u pv, pw;
  {
    const unsigned voff = g * 256 + c * 8, wp = sb + w * 16384 + l * 16;
    pv[0] = (c & 8) ? 0xEEEE : 0x4444;
    pv[1] = sb + C_SM + l * 16;
    const unsigned ra = sb + C_SQ + (g >> 1) * 512 + (c ^ ((g & 1) * 18)) * 8;
    pv[2] = ATT ? ra : voff;
    pv[3] = ATT ? ra ^ 0x108 : wp;
    pv[4] = sb + ((c & 2) ? C_SQ : C_SO) + w * 256 + (((c & 5) | ((c >> 2) & 2)) + 8 * (g >> 1)) * 16 + 8 * ((g ^ (c >> 1)) & 1);
    pv[5] = voff;
    pv[6] = wp;
    pv[7] = sb + C_SO + (l >> 5) * 1024 + w * 256 + (l & 31) * 8;
    const int q = (l >> 2) & 3, p = l & 3;
    const int f = (q & 1) | ((g & 1) << 1) | ((q >> 1) << 2);
#pragma unroll
    for (int i = 0; i < 4; ++i) pw[i] = (4 * i + g) * (HD * 2) + ((c ^ (((g & 1) | (i << 1)) << 1)) << 4) - 1024 * i;
    pw[4] = sb + (g >> 1) * 16384 + (4 * (g & 1) + (q & 1) + 8 * (q >> 1)) * 256 + (w ^ f) * 32 + p * 8;
    pw[5] = sb + (4 * (g >> 1) + 2 * (q >> 1) + (g & 1)) * 16384 + (q & 1) * 1024 + p * 8 + ((4 * wb) ^ f ^ tb) * 32;
    pw[6] = pw[7] = 0u;
  }
  const size_t wo = (size_t)(16 * w) * HD + h * DH;
#define POL 2
#define PG(w) (POL == 2 ? (w) * 4096 : ((w) & 1) * 4096 + ((w) >> 1) * 16384)
  const v4u rsA = mk_srd(ws + OFF_ACC + L * SZ_ACC + (POL == 1 ? 8192 : 0), SZ_ACC - 8192);
  const v4u rsF = mk_srd(FQ, 64 * 32 * 32 * 4);
  const v4u rsMK = mk_srd(ws + OFF_MEMK + L * SZ_MEM, SZ_MEM);
  const v4u rsMV = mk_srd(ws + OFF_MEMV + L * SZ_MEM, SZ_MEM);
  const v4u pAB = (v4u{0u, 2048u, 3072u, 1024u} ^ (1536u * tb)) + PG(wb);
  const v4u rsT = mk_srd(FL + FL_FK + L * 32, 64 * 1024 - 128);
  unsigned sc[8];
  sc[0] = h * 128;
  sc[1] = (h * 4 + w - 4) * 512;
  sc[2] = (h * 4 + w - 4) * 512;
  sc[3] = NSEG - 1;
  sc[5] = w;
  sc[6] = sb;
  const char* dbg = ws;
#define CHAIN_OPS                                                                                                          \
  ::"{v[0:7]}"(pv), "{v[24:31]}"(pw), "{s[4:5]}"(A.Wv + wo), "{s[6:7]}"(A.Wq + wo), "{s[8:9]}"(A.Wk + wo), "{s[10:11]}"(tag),         \
      "{s[12:13]}"(ws + OFF_RD + (blockIdx.x & 7) * 512), "{s[14:15]}"(ws + OFF_G + (blockIdx.x & 7) * 256), "{s[16:17]}"(ws + OFF_G + h * 8), \
      "{s18}"(w == 4 && L == 0 ? 255 : 0), "{s19}"(sb + w * 16384), "{s[20:21]}"(ws + WS_TOTAL + (w == 0 ? 534 * 256 + A.vb : 610 * 256) * 8), \
      "{s[40:43]}"(rsA), "{s[44:47]}"(rsF), "{s[48:51]}"(rsMK), "{s[52:55]}"(rsMV), "{s[56:59]}"(pAB), "{s60}"(PG(w)), "{s61}"(ATT ? PG(h >> 2) + (h & 3) * 1024 : L), \
      "{s[62:63]}"(gK), "{s[64:65]}"(gV), "{s66}"(0), "{s67}"(w == 4 ? (h >> 3) * 256 + (h & 7) * 8 : 0x40000000), "{s68}"(sc[0]), "{s69}"(sc[1]), "{s70}"(sc[2]),    \
      "{s71}"(sc[3]), "{s73}"(sc[5]), "{s74}"(sc[6]), "{s[36:37]}"(dbg),                   \
      "{s[0:3]}"(rsT)                                                                                                      \
      : "memory", "vcc", "scc", "s99", "v255"
  if (ATT) asm volatile("s_mov_b64 exec, -1\ns_nop 4\n" CHAIN_ASM_ATT ".LKG_LAS2:\n" CHAIN_OPS);
  else asm volatile("s_mov_b64 exec, -1\ns_nop 4\n" CHAIN_ASM_VO CHAIN_OPS);
#undef CHAIN_OPS
}

__device__ __forceinline__ void role_chain(const MegaArgs& A, char* smem, const int L, const int h, const int rep) {
  if (A.wv < 4) role_chain_t<true>(A, smem, L, h, rep);
  else role_chain_t<false>(A, smem, L, h, rep);
}

constexpr int AW_SO = 0, AW_TK = 32768, AW_CNT = AW_TK + 98304;
__device__ __forceinline__ unsigned umin3(unsigned a, unsigned b, unsigned c) {
  return __builtin_elementwise_min(__builtin_elementwise_min(a, b), c);
}
#define PW_FREE(L_, s_, i_) \
  if (l == 0) set_flag(FL + FL_FD + ((s_) * 2 + (L_)) * 32 + (i_))
#define AW_ASM_X \
  "s_mov_b32 s61, 0\n" \
  "s_mov_b32 s67, 1\n" \
  "s_mov_b32 s68, 0\n" \
  "s_mov_b32 s70, 0\n" \
  "s_mov_b32 s73, 0\n" \
  "s_mov_b32 s71, 0x3e0293ee\n" \
  "s_mov_b32 s72, 0xbe0293ee\n" \
  "s_mov_b32 s65, 49152\n" \
  "s_bitcmp0_b32 s58, 2\n" \
  "s_cbranch_scc1 .LCX_OWN_%=\n" \
  "v_swap_b32 v192, v208\n" \
  "v_swap_b32 v193, v209\n" \
  "v_swap_b32 v194, v210\n" \
  "v_swap_b32 v195, v211\n" \
  "v_swap_b32 v196, v212\n" \
  "v_swap_b32 v197, v213\n" \
  "v_swap_b32 v198, v214\n" \
  "v_swap_b32 v199, v215\n" \
  "v_swap_b32 v200, v216\n" \
  "v_swap_b32 v201, v217\n" \
  "v_swap_b32 v202, v218\n" \
  "v_swap_b32 v203, v219\n" \
  "v_swap_b32 v204, v220\n" \
  "v_swap_b32 v205, v221\n" \
  "v_swap_b32 v206, v222\n" \
  "v_swap_b32 v207, v223\n" \
  "v_swap_b32 v224, v240\n" \
  "v_swap_b32 v225, v241\n" \
  "v_swap_b32 v226, v242\n" \
  "v_swap_b32 v227, v243\n" \
  "v_swap_b32 v228, v244\n" \
  "v_swap_b32 v229, v245\n" \
  "v_swap_b32 v230, v246\n" \
  "v_swap_b32 v231, v247\n" \
  "v_swap_b32 v232, v248\n" \
  "v_swap_b32 v233, v249\n" \
  "v_swap_b32 v234, v250\n" \
  "v_swap_b32 v235, v251\n" \
  "v_swap_b32 v236, v252\n" \
  "v_swap_b32 v237, v253\n" \
  "v_swap_b32 v238, v254\n" \
  "v_swap_b32 v239, v255\n" \
  ".LCX_OWN_%=:\n" \
  "v_mov_b32 v38, s58\n" \
  "s_memrealtime s[38:39]\n" \
  "s_waitcnt lgkmcnt(0)\n" \
  "s_mov_b32 s69, s38\n" \
  "s_setprio 3\n" \
  ".p2align 6\n" \
  ".LCX_TOP_%=:\n" \
  "s_mov_b32 s62, 0x200000\n" \
  "s_cmp_ge_u32 s73, s67\n" \
  "s_cbranch_scc1 .LCX_TOK_%=\n" \
  "s_cmp_eq_u32 s61, 0\n" \
  "s_cbranch_scc1 .LCX_SPIN_%=\n" \
  ".LCX_RE_%=:\n" \
  "ds_read_b32 v37, v38\n" \
  "s_add_u32 s62, s62, 1\n" \
  "s_waitcnt lgkmcnt(0)\n" \
  "v_readfirstlane_b32 s73, v37\n" \
  "s_bfe_u32 s64, s73, 0x80008\n" \
  "s_and_b32 s73, s73, 0xff\n" \
  "s_min_u32 s73, s73, s64\n" \
  "s_cmp_ge_u32 s73, s67\n" \
  "s_cbranch_scc1 .LCX_TOK_%=\n" \
  "s_sleep 1\n" \
  "s_cmp_lt_u32 s62, 2097158\n" \
  "s_cbranch_scc1 .LCX_RE_%=\n" \
  ".LCX_SPIN_%=:\n" \
  "s_sleep 2\n" \
  "s_memrealtime s[38:39]\n" \
  "ds_read_b32 v37, v38\n" \
  "s_sub_u32 s62, s62, 1\n" \
  "s_cbranch_scc1 .LCX_END_%=\n" \
  "s_waitcnt lgkmcnt(0)\n" \
  "v_readfirstlane_b32 s73, v37\n" \
  "s_add_u32 s38, s38, 80\n" \
  "s_bfe_u32 s64, s73, 0x80008\n" \
  "s_and_b32 s73, s73, 0xff\n" \
  "s_min_u32 s73, s73, s64\n" \
  "s_cmp_ge_u32 s73, s67\n" \
  "s_cbranch_scc0 .LCX_SPIN_%=\n" \
  "s_cmp_eq_u32 s70, 0\n" \
  "s_cbranch_scc1 .LCX_TOK_%=\n" \
  "s_sub_u32 s63, s38, s69\n" \
  "s_max_i32 s63, s63, 0\n" \
  "s_add_u32 s69, s69, s63\n" \
  ".LCX_TOK_%=:\n" \
  "ds_read_b128 v[48:51], v6 offset:0\n" \
  "ds_read_b128 v[64:67], v0 offset:8192\n" \
  "ds_read_b128 v[80:83], v0 offset:12288\n" \
  "ds_read_b128 v[52:55], v6 offset:1024\n" \
  "ds_read_b128 v[68:71], v0 offset:9216\n" \
  "ds_read_b128 v[84:87], v0 offset:13312\n" \
  "ds_read_b128 v[56:59], v6 offset:2048\n" \
  "ds_read_b128 v[72:75], v0 offset:10240\n" \
  "ds_read_b128 v[88:91], v0 offset:14336\n" \
  "ds_read_b128 v[60:63], v6 offset:3072\n" \
  "ds_read_b128 v[76:79], v0 offset:11264\n" \
  "ds_read_b128 v[92:95], v0 offset:15360\n" \
  "s_waitcnt lgkmcnt(9)\n" \
  "v_mfma_f32_16x16x32_bf16 v[20:23], v[64:67], v[48:51], v[8:11]\n" \
  "v_mfma_f32_16x16x32_bf16 v[24:27], v[80:83], v[48:51], v[12:15]\n" \
  "ds_read_b128 v[96:99], v0 offset:16384\n" \
  "ds_read_b128 v[100:103], v0 offset:17408\n" \
  "s_waitcnt lgkmcnt(8)\n" \
  "v_mfma_f32_16x16x32_bf16 v[20:23], v[68:71], v[52:55], v[20:23]\n" \
  "v_mfma_f32_16x16x32_bf16 v[24:27], v[84:87], v[52:55], v[24:27]\n" \
  "ds_read_b128 v[104:107], v0 offset:18432\n" \
  "ds_read_b128 v[108:111], v0 offset:19456\n" \
  "s_waitcnt lgkmcnt(7)\n" \
  "v_mfma_f32_16x16x32_bf16 v[20:23], v[72:75], v[56:59], v[20:23]\n" \
  "v_mfma_f32_16x16x32_bf16 v[24:27], v[88:91], v[56:59], v[24:27]\n" \
  "ds_read_b128 v[112:115], v0 offset:20480\n" \
  "ds_read_b128 v[116:119], v0 offset:21504\n" \
  "s_waitcnt lgkmcnt(6)\n" \
  "v_mfma_f32_16x16x32_bf16 v[20:23], v[76:79], v[60:63], v[20:23]\n" \
  "v_mfma_f32_16x16x32_bf16 v[24:27], v[92:95], v[60:63], v[24:27]\n" \
  "ds_read_b128 v[120:123], v0 offset:22528\n" \
  "ds_read_b128 v[124:127], v0 offset:23552\n" \
  "s_add_u32 s67, s67, 1\n" \
  "v_add_u32 v0, s65, v0\n" \
  "v_add_u32 v6, s65, v6\n" \
  "s_sub_u32 s65, 0, s65\n" \
  "s_nop 1\n" \
  "v_max3_f32 v32, v20, v21, v22\n" \
  "v_max3_f32 v33, v23, v24, v25\n" \
  "v_max3_f32 v32, v32, v26, v27\n" \
  "v_max_f32 v32, v32, v33\n" \
  "v_mov_b32 v33, v32\n" \
  "s_nop 1\n" \
  "v_permlane16_swap_b32 v32, v33\n" \
  "v_max_f32 v32, v32, v33\n" \
  "v_mov_b32 v33, v32\n" \
  "s_nop 1\n" \
  "v_permlane32_swap_b32 v32, v33\n" \
  "v_max_f32 v32, v32, v33\n" \
  "v_mul_f32 v33, s72, v32\n" \
  "v_fma_f32 v20, v20, s71, v33\n" \
  "v_fma_f32 v21, v21, s71, v33\n" \
  "v_fma_f32 v22, v22, s71, v33\n" \
  "v_fma_f32 v23, v23, s71, v33\n" \
  "v_fma_f32 v24, v24, s71, v33\n" \
  "v_fma_f32 v25, v25, s71, v33\n" \
  "v_fma_f32 v26, v26, s71, v33\n" \
  "v_fma_f32 v27, v27, s71, v33\n" \
  "v_exp_f32 v20, v20\n" \
  "v_exp_f32 v21, v21\n" \
  "v_exp_f32 v22, v22\n" \
  "v_exp_f32 v23, v23\n" \
  "v_exp_f32 v24, v24\n" \
  "v_exp_f32 v25, v25\n" \
  "v_exp_f32 v26, v26\n" \
  "v_exp_f32 v27, v27\n" \
  "v_add_f32 v34, v20, v21\n" \
  "v_add_f32 v33, v22, v23\n" \
  "v_add_f32 v36, v24, v25\n" \
  "v_add_f32 v34, v34, v33\n" \
  "v_add_f32 v33, v26, v27\n" \
  "v_add_f32 v34, v34, v36\n" \
  "v_add_f32 v34, v34, v33\n" \
  "v_mov_b64 v[28:29], 0\n" \
  "v_mov_b64 v[30:31], 0\n" \
  "s_waitcnt vmcnt(2) lgkmcnt(0)\n" \
  "s_cmp_gt_i32 s68, -2\n" \
  "s_cbranch_scc1 .LCX_NP_%=\n" \
  "s_waitcnt vmcnt(0)\n" \
  ".LCX_NP_%=:\n" \
  "s_cmp_lg_u32 s68, 0\n" \
  "s_cbranch_scc1 .LCX_TEST_%=\n" \
  ".LCX_WF_%=:\n" \
  "s_memrealtime s[38:39]\n" \
  "s_waitcnt lgkmcnt(0)\n" \
  "s_sub_u32 s63, s69, s38\n" \
  "s_cmp_le_i32 s63, 0\n" \
  "s_cbranch_scc1 .LCX_GO_%=\n" \
  "s_cmp_lt_i32 s63, 12\n" \
  "s_cbranch_scc1 .LCX_WF_%=\n" \
  "s_sleep 3\n" \
  "s_branch .LCX_WF_%=\n" \
  ".LCX_GO_%=:\n" \
  "v_mov_b32 v35, 0\n" \
  "global_load_dwordx4 v[128:131], v5, s[48:49] offset:0 sc1\n" \
  "global_load_dwordx4 v[132:135], v5, s[48:49] offset:1024 sc1\n" \
  "global_load_dwordx4 v[136:139], v3, s[48:49] offset:0 sc1\n" \
  "global_load_dwordx4 v[140:143], v3, s[48:49] offset:512 sc1\n" \
  "global_load_dword v35, v35, s[50:51] sc1\n" \
  "global_load_dword v146, v2, s[52:53] sc1\n" \
  "s_sub_u32 s62, s62, 8\n" \
  "s_cbranch_scc1 .LCX_END_%=\n" \
  "s_waitcnt vmcnt(0)\n" \
  ".LCX_TEST_%=:\n" \
  "s_setprio 3\n" \
  "v_smfmac_f32_16x16x64_bf16 v[28:31], v[128:131], v[48:55], v1\n" \
  "v_smfmac_f32_16x16x64_bf16 v[28:31], v[132:135], v[56:63], v1\n" \
  "v_min3_u32 v33, v128, v129, v130\n" \
  "v_min3_u32 v36, v131, v132, v133\n" \
  "v_min3_u32 v33, v33, v134, v135\n" \
  "v_min3_u32 v36, v36, v136, v137\n" \
  "v_min3_u32 v33, v33, v138, v139\n" \
  "v_min3_u32 v36, v36, v140, v141\n" \
  "v_min3_u32 v33, v33, v142, v143\n" \
  "v_min3_u32 v33, v33, v36, v146\n" \
  "v_cmp_eq_u32 vcc, 0, v33\n" \
  "v_readfirstlane_b32 s64, v35\n" \
  "s_mov_b32 s68, 0\n" \
  "s_cmp_eq_u64 vcc, 0\n" \
  "s_cbranch_scc0 .LCX_FAIL_%=\n" \
  "v_permlane32_swap_b32 v28, v30\n" \
  "v_permlane32_swap_b32 v29, v31\n" \
  "v_mov_b32 v33, s67\n" \
  "ds_write_b32 v38, v33 offset:16\n" \
  "v_add_f32 v28, v28, v30\n" \
  "v_add_f32 v29, v29, v31\n" \
  "v_max3_f32 v36, v28, v29, v32\n" \
  "v_mov_b32 v33, v36\n" \
  "s_cmp_eq_u32 s64, 0\n" \
  "s_cselect_b32 s64, s38, s64\n" \
  "v_permlane16_swap_b32 v36, v33\n" \
  "v_max_f32 v36, v36, v33\n" \
  "v_mov_b32 v33, v36\n" \
  "s_sub_u32 s63, s64, s70\n" \
  "s_mov_b32 s70, s64\n" \
  "v_permlane32_swap_b32 v36, v33\n" \
  "v_max_f32 v36, v36, v33\n" \
  "v_mul_f32 v36, s72, v36\n" \
  "v_fma_f32 v32, v32, s71, v36\n" \
  "v_fma_f32 v28, v28, s71, v36\n" \
  "v_fma_f32 v29, v29, s71, v36\n" \
  "v_exp_f32 v32, v32\n" \
  "v_exp_f32 v28, v28\n" \
  "v_exp_f32 v29, v29\n" \
  "v_fma_f32 v34, v34, v32, v28\n" \
  "v_add_f32 v34, v34, v29\n" \
  "v_mov_b32 v33, v34\n" \
  "s_min_u32 s63, s63, 250\n" \
  "s_add_u32 s69, s64, s63\n" \
  "v_permlane16_swap_b32 v34, v33\n" \
  "v_add_f32 v34, v34, v33\n" \
  "v_mov_b32 v33, v34\n" \
  "s_add_u32 s69, s69, -48\n" \
  "s_add_u32 s48, s48, 0x10000\n" \
  "s_addc_u32 s49, s49, 0\n" \
  "v_permlane32_swap_b32 v34, v33\n" \
  "v_add_f32 v34, v34, v33\n" \
  "v_rcp_f32 v34, v34\n" \
  "s_add_u32 s50, s50, 1024\n" \
  "s_addc_u32 s51, s51, 0\n" \
  "s_add_u32 s52, s52, 256\n" \
  "s_addc_u32 s53, s53, 0\n" \
  "v_mul_f32 v32, v32, v34\n" \
  "v_mul_f32 v20, v20, v32\n" \
  "v_mul_f32 v21, v21, v32\n" \
  "v_mul_f32 v22, v22, v32\n" \
  "v_mul_f32 v23, v23, v32\n" \
  "v_mul_f32 v24, v24, v32\n" \
  "v_mul_f32 v25, v25, v32\n" \
  "v_mul_f32 v26, v26, v32\n" \
  "v_mul_f32 v27, v27, v32\n" \
  "v_cvt_pk_bf16_f32 v40, v20, v21\n" \
  "v_cvt_pk_bf16_f32 v41, v22, v23\n" \
  "v_cvt_pk_bf16_f32 v42, v24, v25\n" \
  "v_cvt_pk_bf16_f32 v43, v26, v27\n" \
  "v_mul_f32 v28, v28, v34\n" \
  "v_mul_f32 v29, v29, v34\n" \
  "v_mfma_f32_16x16x32_bf16 v[64:67], v[96:99], v[40:43], 0\n" \
  "v_cvt_pk_bf16_f32 v44, v28, v29\n" \
  "v_mov_b32 v45, 0\n" \
  "v_mfma_f32_16x16x32_bf16 v[68:71], v[100:103], v[40:43], 0\n" \
  "v_cvt_pk_bf16_f32 v47, v28, v29\n" \
  "v_mov_b32 v46, 0\n" \
  "v_mfma_f32_16x16x32_bf16 v[72:75], v[104:107], v[40:43], 0\n" \
  "v_permlane32_swap_b32 v44, v45\n" \
  "v_mfma_f32_16x16x32_bf16 v[76:79], v[108:111], v[40:43], 0\n" \
  "v_permlane32_swap_b32 v46, v47\n" \
  "v_mfma_f32_16x16x32_bf16 v[80:83], v[112:115], v[40:43], 0\n" \
  "v_xor_b32 v16, 4096, v7\n" \
  "v_mfma_f32_16x16x32_bf16 v[84:87], v[116:119], v[40:43], 0\n" \
  "v_xor_b32 v17, 8192, v7\n" \
  "v_mfma_f32_16x16x32_bf16 v[88:91], v[120:123], v[40:43], 0\n" \
  "v_xor_b32 v18, 12288, v7\n" \
  "v_mfma_f32_16x16x32_bf16 v[92:95], v[124:127], v[40:43], 0\n" \
  "s_memrealtime s[38:39]\n" \
  "ds_read_b32 v37, v38\n" \
  "v_mfma_f32_16x16x16_bf16 v[64:67], v[136:137], v[44:45], v[64:67]\n" \
  "v_mfma_f32_16x16x16_bf16 v[68:71], v[140:141], v[44:45], v[68:71]\n" \
  "v_mfma_f32_16x16x16_bf16 v[72:75], v[136:137], v[46:47], v[72:75]\n" \
  "v_mfma_f32_16x16x16_bf16 v[76:79], v[140:141], v[46:47], v[76:79]\n" \
  "v_mfma_f32_16x16x16_bf16 v[80:83], v[138:139], v[44:45], v[80:83]\n" \
  "v_mfma_f32_16x16x16_bf16 v[84:87], v[142:143], v[44:45], v[84:87]\n" \
  "v_mfma_f32_16x16x16_bf16 v[88:91], v[138:139], v[46:47], v[88:91]\n" \
  "v_mfma_f32_16x16x16_bf16 v[92:95], v[142:143], v[46:47], v[92:95]\n" \
  "s_nop 0\n" \
  "v_cvt_pk_bf16_f32 v160, v64, v65\n" \
  "v_cvt_pk_bf16_f32 v161, v66, v67\n" \
  "v_cvt_pk_bf16_f32 v162, v68, v69\n" \
  "v_cvt_pk_bf16_f32 v163, v70, v71\n" \
  "ds_write_b128 v7, v[160:163] offset:0\n" \
  "v_cvt_pk_bf16_f32 v164, v72, v73\n" \
  "v_cvt_pk_bf16_f32 v165, v74, v75\n" \
  "v_cvt_pk_bf16_f32 v166, v76, v77\n" \
  "v_cvt_pk_bf16_f32 v167, v78, v79\n" \
  "ds_write_b128 v7, v[164:167] offset:1024\n" \
  "v_cvt_pk_bf16_f32 v168, v80, v81\n" \
  "v_cvt_pk_bf16_f32 v169, v82, v83\n" \
  "v_cvt_pk_bf16_f32 v170, v84, v85\n" \
  "v_cvt_pk_bf16_f32 v171, v86, v87\n" \
  "ds_write_b128 v7, v[168:171] offset:2048\n" \
  "v_cvt_pk_bf16_f32 v172, v88, v89\n" \
  "v_cvt_pk_bf16_f32 v173, v90, v91\n" \
  "v_cvt_pk_bf16_f32 v174, v92, v93\n" \
  "v_cvt_pk_bf16_f32 v175, v94, v95\n" \
  "ds_write_b128 v7, v[172:175] offset:3072\n" \
  "v_xor_b32 v7, 0x4000, v7\n" \
  "v_mfma_f32_16x16x32_bf16 v[112:115], v[192:195], v[160:163], 0\n" \
  "v_mfma_f32_16x16x32_bf16 v[116:119], v[224:227], v[160:163], 0\n" \
  "s_waitcnt lgkmcnt(0)\n" \
  "s_barrier\n" \
  "ds_read_b128 v[64:67], v16 offset:0\n" \
  "ds_read_b128 v[68:71], v16 offset:1024\n" \
  "ds_read_b128 v[72:75], v16 offset:2048\n" \
  "ds_read_b128 v[76:79], v16 offset:3072\n" \
  "ds_read_b128 v[80:83], v17 offset:0\n" \
  "ds_read_b128 v[84:87], v17 offset:1024\n" \
  "ds_read_b128 v[88:91], v17 offset:2048\n" \
  "ds_read_b128 v[92:95], v17 offset:3072\n" \
  "ds_read_b128 v[96:99], v18 offset:0\n" \
  "ds_read_b128 v[100:103], v18 offset:1024\n" \
  "ds_read_b128 v[104:107], v18 offset:2048\n" \
  "ds_read_b128 v[108:111], v18 offset:3072\n" \
  "s_sub_u32 s59, s60, 1\n" \
  "s_bfe_u32 s59, s59, 0x10003\n" \
  "v_readfirstlane_b32 s73, v37\n" \
  "s_sub_u32 s63, s38, s69\n" \
  "s_bfe_u32 s64, s73, 0x80010\n" \
  "s_sub_u32 s64, s64, s67\n" \
  "s_or_b32 s63, s63, s64\n" \
  "s_bfe_u32 s64, s73, 0x80008\n" \
  "s_and_b32 s73, s73, 0xff\n" \
  "s_min_u32 s73, s73, s64\n" \
  "s_or_b32 s63, s63, s68\n" \
  "s_cmp_lt_i32 s63, 0\n" \
  "s_cbranch_scc1 .LCX_E2_%=\n" \
  "v_mov_b32 v35, 0\n" \
  "global_load_dwordx4 v[128:131], v5, s[48:49] offset:0 sc1\n" \
  "global_load_dwordx4 v[132:135], v5, s[48:49] offset:1024 sc1\n" \
  "global_load_dwordx4 v[136:139], v3, s[48:49] offset:0 sc1\n" \
  "global_load_dwordx4 v[140:143], v3, s[48:49] offset:512 sc1\n" \
  "global_load_dword v35, v35, s[50:51] sc1\n" \
  "global_load_dword v146, v2, s[52:53] sc1\n" \
  "s_mov_b32 s68, -1\n" \
  ".LCX_E2_%=:\n" \
  "v_mfma_f32_16x16x32_bf16 v[112:115], v[196:199], v[164:167], v[112:115]\n" \
  "v_mfma_f32_16x16x32_bf16 v[116:119], v[228:231], v[164:167], v[116:119]\n" \
  "v_mfma_f32_16x16x32_bf16 v[112:115], v[200:203], v[168:171], v[112:115]\n" \
  "v_mfma_f32_16x16x32_bf16 v[116:119], v[232:235], v[168:171], v[116:119]\n" \
  "v_mfma_f32_16x16x32_bf16 v[112:115], v[204:207], v[172:175], v[112:115]\n" \
  "v_mfma_f32_16x16x32_bf16 v[116:119], v[236:239], v[172:175], v[116:119]\n" \
  "s_waitcnt lgkmcnt(11)\n" \
  "v_mfma_f32_16x16x32_bf16 v[120:123], v[192:195], v[64:67], 0\n" \
  "v_mfma_f32_16x16x32_bf16 v[124:127], v[224:227], v[64:67], 0\n" \
  "s_waitcnt lgkmcnt(10)\n" \
  "v_mfma_f32_16x16x32_bf16 v[120:123], v[196:199], v[68:71], v[120:123]\n" \
  "v_mfma_f32_16x16x32_bf16 v[124:127], v[228:231], v[68:71], v[124:127]\n" \
  "s_waitcnt lgkmcnt(9)\n" \
  "v_mfma_f32_16x16x32_bf16 v[120:123], v[200:203], v[72:75], v[120:123]\n" \
  "v_mfma_f32_16x16x32_bf16 v[124:127], v[232:235], v[72:75], v[124:127]\n" \
  "s_waitcnt lgkmcnt(8)\n" \
  "v_mfma_f32_16x16x32_bf16 v[120:123], v[204:207], v[76:79], v[120:123]\n" \
  "v_mfma_f32_16x16x32_bf16 v[124:127], v[236:239], v[76:79], v[124:127]\n" \
  "s_waitcnt lgkmcnt(7)\n" \
  "v_mfma_f32_16x16x32_bf16 v[112:115], v[208:211], v[80:83], v[112:115]\n" \
  "v_mfma_f32_16x16x32_bf16 v[116:119], v[240:243], v[80:83], v[116:119]\n" \
  "s_waitcnt lgkmcnt(6)\n" \
  "v_mfma_f32_16x16x32_bf16 v[112:115], v[212:215], v[84:87], v[112:115]\n" \
  "v_mfma_f32_16x16x32_bf16 v[116:119], v[244:247], v[84:87], v[116:119]\n" \
  "s_waitcnt lgkmcnt(5)\n" \
  "v_mfma_f32_16x16x32_bf16 v[112:115], v[216:219], v[88:91], v[112:115]\n" \
  "v_mfma_f32_16x16x32_bf16 v[116:119], v[248:251], v[88:91], v[116:119]\n" \
  "s_waitcnt lgkmcnt(4)\n" \
  "v_mfma_f32_16x16x32_bf16 v[112:115], v[220:223], v[92:95], v[112:115]\n" \
  "v_mfma_f32_16x16x32_bf16 v[116:119], v[252:255], v[92:95], v[116:119]\n" \
  "s_memrealtime s[38:39]\n" \
  "ds_read_b32 v37, v38\n" \
  "s_waitcnt lgkmcnt(4)\n" \
  "v_mfma_f32_16x16x32_bf16 v[120:123], v[208:211], v[96:99], v[120:123]\n" \
  "v_mfma_f32_16x16x32_bf16 v[124:127], v[240:243], v[96:99], v[124:127]\n" \
  "s_waitcnt lgkmcnt(3)\n" \
  "v_mfma_f32_16x16x32_bf16 v[120:123], v[212:215], v[100:103], v[120:123]\n" \
  "v_mfma_f32_16x16x32_bf16 v[124:127], v[244:247], v[100:103], v[124:127]\n" \
  "v_cvt_pk_bf16_f32 v20, v112, v113\n" \
  "v_cvt_pk_bf16_f32 v21, v114, v115\n" \
  "v_cvt_pk_bf16_f32 v22, v116, v117\n" \
  "v_cvt_pk_bf16_f32 v23, v118, v119\n" \
  "v_and_or_b32 v20, v20, -2, s59\n" \
  "s_waitcnt lgkmcnt(2)\n" \
  "v_mfma_f32_16x16x32_bf16 v[120:123], v[216:219], v[104:107], v[120:123]\n" \
  "v_mfma_f32_16x16x32_bf16 v[124:127], v[248:251], v[104:107], v[124:127]\n" \
  "buffer_store_dwordx4 v[20:23], v4, s[40:43], s54 offen sc1\n" \
  "s_waitcnt lgkmcnt(0)\n" \
  "v_mfma_f32_16x16x32_bf16 v[120:123], v[220:223], v[108:111], v[120:123]\n" \
  "v_mfma_f32_16x16x32_bf16 v[124:127], v[252:255], v[108:111], v[124:127]\n" \
  "s_xor_b32 s64, s54, 16384\n" \
  "s_mov_b32 s61, 1\n" \
  "s_nop 3\n" \
  "v_cvt_pk_bf16_f32 v24, v120, v121\n" \
  "v_cvt_pk_bf16_f32 v25, v122, v123\n" \
  "v_cvt_pk_bf16_f32 v26, v124, v125\n" \
  "v_cvt_pk_bf16_f32 v27, v126, v127\n" \
  "v_and_or_b32 v24, v24, -2, s59\n" \
  "buffer_store_dwordx4 v[24:27], v4, s[40:43], s64 offen sc1\n" \
  "v_readfirstlane_b32 s73, v37\n" \
  "s_sub_u32 s63, s38, s69\n" \
  "s_bfe_u32 s64, s73, 0x80010\n" \
  "s_sub_u32 s64, s64, s67\n" \
  "s_or_b32 s63, s63, s64\n" \
  "s_bfe_u32 s64, s73, 0x80008\n" \
  "s_and_b32 s73, s73, 0xff\n" \
  "s_min_u32 s73, s73, s64\n" \
  "s_or_b32 s63, s63, s68\n" \
  "s_cmp_lt_i32 s63, 0\n" \
  "s_cbranch_scc1 .LCX_E3_%=\n" \
  "v_mov_b32 v35, 0\n" \
  "global_load_dwordx4 v[128:131], v5, s[48:49] offset:0 sc1\n" \
  "global_load_dwordx4 v[132:135], v5, s[48:49] offset:1024 sc1\n" \
  "global_load_dwordx4 v[136:139], v3, s[48:49] offset:0 sc1\n" \
  "global_load_dwordx4 v[140:143], v3, s[48:49] offset:512 sc1\n" \
  "global_load_dword v35, v35, s[50:51] sc1\n" \
  "global_load_dword v146, v2, s[52:53] sc1\n" \
  "s_mov_b32 s68, -2\n" \
  ".LCX_E3_%=:\n" \
  "s_setprio 0\n" \
  "s_add_u32 s54, s54, 0x80000\n" \
  "s_bitset0_b32 s54, 22\n" \
  "s_sub_u32 s60, s60, 1\n" \
  "s_cmp_lg_u32 s60, 0\n" \
  "s_cbranch_scc1 .LCX_TOP_%=\n" \
  "s_waitcnt vmcnt(0)\n" \
  ".LCX_END_%=:\n" \
  "s_endpgm\n" \
  ".LCX_FAIL_%=:\n" \
  "v_mov_b64 v[28:29], 0\n" \
  "v_mov_b64 v[30:31], 0\n" \
  ".LCX_FAIL2_%=:\n" \
  "s_add_u32 s69, s64, -48\n" \
  "s_cmp_lg_u32 s64, 0\n" \
  "s_cbranch_scc1 .LCX_WF_%=\n" \
  "s_sleep 4\n" \
  "global_load_dword v35, v35, s[50:51] sc1\n" \
  "s_sub_u32 s62, s62, 8\n" \
  "s_cbranch_scc1 .LCX_END_%=\n" \
  "s_waitcnt vmcnt(0)\n" \
  "v_readfirstlane_b32 s64, v35\n" \
  "s_branch .LCX_FAIL2_%=\n"
#define AW_ASM_Y \
  "s_mov_b32 s72, 0\n" \
  "s_mov_b32 s67, 0\n" \
  "s_mov_b32 s68, 2\n" \
  "s_mov_b32 s69, 0\n" \
  "s_mov_b32 s65, 49152\n" \
  "s_mov_b32 s66, 16384\n" \
  "v_mov_b32 v5, s58\n" \
  "v_mov_b32 v60, v3\n" \
  "v_add_u32 v3, 4096, v2\n" \
  "s_mov_b32 s62, 0x40000\n" \
  "s_setprio 1\n" \
  "s_branch .LCY_ASK_%=\n" \
  ".p2align 6\n" \
  ".LCY_BAR_%=:\n" \
  "s_barrier\n" \
  "s_memrealtime s[38:39]\n" \
  "ds_read_b128 v[8:11], v0 offset:0\n" \
  "ds_read_b128 v[12:15], v0 offset:1024\n" \
  "ds_read_b128 v[16:19], v0 offset:2048\n" \
  "s_sub_u32 s59, s60, 1\n" \
  "s_bfe_u32 s59, s59, 0x10003\n" \
  "s_add_u32 s68, s68, 1\n" \
  "s_add_u32 s64, s54, 16384\n" \
  "s_cmp_eq_u32 s69, 0\n" \
  "s_cbranch_scc1 .LCY_NI_%=\n" \
  "s_waitcnt vmcnt(3)\n" \
  "s_cmp_lg_u32 s72, 0\n" \
  "s_cbranch_scc1 .LCY_NJ_%=\n" \
  "s_waitcnt vmcnt(0)\n" \
  ".LCY_NJ_%=:\n" \
  "v_mov_b32 v6, s55\n" \
  "s_mov_b64 exec, 1\n" \
  "ds_add_u32 v5, v6\n" \
  "s_mov_b64 exec, -1\n" \
  "s_mov_b32 s69, 0\n" \
  ".LCY_NI_%=:\n" \
  "s_waitcnt lgkmcnt(0)\n" \
  "s_add_u32 s63, s38, 205\n" \
  "s_mov_b32 s72, 0\n" \
  "v_or_b32 v7, 1, s63\n" \
  "s_mov_b64 exec, 1\n" \
  "global_store_dword v2, v7, s[52:53] sc1\n" \
  "s_mov_b64 exec, -1\n" \
  "s_add_u32 s52, s52, 2048\n" \
  "s_addc_u32 s53, s53, 0\n" \
  "v_cmp_eq_u32 vcc, 0, v4\n" \
  "s_cmp_eq_u64 vcc, 0\n" \
  "s_cbranch_scc0 .LCY_NS_%=\n" \
  "v_lshlrev_b32 v6, 16, s55\n" \
  "s_mov_b64 exec, 1\n" \
  "ds_add_u32 v5, v6\n" \
  "s_mov_b64 exec, -1\n" \
  "s_mov_b32 m0, s56\n" \
  "s_add_u32 s71, s56, 4096\n" \
  "global_load_lds_dwordx4 v2, s[48:49] offset:0 sc1\n" \
  "global_load_lds_dwordx4 v2, s[48:49] offset:1024 sc1\n" \
  "global_load_lds_dwordx4 v2, s[48:49] offset:2048 sc1\n" \
  "global_load_lds_dwordx4 v2, s[48:49] offset:3072 sc1\n" \
  "s_mov_b32 m0, s71\n" \
  "s_add_u32 s56, s56, s65\n" \
  "global_load_lds_dwordx4 v3, s[48:49] offset:0 sc1\n" \
  "global_load_lds_dwordx4 v3, s[48:49] offset:1024 sc1\n" \
  ".LCY_NS_%=:\n" \
  "ds_read_b128 v[20:23], v0 offset:3072\n" \
  "v_mfma_f32_16x16x32_bf16 v[32:35], v[64:67], v[8:11], 0\n" \
  "v_mfma_f32_16x16x32_bf16 v[36:39], v[96:99], v[8:11], 0\n" \
  "v_mfma_f32_16x16x32_bf16 v[40:43], v[128:131], v[8:11], 0\n" \
  "v_mfma_f32_16x16x32_bf16 v[44:47], v[160:163], v[8:11], 0\n" \
  "v_mfma_f32_16x16x32_bf16 v[48:51], v[192:195], v[8:11], 0\n" \
  "v_mfma_f32_16x16x32_bf16 v[52:55], v[224:227], v[8:11], 0\n" \
  "ds_read_b128 v[8:11], v0 offset:8192\n" \
  "v_mfma_f32_16x16x32_bf16 v[32:35], v[68:71], v[12:15], v[32:35]\n" \
  "v_mfma_f32_16x16x32_bf16 v[36:39], v[100:103], v[12:15], v[36:39]\n" \
  "v_mfma_f32_16x16x32_bf16 v[40:43], v[132:135], v[12:15], v[40:43]\n" \
  "v_mfma_f32_16x16x32_bf16 v[44:47], v[164:167], v[12:15], v[44:47]\n" \
  "v_mfma_f32_16x16x32_bf16 v[48:51], v[196:199], v[12:15], v[48:51]\n" \
  "v_mfma_f32_16x16x32_bf16 v[52:55], v[228:231], v[12:15], v[52:55]\n" \
  "s_cbranch_vccnz .LCY_D1_%=\n" \
  "global_load_lds_dwordx4 v3, s[48:49] offset:2048 sc1\n" \
  ".LCY_D1_%=:\n" \
  "ds_read_b128 v[12:15], v0 offset:9216\n" \
  "v_mfma_f32_16x16x32_bf16 v[32:35], v[72:75], v[16:19], v[32:35]\n" \
  "v_mfma_f32_16x16x32_bf16 v[36:39], v[104:107], v[16:19], v[36:39]\n" \
  "v_mfma_f32_16x16x32_bf16 v[40:43], v[136:139], v[16:19], v[40:43]\n" \
  "v_mfma_f32_16x16x32_bf16 v[44:47], v[168:171], v[16:19], v[44:47]\n" \
  "v_mfma_f32_16x16x32_bf16 v[48:51], v[200:203], v[16:19], v[48:51]\n" \
  "v_mfma_f32_16x16x32_bf16 v[52:55], v[232:235], v[16:19], v[52:55]\n" \
  "s_cbranch_vccnz .LCY_D2_%=\n" \
  "global_load_lds_dwordx4 v3, s[48:49] offset:3072 sc1\n" \
  "s_add_u32 s48, s48, 0x80000\n" \
  "s_addc_u32 s49, s49, 0\n" \
  ".LCY_D2_%=:\n" \
  "ds_read_b128 v[16:19], v0 offset:10240\n" \
  "s_waitcnt lgkmcnt(3)\n" \
  "v_mfma_f32_16x16x32_bf16 v[32:35], v[76:79], v[20:23], v[32:35]\n" \
  "v_mfma_f32_16x16x32_bf16 v[36:39], v[108:111], v[20:23], v[36:39]\n" \
  "v_mfma_f32_16x16x32_bf16 v[40:43], v[140:143], v[20:23], v[40:43]\n" \
  "v_mfma_f32_16x16x32_bf16 v[44:47], v[172:175], v[20:23], v[44:47]\n" \
  "v_mfma_f32_16x16x32_bf16 v[48:51], v[204:207], v[20:23], v[48:51]\n" \
  "v_mfma_f32_16x16x32_bf16 v[52:55], v[236:239], v[20:23], v[52:55]\n" \
  "s_cbranch_vccnz .LCY_D3_%=\n" \
  "s_mov_b32 m0, s57\n" \
  "s_add_u32 s57, s57, s65\n" \
  "global_load_lds_dwordx4 v2, s[50:51] offset:0 sc1\n" \
  ".LCY_D3_%=:\n" \
  "ds_read_b128 v[20:23], v0 offset:11264\n" \
  "s_waitcnt lgkmcnt(3)\n" \
  "v_mfma_f32_16x16x32_bf16 v[32:35], v[80:83], v[8:11], v[32:35]\n" \
  "v_mfma_f32_16x16x32_bf16 v[36:39], v[112:115], v[8:11], v[36:39]\n" \
  "v_mfma_f32_16x16x32_bf16 v[40:43], v[144:147], v[8:11], v[40:43]\n" \
  "v_mfma_f32_16x16x32_bf16 v[44:47], v[176:179], v[8:11], v[44:47]\n" \
  "v_mfma_f32_16x16x32_bf16 v[48:51], v[208:211], v[8:11], v[48:51]\n" \
  "v_mfma_f32_16x16x32_bf16 v[52:55], v[240:243], v[8:11], v[52:55]\n" \
  "s_cbranch_vccnz .LCY_D4_%=\n" \
  "global_load_lds_dwordx4 v2, s[50:51] offset:1024 sc1\n" \
  "s_sub_u32 s65, 0, s65\n" \
  "s_add_u32 s67, s67, 4096\n" \
  ".LCY_D4_%=:\n" \
  "ds_read_b128 v[8:11], v0 offset:4096\n" \
  "s_waitcnt lgkmcnt(3)\n" \
  "v_mfma_f32_16x16x32_bf16 v[32:35], v[84:87], v[12:15], v[32:35]\n" \
  "v_mfma_f32_16x16x32_bf16 v[36:39], v[116:119], v[12:15], v[36:39]\n" \
  "v_mfma_f32_16x16x32_bf16 v[40:43], v[148:151], v[12:15], v[40:43]\n" \
  "v_mfma_f32_16x16x32_bf16 v[44:47], v[180:183], v[12:15], v[44:47]\n" \
  "v_mfma_f32_16x16x32_bf16 v[48:51], v[212:215], v[12:15], v[48:51]\n" \
  "v_mfma_f32_16x16x32_bf16 v[52:55], v[244:247], v[12:15], v[52:55]\n" \
  "s_cbranch_vccnz .LCY_D5_%=\n" \
  "global_load_lds_dwordx4 v2, s[50:51] offset:2048 sc1\n" \
  "s_sub_u32 s68, s68, 1\n" \
  "s_mov_b32 s69, 1\n" \
  ".LCY_D5_%=:\n" \
  "ds_read_b128 v[12:15], v0 offset:5120\n" \
  "s_waitcnt lgkmcnt(3)\n" \
  "v_mfma_f32_16x16x32_bf16 v[32:35], v[88:91], v[16:19], v[32:35]\n" \
  "v_mfma_f32_16x16x32_bf16 v[36:39], v[120:123], v[16:19], v[36:39]\n" \
  "v_mfma_f32_16x16x32_bf16 v[40:43], v[152:155], v[16:19], v[40:43]\n" \
  "v_mfma_f32_16x16x32_bf16 v[44:47], v[184:187], v[16:19], v[44:47]\n" \
  "v_mfma_f32_16x16x32_bf16 v[48:51], v[216:219], v[16:19], v[48:51]\n" \
  "v_mfma_f32_16x16x32_bf16 v[52:55], v[248:251], v[16:19], v[52:55]\n" \
  "s_cbranch_vccnz .LCY_D6_%=\n" \
  "global_load_lds_dwordx4 v2, s[50:51] offset:3072 sc1\n" \
  "s_add_u32 s50, s50, 0x40000\n" \
  "s_addc_u32 s51, s51, 0\n" \
  ".LCY_D6_%=:\n" \
  "ds_read_b128 v[16:19], v0 offset:6144\n" \
  "s_waitcnt lgkmcnt(3)\n" \
  "v_mfma_f32_16x16x32_bf16 v[32:35], v[92:95], v[20:23], v[32:35]\n" \
  "v_mfma_f32_16x16x32_bf16 v[36:39], v[124:127], v[20:23], v[36:39]\n" \
  "v_mfma_f32_16x16x32_bf16 v[40:43], v[156:159], v[20:23], v[40:43]\n" \
  "v_mfma_f32_16x16x32_bf16 v[44:47], v[188:191], v[20:23], v[44:47]\n" \
  "v_mfma_f32_16x16x32_bf16 v[48:51], v[220:223], v[20:23], v[48:51]\n" \
  "v_mfma_f32_16x16x32_bf16 v[52:55], v[252:255], v[20:23], v[52:55]\n" \
  "ds_read_b128 v[20:23], v0 offset:7168\n" \
  "s_waitcnt lgkmcnt(3)\n" \
  "s_nop 1\n" \
  "v_cvt_pk_bf16_f32 v24, v32, v33\n" \
  "v_cvt_pk_bf16_f32 v25, v34, v35\n" \
  "v_cvt_pk_bf16_f32 v26, v36, v37\n" \
  "v_cvt_pk_bf16_f32 v27, v38, v39\n" \
  "s_nop 0\n" \
  "v_mfma_f32_16x16x32_bf16 v[32:35], v[64:67], v[8:11], 0\n" \
  "v_mfma_f32_16x16x32_bf16 v[36:39], v[96:99], v[8:11], 0\n" \
  "v_and_or_b32 v24, v24, -2, s59\n" \
  "buffer_store_dwordx4 v[24:27], v1, s[40:43], s54 offen offset:0 sc1\n" \
  "v_cvt_pk_bf16_f32 v28, v40, v41\n" \
  "v_cvt_pk_bf16_f32 v29, v42, v43\n" \
  "v_cvt_pk_bf16_f32 v30, v44, v45\n" \
  "v_cvt_pk_bf16_f32 v31, v46, v47\n" \
  "s_nop 0\n" \
  "v_mfma_f32_16x16x32_bf16 v[40:43], v[128:131], v[8:11], 0\n" \
  "v_mfma_f32_16x16x32_bf16 v[44:47], v[160:163], v[8:11], 0\n" \
  "v_and_or_b32 v28, v28, -2, s59\n" \
  "buffer_store_dwordx4 v[28:31], v1, s[40:43], s54 offen offset:128 sc1\n" \
  "v_cvt_pk_bf16_f32 v56, v48, v49\n" \
  "v_cvt_pk_bf16_f32 v57, v50, v51\n" \
  "v_cvt_pk_bf16_f32 v58, v52, v53\n" \
  "v_cvt_pk_bf16_f32 v59, v54, v55\n" \
  "s_nop 0\n" \
  "v_mfma_f32_16x16x32_bf16 v[48:51], v[192:195], v[8:11], 0\n" \
  "v_mfma_f32_16x16x32_bf16 v[52:55], v[224:227], v[8:11], 0\n" \
  "v_and_or_b32 v56, v56, -2, s59\n" \
  "buffer_store_dwordx4 v[56:59], v1, s[40:43], s54 offen offset:256 sc1\n" \
  "ds_read_b128 v[8:11], v0 offset:12288\n" \
  "s_waitcnt lgkmcnt(3)\n" \
  "v_mfma_f32_16x16x32_bf16 v[32:35], v[68:71], v[12:15], v[32:35]\n" \
  "v_mfma_f32_16x16x32_bf16 v[36:39], v[100:103], v[12:15], v[36:39]\n" \
  "v_mfma_f32_16x16x32_bf16 v[40:43], v[132:135], v[12:15], v[40:43]\n" \
  "v_mfma_f32_16x16x32_bf16 v[44:47], v[164:167], v[12:15], v[44:47]\n" \
  "v_mfma_f32_16x16x32_bf16 v[48:51], v[196:199], v[12:15], v[48:51]\n" \
  "v_mfma_f32_16x16x32_bf16 v[52:55], v[228:231], v[12:15], v[52:55]\n" \
  "ds_read_b128 v[12:15], v0 offset:13312\n" \
  "s_waitcnt lgkmcnt(3)\n" \
  "v_mfma_f32_16x16x32_bf16 v[32:35], v[72:75], v[16:19], v[32:35]\n" \
  "v_mfma_f32_16x16x32_bf16 v[36:39], v[104:107], v[16:19], v[36:39]\n" \
  "v_mfma_f32_16x16x32_bf16 v[40:43], v[136:139], v[16:19], v[40:43]\n" \
  "v_mfma_f32_16x16x32_bf16 v[44:47], v[168:171], v[16:19], v[44:47]\n" \
  "v_mfma_f32_16x16x32_bf16 v[48:51], v[200:203], v[16:19], v[48:51]\n" \
  "v_mfma_f32_16x16x32_bf16 v[52:55], v[232:235], v[16:19], v[52:55]\n" \
  "ds_read_b128 v[16:19], v0 offset:14336\n" \
  "s_waitcnt lgkmcnt(3)\n" \
  "v_mfma_f32_16x16x32_bf16 v[32:35], v[76:79], v[20:23], v[32:35]\n" \
  "v_mfma_f32_16x16x32_bf16 v[36:39], v[108:111], v[20:23], v[36:39]\n" \
  "v_mfma_f32_16x16x32_bf16 v[40:43], v[140:143], v[20:23], v[40:43]\n" \
  "v_mfma_f32_16x16x32_bf16 v[44:47], v[172:175], v[20:23], v[44:47]\n" \
  "v_mfma_f32_16x16x32_bf16 v[48:51], v[204:207], v[20:23], v[48:51]\n" \
  "v_mfma_f32_16x16x32_bf16 v[52:55], v[236:239], v[20:23], v[52:55]\n" \
  "ds_read_b128 v[20:23], v0 offset:15360\n" \
  "s_waitcnt lgkmcnt(3)\n" \
  "v_mfma_f32_16x16x32_bf16 v[32:35], v[80:83], v[8:11], v[32:35]\n" \
  "v_mfma_f32_16x16x32_bf16 v[36:39], v[112:115], v[8:11], v[36:39]\n" \
  "v_mfma_f32_16x16x32_bf16 v[40:43], v[144:147], v[8:11], v[40:43]\n" \
  "v_mfma_f32_16x16x32_bf16 v[44:47], v[176:179], v[8:11], v[44:47]\n" \
  "v_mfma_f32_16x16x32_bf16 v[48:51], v[208:211], v[8:11], v[48:51]\n" \
  "v_mfma_f32_16x16x32_bf16 v[52:55], v[240:243], v[8:11], v[52:55]\n" \
  "s_waitcnt lgkmcnt(2)\n" \
  "v_mfma_f32_16x16x32_bf16 v[32:35], v[84:87], v[12:15], v[32:35]\n" \
  "v_mfma_f32_16x16x32_bf16 v[36:39], v[116:119], v[12:15], v[36:39]\n" \
  "v_mfma_f32_16x16x32_bf16 v[40:43], v[148:151], v[12:15], v[40:43]\n" \
  "v_mfma_f32_16x16x32_bf16 v[44:47], v[180:183], v[12:15], v[44:47]\n" \
  "v_mfma_f32_16x16x32_bf16 v[48:51], v[212:215], v[12:15], v[48:51]\n" \
  "v_mfma_f32_16x16x32_bf16 v[52:55], v[244:247], v[12:15], v[52:55]\n" \
  "s_waitcnt lgkmcnt(1)\n" \
  "v_mfma_f32_16x16x32_bf16 v[32:35], v[88:91], v[16:19], v[32:35]\n" \
  "v_mfma_f32_16x16x32_bf16 v[36:39], v[120:123], v[16:19], v[36:39]\n" \
  "v_mfma_f32_16x16x32_bf16 v[40:43], v[152:155], v[16:19], v[40:43]\n" \
  "v_mfma_f32_16x16x32_bf16 v[44:47], v[184:187], v[16:19], v[44:47]\n" \
  "v_mfma_f32_16x16x32_bf16 v[48:51], v[216:219], v[16:19], v[48:51]\n" \
  "v_mfma_f32_16x16x32_bf16 v[52:55], v[248:251], v[16:19], v[52:55]\n" \
  "s_cbranch_vccnz .LCY_NT_%=\n" \
  "v_mov_b32 v4, 0\n" \
  "buffer_load_dword v4, v60, s[44:47], s67 offen sc1\n" \
  ".LCY_NT_%=:\n" \
  "s_waitcnt lgkmcnt(0)\n" \
  "v_mfma_f32_16x16x32_bf16 v[32:35], v[92:95], v[20:23], v[32:35]\n" \
  "v_mfma_f32_16x16x32_bf16 v[36:39], v[124:127], v[20:23], v[36:39]\n" \
  "v_mfma_f32_16x16x32_bf16 v[40:43], v[156:159], v[20:23], v[40:43]\n" \
  "v_mfma_f32_16x16x32_bf16 v[44:47], v[188:191], v[20:23], v[44:47]\n" \
  "v_mfma_f32_16x16x32_bf16 v[48:51], v[220:223], v[20:23], v[48:51]\n" \
  "v_mfma_f32_16x16x32_bf16 v[52:55], v[252:255], v[20:23], v[52:55]\n" \
  "v_add_u32 v0, s66, v0\n" \
  "s_sub_u32 s66, 0, s66\n" \
  "s_add_u32 s54, s54, 0x80000\n" \
  "s_bitset0_b32 s54, 22\n" \
  "s_mov_b32 s62, 0x40000\n" \
  "s_nop 0\n" \
  "v_cvt_pk_bf16_f32 v24, v32, v33\n" \
  "v_cvt_pk_bf16_f32 v25, v34, v35\n" \
  "v_cvt_pk_bf16_f32 v26, v36, v37\n" \
  "v_cvt_pk_bf16_f32 v27, v38, v39\n" \
  "v_and_or_b32 v24, v24, -2, s59\n" \
  "buffer_store_dwordx4 v[24:27], v1, s[40:43], s64 offen offset:0 sc1\n" \
  "v_cvt_pk_bf16_f32 v28, v40, v41\n" \
  "v_cvt_pk_bf16_f32 v29, v42, v43\n" \
  "v_cvt_pk_bf16_f32 v30, v44, v45\n" \
  "v_cvt_pk_bf16_f32 v31, v46, v47\n" \
  "v_and_or_b32 v28, v28, -2, s59\n" \
  "buffer_store_dwordx4 v[28:31], v1, s[40:43], s64 offen offset:128 sc1\n" \
  "v_cvt_pk_bf16_f32 v56, v48, v49\n" \
  "v_cvt_pk_bf16_f32 v57, v50, v51\n" \
  "v_cvt_pk_bf16_f32 v58, v52, v53\n" \
  "v_cvt_pk_bf16_f32 v59, v54, v55\n" \
  "v_and_or_b32 v56, v56, -2, s59\n" \
  "buffer_store_dwordx4 v[56:59], v1, s[40:43], s64 offen offset:256 sc1\n" \
  "s_cbranch_vccnz .LCY_ND_%=\n" \
  "s_cmp_eq_u32 s68, 0\n" \
  "s_cselect_b32 s72, 1, 0\n" \
  "s_cbranch_scc1 .LCY_DEF_%=\n" \
  "s_waitcnt vmcnt(0)\n" \
  "v_mov_b32 v6, s55\n" \
  "s_mov_b64 exec, 1\n" \
  "ds_add_u32 v5, v6\n" \
  "s_mov_b64 exec, -1\n" \
  "s_mov_b32 s69, 0\n" \
  "s_branch .LCY_WC_%=\n" \
  ".LCY_ND_%=:\n" \
  "v_mov_b32 v4, 0\n" \
  "buffer_load_dword v4, v60, s[44:47], s67 offen sc1\n" \
  "s_waitcnt vmcnt(0)\n" \
  ".LCY_WC_%=:\n" \
  ".LCY_DEF_%=:\n" \
  "s_sub_u32 s60, s60, 1\n" \
  "s_cmp_eq_u32 s60, 0\n" \
  "s_cbranch_scc1 .LCY_END_%=\n" \
  ".LCY_NEED_%=:\n" \
  "s_cmp_eq_u32 s68, 0\n" \
  "s_cbranch_scc1 .LCY_BAR_%=\n" \
  "v_cmp_eq_u32 vcc, 0, v4\n" \
  "s_cmp_eq_u64 vcc, 0\n" \
  "s_cbranch_scc1 .LCY_GET_%=\n" \
  "s_cmp_ge_u32 s67, 0x40000\n" \
  "s_cbranch_scc1 .LCY_BAR_%=\n" \
  "s_sleep 4\n" \
  "ds_read_b32 v6, v5 offset:16\n" \
  "s_waitcnt lgkmcnt(0)\n" \
  "v_readfirstlane_b32 s63, v6\n" \
  "s_add_u32 s63, s63, s60\n" \
  "s_cmp_ge_u32 s63, 66\n" \
  "s_cbranch_scc1 .LCY_BAR_%=\n" \
  ".LCY_ASK_%=:\n" \
  "v_mov_b32 v4, 0\n" \
  "buffer_load_dword v4, v60, s[44:47], s67 offen sc1\n" \
  "s_sub_u32 s62, s62, 1\n" \
  "s_cbranch_scc1 .LCY_END_%=\n" \
  "s_waitcnt vmcnt(0)\n" \
  "s_branch .LCY_NEED_%=\n" \
  ".LCY_GET_%=:\n" \
  "v_lshlrev_b32 v6, 16, s55\n" \
  "s_mov_b64 exec, 1\n" \
  "ds_add_u32 v5, v6\n" \
  "s_mov_b64 exec, -1\n" \
  "s_mov_b32 m0, s56\n" \
  "s_add_u32 s71, s56, 4096\n" \
  "global_load_lds_dwordx4 v2, s[48:49] offset:0 sc1\n" \
  "global_load_lds_dwordx4 v2, s[48:49] offset:1024 sc1\n" \
  "global_load_lds_dwordx4 v2, s[48:49] offset:2048 sc1\n" \
  "global_load_lds_dwordx4 v2, s[48:49] offset:3072 sc1\n" \
  "s_mov_b32 m0, s71\n" \
  "s_add_u32 s56, s56, s65\n" \
  "global_load_lds_dwordx4 v3, s[48:49] offset:0 sc1\n" \
  "global_load_lds_dwordx4 v3, s[48:49] offset:1024 sc1\n" \
  "global_load_lds_dwordx4 v3, s[48:49] offset:2048 sc1\n" \
  "global_load_lds_dwordx4 v3, s[48:49] offset:3072 sc1\n" \
  "s_add_u32 s48, s48, 0x80000\n" \
  "s_addc_u32 s49, s49, 0\n" \
  "s_mov_b32 m0, s57\n" \
  "s_add_u32 s57, s57, s65\n" \
  "global_load_lds_dwordx4 v2, s[50:51] offset:0 sc1\n" \
  "global_load_lds_dwordx4 v2, s[50:51] offset:1024 sc1\n" \
  "s_sub_u32 s65, 0, s65\n" \
  "s_add_u32 s67, s67, 4096\n" \
  "global_load_lds_dwordx4 v2, s[50:51] offset:2048 sc1\n" \
  "s_sub_u32 s68, s68, 1\n" \
  "s_mov_b32 s69, 1\n" \
  "global_load_lds_dwordx4 v2, s[50:51] offset:3072 sc1\n" \
  "s_add_u32 s50, s50, 0x40000\n" \
  "s_addc_u32 s51, s51, 0\n" \
  "v_mov_b32 v4, 0\n" \
  "buffer_load_dword v4, v60, s[44:47], s67 offen sc1\n" \
  "s_mov_b32 s61, 0\n" \
  "s_cmp_eq_u32 s68, 0\n" \
  "s_cbranch_scc1 .LCY_BAR_%=\n" \
  "s_waitcnt vmcnt(0)\n" \
  "v_mov_b32 v6, s55\n" \
  "s_mov_b64 exec, 1\n" \
  "ds_add_u32 v5, v6\n" \
  "s_mov_b64 exec, -1\n" \
  "s_mov_b32 s69, 0\n" \
  "s_branch .LCY_NEED_%=\n" \
  ".LCY_END_%=:\n" \
  "s_endpgm\n"
template <bool AT>
__device__ __forceinline__ void role_aw_t(const MegaArgs& A, char* smem, const int L, const int a, const u64 tag) {
  const int w = A.wv;
  const int l = lane_id(), c = l & 15, g = l >> 4;
  const int qt = w & 1, hh = (w >> 1) & 1, h = 2 * a + hh;
  char* ws = A.ws;
  unsigned* FL = (unsigned*)(ws + OFF_FL);
  const unsigned sb = lds_base(smem);
  if (w == 0 && l < 8) reinterpret_cast<unsigned*>(smem + AW_CNT)[l] = 0u;
  constexpr int NP = AT ? 1 : 3;
  const int cp0 = AT ? w : 3 * w - 8;
  v8u wo[2 * NP][4];
  {
    const int q = (l >> 2) & 3, p = l & 3, f = q | ((g & 1) << 2);
    const unsigned ra = sb + (4 * g + q) * 256 + (p & 1) * 16;
#define PW_CLEAR(n)                                                                                                         \
  _Pragma("unroll") for (int i = 0; i < 16; ++i)                                                                            \
    ST128((v4u{0u, 0u, 0u, 0u}), MKRS(ws + OFF_PW, SZ_PW), l * 16 + (i & 3) * 1024,                                         \
          ((((n) >> RGB) * 64 + ((n) & ((1 << RGB) - 1))) * 32 + 2 * a + qt) * 16384 + (i >> 2) * 4096);
    if (EF && AT && L) { PW_CLEAR(hh) }
#pragma unroll
    for (int r = 0; r < 2; ++r) {
#pragma unroll
      for (int k = 0; k < 2; ++k) {
        const char* base_ = (const char*)A.Wo + ((size_t)((2 * a + r) * DH + 64 * (w & 1) + 32 * k) * DM + 128 * (w >> 1)) * 2;
#pragma unroll
        for (int i = 0; i < 8; ++i) {
          const int fd = g | ((i & 1) << 2);
          glds16(base_, (unsigned)((4 * i + g) * (DM * 2) + ((c ^ (fd << 1)) << 4)), sb + w * 16384 + k * 8192 + i * 1024, WNT & 16);
        }
      }
      SST(4 + 3 * r);
      if (r == 1) ready_wait(A, tag, ready_look(A));
      WAIT_VM(0);
      if (EF && AT && r == 0 && L) {
        ready_wait(A, tag, ready_look(A));
        PW_FREE(0, hh, 2 * a + qt);
      }
      RAW_BAR();
      SST(5 + 3 * r);
#pragma unroll
      for (int k2 = 0; k2 < 2; ++k2) {
#pragma unroll
        for (int ct = 0; ct < 2 * NP; ++ct) {
          const int cp = cp0 + (ct >> 1);
          const unsigned a_ = ra + (cp >> 2) * 32768 + k2 * 16384 + ((2 * (cp & 3) + (p >> 1)) ^ f) * 32 + (ct & 1) * 8;
          const v2u r0 = trd(a_), r1 = trd(a_ + 4096), r2 = trd(a_ + 8192), r3 = trd(a_ + 12288);
          wo[ct][2 * r + k2] = v8u{r0.x, r0.y, r1.x, r1.y, r2.x, r2.y, r3.x, r3.y};
          asm volatile("" : "+v"(wo[ct][2 * r + k2]));
        }
      }
      RAW_BAR();
      SST(6 + 3 * r);
    }
  }
  WSTAMP2();
  if (EF && L) {
    const unsigned t0 = now10();
#pragma unroll 1
    for (int n = 2 + (w >> 1); n < (2 << RGB); n += 4) {
      gate(t0 + (n - 2) * PCD);
      PW_CLEAR(n)
      DRAIN();
      PW_FREE(n >> RGB, n & ((1 << RGB) - 1), 2 * a + qt);
    }
  }
  const v4u rsW = mk_srd(ws + OFF_PW, SZ_PW);
  const int dp = (FL_FP - FL_FQ + (L * 16 + a - L * 64 * 32 - h) * 32 + 2 * hh) * 4;
  const v4u rsF = mk_srd(FL + FL_FQ + (L * 64 * 32 + h) * 32, (qt ? 0 : dp) + 64 * 4096 - 128);
  const char* p0 = AT ? ws + OFF_MEMK + L * SZ_MEM + h * 2048 : ws + OFF_QK + L * SZ_QK + ((size_t)h * 16 + 8 * qt) * 1024;
  const char* p1 = AT ? (const char*)(FL + FL_FK + L * 32 + (h >> 3) * 64 + (h & 7) * 2) : ws + OFF_VF + L * SZ_O + ((size_t)h * 8 + 4 * qt) * 1024;
  const unsigned* pFW = EF && AT ? FL + FL_FD + L * 32 : FL + FL_FW + (L * 64 * 16 + (a >> 2)) * 32 + (a & 3) + (EF ? (w & 3) * 128 : 0);
  const unsigned tk = sb + AW_TK + hh * 24 * 1024, voff = (c >> 1) * 2048 + (c & 1) * 64 + g * 16;
  const char* dbg = ws + WS_TOTAL + A.vb * 64 * 8;
#define AW_SOPS                                                                                                                 \
  "{s[40:43]}"(rsW), "{s[44:47]}"(rsF), "{s[48:49]}"(p0), "{s[50:51]}"(p1), "{s[52:53]}"(pFW),                                   \
      "{s54}"((L * 64 * 16 + a) * 32768 + cp0 * 128 + (AT ? qt * 16384 : 0)), "{s55}"(1 << (8 * qt)), "{s56}"(tk + qt * 8192), "{s57}"(tk + 16384 + qt * 4096),                  \
      "{s58}"(sb + AW_CNT + 8 + 4 * hh), "{s59}"(sb + AW_CNT), "{s60}"(NSEG), "{s[36:37]}"(dbg)                                  \
      : "memory", "vcc", "scc", "s80"
  if constexpr (AT) {
    const v8u pv = {tk + l * 16, (c & 8) ? 0xEEEEu : 0x4444u, (l & 31) * 4u, (unsigned)(OFF_MEMV - OFF_MEMK) + c * 32 + (g & 1) * 16 + (g >> 1) * 1024,
                    voff, (unsigned)l * 16, tk + qt * 4096 + l * 16, sb + AW_SO + w * 4096 + l * 16};
    const unsigned neg = 0xff61b1e6u;
    v8u bt;
#pragma unroll
    for (int i = 0; i < 4; ++i) {
      const unsigned d = 4 * g + i > c ? neg : 0u;
      bt[i] = qt ? 0u : d;
      bt[4 + i] = qt ? d : neg;
    }
    asm volatile("s_mov_b64 exec, -1\ns_waitcnt lgkmcnt(0)\ns_nop 4\n" AW_ASM_X ".LKG_LAST:\n"
                 ::"{v[192:199]}"(wo[0][0]), "{v[200:207]}"(wo[0][1]), "{v[208:215]}"(wo[0][2]), "{v[216:223]}"(wo[0][3]),
                 "{v[224:231]}"(wo[1][0]), "{v[232:239]}"(wo[1][1]), "{v[240:247]}"(wo[1][2]), "{v[248:255]}"(wo[1][3]),
                 "{v[0:7]}"(pv), "{v[8:15]}"(bt), AW_SOPS);
  } else {
    const v4u pv = {sb + AW_SO + l * 16, voff, (unsigned)l * 16, (l & 2) ? 16u + 8 * qt : qt ? 8u * (l & 1) : dp + 4 * (l & 1)};
    asm volatile("s_mov_b64 exec, -1\ns_waitcnt vmcnt(0) lgkmcnt(0)\ns_nop 4\n" AW_ASM_Y
                 ::"{v[64:71]}"(wo[0][0]), "{v[72:79]}"(wo[0][1]), "{v[80:87]}"(wo[0][2]), "{v[88:95]}"(wo[0][3]),
                 "{v[96:103]}"(wo[1][0]), "{v[104:111]}"(wo[1][1]), "{v[112:119]}"(wo[1][2]), "{v[120:127]}"(wo[1][3]),
                 "{v[128:135]}"(wo[2][0]), "{v[136:143]}"(wo[2][1]), "{v[144:151]}"(wo[2][2]), "{v[152:159]}"(wo[2][3]),
                 "{v[160:167]}"(wo[3][0]), "{v[168:175]}"(wo[3][1]), "{v[176:183]}"(wo[3][2]), "{v[184:191]}"(wo[3][3]),
                 "{v[192:199]}"(wo[4][0]), "{v[200:207]}"(wo[4][1]), "{v[208:215]}"(wo[4][2]), "{v[216:223]}"(wo[4][3]),
                 "{v[224:231]}"(wo[5][0]), "{v[232:239]}"(wo[5][1]), "{v[240:247]}"(wo[5][2]), "{v[248:255]}"(wo[5][3]),
                 "{v[0:3]}"(pv), AW_SOPS);
  }
#undef AW_SOPS
}
__device__ __forceinline__ void role_aw(const MegaArgs& A, char* smem, const int L, const int a, const u64 tag) {
  if (A.wv < 4) role_aw_t<true>(A, smem, L, a, tag);
  else role_aw_t<false>(A, smem, L, a, tag);
}

#define P_ASM \
  "s_mov_b32 s68, 0\n" \
  "s_mov_b32 s66, 3\n" \
  "s_mov_b32 s67, 2\n" \
  "s_mov_b32 s72, 0\n" \
  "v_mov_b32 v7, 1\n" \
  "v_mov_b32 v6, s70\n" \
  "v_mov_b32 v5, 0\n" \
  "v_add_u32 v10, 4096, v2\n" \
  ".p2align 6\n" \
  ".LCP_TOP_%=:\n" \
  "ds_read_b128 v[28:31], v1 offset:0\n" \
  "ds_read_b128 v[32:35], v1 offset:1024\n" \
  "s_add_u32 s66, s66, 3\n" \
  "s_add_u32 s67, s67, 2\n" \
  "s_mov_b32 s69, 0x400000\n" \
  "v_xor_b32 v0, 0x8000, v0\n" \
  "v_xor_b32 v3, 0x100, v3\n" \
  "v_readfirstlane_b32 s71, v5\n" \
  "s_cmp_ge_u32 s71, s66\n" \
  "s_cbranch_scc1 .LCP_GO_%=\n" \
  "s_cmp_eq_u32 s68, 1\n" \
  "s_cbranch_scc0 .LCP_X1_%=\n" \
  "s_waitcnt vmcnt(0)\n" \
  "s_mov_b64 exec, 1\n" \
  "buffer_store_dword v7, off, s[56:59], s72 sc1\n" \
  "s_mov_b64 exec, -1\n" \
  "s_add_u32 s72, s72, 0x1000\n" \
  "s_mov_b32 s68, 0\n" \
  ".LCP_X1_%=:\n" \
  ".LCP_S2_%=:\n" \
  "s_sleep 1\n" \
  "ds_read_b32 v5, v6 offset:0\n" \
  "s_sub_u32 s69, s69, 1\n" \
  "s_cbranch_scc1 .LCP_END_%=\n" \
  "s_waitcnt lgkmcnt(0)\n" \
  "v_readfirstlane_b32 s71, v5\n" \
  "s_cmp_ge_u32 s71, s66\n" \
  "s_cbranch_scc1 .LCP_GO_%=\n" \
  "s_branch .LCP_S2_%=\n" \
  ".LCP_GO_%=:\n" \
  "ds_read_b128 v[12:15], v0 offset:0\n" \
  "ds_read_b128 v[16:19], v0 offset:2048\n" \
  "ds_read_b128 v[20:23], v0 offset:4096\n" \
  "ds_read_b128 v[24:27], v0 offset:6144\n" \
  "ds_read_b128 v[36:39], v1 offset:2048\n" \
  "s_waitcnt lgkmcnt(4)\n" \
  "v_mfma_f32_16x16x32_bf16 v[48:51], v[64:67], v[12:15], 0\n" \
  "v_mfma_f32_16x16x32_bf16 v[52:55], v[128:131], v[12:15], 0\n" \
  "v_mfma_f32_16x16x32_bf16 v[56:59], v[192:195], v[12:15], 0\n" \
  "v_mfma_f32_16x16x32_bf16 v[60:63], v[28:31], v[12:15], 0\n" \
  "ds_read_b128 v[12:15], v0 offset:8192\n" \
  "ds_read_b128 v[28:31], v1 offset:3072\n" \
  "s_waitcnt lgkmcnt(5)\n" \
  "v_mfma_f32_16x16x32_bf16 v[48:51], v[68:71], v[16:19], v[48:51]\n" \
  "v_mfma_f32_16x16x32_bf16 v[52:55], v[132:135], v[16:19], v[52:55]\n" \
  "v_mfma_f32_16x16x32_bf16 v[56:59], v[196:199], v[16:19], v[56:59]\n" \
  "v_mfma_f32_16x16x32_bf16 v[60:63], v[32:35], v[16:19], v[60:63]\n" \
  "ds_read_b128 v[16:19], v0 offset:10240\n" \
  "ds_read_b128 v[32:35], v1 offset:4096\n" \
  "s_waitcnt lgkmcnt(6)\n" \
  "v_mfma_f32_16x16x32_bf16 v[48:51], v[72:75], v[20:23], v[48:51]\n" \
  "v_mfma_f32_16x16x32_bf16 v[52:55], v[136:139], v[20:23], v[52:55]\n" \
  "v_mfma_f32_16x16x32_bf16 v[56:59], v[200:203], v[20:23], v[56:59]\n" \
  "s_waitcnt lgkmcnt(4)\n" \
  "v_mfma_f32_16x16x32_bf16 v[60:63], v[36:39], v[20:23], v[60:63]\n" \
  "ds_read_b128 v[20:23], v0 offset:12288\n" \
  "ds_read_b128 v[36:39], v1 offset:5120\n" \
  "v_mfma_f32_16x16x32_bf16 v[48:51], v[76:79], v[24:27], v[48:51]\n" \
  "v_mfma_f32_16x16x32_bf16 v[52:55], v[140:143], v[24:27], v[52:55]\n" \
  "v_mfma_f32_16x16x32_bf16 v[56:59], v[204:207], v[24:27], v[56:59]\n" \
  "s_waitcnt lgkmcnt(4)\n" \
  "v_mfma_f32_16x16x32_bf16 v[60:63], v[28:31], v[24:27], v[60:63]\n" \
  "ds_read_b128 v[24:27], v0 offset:14336\n" \
  "ds_read_b128 v[28:31], v1 offset:6144\n" \
  "v_mfma_f32_16x16x32_bf16 v[48:51], v[80:83], v[12:15], v[48:51]\n" \
  "v_mfma_f32_16x16x32_bf16 v[52:55], v[144:147], v[12:15], v[52:55]\n" \
  "v_mfma_f32_16x16x32_bf16 v[56:59], v[208:211], v[12:15], v[56:59]\n" \
  "s_waitcnt lgkmcnt(4)\n" \
  "v_mfma_f32_16x16x32_bf16 v[60:63], v[32:35], v[12:15], v[60:63]\n" \
  "ds_read_b128 v[12:15], v0 offset:16384\n" \
  "ds_read_b128 v[32:35], v1 offset:7168\n" \
  "v_mfma_f32_16x16x32_bf16 v[48:51], v[84:87], v[16:19], v[48:51]\n" \
  "v_mfma_f32_16x16x32_bf16 v[52:55], v[148:151], v[16:19], v[52:55]\n" \
  "v_mfma_f32_16x16x32_bf16 v[56:59], v[212:215], v[16:19], v[56:59]\n" \
  "s_waitcnt lgkmcnt(4)\n" \
  "v_mfma_f32_16x16x32_bf16 v[60:63], v[36:39], v[16:19], v[60:63]\n" \
  "ds_read_b128 v[16:19], v0 offset:18432\n" \
  "ds_read_b128 v[36:39], v1 offset:8192\n" \
  "v_mfma_f32_16x16x32_bf16 v[48:51], v[88:91], v[20:23], v[48:51]\n" \
  "v_mfma_f32_16x16x32_bf16 v[52:55], v[152:155], v[20:23], v[52:55]\n" \
  "v_mfma_f32_16x16x32_bf16 v[56:59], v[216:219], v[20:23], v[56:59]\n" \
  "s_waitcnt lgkmcnt(4)\n" \
  "v_mfma_f32_16x16x32_bf16 v[60:63], v[28:31], v[20:23], v[60:63]\n" \
  "ds_read_b128 v[20:23], v0 offset:20480\n" \
  "ds_read_b128 v[28:31], v1 offset:9216\n" \
  "v_mfma_f32_16x16x32_bf16 v[48:51], v[92:95], v[24:27], v[48:51]\n" \
  "v_mfma_f32_16x16x32_bf16 v[52:55], v[156:159], v[24:27], v[52:55]\n" \
  "v_mfma_f32_16x16x32_bf16 v[56:59], v[220:223], v[24:27], v[56:59]\n" \
  "s_waitcnt lgkmcnt(4)\n" \
  "v_mfma_f32_16x16x32_bf16 v[60:63], v[32:35], v[24:27], v[60:63]\n" \
  "ds_read_b128 v[24:27], v0 offset:22528\n" \
  "ds_read_b128 v[32:35], v1 offset:10240\n" \
  "v_mfma_f32_16x16x32_bf16 v[48:51], v[96:99], v[12:15], v[48:51]\n" \
  "v_mfma_f32_16x16x32_bf16 v[52:55], v[160:163], v[12:15], v[52:55]\n" \
  "v_mfma_f32_16x16x32_bf16 v[56:59], v[224:227], v[12:15], v[56:59]\n" \
  "s_waitcnt lgkmcnt(4)\n" \
  "v_mfma_f32_16x16x32_bf16 v[60:63], v[36:39], v[12:15], v[60:63]\n" \
  "ds_read_b128 v[12:15], v0 offset:24576\n" \
  "ds_read_b128 v[36:39], v1 offset:11264\n" \
  "v_mfma_f32_16x16x32_bf16 v[48:51], v[100:103], v[16:19], v[48:51]\n" \
  "v_mfma_f32_16x16x32_bf16 v[52:55], v[164:167], v[16:19], v[52:55]\n" \
  "v_mfma_f32_16x16x32_bf16 v[56:59], v[228:231], v[16:19], v[56:59]\n" \
  "s_waitcnt lgkmcnt(4)\n" \
  "v_mfma_f32_16x16x32_bf16 v[60:63], v[28:31], v[16:19], v[60:63]\n" \
  "ds_read_b128 v[16:19], v0 offset:26624\n" \
  "ds_read_b128 v[28:31], v1 offset:12288\n" \
  "v_mfma_f32_16x16x32_bf16 v[48:51], v[104:107], v[20:23], v[48:51]\n" \
  "v_mfma_f32_16x16x32_bf16 v[52:55], v[168:171], v[20:23], v[52:55]\n" \
  "v_mfma_f32_16x16x32_bf16 v[56:59], v[232:235], v[20:23], v[56:59]\n" \
  "s_waitcnt lgkmcnt(4)\n" \
  "v_mfma_f32_16x16x32_bf16 v[60:63], v[32:35], v[20:23], v[60:63]\n" \
  "ds_read_b128 v[20:23], v0 offset:28672\n" \
  "ds_read_b128 v[32:35], v1 offset:13312\n" \
  "v_mfma_f32_16x16x32_bf16 v[48:51], v[108:111], v[24:27], v[48:51]\n" \
  "v_mfma_f32_16x16x32_bf16 v[52:55], v[172:175], v[24:27], v[52:55]\n" \
  "v_mfma_f32_16x16x32_bf16 v[56:59], v[236:239], v[24:27], v[56:59]\n" \
  "s_waitcnt lgkmcnt(4)\n" \
  "v_mfma_f32_16x16x32_bf16 v[60:63], v[36:39], v[24:27], v[60:63]\n" \
  "s_cmp_eq_u32 s68, 1\n" \
  "s_cbranch_scc0 .LCP_X3_%=\n" \
  "s_waitcnt vmcnt(0)\n" \
  "s_mov_b64 exec, 1\n" \
  "buffer_store_dword v7, off, s[56:59], s72 sc1\n" \
  "s_mov_b64 exec, -1\n" \
  "s_add_u32 s72, s72, 0x1000\n" \
  "s_mov_b32 s68, 0\n" \
  ".LCP_X3_%=:\n" \
  "ds_read_b32 v5, v6 offset:16\n" \
  "ds_read_b128 v[24:27], v0 offset:30720\n" \
  "ds_read_b128 v[36:39], v1 offset:14336\n" \
  "v_mfma_f32_16x16x32_bf16 v[48:51], v[112:115], v[12:15], v[48:51]\n" \
  "v_mfma_f32_16x16x32_bf16 v[52:55], v[176:179], v[12:15], v[52:55]\n" \
  "v_mfma_f32_16x16x32_bf16 v[56:59], v[240:243], v[12:15], v[56:59]\n" \
  "s_waitcnt lgkmcnt(5)\n" \
  "v_mfma_f32_16x16x32_bf16 v[60:63], v[28:31], v[12:15], v[60:63]\n" \
  "ds_read_b128 v[12:15], v0 offset:256\n" \
  "ds_read_b128 v[28:31], v1 offset:15360\n" \
  "ds_read_b128 v[40:43], v4 offset:0\n" \
  "ds_read_b128 v[44:47], v4 offset:64\n" \
  "v_mfma_f32_16x16x32_bf16 v[48:51], v[116:119], v[16:19], v[48:51]\n" \
  "v_mfma_f32_16x16x32_bf16 v[52:55], v[180:183], v[16:19], v[52:55]\n" \
  "v_mfma_f32_16x16x32_bf16 v[56:59], v[244:247], v[16:19], v[56:59]\n" \
  "s_waitcnt lgkmcnt(7)\n" \
  "v_mfma_f32_16x16x32_bf16 v[60:63], v[32:35], v[16:19], v[60:63]\n" \
  "s_waitcnt lgkmcnt(6)\n" \
  "v_readfirstlane_b32 s71, v5\n" \
  "s_cmp_ge_u32 s71, s67\n" \
  "s_cbranch_scc1 .LCP_ST_%=\n" \
  ".LCP_S4_%=:\n" \
  "s_sleep 1\n" \
  "ds_read_b32 v5, v6 offset:16\n" \
  "s_sub_u32 s69, s69, 1\n" \
  "s_cbranch_scc1 .LCP_END_%=\n" \
  "s_waitcnt lgkmcnt(0)\n" \
  "v_readfirstlane_b32 s71, v5\n" \
  "s_cmp_ge_u32 s71, s67\n" \
  "s_cbranch_scc1 .LCP_ST_%=\n" \
  "s_branch .LCP_S4_%=\n" \
  ".LCP_ST_%=:\n" \
  "ds_read_b128 v[16:19], v0 offset:2304\n" \
  "ds_read_b128 v[32:35], v4 offset:128\n" \
  "ds_read2_b32 v[8:9], v3 offset0:0 offset1:32\n" \
  "v_mfma_f32_16x16x32_bf16 v[48:51], v[120:123], v[20:23], v[48:51]\n" \
  "v_mfma_f32_16x16x32_bf16 v[52:55], v[184:187], v[20:23], v[52:55]\n" \
  "v_mfma_f32_16x16x32_bf16 v[56:59], v[248:251], v[20:23], v[56:59]\n" \
  "s_waitcnt lgkmcnt(7)\n" \
  "v_mfma_f32_16x16x32_bf16 v[60:63], v[36:39], v[20:23], v[60:63]\n" \
  "ds_read_b128 v[20:23], v0 offset:4352\n" \
  "ds_read_b128 v[36:39], v4 offset:192\n" \
  "v_mfma_f32_16x16x32_bf16 v[48:51], v[124:127], v[24:27], v[48:51]\n" \
  "v_mfma_f32_16x16x32_bf16 v[52:55], v[188:191], v[24:27], v[52:55]\n" \
  "v_mfma_f32_16x16x32_bf16 v[56:59], v[252:255], v[24:27], v[56:59]\n" \
  "s_waitcnt lgkmcnt(7)\n" \
  "v_mfma_f32_16x16x32_bf16 v[60:63], v[28:31], v[24:27], v[60:63]\n" \
  "s_waitcnt lgkmcnt(0)\n" \
  "s_nop 4\n" \
  "v_fma_f32 v48, -v8, v40, v48\n" \
  "v_fma_f32 v49, -v8, v41, v49\n" \
  "v_fma_f32 v50, -v8, v42, v50\n" \
  "v_fma_f32 v51, -v8, v43, v51\n" \
  "v_fma_f32 v52, -v8, v44, v52\n" \
  "v_fma_f32 v53, -v8, v45, v53\n" \
  "v_fma_f32 v54, -v8, v46, v54\n" \
  "v_fma_f32 v55, -v8, v47, v55\n" \
  "v_fma_f32 v56, -v8, v32, v56\n" \
  "v_fma_f32 v57, -v8, v33, v57\n" \
  "v_fma_f32 v58, -v8, v34, v58\n" \
  "v_fma_f32 v59, -v8, v35, v59\n" \
  "v_fma_f32 v60, -v8, v36, v60\n" \
  "v_fma_f32 v61, -v8, v37, v61\n" \
  "v_fma_f32 v62, -v8, v38, v62\n" \
  "v_fma_f32 v63, -v8, v39, v63\n" \
  "ds_read_b128 v[32:35], v1 offset:0\n" \
  "ds_read_b128 v[36:39], v1 offset:1024\n" \
  "v_mul_f32 v48, v48, v9\n" \
  "v_mul_f32 v49, v49, v9\n" \
  "v_mul_f32 v50, v50, v9\n" \
  "v_mul_f32 v51, v51, v9\n" \
  "v_mul_f32 v52, v52, v9\n" \
  "v_mul_f32 v53, v53, v9\n" \
  "v_mul_f32 v54, v54, v9\n" \
  "v_mul_f32 v55, v55, v9\n" \
  "v_cvt_pk_bf16_f32 v40, v48, v49\n" \
  "v_cvt_pk_bf16_f32 v41, v50, v51\n" \
  "v_cvt_pk_bf16_f32 v42, v52, v53\n" \
  "v_cvt_pk_bf16_f32 v43, v54, v55\n" \
  "buffer_store_dwordx4 v[40:43], v2, s[40:43], s65 offen offset:0 sc1\n" \
  "v_mul_f32 v56, v56, v9\n" \
  "v_mul_f32 v57, v57, v9\n" \
  "v_mul_f32 v58, v58, v9\n" \
  "v_mul_f32 v59, v59, v9\n" \
  "v_mul_f32 v60, v60, v9\n" \
  "v_mul_f32 v61, v61, v9\n" \
  "v_mul_f32 v62, v62, v9\n" \
  "v_mul_f32 v63, v63, v9\n" \
  "v_cvt_pk_bf16_f32 v44, v56, v57\n" \
  "v_cvt_pk_bf16_f32 v45, v58, v59\n" \
  "v_cvt_pk_bf16_f32 v46, v60, v61\n" \
  "v_cvt_pk_bf16_f32 v47, v62, v63\n" \
  "buffer_store_dwordx4 v[44:47], v2, s[40:43], s65 offen offset:1024 sc1\n" \
  "ds_read_b128 v[24:27], v0 offset:6400\n" \
  "ds_read_b128 v[28:31], v1 offset:2048\n" \
  "v_mfma_f32_16x16x32_bf16 v[48:51], v[64:67], v[12:15], 0\n" \
  "v_mfma_f32_16x16x32_bf16 v[52:55], v[128:131], v[12:15], 0\n" \
  "v_mfma_f32_16x16x32_bf16 v[56:59], v[192:195], v[12:15], 0\n" \
  "s_waitcnt lgkmcnt(3)\n" \
  "v_mfma_f32_16x16x32_bf16 v[60:63], v[32:35], v[12:15], 0\n" \
  "ds_read_b128 v[12:15], v0 offset:8448\n" \
  "ds_read_b128 v[32:35], v1 offset:3072\n" \
  "v_mfma_f32_16x16x32_bf16 v[48:51], v[68:71], v[16:19], v[48:51]\n" \
  "v_mfma_f32_16x16x32_bf16 v[52:55], v[132:135], v[16:19], v[52:55]\n" \
  "v_mfma_f32_16x16x32_bf16 v[56:59], v[196:199], v[16:19], v[56:59]\n" \
  "s_waitcnt lgkmcnt(4)\n" \
  "v_mfma_f32_16x16x32_bf16 v[60:63], v[36:39], v[16:19], v[60:63]\n" \
  "ds_read_b128 v[16:19], v0 offset:10496\n" \
  "ds_read_b128 v[36:39], v1 offset:4096\n" \
  "v_mfma_f32_16x16x32_bf16 v[48:51], v[72:75], v[20:23], v[48:51]\n" \
  "v_mfma_f32_16x16x32_bf16 v[52:55], v[136:139], v[20:23], v[52:55]\n" \
  "v_mfma_f32_16x16x32_bf16 v[56:59], v[200:203], v[20:23], v[56:59]\n" \
  "s_waitcnt lgkmcnt(4)\n" \
  "v_mfma_f32_16x16x32_bf16 v[60:63], v[28:31], v[20:23], v[60:63]\n" \
  "ds_read_b128 v[20:23], v0 offset:12544\n" \
  "ds_read_b128 v[28:31], v1 offset:5120\n" \
  "v_mfma_f32_16x16x32_bf16 v[48:51], v[76:79], v[24:27], v[48:51]\n" \
  "v_mfma_f32_16x16x32_bf16 v[52:55], v[140:143], v[24:27], v[52:55]\n" \
  "v_mfma_f32_16x16x32_bf16 v[56:59], v[204:207], v[24:27], v[56:59]\n" \
  "s_waitcnt lgkmcnt(4)\n" \
  "v_mfma_f32_16x16x32_bf16 v[60:63], v[32:35], v[24:27], v[60:63]\n" \
  "ds_read_b128 v[24:27], v0 offset:14592\n" \
  "ds_read_b128 v[32:35], v1 offset:6144\n" \
  "v_mfma_f32_16x16x32_bf16 v[48:51], v[80:83], v[12:15], v[48:51]\n" \
  "v_mfma_f32_16x16x32_bf16 v[52:55], v[144:147], v[12:15], v[52:55]\n" \
  "v_mfma_f32_16x16x32_bf16 v[56:59], v[208:211], v[12:15], v[56:59]\n" \
  "s_waitcnt lgkmcnt(4)\n" \
  "v_mfma_f32_16x16x32_bf16 v[60:63], v[36:39], v[12:15], v[60:63]\n" \
  "ds_read_b128 v[12:15], v0 offset:16640\n" \
  "ds_read_b128 v[36:39], v1 offset:7168\n" \
  "v_mfma_f32_16x16x32_bf16 v[48:51], v[84:87], v[16:19], v[48:51]\n" \
  "v_mfma_f32_16x16x32_bf16 v[52:55], v[148:151], v[16:19], v[52:55]\n" \
  "v_mfma_f32_16x16x32_bf16 v[56:59], v[212:215], v[16:19], v[56:59]\n" \
  "s_waitcnt lgkmcnt(4)\n" \
  "v_mfma_f32_16x16x32_bf16 v[60:63], v[28:31], v[16:19], v[60:63]\n" \
  "ds_read_b128 v[16:19], v0 offset:18688\n" \
  "ds_read_b128 v[28:31], v1 offset:8192\n" \
  "v_mfma_f32_16x16x32_bf16 v[48:51], v[88:91], v[20:23], v[48:51]\n" \
  "v_mfma_f32_16x16x32_bf16 v[52:55], v[152:155], v[20:23], v[52:55]\n" \
  "v_mfma_f32_16x16x32_bf16 v[56:59], v[216:219], v[20:23], v[56:59]\n" \
  "s_waitcnt lgkmcnt(4)\n" \
  "v_mfma_f32_16x16x32_bf16 v[60:63], v[32:35], v[20:23], v[60:63]\n" \
  "ds_read_b128 v[20:23], v0 offset:20736\n" \
  "ds_read_b128 v[32:35], v1 offset:9216\n" \
  "v_mfma_f32_16x16x32_bf16 v[48:51], v[92:95], v[24:27], v[48:51]\n" \
  "v_mfma_f32_16x16x32_bf16 v[52:55], v[156:159], v[24:27], v[52:55]\n" \
  "v_mfma_f32_16x16x32_bf16 v[56:59], v[220:223], v[24:27], v[56:59]\n" \
  "s_waitcnt lgkmcnt(4)\n" \
  "v_mfma_f32_16x16x32_bf16 v[60:63], v[36:39], v[24:27], v[60:63]\n" \
  "ds_read_b128 v[24:27], v0 offset:22784\n" \
  "ds_read_b128 v[36:39], v1 offset:10240\n" \
  "v_mfma_f32_16x16x32_bf16 v[48:51], v[96:99], v[12:15], v[48:51]\n" \
  "v_mfma_f32_16x16x32_bf16 v[52:55], v[160:163], v[12:15], v[52:55]\n" \
  "v_mfma_f32_16x16x32_bf16 v[56:59], v[224:227], v[12:15], v[56:59]\n" \
  "s_waitcnt lgkmcnt(4)\n" \
  "v_mfma_f32_16x16x32_bf16 v[60:63], v[28:31], v[12:15], v[60:63]\n" \
  "ds_read_b128 v[12:15], v0 offset:24832\n" \
  "ds_read_b128 v[28:31], v1 offset:11264\n" \
  "v_mfma_f32_16x16x32_bf16 v[48:51], v[100:103], v[16:19], v[48:51]\n" \
  "v_mfma_f32_16x16x32_bf16 v[52:55], v[164:167], v[16:19], v[52:55]\n" \
  "v_mfma_f32_16x16x32_bf16 v[56:59], v[228:231], v[16:19], v[56:59]\n" \
  "s_waitcnt lgkmcnt(4)\n" \
  "v_mfma_f32_16x16x32_bf16 v[60:63], v[32:35], v[16:19], v[60:63]\n" \
  "ds_read_b128 v[16:19], v0 offset:26880\n" \
  "ds_read_b128 v[32:35], v1 offset:12288\n" \
  "v_mfma_f32_16x16x32_bf16 v[48:51], v[104:107], v[20:23], v[48:51]\n" \
  "v_mfma_f32_16x16x32_bf16 v[52:55], v[168:171], v[20:23], v[52:55]\n" \
  "v_mfma_f32_16x16x32_bf16 v[56:59], v[232:235], v[20:23], v[56:59]\n" \
  "s_waitcnt lgkmcnt(4)\n" \
  "v_mfma_f32_16x16x32_bf16 v[60:63], v[36:39], v[20:23], v[60:63]\n" \
  "ds_read_b128 v[20:23], v0 offset:28928\n" \
  "ds_read_b128 v[36:39], v1 offset:13312\n" \
  "v_mfma_f32_16x16x32_bf16 v[48:51], v[108:111], v[24:27], v[48:51]\n" \
  "v_mfma_f32_16x16x32_bf16 v[52:55], v[172:175], v[24:27], v[52:55]\n" \
  "v_mfma_f32_16x16x32_bf16 v[56:59], v[236:239], v[24:27], v[56:59]\n" \
  "s_waitcnt lgkmcnt(4)\n" \
  "v_mfma_f32_16x16x32_bf16 v[60:63], v[28:31], v[24:27], v[60:63]\n" \
  "ds_read_b128 v[24:27], v0 offset:30976\n" \
  "ds_read_b128 v[28:31], v1 offset:14336\n" \
  "v_mfma_f32_16x16x32_bf16 v[48:51], v[112:115], v[12:15], v[48:51]\n" \
  "v_mfma_f32_16x16x32_bf16 v[52:55], v[176:179], v[12:15], v[52:55]\n" \
  "v_mfma_f32_16x16x32_bf16 v[56:59], v[240:243], v[12:15], v[56:59]\n" \
  "s_waitcnt lgkmcnt(4)\n" \
  "v_mfma_f32_16x16x32_bf16 v[60:63], v[32:35], v[12:15], v[60:63]\n" \
  "ds_read_b128 v[32:35], v1 offset:15360\n" \
  "ds_read_b128 v[40:43], v4 offset:0\n" \
  "ds_read_b128 v[44:47], v4 offset:64\n" \
  "v_mfma_f32_16x16x32_bf16 v[48:51], v[116:119], v[16:19], v[48:51]\n" \
  "v_mfma_f32_16x16x32_bf16 v[52:55], v[180:183], v[16:19], v[52:55]\n" \
  "v_mfma_f32_16x16x32_bf16 v[56:59], v[244:247], v[16:19], v[56:59]\n" \
  "s_waitcnt lgkmcnt(5)\n" \
  "v_mfma_f32_16x16x32_bf16 v[60:63], v[36:39], v[16:19], v[60:63]\n" \
  "ds_read_b128 v[36:39], v4 offset:128\n" \
  "ds_read2_b32 v[8:9], v3 offset0:16 offset1:48\n" \
  "v_mfma_f32_16x16x32_bf16 v[48:51], v[120:123], v[20:23], v[48:51]\n" \
  "v_mfma_f32_16x16x32_bf16 v[52:55], v[184:187], v[20:23], v[52:55]\n" \
  "v_mfma_f32_16x16x32_bf16 v[56:59], v[248:251], v[20:23], v[56:59]\n" \
  "s_waitcnt lgkmcnt(5)\n" \
  "v_mfma_f32_16x16x32_bf16 v[60:63], v[28:31], v[20:23], v[60:63]\n" \
  "ds_read_b128 v[28:31], v4 offset:192\n" \
  "s_mov_b64 exec, 1\n" \
  "ds_add_u32 v6, v7 offset:8\n" \
  "s_mov_b64 exec, -1\n" \
  "v_xor_b32 v6, 4, v6\n" \
  "v_mfma_f32_16x16x32_bf16 v[48:51], v[124:127], v[24:27], v[48:51]\n" \
  "v_mfma_f32_16x16x32_bf16 v[52:55], v[188:191], v[24:27], v[52:55]\n" \
  "v_mfma_f32_16x16x32_bf16 v[56:59], v[252:255], v[24:27], v[56:59]\n" \
  "s_waitcnt lgkmcnt(6)\n" \
  "v_mfma_f32_16x16x32_bf16 v[60:63], v[32:35], v[24:27], v[60:63]\n" \
  "s_waitcnt lgkmcnt(1)\n" \
  "s_nop 4\n" \
  "ds_read_b32 v5, v6 offset:0\n" \
  "v_fma_f32 v48, -v8, v40, v48\n" \
  "v_fma_f32 v49, -v8, v41, v49\n" \
  "v_fma_f32 v50, -v8, v42, v50\n" \
  "v_fma_f32 v51, -v8, v43, v51\n" \
  "v_fma_f32 v52, -v8, v44, v52\n" \
  "v_fma_f32 v53, -v8, v45, v53\n" \
  "v_fma_f32 v54, -v8, v46, v54\n" \
  "v_fma_f32 v55, -v8, v47, v55\n" \
  "v_fma_f32 v56, -v8, v36, v56\n" \
  "v_fma_f32 v57, -v8, v37, v57\n" \
  "v_fma_f32 v58, -v8, v38, v58\n" \
  "v_fma_f32 v59, -v8, v39, v59\n" \
  "v_fma_f32 v60, -v8, v28, v60\n" \
  "v_fma_f32 v61, -v8, v29, v61\n" \
  "v_fma_f32 v62, -v8, v30, v62\n" \
  "v_fma_f32 v63, -v8, v31, v63\n" \
  "v_mul_f32 v48, v48, v9\n" \
  "v_mul_f32 v49, v49, v9\n" \
  "v_mul_f32 v50, v50, v9\n" \
  "v_mul_f32 v51, v51, v9\n" \
  "v_mul_f32 v52, v52, v9\n" \
  "v_mul_f32 v53, v53, v9\n" \
  "v_mul_f32 v54, v54, v9\n" \
  "v_mul_f32 v55, v55, v9\n" \
  "v_cvt_pk_bf16_f32 v40, v48, v49\n" \
  "v_cvt_pk_bf16_f32 v41, v50, v51\n" \
  "v_cvt_pk_bf16_f32 v42, v52, v53\n" \
  "v_cvt_pk_bf16_f32 v43, v54, v55\n" \
  "buffer_store_dwordx4 v[40:43], v10, s[40:43], s65 offen offset:0 sc1\n" \
  "v_mul_f32 v56, v56, v9\n" \
  "v_mul_f32 v57, v57, v9\n" \
  "v_mul_f32 v58, v58, v9\n" \
  "v_mul_f32 v59, v59, v9\n" \
  "v_mul_f32 v60, v60, v9\n" \
  "v_mul_f32 v61, v61, v9\n" \
  "v_mul_f32 v62, v62, v9\n" \
  "v_mul_f32 v63, v63, v9\n" \
  "v_cvt_pk_bf16_f32 v44, v56, v57\n" \
  "v_cvt_pk_bf16_f32 v45, v58, v59\n" \
  "v_cvt_pk_bf16_f32 v46, v60, v61\n" \
  "v_cvt_pk_bf16_f32 v47, v62, v63\n" \
  "buffer_store_dwordx4 v[44:47], v10, s[40:43], s65 offen offset:1024 sc1\n" \
  "s_waitcnt lgkmcnt(0)\n" \
  "s_add_u32 s65, s65, 0x80000\n" \
  "s_sub_u32 s64, s64, 1\n" \
  "s_mov_b32 s68, 1\n" \
  "s_cmp_lg_u32 s64, 0\n" \
  "s_cbranch_scc1 .LCP_TOP_%=\n" \
  "s_waitcnt vmcnt(0)\n" \
  "s_mov_b64 exec, 1\n" \
  "buffer_store_dword v7, off, s[56:59], s72 sc1\n" \
  "s_mov_b64 exec, -1\n" \
  "s_add_u32 s72, s72, 0x1000\n" \
  "s_mov_b32 s68, 0\n" \
  ".LCP_END_%=:\n" \
  "s_endpgm\n"
#define V_ASM \
  "s_mov_b32 s68, 0\n" \
  "s_mov_b32 s66, 3\n" \
  "s_mov_b32 s67, 2\n" \
  "s_mov_b32 s72, 0\n" \
  "v_mov_b32 v7, 1\n" \
  "v_mov_b32 v6, s70\n" \
  "v_mov_b32 v5, 0\n" \
  "ds_read_b32 v8, v4 offset:0\n" \
  "ds_read_b32 v9, v4 offset:64\n" \
  "ds_read_b32 v10, v4 offset:128\n" \
  "ds_read_b32 v4, v4 offset:192\n" \
  "s_waitcnt lgkmcnt(0)\n" \
  ".p2align 6\n" \
  ".LCW_TOP_%=:\n" \
  "ds_read_b128 v[28:31], v1 offset:0\n" \
  "ds_read_b128 v[32:35], v1 offset:1024\n" \
  "s_add_u32 s66, s66, 3\n" \
  "s_add_u32 s67, s67, 2\n" \
  "s_mov_b32 s69, 0x400000\n" \
  "v_xor_b32 v0, 0x8000, v0\n" \
  "v_xor_b32 v3, 0x100, v3\n" \
  "v_readfirstlane_b32 s71, v5\n" \
  "s_cmp_ge_u32 s71, s66\n" \
  "s_cbranch_scc1 .LCW_GO_%=\n" \
  "s_cmp_eq_u32 s68, 1\n" \
  "s_cbranch_scc0 .LCW_X1_%=\n" \
  "s_waitcnt vmcnt(0)\n" \
  "s_mov_b64 exec, 1\n" \
  "buffer_store_dword v7, off, s[56:59], s72 sc1\n" \
  "s_mov_b64 exec, -1\n" \
  "s_add_u32 s72, s72, 0x1000\n" \
  "s_mov_b32 s68, 0\n" \
  ".LCW_X1_%=:\n" \
  ".LCW_S2_%=:\n" \
  "s_sleep 1\n" \
  "ds_read_b32 v5, v6 offset:0\n" \
  "s_sub_u32 s69, s69, 1\n" \
  "s_cbranch_scc1 .LCW_END_%=\n" \
  "s_waitcnt lgkmcnt(0)\n" \
  "v_readfirstlane_b32 s71, v5\n" \
  "s_cmp_ge_u32 s71, s66\n" \
  "s_cbranch_scc1 .LCW_GO_%=\n" \
  "s_branch .LCW_S2_%=\n" \
  ".LCW_GO_%=:\n" \
  "ds_read_b128 v[12:15], v0 offset:0\n" \
  "ds_read_b128 v[16:19], v0 offset:2048\n" \
  "ds_read_b128 v[20:23], v0 offset:4096\n" \
  "ds_read_b128 v[24:27], v0 offset:6144\n" \
  "ds_read_b128 v[36:39], v1 offset:2048\n" \
  "s_waitcnt lgkmcnt(4)\n" \
  "v_mfma_f32_16x16x32_bf16 v[42:45], v[12:15], v[64:67], 0\n" \
  "v_mfma_f32_16x16x32_bf16 v[48:51], v[12:15], v[128:131], 0\n" \
  "v_mfma_f32_16x16x32_bf16 v[54:57], v[12:15], v[192:195], 0\n" \
  "v_mfma_f32_16x16x32_bf16 v[60:63], v[12:15], v[28:31], 0\n" \
  "ds_read_b128 v[12:15], v0 offset:8192\n" \
  "ds_read_b128 v[28:31], v1 offset:3072\n" \
  "s_waitcnt lgkmcnt(5)\n" \
  "v_mfma_f32_16x16x32_bf16 v[42:45], v[16:19], v[68:71], v[42:45]\n" \
  "v_mfma_f32_16x16x32_bf16 v[48:51], v[16:19], v[132:135], v[48:51]\n" \
  "v_mfma_f32_16x16x32_bf16 v[54:57], v[16:19], v[196:199], v[54:57]\n" \
  "v_mfma_f32_16x16x32_bf16 v[60:63], v[16:19], v[32:35], v[60:63]\n" \
  "ds_read_b128 v[16:19], v0 offset:10240\n" \
  "ds_read_b128 v[32:35], v1 offset:4096\n" \
  "s_waitcnt lgkmcnt(6)\n" \
  "v_mfma_f32_16x16x32_bf16 v[42:45], v[20:23], v[72:75], v[42:45]\n" \
  "v_mfma_f32_16x16x32_bf16 v[48:51], v[20:23], v[136:139], v[48:51]\n" \
  "v_mfma_f32_16x16x32_bf16 v[54:57], v[20:23], v[200:203], v[54:57]\n" \
  "s_waitcnt lgkmcnt(4)\n" \
  "v_mfma_f32_16x16x32_bf16 v[60:63], v[20:23], v[36:39], v[60:63]\n" \
  "ds_read_b128 v[20:23], v0 offset:12288\n" \
  "ds_read_b128 v[36:39], v1 offset:5120\n" \
  "v_mfma_f32_16x16x32_bf16 v[42:45], v[24:27], v[76:79], v[42:45]\n" \
  "v_mfma_f32_16x16x32_bf16 v[48:51], v[24:27], v[140:143], v[48:51]\n" \
  "v_mfma_f32_16x16x32_bf16 v[54:57], v[24:27], v[204:207], v[54:57]\n" \
  "s_waitcnt lgkmcnt(4)\n" \
  "v_mfma_f32_16x16x32_bf16 v[60:63], v[24:27], v[28:31], v[60:63]\n" \
  "ds_read_b128 v[24:27], v0 offset:14336\n" \
  "ds_read_b128 v[28:31], v1 offset:6144\n" \
  "v_mfma_f32_16x16x32_bf16 v[42:45], v[12:15], v[80:83], v[42:45]\n" \
  "v_mfma_f32_16x16x32_bf16 v[48:51], v[12:15], v[144:147], v[48:51]\n" \
  "v_mfma_f32_16x16x32_bf16 v[54:57], v[12:15], v[208:211], v[54:57]\n" \
  "s_waitcnt lgkmcnt(4)\n" \
  "v_mfma_f32_16x16x32_bf16 v[60:63], v[12:15], v[32:35], v[60:63]\n" \
  "ds_read_b128 v[12:15], v0 offset:16384\n" \
  "ds_read_b128 v[32:35], v1 offset:7168\n" \
  "v_mfma_f32_16x16x32_bf16 v[42:45], v[16:19], v[84:87], v[42:45]\n" \
  "v_mfma_f32_16x16x32_bf16 v[48:51], v[16:19], v[148:151], v[48:51]\n" \
  "v_mfma_f32_16x16x32_bf16 v[54:57], v[16:19], v[212:215], v[54:57]\n" \
  "s_waitcnt lgkmcnt(4)\n" \
  "v_mfma_f32_16x16x32_bf16 v[60:63], v[16:19], v[36:39], v[60:63]\n" \
  "ds_read_b128 v[16:19], v0 offset:18432\n" \
  "ds_read_b128 v[36:39], v1 offset:8192\n" \
  "v_mfma_f32_16x16x32_bf16 v[42:45], v[20:23], v[88:91], v[42:45]\n" \
  "v_mfma_f32_16x16x32_bf16 v[48:51], v[20:23], v[152:155], v[48:51]\n" \
  "v_mfma_f32_16x16x32_bf16 v[54:57], v[20:23], v[216:219], v[54:57]\n" \
  "s_waitcnt lgkmcnt(4)\n" \
  "v_mfma_f32_16x16x32_bf16 v[60:63], v[20:23], v[28:31], v[60:63]\n" \
  "ds_read_b128 v[20:23], v0 offset:20480\n" \
  "ds_read_b128 v[28:31], v1 offset:9216\n" \
  "v_mfma_f32_16x16x32_bf16 v[42:45], v[24:27], v[92:95], v[42:45]\n" \
  "v_mfma_f32_16x16x32_bf16 v[48:51], v[24:27], v[156:159], v[48:51]\n" \
  "v_mfma_f32_16x16x32_bf16 v[54:57], v[24:27], v[220:223], v[54:57]\n" \
  "s_waitcnt lgkmcnt(4)\n" \
  "v_mfma_f32_16x16x32_bf16 v[60:63], v[24:27], v[32:35], v[60:63]\n" \
  "ds_read_b128 v[24:27], v0 offset:22528\n" \
  "ds_read_b128 v[32:35], v1 offset:10240\n" \
  "v_mfma_f32_16x16x32_bf16 v[42:45], v[12:15], v[96:99], v[42:45]\n" \
  "v_mfma_f32_16x16x32_bf16 v[48:51], v[12:15], v[160:163], v[48:51]\n" \
  "v_mfma_f32_16x16x32_bf16 v[54:57], v[12:15], v[224:227], v[54:57]\n" \
  "s_waitcnt lgkmcnt(4)\n" \
  "v_mfma_f32_16x16x32_bf16 v[60:63], v[12:15], v[36:39], v[60:63]\n" \
  "ds_read_b128 v[12:15], v0 offset:24576\n" \
  "ds_read_b128 v[36:39], v1 offset:11264\n" \
  "v_mfma_f32_16x16x32_bf16 v[42:45], v[16:19], v[100:103], v[42:45]\n" \
  "v_mfma_f32_16x16x32_bf16 v[48:51], v[16:19], v[164:167], v[48:51]\n" \
  "v_mfma_f32_16x16x32_bf16 v[54:57], v[16:19], v[228:231], v[54:57]\n" \
  "s_waitcnt lgkmcnt(4)\n" \
  "v_mfma_f32_16x16x32_bf16 v[60:63], v[16:19], v[28:31], v[60:63]\n" \
  "ds_read_b128 v[16:19], v0 offset:26624\n" \
  "ds_read_b128 v[28:31], v1 offset:12288\n" \
  "v_mfma_f32_16x16x32_bf16 v[42:45], v[20:23], v[104:107], v[42:45]\n" \
  "v_mfma_f32_16x16x32_bf16 v[48:51], v[20:23], v[168:171], v[48:51]\n" \
  "v_mfma_f32_16x16x32_bf16 v[54:57], v[20:23], v[232:235], v[54:57]\n" \
  "s_waitcnt lgkmcnt(4)\n" \
  "v_mfma_f32_16x16x32_bf16 v[60:63], v[20:23], v[32:35], v[60:63]\n" \
  "ds_read_b128 v[20:23], v0 offset:28672\n" \
  "ds_read_b128 v[32:35], v1 offset:13312\n" \
  "v_mfma_f32_16x16x32_bf16 v[42:45], v[24:27], v[108:111], v[42:45]\n" \
  "v_mfma_f32_16x16x32_bf16 v[48:51], v[24:27], v[172:175], v[48:51]\n" \
  "v_mfma_f32_16x16x32_bf16 v[54:57], v[24:27], v[236:239], v[54:57]\n" \
  "s_waitcnt lgkmcnt(4)\n" \
  "v_mfma_f32_16x16x32_bf16 v[60:63], v[24:27], v[36:39], v[60:63]\n" \
  "s_cmp_eq_u32 s68, 1\n" \
  "s_cbranch_scc0 .LCW_X3_%=\n" \
  "s_waitcnt vmcnt(0)\n" \
  "s_mov_b64 exec, 1\n" \
  "buffer_store_dword v7, off, s[56:59], s72 sc1\n" \
  "s_mov_b64 exec, -1\n" \
  "s_add_u32 s72, s72, 0x1000\n" \
  "s_mov_b32 s68, 0\n" \
  ".LCW_X3_%=:\n" \
  "ds_read_b32 v5, v6 offset:16\n" \
  "ds_read_b128 v[24:27], v0 offset:30720\n" \
  "ds_read_b128 v[36:39], v1 offset:14336\n" \
  "v_mfma_f32_16x16x32_bf16 v[42:45], v[12:15], v[112:115], v[42:45]\n" \
  "v_mfma_f32_16x16x32_bf16 v[48:51], v[12:15], v[176:179], v[48:51]\n" \
  "v_mfma_f32_16x16x32_bf16 v[54:57], v[12:15], v[240:243], v[54:57]\n" \
  "s_waitcnt lgkmcnt(5)\n" \
  "v_mfma_f32_16x16x32_bf16 v[60:63], v[12:15], v[28:31], v[60:63]\n" \
  "ds_read_b128 v[12:15], v0 offset:256\n" \
  "ds_read_b128 v[28:31], v1 offset:15360\n" \
  "v_mfma_f32_16x16x32_bf16 v[42:45], v[16:19], v[116:119], v[42:45]\n" \
  "v_mfma_f32_16x16x32_bf16 v[48:51], v[16:19], v[180:183], v[48:51]\n" \
  "v_mfma_f32_16x16x32_bf16 v[54:57], v[16:19], v[244:247], v[54:57]\n" \
  "s_waitcnt lgkmcnt(5)\n" \
  "v_mfma_f32_16x16x32_bf16 v[60:63], v[16:19], v[32:35], v[60:63]\n" \
  "s_waitcnt lgkmcnt(4)\n" \
  "v_readfirstlane_b32 s71, v5\n" \
  "s_cmp_ge_u32 s71, s67\n" \
  "s_cbranch_scc1 .LCW_ST_%=\n" \
  ".LCW_S4_%=:\n" \
  "s_sleep 1\n" \
  "ds_read_b32 v5, v6 offset:16\n" \
  "s_sub_u32 s69, s69, 1\n" \
  "s_cbranch_scc1 .LCW_END_%=\n" \
  "s_waitcnt lgkmcnt(0)\n" \
  "v_readfirstlane_b32 s71, v5\n" \
  "s_cmp_ge_u32 s71, s67\n" \
  "s_cbranch_scc1 .LCW_ST_%=\n" \
  "s_branch .LCW_S4_%=\n" \
  ".LCW_ST_%=:\n" \
  "ds_read_b128 v[16:19], v0 offset:2304\n" \
  "ds_read_b128 v[32:35], v3 offset:0\n" \
  "v_mfma_f32_16x16x32_bf16 v[42:45], v[20:23], v[120:123], v[42:45]\n" \
  "v_mfma_f32_16x16x32_bf16 v[48:51], v[20:23], v[184:187], v[48:51]\n" \
  "v_mfma_f32_16x16x32_bf16 v[54:57], v[20:23], v[248:251], v[54:57]\n" \
  "s_waitcnt lgkmcnt(4)\n" \
  "v_mfma_f32_16x16x32_bf16 v[60:63], v[20:23], v[36:39], v[60:63]\n" \
  "ds_read_b128 v[20:23], v0 offset:4352\n" \
  "ds_read_b128 v[36:39], v3 offset:128\n" \
  "v_mfma_f32_16x16x32_bf16 v[42:45], v[24:27], v[124:127], v[42:45]\n" \
  "v_mfma_f32_16x16x32_bf16 v[48:51], v[24:27], v[188:191], v[48:51]\n" \
  "v_mfma_f32_16x16x32_bf16 v[54:57], v[24:27], v[252:255], v[54:57]\n" \
  "s_waitcnt lgkmcnt(4)\n" \
  "v_mfma_f32_16x16x32_bf16 v[60:63], v[24:27], v[28:31], v[60:63]\n" \
  "s_waitcnt lgkmcnt(0)\n" \
  "s_nop 4\n" \
  "v_fma_f32 v42, -v32, v8, v42\n" \
  "v_fma_f32 v43, -v33, v8, v43\n" \
  "v_fma_f32 v44, -v34, v8, v44\n" \
  "v_fma_f32 v45, -v35, v8, v45\n" \
  "v_fma_f32 v48, -v32, v9, v48\n" \
  "v_fma_f32 v49, -v33, v9, v49\n" \
  "v_fma_f32 v50, -v34, v9, v50\n" \
  "v_fma_f32 v51, -v35, v9, v51\n" \
  "v_fma_f32 v54, -v32, v10, v54\n" \
  "v_fma_f32 v55, -v33, v10, v55\n" \
  "v_fma_f32 v56, -v34, v10, v56\n" \
  "v_fma_f32 v57, -v35, v10, v57\n" \
  "v_fma_f32 v60, -v32, v4, v60\n" \
  "v_fma_f32 v61, -v33, v4, v61\n" \
  "v_fma_f32 v62, -v34, v4, v62\n" \
  "v_fma_f32 v63, -v35, v4, v63\n" \
  "ds_read_b128 v[32:35], v1 offset:0\n" \
  "v_mul_f32 v42, v42, v36\n" \
  "v_mul_f32 v43, v43, v37\n" \
  "v_mul_f32 v44, v44, v38\n" \
  "v_mul_f32 v45, v45, v39\n" \
  "v_mul_f32 v48, v48, v36\n" \
  "v_mul_f32 v49, v49, v37\n" \
  "v_mul_f32 v50, v50, v38\n" \
  "v_mul_f32 v51, v51, v39\n" \
  "v_mul_f32 v54, v54, v36\n" \
  "v_mul_f32 v55, v55, v37\n" \
  "v_mul_f32 v56, v56, v38\n" \
  "v_mul_f32 v57, v57, v39\n" \
  "v_mul_f32 v60, v60, v36\n" \
  "v_mul_f32 v61, v61, v37\n" \
  "v_mul_f32 v62, v62, v38\n" \
  "v_mul_f32 v63, v63, v39\n" \
  "ds_read_b128 v[36:39], v1 offset:1024\n" \
  "v_cvt_pk_bf16_f32 v40, v42, v43\n" \
  "v_cvt_pk_bf16_f32 v41, v44, v45\n" \
  "v_cvt_pk_bf16_f32 v46, v48, v49\n" \
  "v_cvt_pk_bf16_f32 v47, v50, v51\n" \
  "v_cvt_pk_bf16_f32 v52, v54, v55\n" \
  "v_cvt_pk_bf16_f32 v53, v56, v57\n" \
  "v_cvt_pk_bf16_f32 v58, v60, v61\n" \
  "v_cvt_pk_bf16_f32 v59, v62, v63\n" \
  "ds_read_b128 v[24:27], v0 offset:6400\n" \
  "ds_read_b128 v[28:31], v1 offset:2048\n" \
  "v_mfma_f32_16x16x32_bf16 v[42:45], v[12:15], v[64:67], 0\n" \
  "v_mfma_f32_16x16x32_bf16 v[48:51], v[12:15], v[128:131], 0\n" \
  "v_mfma_f32_16x16x32_bf16 v[54:57], v[12:15], v[192:195], 0\n" \
  "s_waitcnt lgkmcnt(3)\n" \
  "v_mfma_f32_16x16x32_bf16 v[60:63], v[12:15], v[32:35], 0\n" \
  "ds_read_b128 v[12:15], v0 offset:8448\n" \
  "ds_read_b128 v[32:35], v1 offset:3072\n" \
  "v_mfma_f32_16x16x32_bf16 v[42:45], v[16:19], v[68:71], v[42:45]\n" \
  "v_mfma_f32_16x16x32_bf16 v[48:51], v[16:19], v[132:135], v[48:51]\n" \
  "v_mfma_f32_16x16x32_bf16 v[54:57], v[16:19], v[196:199], v[54:57]\n" \
  "s_waitcnt lgkmcnt(4)\n" \
  "v_mfma_f32_16x16x32_bf16 v[60:63], v[16:19], v[36:39], v[60:63]\n" \
  "ds_read_b128 v[16:19], v0 offset:10496\n" \
  "ds_read_b128 v[36:39], v1 offset:4096\n" \
  "v_mfma_f32_16x16x32_bf16 v[42:45], v[20:23], v[72:75], v[42:45]\n" \
  "v_mfma_f32_16x16x32_bf16 v[48:51], v[20:23], v[136:139], v[48:51]\n" \
  "v_mfma_f32_16x16x32_bf16 v[54:57], v[20:23], v[200:203], v[54:57]\n" \
  "s_waitcnt lgkmcnt(4)\n" \
  "v_mfma_f32_16x16x32_bf16 v[60:63], v[20:23], v[28:31], v[60:63]\n" \
  "ds_read_b128 v[20:23], v0 offset:12544\n" \
  "ds_read_b128 v[28:31], v1 offset:5120\n" \
  "v_mfma_f32_16x16x32_bf16 v[42:45], v[24:27], v[76:79], v[42:45]\n" \
  "v_mfma_f32_16x16x32_bf16 v[48:51], v[24:27], v[140:143], v[48:51]\n" \
  "v_mfma_f32_16x16x32_bf16 v[54:57], v[24:27], v[204:207], v[54:57]\n" \
  "s_waitcnt lgkmcnt(4)\n" \
  "v_mfma_f32_16x16x32_bf16 v[60:63], v[24:27], v[32:35], v[60:63]\n" \
  "ds_read_b128 v[24:27], v0 offset:14592\n" \
  "ds_read_b128 v[32:35], v1 offset:6144\n" \
  "v_mfma_f32_16x16x32_bf16 v[42:45], v[12:15], v[80:83], v[42:45]\n" \
  "v_mfma_f32_16x16x32_bf16 v[48:51], v[12:15], v[144:147], v[48:51]\n" \
  "v_mfma_f32_16x16x32_bf16 v[54:57], v[12:15], v[208:211], v[54:57]\n" \
  "s_waitcnt lgkmcnt(4)\n" \
  "v_mfma_f32_16x16x32_bf16 v[60:63], v[12:15], v[36:39], v[60:63]\n" \
  "ds_read_b128 v[12:15], v0 offset:16640\n" \
  "ds_read_b128 v[36:39], v1 offset:7168\n" \
  "v_mfma_f32_16x16x32_bf16 v[42:45], v[16:19], v[84:87], v[42:45]\n" \
  "v_mfma_f32_16x16x32_bf16 v[48:51], v[16:19], v[148:151], v[48:51]\n" \
  "v_mfma_f32_16x16x32_bf16 v[54:57], v[16:19], v[212:215], v[54:57]\n" \
  "s_waitcnt lgkmcnt(4)\n" \
  "v_mfma_f32_16x16x32_bf16 v[60:63], v[16:19], v[28:31], v[60:63]\n" \
  "ds_read_b128 v[16:19], v0 offset:18688\n" \
  "ds_read_b128 v[28:31], v1 offset:8192\n" \
  "v_mfma_f32_16x16x32_bf16 v[42:45], v[20:23], v[88:91], v[42:45]\n" \
  "v_mfma_f32_16x16x32_bf16 v[48:51], v[20:23], v[152:155], v[48:51]\n" \
  "v_mfma_f32_16x16x32_bf16 v[54:57], v[20:23], v[216:219], v[54:57]\n" \
  "s_waitcnt lgkmcnt(4)\n" \
  "v_mfma_f32_16x16x32_bf16 v[60:63], v[20:23], v[32:35], v[60:63]\n" \
  "ds_read_b128 v[20:23], v0 offset:20736\n" \
  "ds_read_b128 v[32:35], v1 offset:9216\n" \
  "v_mfma_f32_16x16x32_bf16 v[42:45], v[24:27], v[92:95], v[42:45]\n" \
  "v_mfma_f32_16x16x32_bf16 v[48:51], v[24:27], v[156:159], v[48:51]\n" \
  "v_mfma_f32_16x16x32_bf16 v[54:57], v[24:27], v[220:223], v[54:57]\n" \
  "s_waitcnt lgkmcnt(4)\n" \
  "v_mfma_f32_16x16x32_bf16 v[60:63], v[24:27], v[36:39], v[60:63]\n" \
  "ds_read_b128 v[24:27], v0 offset:22784\n" \
  "ds_read_b128 v[36:39], v1 offset:10240\n" \
  "v_mfma_f32_16x16x32_bf16 v[42:45], v[12:15], v[96:99], v[42:45]\n" \
  "v_mfma_f32_16x16x32_bf16 v[48:51], v[12:15], v[160:163], v[48:51]\n" \
  "v_mfma_f32_16x16x32_bf16 v[54:57], v[12:15], v[224:227], v[54:57]\n" \
  "s_waitcnt lgkmcnt(4)\n" \
  "v_mfma_f32_16x16x32_bf16 v[60:63], v[12:15], v[28:31], v[60:63]\n" \
  "ds_read_b128 v[12:15], v0 offset:24832\n" \
  "ds_read_b128 v[28:31], v1 offset:11264\n" \
  "v_mfma_f32_16x16x32_bf16 v[42:45], v[16:19], v[100:103], v[42:45]\n" \
  "v_mfma_f32_16x16x32_bf16 v[48:51], v[16:19], v[164:167], v[48:51]\n" \
  "v_mfma_f32_16x16x32_bf16 v[54:57], v[16:19], v[228:231], v[54:57]\n" \
  "s_waitcnt lgkmcnt(4)\n" \
  "v_mfma_f32_16x16x32_bf16 v[60:63], v[16:19], v[32:35], v[60:63]\n" \
  "ds_read_b128 v[16:19], v0 offset:26880\n" \
  "ds_read_b128 v[32:35], v1 offset:12288\n" \
  "v_mfma_f32_16x16x32_bf16 v[42:45], v[20:23], v[104:107], v[42:45]\n" \
  "v_mfma_f32_16x16x32_bf16 v[48:51], v[20:23], v[168:171], v[48:51]\n" \
  "v_mfma_f32_16x16x32_bf16 v[54:57], v[20:23], v[232:235], v[54:57]\n" \
  "s_waitcnt lgkmcnt(4)\n" \
  "v_mfma_f32_16x16x32_bf16 v[60:63], v[20:23], v[36:39], v[60:63]\n" \
  "ds_read_b128 v[20:23], v0 offset:28928\n" \
  "ds_read_b128 v[36:39], v1 offset:13312\n" \
  "v_mfma_f32_16x16x32_bf16 v[42:45], v[24:27], v[108:111], v[42:45]\n" \
  "v_mfma_f32_16x16x32_bf16 v[48:51], v[24:27], v[172:175], v[48:51]\n" \
  "v_mfma_f32_16x16x32_bf16 v[54:57], v[24:27], v[236:239], v[54:57]\n" \
  "s_waitcnt lgkmcnt(4)\n" \
  "v_mfma_f32_16x16x32_bf16 v[60:63], v[24:27], v[28:31], v[60:63]\n" \
  "ds_read_b128 v[24:27], v0 offset:30976\n" \
  "ds_read_b128 v[28:31], v1 offset:14336\n" \
  "v_mfma_f32_16x16x32_bf16 v[42:45], v[12:15], v[112:115], v[42:45]\n" \
  "v_mfma_f32_16x16x32_bf16 v[48:51], v[12:15], v[176:179], v[48:51]\n" \
  "v_mfma_f32_16x16x32_bf16 v[54:57], v[12:15], v[240:243], v[54:57]\n" \
  "s_waitcnt lgkmcnt(4)\n" \
  "v_mfma_f32_16x16x32_bf16 v[60:63], v[12:15], v[32:35], v[60:63]\n" \
  "ds_read_b128 v[32:35], v1 offset:15360\n" \
  "v_mfma_f32_16x16x32_bf16 v[42:45], v[16:19], v[116:119], v[42:45]\n" \
  "v_mfma_f32_16x16x32_bf16 v[48:51], v[16:19], v[180:183], v[48:51]\n" \
  "v_mfma_f32_16x16x32_bf16 v[54:57], v[16:19], v[244:247], v[54:57]\n" \
  "s_waitcnt lgkmcnt(3)\n" \
  "v_mfma_f32_16x16x32_bf16 v[60:63], v[16:19], v[36:39], v[60:63]\n" \
  "ds_read_b128 v[36:39], v3 offset:64\n" \
  "v_mfma_f32_16x16x32_bf16 v[42:45], v[20:23], v[120:123], v[42:45]\n" \
  "v_mfma_f32_16x16x32_bf16 v[48:51], v[20:23], v[184:187], v[48:51]\n" \
  "v_mfma_f32_16x16x32_bf16 v[54:57], v[20:23], v[248:251], v[54:57]\n" \
  "s_waitcnt lgkmcnt(2)\n" \
  "v_mfma_f32_16x16x32_bf16 v[60:63], v[20:23], v[28:31], v[60:63]\n" \
  "ds_read_b128 v[28:31], v3 offset:192\n" \
  "s_mov_b64 exec, 1\n" \
  "ds_add_u32 v6, v7 offset:8\n" \
  "s_mov_b64 exec, -1\n" \
  "v_xor_b32 v6, 4, v6\n" \
  "v_mfma_f32_16x16x32_bf16 v[42:45], v[24:27], v[124:127], v[42:45]\n" \
  "v_mfma_f32_16x16x32_bf16 v[48:51], v[24:27], v[188:191], v[48:51]\n" \
  "v_mfma_f32_16x16x32_bf16 v[54:57], v[24:27], v[252:255], v[54:57]\n" \
  "s_waitcnt lgkmcnt(3)\n" \
  "v_mfma_f32_16x16x32_bf16 v[60:63], v[24:27], v[32:35], v[60:63]\n" \
  "s_waitcnt lgkmcnt(1)\n" \
  "s_nop 4\n" \
  "ds_read_b32 v5, v6 offset:0\n" \
  "v_fma_f32 v42, -v36, v8, v42\n" \
  "v_fma_f32 v43, -v37, v8, v43\n" \
  "v_fma_f32 v44, -v38, v8, v44\n" \
  "v_fma_f32 v45, -v39, v8, v45\n" \
  "v_mul_f32 v42, v42, v28\n" \
  "v_mul_f32 v43, v43, v29\n" \
  "v_mul_f32 v44, v44, v30\n" \
  "v_mul_f32 v45, v45, v31\n" \
  "v_cvt_pk_bf16_f32 v42, v42, v43\n" \
  "v_cvt_pk_bf16_f32 v43, v44, v45\n" \
  "buffer_store_dwordx4 v[40:43], v2, s[40:43], s65 offen offset:0 sc1\n" \
  "v_fma_f32 v48, -v36, v9, v48\n" \
  "v_fma_f32 v49, -v37, v9, v49\n" \
  "v_fma_f32 v50, -v38, v9, v50\n" \
  "v_fma_f32 v51, -v39, v9, v51\n" \
  "v_mul_f32 v48, v48, v28\n" \
  "v_mul_f32 v49, v49, v29\n" \
  "v_mul_f32 v50, v50, v30\n" \
  "v_mul_f32 v51, v51, v31\n" \
  "v_cvt_pk_bf16_f32 v48, v48, v49\n" \
  "v_cvt_pk_bf16_f32 v49, v50, v51\n" \
  "buffer_store_dwordx4 v[46:49], v2, s[40:43], s65 offen offset:1024 sc1\n" \
  "v_fma_f32 v54, -v36, v10, v54\n" \
  "v_fma_f32 v55, -v37, v10, v55\n" \
  "v_fma_f32 v56, -v38, v10, v56\n" \
  "v_fma_f32 v57, -v39, v10, v57\n" \
  "v_mul_f32 v54, v54, v28\n" \
  "v_mul_f32 v55, v55, v29\n" \
  "v_mul_f32 v56, v56, v30\n" \
  "v_mul_f32 v57, v57, v31\n" \
  "v_cvt_pk_bf16_f32 v54, v54, v55\n" \
  "v_cvt_pk_bf16_f32 v55, v56, v57\n" \
  "buffer_store_dwordx4 v[52:55], v2, s[40:43], s65 offen offset:2048 sc1\n" \
  "v_fma_f32 v60, -v36, v4, v60\n" \
  "v_fma_f32 v61, -v37, v4, v61\n" \
  "v_fma_f32 v62, -v38, v4, v62\n" \
  "v_fma_f32 v63, -v39, v4, v63\n" \
  "v_mul_f32 v60, v60, v28\n" \
  "v_mul_f32 v61, v61, v29\n" \
  "v_mul_f32 v62, v62, v30\n" \
  "v_mul_f32 v63, v63, v31\n" \
  "v_cvt_pk_bf16_f32 v60, v60, v61\n" \
  "v_cvt_pk_bf16_f32 v61, v62, v63\n" \
  "buffer_store_dwordx4 v[58:61], v2, s[40:43], s65 offen offset:3072 sc1\n" \
  "s_waitcnt lgkmcnt(0)\n" \
  "s_add_u32 s65, s65, 0x40000\n" \
  "s_sub_u32 s64, s64, 1\n" \
  "s_mov_b32 s68, 1\n" \
  "s_cmp_lg_u32 s64, 0\n" \
  "s_cbranch_scc1 .LCW_TOP_%=\n" \
  "s_waitcnt vmcnt(0)\n" \
  "s_mov_b64 exec, 1\n" \
  "buffer_store_dword v7, off, s[56:59], s72 sc1\n" \
  "s_mov_b64 exec, -1\n" \
  "s_add_u32 s72, s72, 0x1000\n" \
  "s_mov_b32 s68, 0\n" \
  ".LCW_END_%=:\n" \
  "s_endpgm\n"
constexpr int RG = 2;
#define RFL(x) __builtin_amdgcn_readfirstlane(x)
#define HALF(x, i) BF8(((i) ? __builtin_shufflevector(x, x, 4, 5, 6, 7) : __builtin_shufflevector(x, x, 0, 1, 2, 3)))
constexpr int P_W = 65536, P_ST = 131072, P_SS = P_ST + 1024, P_CN = P_SS + 512;
template <bool PR>
__device__ __forceinline__ void role_proj_t(const MegaArgs& A, char* smem, const int L, const int idx, const bool kv) {
  const int w = A.wv;
  const int l = lane_id();
  const int c = l & 15, g = l >> 4;
  char* ws = A.ws;
  unsigned* FL = (unsigned*)(ws + OFF_FL);
  unsigned* TMO = FL + FL_TMO;
  const unsigned sb = lds_base(smem);
  auto cn = (volatile __attribute__((address_space(3))) unsigned*)(unsigned long)(sb + P_CN);
  u64 tag;
  const bool h4 = kv && L;
  const __amdgpu_buffer_rsrc_t rsM = MKRS(ws + OFF_MEMK + L * SZ_MEM, kv && w == 2 ? (4 - L) * SZ_MEM : 0);
  const bool mc = kv && w == 2 && (MCE < 2 || !L);
  constexpr int NMC = MCE == 2 ? 8 : 4;
  const int b0 = RFL(h4 ? (H4S ? 2 : 3) * w : w == 2 ? 0 : HX0),
            b1 = RFL(h4 ? (H4S ? 2 * w + 2 : w == 2 ? 8 : 3 * w + 3 * (w < 2)) : w == 2 ? HX0 : 8);
  const __amdgpu_buffer_rsrc_t rsF = MKRS(FL + FL_FC + L * 64 * 32 * 32, 64 * 4096);
  const int fo = ((blockIdx.x & 3) * 8 + ((H4S && h4) || w == 3 ? l >> 3 : min(b0 + (l >> 3), b1 - 1))) * 128 + (l & 7) * 4;
#define FC_LOOK(s) __builtin_amdgcn_raw_buffer_load_b32(rsF, fo, (s) * 4096, AUX_SC1)
#define MEM_CLEAR(s)                                                                                                          \
  _Pragma("unroll") for (int i = 0; i < NMC; ++i)                                                                             \
    ST128((v4u{0u, 0u, 0u, 0u}), rsM, l * 16 + (i & 1) * 1024, (s) * 65536 + idx * 2048 + (i >> 1) * (int)SZ_MEM * (MCE == 2 ? 1 : 2));
  const char* Xb = ws + (L ? OFF_X3 : OFF_XA);
  const __amdgpu_buffer_rsrc_t rsS = MKRS(ws + OFF_H + L * (SZ_X / 2), SZ_X / 2);
  unsigned nf = 0u;
  bool early = false;
  v4u t0[16];
#define X_ASK(s) \
  for (int i = b0; i < b1; ++i) glds64c(Xb + RFL((s) * 32768 + i * 4096), l * 16, RFL(sb + ((s) & 1) * 32768 + i * 4096));
#define S_ASK(s, t) _Pragma("unroll") for (int i = 0; i < 16; ++i) t[i] = LD128(rsS, l * 16, (s) * 16384 + i * 1024);
  {
    const int j = w & 3;
    const bool vt = kv && j >= 2;
    constexpr int NT = PR ? 3 : 1;
    v8u wq[NT][8];
    const u16* Wm = (kv ? (PR ? A.Wv : A.Wk) : A.Wq) - (PR ? 128 : 0) * HD;
    const int hm = kv ? idx : 2 * idx + PR;
    const int q = (l >> 2) & 3, p = l & 3, f = q | ((g & 1) << 2);
    unsigned ra[NT];
#pragma unroll
    for (int m = 0; m < NT; ++m) {
      const int ct = PR ? m : 3;
      ra[m] = sb + (j >> 1) * 65536 + (8 * g + q) * 256 + ((4 * (j & 1) + (vt ? ct : (ct & 2) + (p >> 1))) ^ f) * 32 + (vt ? p * 8 : (p & 1) * 16 + (ct & 1) * 8);
    }
#define W_ISSUE(n) wqkv_issue<false>(Wm + (128 * (n) - 256 * ((n) & 1)) * HD, (n) & 1, hm, w, l, sb, (WNT >> (kv ? 1 + PR : 3) & 1) && (L == 0 || (WNT & 32)))
#define W_STAGE(n, WAIT, NEXT)                                                                             \
  {                                                                                                        \
    WAIT;                                                                                                  \
    if (n == 3 && EL == 2 && !PR && L == 0 && w >= 2) nf = FC_LOOK(0);                                     \
    if (n == 3 && EL == 1 && !PR && L == 0 && w >= 2) {                                                    \
      early = __all(nf != 0u);                                                                             \
      if (early) {                                                                                         \
        X_ASK(0)                                                                                           \
        if (w == 3) { S_ASK(0, t0) }                                                                       \
      }                                                                                                    \
      nf = FC_LOOK(early);                                                                                 \
    }                                                                                                      \
    RAW_BAR();                                                                                             \
    if (n == 3 && EL == 3 && !PR && L == 0 && w >= 2) nf = FC_LOOK(0);                                     \
    _Pragma("unroll") for (int m = 0; m < NT; ++m) {                                                       \
      _Pragma("unroll") for (int t = 0; t < 2; ++t) {                                                      \
        const unsigned a_ = ra[m] + ((n) & 1) * 32768 + t * 16384;                                         \
        const v2u r0 = trd(a_), r1 = trd(a_ + 1024), r2 = trd(a_ + 8192), r3 = trd(a_ + 8192 + 1024);      \
        wq[m][2 * (n) + t] = v8u{r0.x, r0.y, r1.x, r1.y, r2.x, r2.y, r3.x, r3.y};                          \
      }                                                                                                    \
    }                                                                                                      \
    _Pragma("unroll") for (int m = 0; m < 2 * NT; ++m) asm volatile("" : "+v"(wq[m >> 1][2 * (n) + (m & 1)])); \
    RAW_BAR();                                                                                             \
    NEXT;                                                                                                  \
    _Pragma("unroll") for (int m = 0; m < 4 * NT; ++m)                                                     \
      cs[m % NT] = MFMA16(HALF(wq[m % NT][2 * (n) + m / NT / 2], (m / NT) & 1), BF8(one), cs[m % NT]);     \
    _Pragma("unroll") for (int m = 0; m < NT; ++m) asm volatile("" : "+v"(cs[m]));                         \
  }
    if (EL && !MC3 && mc) { MEM_CLEAR(0) }
    W_ISSUE(0);
    W_ISSUE(1);
    tag = call_tag(A);
    if (w == 0 && l < 8) cn[l] = l == 1 ? 3u : l == 5 ? 2u : 0u;
    const v4u one = {0x3f803f80u, 0x3f803f80u, 0x3f803f80u, 0x3f803f80u};
    f32x4 cs[NT] = {};
    u64 rv = 0;
    W_STAGE(0, WAIT_VM(8), W_ISSUE(2); if (EL) RD_ASK(rv));
    SST(4);
    W_STAGE(1, if (EL) WAIT_VM(9); else WAIT_VM(8), W_ISSUE(3); if (EL != 0 && MC3 != 0 && mc) { MEM_CLEAR(0) if (MC3 == 2) { MEM_CLEAR(1) } });
    SST(5);
    W_STAGE(2, if (EL != 0 && MC3 == 2 && mc) WAIT_VM(16); else if (EL != 0 && MC3 != 0 && mc) WAIT_VM(12); else WAIT_VM(8), (void)0);
    SST(6);
    asm volatile("" : "+v"(rv));
    if (!EL) ready_wait(A, tag, ready_look(A));
    for (unsigned spins = 0; EL && !__all(rv == tag) && spins < SPIN_CAP; ++spins) {
      __builtin_amdgcn_s_sleep(8);
      RD_ASK(rv);
      asm volatile("s_waitcnt vmcnt(0)" : "+v"(rv)::"memory");
    }
    if (EL == 1 && !PR && L == 0 && w >= 2) nf = FC_LOOK(0);
    W_STAGE(3, if (EL >= 2 && MC3 == 2 && mc) WAIT_VM(8); else if (EL >= 2 && MC3 == 1 && mc) WAIT_VM(4); else WAIT_VM(0), if (!PR) {
      _Pragma("unroll") for (int ks = 0; ks < 16; ++ks)
        *reinterpret_cast<bf16x8*>(smem + P_W + j * 16384 + ks * 1024 + l * 16) = HALF(wq[0][ks >> 1], ks & 1);
    });
    SST(7);
#undef W_STAGE
#undef W_ISSUE
#pragma unroll
    for (int m = 0; m < NT; ++m) *reinterpret_cast<f32x4*>(smem + P_ST + j * 256 + (PR ? m : 3) * 64 + g * 16) = cs[m];
    RAW_BAR();
    SST(8);
    if constexpr (PR) {
      const v4u rsO = mk_srd(ws + RFL((unsigned)(vt ? OFF_VF + L * SZ_O : OFF_QK + L * SZ_QK)), RFL((unsigned)(vt ? SZ_O : SZ_QK)));
      const v4u rsP = mk_srd(FL + RFL(kv ? FL_FQ + (L * 64 * 32 + idx) * 32 + 2 * j : FL_FP + (L * 16 + idx) * 32 + j), 63 * 4096 + 4);
      const int so = RFL(vt ? idx * 8 + 4 * (j & 1) : (kv ? idx * 16 + 8 : (2 * idx + (j >> 1)) * 16) + 2 * (j & 1));
      const v8u pv = {sb + 32768 + g * 512 + c * 16, sb + P_W + j * 16384 + l * 16, (unsigned)l * 16, sb + P_SS + 256 + (vt ? g * 16 : c * 4),
                      sb + P_ST + j * 256 + (vt ? c * 4 : g * 16), 0u, 0u, 0u};
      const char* dbg = nullptr;
#define P_OPS                                                                                                                               \
  ::"{v[64:71]}"(wq[0][0]), "{v[72:79]}"(wq[0][1]), "{v[80:87]}"(wq[0][2]), "{v[88:95]}"(wq[0][3]), "{v[96:103]}"(wq[0][4]),                \
      "{v[104:111]}"(wq[0][5]), "{v[112:119]}"(wq[0][6]), "{v[120:127]}"(wq[0][7]), "{v[128:135]}"(wq[1][0]), "{v[136:143]}"(wq[1][1]),     \
      "{v[144:151]}"(wq[1][2]), "{v[152:159]}"(wq[1][3]), "{v[160:167]}"(wq[1][4]), "{v[168:175]}"(wq[1][5]), "{v[176:183]}"(wq[1][6]),     \
      "{v[184:191]}"(wq[1][7]), "{v[192:199]}"(wq[2][0]), "{v[200:207]}"(wq[2][1]), "{v[208:215]}"(wq[2][2]), "{v[216:223]}"(wq[2][3]),     \
      "{v[224:231]}"(wq[2][4]), "{v[232:239]}"(wq[2][5]), "{v[240:247]}"(wq[2][6]), "{v[248:255]}"(wq[2][7]), "{v[0:7]}"(pv),               \
      "{s[40:43]}"(rsO), "{s[56:59]}"(rsP), "{s64}"(NSEG), "{s65}"(so * 1024), "{s70}"(sb + P_CN), "{s[36:37]}"(dbg)                        \
      : "memory", "vcc", "scc", "s80", "v63"
      if (vt) asm volatile("s_mov_b64 exec, -1\ns_waitcnt vmcnt(0) lgkmcnt(0)\ns_nop 4\n" V_ASM P_OPS);
      else asm volatile("s_mov_b64 exec, -1\ns_waitcnt vmcnt(0) lgkmcnt(0)\ns_nop 4\n" P_ASM P_OPS);
#undef P_OPS
      return;
    }
  }
  __builtin_amdgcn_s_setprio(RPRIO);
  if (h4 || w >= 2) {
    if (!EL && mc) { MEM_CLEAR(0) }
    auto stats = [&](const v4u (&t)[16], const int s) {
      float a = 0.f, b = 0.f, qa = 0.f, qb = 0.f;
#pragma unroll
      for (int i = 0; i < 16; ++i) {
        a = one(a + __uint_as_float(t[i].x));
        b = one(b + __uint_as_float(t[i].z));
        qa = one(qa + __uint_as_float(t[i].y));
        qb = one(qb + __uint_as_float(t[i].w));
      }
      a = xsum32(xsum16(a));
      b = xsum32(xsum16(b));
      float da = 0.f, db = 0.f;
#pragma unroll
      for (int i = 0; i < 16; ++i) {
        const float u = one(__uint_as_float(t[i].x) - a * (1.0f / 64)), v = one(__uint_as_float(t[i].z) - b * (1.0f / 64));
        da = one(da + u * u);
        db = one(db + v * v);
      }
      const float var = xsum32(swap_add16(qa + da * 0.125f, qb + db * 0.125f)) * (1.0f / DM);
      const float ri = 1.0f / bfr1(sqrtf(bfr1(bfr1(var) + 1e-5f)));
      reinterpret_cast<float*>(smem + P_SS)[(s & 1) * 64 + (g >> 1) * 32 + 2 * c + (g & 1)] = g < 2 ? bfr1(((g & 1) ? b : a) * (1.0f / DM)) : ri;
      asm volatile("" ::: "memory");
      if (l == 0) cn[4 + (s & 1)] = 2 * s + 4;
    };
    static_assert(EL != 1 || MC3 == 2, "clear");
    if (early) {
      if (w == 3) WAIT_VM(17);
      else WAIT_VM(1);
      if (l == 0) __hip_atomic_fetch_add((__attribute__((address_space(3))) unsigned*)cn, 3u, __ATOMIC_RELAXED, __HIP_MEMORY_SCOPE_WORKGROUP);
      SSX(10, w == 2);
      SSX(9, w == 2);
      if (w == 3) stats(t0, 0);
      SSX(11, w == 3);
    }
    unsigned tn = now10(), tg = tn, mk = 0u;
    const __amdgpu_buffer_rsrc_t rm = MKRS(FL + FL_FW + (blockIdx.x & 3) * 128, L ? 63 * 2048 + 512 : 0);
    for (int s = early; s < NSEG; ++s) {
      for (unsigned spins = 0; !__all(nf != 0u); ++spins) {
        if (spin_timeout(spins, TMO)) return;
        if (spins) __builtin_amdgcn_s_sleep(SLP_H);
        else if (KE && __all(mk != 0u)) gate(latest(mk, tg) + (kv ? KE : PE));
        else if (GF_H && (h4 || (GF_P && L)) && s >= GF0 && s < NSEG - 6) gate(tg);
        tg = now10() + GF_H;
        nf = FC_LOOK(s);
        asm volatile("" ::: "memory");
      }
      if (NFW) asm volatile("" ::"v"(nf));
      if (KE) mk = __builtin_amdgcn_raw_buffer_load_b32(rm, (c >> 2) * 128 + (c & 3) * 4, (s + 1) * 2048, AUX_SC1);
      TS(4, s, 128);
      for (unsigned spins = 0; cn[2 + (s & 1)] < 4u * (s >> 1); ++spins) {
        if (spins > 64u * SPIN_CAP) return;
        __builtin_amdgcn_s_sleep(2);
      }
      if (L == 0 && s >= PACE0) gate(tn);
      else tn = now10();
      tn += PACE;
      TS(5, s, 128);
      X_ASK(s)
      if (H4S && h4) {
        v4u t[4];
#pragma unroll
        for (int i = 0; i < 4; ++i) t[i] = LD128(rsS, (l >> 2) * 256 + (l & 3) * 16 + w * 64, s * 16384 + i * 4096);
        nf = 0u;
        if (MCE == 1 && w == 2 && s < NSEG - 1) {
          MEM_CLEAR(s + 1)
          WAIT_VM(8);
        } else {
          WAIT_VM(4);
        }
        if (l == 0) __hip_atomic_fetch_add((__attribute__((address_space(3))) unsigned*)cn + (s & 1), w < 2 ? 2u : 1u, __ATOMIC_RELAXED, __HIP_MEMORY_SCOPE_WORKGROUP);
        TS(6, s, 128);
#define F(x) __uint_as_float(x)
        const float a = red_mod4(one(F(t[0].x) + F(t[1].x)) + one(F(t[2].x) + F(t[3].x)));
        const float b = red_mod4(one(F(t[0].z) + F(t[1].z)) + one(F(t[2].z) + F(t[3].z)));
        float da = 0.f, db = 0.f;
#pragma unroll
        for (int i = 0; i < 4; ++i) {
          const float u = one(F(t[i].x) - a * (1.0f / 64)), v = one(F(t[i].z) - b * (1.0f / 64));
          da = one(da + u * u);
          db = one(db + v * v);
        }
        const float qa = one(F(t[0].y) + F(t[1].y)) + one(F(t[2].y) + F(t[3].y)), qb = one(F(t[0].w) + F(t[1].w)) + one(F(t[2].w) + F(t[3].w));
#undef F
        float var = xsum32(swap_add16(one(qa + da * 0.125f), one(qb + db * 0.125f)));
        var += dppr<0x124>(var);
        var += dppr<0x128>(var);
        const float ri = 1.0f / bfr1(sqrtf(bfr1(bfr1(var * (1.0f / DM)) + 1e-5f)));
        if (c < 4) reinterpret_cast<float*>(smem + P_SS)[(s & 1) * 64 + (g >> 1) * 32 + 8 * w + 2 * c + (g & 1)] = g < 2 ? bfr1(((g & 1) ? b : a) * (1.0f / DM)) : ri;
        asm volatile("" ::: "memory");
        if (l == 0) __hip_atomic_fetch_add((__attribute__((address_space(3))) unsigned*)cn + 4 + (s & 1), 1u, __ATOMIC_RELAXED, __HIP_MEMORY_SCOPE_WORKGROUP);
        TS(7, s, 192);
        if (!MCE && w == 2 && s < NSEG - 1) { MEM_CLEAR(s + 1) }
      } else if (w != 3) {
        nf = L ? 0u : FC_LOOK(s + 1);
        if (mc && s < NSEG - 1 && (EL == 0 || MC3 != 2 || s)) {
          MEM_CLEAR(s + 1)
          if (NMC == 8) WAIT_VM(8);
          else WAIT_VM(4);
        } else {
          WAIT_VM(0);
        }
        if (l == 0) __hip_atomic_fetch_add((__attribute__((address_space(3))) unsigned*)cn + (s & 1), h4 ? 2u : 3u, __ATOMIC_RELAXED, __HIP_MEMORY_SCOPE_WORKGROUP);
        TS(6, s, 128);
        SSX(10, s == 0 && w == 2);
      } else {
        v4u t[16];
        S_ASK(s, t)
        WAIT_VM(16);
        nf = L ? 0u : FC_LOOK(s + 1);
        if (l == 0 && !h4) __hip_atomic_fetch_add((__attribute__((address_space(3))) unsigned*)cn + (s & 1), 3u, __ATOMIC_RELAXED, __HIP_MEMORY_SCOPE_WORKGROUP);
        stats(t, s);
        SSX(11, s == 0);
        TS(7, s, 192);
      }
    }
    return;
  }
  const int RL = !kv, rp = idx & 15, u = RFL(kv ? idx >> 4 : L);
  const __amdgpu_buffer_rsrc_t rsW = MKRS(ws + OFF_PW, SZ_PW);
  const __amdgpu_buffer_rsrc_t rsX = MKRS(ws + (RL ? OFF_X3 : OFF_XA), SZ_X);
  const __amdgpu_buffer_rsrc_t rsY = RL ? MKRS(A.out, SZ_X) : MKRS(ws + OFF_X2, SZ_X);
  const int r = 2 * rp + (g >> 1), yo = r * (DM * 2) + (l & 31) * 16 + u * 512;
  if (kv) {
    if (XZD) gate(now10() + XZD);
    if (XZP & 3) {
      if (!wait_n<SLP_V, 32>(FL + FL_FZ + (blockIdx.x & 7) * 64, 64, TMO)) return;
    } else {
      for (int s = w; s < (XZP ? 2 * RG : NSEG); s += RG) ST128((v4u{0u, 0u, 0u, 0u}), rsY, yo, s * SEG * DM * 2);
      DRAIN();
      if (l < 8) set_flag(FL + FL_FZ + l * 64 + 2 * idx + w);
    }
  }
  v4u sel;
#pragma unroll
  for (int e = 0; e < 4; ++e) sel[e] = (g >> 1) == (c >> 3) && ((c & 7) >> 1) == e ? 0x3f80u << (16 * (c & 1)) : 0u;
  unsigned tl = now10();
  int po = (g & 1) * 32768 + (c >> 2) * 128 + (g >> 1) * 64 + (c & 3) * 16;
  asm volatile("" : "+v"(po));
  for (int s = w; s < NSEG; s += RG) {
    v4u xr;
    if (GF_R) {
      gate(tl);
      const gu32* f = (gu32*)(FL + FL_FW) + ((RL * 64 + s) * 4 + (blockIdx.x & 3)) * 4 * 32 + (c >> 2) * 32 + (c & 3);
      unsigned mk;
      for (unsigned spins = 0;; ++spins) {
        tl = now10() + GF_R;
        mk = __hip_atomic_load(f, RLX_AGENT);
        if (__all(mk != 0u) || spin_timeout(spins, TMO)) break;
        if (s < NSEG - RFA) __builtin_amdgcn_s_sleep(SLP_R);
      }
      if (EF) {
        const unsigned tp = latest(mk, tl);
        asm volatile("" ::: "memory");
        xr = LD128(rsX, (l & 31) * 512 + r * 16, s * 32768 + u * 16384);
        gate(tp - (RL && s >= NSEG - RLS ? RLEAD : 0));
        if (GR2) tl = tp + (s >= NSEG - 12 ? GR3 : GR2);
      }
      asm volatile("" ::: "memory");
    } else
    grp_wait<SLP_R, 4, GATE_R>(true, nullptr, 0, FL + FL_FW + ((RL * 64 + s) * 4 + (blockIdx.x & 3)) * 4 * 32, 16, TMO, tl);
    if (s >= NSEG - 6 && !(EF != 0 && GR2 != 0)) tl -= GF_R - (s >= NSEG - 4 ? GF_RT : GF_RU);
    if (!EF) DRAIN();
    TS(0, s, w * 64);
    v4u p[8][2];
    if (!EF) xr = LD128(rsX, (l & 31) * 512 + r * 16, s * 32768 + u * 16384);
    f32x4 ac[2];
    const int sr = EF ? s & ((1 << RGB) - 1) : s;
    const unsigned old = (s >> RGB) & 1;
    for (unsigned spins = 0;; ++spins) {
      int so = (RL * 64 + sr) * 16 * 32768 + rp * 2048 + u * 1024;
#pragma unroll
      for (int k = 0; k < 8; ++k) {
        asm volatile("" : "+s"(so));
#pragma unroll
        for (int b = 0; b < 2; ++b) p[k][b] = __builtin_amdgcn_raw_buffer_load_b128(rsW, po + b * 512, so, RAUX);
        so += 65536;
      }
      __builtin_amdgcn_sched_barrier(0);
      unsigned z[2] = {1u, 1u};
      ac[0] = ac[1] = f32x4{};
#pragma unroll
      for (int k = 0; k < 8; ++k)
#pragma unroll
        for (int b = 0; b < 2; ++b) {
          ac[b] = MFMA16(BF8(sel), BF8(p[k][b]), ac[b]);
          z[b] = __builtin_amdgcn_bitop3_b32(z[b], p[k][b].x, old, 0x60);
          __builtin_amdgcn_sched_barrier(0);
        }
      if (!EF || !RVAL || __all((z[0] & z[1]) != 0u) || spin_timeout(spins, TMO)) break;
      if (SLP_D) __builtin_amdgcn_s_sleep(SLP_D);
      asm volatile("" ::: "memory");
    }
    v4u xo;
    {
      const auto x = __builtin_amdgcn_permlane16_swap(pk2s(ac[0][0], ac[0][1]), pk2s(ac[1][0], ac[1][1]), false, false);
      const auto y = __builtin_amdgcn_permlane16_swap(pk2s(ac[0][2], ac[0][3]), pk2s(ac[1][2], ac[1][3]), false, false);
      const v4u a = {x[0], y[0], x[1], y[1]};
#pragma unroll
      for (int e = 0; e < 4; ++e) {
        xo[e] = pk2s(lo16(xr[e]) + lo16(a[e]), hi16(xr[e]) + hi16(a[e]));
        if (kv) xo[e] = xo[e] ? xo[e] : 0x8000u;
      }
      if (kv) ST128(xo, rsY, yo, s * SEG * DM * 2);
      else ST128A(xo, rsY, yo, s * SEG * DM * 2, OAUX);
    }
    TS(1, s, w * 64);
    if (kv) {
      const float pv = lo16(g < 2 ? __builtin_amdgcn_readlane(xo[0], 0) : __builtin_amdgcn_readlane(xo[0], 32));
      float sm = 0.f, q0 = 0.f, q1 = 0.f;
#pragma unroll
      for (int e = 0; e < 4; ++e) {
        const float a = one(lo16(xo[e]) - pv), b = one(hi16(xo[e]) - pv);
        sm = one(sm + one(a + b));
        q0 = one(q0 + a * a);
        q1 = one(q1 + b * b);
      }
      sm = half_sum(sm);
      const float q = fmaxf(one(half_sum(q0 + q1)) - sm * sm * (1.0f / 256), 0.f);
      const u64 st = (u64)__float_as_uint(sm + pv * 256.f) | (u64)(__float_as_uint(q) | 1u) << 32;
      if ((l & 31) < 4) __hip_atomic_store((gu64*)(FL + FL_FR + ((s * 4 + (l & 3)) * 4 + (r >> 3)) * 32 + (r & 7) * 4 + 2 * u), st, RLX_AGENT);
      if (XZP == 4 && s + 2 * RG < NSEG) ST128((v4u{0u, 0u, 0u, 0u}), rsY, yo, (s + 2 * RG) * SEG * DM * 2);
    }
    if (EF && s + (1 << RGB) < NSEG) PW_FREE(RL, s + (1 << RGB), 2 * rp + u);
    TS(3, s, w * 64);
  }
  if (L == 1 && idx == 0 && w == 0 && l == 0) __hip_atomic_store((gu64*)(ws + OFF_EP), tag, RLX_AGENT);
}
__device__ __forceinline__ void role_proj(const MegaArgs& A, char* smem, const int L, const int idx, const bool kv) {
  if (A.wv < 4) role_proj_t<false>(A, smem, L, idx, kv);
  else role_proj_t<true>(A, smem, L, idx, kv);
}

constexpr int CUN = 2144;
__device__ __forceinline__ v2u part_stats(const v4u o) {
  float v[8];
#pragma unroll
  for (int e = 0; e < 4; ++e) { v[2 * e] = lo16(o[e]); v[2 * e + 1] = hi16(o[e]); }
  const float s = ((v[0] + v[1]) + (v[2] + v[3])) + ((v[4] + v[5]) + (v[6] + v[7])), m = s * 0.125f;
  float q0 = 0.f, q1 = 0.f;
#pragma unroll
  for (int e = 0; e < 4; ++e) {
    const float a = v[e] - m, b = v[4 + e] - m;
    q0 += a * a;
    q1 += b * b;
  }
  return v2u{__float_as_uint(s), __float_as_uint(q0 + q1)};
}
constexpr int CRN = 176;
constexpr int CTN = 272;
constexpr int CV_LDS = (8 * CKN + 8 * 2 * CUN + 64 + 2 * 32 * 8 + 16 * CRN + 64 + 8 * CTN) * 2 + 8 * 1568;
__device__ __forceinline__ void role_conv(const MegaArgs& A, char* smem, const int cv) {
  u16* Kr = reinterpret_cast<u16*>(smem);
  u16* Uc = Kr + 8 * CKN;
  u16* Us = Uc + 8 * CUN + 63;
  u16* To = Uc + 8 * 2 * CUN + 64;
  u16* Sx = To + 32 * 8;
  u16* Rc = Sx + 32 * 8;
  u16* Rs = Rc + 8 * CRN + 63;
  u16* Ta = Rc + 16 * CRN + 64;
  const int w = A.wv;
  const int l = lane_id(), tid = 64 * w + l, c = l & 15, g = l >> 4;
  const int d = 8 * cv + w;
  char* ws = A.ws;
  unsigned* FL = (unsigned*)(ws + OFF_FL);
  unsigned* TMO = FL + FL_TMO;
  u16* kr = Kr + w * CKN;
  const u16* ka = kr + 2080 - 16 * c + 8 * g;
#define KFRAG(D0) (*reinterpret_cast<const f16x8*>(ka - (D0)))
  typedef __attribute__((address_space(3))) const unsigned* lpu;
#define BFRAG(a) __builtin_bit_cast(f16x8, (v4u{((lpu)(unsigned long)(a))[0], ((lpu)(unsigned long)(a))[1], ((lpu)(unsigned long)(a))[2], ((lpu)(unsigned long)(a))[3]}))
  f32x4 acc[8];
  v4u tx[4];
  const __amdgpu_buffer_rsrc_t rz1 = MKRS(ws + OFF_XA, SZ_X), rps = MKRS(ws + OFF_H, SZ_X);
  u16* uc = Uc + w * CUN;
  const unsigned ub = lds_base(reinterpret_cast<char*>(((c & 1) ? Us : Uc) + w * CUN + c + 8 * g + 32));
  const int t0 = 16 * c + 4 * g;
  float dsk, ysc;
#define SKIP(T) (*reinterpret_cast<const v2u*>(uc + 32 + 256 * (T) + t0))
#define TILE_OUT(dst, a, uu)                                                                   \
  {                                                                                            \
    v2u v_;                                                                                    \
    _Pragma("unroll") for (int e = 0; e < 2; ++e) {                                            \
      const unsigned yy = pk2(a[2 * e] * ysc, a[2 * e + 1] * ysc), u_ = uu[e];                 \
      const unsigned k_ = pk2(h2f((u16)u_) * dsk, h2f((u16)(u_ >> 16)) * dsk);                 \
      v_[e] = pk2(lo16(yy) + lo16(k_), hi16(yy) + hi16(k_));                                   \
    }                                                                                          \
    *reinterpret_cast<v2u*>((dst) + t0) = v_;                                                  \
  }
#define CHUNK_ST(i, src, N) CHUNK_STX(i, tx[i], src, N)
#define CHUNK_STX(i, X, src, N)                                                                \
  {                                                                                            \
    const unsigned p_ = lds_base((char*)((src) + (c >> 2) * (N) + (tid & ~15) + 4 * (c & 3))); \
    const v2u a0_ = trd(p_), a1_ = trd(p_ + 8 * (N));                                          \
    const v4u a_ = {a0_.x, a0_.y, a1_.x, a1_.y};                                               \
    v4u o;                                                                                     \
    _Pragma("unroll") for (int e = 0; e < 4; ++e) o[e] = pk2(lo16(X[e]) + lo16(a_[e]), hi16(X[e]) + hi16(a_[e])); \
    ST128(o, rz1, (l >> 5) * 32768 + (l & 31) * 16, (2 * (w + 8 * (i)) * 64 + cv) * 512);      \
    ST64(part_stats(o), rps, (l >> 5) * 16384 + (l & 31) * 8, (2 * (w + 8 * (i)) * 64 + cv) * 256); \
  }
#define CHUNK_FLAG(i) set_flag(FL + FL_FC + (((2 * w + 16 * ((i) + (l >> 3)) + ((l >> 2) & 1)) * 4 + (l & 3)) * 8 + (cv >> 3)) * 32 + (cv & 7))
  {
#define U_VAL(i)                                                                               \
  {                                                                                            \
    const float mb_ = lo16(st[i]), ri_ = 1024.0f / hi16(st[i]);                                 \
    _Pragma("unroll") for (int e = 0; e < 4; ++e) {                                            \
      const unsigned t_ = pk2(lo16(tx[i][e]) - mb_, hi16(tx[i][e]) - mb_);                     \
      const unsigned q_ = pk2(lo16(t_) * ri_, hi16(t_) * ri_);                                 \
      uv[e] = pkh(lo16(q_), hi16(q_));                                                         \
    }                                                                                          \
  }
    unsigned ta[3], k0p, st[4];
    v4u uv;
    v2u kn;
    u64 tag;
    const __amdgpu_buffer_rsrc_t rst = MKRS(ws + OFF_GT, (L_SEQ + 512) * 16);
    {
      KGen G;
      G.load(A, d, l);
      const u16 pk = A.Dskip[d];
      asm volatile("" ::: "memory");
      if (XL == 1) ask_slow(A, 16 * (w - 4) + (cv & 15), ta);
      if (XL) asm volatile("" : "+v"(G.pa), "+v"(G.pb), "+v"(G.pc), "+v"(G.pd), "+v"(G.pe)::"memory");
      tx[0] = *reinterpret_cast<const v4u*>(A.x0 + (size_t)tid * DM + 8 * cv);
      if (XL != 1) ask_slow(A, 16 * (w - 4) + (cv & 15), ta);
      tag = call_tag(A);
      G.setup();
      dsk = bf2f(pk) * (1.0f / 1024);
      G.st = smem + w * 16640;
      SST(0);
      ysc = G.scale();
      const Cx z = G.power(1.f);
      Cx y = cmul(G.C, z);
      G.rows(G.st + l * 8, y, z, 16);
      G.get(G.bh, G.bl, l);
      const float k0 = 2.0f * wave_sum(G.C.r.x + G.C.r.y);
      k0p = l == 0 ? pkh(k0, 0.f) : 0u;
      if (TP) {
        constexpr int R = 64 * TP, U0 = 2 * R + 32, U1 = 3 * R + 111, KN = 4 * R + 192;
        u16* k1 = reinterpret_cast<u16*>(smem + 8 * 16640) + w * KN;
        u16* t1 = reinterpret_cast<u16*>(smem + 8 * 16640) + 8 * KN;
        v4u s1 = {};
        u64 rv = 0;
        if (w < TP) {
          s1 = LD128(rst, tid * 16, (L_SEQ + R * (cv >> 4)) * 16);
          rv = ready_look(A);
        }
        if (l < R / 8 + 4) reinterpret_cast<v4u*>(k1)[l < R / 8 ? l + R / 8 + 4 : l - R / 8] = v4u{k0p, 0u, 0u, 0u};
        k1[U0 + (l < 32 ? l : R + l)] = k1[U1 + (l < 32 ? l : R + l)] = 0;
        G.columns(l, 0, R / 16);
        const f32x4 kt = G.tile<true>(l);
        if (c < R / 16) *reinterpret_cast<v2u*>(k1 + R + 28 - 16 * c - 4 * g) = v2u{pkh(kt.w, kt.z), pkh(kt.y, kt.x)};
        if (w < TP) {
          for (unsigned spins = 0; !__all(has_tag(tag, s1.x, s1.y) && has_tag(tag, s1.z, s1.w)) && spins < SPIN_CAP; ++spins) {
            __builtin_amdgcn_s_sleep(4);
            s1 = LD128(rst, tid * 16, (L_SEQ + R * (cv >> 4)) * 16);
          }
          st[0] = __builtin_amdgcn_perm(s1.w, s1.y, 0x07060302);
          U_VAL(0);
          u16* up = reinterpret_cast<u16*>(smem + 8 * 16640) + 32 + tid;
#pragma unroll
          for (int e = 0; e < 8; ++e) up[e * KN + U0] = up[e * KN + U1] = (u16)(uv[e >> 1] >> (16 * (e & 1)));
        }
        RAW_BAR();
        const unsigned u1 = lds_base(reinterpret_cast<char*>(k1 + ((c & 1) ? U1 : U0) + c + 8 * g));
        f32x4 a = {};
#pragma unroll
        for (int b = 0; b <= R / 32; ++b)
          a = MFMAH(BFRAG(u1 + 64 * b), *reinterpret_cast<const f16x8*>(k1 + R - 16 * (c & (R / 16 - 1)) + 8 * g + 32 * b), a);
        if (c < R / 16) {
          const v2u sk = *reinterpret_cast<const v2u*>(k1 + U0 + 32 + t0);
          TILE_OUT(t1 + w * (R + 16), a, sk);
        }
        RAW_BAR();
        SST(1);
        if (w < TP) CHUNK_ST(0, t1, R + 16);
        G.columns(l, R / 16, TPS);
        if (w < TP) {
          DRAIN();
          ready_wait(A, tag, rv);
          if (l < 8) CHUNK_FLAG(0);
        }
        SST(6);
        G.columns(l, TPS, 16);
      } else {
        G.columns(l, 0, 16);
      }
      const f32x4 kt = G.tile<true>(l);
      kn = v2u{pkh(kt.w, kt.z), pkh(kt.y, kt.x)};
    }
#define U_PUT(i)                                                                               \
  _Pragma("unroll") for (int e = 0; e < 4; ++e) {                                              \
    ((lph)(unsigned long)up0)[(2 * e) * CUN + 512 * (i)] = (u16)uv[e];                         \
    ((lph)(unsigned long)up0)[(2 * e + 1) * CUN + 512 * (i)] = (u16)(uv[e] >> 16);             \
    ((lph)(unsigned long)up1)[(2 * e) * CUN + 512 * (i)] = (u16)uv[e];                         \
    ((lph)(unsigned long)up1)[(2 * e + 1) * CUN + 512 * (i)] = (u16)(uv[e] >> 16);             \
  }
#define U_ROW(i) { U_VAL(i) U_PUT(i) }
    typedef __attribute__((address_space(3))) u16* lph;
    unsigned up0 = lds_base((char*)(Uc + 32 + tid)), up1 = lds_base((char*)(Us + 32 + tid));
    asm volatile("" : "+v"(up0), "+v"(up1), "+v"(kn), "+v"(k0p));
#define ST_GET(I0, I1)                                                                                                        \
  for (unsigned spins = 0; spins < SPIN_CAP; ++spins) {                                                                       \
    bool ok = true;                                                                                                           \
    v4u t_[4];                                                                                                                \
    _Pragma("unroll") for (int i = I0; i < I1; ++i) t_[i] = LD128(rst, (tid + 512 * i) * 16, 0);                              \
    _Pragma("unroll") for (int i = I0; i < I1; ++i) {                                                                         \
      ok = ok && has_tag(tag, t_[i].x, t_[i].y) && has_tag(tag, t_[i].z, t_[i].w);                                            \
      st[i] = __builtin_amdgcn_perm(t_[i].w, t_[i].y, 0x07060302);                                                            \
    }                                                                                                                         \
    if (__all(ok)) break;                                                                                                     \
    __builtin_amdgcn_s_sleep(4);                                                                                              \
    asm volatile("" ::: "memory");                                                                                            \
  }
    if (w < 4) {
      ST_GET(0, 2);
      U_VAL(0);
      asm volatile("" : "+v"(uv));
    }
#pragma unroll
    for (int i = 1; i < 4; ++i) tx[i] = *reinterpret_cast<const v4u*>(A.x0 + (size_t)(tid + 512 * i) * DM + 8 * cv);
    RAW_BAR();
    SST(2);
    if (w < 4) { U_PUT(0) }
    *reinterpret_cast<v2u*>(kr + 2076 - 16 * c - 4 * g) = kn;
    if (l < 38) reinterpret_cast<v4u*>(kr)[l < 32 ? 260 + l : l < 34 ? 194 + l : l - 34] = v4u{k0p, 0u, 0u, 0u};
    (l < 32 ? uc : Us + w * CUN - 32)[l] = 0;
    uc[32 + L_SEQ + l] = Us[w * CUN + 32 + L_SEQ + l] = 0;
    uc[32 + 256 + l] = Us[w * CUN + 32 + 256 + l] = 0;
    RAW_BAR();
    SST(11);
    {
      f32x4 a0 = {}, a1 = {};
      f16x8 bf[9], af[9];
#pragma unroll
      for (int b = 0; b < 9; ++b) {
        af[b] = KFRAG(32 - 32 * b);
        bf[b] = BFRAG(ub + 64 * (b - 1));
      }
      const v2u k0 = SKIP(0);
      __builtin_amdgcn_sched_barrier(0);
#pragma unroll
      for (int b = 0; b < 9; ++b) (b & 1 ? a1 : a0) = MFMAH(bf[b], af[b], (b & 1 ? a1 : a0));
      a0 += a1;
      TILE_OUT(Ta + w * CTN, a0, k0);
    }
    const u64 rv = ready_look(A);
    RAW_BAR();
    SST(3);
    if (w < 4) {
      if (w >= TP) CHUNK_ST(0, Ta, CTN);
      asm volatile("" : "+v"(tx[1]));
      U_ROW(1);
      DRAIN();
      ready_wait(A, tag, rv);
      if (l < 8 && w >= TP) CHUNK_FLAG(0);
      ST_GET(2, 4);
    } else {
      ST_GET(0, 4);
      ready_wait(A, tag, rv);
      asm volatile("" : "+v"(tx[0]), "+v"(tx[1]));
      U_ROW(0);
      U_ROW(1);
    }
    ASKED(ta);
    if (tid < (16 * CRN + 64) / 8) reinterpret_cast<v4u*>(Rc)[tid] = v4u{0u, 0u, 0u, 0u};
    SST(4);
    const gu64* fk = (gu64*)(ws + OFF_KG) + cv * 16 + w;
    u64 kg = __hip_atomic_load(fk, RLX_AGENT);
    U_ROW(2);
    for (unsigned spins = 0; !__all(kg == tag) && spins < SPIN_CAP; ++spins) {
      __builtin_amdgcn_s_sleep(8);
      kg = __hip_atomic_load(fk, RLX_AGENT);
    }
    asm volatile("" : "+v"(tx[3]), "+v"(st[3])::"memory");
    SST(5);
    const unsigned* kd = reinterpret_cast<const unsigned*>(Ta + 8 * CTN) + w * 392;
    glds16c(ws + OFF_KF + d * 2048, l * 16, lds_base((char*)kd));
    if (l < 34) glds16c(ws + OFF_KF + d * 2048 + 1024, l * 16, lds_base((char*)kd) + 1024);
    U_ROW(3);
    DRAIN();
    {
#define MIX(lo, hi) __builtin_amdgcn_perm(hi, lo, 0x07060100)
      const unsigned a0 = kd[384 - l], b0 = kd[383 - l], a1 = kd[320 - l], b1 = kd[319 - l];
      const unsigned a2 = kd[256 - 2 * l], m2 = kd[255 - 2 * l], b2 = kd[254 - 2 * l];
      const unsigned* k3 = kd + 124 - 4 * (l & 31);
      v4u* ko = reinterpret_cast<v4u*>(kr) + l;
      ko[4] = v4u{a0, a0, (l & 1) ? b0 : MIX(a0, b0), b0};
      ko[68] = v4u{a1, a1, (l & 1) ? b1 : MIX(a1, b1), b1};
      ko[132] = v4u{a2, MIX(a2, m2), m2, b2};
      if (l < 32) ko[196] = v4u{k3[4], MIX(k3[3], k3[2]), k3[2], MIX(k3[1], k3[0])};
#undef MIX
    }
    RAW_BAR();
    SST(7);
    {
      unsigned uq[4] = {ub, ub + 1024, ub + 2048, ub + 3072};
      f16x8 bf[8], a[4];
      const f16x8 bm = BFRAG(ub - 64);
      asm volatile("" : "+v"(uq[0]), "+v"(uq[1]), "+v"(uq[2]), "+v"(uq[3]));
#pragma unroll
      for (int j = 1; j < 8; ++j) bf[j] = BFRAG(uq[j >> 1] + 512 * (j & 1));
#pragma unroll
      for (int T = 1; T < 8; ++T) acc[T] = MFMAH(bm, KFRAG(256 * T + 32), (f32x4{}));
#pragma unroll
      for (int e = 0; e < 3; ++e) a[e] = KFRAG(256 * e);
      const u16* kq = ka - 256 * 7;
#pragma unroll 1
      for (int r = 0; r < 8; ++r) {
#pragma unroll
        for (int e = 0; e < 8; ++e) {
          a[(e + 3) & 3] = *reinterpret_cast<const f16x8*>(kq + 256 * (7 - ((e + 3) & 7)) + (e >= 5 ? 32 : 0));
          const int j = (8 - e) & 7;
          bf[j] = BFRAG(uq[j >> 1] + 512 * (j & 1) + (e ? 64 : 0));
          __builtin_amdgcn_sched_barrier(0);
#pragma unroll
          for (int T = 7; T >= (e > 1 ? e : 1); --T) acc[T] = MFMAH(bf[T - e], a[e & 3], acc[T]);
          __builtin_amdgcn_sched_barrier(0);
        }
        kq += 32;
#pragma unroll
        for (int j = 0; j < 4; ++j) uq[j] += 64;
        asm volatile("" : "+v"(uq[0]), "+v"(uq[1]), "+v"(uq[2]), "+v"(uq[3]));
      }
    }
    SST(8);
    {
      v2u k[8];
#pragma unroll
      for (int T = 1; T < 8; ++T) k[T] = SKIP(T);
#pragma unroll
      for (int T = 1; T < 8; ++T) TILE_OUT(uc + 32 + 256 * T, acc[T], k[T]);
    }
    RAW_BAR();
    SST(10);
    if (w >= 4) CHUNK_ST(0, Uc + 32, CUN);
    CHUNK_ST(1, Uc + 32 + 512, CUN);
    if (PP < 0) {
      if (CVSP) __builtin_amdgcn_s_sleep(CVSP);
      CHUNK_ST(2, Uc + 32 + 1024, CUN);
      if (CVSP) __builtin_amdgcn_s_sleep(CVSP);
      CHUNK_ST(3, Uc + 32 + 1536, CUN);
    }
    if (w == 1) (void)wait_n<SLP_V, 32>(FL + FL_FZ + (blockIdx.x & 7) * 64, 64, TMO);
    DRAIN();
    if (l < (PP < 0 ? 32 : 16)) CHUNK_FLAG(0);
    SST(9);
#undef U_ROW
#undef U_VAL
#undef U_PUT
#undef ST_GET
  }
  const unsigned rb = lds_base(reinterpret_cast<char*>(((c & 1) ? Rs : Rc) + w * CRN + c + 8 * g));
#pragma unroll
  for (int j = 0; j < 8; ++j) acc[j] = f32x4{};
  const __amdgpu_buffer_rsrc_t rx = MKRS(ws + OFF_X2, SZ_X), rz = MKRS(ws + OFF_X3, SZ_X);
  const __amdgpu_buffer_rsrc_t rf = MKRS(FL + FL_FR, 64 * 16 * 128);
  v2u st = {0u, 0u}, xx = {0u, 0u};
#define X2_LOAD(ss, far)                                                                                                      \
  {                                                                                                                      \
    xx = LD64(rx, (l >> 1) * (DM * 2) + (l & 1) * 8, (far) ? (int)SZ_X : ((ss) * SEG * DM + 8 * cv) * 2);                 \
    st = LD64(rf, l * 8, ((ss) * 4 + (blockIdx.x & 3)) * 512);                                                           \
  }
  unsigned tl = now10(), mk = 0u;
  const __amdgpu_buffer_rsrc_t rm = MKRS(FL + FL_FW + (blockIdx.x & 3) * 128, 63 * 2048 + 512);
  bool late = false;
  for (int s = 0; s < NSEG; ++s) {
    const int q = s & 3, q8 = s & 7;
    if (w == 1) {
      late = __builtin_popcountll(__ballot(st.y != 0u)) >= CVLATE;
      for (unsigned spins = 0; !__all(umin3(xx.x, xx.y, st.y) != 0u); ++spins) {
        if (spin_timeout(spins, TMO)) break;
        if (CE) {
          if (spins) __builtin_amdgcn_s_sleep(SLP_V);
          else gate(__all(mk != 0u) ? latest(mk, tl) + CE : tl);
          X2_LOAD(s, spins && __builtin_popcountll(__ballot(st.y != 0u)) < CVT2);
          continue;
        }
        if (GF_V && s >= GF0) {
          if (spins) __builtin_amdgcn_s_sleep(SLP_V);
          else if (s < NSEG - 6) gate(tl);
          tl = now10() + GF_V;
          X2_LOAD(s, CVT2 > 64 ? 0 : spins && __builtin_popcountll(__ballot(st.y != 0u)) < CVT2);
          continue;
        }
        if (GATE_V && spins == 0 && !__any(st.y != 0u)) gate(tl);
        if (spins && !__all(st.y != 0u)) __builtin_amdgcn_s_sleep(SLP_V);
        X2_LOAD(s, __builtin_popcountll(__ballot(st.y != 0u)) < CVTHR);
      }
      if (!GF_V || s < GF0) tl = now10() + GATE_V;
      if (CE) mk = __builtin_amdgcn_raw_buffer_load_b32(rm, (c >> 2) * 128 + (c & 3) * 4, (s + 1) * 2048, AUX_SC1);
      TS(0, s, 64);
      const float s0 = __uint_as_float(st.x), s1 = dppr<0xb1>(s0), m2 = __uint_as_float(st.y);
      const float var = (m2 + dppr<0xb1>(m2) + (s0 - s1) * (s0 - s1) * (1.0f / DM)) * (1.0f / DM);
      const float mb = bfr1((s0 + s1) * (1.0f / DM)), ri = 1024.0f * __builtin_amdgcn_rcpf(bfr1(__builtin_amdgcn_sqrtf(bfr1(bfr1(var) + 1e-5f))));
      const unsigned t0 = pk2(lo16(xx.x) - mb, hi16(xx.x) - mb), t1 = pk2(lo16(xx.y) - mb, hi16(xx.y) - mb);
      const unsigned b0 = pk2(lo16(t0) * ri, hi16(t0) * ri), b1 = pk2(lo16(t1) * ri, hi16(t1) * ri);
      const unsigned u0 = pkh(lo16(b0), hi16(b0)), u1 = pkh(lo16(b1), hi16(b1));
      u16* rp = Rc + (l & 1) * 4 * CRN + 32 * q + (l >> 1);
#define R_PUT(o) { rp[o] = (u16)u0; rp[(o) + CRN] = (u16)(u0 >> 16); rp[(o) + 2 * CRN] = (u16)u1; rp[(o) + 3 * CRN] = (u16)(u1 >> 16); }
      R_PUT(0) R_PUT(8 * CRN + 63)
      if (q == 0) { R_PUT(128) R_PUT(8 * CRN + 63 + 128) }
#undef R_PUT
      *reinterpret_cast<v2u*>(Sx + l * 4) = xx;
    }
    const u16* kq = ka + 32 * q8;
    const f16x8 a0 = *reinterpret_cast<const f16x8*>(kq - 32), a1 = *reinterpret_cast<const f16x8*>(kq);
    __syncthreads();
    TS(1, s, 0);
    const f16x8 bp = BFRAG(rb + 64 * ((q + 3) & 3)), bc = BFRAG(rb + 64 * q);
    const float u0 = h2f(Rc[w * CRN + 32 * q + c]), u1 = h2f(Rc[w * CRN + 32 * q + 16 + c]);
    const float x0 = bf2f(Sx[c * 8 + w]), x1 = bf2f(Sx[(16 + c) * 8 + w]);
    acc[0] = MFMAH(a0, bp, (q8 == 0 ? acc[1] : acc[0]));
    const f32x4 tmp = MFMAH(a1, bc, acc[0]);
    const unsigned uu = pk2(u0 * dsk, u1 * dsk);
    const unsigned yy = pk2(((q & 1) ? tmp[2] : tmp[0]) * ysc, ((q & 1) ? tmp[3] : tmp[1]) * ysc);
    const unsigned aa = pk2(lo16(yy) + lo16(uu), hi16(yy) + hi16(uu)), oo = pk2(x0 + lo16(aa), x1 + hi16(aa));
    if (g == (q8 >> 1)) {
      To[c * 8 + w] = (u16)oo;
      To[(16 + c) * 8 + w] = (u16)(oo >> 16);
    }
    __syncthreads();
    if (w == 0 && l < 32) {
      const v4u o = *reinterpret_cast<const v4u*>(To + l * 8);
      ST128A(o, rz, l * 16, (s * 64 + cv) * 512, XAUX);
      ST64(part_stats(o), rps, l * 8, ((64 + s) * 64 + cv) * 256);
    }
    if (w == 1) {
      if (CE || (GF_V && s + 1 >= GF0)) st.y = 0u;
      else X2_LOAD(s + 1, !late);
    }
    const int left = 7 - (s >> 3);
#define PUSH(J0, SH)                                                                                              \
  if (left >= (J0)) {                                                                                             \
    _Pragma("unroll") for (int j = (J0); j < (J0) + 2 && j < 8; ++j)                                              \
      acc[j] = MFMAH(*reinterpret_cast<const f16x8*>(kq - 32 - 256 * j), bp, (j + (SH) < 8 ? acc[(j + (SH)) & 7] : f32x4{})); \
  }
    TS(2, s, 0);
    if (w == 0) {
      DRAIN();
      if (l < 4) set_flag(FL + FL_FC + (((64 + s) * 4 + l) * 8 + (cv >> 3)) * 32 + (cv & 7));
    }
    TS(3, s, 0);
    if (q8 == 0) { PUSH(1, 1) PUSH(3, 1) PUSH(5, 1) PUSH(7, 1) }
    else { PUSH(1, 0) PUSH(3, 0) PUSH(5, 0) PUSH(7, 0) }
#undef PUSH
    if (PP >= 0 && (unsigned)(s - PP) < 3u) {
      const int i = 2 + s - PP;
      if (i > 2) {
        DRAIN();
        if (l < 8) CHUNK_FLAG(i - 1);
        tx[2] = tx[3];
      }
      if (i < 4) CHUNK_STX(i, tx[2], Uc + 32 + 512 * i, CUN);
    }
    MSTAMP();
  }
#undef KFRAG
#undef BFRAG
#undef X2_LOAD
#undef TILE_OUT
#undef SKIP
#undef CHUNK_ST
#undef CHUNK_STX
#undef CHUNK_FLAG
}

__device__ __forceinline__ v4u ln_row_packed(const v4u xr) {
  float v[8];
#pragma unroll
  for (int e = 0; e < 4; ++e) { v[2 * e] = lo16(xr[e]); v[2 * e + 1] = hi16(xr[e]); }
  float s = ((v[0] + v[1]) + (v[2] + v[3])) + ((v[4] + v[5]) + (v[6] + v[7]));
  s = wave_sum(s);
  const float mean = s * (1.0f / DM);
  float q = 0.f;
#pragma unroll
  for (int j = 0; j < 8; ++j) { float t = v[j] - mean; q += t * t; }
  q = wave_sum(q);
  const float var = q * (1.0f / DM);
  const float mb = bfr(mean);
  const float sd = bfr(sqrtf(bfr(bfr(var) + 1e-5f)));
  const float ri = 1.0f / sd;
  v4u o;
#pragma unroll
  for (int e = 0; e < 4; ++e) {
    const unsigned t = pk2(v[2 * e] - mb, v[2 * e + 1] - mb);
    const float x0 = lo16(t), x1 = hi16(t);
    float q0 = x0 * ri, q1 = x1 * ri;
    q0 = __builtin_fmaf(__builtin_fmaf(-q0, sd, x0), ri, q0);
    q1 = __builtin_fmaf(__builtin_fmaf(-q1, sd, x1), ri, q1);
    o[e] = pk2(q0, q1);
  }
  return o;
}

__device__ __forceinline__ void ln4_packed(v4u (&x)[4]) {
  f32x2_t v[16];
#pragma unroll
  for (int e = 0; e < 16; ++e) v[e] = f32x2_t{lo16(x[e >> 2][e & 3]), hi16(x[e >> 2][e & 3])};
  const f32x2_t t = (((v[0] + v[1]) + (v[2] + v[3])) + ((v[4] + v[5]) + (v[6] + v[7]))) +
                    (((v[8] + v[9]) + (v[10] + v[11])) + ((v[12] + v[13]) + (v[14] + v[15])));
  const float mean = red_mod4(t.x + t.y) * (1.0f / DM);
  const float mb = bfr1(mean);
  f32x2_t q0 = {0.f, 0.f}, q1 = {0.f, 0.f};
#pragma unroll
  for (int e = 0; e < 16; e += 2) {
    v[e] -= mb;
    v[e + 1] -= mb;
    q0 += v[e] * v[e];
    q1 += v[e + 1] * v[e + 1];
  }
  q0 += q1;
  const float dm = mean - mb;
  const float var = fmaxf(red_mod4(q0.x + q0.y) * (1.0f / DM) - dm * dm, 0.f);
  const float sd = bfr1(sqrtf(bfr1(bfr1(var) + 1e-5f)));
  const float ri = 1.0f / sd;
#pragma unroll
  for (int e = 0; e < 16; ++e) {
    const unsigned t2 = pk2(v[e].x, v[e].y);
    const f32x2_t q = f32x2_t{lo16(t2), hi16(t2)} * ri;
    x[e >> 2][e & 3] = pk2(q.x, q.y);
  }
}

constexpr int MEGA_WGS = 256;
constexpr size_t MEGA_LDS = (size_t)(32 * 4 * 64 * 8 + 8 * SMS + 15 * QS + 128 + 16 * 64 * 8 + 8 * SOS + 512) * 2;
static_assert(MEGA_LDS <= 163840 && MEGA_LDS - 480 >= C_SQ + 4096, "lds");
static_assert(MEGA_LDS >= (size_t)AW_CNT + 128 && MEGA_LDS >= (size_t)P_CN + 512, "lds");
static_assert(MEGA_LDS >= (size_t)CV_LDS, "lds");
static_assert(MEGA_LDS >= (size_t)2 * 32 * SMS * 2 && MEGA_LDS >= 131072, "lds");
static_assert(MEGA_LDS >= (size_t)8 * (16640 + 2 * (704 + 144)), "lds");
__device__ __forceinline__ MegaArgs role_args(const int b, const int wv, const unsigned k0, const unsigned k1) {
  auto p = (const __attribute__((address_space(4))) u64*)__builtin_amdgcn_kernarg_segment_ptr();
  asm volatile("s_waitcnt lgkmcnt(0)" : "+s"(p) : "s"(k0), "s"(k1));
#define KA(i) ((u16*)(__attribute__((address_space(1))) u16*)p[i])
  return MegaArgs{KA(0), KA(1), (char*)KA(2), KA(3), KA(4), KA(5), KA(6), KA(7), KA(8), KA(9), KA(10), KA(11), KA(12), KA(13), b, wv};
#undef KA
}
__global__ __launch_bounds__(512, 2) void mega_kernel(const u16* mem0, const u16* Dskip, char* ws, u16* out, const u16* x0,
                                                      const u16* logAre, const u16* Aim, const u16* Cre, const u16* Cim,
                                                      const u16* logdt, const u16* Wq, const u16* Wk, const u16* Wv,
                                                      const u16* Wo) {
  extern __shared__ __attribute__((aligned(16))) char smem[];
  unsigned k0 = 0u, k1 = 0u;
  if (IPF == 3) {
    unsigned o;
    asm volatile(
        "s_load_dword %0, %3, 0\n\ts_load_dword %1, %3, 64\n\ts_getpc_b64 s[6:7]\n.LKG_PC:\n\ts_bfe_u32 s4, %4, 0x30003\n\t"
        "v_lshl_add_u32 %2, %5, 3, s4\n\tv_lshlrev_b32 %2, 7, %2\n\ts_mov_b32 m0, %6\n\ts_mov_b32 s4, .LKG_LAST-.LKG_PC\n\t"
        "s_max_i32 s4, s4, .LKG_LAS2-.LKG_PC\n\tv_cmp_gt_i32 vcc, s4, %2\n\t"
        "s_and_saveexec_b64 s[4:5], vcc\n\tglobal_load_lds_dword %2, s[6:7]\n\ts_mov_b64 exec, s[4:5]"
        : "=&s"(k0), "=&s"(k1), "=&v"(o)
        : "s"(__builtin_amdgcn_kernarg_segment_ptr()), "s"(blockIdx.x), "v"(threadIdx.x), "s"(lds_base(smem) + (unsigned)MEGA_LDS - 480)
        : "memory", "vcc", "scc", "s4", "s5", "s6", "s7");
  }
  const int b = vbid(), wv = __builtin_amdgcn_readfirstlane(threadIdx.x >> 6);
#define A role_args(b, wv, k0, k1)
  u64 tag = 0;
  if (__builtin_expect((0xde000ff0u >> (b >> 3)) & 1u, 0)) {
    const unsigned t0 = now10();
    if ((b >= 32 && b < 64) || (b >= 200 && b < 232)) start_kgen(A, smem, b < 64 ? b - 32 : b - 168);
    else if (b >= 64 && b < 96) tag = start_stats<4>(A, b - 64);
    else if (b >= 240) (void)start_stats<8>(A, b - 224);
    const unsigned dl = (b >= 32 && b < 64) ? DL_C : (b >= 64 && b < 96) ? DL_A : b >= 240 ? DL_R2 : ((b >= 96 && b < 104) || b >= 232) ? DL_R : (b >= 200 && b < 232) ? DL_Q : 0u;
    while (now10() - t0 < dl) __builtin_amdgcn_s_sleep(16);
    __builtin_amdgcn_s_waitcnt(0x0f70);
  }
  if ((EXP_OFF & 1) && b >= 64 && b < 80) return;
  if ((EXP_OFF & 8) && b >= 200 && b < 232) return;
  if ((EXP_OFF & 16) && b >= 32 && b < 64) return;
  if ((EXP_OFF & 32) && b >= 80 && b < 96) return;
  if (b < 64) role_chain(A, smem, b >> 5, b & 31, 0);
  else if (b < 96) role_aw(A, smem, (b >> 4) & 1, b & 15, tag);
  else if (b >= 104 && b < 168) role_conv(A, smem, b - 104);
  else {
    const bool kv = b >= 168 && b < 232;
    role_proj(A, smem, b >= (kv ? 200 : 240), kv ? (b - 168) & 31 : b & 15, kv);
  }
#undef A
}

extern "C" void kernel_launch(void* const* d_in, const int* in_sizes, int n_in, void* d_out, int out_size, void* d_ws,
                              size_t ws_size, hipStream_t stream) {
  MegaArgs A;
  A.x0 = (const u16*)d_in[0];
  A.logAre = (const u16*)d_in[1];
  A.Aim = (const u16*)d_in[2];
  A.Cre = (const u16*)d_in[3];
  A.Cim = (const u16*)d_in[4];
  A.logdt = (const u16*)d_in[5];
  A.Dskip = (const u16*)d_in[6];
  A.mem0 = (const u16*)d_in[8];
  A.Wq = (const u16*)d_in[9];
  A.Wk = (const u16*)d_in[10];
  A.Wv = (const u16*)d_in[11];
  A.Wo = (const u16*)d_in[12];
  A.ws = (char*)d_ws;
  A.out = (u16*)d_out;

  (void)hipFuncSetAttribute((const void*)mega_kernel, hipFuncAttributeMaxDynamicSharedMemorySize, (int)MEGA_LDS);
  mega_kernel<<<MEGA_WGS, 512, MEGA_LDS, stream>>>(A.mem0, A.Dskip, A.ws, A.out, A.x0, A.logAre, A.Aim, A.Cre, A.Cim, A.logdt,
                                                   A.Wq, A.Wk, A.Wv, A.Wo);
}
```

```cpp
#include <hip/hip_runtime.h>
#include <hip/hip_bf16.h>
#include <stdint.h>

constexpr int L_SEQ = 2048;
constexpr int DM    = 512;
constexpr int NST   = 128;
constexpr int NH    = 32;
constexpr int DH    = 128;
constexpr int HD    = NH * DH;
constexpr int MM    = 8;
constexpr int SEG   = 32;
constexpr int NSEG  = L_SEQ / SEG;
constexpr int LK    = MM + SEG;

typedef unsigned short u16;
using bf16x8 = __attribute__((ext_vector_type(8))) short;
using f32x4  = __attribute__((ext_vector_type(4))) float;
using f32x16 = __attribute__((ext_vector_type(16))) float;

static __device__ __forceinline__ u16 f2bf(float f) {
  unsigned u = __float_as_uint(f);
  u += 0x7fffu + ((u >> 16) & 1u);
  return (u16)(u >> 16);
}
static __device__ __forceinline__ float bf2f(u16 h) { return __uint_as_float(((unsigned)h) << 16); }
static __device__ __forceinline__ float bfr(float f) { return bf2f(f2bf(f)); }

constexpr size_t SZ_X      = (size_t)L_SEQ * DM * 2;
constexpr size_t OFF_KF    = 0;
constexpr size_t OFF_UT    = OFF_KF + (size_t)DM * L_SEQ * 4;
constexpr size_t OFF_YT    = OFF_UT + SZ_X;
constexpr size_t OFF_XA    = OFF_YT + SZ_X;
constexpr size_t OFF_H     = OFF_XA + SZ_X;
constexpr size_t OFF_X2    = OFF_H + SZ_X;
constexpr size_t OFF_U2    = OFF_X2 + SZ_X;
constexpr size_t OFF_X3    = OFF_U2 + SZ_X;
constexpr size_t SZ_QK     = (size_t)L_SEQ * 2 * HD * 2;
constexpr size_t OFF_QK    = OFF_X3 + SZ_X;
constexpr size_t SZ_O      = (size_t)L_SEQ * HD * 2;
constexpr size_t OFF_VF    = OFF_QK + 2 * SZ_QK;
constexpr size_t SZ_PW     = (size_t)2 * NSEG * 16 * SEG * DM * 2;
constexpr size_t OFF_PW    = OFF_VF + 2 * SZ_O;
constexpr size_t SZ_MEM    = (size_t)NSEG * MM * HD * 2;
constexpr size_t OFF_MEMK  = OFF_PW + SZ_PW;
constexpr size_t OFF_MEMV  = OFF_MEMK + 2 * SZ_MEM;
constexpr size_t OFF_GT    = OFF_MEMV + 2 * SZ_MEM;
constexpr size_t OFF_EP    = OFF_GT + 65536;
constexpr size_t OFF_RD    = OFF_EP + 4096;
constexpr size_t OFF_KG    = OFF_RD + 4096;
constexpr size_t OFF_G     = OFF_KG + 8192;
constexpr size_t SZ_ACC    = (size_t)2 * 65536;
constexpr size_t OFF_Z     = OFF_GT + (size_t)128 * DM * 4;
constexpr size_t OFF_ACC   = OFF_Z;
constexpr size_t OFF_FL    = OFF_ACC + 2 * SZ_ACC;
constexpr int FL_FK  = 0;
constexpr int FL_FW  = FL_FK + 64 * 8 * 32;
constexpr int FL_FR  = FL_FW + 2 * 64 * 16 * 32;
constexpr int FL_FC  = FL_FR + 64 * 16 * 32;
constexpr int FL_FQ  = FL_FC + 2 * 64 * 32 * 32;
constexpr int FL_FP  = FL_FQ + 2 * 64 * 32 * 32;
constexpr int FL_FD  = FL_FP + 64 * 32 * 32;
constexpr int FL_FZ  = FL_FD + 64 * 2 * 32;
constexpr int FL_TMO = FL_FZ + 16 * 32;
constexpr int FL_END = FL_TMO + 64;
constexpr size_t Z_BYTES   = OFF_FL + (size_t)FL_END * 4 - OFF_Z;
constexpr size_t WS_TOTAL  = OFF_Z + Z_BYTES;
static_assert(Z_BYTES % 16 == 0 && OFF_Z % 256 == 0 && Z_BYTES <= 4 * 64 * 8192 && OFF_G + 2048 <= OFF_Z, "zero region");
static_assert(WS_TOTAL + (4 << 20) <= (size_t)256 * 1024 * 1024, "workspace too large");

static_assert(WS_TOTAL - OFF_XA <= (size_t)384 * 8 * 65536, "translation warm-up");
constexpr int CKN = 2336;
static_assert((size_t)DM * 2 * CKN * 2 <= (size_t)DM * L_SEQ * 4 + SZ_X, "K buffer");
__device__ __forceinline__ float pos_of_index(int p) {
  if (p < 256) return (float)p;
  if (p < 384) return (float)(256 + 2 * (p - 256));
  if (p < 512) return (float)(512 + 4 * (p - 384));
  if (p < 640) return (float)(1024 + 8 * (p - 512));
  return 2048.0f;
}
__device__ __forceinline__ int index_of_l(int l) {
  float pf = bfr((float)l);
  int p = (int)pf;
  if (p < 256) return p;
  if (p < 512) return 256 + ((p - 256) >> 1);
  if (p < 1024) return 384 + ((p - 512) >> 2);
  if (p < 2048) return 512 + ((p - 1024) >> 3);
  return 640;
}

template <int CTRL> __device__ __forceinline__ float dppr(float x) {
  return __builtin_bit_cast(float, __builtin_amdgcn_update_dpp(0, __builtin_bit_cast(int, x), CTRL, 0xf, 0xf, false));
}
__device__ __forceinline__ float wave_sum(float v) {
  v += dppr<0x121>(v);
  v += dppr<0x122>(v);
  v += dppr<0x124>(v);
  v += dppr<0x128>(v);
  {
    auto r = __builtin_amdgcn_permlane16_swap(__float_as_uint(v), __float_as_uint(v), false, false);
    v = __uint_as_float(r[0]) + __uint_as_float(r[1]);
  }
  {
    auto r = __builtin_amdgcn_permlane32_swap(__float_as_uint(v), __float_as_uint(v), false, false);
    v = __uint_as_float(r[0]) + __uint_as_float(r[1]);
  }
  return v;
}
__device__ __forceinline__ void ln_row8(float (&v)[8]) {
  float s = 0.f;
#pragma unroll
  for (int j = 0; j < 8; ++j) s += v[j];
  s = wave_sum(s);
  const float mean = s * (1.0f / DM);
  float q = 0.f;
#pragma unroll
  for (int j = 0; j < 8; ++j) { float t = v[j] - mean; q += t * t; }
  q = wave_sum(q);
  const float var = q * (1.0f / DM);
  const float mb = bfr(mean);
  const float sd = bfr(sqrtf(bfr(bfr(var) + 1e-5f)));
#pragma unroll
  for (int j = 0; j < 8; ++j) v[j] = bfr(bfr(v[j] - mb) / sd);
}

#define RLX_AGENT __ATOMIC_RELAXED, __HIP_MEMORY_SCOPE_AGENT
typedef __attribute__((address_space(1))) unsigned gu32;
typedef unsigned v4u __attribute__((ext_vector_type(4)));
constexpr int AUX_SC1 = 16;
constexpr unsigned SPIN_CAP = 1u << 18;

__device__ __forceinline__ bool wait_flags(const unsigned* flags, int lane, int nfl, unsigned want, unsigned* tmo) {
  for (unsigned spins = 0;; ++spins) {
    unsigned v = want;
    if (lane < nfl) v = __hip_atomic_load((gu32*)flags + lane * 32, RLX_AGENT);
    if (__all(v >= want)) break;
    if (spins > SPIN_CAP) {
      if (lane == 0) __hip_atomic_store((gu32*)tmo, 1u, RLX_AGENT);
      return false;
    }
  }
  __builtin_amdgcn_fence(__ATOMIC_ACQUIRE, "wavefront");
  return true;
}

#define STAMP(i)
#define STAMP_DUMP()
#define WSTAMP(i)
template <int CTRL> __device__ __forceinline__ float dppf(float old, float x) {
  return __builtin_bit_cast(float, __builtin_amdgcn_update_dpp(__builtin_bit_cast(int, old), __builtin_bit_cast(int, x), CTRL, 0xf, 0xf, false));
}
__device__ __forceinline__ float wave_max_dpp(float x) {
  const float ninf = -3e38f;
  x = fmaxf(x, dppf<0x111>(ninf, x));
  x = fmaxf(x, dppf<0x112>(ninf, x));
  x = fmaxf(x, dppf<0x114>(ninf, x));
  x = fmaxf(x, dppf<0x118>(ninf, x));
  x = fmaxf(x, dppf<0x142>(ninf, x));
  x = fmaxf(x, dppf<0x143>(ninf, x));
  return __builtin_bit_cast(float, __builtin_amdgcn_readlane(__builtin_bit_cast(int, x), 63));
}
__device__ __forceinline__ float wave_sum_dpp(float x) {
  x += dppf<0x111>(0.f, x);
  x += dppf<0x112>(0.f, x);
  x += dppf<0x114>(0.f, x);
  x += dppf<0x118>(0.f, x);
  x += dppf<0x142>(0.f, x);
  x += dppf<0x143>(0.f, x);
  return __builtin_bit_cast(float, __builtin_amdgcn_readlane(__builtin_bit_cast(int, x), 63));
}
constexpr int SMS = 528;
constexpr int QS = 144;
constexpr int VS = 80;
constexpr int SOS = 144;
#define VAR_NREP 4
constexpr int NREP = VAR_NREP;
__device__ __forceinline__ float xsum16(float x) {
  auto r = __builtin_amdgcn_permlane16_swap(__float_as_uint(x), __float_as_uint(x), false, false);
  return __uint_as_float(r[0]) + __uint_as_float(r[1]);
}
__device__ __forceinline__ float xsum32(float x) {
  auto r = __builtin_amdgcn_permlane32_swap(__float_as_uint(x), __float_as_uint(x), false, false);
  return __uint_as_float(r[0]) + __uint_as_float(r[1]);
}
__device__ __forceinline__ float xmax16(float x) {
  auto r = __builtin_amdgcn_permlane16_swap(__float_as_uint(x), __float_as_uint(x), false, false);
  return fmaxf(__uint_as_float(r[0]), __uint_as_float(r[1]));
}
__device__ __forceinline__ float xmax32(float x) {
  auto r = __builtin_amdgcn_permlane32_swap(__float_as_uint(x), __float_as_uint(x), false, false);
  return fmaxf(__uint_as_float(r[0]), __uint_as_float(r[1]));
}
__device__ __forceinline__ float red_mod4(float x) {
  x += dppf<0x124>(0.f, x);
  x += dppf<0x128>(0.f, x);
  return xsum32(xsum16(x));
}
typedef __bf16 bf16x2_t __attribute__((ext_vector_type(2)));
typedef float f32x2_t __attribute__((ext_vector_type(2)));
__device__ __forceinline__ unsigned pk2(float a, float b) {
  f32x2_t v = {a, b};
  bf16x2_t r = __builtin_convertvector(v, bf16x2_t);
  return __builtin_bit_cast(unsigned, r);
}
typedef _Float16 f16x2_t __attribute__((ext_vector_type(2)));
typedef _Float16 f16x8 __attribute__((ext_vector_type(8)));
__device__ __forceinline__ unsigned pkh(float a, float b) {
  f32x2_t v = {a, b};
  return __builtin_bit_cast(unsigned, __builtin_convertvector(v, f16x2_t));
}
__device__ __forceinline__ float h2f(u16 h) { return (float)__builtin_bit_cast(_Float16, h); }
__device__ __forceinline__ float one(float x) {
  asm("" : "+v"(x));
  return x;
}
__device__ __forceinline__ unsigned pk2s(float a, float b) { return pk2(one(a), one(b)); }
__device__ __forceinline__ float dot2sq(const unsigned x, const float acc) {
  return __builtin_amdgcn_fdot2_f32_bf16(__builtin_bit_cast(bf16x2_t, x), __builtin_bit_cast(bf16x2_t, x), acc, false);
}
__device__ __forceinline__ float lo16(unsigned u) { return __uint_as_float(u << 16); }
__device__ __forceinline__ float hi16(unsigned u) { return __uint_as_float(u & 0xffff0000u); }
__device__ __forceinline__ float bfr1(float x) { return lo16(pk2(x, x)); }
__device__ __forceinline__ float half_sum(float v) {
  v += dppr<0x121>(v);
  v += dppr<0x122>(v);
  v += dppr<0x124>(v);
  v += dppr<0x128>(v);
  return xsum16(v);
}
__device__ __forceinline__ void scale_pair(float a, float b, float scale, float& oa, float& ob) {
  unsigned u = pk2(a, b);
  unsigned v = pk2s(lo16(u) * scale, hi16(u) * scale);
  oa = lo16(v);
  ob = hi16(v);
}

#define XMAP 2
__device__ __forceinline__ int vbid() {
  const int b = blockIdx.x, x = b & 7, t = b >> 3;
  if (XMAP == 2) {
    const int q = (t >> 2) + 2 * (x & 4);
    const unsigned long long m = (q & 8) ? 0xf0d85040cdc9ada9ull : 0x76726e6a25210501ull;
    const int e = (int)(m >> (8 * (q & 7))) & 255;
    return (e & 252) + ((x & 3) << (2 + (e & 3))) + (t & 3) + (q == 14 ? ((x & 2) ? 8 : -120) : 0);
  }
  if (XMAP == 0) {
    if (b < 64) return (b & 32) + 4 * x + (t & 3);
    if (b < 96) return (b & ~15) + 2 * x + (t & 1);
    if (b >= 104 && b < 168) return 104 + 8 * x + t - 13;
    if (b >= 168 && b < 232) return 168 + ((b - 168) & 32) + 4 * x + ((t - 21) & 3);
    return b;
  }
  if (XMAP == 3) {
    const int y = x & 3;
    if (t >= 16 && t < 24) return 104 + 16 * y + 2 * (x & 4) + t - 16;
    if (x < 4) return t < 16 ? 4 * (t & 8) + 8 * x + (t & 7) : 64 + 4 * (t & 4) + 4 * y + (t & 3);
    if (t >= 24) return (t >= 28 ? 240 : y < 2 ? 96 : 224) + 4 * y + (t & 3);
    return 168 + 4 * (t & 8) + 8 * y + (t & 7);
  }
  if (XMAP == 4) {
    const int y = x & 3;
    if (x < 4) return t < 16 ? 4 * (t & 8) + 8 * x + (t & 7) : t < 28 ? 104 + 16 * y + t - 16 : 64 + 4 * y + (t & 3);
    if (t >= 24) return (t >= 28 ? 240 : y < 2 ? 96 : 224) + 4 * y + (t & 3);
    if (t < 16) return 168 + 4 * (t & 8) + 8 * y + (t & 7);
    return t < 20 ? 104 + 16 * y + t - 4 : 80 + 4 * y + (t & 3);
  }
  if (XMAP == 2 || XMAP >= 5) {
    if (XMAP == 5 && x < 4 && t < 16) return 16 * x + t;
    if (XMAP == 6 && x < 4 && t < 16) return 32 * (x & 1) + 8 * (x & 2) + t;
    if (XMAP == 7 && x < 4) return (x & 1) ? 104 + 16 * (x & 2) + t : 16 * x + t;
    if (x < 4) return t < 16 ? 4 * (t & 8) + 8 * x + (t & 7) : 104 + 16 * x + t - 16;
    const int y = x - 4;
    if (t >= 24) return (t >= 28 ? 240 : y < 2 ? 96 : 224) + 4 * y + (t & 3);
    if (t < 16) return 168 + 4 * (t & 8) + 8 * y + (t & 7);
    return 64 + 4 * (t & 4) + 4 * y + (t & 3);
  }
  if (x < 2) return 2 * (t & 16) + 16 * x + (t & 15);
  const int i = 16 * (x - 2) + (t & 15);
  if (t < 16) {
    const int k = (i * 171) >> 9, r = i - 3 * k;
    return r == 0 ? 168 + k : r == 1 ? 200 + k : 64 + 16 * (k & 1) + (k >> 1);
  }
  const int g = i >> 3, e = i & 7;
  if (g == 3) return 96 + e;
  if (g == 7) return 232 + e;
  if (g == 9) return 240 + e;
  if (g == 11) return 248 + e;
  return 104 + 8 * (g - (g > 3) - (g > 7) - (g > 9)) + e;
}
#define MSTAMP()
#define WSTAMP2()
#define SST(k)
#define SSX(k, c)
#define SSW(k, v)
#define TS(k, ss, t)
#define LS(k, ss)
#define LS_DUMP()
struct MegaArgs {
  const u16* mem0;
  const u16* Dskip;
  char* ws;
  u16* out;
  const u16* x0;
  const u16 *logAre, *Aim, *Cre, *Cim, *logdt;
  const u16 *Wq, *Wk, *Wv, *Wo;
  int vb, wv;
};

__device__ __forceinline__ int lane_id() {
  int l;
  asm volatile("v_mbcnt_lo_u32_b32 %0, -1, 0\n\tv_mbcnt_hi_u32_b32 %0, -1, %0" : "=v"(l));
  __builtin_assume(l >= 0 && l < 64);
  return l;
}
__device__ __forceinline__ bool spin_timeout(unsigned spins, unsigned* tmo) {
  if (spins > SPIN_CAP) {
    if (lane_id() == 0) __hip_atomic_store((gu32*)tmo, 1u, RLX_AGENT);
    return true;
  }
  return false;
}
#define ROT_H (4 * (h & 1))
#define ROT_T (h & 2)
#define C2G 420
#define CVSP 40
#define PFA 0
#define PFB 0
#define WO1 1
#define AWH 0
#define PEEK0 1
#define SLP0 2
#define GF_R 400
#define GF_V 0
#define PP (8 - R0)
#define EL 1
#define IPF 3
#define XL 0
#define MC3 2
#define XZD 300
#define XZP 4
#define NFW 1
#define IPS 128u
#define WNT 17
#define OAUX 16
#define XAUX 16
#define GF_RT 0
#define GF_RU 0
#define EF 1
#define RGB 3
#define GR2 345
#define GR3 280
#define RLS 1
#define GT1 120
#define GT2 32
#define GT3 1
#define CE 190
#define KE 0
#define PE KE
#define KD 360
#define PD (KD + 20)
#define PC0 2
#define GR0 6
#define DL_A2 DL_A
#define PCT 0
#define EP 1
#define CQ 1
#define K0 40
#define PS 1
#define R0 3
#define PCD 100
#define RAUX AUX_SC1
#define RVAL 1
#define RLEAD 60
#define SLP_D 0
#define RFA 1
#define TP 1
#define TPS (4 * TP + 4)
#define MCE 1
#define H4S 0
#define GF_H 150
#define CVT2 48
#define GF_P 0
#define GF0 8
#define SLP_H 12
#define HX0 6
#define CVSTEP 2
#define CVLATE 64
#define PACE 204
#define PACE0 12
#define RPRIO 3
#define DL_R2 1400
#define PNL 8
#define GSL 12
#define AWDIR 1
#define AWAT 0
#define AWRP 0
#define PEPS 4
#define FGATE 0
#define DL_C 0
#define DL_A 300
#define DL_R 0
#define DL_Q 0
#define KC0 0
#define CPRIO 1
#define P1PRIO 3
#define QLEAD 2
#define POLL_SLEEP 12
#define SLP_C POLL_SLEEP
#define SLP_A POLL_SLEEP
#define SLP_W POLL_SLEEP
#define SLP_R POLL_SLEEP
#define SLP_V POLL_SLEEP
#define SLP_Q POLL_SLEEP
#define SLP_M 4
#define AWPRIO 2
#define SLP_S 8
#define SPOS 4
#define CVPRE 12
#define QPD 3
#define QPRI 2
#define TLBW 3
#define QHV 0
#define QCHK 0
#define LEADN 99
#define GATE_V 170
#define GATE_R 380
#define GATE_N 380
#define AWAHEAD 1
#define GATE_M 150
#define PRI_A 3
#define PRI_WA 2
#define PRI_WB 1
#define AWSKIP 3
#define PRI_0 3
#define AWDEF 0
#define SLP_F 2
#define EXP_OFF 0
#define AWSPEC 1
#define CVTHR 48
#define NR_C 8
#define NR_R 8
#define NR_W 8
#define NR_N 2
#define QAHEAD 0
#define NS0 4
#define SLP_N POLL_SLEEP
template <int SLP, int WPL>
__device__ __forceinline__ bool wait_n(const unsigned* f, int n, unsigned* tmo) {
  const int l = lane_id();
  for (unsigned spins = 0;; ++spins) {
    unsigned v = 1u;
    if (l < n) v = __hip_atomic_load((gu32*)f + (l / WPL) * 32 + l % WPL, RLX_AGENT);
    if (__all(v != 0u)) break;
    if (spin_timeout(spins, tmo)) return false;
    __builtin_amdgcn_s_sleep(SLP);
  }
  asm volatile("" ::: "memory");
  return true;
}
__device__ __forceinline__ unsigned peek(const unsigned* base, const unsigned off) {
  unsigned r;
  asm volatile("global_atomic_or %0, %1, %2, %3 sc0\n\ts_waitcnt vmcnt(0)" : "=&v"(r) : "v"(off), "v"(0u), "s"(base) : "memory");
  return r;
}
__device__ __forceinline__ unsigned now10() { return (unsigned)__builtin_amdgcn_s_memrealtime(); }
__device__ __forceinline__ void gate(const unsigned t) {
#pragma unroll 1
  for (int i = 0; i < 64; ++i) {
    const int d = (int)(t - now10());
    if (d <= 0) break;
    if (d > GT1) __builtin_amdgcn_s_sleep(32);
    else if (d > GT2) __builtin_amdgcn_s_sleep(8);
    else __builtin_amdgcn_s_sleep(GT3);
  }
}
__device__ __forceinline__ unsigned latest(const unsigned mk, const unsigned t) {
  int d = (int)(mk - t);
  d = max(d, __builtin_amdgcn_update_dpp(0, d, 0x121, 0xf, 0xf, false));
  d = max(d, __builtin_amdgcn_update_dpp(0, d, 0x122, 0xf, 0xf, false));
  d = max(d, __builtin_amdgcn_update_dpp(0, d, 0x124, 0xf, 0xf, false));
  d = max(d, __builtin_amdgcn_update_dpp(0, d, 0x128, 0xf, 0xf, false));
  return t + __builtin_amdgcn_readfirstlane(d);
}
template <int SLP, int WPL, int GT>
__device__ __forceinline__ void grp_wait(const bool leader, volatile unsigned* sw, const unsigned val, const unsigned* f, int n,
                                         unsigned* tmo, unsigned& tl) {
  if (leader) {
    const int l = lane_id();
    for (unsigned spins = 0;; ++spins) {
      unsigned v = 1u;
      if (l < n) v = __hip_atomic_load((gu32*)f + (l / WPL) * 32 + l % WPL, RLX_AGENT);
      if (__all(v != 0u) || spin_timeout(spins, tmo)) break;
      if (GT && spins == 0) gate(tl);
      else __builtin_amdgcn_s_sleep(SLP);
    }
    if (GT) tl = now10() + GT;
    if (sw && l == 0) *sw = val;
  } else {
    for (unsigned spins = 0; *sw < val; ++spins) {
      if (spins > 64u * SPIN_CAP) break;
      if (FGATE && GT && spins == 0) gate(tl);
      __builtin_amdgcn_s_sleep(SLP_F);
    }
    if (FGATE && GT) tl = now10() + GT;
  }
  asm volatile("" ::: "memory");
}
template <int SLP, int WPL>
__device__ __forceinline__ bool wg_wait(const unsigned* f, int n, unsigned* tmo) {
  if (threadIdx.x < 64) (void)wait_n<SLP, WPL>(f, n, tmo);
  __syncthreads();
  asm volatile("" ::: "memory");
  return true;
}
__device__ __forceinline__ void set_flag(unsigned* f) { __hip_atomic_store((gu32*)f, 1u, RLX_AGENT); }
#define DRAIN() asm volatile("s_waitcnt vmcnt(0)" ::: "memory")
#define MKRS(ptr, bytes) __builtin_amdgcn_make_buffer_rsrc((void*)(ptr), 0, (int)(bytes), 0x00020000)
typedef unsigned v2u __attribute__((ext_vector_type(2)));
#define LD128(rs, voff, soff) __builtin_amdgcn_raw_buffer_load_b128(rs, voff, soff, AUX_SC1)
#define LD64(rs, voff, soff) __builtin_amdgcn_raw_buffer_load_b64(rs, voff, soff, AUX_SC1)
#define ST128(v, rs, voff, soff) ST128A(v, rs, voff, soff, AUX_SC1)
#define ST128A(v, rs, voff, soff, aux)                                        \
  do {                                                                        \
    const v4u st_d_ = (v);                                                    \
    __builtin_amdgcn_raw_buffer_store_b128(st_d_, rs, voff, soff, aux);       \
    asm volatile("s_nop 1" ::"v"(st_d_) : "memory");                          \
  } while (0)
#define ST64(v, rs, voff, soff) __builtin_amdgcn_raw_buffer_store_b64(v, rs, voff, soff, AUX_SC1)
#define BF8(x) __builtin_bit_cast(bf16x8, x)
#define MFMA16(a, b, c) __builtin_amdgcn_mfma_f32_16x16x32_bf16(a, b, c, 0, 0, 0)
typedef short bf16x4 __attribute__((ext_vector_type(4)));
#define MFMA16K(a, b, c) \
  __builtin_amdgcn_mfma_f32_16x16x16bf16_1k(__builtin_bit_cast(bf16x4, a), __builtin_bit_cast(bf16x4, b), c, 0, 0, 0)
__device__ __forceinline__ float fmax_nn(float a, float b) { return __builtin_elementwise_maximum(a, b); }
typedef __bf16 bfv8 __attribute__((ext_vector_type(8)));
typedef __bf16 bfv16 __attribute__((ext_vector_type(16)));
typedef unsigned v8u __attribute__((ext_vector_type(8)));
#define SMF(a, b, c, idx) \
  __builtin_amdgcn_smfmac_f32_16x16x64_bf16(__builtin_bit_cast(bfv8, a), __builtin_bit_cast(bfv16, b), c, idx, 0, 0)
__device__ __forceinline__ float swap_add32(float a, float b) {
  auto r = __builtin_amdgcn_permlane32_swap(__float_as_uint(a), __float_as_uint(b), false, false);
  return __uint_as_float(r[0]) + __uint_as_float(r[1]);
}
__device__ __forceinline__ float swap_add16(float a, float b) {
  auto r = __builtin_amdgcn_permlane16_swap(__float_as_uint(a), __float_as_uint(b), false, false);
  return __uint_as_float(r[0]) + __uint_as_float(r[1]);
}
__device__ __forceinline__ v4u smf_fix(const v4u d) {
  auto x = __builtin_amdgcn_permlane16_swap(d.x, d.z, false, false);
  auto y = __builtin_amdgcn_permlane16_swap(d.y, d.w, false, false);
  return v4u{x[0], x[1], y[0], y[1]};
}
__device__ __forceinline__ v8u smf_bfrag(const u16* p) {
  const v2u a = *reinterpret_cast<const v2u*>(p), b = *reinterpret_cast<const v2u*>(p + 8);
  const v2u c = *reinterpret_cast<const v2u*>(p + 32), d = *reinterpret_cast<const v2u*>(p + 40);
  return v8u{a.x, b.x, a.y, b.y, c.x, d.x, c.y, d.y};
}

#define VAR_PPA 0
#define VAR_PPB 0
#define VAR_THR 0
#define VAR_THR2 0
#define CV_LEAD 100
#define VAR_EARLY 1
#define VAR_DRAIN 1
#define VAR_D1 0
#define VAR_D2 0
#define VAR_DR2 1
#define VAR_S1 0
#define VAR_S2 0
typedef __attribute__((address_space(1))) unsigned long long gu64;
typedef __attribute__((address_space(1))) char gch;
__device__ __forceinline__ int cvt_rpi(float x) {
  int r;
  asm("v_cvt_rpi_i32_f32 %0, %1" : "=v"(r) : "v"(x));
  return r;
}
typedef unsigned long long u64;
__device__ __forceinline__ u64 call_tag(const MegaArgs& A) {
  u64 t = *(const __attribute__((address_space(4))) u64*)(A.ws + OFF_EP) * 6364136223846793005ull + 1442695040888963407ull;
  asm volatile("" : "+s"(t)::"memory");
  return t;
}
__device__ __forceinline__ u64 tagged(const u64 tag, const unsigned v) { return (tag & 0xffffffffffffull) | (u64)(v >> 16) << 48; }
__device__ __forceinline__ bool has_tag(const u64 tag, const unsigned lo, const unsigned hi) {
  return lo == (unsigned)tag && (hi & 0xffffu) == ((unsigned)(tag >> 32) & 0xffffu);
}
__device__ __forceinline__ u64 ready_look(const MegaArgs& A) {
  return __hip_atomic_load((gu64*)(A.ws + OFF_RD) + (blockIdx.x & 7) * 64 + lane_id(), RLX_AGENT);
}
#define RD_ASK(v) \
  asm volatile("global_load_dwordx2 %0, %1, %2 sc1" : "=v"(v) : "v"(lane_id() * 8), "s"(A.ws + OFF_RD + (blockIdx.x & 7) * 512) : "memory")
__device__ __forceinline__ void ready_wait(const MegaArgs& A, const u64 tag, u64 v) {
  for (unsigned spins = 0; !__all(v == tag) && spins < SPIN_CAP; ++spins) {
    __builtin_amdgcn_s_sleep(8);
    v = ready_look(A);
  }
  asm volatile("" ::: "memory");
}
typedef short v4s __attribute__((ext_vector_type(4)));
typedef __attribute__((address_space(3))) v4s* lp4s;
__device__ __forceinline__ void glds16(const void* sbase, const unsigned voff, const unsigned lds, const bool nt = false) {
  unsigned keep;
#define GLDS16(M)                                                                                                                     \
  asm volatile("s_nop 4\n\ts_mov_b32 %0, m0\n\ts_mov_b32 m0, %3\n\ts_nop 0\n\tglobal_load_lds_dwordx4 %1, %2" M "\n\ts_mov_b32 m0, %0" \
               : "=&s"(keep)                                                                                                          \
               : "v"(voff), "s"(sbase), "s"(lds)                                                                                      \
               : "memory")
  if (nt) GLDS16(" nt");
  else GLDS16("");
#undef GLDS16
}
__device__ __forceinline__ void glds16c(const void* sbase, const unsigned voff, const unsigned lds) {
  unsigned keep;
  asm volatile("s_nop 4\n\ts_mov_b32 %0, m0\n\ts_mov_b32 m0, %3\n\ts_nop 0\n\tglobal_load_lds_dwordx4 %1, %2 sc1\n\ts_mov_b32 m0, %0"
               : "=&s"(keep)
               : "v"(voff), "s"(sbase), "s"(lds)
               : "memory");
}
__device__ __forceinline__ void glds64c(const void* sbase, const unsigned voff, const unsigned lds) {
  unsigned keep;
  asm volatile("s_nop 4\n\ts_mov_b32 %0, m0\n\ts_mov_b32 m0, %3\n\ts_nop 0\n\tglobal_load_lds_dwordx4 %1, %2 sc1\n\t"
               "global_load_lds_dwordx4 %1, %2 offset:1024 sc1\n\tglobal_load_lds_dwordx4 %1, %2 offset:2048 sc1\n\t"
               "global_load_lds_dwordx4 %1, %2 offset:3072 sc1\n\ts_mov_b32 m0, %0"
               : "=&s"(keep)
               : "v"(voff), "s"(sbase), "s"(lds)
               : "memory");
}
__device__ __forceinline__ v2u trd(const unsigned a) {
  return __builtin_bit_cast(v2u, __builtin_amdgcn_ds_read_tr16_b64_v4i16((lp4s)(unsigned long)a));
}
__device__ __forceinline__ unsigned lds_base(char* smem) { return (unsigned)(unsigned long)(__attribute__((address_space(3))) char*)smem; }
#define RAW_BAR()                                           \
  do {                                                      \
    asm volatile("s_waitcnt lgkmcnt(0)" ::: "memory");      \
    __builtin_amdgcn_s_barrier();                           \
    asm volatile("" ::: "memory");                          \
  } while (0)
#define WAIT_VM(n) asm volatile("s_waitcnt vmcnt(" #n ")" ::: "memory")
template <bool SP>
__device__ __forceinline__ void wqkv_issue(const u16* Wm, const int hf, const int h, const int w, const int l, const unsigned sb, const bool nt) {
  const char* base = (const char*)Wm + (size_t)(256 * hf + 32 * w) * (HD * 2) + h * (DH * 2);
#define WQKV(NT_)                                                                                                       \
  _Pragma("unroll") for (int i = 0; i < 8; ++i) {                                                                      \
    const int r = l >> 4;                                                                                              \
    const int f = SP ? ((r & 1) | ((i & 1) << 1) | (((i >> 1) & 1) << 2)) : (r | (((i >> 1) & 1) << 2));               \
    glds16(base, (unsigned)((4 * i + r) * (HD * 2) + (((l & 15) ^ (f << 1)) << 4)),                                    \
           sb + (w >> 2) * 65536 + hf * 32768 + (w & 3) * 8192 + i * 1024, NT_);                                       \
  }
  if (nt) { WQKV(true) } else { WQKV(false) }
#undef WQKV
}

__device__ __forceinline__ unsigned row_stats(const v4u raw) {
  float v[8];
#pragma unroll
  for (int e = 0; e < 4; ++e) { v[2 * e] = lo16(raw[e]); v[2 * e + 1] = hi16(raw[e]); }
  float s = 0.f;
#pragma unroll
  for (int e = 0; e < 8; ++e) s += v[e];
  const float mean = wave_sum(s) * (1.0f / DM);
  float q = 0.f;
#pragma unroll
  for (int e = 0; e < 8; ++e) { const float t = v[e] - mean; q += t * t; }
  const float var = wave_sum(q) * (1.0f / DM);
  const float sd = bfr(sqrtf(bfr(bfr(var) + 1e-5f)));
  return (unsigned)f2bf(mean) | ((unsigned)f2bf(sd) << 16);
}
__device__ __forceinline__ void ask_slow(const MegaArgs& A, const int k, unsigned (&t)[3]) {
  const int l = lane_id(), x = blockIdx.x & 7;
  const char *wq = (const char*)A.Wq, *wv = (const char*)A.Wv;
  asm("" : "+s"(wq), "+s"(wv));
#pragma unroll
  for (int m = 0; m < 3; ++m) {
    const char* W = m == 1 ? (const char*)A.Wk : ((m == 0) == (x < 4)) ? wv : wq;
    const int a = (int)((unsigned long)W >> 8) + 8 * (x & 3), h = k < 32 ? ((1 - a) & 3) + 4 * (k & 1) : (31 - a) & 31;
    const int r = k < 32 ? k >> 1 : k - 32;
    t[m] = __builtin_amdgcn_raw_buffer_load_b32(MKRS(W, (unsigned)k < 48u && h < 8 ? DM * HD * 2 : 0), (l >> 1) * (HD * 2) + (l & 1) * 128,
                                                (32 * (r & 15)) * (HD * 2) + (8 * (x & 3) + (h & 7)) * 256, 0);
  }
}
#define ASKED(t) asm volatile("" ::"v"(t[0]), "v"(t[1]), "v"(t[2]))
template <int N>
__device__ __forceinline__ u64 start_stats(const MegaArgs& A, const int idx) {
  const int w = A.wv, l = lane_id();
  const int r0 = N * (8 * idx + w);
  unsigned ta[3], tb[3];
  v4u raw[N];
#pragma unroll
  for (int i = 0; i < N; ++i) raw[i] = *reinterpret_cast<const v4u*>(A.x0 + (size_t)(r0 + i) * DM + l * 8);
  asm volatile("" ::: "memory");
  ask_slow(A, 16 * w + (idx & 3) + (idx >> 4) * N, ta);
  if (N > 4) ask_slow(A, 16 * w + (idx & 3) + 12, tb);
  unsigned tc[2] = {};
  if (N == 4 && AWH > 0) tc[0] = *reinterpret_cast<const unsigned*>(A.Wo + (size_t)idx * 65536 + 32768 + (64 * w + l) * 64);
  if (N == 4 && AWH > 1 && w >= 4) tc[1] = *reinterpret_cast<const unsigned*>(A.Wo + (size_t)idx * 65536 + (64 * w + l) * 64);
  const u64 tag = call_tag(A);
  unsigned stw = 0u;
#pragma unroll
  for (int i = 0; i < N; ++i) {
    const unsigned t = row_stats(raw[i]);
    if ((l >> 1) == i) stw = t;
  }
  if (l < 2 * N) __hip_atomic_store((gu64*)(A.ws + OFF_GT) + 2 * r0 + l, tagged(tag, (l & 1) ? stw : stw << 16), RLX_AGENT);
  SST(0);
  ASKED(ta);
  if (N > 4) ASKED(tb);
  asm volatile("" ::"v"(tc[0]), "v"(tc[1]));
  return tag;
}
#define MFMAH(a, b, c) __builtin_amdgcn_mfma_f32_16x16x32_f16(a, b, c, 0, 0, 0)
struct Cx {
  f32x2_t r, i;
};
__device__ __forceinline__ Cx cmul(const Cx a, const Cx b) { return Cx{a.r * b.r - a.i * b.i, a.r * b.i + a.i * b.r}; }
struct KGen {
  unsigned pa, pb, pc, pd;
  u16 pe;
  f32x2_t ar, ai;
  Cx C;
  char* st;
  v4u bh[8], bl[8];
  __device__ __forceinline__ void load(const MegaArgs& A, const int d, const int l) {
    pa = *reinterpret_cast<const unsigned*>(A.logAre + d * NST + 2 * l);
    pb = *reinterpret_cast<const unsigned*>(A.Aim + d * NST + 2 * l);
    pc = *reinterpret_cast<const unsigned*>(A.Cre + d * NST + 2 * l);
    pd = *reinterpret_cast<const unsigned*>(A.Cim + d * NST + 2 * l);
    pe = A.logdt[d];
  }
  __device__ __forceinline__ void setup() {
    const float dt = bfr(expf(bf2f(pe)));
    ar = f32x2_t{-bfr(expf(lo16(pa))) * dt, -bfr(expf(hi16(pa))) * dt};
    ai = f32x2_t{lo16(pb) * dt, hi16(pb) * dt};
    C = Cx{f32x2_t{lo16(pc) * dt, hi16(pc) * dt}, f32x2_t{lo16(pd) * dt, hi16(pd) * dt}};
  }
  __device__ __forceinline__ float scale() {
    const float b = 2.0f * wave_sum((fabsf(C.r.x) + fabsf(C.r.y)) + (fabsf(C.i.x) + fabsf(C.i.y)));
    const unsigned eb = max(__float_as_uint(b) >> 23, 25u);
    const float ks = __uint_as_float((268u - eb) << 23);
    C.r *= ks;
    C.i *= ks;
    return __uint_as_float((eb - 24u) << 23);
  }
  __device__ __forceinline__ Cx power(const float p) const {
    Cx z;
#pragma unroll
    for (int e = 0; e < 2; ++e) {
      const float x = ai[e] * p;
      const float f = __builtin_fmaf(x, 6.4206382e-9f, __builtin_fmaf(x, 0.15915494f, -__builtin_rintf(x * 0.15915494f)));
      const float m = __expf(ar[e] * p);
      z.r[e] = m * __builtin_amdgcn_cosf(f);
      z.i[e] = m * __builtin_amdgcn_sinf(f);
    }
    return z;
  }
  static __device__ __forceinline__ unsigned rest(const f32x2_t y, const unsigned h) {
    unsigned r;
    asm("v_fma_mixlo_f16 %0, %1, -1.0, %2 op_sel_hi:[1,0,0]" : "=v"(r) : "v"(h), "v"(y.x));
    asm("v_fma_mixhi_f16 %0, %1, -1.0, %2 op_sel:[1,0,0] op_sel_hi:[1,0,0]" : "+v"(r) : "v"(h), "v"(y.y));
    return r;
  }
  __device__ __forceinline__ char* rows(char* p, Cx& y, const Cx s, const int n) const {
#pragma unroll 1
    for (int i = 0; i < n; i += 2) {
#pragma unroll
      for (int k = 0; k < 2; ++k) {
        const unsigned h0 = pkh(y.r.x, y.r.y), h1 = pkh(y.i.x, y.i.y);
        *reinterpret_cast<v2u*>(p + 1040 * k) = v2u{h0, h1};
        *reinterpret_cast<v2u*>(p + 1040 * k + 512) = v2u{rest(y.r, h0), rest(y.i, h1)};
        y = cmul(y, s);
      }
      p += 2080;
    }
    return p;
  }
  __device__ __forceinline__ void get(v4u (&h)[8], v4u (&o)[8], const int l) const {
    const char* p = st + (l & 15) * 1040 + (l >> 4) * 16;
    asm volatile("" ::: "memory");
#pragma unroll
    for (int k = 0; k < 8; ++k) {
      h[k] = *reinterpret_cast<const v4u*>(p + 64 * k);
      o[k] = *reinterpret_cast<const v4u*>(p + 512 + 64 * k);
    }
    asm volatile("" ::: "memory");
  }
  Cx cy, cs;
  __device__ __forceinline__ void columns(const int l, const int j0, const int j1) {
    if (j0 == 0) {
      cs = power(16.f);
      cs.i = -cs.i;
      cy = Cx{f32x2_t{16384.f, 16384.f}, f32x2_t{0.f, 0.f}};
    }
    rows(st + l * 8 + 1040 * j0, cy, cs, j1 - j0);
  }
  template <bool COL>
  __device__ __forceinline__ f32x4 tile(const int l) const {
    v4u h[8], o[8];
    get(h, o, l);
    f32x4 d = {}, e = {}, f = {};
#define H8(x) __builtin_bit_cast(f16x8, x)
#define KMM(a, b, d) (COL ? MFMAH(H8(b), H8(a), d) : MFMAH(H8(a), H8(b), d))
#pragma unroll
    for (int k = 0; k < 8; ++k) {
      d = KMM(h[k], bh[k], d);
      e = KMM(o[k], bh[k], e);
      f = KMM(h[k], bl[k], f);
    }
#undef KMM
#undef H8
    return (d + (e + f)) * (1.0f / 8192);
  }
};
#define ACC_INIT 0xc9b26c86c9b26ca0ull
#define CHAIN_ASM_ATT \
  "s_mov_b32 s95, 272\n" \
  "s_mov_b32 s91, 0x4c7c2000\n" \
  "s_mov_b32 s85, 0xc9b26c9b\n" \
  "s_mov_b32 s92, 0xc9326c9b\n" \
  "s_mov_b32 s93, 0xc9326c9b\n" \
  "s_mov_b32 s78, 0x3e0293ee\n" \
  "s_mov_b32 s76, 0x33333333\n" \
  "s_mov_b32 s77, 0x33333333\n" \
  "s_lshl_b32 s98, s73, 10\n" \
  "s_add_u32 s98, s98, s74\n" \
  "s_add_u32 s98, s98, 131072\n" \
  "s_lshl_b32 s97, s73, 9\n" \
  "s_add_u32 s97, s97, s74\n" \
  "s_add_u32 s97, s97, 158720\n" \
  "s_lshl_b32 s99, s73, 10\n" \
  "v_mbcnt_lo_u32_b32 v48, -1, 0\n" \
  "v_mbcnt_hi_u32_b32 v48, -1, v48\n" \
  "s_add_u32 s88, s74, 157760\n" \
  "v_mov_b32 v59, s88\n" \
  "v_lshrrev_b32 v49, 2, v48\n" \
  "v_lshrrev_b32 v50, 4, v48\n" \
  "v_bitop3_b32 v50, v49, v50, 3 bitop3:0x28\n" \
  "v_lshl_add_u32 v8, v50, 3, s97\n" \
  "v_and_b32 v50, 3, v48\n" \
  "v_lshl_add_u32 v8, v50, 5, v8\n" \
  "v_and_b32 v49, 1, v49\n" \
  "v_lshl_add_u32 v8, v49, 8, v8\n" \
  "v_and_b32 v49, 8, v48\n" \
  "v_lshl_add_u32 v8, v49, 4, v8\n" \
  "v_lshrrev_b32 v49, 5, v48\n" \
  "v_add_u32 v10, s99, v1\n" \
  "s_lshr_b32 s88, s99, 2\n" \
  "s_add_u32 s88, s88, s74\n" \
  "s_add_u32 s88, s88, 139264\n" \
  "v_lshl_add_u32 v7, v49, 10, s88\n" \
  "v_and_b32 v49, 31, v48\n" \
  "v_lshl_add_u32 v7, v49, 3, v7\n" \
  "v_mov_b64 v[12:13], 0\n" \
  "v_mov_b64 v[14:15], 0\n" \
  "v_mov_b64 v[16:17], 0\n" \
  "v_mov_b64 v[18:19], 0\n" \
  "v_mov_b64 v[20:21], 0\n" \
  "v_mov_b64 v[22:23], 0\n" \
  "v_mov_b32 v34, 0xc9b26ca0\n" \
  "v_mov_b32 v35, 0xc9b26c86\n" \
  "s_mov_b32 s87, 0x40000\n" \
  "v_subrev_u32 v30, 64, v59\n" \
  "v_add_u32 v17, 0x10000, v28\n" \
  "v_xor_b32 v18, 128, v28\n" \
  "v_xor_b32 v19, 128, v17\n" \
  "ds_write_b32 v30, v12 offset:0\n" \
  "ds_write_b32 v30, v12 offset:32\n" \
  "ds_write_b32 v30, v12 offset:64\n" \
  "v_xor_b32 v20, 0, v29\n" \
  "v_xor_b32 v21, 32, v29\n" \
  "v_xor_b32 v22, 64, v29\n" \
  "v_xor_b32 v23, 96, v29\n" \
  "s_waitcnt vmcnt(12) lgkmcnt(0)\n" \
  "s_barrier\n" \
  "ds_read_b64_tr_b16 v[160:161], v20 offset:0\n" \
  "ds_read_b64_tr_b16 v[162:163], v20 offset:2048\n" \
  "ds_read_b64_tr_b16 v[168:169], v21 offset:0\n" \
  "ds_read_b64_tr_b16 v[170:171], v21 offset:2048\n" \
  "ds_read_b64_tr_b16 v[176:177], v22 offset:0\n" \
  "ds_read_b64_tr_b16 v[178:179], v22 offset:2048\n" \
  "ds_read_b64_tr_b16 v[184:185], v23 offset:0\n" \
  "ds_read_b64_tr_b16 v[186:187], v23 offset:2048\n" \
  "s_lshl_b32 s88, s73, 10\n" \
  "v_add_u32 v48, s88, v1\n" \
  "ds_read_b128 v[52:55], v48 offset:0\n" \
  "s_waitcnt lgkmcnt(0)\n" \
  "v_mov_b32 v11, 0\n" \
  "v_dot2c_f32_bf16 v11, v52, v52\n" \
  "v_dot2c_f32_bf16 v11, v53, v53\n" \
  "v_dot2c_f32_bf16 v11, v54, v54\n" \
  "v_dot2c_f32_bf16 v11, v55, v55\n" \
  "s_nop 0\n" \
  "s_nop 1\n" \
  "v_max_u32_dpp v11, v11, v11 row_ror:1 row_mask:0xf bank_mask:0xf\n" \
  "s_nop 1\n" \
  "v_max_u32_dpp v11, v11, v11 row_ror:2 row_mask:0xf bank_mask:0xf\n" \
  "s_nop 1\n" \
  "v_max_u32_dpp v11, v11, v11 row_ror:4 row_mask:0xf bank_mask:0xf\n" \
  "s_nop 1\n" \
  "v_max_u32_dpp v11, v11, v11 row_ror:8 row_mask:0xf bank_mask:0xf\n" \
  "v_mov_b32 v31, v11\n" \
  "s_nop 1\n" \
  "v_permlane16_swap_b32 v11, v31\n" \
  "v_max_u32 v11, v11, v31\n" \
  "v_mov_b32 v31, v11\n" \
  "s_nop 1\n" \
  "v_permlane32_swap_b32 v11, v31\n" \
  "v_max_u32 v11, v11, v31\n" \
  "s_mov_b64 exec, 1\n" \
  "ds_max_u32 v59, v11\n" \
  "s_mov_b64 exec, -1\n" \
  "s_waitcnt vmcnt(8) lgkmcnt(0)\n" \
  "s_barrier\n" \
  "ds_read_b64_tr_b16 v[164:165], v20 offset:4096\n" \
  "ds_read_b64_tr_b16 v[166:167], v20 offset:6144\n" \
  "ds_read_b64_tr_b16 v[172:173], v21 offset:4096\n" \
  "ds_read_b64_tr_b16 v[174:175], v21 offset:6144\n" \
  "ds_read_b64_tr_b16 v[180:181], v22 offset:4096\n" \
  "ds_read_b64_tr_b16 v[182:183], v22 offset:6144\n" \
  "ds_read_b64_tr_b16 v[188:189], v23 offset:4096\n" \
  "ds_read_b64_tr_b16 v[190:191], v23 offset:6144\n" \
  "s_add_u32 m0, s19, 0\n" \
  "s_nop 0\n" \
  "global_load_lds_dwordx4 v24, s[4:5] offset:0\n" \
  "global_load_lds_dwordx4 v25, s[4:5] offset:1024\n" \
  "global_load_lds_dwordx4 v26, s[4:5] offset:2048\n" \
  "global_load_lds_dwordx4 v27, s[4:5] offset:3072\n" \
  "s_add_u32 s4, s4, 0x100000\n" \
  "s_addc_u32 s5, s5, 0\n" \
  "v_mfma_f32_16x16x32_bf16 v[64:67], v[160:163], v[160:163], 0\n" \
  "v_mfma_f32_16x16x32_bf16 v[68:71], v[168:171], v[168:171], 0\n" \
  "v_mfma_f32_16x16x32_bf16 v[72:75], v[176:179], v[176:179], 0\n" \
  "v_mfma_f32_16x16x32_bf16 v[76:79], v[184:187], v[184:187], 0\n" \
  "s_waitcnt vmcnt(8) lgkmcnt(0)\n" \
  "s_barrier\n" \
  "ds_read_b64_tr_b16 v[32:33], v20 offset:8192\n" \
  "ds_read_b64_tr_b16 v[34:35], v20 offset:10240\n" \
  "ds_read_b64_tr_b16 v[40:41], v21 offset:8192\n" \
  "ds_read_b64_tr_b16 v[42:43], v21 offset:10240\n" \
  "ds_read_b64_tr_b16 v[48:49], v22 offset:8192\n" \
  "ds_read_b64_tr_b16 v[50:51], v22 offset:10240\n" \
  "ds_read_b64_tr_b16 v[56:57], v23 offset:8192\n" \
  "ds_read_b64_tr_b16 v[58:59], v23 offset:10240\n" \
  "s_add_u32 m0, s19, 4096\n" \
  "s_nop 0\n" \
  "global_load_lds_dwordx4 v24, s[4:5] offset:0\n" \
  "global_load_lds_dwordx4 v25, s[4:5] offset:1024\n" \
  "global_load_lds_dwordx4 v26, s[4:5] offset:2048\n" \
  "global_load_lds_dwordx4 v27, s[4:5] offset:3072\n" \
  "s_add_u32 s4, s4, 0x100000\n" \
  "s_addc_u32 s5, s5, 0\n" \
  "v_mfma_f32_16x16x32_bf16 v[64:67], v[164:167], v[164:167], v[64:67]\n" \
  "v_mfma_f32_16x16x32_bf16 v[68:71], v[172:175], v[172:175], v[68:71]\n" \
  "v_mfma_f32_16x16x32_bf16 v[72:75], v[180:183], v[180:183], v[72:75]\n" \
  "v_mfma_f32_16x16x32_bf16 v[76:79], v[188:191], v[188:191], v[76:79]\n" \
  "s_waitcnt vmcnt(8) lgkmcnt(0)\n" \
  "s_barrier\n" \
  "ds_read_b64_tr_b16 v[36:37], v20 offset:12288\n" \
  "ds_read_b64_tr_b16 v[38:39], v20 offset:14336\n" \
  "ds_read_b64_tr_b16 v[44:45], v21 offset:12288\n" \
  "ds_read_b64_tr_b16 v[46:47], v21 offset:14336\n" \
  "ds_read_b64_tr_b16 v[52:53], v22 offset:12288\n" \
  "ds_read_b64_tr_b16 v[54:55], v22 offset:14336\n" \
  "ds_read_b64_tr_b16 v[60:61], v23 offset:12288\n" \
  "ds_read_b64_tr_b16 v[62:63], v23 offset:14336\n" \
  "s_add_u32 m0, s19, 8192\n" \
  "s_nop 0\n" \
  "global_load_lds_dwordx4 v24, s[4:5] offset:0\n" \
  "global_load_lds_dwordx4 v25, s[4:5] offset:1024\n" \
  "global_load_lds_dwordx4 v26, s[4:5] offset:2048\n" \
  "global_load_lds_dwordx4 v27, s[4:5] offset:3072\n" \
  "s_add_u32 s4, s4, 0x100000\n" \
  "s_addc_u32 s5, s5, 0\n" \
  "v_mfma_f32_16x16x32_bf16 v[64:67], v[32:35], v[32:35], v[64:67]\n" \
  "v_mfma_f32_16x16x32_bf16 v[68:71], v[40:43], v[40:43], v[68:71]\n" \
  "v_mfma_f32_16x16x32_bf16 v[72:75], v[48:51], v[48:51], v[72:75]\n" \
  "v_mfma_f32_16x16x32_bf16 v[76:79], v[56:59], v[56:59], v[76:79]\n" \
  "s_waitcnt vmcnt(8) lgkmcnt(0)\n" \
  "s_barrier\n" \
  "s_add_u32 m0, s19, 12288\n" \
  "s_nop 0\n" \
  "global_load_lds_dwordx4 v24, s[4:5] offset:0\n" \
  "global_load_lds_dwordx4 v25, s[4:5] offset:1024\n" \
  "global_load_lds_dwordx4 v26, s[4:5] offset:2048\n" \
  "global_load_lds_dwordx4 v27, s[4:5] offset:3072\n" \
  "s_add_u32 s4, s4, 0x100000\n" \
  "s_addc_u32 s5, s5, 0\n" \
  "v_mfma_f32_16x16x32_bf16 v[64:67], v[36:39], v[36:39], v[64:67]\n" \
  "v_mfma_f32_16x16x32_bf16 v[68:71], v[44:47], v[44:47], v[68:71]\n" \
  "v_mfma_f32_16x16x32_bf16 v[72:75], v[52:55], v[52:55], v[72:75]\n" \
  "v_mfma_f32_16x16x32_bf16 v[76:79], v[60:63], v[60:63], v[76:79]\n" \
  "s_nop 7\n" \
  "v_max3_f32 v13, v64, v65, v66\n" \
  "v_max3_f32 v13, v13, v67, v68\n" \
  "v_max3_f32 v13, v13, v69, v70\n" \
  "v_max3_f32 v13, v13, v71, v72\n" \
  "v_max3_f32 v13, v13, v73, v74\n" \
  "v_max3_f32 v13, v13, v75, v76\n" \
  "v_max3_f32 v13, v13, v77, v78\n" \
  "v_max_f32 v13, v13, v79\n" \
  "s_nop 1\n" \
  "v_max_f32_dpp v13, v13, v13 row_ror:1 row_mask:0xf bank_mask:0xf\n" \
  "s_nop 1\n" \
  "v_max_f32_dpp v13, v13, v13 row_ror:2 row_mask:0xf bank_mask:0xf\n" \
  "s_nop 1\n" \
  "v_max_f32_dpp v13, v13, v13 row_ror:4 row_mask:0xf bank_mask:0xf\n" \
  "s_nop 1\n" \
  "v_max_f32_dpp v13, v13, v13 row_ror:8 row_mask:0xf bank_mask:0xf\n" \
  "v_mov_b32 v31, v13\n" \
  "s_nop 1\n" \
  "v_permlane16_swap_b32 v13, v31\n" \
  "v_max_f32 v13, v13, v31\n" \
  "v_mov_b32 v31, v13\n" \
  "s_nop 1\n" \
  "v_permlane32_swap_b32 v13, v31\n" \
  "v_max_f32 v13, v13, v31\n" \
  "s_mov_b64 exec, 1\n" \
  "ds_max_u32 v30, v13 offset:32\n" \
  "s_mov_b64 exec, -1\n" \
  "s_waitcnt vmcnt(8) lgkmcnt(0)\n" \
  "s_barrier\n" \
  "s_add_u32 m0, s19, 0\n" \
  "s_nop 0\n" \
  "global_load_lds_dwordx4 v24, s[6:7] offset:0\n" \
  "global_load_lds_dwordx4 v25, s[6:7] offset:1024\n" \
  "global_load_lds_dwordx4 v26, s[6:7] offset:2048\n" \
  "global_load_lds_dwordx4 v27, s[6:7] offset:3072\n" \
  "s_add_u32 s6, s6, 0x100000\n" \
  "s_addc_u32 s7, s7, 0\n" \
  "s_waitcnt vmcnt(8) lgkmcnt(0)\n" \
  "s_barrier\n" \
  "s_add_u32 m0, s19, 4096\n" \
  "s_nop 0\n" \
  "global_load_lds_dwordx4 v24, s[6:7] offset:0\n" \
  "global_load_lds_dwordx4 v25, s[6:7] offset:1024\n" \
  "global_load_lds_dwordx4 v26, s[6:7] offset:2048\n" \
  "global_load_lds_dwordx4 v27, s[6:7] offset:3072\n" \
  "s_add_u32 s6, s6, 0x100000\n" \
  "s_addc_u32 s7, s7, 0\n" \
  "s_waitcnt vmcnt(8) lgkmcnt(0)\n" \
  "s_barrier\n" \
  "s_add_u32 m0, s19, 8192\n" \
  "s_nop 0\n" \
  "global_load_lds_dwordx4 v24, s[6:7] offset:0\n" \
  "global_load_lds_dwordx4 v25, s[6:7] offset:1024\n" \
  "global_load_lds_dwordx4 v26, s[6:7] offset:2048\n" \
  "global_load_lds_dwordx4 v27, s[6:7] offset:3072\n" \
  "s_add_u32 s6, s6, 0x100000\n" \
  "s_addc_u32 s7, s7, 0\n" \
  "s_waitcnt vmcnt(8) lgkmcnt(0)\n" \
  "s_barrier\n" \
  "ds_read_b64_tr_b16 v[64:65], v28 offset:0\n" \
  "ds_read_b64_tr_b16 v[66:67], v28 offset:512\n" \
  "ds_read_b64_tr_b16 v[68:69], v28 offset:32768\n" \
  "ds_read_b64_tr_b16 v[70:71], v28 offset:33280\n" \
  "ds_read_b64_tr_b16 v[72:73], v17 offset:0\n" \
  "ds_read_b64_tr_b16 v[74:75], v17 offset:512\n" \
  "ds_read_b64_tr_b16 v[76:77], v17 offset:32768\n" \
  "ds_read_b64_tr_b16 v[78:79], v17 offset:33280\n" \
  "ds_read_b64_tr_b16 v[192:193], v18 offset:0\n" \
  "ds_read_b64_tr_b16 v[194:195], v18 offset:512\n" \
  "ds_read_b64_tr_b16 v[196:197], v18 offset:32768\n" \
  "ds_read_b64_tr_b16 v[198:199], v18 offset:33280\n" \
  "ds_read_b64_tr_b16 v[200:201], v19 offset:0\n" \
  "ds_read_b64_tr_b16 v[202:203], v19 offset:512\n" \
  "ds_read_b64_tr_b16 v[204:205], v19 offset:32768\n" \
  "ds_read_b64_tr_b16 v[206:207], v19 offset:33280\n" \
  "s_add_u32 m0, s19, 12288\n" \
  "s_nop 0\n" \
  "global_load_lds_dwordx4 v24, s[6:7] offset:0\n" \
  "global_load_lds_dwordx4 v25, s[6:7] offset:1024\n" \
  "global_load_lds_dwordx4 v26, s[6:7] offset:2048\n" \
  "global_load_lds_dwordx4 v27, s[6:7] offset:3072\n" \
  "s_add_u32 s6, s6, 0x100000\n" \
  "s_addc_u32 s7, s7, 0\n" \
  "s_waitcnt vmcnt(8) lgkmcnt(0)\n" \
  "s_barrier\n" \
  "ds_read_b64_tr_b16 v[80:81], v28 offset:4096\n" \
  "ds_read_b64_tr_b16 v[82:83], v28 offset:4608\n" \
  "ds_read_b64_tr_b16 v[84:85], v28 offset:36864\n" \
  "ds_read_b64_tr_b16 v[86:87], v28 offset:37376\n" \
  "ds_read_b64_tr_b16 v[88:89], v17 offset:4096\n" \
  "ds_read_b64_tr_b16 v[90:91], v17 offset:4608\n" \
  "ds_read_b64_tr_b16 v[92:93], v17 offset:36864\n" \
  "ds_read_b64_tr_b16 v[94:95], v17 offset:37376\n" \
  "ds_read_b64_tr_b16 v[208:209], v18 offset:4096\n" \
  "ds_read_b64_tr_b16 v[210:211], v18 offset:4608\n" \
  "ds_read_b64_tr_b16 v[212:213], v18 offset:36864\n" \
  "ds_read_b64_tr_b16 v[214:215], v18 offset:37376\n" \
  "ds_read_b64_tr_b16 v[216:217], v19 offset:4096\n" \
  "ds_read_b64_tr_b16 v[218:219], v19 offset:4608\n" \
  "ds_read_b64_tr_b16 v[220:221], v19 offset:36864\n" \
  "ds_read_b64_tr_b16 v[222:223], v19 offset:37376\n" \
  "s_add_u32 m0, s19, 0\n" \
  "s_nop 0\n" \
  "global_load_lds_dwordx4 v24, s[8:9] offset:0\n" \
  "global_load_lds_dwordx4 v25, s[8:9] offset:1024\n" \
  "global_load_lds_dwordx4 v26, s[8:9] offset:2048\n" \
  "global_load_lds_dwordx4 v27, s[8:9] offset:3072\n" \
  "s_add_u32 s8, s8, 0x100000\n" \
  "s_addc_u32 s9, s9, 0\n" \
  "s_waitcnt vmcnt(8) lgkmcnt(0)\n" \
  "s_barrier\n" \
  "ds_read_b64_tr_b16 v[96:97], v28 offset:8192\n" \
  "ds_read_b64_tr_b16 v[98:99], v28 offset:8704\n" \
  "ds_read_b64_tr_b16 v[100:101], v28 offset:40960\n" \
  "ds_read_b64_tr_b16 v[102:103], v28 offset:41472\n" \
  "ds_read_b64_tr_b16 v[104:105], v17 offset:8192\n" \
  "ds_read_b64_tr_b16 v[106:107], v17 offset:8704\n" \
  "ds_read_b64_tr_b16 v[108:109], v17 offset:40960\n" \
  "ds_read_b64_tr_b16 v[110:111], v17 offset:41472\n" \
  "ds_read_b64_tr_b16 v[224:225], v18 offset:8192\n" \
  "ds_read_b64_tr_b16 v[226:227], v18 offset:8704\n" \
  "ds_read_b64_tr_b16 v[228:229], v18 offset:40960\n" \
  "ds_read_b64_tr_b16 v[230:231], v18 offset:41472\n" \
  "ds_read_b64_tr_b16 v[232:233], v19 offset:8192\n" \
  "ds_read_b64_tr_b16 v[234:235], v19 offset:8704\n" \
  "ds_read_b64_tr_b16 v[236:237], v19 offset:40960\n" \
  "ds_read_b64_tr_b16 v[238:239], v19 offset:41472\n" \
  "s_add_u32 m0, s19, 4096\n" \
  "s_nop 0\n" \
  "global_load_lds_dwordx4 v24, s[8:9] offset:0\n" \
  "global_load_lds_dwordx4 v25, s[8:9] offset:1024\n" \
  "global_load_lds_dwordx4 v26, s[8:9] offset:2048\n" \
  "global_load_lds_dwordx4 v27, s[8:9] offset:3072\n" \
  "s_add_u32 s8, s8, 0x100000\n" \
  "s_addc_u32 s9, s9, 0\n" \
  "s_waitcnt vmcnt(8) lgkmcnt(0)\n" \
  "s_barrier\n" \
  "ds_read_b64_tr_b16 v[112:113], v28 offset:12288\n" \
  "ds_read_b64_tr_b16 v[114:115], v28 offset:12800\n" \
  "ds_read_b64_tr_b16 v[116:117], v28 offset:45056\n" \
  "ds_read_b64_tr_b16 v[118:119], v28 offset:45568\n" \
  "ds_read_b64_tr_b16 v[120:121], v17 offset:12288\n" \
  "ds_read_b64_tr_b16 v[122:123], v17 offset:12800\n" \
  "ds_read_b64_tr_b16 v[124:125], v17 offset:45056\n" \
  "ds_read_b64_tr_b16 v[126:127], v17 offset:45568\n" \
  "ds_read_b64_tr_b16 v[240:241], v18 offset:12288\n" \
  "ds_read_b64_tr_b16 v[242:243], v18 offset:12800\n" \
  "ds_read_b64_tr_b16 v[244:245], v18 offset:45056\n" \
  "ds_read_b64_tr_b16 v[246:247], v18 offset:45568\n" \
  "ds_read_b64_tr_b16 v[248:249], v19 offset:12288\n" \
  "ds_read_b64_tr_b16 v[250:251], v19 offset:12800\n" \
  "ds_read_b64_tr_b16 v[252:253], v19 offset:45056\n" \
  "ds_read_b64_tr_b16 v[254:255], v19 offset:45568\n" \
  "s_add_u32 m0, s19, 8192\n" \
  "s_nop 0\n" \
  "global_load_lds_dwordx4 v24, s[8:9] offset:0\n" \
  "global_load_lds_dwordx4 v25, s[8:9] offset:1024\n" \
  "global_load_lds_dwordx4 v26, s[8:9] offset:2048\n" \
  "global_load_lds_dwordx4 v27, s[8:9] offset:3072\n" \
  "s_add_u32 s8, s8, 0x100000\n" \
  "s_addc_u32 s9, s9, 0\n" \
  "s_waitcnt vmcnt(8) lgkmcnt(0)\n" \
  "s_barrier\n" \
  "ds_read_b64_tr_b16 v[128:129], v28 offset:0\n" \
  "ds_read_b64_tr_b16 v[130:131], v28 offset:512\n" \
  "ds_read_b64_tr_b16 v[132:133], v28 offset:32768\n" \
  "ds_read_b64_tr_b16 v[134:135], v28 offset:33280\n" \
  "ds_read_b64_tr_b16 v[136:137], v17 offset:0\n" \
  "ds_read_b64_tr_b16 v[138:139], v17 offset:512\n" \
  "ds_read_b64_tr_b16 v[140:141], v17 offset:32768\n" \
  "ds_read_b64_tr_b16 v[142:143], v17 offset:33280\n" \
  "s_add_u32 m0, s19, 12288\n" \
  "s_nop 0\n" \
  "global_load_lds_dwordx4 v24, s[8:9] offset:0\n" \
  "global_load_lds_dwordx4 v25, s[8:9] offset:1024\n" \
  "global_load_lds_dwordx4 v26, s[8:9] offset:2048\n" \
  "global_load_lds_dwordx4 v27, s[8:9] offset:3072\n" \
  "s_add_u32 s8, s8, 0x100000\n" \
  "s_addc_u32 s9, s9, 0\n" \
  "s_waitcnt vmcnt(8) lgkmcnt(0)\n" \
  "s_barrier\n" \
  "ds_write_b128 v6, v[160:163] offset:0\n" \
  "ds_write_b128 v6, v[164:167] offset:1024\n" \
  "ds_write_b128 v6, v[168:171] offset:2048\n" \
  "ds_write_b128 v6, v[172:175] offset:3072\n" \
  "ds_read_b64_tr_b16 v[144:145], v28 offset:4096\n" \
  "ds_read_b64_tr_b16 v[146:147], v28 offset:4608\n" \
  "ds_read_b64_tr_b16 v[148:149], v28 offset:36864\n" \
  "ds_read_b64_tr_b16 v[150:151], v28 offset:37376\n" \
  "ds_read_b64_tr_b16 v[152:153], v17 offset:4096\n" \
  "ds_read_b64_tr_b16 v[154:155], v17 offset:4608\n" \
  "ds_read_b64_tr_b16 v[156:157], v17 offset:36864\n" \
  "ds_read_b64_tr_b16 v[158:159], v17 offset:37376\n" \
  "s_waitcnt vmcnt(4) lgkmcnt(0)\n" \
  "s_barrier\n" \
  "ds_write_b128 v6, v[176:179] offset:4096\n" \
  "ds_write_b128 v6, v[180:183] offset:5120\n" \
  "ds_write_b128 v6, v[184:187] offset:6144\n" \
  "ds_write_b128 v6, v[188:191] offset:7168\n" \
  "ds_read_b64_tr_b16 v[160:161], v28 offset:8192\n" \
  "ds_read_b64_tr_b16 v[162:163], v28 offset:8704\n" \
  "ds_read_b64_tr_b16 v[164:165], v28 offset:40960\n" \
  "ds_read_b64_tr_b16 v[166:167], v28 offset:41472\n" \
  "ds_read_b64_tr_b16 v[168:169], v17 offset:8192\n" \
  "ds_read_b64_tr_b16 v[170:171], v17 offset:8704\n" \
  "ds_read_b64_tr_b16 v[172:173], v17 offset:40960\n" \
  "ds_read_b64_tr_b16 v[174:175], v17 offset:41472\n" \
  "s_waitcnt vmcnt(0) lgkmcnt(0)\n" \
  "s_barrier\n" \
  "ds_write_b128 v6, v[32:35] offset:8192\n" \
  "ds_write_b128 v6, v[36:39] offset:9216\n" \
  "ds_write_b128 v6, v[40:43] offset:10240\n" \
  "ds_write_b128 v6, v[44:47] offset:11264\n" \
  "ds_read_b64_tr_b16 v[176:177], v28 offset:12288\n" \
  "ds_read_b64_tr_b16 v[178:179], v28 offset:12800\n" \
  "ds_read_b64_tr_b16 v[180:181], v28 offset:45056\n" \
  "ds_read_b64_tr_b16 v[182:183], v28 offset:45568\n" \
  "ds_read_b64_tr_b16 v[184:185], v17 offset:12288\n" \
  "ds_read_b64_tr_b16 v[186:187], v17 offset:12800\n" \
  "ds_read_b64_tr_b16 v[188:189], v17 offset:45056\n" \
  "ds_read_b64_tr_b16 v[190:191], v17 offset:45568\n" \
  "ds_read_b32 v31, v30 offset:0\n" \
  "s_waitcnt lgkmcnt(0)\n" \
  "s_barrier\n" \
  "ds_write_b128 v6, v[48:51] offset:12288\n" \
  "ds_write_b128 v6, v[52:55] offset:13312\n" \
  "ds_write_b128 v6, v[56:59] offset:14336\n" \
  "ds_write_b128 v6, v[60:63] offset:15360\n" \
  "v_mul_f32 v31, 0x3f8e353f, v31\n" \
  "v_mul_f32 v30, 0x44870000, v31\n" \
  "v_mov_b64 v[12:13], 0\n" \
  "v_mov_b64 v[14:15], 0\n" \
  "v_mov_b64 v[16:17], 0\n" \
  "v_mov_b64 v[18:19], 0\n" \
  "v_mov_b64 v[20:21], 0\n" \
  "v_mov_b64 v[22:23], 0\n" \
  "v_readfirstlane_b32 s72, v31\n" \
  "v_readfirstlane_b32 s86, v30\n" \
  "s_lshr_b32 s86, s86, 23\n" \
  "s_addk_i32 s86, 0xff83\n" \
  "s_ashr_i32 s86, s86, 1\n" \
  "s_max_i32 s86, s86, -20\n" \
  "s_min_i32 s86, s86, 100\n" \
  "s_mov_b32 s82, s86\n" \
  "s_lshr_b32 s83, s72, 23\n" \
  "s_sub_i32 s83, s83, 243\n" \
  "s_waitcnt lgkmcnt(0)\n" \
  "s_add_u32 s88, s74, 157760\n" \
  "v_mov_b32 v59, s88\n" \
  "v_mov_b32 v34, 0xc9b26ca0\n" \
  "v_mov_b32 v35, 0xc9b26c86\n" \
  ".p2align 6\n" \
  "s_nop 0\n" \
  "s_nop 0\n" \
  "s_nop 0\n" \
  ".LCA_TOP_%=:\n" \
  "s_waitcnt lgkmcnt(0)\n" \
  "s_barrier\n" \
  "ds_read_b128 v[40:43], v1 offset:0\n" \
  "ds_read_b128 v[44:47], v1 offset:1024\n" \
  "ds_read_b128 v[48:51], v1 offset:2048\n" \
  "ds_read_b128 v[52:55], v1 offset:3072\n" \
  "ds_read_b128 v[56:59], v1 offset:4096\n" \
  "ds_read_b128 v[60:63], v1 offset:5120\n" \
  "s_cmp_lg_u32 s73, 0\n" \
  "s_cbranch_scc1 .LCA_NOZ_%=\n" \
  "s_xor_b32 s88, s60, 0x10000\n" \
  "s_add_u32 s88, s88, s61\n" \
  "buffer_store_dwordx2 v[34:35], v5, s[40:43], s88 offen sc1\n" \
  ".LCA_NOZ_%=:\n" \
  "s_waitcnt lgkmcnt(5)\n" \
  "v_smfmac_f32_16x16x64_bf16 v[20:23], v[40:43], v[192:199], v0\n" \
  "v_smfmac_f32_16x16x64_bf16 v[12:15], v[40:43], v[64:71], v0\n" \
  "v_smfmac_f32_16x16x64_bf16 v[16:19], v[40:43], v[128:135], v0\n" \
  "ds_read_b128 v[40:43], v1 offset:6144\n" \
  "s_waitcnt lgkmcnt(5)\n" \
  "v_smfmac_f32_16x16x64_bf16 v[20:23], v[44:47], v[200:207], v0\n" \
  "v_smfmac_f32_16x16x64_bf16 v[12:15], v[44:47], v[72:79], v0\n" \
  "v_smfmac_f32_16x16x64_bf16 v[16:19], v[44:47], v[136:143], v0\n" \
  "ds_read_b128 v[44:47], v1 offset:7168\n" \
  "s_waitcnt lgkmcnt(5)\n" \
  "v_smfmac_f32_16x16x64_bf16 v[20:23], v[48:51], v[208:215], v0\n" \
  "v_smfmac_f32_16x16x64_bf16 v[12:15], v[48:51], v[80:87], v0\n" \
  "v_smfmac_f32_16x16x64_bf16 v[16:19], v[48:51], v[144:151], v0\n" \
  "ds_read_b128 v[24:27], v1 offset:10240\n" \
  "ds_read_b128 v[28:31], v1 offset:14336\n" \
  "s_waitcnt lgkmcnt(6)\n" \
  "v_smfmac_f32_16x16x64_bf16 v[20:23], v[52:55], v[216:223], v0\n" \
  "v_smfmac_f32_16x16x64_bf16 v[12:15], v[52:55], v[88:95], v0\n" \
  "v_smfmac_f32_16x16x64_bf16 v[16:19], v[52:55], v[152:159], v0\n" \
  "ds_read_b128 v[32:35], v1 offset:11264\n" \
  "ds_read_b128 v[36:39], v1 offset:15360\n" \
  "s_waitcnt lgkmcnt(7)\n" \
  "v_smfmac_f32_16x16x64_bf16 v[20:23], v[56:59], v[224:231], v0\n" \
  "v_smfmac_f32_16x16x64_bf16 v[12:15], v[56:59], v[96:103], v0\n" \
  "v_smfmac_f32_16x16x64_bf16 v[16:19], v[56:59], v[160:167], v0\n" \
  "ds_read_b128 v[56:59], v1 offset:12288\n" \
  "s_waitcnt lgkmcnt(7)\n" \
  "v_smfmac_f32_16x16x64_bf16 v[20:23], v[60:63], v[232:239], v0\n" \
  "v_smfmac_f32_16x16x64_bf16 v[12:15], v[60:63], v[104:111], v0\n" \
  "v_smfmac_f32_16x16x64_bf16 v[16:19], v[60:63], v[168:175], v0\n" \
  "ds_read_b128 v[60:63], v1 offset:16384\n" \
  "s_waitcnt lgkmcnt(7)\n" \
  "v_smfmac_f32_16x16x64_bf16 v[20:23], v[40:43], v[240:247], v0\n" \
  "v_smfmac_f32_16x16x64_bf16 v[12:15], v[40:43], v[112:119], v0\n" \
  "v_smfmac_f32_16x16x64_bf16 v[16:19], v[40:43], v[176:183], v0\n" \
  "s_waitcnt lgkmcnt(6)\n" \
  "v_smfmac_f32_16x16x64_bf16 v[20:23], v[44:47], v[248:255], v0\n" \
  "v_smfmac_f32_16x16x64_bf16 v[12:15], v[44:47], v[120:127], v0\n" \
  "v_smfmac_f32_16x16x64_bf16 v[16:19], v[44:47], v[184:191], v0\n" \
  "s_cmp_eq_u32 s71, 0\n" \
  "v_permlane32_swap_b32 v20, v22\n" \
  "v_permlane32_swap_b32 v21, v23\n" \
  "v_pk_add_f32 v[20:21], v[20:21], v[22:23]\n" \
  "s_mov_b64 vcc, s[76:77]\n" \
  "v_permlane32_swap_b32 v12, v14\n" \
  "v_permlane32_swap_b32 v13, v15\n" \
  "v_cvt_pk_bf16_f32 v20, v20, v21\n" \
  "v_pk_add_f32 v[12:13], v[12:13], v[14:15]\n" \
  "v_permlane32_swap_b32 v16, v18\n" \
  "v_permlane32_swap_b32 v17, v19\n" \
  "ds_write_b32 v8, v20 offset:2048\n" \
  "v_cvt_pk_bf16_f32 v12, v12, v13\n" \
  "v_pk_add_f32 v[16:17], v[16:17], v[18:19]\n" \
  "s_nop 0\n" \
  "v_cvt_pk_bf16_f32 v13, v16, v17\n" \
  "ds_write_b64 v8, v[12:13]\n" \
  "s_setprio 1\n" \
  "s_waitcnt lgkmcnt(0)\n" \
  "s_barrier\n" \
  "s_cbranch_scc1 .LCA_LAST_%=\n" \
  "ds_read_b64_tr_b16 v[40:41], v2 offset:0\n" \
  "ds_read_b64_tr_b16 v[42:43], v3 offset:0\n" \
  "ds_read_b64_tr_b16 v[44:45], v2 offset:1024\n" \
  "ds_read_b64_tr_b16 v[46:47], v3 offset:1024\n" \
  "ds_read_b64_tr_b16 v[48:49], v2 offset:2048\n" \
  "ds_read_b64_tr_b16 v[50:51], v3 offset:2048\n" \
  "ds_read_b64_tr_b16 v[52:53], v2 offset:3072\n" \
  "ds_read_b64_tr_b16 v[54:55], v3 offset:3072\n" \
  "ds_read_b32 v11, v1 offset:26688\n" \
  "s_waitcnt lgkmcnt(7)\n" \
  "v_mfma_f32_16x16x32_bf16 v[12:15], v[24:27], v[40:43], 0\n" \
  "v_mfma_f32_16x16x32_bf16 v[16:19], v[28:31], v[40:43], 0\n" \
  "ds_read_b128 v[24:27], v1 offset:13312\n" \
  "ds_read_b128 v[28:31], v1 offset:17408\n" \
  "v_mfma_f32_16x16x32_bf16 v[20:23], v[40:43], v[40:43], 0\n" \
  "s_waitcnt lgkmcnt(7)\n" \
  "v_mfma_f32_16x16x32_bf16 v[12:15], v[32:35], v[44:47], v[12:15]\n" \
  "v_mfma_f32_16x16x32_bf16 v[16:19], v[36:39], v[44:47], v[16:19]\n" \
  "v_mfma_f32_16x16x32_bf16 v[20:23], v[44:47], v[44:47], v[20:23]\n" \
  "s_waitcnt lgkmcnt(2)\n" \
  "v_mfma_f32_16x16x32_bf16 v[12:15], v[56:59], v[48:51], v[12:15]\n" \
  "v_readfirstlane_b32 s86, v11\n" \
  "v_mfma_f32_16x16x32_bf16 v[16:19], v[60:63], v[48:51], v[16:19]\n" \
  "ds_read_b128 v[32:35], v6 offset:8192\n" \
  "ds_read_b128 v[36:39], v6 offset:9216\n" \
  "v_mfma_f32_16x16x32_bf16 v[20:23], v[48:51], v[48:51], v[20:23]\n" \
  "s_lshr_b32 s86, s86, 23\n" \
  "s_add_i32 s86, s86, s83\n" \
  "s_ashr_i32 s86, s86, 1\n" \
  "ds_read_b128 v[48:51], v10 offset:18432\n" \
  "v_mfma_f32_16x16x32_bf16 v[20:23], v[52:55], v[52:55], v[20:23]\n" \
  "s_max_i32 s86, s86, s82\n" \
  "s_min_i32 s86, s86, 100\n" \
  "s_lshl_b32 s86, s86, 23\n" \
  "s_sub_i32 s84, 0xc8800000, s86\n" \
  "s_waitcnt lgkmcnt(3)\n" \
  "v_mfma_f32_16x16x32_bf16 v[12:15], v[24:27], v[52:55], v[12:15]\n" \
  "v_mfma_f32_16x16x32_bf16 v[16:19], v[28:31], v[52:55], v[16:19]\n" \
  "ds_read_b128 v[52:55], v10 offset:22528\n" \
  "ds_read_b128 v[56:59], v6 offset:0\n" \
  "ds_read_b128 v[60:63], v6 offset:1024\n" \
  "ds_read_b128 v[24:27], v6 offset:2048\n" \
  "ds_read_b128 v[28:31], v6 offset:3072\n" \
  "v_cndmask_b32_dpp v20, v23, v22, vcc quad_perm:[2,3,0,1] row_mask:0xf bank_mask:0xf\n" \
  "v_mov_b32 v41, 0\n" \
  "v_mov_b32 v42, 0\n" \
  "v_cndmask_b32_dpp v12, v16, v12, vcc quad_perm:[2,3,0,1] row_mask:0xf bank_mask:0xf\n" \
  "v_cndmask_b32_dpp v13, v17, v13, vcc quad_perm:[2,3,0,1] row_mask:0xf bank_mask:0xf\n" \
  "v_cndmask_b32_dpp v14, v18, v14, vcc quad_perm:[2,3,0,1] row_mask:0xf bank_mask:0xf\n" \
  "v_cndmask_b32_dpp v15, v19, v15, vcc quad_perm:[2,3,0,1] row_mask:0xf bank_mask:0xf\n" \
  "v_maximum3_f32 v16, v20, v12, v13\n" \
  "v_maximum3_f32 v16, v16, v14, v15\n" \
  "s_nop 1\n" \
  "v_max_f32_dpp v16, v16, v16 quad_perm:[2,3,0,1] row_mask:0xf bank_mask:0xf\n" \
  "v_mov_b32 v17, v16\n" \
  "s_nop 1\n" \
  "v_permlane16_swap_b32 v16, v17\n" \
  "v_max_f32 v16, v16, v17\n" \
  "v_mov_b32 v17, v16\n" \
  "s_nop 1\n" \
  "v_permlane32_swap_b32 v16, v17\n" \
  "v_max_f32 v16, v16, v17\n" \
  "v_mul_f32 v16, s78, v16\n" \
  "v_pk_fma_f32 v[12:13], v[12:13], s[78:79], v[16:17] op_sel_hi:[1,0,0] neg_lo:[0,0,1] neg_hi:[0,0,1]\n" \
  "v_pk_fma_f32 v[14:15], v[14:15], s[78:79], v[16:17] op_sel_hi:[1,0,0] neg_lo:[0,0,1] neg_hi:[0,0,1]\n" \
  "v_fma_f32 v20, v20, s78, -v16\n" \
  "v_exp_f32 v12, v12\n" \
  "v_exp_f32 v13, v13\n" \
  "v_exp_f32 v14, v14\n" \
  "v_exp_f32 v15, v15\n" \
  "v_exp_f32 v20, v20\n" \
  "s_waitcnt lgkmcnt(4)\n" \
  "v_cvt_pk_bf16_f32 v44, v12, v13\n" \
  "v_cvt_pk_bf16_f32 v45, v14, v15\n" \
  "v_pk_add_f32 v[8:9], v[12:13], v[14:15]\n" \
  "v_mov_b32_dpp v46, v44 quad_perm:[2,3,0,1] row_mask:0xf bank_mask:0xf\n" \
  "v_mov_b32_dpp v47, v45 quad_perm:[2,3,0,1] row_mask:0xf bank_mask:0xf\n" \
  "v_add_f32 v10, v8, v20\n" \
  "v_add_f32 v10, v10, v9\n" \
  "v_mfma_f32_16x16x32_bf16 v[12:15], v[48:51], v[44:47], 0\n" \
  "v_mov_b32_dpp v21, v20 quad_perm:[2,3,0,1] row_mask:0xf bank_mask:0xf\n" \
  "v_add_f32_dpp v10, v10, v10 quad_perm:[2,3,0,1] row_mask:0xf bank_mask:0xf\n" \
  "v_mov_b32 v11, v10\n" \
  "v_mfma_f32_16x16x32_bf16 v[16:19], v[52:55], v[44:47], 0\n" \
  "s_setprio 3\n" \
  "s_barrier\n" \
  "ds_read_b64 v[22:23], v7 offset:0\n" \
  "s_nop 0\n" \
  "v_permlane16_swap_b32 v10, v11\n" \
  "v_add_f32 v10, v10, v11\n" \
  "v_mov_b32 v11, v10\n" \
  "v_mov_b64 v[8:9], s[92:93]\n" \
  "s_nop 0\n" \
  "v_permlane32_swap_b32 v10, v11\n" \
  "v_add_f32 v10, v10, v11\n" \
  "v_rcp_f32 v10, v10\n" \
  "s_nop 0\n" \
  "v_mul_f32 v10, s84, v10\n" \
  "v_pk_mul_f32 v[20:21], v[20:21], v[10:11] op_sel_hi:[1,0]\n" \
  "v_pk_mul_f32 v[12:13], v[12:13], v[10:11] op_sel_hi:[1,0]\n" \
  "v_pk_mul_f32 v[14:15], v[14:15], v[10:11] op_sel_hi:[1,0]\n" \
  "v_cvt_pk_bf16_f32 v40, v20, v21\n" \
  "v_pk_mul_f32 v[16:17], v[16:17], v[10:11] op_sel_hi:[1,0]\n" \
  "v_cvt_pk_bf16_f32 v43, v20, v21\n" \
  "v_pk_mul_f32 v[18:19], v[18:19], v[10:11] op_sel_hi:[1,0]\n" \
  "s_waitcnt lgkmcnt(0)\n" \
  "v_mfma_f32_16x16x16_bf16 v[12:15], v[22:23], v[40:41], v[12:15]\n" \
  "v_mfma_f32_16x16x16_bf16 v[16:19], v[22:23], v[42:43], v[16:19]\n" \
  "v_mov_b64 v[10:11], s[92:93]\n" \
  "ds_read_b128 v[48:51], v6 offset:10240\n" \
  "ds_read_b128 v[52:55], v6 offset:11264\n" \
  "v_mov_b64 v[20:21], s[92:93]\n" \
  "v_mov_b64 v[22:23], s[92:93]\n" \
  "s_nop 1\n" \
  "v_cvt_pk_bf16_f32 v40, v12, v13\n" \
  "v_cvt_pk_bf16_f32 v41, v14, v15\n" \
  "v_cvt_pk_bf16_f32 v42, v16, v17\n" \
  "ds_write_b64 v4, v[40:41]\n" \
  "v_cvt_pk_bf16_f32 v43, v18, v19\n" \
  "ds_write_b64 v4, v[42:43] offset:1024\n" \
  "v_mov_b64 v[12:13], s[92:93]\n" \
  "v_mov_b64 v[14:15], s[92:93]\n" \
  "v_mov_b64 v[16:17], s[92:93]\n" \
  "v_mov_b64 v[18:19], s[92:93]\n" \
  "s_setprio 0\n" \
  "s_waitcnt lgkmcnt(0)\n" \
  "s_barrier\n" \
  "ds_read_b128 v[40:43], v1 offset:8192\n" \
  "ds_read_b128 v[44:47], v1 offset:9216\n" \
  "s_waitcnt lgkmcnt(1)\n" \
  "v_smfmac_f32_16x16x64_bf16 v[12:15], v[40:43], v[56:63], v0\n" \
  "ds_read_b128 v[56:59], v6 offset:4096\n" \
  "ds_read_b128 v[60:63], v6 offset:5120\n" \
  "v_smfmac_f32_16x16x64_bf16 v[16:19], v[40:43], v[24:31], v0\n" \
  "ds_read_b128 v[24:27], v6 offset:6144\n" \
  "ds_read_b128 v[28:31], v6 offset:7168\n" \
  "s_waitcnt lgkmcnt(4)\n" \
  "v_smfmac_f32_16x16x64_bf16 v[12:15], v[44:47], v[32:39], v0\n" \
  "ds_read_b128 v[32:35], v6 offset:12288\n" \
  "ds_read_b128 v[36:39], v6 offset:13312\n" \
  "v_smfmac_f32_16x16x64_bf16 v[16:19], v[44:47], v[48:55], v0\n" \
  "ds_read_b128 v[48:51], v6 offset:14336\n" \
  "ds_read_b128 v[52:55], v6 offset:15360\n" \
  "s_nop 5\n" \
  "v_permlane32_swap_b32 v12, v17\n" \
  "v_permlane32_swap_b32 v13, v16\n" \
  "v_permlane32_swap_b32 v14, v19\n" \
  "v_permlane32_swap_b32 v15, v18\n" \
  "v_pk_add_f32 v[12:13], v[12:13], v[16:17] op_sel:[0,1] op_sel_hi:[1,0]\n" \
  "v_pk_add_f32 v[14:15], v[14:15], v[18:19] op_sel:[0,1] op_sel_hi:[1,0]\n" \
  "buffer_atomic_add_x2 v[12:13], v5, s[40:43], s56 offen\n" \
  "buffer_atomic_add_x2 v[14:15], v5, s[40:43], s57 offen\n" \
  "s_waitcnt lgkmcnt(6)\n" \
  "v_smfmac_f32_16x16x64_bf16 v[20:23], v[40:43], v[56:63], v0\n" \
  "s_waitcnt lgkmcnt(4)\n" \
  "v_smfmac_f32_16x16x64_bf16 v[8:11], v[40:43], v[24:31], v0\n" \
  "s_waitcnt lgkmcnt(2)\n" \
  "v_smfmac_f32_16x16x64_bf16 v[20:23], v[44:47], v[32:39], v0\n" \
  "s_waitcnt lgkmcnt(0)\n" \
  "v_smfmac_f32_16x16x64_bf16 v[8:11], v[44:47], v[48:55], v0\n" \
  "s_nop 7\n" \
  "v_permlane32_swap_b32 v20, v9\n" \
  "v_permlane32_swap_b32 v21, v8\n" \
  "v_permlane32_swap_b32 v22, v11\n" \
  "v_permlane32_swap_b32 v23, v10\n" \
  "v_pk_add_f32 v[20:21], v[20:21], v[8:9] op_sel:[0,1] op_sel_hi:[1,0]\n" \
  "v_pk_add_f32 v[22:23], v[22:23], v[10:11] op_sel:[0,1] op_sel_hi:[1,0]\n" \
  "buffer_atomic_add_x2 v[20:21], v5, s[40:43], s58 offen\n" \
  "buffer_atomic_add_x2 v[22:23], v5, s[40:43], s59 offen\n" \
  "s_memrealtime s[38:39]\n" \
  "v_mbcnt_lo_u32_b32 v48, -1, 0\n" \
  "v_mbcnt_hi_u32_b32 v48, -1, v48\n" \
  "s_add_u32 s88, s74, 157760\n" \
  "v_mov_b32 v59, s88\n" \
  "v_lshl_add_u32 v52, v48, 2, v59\n" \
  "ds_read_b32 v52, v52 offset:192\n" \
  "v_lshrrev_b32 v49, 3, v48\n" \
  "v_and_b32 v50, 7, v48\n" \
  "v_lshlrev_b32 v50, 4, v50\n" \
  "v_lshl_or_b32 v44, v49, 8, v50\n" \
  "s_mov_b32 s87, 0x40000\n" \
  "v_lshrrev_b32 v49, 2, v48\n" \
  "v_lshrrev_b32 v50, 4, v48\n" \
  "v_bitop3_b32 v50, v49, v50, 3 bitop3:0x28\n" \
  "v_lshl_add_u32 v8, v50, 3, s97\n" \
  "v_and_b32 v50, 3, v48\n" \
  "v_lshl_add_u32 v8, v50, 5, v8\n" \
  "v_and_b32 v49, 1, v49\n" \
  "v_lshl_add_u32 v8, v49, 8, v8\n" \
  "v_and_b32 v49, 8, v48\n" \
  "v_lshl_add_u32 v8, v49, 4, v8\n" \
  "v_lshrrev_b32 v49, 5, v48\n" \
  "v_add_u32 v10, s99, v1\n" \
  "s_lshr_b32 s88, s99, 2\n" \
  "s_add_u32 s88, s88, s74\n" \
  "s_add_u32 s88, s88, 139264\n" \
  "v_lshl_add_u32 v7, v49, 10, s88\n" \
  "v_and_b32 v49, 31, v48\n" \
  "v_lshl_add_u32 v7, v49, 3, v7\n" \
  "v_mov_b64 v[12:13], 0\n" \
  "v_mov_b64 v[14:15], 0\n" \
  "v_mov_b64 v[16:17], 0\n" \
  "v_mov_b64 v[18:19], 0\n" \
  "v_mov_b64 v[20:21], 0\n" \
  "v_mov_b64 v[22:23], 0\n" \
  "v_mov_b32 v34, 0xc9b26ca0\n" \
  "v_mov_b32 v35, 0xc9b26c86\n" \
  "v_and_b32 v50, 3, v48\n" \
  "v_lshl_add_u32 v40, v50, 2, s98\n" \
  "v_lshrrev_b32 v50, 4, v48\n" \
  "v_mad_u32_u24 v40, v50, s95, v40\n" \
  "v_bfe_u32 v49, v48, 3, 1\n" \
  "v_lshl_add_u32 v40, v49, 6, v40\n" \
  "v_bfe_u32 v49, v48, 2, 1\n" \
  "v_lshl_add_u32 v40, v49, 7, v40\n" \
  "v_xor_b32 v41, 16, v40\n" \
  "v_xor_b32 v42, 32, v40\n" \
  "v_xor_b32 v43, 48, v40\n" \
  "s_add_i32 s80, s86, 0x35000000\n" \
  "v_mov_b32 v11, 0\n" \
  "s_mov_b32 s89, s60\n" \
  "s_bitcmp1_b32 s60, 7\n" \
  "s_cselect_b32 s88, 0x10000, 0\n" \
  "s_bitset1_b32 s88, 7\n" \
  "s_xor_b32 s56, s56, s88\n" \
  "s_xor_b32 s57, s57, s88\n" \
  "s_xor_b32 s58, s58, s88\n" \
  "s_xor_b32 s59, s59, s88\n" \
  "s_xor_b32 s60, s60, s88\n" \
  "s_sub_u32 s71, s71, 1\n" \
  "s_waitcnt lgkmcnt(0)\n" \
  "s_sub_u32 s39, s38, 150\n" \
  "v_subrev_u32 v52, s39, v52\n" \
  "v_cmp_lt_u32 vcc, 0x1000, v52\n" \
  "s_cmp_eq_u64 vcc, 0\n" \
  "s_cbranch_scc1 .LCA_OKM_%=\n" \
  ".LCA_AGN_%=:\n" \
  "buffer_load_dword v52, v5, s[0:3], s66 offen sc1\n" \
  "s_sub_u32 s87, s87, 1\n" \
  "s_cbranch_scc1 .LCA_END_%=\n" \
  "s_waitcnt vmcnt(0)\n" \
  "v_cmp_eq_u32 vcc, 0, v52\n" \
  "v_subrev_u32 v52, s39, v52\n" \
  "s_cmp_eq_u64 vcc, 0\n" \
  "s_cbranch_scc1 .LCA_OKM_%=\n" \
  "s_sleep 4\n" \
  "s_branch .LCA_AGN_%=\n" \
  ".LCA_OKM_%=:\n" \
  "s_nop 1\n" \
  "v_max_i32_dpp v52, v52, v52 row_ror:1 row_mask:0xf bank_mask:0xf\n" \
  "s_nop 1\n" \
  "v_max_i32_dpp v52, v52, v52 row_ror:2 row_mask:0xf bank_mask:0xf\n" \
  "s_nop 1\n" \
  "v_max_i32_dpp v52, v52, v52 row_ror:4 row_mask:0xf bank_mask:0xf\n" \
  "s_nop 1\n" \
  "v_max_i32_dpp v52, v52, v52 row_ror:8 row_mask:0xf bank_mask:0xf\n" \
  "s_movk_i32 vcc_lo, 150\n" \
  "v_readlane_b32 s88, v52, 0\n" \
  "s_max_i32 vcc_lo, vcc_lo, s88\n" \
  "v_readlane_b32 s88, v52, 16\n" \
  "s_max_i32 vcc_lo, vcc_lo, s88\n" \
  "v_readlane_b32 s88, v52, 32\n" \
  "s_max_i32 vcc_lo, vcc_lo, s88\n" \
  "v_readlane_b32 s88, v52, 48\n" \
  "s_max_i32 vcc_lo, vcc_lo, s88\n" \
  "s_add_u32 s66, s66, 1024\n" \
  "s_add_u32 vcc_lo, vcc_lo, s39\n" \
  "s_add_u32 vcc_lo, vcc_lo, 23\n" \
  ".LCA_TW_%=:\n" \
  "s_memrealtime s[38:39]\n" \
  "s_waitcnt lgkmcnt(0)\n" \
  "s_sub_u32 s38, s38, vcc_lo\n" \
  "s_cmp_lt_i32 s38, 0\n" \
  "s_cbranch_scc1 .LCA_TW_%=\n" \
  "buffer_load_dwordx4 v[24:27], v44, s[40:43], s89 offen sc1\n" \
  "buffer_load_dwordx4 v[28:31], v44, s[40:43], s89 offen offset:2048 sc1\n" \
  ".LCA_SEE_%=:\n" \
  "s_waitcnt vmcnt(1)\n" \
  "v_cvt_f32_i32 v24, v24\n" \
  "v_cvt_f32_i32 v25, v25\n" \
  "v_cvt_f32_i32 v26, v26\n" \
  "v_cvt_f32_i32 v27, v27\n" \
  "v_max_f32 v45, |v24|, |v26|\n" \
  "v_pk_mul_f32 v[24:25], v[24:25], s[80:81] op_sel_hi:[1,0]\n" \
  "v_pk_mul_f32 v[26:27], v[26:27], s[80:81] op_sel_hi:[1,0]\n" \
  "s_nop 0\n" \
  "v_cvt_pk_bf16_f32 v32, v24, v26\n" \
  "v_cvt_pk_bf16_f32 v33, v25, v27\n" \
  "ds_write_b32 v40, v32\n" \
  "ds_write_b32 v41, v33\n" \
  "s_waitcnt vmcnt(0)\n" \
  "v_cvt_f32_i32 v28, v28\n" \
  "v_cvt_f32_i32 v30, v30\n" \
  "v_cvt_f32_i32 v29, v29\n" \
  "v_max3_f32 v45, v45, |v28|, |v30|\n" \
  "v_cvt_f32_i32 v31, v31\n" \
  "v_cmp_lt_f32 vcc, s91, v45\n" \
  "v_pk_mul_f32 v[28:29], v[28:29], s[80:81] op_sel_hi:[1,0]\n" \
  "v_pk_mul_f32 v[30:31], v[30:31], s[80:81] op_sel_hi:[1,0]\n" \
  "s_cmp_eq_u64 vcc, 0\n" \
  "s_cbranch_scc0 .LCA_POLL_%=\n" \
  "v_cvt_pk_bf16_f32 v36, v28, v30\n" \
  "v_cvt_pk_bf16_f32 v37, v29, v31\n" \
  "ds_write_b32 v42, v36\n" \
  "ds_write_b32 v43, v37\n" \
  "v_dot2c_f32_bf16 v11, v32, v32\n" \
  "v_dot2c_f32_bf16 v11, v33, v33\n" \
  "v_dot2c_f32_bf16 v11, v36, v36\n" \
  "v_dot2c_f32_bf16 v11, v37, v37\n" \
  "s_nop 1\n" \
  "s_nop 0\n" \
  "v_cmp_lt_f32 vcc, 4.0, v11\n" \
  "s_cbranch_vccz .LCA_TOP_%=\n" \
  "s_mov_b64 exec, vcc\n" \
  "ds_max_u32 v59, v11\n" \
  "s_mov_b64 exec, -1\n" \
  "s_branch .LCA_TOP_%=\n" \
  ".LCA_POLL_%=:\n" \
  "buffer_load_dwordx4 v[24:27], v44, s[40:43], s89 offen sc1\n" \
  "buffer_load_dwordx4 v[28:31], v44, s[40:43], s89 offen offset:2048 sc1\n" \
  "s_sub_u32 s87, s87, 1\n" \
  "s_cbranch_scc1 .LCA_END_%=\n" \
  "s_branch .LCA_SEE_%=\n" \
  ".LCA_LAST_%=:\n" \
  ".LCA_END_%=:\n" \
  "s_endpgm\n"
#define CHAIN_ASM_VO \
  "s_mov_b32 s95, 272\n" \
  "s_mov_b32 s91, 0x4c7c2000\n" \
  "s_mov_b32 s85, 0xc9b26c9b\n" \
  "s_mov_b32 s92, 0xc9326c9b\n" \
  "s_mov_b32 s93, 0xc9326c9b\n" \
  "s_mov_b32 s78, 0x3e0293ee\n" \
  "s_mov_b32 s76, 0x33333333\n" \
  "s_mov_b32 s77, 0x33333333\n" \
  "s_lshl_b32 s98, s73, 10\n" \
  "s_add_u32 s98, s98, s74\n" \
  "s_add_u32 s98, s98, 131072\n" \
  "s_lshl_b32 s97, s73, 9\n" \
  "s_add_u32 s97, s97, s74\n" \
  "s_add_u32 s97, s97, 158724\n" \
  "s_lshl_b32 s99, s73, 11\n" \
  "s_add_u32 s99, s99, s74\n" \
  "s_add_u32 s99, s99, 133120\n" \
  "s_lshl_b32 s94, s73, 4\n" \
  "s_add_u32 s94, s94, s74\n" \
  "s_add_u32 s94, s94, 157760\n" \
  "v_mbcnt_lo_u32_b32 v48, -1, 0\n" \
  "v_mbcnt_hi_u32_b32 v48, -1, v48\n" \
  "s_add_u32 s88, s74, 157760\n" \
  "v_mov_b32 v59, s88\n" \
  "s_sub_u32 s88, s73, 4\n" \
  "v_lshrrev_b32 v49, 2, v48\n" \
  "v_lshrrev_b32 v50, 4, v48\n" \
  "v_bitop3_b32 v50, v49, v50, 3 bitop3:0x28\n" \
  "v_lshl_add_u32 v4, v50, 3, s97\n" \
  "v_and_b32 v50, 3, v48\n" \
  "v_lshl_add_u32 v4, v50, 5, v4\n" \
  "v_and_b32 v49, 1, v49\n" \
  "v_lshl_add_u32 v4, v49, 8, v4\n" \
  "v_and_b32 v49, 8, v48\n" \
  "v_lshl_add_u32 v4, v49, 4, v4\n" \
  "v_and_b32 v51, 15, v48\n" \
  "v_bfe_u32 v49, v48, 4, 1\n" \
  "v_lshrrev_b32 v50, 5, v48\n" \
  "s_lshl_b32 s89, s88, 10\n" \
  "s_add_u32 s89, s89, s74\n" \
  "s_add_u32 s89, s89, 158720\n" \
  "v_lshrrev_b32 v9, 3, v51\n" \
  "v_mul_u32_u24 v6, 41, v9\n" \
  "v_xor_b32 v6, v6, v51\n" \
  "v_lshl_add_u32 v6, v6, 3, s89\n" \
  "v_lshl_add_u32 v6, v49, 6, v6\n" \
  "v_lshl_add_u32 v6, v50, 9, v6\n" \
  "v_xor_b32 v9, 0x90, v6\n" \
  "v_lshlrev_b32 v8, 4, v49\n" \
  "v_lshl_add_u32 v8, v51, 5, v8\n" \
  "v_lshl_add_u32 v8, v50, 3, v8\n" \
  "s_lshl_b32 s89, s88, 8\n" \
  "s_add_u32 s89, s89, s74\n" \
  "s_add_u32 s89, s89, 139264\n" \
  "v_and_b32 v49, 31, v48\n" \
  "v_lshl_add_u32 v5, v49, 3, s89\n" \
  "v_lshl_add_u32 v5, v50, 10, v5\n" \
  "v_lshrrev_b32 v49, 2, v51\n" \
  "v_and_b32 v50, 5, v51\n" \
  "v_and_or_b32 v49, v49, 2, v50\n" \
  "v_and_b32 v50, 48, v48\n" \
  "v_lshlrev_b32 v7, 3, v50\n" \
  "v_lshl_add_u32 v7, v49, 4, v7\n" \
  "v_bitop3_b32 v49, v51, 2, 2 bitop3:0x0c\n" \
  "v_lshl_add_u32 v7, v49, 29, v7\n" \
  "v_mov_b64 v[12:13], 0\n" \
  "v_mov_b64 v[14:15], 0\n" \
  "v_mov_b64 v[16:17], 0\n" \
  "v_mov_b64 v[18:19], 0\n" \
  "v_mov_b64 v[20:21], 0\n" \
  "v_mov_b64 v[22:23], 0\n" \
  "v_lshlrev_b32 v10, 4, v48\n" \
  "s_add_u32 s78, s74, 157952\n" \
  "v_lshl_add_u32 v52, v48, 2, v59\n" \
  "ds_write_b32 v52, v12 offset:192\n" \
  "s_movk_i32 s79, 105\n" \
  "s_movk_i32 s96, 25\n" \
  "s_mov_b32 s87, 0x40000\n" \
  "v_subrev_u32 v30, 64, v59\n" \
  "v_add_u32 v17, 0x10000, v28\n" \
  "v_xor_b32 v18, 128, v28\n" \
  "v_xor_b32 v19, 128, v17\n" \
  "ds_write_b32 v30, v12 offset:0\n" \
  "ds_write_b32 v30, v12 offset:32\n" \
  "ds_write_b32 v30, v12 offset:64\n" \
  "v_xor_b32 v20, 0, v29\n" \
  "v_xor_b32 v21, 32, v29\n" \
  "v_xor_b32 v22, 64, v29\n" \
  "v_xor_b32 v23, 96, v29\n" \
  "s_waitcnt vmcnt(12) lgkmcnt(0)\n" \
  "s_barrier\n" \
  "ds_read_b64_tr_b16 v[96:97], v20 offset:0\n" \
  "ds_read_b64_tr_b16 v[98:99], v20 offset:2048\n" \
  "ds_read_b64_tr_b16 v[104:105], v21 offset:0\n" \
  "ds_read_b64_tr_b16 v[106:107], v21 offset:2048\n" \
  "ds_read_b64_tr_b16 v[112:113], v22 offset:0\n" \
  "ds_read_b64_tr_b16 v[114:115], v22 offset:2048\n" \
  "ds_read_b64_tr_b16 v[120:121], v23 offset:0\n" \
  "ds_read_b64_tr_b16 v[122:123], v23 offset:2048\n" \
  "s_lshl_b32 s88, s73, 10\n" \
  "v_add_u32 v48, s88, v1\n" \
  "ds_read_b128 v[52:55], v48 offset:0\n" \
  "s_waitcnt lgkmcnt(0)\n" \
  "v_mov_b32 v11, 0\n" \
  "v_dot2c_f32_bf16 v11, v52, v52\n" \
  "v_dot2c_f32_bf16 v11, v53, v53\n" \
  "v_dot2c_f32_bf16 v11, v54, v54\n" \
  "v_dot2c_f32_bf16 v11, v55, v55\n" \
  "s_nop 0\n" \
  "s_nop 1\n" \
  "v_max_u32_dpp v11, v11, v11 row_ror:1 row_mask:0xf bank_mask:0xf\n" \
  "s_nop 1\n" \
  "v_max_u32_dpp v11, v11, v11 row_ror:2 row_mask:0xf bank_mask:0xf\n" \
  "s_nop 1\n" \
  "v_max_u32_dpp v11, v11, v11 row_ror:4 row_mask:0xf bank_mask:0xf\n" \
  "s_nop 1\n" \
  "v_max_u32_dpp v11, v11, v11 row_ror:8 row_mask:0xf bank_mask:0xf\n" \
  "v_mov_b32 v31, v11\n" \
  "s_nop 1\n" \
  "v_permlane16_swap_b32 v11, v31\n" \
  "v_max_u32 v11, v11, v31\n" \
  "v_mov_b32 v31, v11\n" \
  "s_nop 1\n" \
  "v_permlane32_swap_b32 v11, v31\n" \
  "v_max_u32 v11, v11, v31\n" \
  "s_mov_b64 exec, 1\n" \
  "ds_max_u32 v59, v11\n" \
  "s_mov_b64 exec, -1\n" \
  "s_waitcnt vmcnt(8) lgkmcnt(0)\n" \
  "s_barrier\n" \
  "ds_read_b64_tr_b16 v[100:101], v20 offset:4096\n" \
  "ds_read_b64_tr_b16 v[102:103], v20 offset:6144\n" \
  "ds_read_b64_tr_b16 v[108:109], v21 offset:4096\n" \
  "ds_read_b64_tr_b16 v[110:111], v21 offset:6144\n" \
  "ds_read_b64_tr_b16 v[116:117], v22 offset:4096\n" \
  "ds_read_b64_tr_b16 v[118:119], v22 offset:6144\n" \
  "ds_read_b64_tr_b16 v[124:125], v23 offset:4096\n" \
  "ds_read_b64_tr_b16 v[126:127], v23 offset:6144\n" \
  "s_add_u32 m0, s19, 0\n" \
  "s_nop 0\n" \
  "global_load_lds_dwordx4 v24, s[4:5] offset:0\n" \
  "global_load_lds_dwordx4 v25, s[4:5] offset:1024\n" \
  "global_load_lds_dwordx4 v26, s[4:5] offset:2048\n" \
  "global_load_lds_dwordx4 v27, s[4:5] offset:3072\n" \
  "s_add_u32 s4, s4, 0x100000\n" \
  "s_addc_u32 s5, s5, 0\n" \
  "v_mfma_f32_16x16x32_bf16 v[64:67], v[96:99], v[96:99], 0\n" \
  "v_mfma_f32_16x16x32_bf16 v[68:71], v[104:107], v[104:107], 0\n" \
  "v_mfma_f32_16x16x32_bf16 v[72:75], v[112:115], v[112:115], 0\n" \
  "v_mfma_f32_16x16x32_bf16 v[76:79], v[120:123], v[120:123], 0\n" \
  "s_waitcnt vmcnt(8) lgkmcnt(0)\n" \
  "s_barrier\n" \
  "ds_read_b64_tr_b16 v[32:33], v20 offset:8192\n" \
  "ds_read_b64_tr_b16 v[34:35], v20 offset:10240\n" \
  "ds_read_b64_tr_b16 v[40:41], v21 offset:8192\n" \
  "ds_read_b64_tr_b16 v[42:43], v21 offset:10240\n" \
  "ds_read_b64_tr_b16 v[48:49], v22 offset:8192\n" \
  "ds_read_b64_tr_b16 v[50:51], v22 offset:10240\n" \
  "ds_read_b64_tr_b16 v[56:57], v23 offset:8192\n" \
  "ds_read_b64_tr_b16 v[58:59], v23 offset:10240\n" \
  "s_add_u32 m0, s19, 4096\n" \
  "s_nop 0\n" \
  "global_load_lds_dwordx4 v24, s[4:5] offset:0\n" \
  "global_load_lds_dwordx4 v25, s[4:5] offset:1024\n" \
  "global_load_lds_dwordx4 v26, s[4:5] offset:2048\n" \
  "global_load_lds_dwordx4 v27, s[4:5] offset:3072\n" \
  "s_add_u32 s4, s4, 0x100000\n" \
  "s_addc_u32 s5, s5, 0\n" \
  "v_mfma_f32_16x16x32_bf16 v[64:67], v[100:103], v[100:103], v[64:67]\n" \
  "v_mfma_f32_16x16x32_bf16 v[68:71], v[108:111], v[108:111], v[68:71]\n" \
  "v_mfma_f32_16x16x32_bf16 v[72:75], v[116:119], v[116:119], v[72:75]\n" \
  "v_mfma_f32_16x16x32_bf16 v[76:79], v[124:127], v[124:127], v[76:79]\n" \
  "s_waitcnt vmcnt(8) lgkmcnt(0)\n" \
  "s_barrier\n" \
  "ds_read_b64_tr_b16 v[36:37], v20 offset:12288\n" \
  "ds_read_b64_tr_b16 v[38:39], v20 offset:14336\n" \
  "ds_read_b64_tr_b16 v[44:45], v21 offset:12288\n" \
  "ds_read_b64_tr_b16 v[46:47], v21 offset:14336\n" \
  "ds_read_b64_tr_b16 v[52:53], v22 offset:12288\n" \
  "ds_read_b64_tr_b16 v[54:55], v22 offset:14336\n" \
  "ds_read_b64_tr_b16 v[60:61], v23 offset:12288\n" \
  "ds_read_b64_tr_b16 v[62:63], v23 offset:14336\n" \
  "s_add_u32 m0, s19, 8192\n" \
  "s_nop 0\n" \
  "global_load_lds_dwordx4 v24, s[4:5] offset:0\n" \
  "global_load_lds_dwordx4 v25, s[4:5] offset:1024\n" \
  "global_load_lds_dwordx4 v26, s[4:5] offset:2048\n" \
  "global_load_lds_dwordx4 v27, s[4:5] offset:3072\n" \
  "s_add_u32 s4, s4, 0x100000\n" \
  "s_addc_u32 s5, s5, 0\n" \
  "v_mfma_f32_16x16x32_bf16 v[64:67], v[32:35], v[32:35], v[64:67]\n" \
  "v_mfma_f32_16x16x32_bf16 v[68:71], v[40:43], v[40:43], v[68:71]\n" \
  "v_mfma_f32_16x16x32_bf16 v[72:75], v[48:51], v[48:51], v[72:75]\n" \
  "v_mfma_f32_16x16x32_bf16 v[76:79], v[56:59], v[56:59], v[76:79]\n" \
  "s_waitcnt vmcnt(8) lgkmcnt(0)\n" \
  "s_barrier\n" \
  "ds_read_b64_tr_b16 v[128:129], v18 offset:0\n" \
  "ds_read_b64_tr_b16 v[130:131], v18 offset:512\n" \
  "ds_read_b64_tr_b16 v[132:133], v18 offset:32768\n" \
  "ds_read_b64_tr_b16 v[134:135], v18 offset:33280\n" \
  "ds_read_b64_tr_b16 v[136:137], v19 offset:0\n" \
  "ds_read_b64_tr_b16 v[138:139], v19 offset:512\n" \
  "ds_read_b64_tr_b16 v[140:141], v19 offset:32768\n" \
  "ds_read_b64_tr_b16 v[142:143], v19 offset:33280\n" \
  "ds_read_b64_tr_b16 v[192:193], v28 offset:0\n" \
  "ds_read_b64_tr_b16 v[194:195], v28 offset:512\n" \
  "ds_read_b64_tr_b16 v[196:197], v28 offset:32768\n" \
  "ds_read_b64_tr_b16 v[198:199], v28 offset:33280\n" \
  "ds_read_b64_tr_b16 v[200:201], v17 offset:0\n" \
  "ds_read_b64_tr_b16 v[202:203], v17 offset:512\n" \
  "ds_read_b64_tr_b16 v[204:205], v17 offset:32768\n" \
  "ds_read_b64_tr_b16 v[206:207], v17 offset:33280\n" \
  "s_add_u32 m0, s19, 12288\n" \
  "s_nop 0\n" \
  "global_load_lds_dwordx4 v24, s[4:5] offset:0\n" \
  "global_load_lds_dwordx4 v25, s[4:5] offset:1024\n" \
  "global_load_lds_dwordx4 v26, s[4:5] offset:2048\n" \
  "global_load_lds_dwordx4 v27, s[4:5] offset:3072\n" \
  "s_add_u32 s4, s4, 0x100000\n" \
  "s_addc_u32 s5, s5, 0\n" \
  "v_mfma_f32_16x16x32_bf16 v[64:67], v[36:39], v[36:39], v[64:67]\n" \
  "v_mfma_f32_16x16x32_bf16 v[68:71], v[44:47], v[44:47], v[68:71]\n" \
  "v_mfma_f32_16x16x32_bf16 v[72:75], v[52:55], v[52:55], v[72:75]\n" \
  "v_mfma_f32_16x16x32_bf16 v[76:79], v[60:63], v[60:63], v[76:79]\n" \
  "s_nop 7\n" \
  "v_max3_f32 v13, v64, v65, v66\n" \
  "v_max3_f32 v13, v13, v67, v68\n" \
  "v_max3_f32 v13, v13, v69, v70\n" \
  "v_max3_f32 v13, v13, v71, v72\n" \
  "v_max3_f32 v13, v13, v73, v74\n" \
  "v_max3_f32 v13, v13, v75, v76\n" \
  "v_max3_f32 v13, v13, v77, v78\n" \
  "v_max_f32 v13, v13, v79\n" \
  "s_nop 1\n" \
  "v_max_f32_dpp v13, v13, v13 row_ror:1 row_mask:0xf bank_mask:0xf\n" \
  "s_nop 1\n" \
  "v_max_f32_dpp v13, v13, v13 row_ror:2 row_mask:0xf bank_mask:0xf\n" \
  "s_nop 1\n" \
  "v_max_f32_dpp v13, v13, v13 row_ror:4 row_mask:0xf bank_mask:0xf\n" \
  "s_nop 1\n" \
  "v_max_f32_dpp v13, v13, v13 row_ror:8 row_mask:0xf bank_mask:0xf\n" \
  "v_mov_b32 v31, v13\n" \
  "s_nop 1\n" \
  "v_permlane16_swap_b32 v13, v31\n" \
  "v_max_f32 v13, v13, v31\n" \
  "v_mov_b32 v31, v13\n" \
  "s_nop 1\n" \
  "v_permlane32_swap_b32 v13, v31\n" \
  "v_max_f32 v13, v13, v31\n" \
  "s_mov_b64 exec, 1\n" \
  "ds_max_u32 v30, v13 offset:32\n" \
  "s_mov_b64 exec, -1\n" \
  "s_waitcnt vmcnt(8) lgkmcnt(0)\n" \
  "s_barrier\n" \
  "ds_read_b64_tr_b16 v[144:145], v18 offset:4096\n" \
  "ds_read_b64_tr_b16 v[146:147], v18 offset:4608\n" \
  "ds_read_b64_tr_b16 v[148:149], v18 offset:36864\n" \
  "ds_read_b64_tr_b16 v[150:151], v18 offset:37376\n" \
  "ds_read_b64_tr_b16 v[152:153], v19 offset:4096\n" \
  "ds_read_b64_tr_b16 v[154:155], v19 offset:4608\n" \
  "ds_read_b64_tr_b16 v[156:157], v19 offset:36864\n" \
  "ds_read_b64_tr_b16 v[158:159], v19 offset:37376\n" \
  "ds_read_b64_tr_b16 v[208:209], v28 offset:4096\n" \
  "ds_read_b64_tr_b16 v[210:211], v28 offset:4608\n" \
  "ds_read_b64_tr_b16 v[212:213], v28 offset:36864\n" \
  "ds_read_b64_tr_b16 v[214:215], v28 offset:37376\n" \
  "ds_read_b64_tr_b16 v[216:217], v17 offset:4096\n" \
  "ds_read_b64_tr_b16 v[218:219], v17 offset:4608\n" \
  "ds_read_b64_tr_b16 v[220:221], v17 offset:36864\n" \
  "ds_read_b64_tr_b16 v[222:223], v17 offset:37376\n" \
  "s_add_u32 m0, s19, 0\n" \
  "s_nop 0\n" \
  "global_load_lds_dwordx4 v24, s[6:7] offset:0\n" \
  "global_load_lds_dwordx4 v25, s[6:7] offset:1024\n" \
  "global_load_lds_dwordx4 v26, s[6:7] offset:2048\n" \
  "global_load_lds_dwordx4 v27, s[6:7] offset:3072\n" \
  "s_add_u32 s6, s6, 0x100000\n" \
  "s_addc_u32 s7, s7, 0\n" \
  "v_mfma_f32_16x16x32_bf16 v[64:67], v[128:131], v[128:131], 0\n" \
  "v_mfma_f32_16x16x32_bf16 v[68:71], v[192:195], v[192:195], 0\n" \
  "v_mfma_f32_16x16x32_bf16 v[64:67], v[132:135], v[132:135], v[64:67]\n" \
  "v_mfma_f32_16x16x32_bf16 v[68:71], v[196:199], v[196:199], v[68:71]\n" \
  "v_mfma_f32_16x16x32_bf16 v[64:67], v[136:139], v[136:139], v[64:67]\n" \
  "v_mfma_f32_16x16x32_bf16 v[68:71], v[200:203], v[200:203], v[68:71]\n" \
  "v_mfma_f32_16x16x32_bf16 v[64:67], v[140:143], v[140:143], v[64:67]\n" \
  "v_mfma_f32_16x16x32_bf16 v[68:71], v[204:207], v[204:207], v[68:71]\n" \
  "s_waitcnt vmcnt(8) lgkmcnt(0)\n" \
  "s_barrier\n" \
  "ds_read_b64_tr_b16 v[160:161], v18 offset:8192\n" \
  "ds_read_b64_tr_b16 v[162:163], v18 offset:8704\n" \
  "ds_read_b64_tr_b16 v[164:165], v18 offset:40960\n" \
  "ds_read_b64_tr_b16 v[166:167], v18 offset:41472\n" \
  "ds_read_b64_tr_b16 v[168:169], v19 offset:8192\n" \
  "ds_read_b64_tr_b16 v[170:171], v19 offset:8704\n" \
  "ds_read_b64_tr_b16 v[172:173], v19 offset:40960\n" \
  "ds_read_b64_tr_b16 v[174:175], v19 offset:41472\n" \
  "ds_read_b64_tr_b16 v[224:225], v28 offset:8192\n" \
  "ds_read_b64_tr_b16 v[226:227], v28 offset:8704\n" \
  "ds_read_b64_tr_b16 v[228:229], v28 offset:40960\n" \
  "ds_read_b64_tr_b16 v[230:231], v28 offset:41472\n" \
  "ds_read_b64_tr_b16 v[232:233], v17 offset:8192\n" \
  "ds_read_b64_tr_b16 v[234:235], v17 offset:8704\n" \
  "ds_read_b64_tr_b16 v[236:237], v17 offset:40960\n" \
  "ds_read_b64_tr_b16 v[238:239], v17 offset:41472\n" \
  "s_add_u32 m0, s19, 4096\n" \
  "s_nop 0\n" \
  "global_load_lds_dwordx4 v24, s[6:7] offset:0\n" \
  "global_load_lds_dwordx4 v25, s[6:7] offset:1024\n" \
  "global_load_lds_dwordx4 v26, s[6:7] offset:2048\n" \
  "global_load_lds_dwordx4 v27, s[6:7] offset:3072\n" \
  "s_add_u32 s6, s6, 0x100000\n" \
  "s_addc_u32 s7, s7, 0\n" \
  "v_mfma_f32_16x16x32_bf16 v[64:67], v[144:147], v[144:147], v[64:67]\n" \
  "v_mfma_f32_16x16x32_bf16 v[68:71], v[208:211], v[208:211], v[68:71]\n" \
  "v_mfma_f32_16x16x32_bf16 v[64:67], v[148:151], v[148:151], v[64:67]\n" \
  "v_mfma_f32_16x16x32_bf16 v[68:71], v[212:215], v[212:215], v[68:71]\n" \
  "v_mfma_f32_16x16x32_bf16 v[64:67], v[152:155], v[152:155], v[64:67]\n" \
  "v_mfma_f32_16x16x32_bf16 v[68:71], v[216:219], v[216:219], v[68:71]\n" \
  "v_mfma_f32_16x16x32_bf16 v[64:67], v[156:159], v[156:159], v[64:67]\n" \
  "v_mfma_f32_16x16x32_bf16 v[68:71], v[220:223], v[220:223], v[68:71]\n" \
  "s_waitcnt vmcnt(8) lgkmcnt(0)\n" \
  "s_barrier\n" \
  "ds_read_b64_tr_b16 v[176:177], v18 offset:12288\n" \
  "ds_read_b64_tr_b16 v[178:179], v18 offset:12800\n" \
  "ds_read_b64_tr_b16 v[180:181], v18 offset:45056\n" \
  "ds_read_b64_tr_b16 v[182:183], v18 offset:45568\n" \
  "ds_read_b64_tr_b16 v[184:185], v19 offset:12288\n" \
  "ds_read_b64_tr_b16 v[186:187], v19 offset:12800\n" \
  "ds_read_b64_tr_b16 v[188:189], v19 offset:45056\n" \
  "ds_read_b64_tr_b16 v[190:191], v19 offset:45568\n" \
  "ds_read_b64_tr_b16 v[240:241], v28 offset:12288\n" \
  "ds_read_b64_tr_b16 v[242:243], v28 offset:12800\n" \
  "ds_read_b64_tr_b16 v[244:245], v28 offset:45056\n" \
  "ds_read_b64_tr_b16 v[246:247], v28 offset:45568\n" \
  "ds_read_b64_tr_b16 v[248:249], v17 offset:12288\n" \
  "ds_read_b64_tr_b16 v[250:251], v17 offset:12800\n" \
  "ds_read_b64_tr_b16 v[252:253], v17 offset:45056\n" \
  "ds_read_b64_tr_b16 v[254:255], v17 offset:45568\n" \
  "s_add_u32 m0, s19, 8192\n" \
  "s_nop 0\n" \
  "global_load_lds_dwordx4 v24, s[6:7] offset:0\n" \
  "global_load_lds_dwordx4 v25, s[6:7] offset:1024\n" \
  "global_load_lds_dwordx4 v26, s[6:7] offset:2048\n" \
  "global_load_lds_dwordx4 v27, s[6:7] offset:3072\n" \
  "s_add_u32 s6, s6, 0x100000\n" \
  "s_addc_u32 s7, s7, 0\n" \
  "v_mfma_f32_16x16x32_bf16 v[64:67], v[160:163], v[160:163], v[64:67]\n" \
  "v_mfma_f32_16x16x32_bf16 v[68:71], v[224:227], v[224:227], v[68:71]\n" \
  "v_mfma_f32_16x16x32_bf16 v[64:67], v[164:167], v[164:167], v[64:67]\n" \
  "v_mfma_f32_16x16x32_bf16 v[68:71], v[228:231], v[228:231], v[68:71]\n" \
  "v_mfma_f32_16x16x32_bf16 v[64:67], v[168:171], v[168:171], v[64:67]\n" \
  "v_mfma_f32_16x16x32_bf16 v[68:71], v[232:235], v[232:235], v[68:71]\n" \
  "v_mfma_f32_16x16x32_bf16 v[64:67], v[172:175], v[172:175], v[64:67]\n" \
  "v_mfma_f32_16x16x32_bf16 v[68:71], v[236:239], v[236:239], v[68:71]\n" \
  "s_waitcnt vmcnt(8) lgkmcnt(0)\n" \
  "s_barrier\n" \
  "s_add_u32 m0, s19, 12288\n" \
  "s_nop 0\n" \
  "global_load_lds_dwordx4 v24, s[6:7] offset:0\n" \
  "global_load_lds_dwordx4 v25, s[6:7] offset:1024\n" \
  "global_load_lds_dwordx4 v26, s[6:7] offset:2048\n" \
  "global_load_lds_dwordx4 v27, s[6:7] offset:3072\n" \
  "s_add_u32 s6, s6, 0x100000\n" \
  "s_addc_u32 s7, s7, 0\n" \
  "v_mfma_f32_16x16x32_bf16 v[64:67], v[176:179], v[176:179], v[64:67]\n" \
  "v_mfma_f32_16x16x32_bf16 v[68:71], v[240:243], v[240:243], v[68:71]\n" \
  "v_mfma_f32_16x16x32_bf16 v[64:67], v[180:183], v[180:183], v[64:67]\n" \
  "v_mfma_f32_16x16x32_bf16 v[68:71], v[244:247], v[244:247], v[68:71]\n" \
  "v_mfma_f32_16x16x32_bf16 v[64:67], v[184:187], v[184:187], v[64:67]\n" \
  "v_mfma_f32_16x16x32_bf16 v[68:71], v[248:251], v[248:251], v[68:71]\n" \
  "v_mfma_f32_16x16x32_bf16 v[64:67], v[188:191], v[188:191], v[64:67]\n" \
  "v_mfma_f32_16x16x32_bf16 v[68:71], v[252:255], v[252:255], v[68:71]\n" \
  "s_nop 7\n" \
  "s_mov_b32 exec_lo, 0x100001\n" \
  "s_mov_b32 exec_hi, 0x10000100\n" \
  "v_add_f32 v12, v64, v68\n" \
  "s_mov_b32 exec_lo, 0x200002\n" \
  "s_mov_b32 exec_hi, 0x20000200\n" \
  "v_add_f32 v12, v65, v69\n" \
  "s_mov_b32 exec_lo, 0x400004\n" \
  "s_mov_b32 exec_hi, 0x40000400\n" \
  "v_add_f32 v12, v66, v70\n" \
  "s_mov_b32 exec_lo, 0x800008\n" \
  "s_mov_b32 exec_hi, 0x80000800\n" \
  "v_add_f32 v12, v67, v71\n" \
  "s_mov_b64 exec, -1\n" \
  "s_nop 1\n" \
  "v_add_f32_dpp v12, v12, v12 row_ror:1 row_mask:0xf bank_mask:0xf\n" \
  "s_nop 1\n" \
  "v_add_f32_dpp v12, v12, v12 row_ror:2 row_mask:0xf bank_mask:0xf\n" \
  "s_nop 1\n" \
  "v_add_f32_dpp v12, v12, v12 row_ror:4 row_mask:0xf bank_mask:0xf\n" \
  "s_nop 1\n" \
  "v_add_f32_dpp v12, v12, v12 row_ror:8 row_mask:0xf bank_mask:0xf\n" \
  "v_mov_b32 v31, v12\n" \
  "s_nop 1\n" \
  "v_permlane16_swap_b32 v12, v31\n" \
  "v_add_f32 v12, v12, v31\n" \
  "v_mov_b32 v31, v12\n" \
  "s_nop 1\n" \
  "v_permlane32_swap_b32 v12, v31\n" \
  "v_add_f32 v12, v12, v31\n" \
  "v_lshl_add_u32 v31, s73, 2, v30\n" \
  "s_mov_b64 exec, 1\n" \
  "ds_write_b32 v31, v12\n" \
  "s_mov_b64 exec, -1\n" \
  "s_waitcnt vmcnt(8) lgkmcnt(0)\n" \
  "s_barrier\n" \
  "s_add_u32 m0, s19, 0\n" \
  "s_nop 0\n" \
  "global_load_lds_dwordx4 v24, s[8:9] offset:0\n" \
  "global_load_lds_dwordx4 v25, s[8:9] offset:1024\n" \
  "global_load_lds_dwordx4 v26, s[8:9] offset:2048\n" \
  "global_load_lds_dwordx4 v27, s[8:9] offset:3072\n" \
  "s_add_u32 s8, s8, 0x100000\n" \
  "s_addc_u32 s9, s9, 0\n" \
  "ds_read_b128 v[20:23], v30 offset:16\n" \
  "ds_read_b32 v31, v30 offset:32\n" \
  "v_mbcnt_lo_u32_b32 v16, -1, 0\n" \
  "v_mbcnt_hi_u32_b32 v16, -1, v16\n" \
  "v_lshlrev_b32 v15, 8, v16\n" \
  "s_waitcnt lgkmcnt(0)\n" \
  "v_add_f32 v20, v20, v21\n" \
  "v_add_f32 v22, v22, v23\n" \
  "v_add_f32 v20, v20, v22\n" \
  "v_mul_f32 v31, v20, v31\n" \
  "v_mul_f32 v31, 0x3f8147ae, v31\n" \
  "v_mov_b32 v12, s10\n" \
  "v_mov_b32 v13, 0xffff\n" \
  "v_bfi_b32 v13, v13, s11, v31\n" \
  "s_mov_b32 exec_hi, 0\n" \
  "s_mov_b32 exec_lo, s18\n" \
  "global_store_dwordx2 v15, v[12:13], s[16:17] sc1\n" \
  "s_mov_b64 exec, -1\n" \
  "v_lshlrev_b32 v16, 3, v16\n" \
  "s_waitcnt vmcnt(8) lgkmcnt(0)\n" \
  "s_barrier\n" \
  "s_add_u32 m0, s19, 4096\n" \
  "s_nop 0\n" \
  "global_load_lds_dwordx4 v24, s[8:9] offset:0\n" \
  "global_load_lds_dwordx4 v25, s[8:9] offset:1024\n" \
  "global_load_lds_dwordx4 v26, s[8:9] offset:2048\n" \
  "global_load_lds_dwordx4 v27, s[8:9] offset:3072\n" \
  "s_add_u32 s8, s8, 0x100000\n" \
  "s_addc_u32 s9, s9, 0\n" \
  "global_load_dwordx2 v[20:21], v16, s[12:13] sc1\n" \
  "s_waitcnt vmcnt(9) lgkmcnt(0)\n" \
  "s_barrier\n" \
  "s_add_u32 m0, s19, 8192\n" \
  "s_nop 0\n" \
  "global_load_lds_dwordx4 v24, s[8:9] offset:0\n" \
  "global_load_lds_dwordx4 v25, s[8:9] offset:1024\n" \
  "global_load_lds_dwordx4 v26, s[8:9] offset:2048\n" \
  "global_load_lds_dwordx4 v27, s[8:9] offset:3072\n" \
  "s_add_u32 s8, s8, 0x100000\n" \
  "s_addc_u32 s9, s9, 0\n" \
  "s_waitcnt vmcnt(9) lgkmcnt(0)\n" \
  "s_barrier\n" \
  "ds_read_b64_tr_b16 v[64:65], v28 offset:0\n" \
  "ds_read_b64_tr_b16 v[66:67], v28 offset:512\n" \
  "ds_read_b64_tr_b16 v[68:69], v28 offset:32768\n" \
  "ds_read_b64_tr_b16 v[70:71], v28 offset:33280\n" \
  "ds_read_b64_tr_b16 v[72:73], v17 offset:0\n" \
  "ds_read_b64_tr_b16 v[74:75], v17 offset:512\n" \
  "ds_read_b64_tr_b16 v[76:77], v17 offset:32768\n" \
  "ds_read_b64_tr_b16 v[78:79], v17 offset:33280\n" \
  "s_add_u32 m0, s19, 12288\n" \
  "s_nop 0\n" \
  "global_load_lds_dwordx4 v24, s[8:9] offset:0\n" \
  "global_load_lds_dwordx4 v25, s[8:9] offset:1024\n" \
  "global_load_lds_dwordx4 v26, s[8:9] offset:2048\n" \
  "global_load_lds_dwordx4 v27, s[8:9] offset:3072\n" \
  "s_add_u32 s8, s8, 0x100000\n" \
  "s_addc_u32 s9, s9, 0\n" \
  "s_waitcnt vmcnt(8)\n" \
  ".LCV_RD0_%=:\n" \
  "v_cmp_ne_u32 vcc, s10, v20\n" \
  "v_xor_b32 v31, s11, v21\n" \
  "v_cmp_ne_u32 s[22:23], 0, v31\n" \
  "s_or_b64 vcc, vcc, s[22:23]\n" \
  "s_cbranch_scc0 .LCV_RD1_%=\n" \
  "s_sleep 8\n" \
  "global_load_dwordx2 v[20:21], v16, s[12:13] sc1\n" \
  "s_sub_u32 s87, s87, 1\n" \
  "s_cbranch_scc1 .LCV_END_%=\n" \
  "s_waitcnt vmcnt(0)\n" \
  "s_branch .LCV_RD0_%=\n" \
  ".LCV_RD1_%=:\n" \
  "s_waitcnt vmcnt(8) lgkmcnt(0)\n" \
  "s_barrier\n" \
  "ds_write_b128 v3, v[96:99] offset:0\n" \
  "ds_write_b128 v3, v[100:103] offset:1024\n" \
  "ds_write_b128 v3, v[104:107] offset:2048\n" \
  "ds_write_b128 v3, v[108:111] offset:3072\n" \
  "ds_read_b64_tr_b16 v[80:81], v28 offset:4096\n" \
  "ds_read_b64_tr_b16 v[82:83], v28 offset:4608\n" \
  "ds_read_b64_tr_b16 v[84:85], v28 offset:36864\n" \
  "ds_read_b64_tr_b16 v[86:87], v28 offset:37376\n" \
  "ds_read_b64_tr_b16 v[88:89], v17 offset:4096\n" \
  "ds_read_b64_tr_b16 v[90:91], v17 offset:4608\n" \
  "ds_read_b64_tr_b16 v[92:93], v17 offset:36864\n" \
  "ds_read_b64_tr_b16 v[94:95], v17 offset:37376\n" \
  "v_and_b32 v16, 0xff, v16\n" \
  "global_load_dwordx2 v[22:23], v16, s[14:15] sc1\n" \
  "s_mov_b64 exec, 15\n" \
  "buffer_load_dword v13, v2, s[44:47], s68 offen sc1\n" \
  "s_mov_b64 exec, -1\n" \
  "s_waitcnt vmcnt(6) lgkmcnt(0)\n" \
  "s_barrier\n" \
  "ds_write_b128 v3, v[112:115] offset:4096\n" \
  "ds_write_b128 v3, v[116:119] offset:5120\n" \
  "ds_write_b128 v3, v[120:123] offset:6144\n" \
  "ds_write_b128 v3, v[124:127] offset:7168\n" \
  "ds_read_b64_tr_b16 v[96:97], v28 offset:8192\n" \
  "ds_read_b64_tr_b16 v[98:99], v28 offset:8704\n" \
  "ds_read_b64_tr_b16 v[100:101], v28 offset:40960\n" \
  "ds_read_b64_tr_b16 v[102:103], v28 offset:41472\n" \
  "ds_read_b64_tr_b16 v[104:105], v17 offset:8192\n" \
  "ds_read_b64_tr_b16 v[106:107], v17 offset:8704\n" \
  "ds_read_b64_tr_b16 v[108:109], v17 offset:40960\n" \
  "ds_read_b64_tr_b16 v[110:111], v17 offset:41472\n" \
  "s_waitcnt vmcnt(0)\n" \
  "v_cmp_eq_u32 vcc, 0, v13\n" \
  "s_and_b32 s90, vcc_lo, 15\n" \
  "s_cbranch_scc1 .LCV_GL0_%=\n" \
  "s_mov_b32 m0, s99\n" \
  "s_add_u32 s88, s99, 8192\n" \
  "global_load_lds_dwordx4 v10, s[62:63] sc1\n" \
  "global_load_lds_dwordx4 v10, s[62:63] offset:1024 sc1\n" \
  "s_mov_b32 m0, s88\n" \
  "s_nop 0\n" \
  "global_load_lds_dwordx4 v10, s[64:65] sc1\n" \
  "global_load_lds_dwordx4 v10, s[64:65] offset:1024 sc1\n" \
  ".LCV_GL0_%=:\n" \
  "v_cmp_ne_u32 vcc, s10, v22\n" \
  "v_xor_b32 v31, s11, v23\n" \
  "v_and_b32 v31, 0xffff, v31\n" \
  "v_cmp_ne_u32 s[22:23], 0, v31\n" \
  "s_or_b64 vcc, vcc, s[22:23]\n" \
  "s_cbranch_scc0 .LCV_GL1_%=\n" \
  "s_sleep 12\n" \
  "global_load_dwordx2 v[22:23], v16, s[14:15] sc1\n" \
  "s_sub_u32 s87, s87, 1\n" \
  "s_cbranch_scc1 .LCV_END_%=\n" \
  "s_waitcnt vmcnt(0)\n" \
  "s_branch .LCV_GL0_%=\n" \
  ".LCV_GL1_%=:\n" \
  "v_and_b32 v31, 0xffff0000, v23\n" \
  "s_nop 1\n" \
  "v_max_u32_dpp v31, v31, v31 row_ror:1 row_mask:0xf bank_mask:0xf\n" \
  "s_nop 1\n" \
  "v_max_u32_dpp v31, v31, v31 row_ror:2 row_mask:0xf bank_mask:0xf\n" \
  "s_nop 1\n" \
  "v_max_u32_dpp v31, v31, v31 row_ror:4 row_mask:0xf bank_mask:0xf\n" \
  "s_nop 1\n" \
  "v_max_u32_dpp v31, v31, v31 row_ror:8 row_mask:0xf bank_mask:0xf\n" \
  "v_mov_b32 v12, v31\n" \
  "s_nop 1\n" \
  "v_permlane16_swap_b32 v31, v12\n" \
  "v_max_u32 v31, v31, v12\n" \
  "v_mov_b32 v12, v31\n" \
  "s_nop 1\n" \
  "v_permlane32_swap_b32 v31, v12\n" \
  "v_max_u32 v31, v31, v12\n" \
  "s_mov_b64 exec, 1\n" \
  "ds_write_b32 v30, v31\n" \
  "s_mov_b64 exec, -1\n" \
  "s_waitcnt vmcnt(4) lgkmcnt(0)\n" \
  "s_barrier\n" \
  "ds_write_b128 v3, v[32:35] offset:8192\n" \
  "ds_write_b128 v3, v[36:39] offset:9216\n" \
  "ds_write_b128 v3, v[40:43] offset:10240\n" \
  "ds_write_b128 v3, v[44:47] offset:11264\n" \
  "ds_read_b64_tr_b16 v[112:113], v28 offset:12288\n" \
  "ds_read_b64_tr_b16 v[114:115], v28 offset:12800\n" \
  "ds_read_b64_tr_b16 v[116:117], v28 offset:45056\n" \
  "ds_read_b64_tr_b16 v[118:119], v28 offset:45568\n" \
  "ds_read_b64_tr_b16 v[120:121], v17 offset:12288\n" \
  "ds_read_b64_tr_b16 v[122:123], v17 offset:12800\n" \
  "ds_read_b64_tr_b16 v[124:125], v17 offset:45056\n" \
  "ds_read_b64_tr_b16 v[126:127], v17 offset:45568\n" \
  "ds_read_b32 v31, v30 offset:0\n" \
  "s_waitcnt lgkmcnt(0)\n" \
  "s_barrier\n" \
  "ds_write_b128 v3, v[48:51] offset:12288\n" \
  "ds_write_b128 v3, v[52:55] offset:13312\n" \
  "ds_write_b128 v3, v[56:59] offset:14336\n" \
  "ds_write_b128 v3, v[60:63] offset:15360\n" \
  "v_mul_f32 v31, 0x3f8e353f, v31\n" \
  "v_mul_f32 v30, 0x44870000, v31\n" \
  "v_mov_b64 v[12:13], 0\n" \
  "v_mov_b64 v[14:15], 0\n" \
  "v_mov_b64 v[16:17], 0\n" \
  "v_mov_b64 v[18:19], 0\n" \
  "v_mov_b64 v[20:21], 0\n" \
  "v_mov_b64 v[22:23], 0\n" \
  "v_readfirstlane_b32 s72, v31\n" \
  "v_readfirstlane_b32 s86, v30\n" \
  "s_lshr_b32 s86, s86, 23\n" \
  "s_addk_i32 s86, 0xff83\n" \
  "s_ashr_i32 s86, s86, 1\n" \
  "s_max_i32 s86, s86, -20\n" \
  "s_min_i32 s86, s86, 100\n" \
  "s_mov_b32 s82, s86\n" \
  "s_lshr_b32 s83, s72, 23\n" \
  "s_sub_i32 s83, s83, 243\n" \
  "s_waitcnt lgkmcnt(0)\n" \
  "s_add_u32 s88, s74, 157760\n" \
  "v_mov_b32 v59, s88\n" \
  "s_cmp_lg_u32 s61, 0\n" \
  "s_movk_i32 s76, -6976\n" \
  "s_cmovk_i32 s76, 8128\n" \
  "s_movk_i32 s77, 90\n" \
  "s_cmovk_i32 s77, 90\n" \
  "s_cselect_b32 s72, -1, 56\n" \
  "s_cmp_eq_u32 s61, 0\n" \
  "s_cbranch_scc1 .LCV_W7_%=\n" \
  "s_movk_i32 s87, 100\n" \
  ".LCV_W5_%=:\n" \
  "buffer_load_dword v58, off, s[0:3], 0 offset:896 sc1\n" \
  "s_waitcnt vmcnt(0)\n" \
  "v_readfirstlane_b32 vcc_lo, v58\n" \
  ".LCV_W6_%=:\n" \
  "s_sleep 16\n" \
  "s_sub_u32 s87, s87, 1\n" \
  "s_cbranch_scc1 .LCV_W7_%=\n" \
  "s_cmp_eq_u32 vcc_lo, 0\n" \
  "s_cbranch_scc1 .LCV_W5_%=\n" \
  "s_memrealtime s[38:39]\n" \
  "s_waitcnt lgkmcnt(0)\n" \
  "s_sub_u32 s38, s38, vcc_lo\n" \
  "s_cmp_lt_i32 s38, 800\n" \
  "s_cbranch_scc1 .LCV_W6_%=\n" \
  ".LCV_W7_%=:\n" \
  "s_mov_b32 s87, 0x40000\n" \
  "s_cmp_eq_u32 s90, 0\n" \
  "s_cbranch_scc1 .LCV_W1_%=\n" \
  "s_branch .LCV_W2_%=\n" \
  ".LCV_W0_%=:\n" \
  "s_sleep 2\n" \
  "s_cmp_eq_u32 s61, 0\n" \
  "s_cbranch_scc1 .LCV_W2_%=\n" \
  "s_sleep 8\n" \
  ".LCV_W2_%=:\n" \
  "s_mov_b64 exec, 15\n" \
  "buffer_load_dword v58, v2, s[44:47], s68 offen sc1\n" \
  "s_mov_b64 exec, -1\n" \
  "s_sub_u32 s87, s87, 1\n" \
  "s_cbranch_scc1 .LCV_END_%=\n" \
  "s_waitcnt vmcnt(0)\n" \
  "v_cmp_eq_u32 vcc, 0, v58\n" \
  "s_and_b32 vcc_lo, vcc_lo, 15\n" \
  "s_cbranch_scc1 .LCV_W0_%=\n" \
  "s_mov_b32 m0, s99\n" \
  "s_add_u32 s88, s99, 8192\n" \
  "global_load_lds_dwordx4 v10, s[62:63] sc1\n" \
  "global_load_lds_dwordx4 v10, s[62:63] offset:1024 sc1\n" \
  "s_mov_b32 m0, s88\n" \
  "s_nop 0\n" \
  "global_load_lds_dwordx4 v10, s[64:65] sc1\n" \
  "global_load_lds_dwordx4 v10, s[64:65] offset:1024 sc1\n" \
  ".LCV_W1_%=:\n" \
  "s_add_u32 s62, s62, 0x80000\n" \
  "s_addc_u32 s63, s63, 0\n" \
  "s_add_u32 s64, s64, 0x40000\n" \
  "s_addc_u32 s65, s65, 0\n" \
  "s_add_u32 s68, s68, 0x1000\n" \
  "s_cmp_eq_u32 s61, 0\n" \
  "s_cbranch_scc1 .LCV_W4_%=\n" \
  "s_add_u32 s38, s67, 3968\n" \
  "v_mov_b32 v58, s38\n" \
  "buffer_load_dword v58, v58, s[0:3], 0 offen sc1\n" \
  "s_waitcnt vmcnt(0)\n" \
  "v_readfirstlane_b32 vcc_lo, v58\n" \
  "s_movk_i32 s87, 200\n" \
  "s_cmp_eq_u32 vcc_lo, 0\n" \
  "s_cbranch_scc1 .LCV_W4_%=\n" \
  "s_add_u32 vcc_lo, vcc_lo, 590\n" \
  ".LCV_W3_%=:\n" \
  "s_sleep 2\n" \
  "s_memrealtime s[38:39]\n" \
  "s_sub_u32 s87, s87, 1\n" \
  "s_cbranch_scc1 .LCV_W4_%=\n" \
  "s_waitcnt lgkmcnt(0)\n" \
  "s_sub_u32 s38, s38, vcc_lo\n" \
  "s_cmp_lt_i32 s38, 0\n" \
  "s_cbranch_scc1 .LCV_W3_%=\n" \
  ".LCV_W4_%=:\n" \
  "s_waitcnt vmcnt(0)\n" \
  ".p2align 6\n" \
  ".LCV_TOP_%=:\n" \
  "s_waitcnt lgkmcnt(0)\n" \
  "s_barrier\n" \
  "s_setprio 3\n" \
  "ds_read_b128 v[24:27], v1 offset:0\n" \
  "ds_read_b128 v[28:31], v1 offset:1024\n" \
  "ds_read_b128 v[32:35], v1 offset:2048\n" \
  "ds_read_b128 v[36:39], v1 offset:3072\n" \
  "ds_read_b128 v[40:43], v1 offset:4096\n" \
  "ds_read_b128 v[44:47], v1 offset:5120\n" \
  "ds_read_b128 v[48:51], v1 offset:6144\n" \
  "ds_read_b128 v[52:55], v1 offset:7168\n" \
  "s_memrealtime s[38:39]\n" \
  "s_waitcnt lgkmcnt(7)\n" \
  "v_smfmac_f32_16x16x64_bf16 v[12:15], v[24:27], v[64:71], v0\n" \
  "s_waitcnt lgkmcnt(6)\n" \
  "v_smfmac_f32_16x16x64_bf16 v[12:15], v[28:31], v[72:79], v0\n" \
  "s_waitcnt lgkmcnt(5)\n" \
  "v_smfmac_f32_16x16x64_bf16 v[12:15], v[32:35], v[80:87], v0\n" \
  "s_waitcnt lgkmcnt(4)\n" \
  "v_smfmac_f32_16x16x64_bf16 v[12:15], v[36:39], v[88:95], v0\n" \
  "s_waitcnt lgkmcnt(3)\n" \
  "v_smfmac_f32_16x16x64_bf16 v[12:15], v[40:43], v[96:103], v0\n" \
  "s_waitcnt lgkmcnt(2)\n" \
  "v_smfmac_f32_16x16x64_bf16 v[12:15], v[44:47], v[104:111], v0\n" \
  "s_waitcnt lgkmcnt(1)\n" \
  "v_smfmac_f32_16x16x64_bf16 v[12:15], v[48:51], v[112:119], v0\n" \
  "s_waitcnt lgkmcnt(0)\n" \
  "v_smfmac_f32_16x16x64_bf16 v[12:15], v[52:55], v[120:127], v0\n" \
  "ds_read_b32 v11, v1 offset:26688\n" \
  "s_mov_b32 s61, s38\n" \
  "s_add_u32 s38, s38, s79\n" \
  "s_add_u32 s38, s38, s96\n" \
  "s_mov_b32 s96, 0\n" \
  "v_or_b32 v57, 1, s38\n" \
  "v_add_u32 v56, s67, v10\n" \
  "s_add_u32 s39, s67, s76\n" \
  "s_add_u32 s38, s38, s77\n" \
  "s_bitset1_b32 s38, 0\n" \
  "v_writelane_b32 v56, s39, 1\n" \
  "v_writelane_b32 v57, s38, 1\n" \
  "s_sub_u32 s39, s71, s72\n" \
  "s_cmp_lt_u32 s39, 8\n" \
  "s_cselect_b64 exec, 19, 3\n" \
  "buffer_store_dword v57, v56, s[0:3], 0 offen sc1\n" \
  "s_mov_b64 exec, -1\n" \
  "v_permlane32_swap_b32 v12, v14\n" \
  "v_permlane32_swap_b32 v13, v15\n" \
  "v_pk_add_f32 v[12:13], v[12:13], v[14:15]\n" \
  "s_nop 0\n" \
  "v_cvt_pk_bf16_f32 v12, v12, v13\n" \
  "ds_write_b32 v4, v12\n" \
  "s_setprio 2\n" \
  "v_mov_b64 v[12:13], s[92:93]\n" \
  "v_mov_b64 v[14:15], s[92:93]\n" \
  "s_waitcnt lgkmcnt(1)\n" \
  "v_readfirstlane_b32 s86, v11\n" \
  "s_lshr_b32 s86, s86, 23\n" \
  "s_add_i32 s86, s86, s83\n" \
  "s_ashr_i32 s86, s86, 1\n" \
  "s_max_i32 s86, s86, s82\n" \
  "s_min_i32 s86, s86, 100\n" \
  "s_lshl_b32 s86, s86, 23\n" \
  "s_sub_i32 s84, 0xc8800000, s86\n" \
  "s_cmp_eq_u32 s73, 5\n" \
  "v_smfmac_f32_16x16x64_bf16 v[16:19], v[24:27], v[128:135], v0\n" \
  "v_smfmac_f32_16x16x64_bf16 v[16:19], v[28:31], v[136:143], v0\n" \
  "v_smfmac_f32_16x16x64_bf16 v[16:19], v[32:35], v[144:151], v0\n" \
  "v_smfmac_f32_16x16x64_bf16 v[16:19], v[36:39], v[152:159], v0\n" \
  "v_smfmac_f32_16x16x64_bf16 v[16:19], v[40:43], v[160:167], v0\n" \
  "v_smfmac_f32_16x16x64_bf16 v[16:19], v[44:47], v[168:175], v0\n" \
  "s_waitcnt lgkmcnt(0)\n" \
  "s_barrier\n" \
  "ds_read_b64_tr_b16 v[60:61], v6 offset:0\n" \
  "ds_read_b64_tr_b16 v[62:63], v9 offset:0\n" \
  "v_smfmac_f32_16x16x64_bf16 v[16:19], v[48:51], v[176:183], v0\n" \
  "v_smfmac_f32_16x16x64_bf16 v[16:19], v[52:55], v[184:191], v0\n" \
  "v_smfmac_f32_16x16x64_bf16 v[20:23], v[24:27], v[192:199], v0\n" \
  "v_smfmac_f32_16x16x64_bf16 v[20:23], v[28:31], v[200:207], v0\n" \
  "v_smfmac_f32_16x16x64_bf16 v[20:23], v[32:35], v[208:215], v0\n" \
  "v_smfmac_f32_16x16x64_bf16 v[20:23], v[36:39], v[216:223], v0\n" \
  "v_smfmac_f32_16x16x64_bf16 v[20:23], v[40:43], v[224:231], v0\n" \
  "v_smfmac_f32_16x16x64_bf16 v[20:23], v[44:47], v[232:239], v0\n" \
  "v_smfmac_f32_16x16x64_bf16 v[20:23], v[48:51], v[240:247], v0\n" \
  "v_smfmac_f32_16x16x64_bf16 v[20:23], v[52:55], v[248:255], v0\n" \
  "s_cmp_eq_u32 s71, 0\n" \
  "v_permlane32_swap_b32 v16, v18\n" \
  "v_permlane32_swap_b32 v17, v19\n" \
  "v_add_f32 v16, v16, v18\n" \
  "v_add_f32 v17, v17, v19\n" \
  "v_cvt_pk_bf16_f32 v16, v16, v17\n" \
  "s_waitcnt lgkmcnt(0)\n" \
  "v_max_u32 v60, 1, v60\n" \
  "v_max_u32 v61, 1, v61\n" \
  "v_max_u32 v62, 1, v62\n" \
  "v_max_u32 v63, 1, v63\n" \
  "buffer_store_dwordx4 v[60:63], v7, s[48:51], s69 offen sc1\n" \
  "v_permlane32_swap_b32 v20, v22\n" \
  "v_permlane32_swap_b32 v21, v23\n" \
  "v_pk_add_f32 v[20:21], v[20:21], v[22:23]\n" \
  "v_max_u32 v18, 1, v16\n" \
  "v_cvt_pk_bf16_f32 v17, v20, v21\n" \
  "ds_write_b64 v5, v[16:17]\n" \
  "v_max_u32 v19, 1, v17\n" \
  "s_nop 1\n" \
  "v_permlane32_swap_b32 v18, v19\n" \
  "buffer_store_dwordx2 v[18:19], v8, s[52:55], s70 offen sc1\n" \
  "ds_read_b128 v[24:27], v3 offset:0\n" \
  "ds_read_b128 v[28:31], v3 offset:1024\n" \
  "ds_read_b128 v[32:35], v3 offset:2048\n" \
  "ds_read_b128 v[36:39], v3 offset:3072\n" \
  "ds_read_b128 v[40:43], v3 offset:8192\n" \
  "ds_read_b128 v[44:47], v3 offset:9216\n" \
  "ds_read_b128 v[48:51], v3 offset:10240\n" \
  "ds_read_b128 v[52:55], v3 offset:11264\n" \
  "s_cbranch_scc1 .LCV_LAST_%=\n" \
  "s_cmp_eq_u32 s73, 5\n" \
  "s_cbranch_scc0 .LCV_NMK_%=\n" \
  "s_mov_b32 m0, s78\n" \
  "s_nop 0\n" \
  "buffer_load_dword v2, s[0:3], s66 offen sc1 lds\n" \
  ".LCV_NMK_%=:\n" \
  "s_setprio 0\n" \
  "s_waitcnt lgkmcnt(8)\n" \
  "s_barrier\n" \
  "s_add_u32 s67, s67, 1024\n" \
  "v_mov_b32 v11, 0\n" \
  "ds_write_b32 v59, v11\n" \
  "ds_read_b128 v[56:59], v3 offset:4096\n" \
  "ds_read_b128 v[60:63], v3 offset:5120\n" \
  "v_mov_b64 v[20:21], s[92:93]\n" \
  "v_mov_b64 v[22:23], s[92:93]\n" \
  "v_mov_b64 v[16:17], s[92:93]\n" \
  "v_mov_b64 v[18:19], s[92:93]\n" \
  "s_mov_b32 m0, s94\n" \
  "s_mov_b64 exec, 15\n" \
  "buffer_load_dword v2, s[44:47], s68 offen sc1 lds\n" \
  "s_mov_b64 exec, -1\n" \
  "s_setprio 2\n" \
  "s_waitcnt lgkmcnt(0)\n" \
  "s_barrier\n" \
  "ds_read_b128 v[4:7], v1 offset:8192\n" \
  "ds_read_b128 v[8:11], v1 offset:9216\n" \
  "s_waitcnt lgkmcnt(1)\n" \
  "v_smfmac_f32_16x16x64_bf16 v[12:15], v[4:7], v[24:31], v0\n" \
  "ds_read_b128 v[24:27], v3 offset:6144\n" \
  "ds_read_b128 v[28:31], v3 offset:7168\n" \
  "v_smfmac_f32_16x16x64_bf16 v[16:19], v[4:7], v[32:39], v0\n" \
  "ds_read_b128 v[32:35], v3 offset:12288\n" \
  "ds_read_b128 v[36:39], v3 offset:13312\n" \
  "s_waitcnt lgkmcnt(4)\n" \
  "v_smfmac_f32_16x16x64_bf16 v[12:15], v[8:11], v[40:47], v0\n" \
  "v_smfmac_f32_16x16x64_bf16 v[16:19], v[8:11], v[48:55], v0\n" \
  "v_smfmac_f32_16x16x64_bf16 v[20:23], v[4:7], v[56:63], v0\n" \
  "ds_read_b128 v[56:59], v3 offset:14336\n" \
  "ds_read_b128 v[60:63], v3 offset:15360\n" \
  "v_mov_b64 v[40:41], s[92:93]\n" \
  "v_mov_b64 v[42:43], s[92:93]\n" \
  "s_nop 2\n" \
  "v_permlane32_swap_b32 v12, v17\n" \
  "v_permlane32_swap_b32 v13, v16\n" \
  "v_permlane32_swap_b32 v14, v19\n" \
  "v_permlane32_swap_b32 v15, v18\n" \
  "v_pk_add_f32 v[12:13], v[12:13], v[16:17] op_sel:[0,1] op_sel_hi:[1,0]\n" \
  "v_pk_add_f32 v[14:15], v[14:15], v[18:19] op_sel:[0,1] op_sel_hi:[1,0]\n" \
  "buffer_atomic_add_x2 v[12:13], v2, s[40:43], s56 offen\n" \
  "buffer_atomic_add_x2 v[14:15], v2, s[40:43], s57 offen\n" \
  "s_waitcnt lgkmcnt(2)\n" \
  "v_smfmac_f32_16x16x64_bf16 v[20:23], v[8:11], v[32:39], v0\n" \
  "v_smfmac_f32_16x16x64_bf16 v[40:43], v[4:7], v[24:31], v0\n" \
  "s_waitcnt lgkmcnt(0)\n" \
  "v_smfmac_f32_16x16x64_bf16 v[40:43], v[8:11], v[56:63], v0\n" \
  "s_nop 7\n" \
  "v_permlane32_swap_b32 v20, v41\n" \
  "v_permlane32_swap_b32 v21, v40\n" \
  "v_permlane32_swap_b32 v22, v43\n" \
  "v_permlane32_swap_b32 v23, v42\n" \
  "v_pk_add_f32 v[20:21], v[20:21], v[40:41] op_sel:[0,1] op_sel_hi:[1,0]\n" \
  "v_pk_add_f32 v[22:23], v[22:23], v[42:43] op_sel:[0,1] op_sel_hi:[1,0]\n" \
  "buffer_atomic_add_x2 v[20:21], v2, s[40:43], s58 offen\n" \
  "buffer_atomic_add_x2 v[22:23], v2, s[40:43], s59 offen\n" \
  "s_setprio 0\n" \
  "s_memrealtime s[38:39]\n" \
  "v_mbcnt_lo_u32_b32 v48, -1, 0\n" \
  "v_mbcnt_hi_u32_b32 v48, -1, v48\n" \
  "s_add_u32 s88, s74, 157760\n" \
  "v_mov_b32 v59, s88\n" \
  "v_lshl_add_u32 v52, v48, 2, v59\n" \
  "ds_read_b32 v52, v52 offset:192\n" \
  "v_lshrrev_b32 v49, 3, v48\n" \
  "v_and_b32 v50, 7, v48\n" \
  "v_lshlrev_b32 v50, 4, v50\n" \
  "v_lshl_or_b32 v44, v49, 8, v50\n" \
  "s_mov_b32 s87, 0x40000\n" \
  "v_lshlrev_b32 v10, 4, v48\n" \
  "v_lshl_add_u32 v58, v48, 2, s94\n" \
  "s_waitcnt vmcnt(4)\n" \
  "ds_read_b32 v58, v58\n" \
  "s_waitcnt lgkmcnt(0)\n" \
  "v_cmp_eq_u32 vcc, 0, v58\n" \
  "s_and_b32 vcc_lo, vcc_lo, 15\n" \
  "s_cbranch_scc0 .LCV_GOT_%=\n" \
  "s_branch .LCV_ASK_%=\n" \
  ".LCV_LATE_%=:\n" \
  "v_cmp_eq_u32 vcc, 0, v58\n" \
  "s_and_b32 vcc_lo, vcc_lo, 15\n" \
  "s_cbranch_scc0 .LCV_GOT_%=\n" \
  "s_sleep 12\n" \
  ".LCV_ASK_%=:\n" \
  "s_mov_b64 exec, 15\n" \
  "buffer_load_dword v58, v2, s[44:47], s68 offen sc1\n" \
  "s_mov_b64 exec, -1\n" \
  "s_sub_u32 s87, s87, 1\n" \
  "s_cbranch_scc1 .LCV_END_%=\n" \
  "s_waitcnt vmcnt(0)\n" \
  "s_branch .LCV_LATE_%=\n" \
  ".LCV_GOT_%=:\n" \
  "s_mov_b32 m0, s99\n" \
  "s_add_u32 s88, s99, 8192\n" \
  "global_load_lds_dwordx4 v10, s[62:63] sc1\n" \
  "global_load_lds_dwordx4 v10, s[62:63] offset:1024 sc1\n" \
  "s_mov_b32 m0, s88\n" \
  "s_nop 0\n" \
  "global_load_lds_dwordx4 v10, s[64:65] sc1\n" \
  "global_load_lds_dwordx4 v10, s[64:65] offset:1024 sc1\n" \
  ".LCV_HAVE_%=:\n" \
  "s_waitcnt lgkmcnt(0)\n" \
  "s_sub_u32 s61, s38, s61\n" \
  "s_add_u32 s79, s79, 2\n" \
  "s_min_u32 s79, s79, s61\n" \
  "s_sub_u32 s39, s38, 150\n" \
  "v_subrev_u32 v52, s39, v52\n" \
  "v_cmp_lt_u32 vcc, 0x1000, v52\n" \
  "s_cmp_eq_u64 vcc, 0\n" \
  "s_cbranch_scc1 .LCV_OKM_%=\n" \
  ".LCV_AGN_%=:\n" \
  "buffer_load_dword v52, v2, s[0:3], s66 offen sc1\n" \
  "s_sub_u32 s87, s87, 1\n" \
  "s_cbranch_scc1 .LCV_END_%=\n" \
  "s_waitcnt vmcnt(0)\n" \
  "v_cmp_eq_u32 vcc, 0, v52\n" \
  "v_subrev_u32 v52, s39, v52\n" \
  "s_cmp_eq_u64 vcc, 0\n" \
  "s_cbranch_scc1 .LCV_OKM_%=\n" \
  "s_sleep 4\n" \
  "s_branch .LCV_AGN_%=\n" \
  ".LCV_OKM_%=:\n" \
  "s_nop 1\n" \
  "v_max_i32_dpp v52, v52, v52 row_ror:1 row_mask:0xf bank_mask:0xf\n" \
  "s_nop 1\n" \
  "v_max_i32_dpp v52, v52, v52 row_ror:2 row_mask:0xf bank_mask:0xf\n" \
  "s_nop 1\n" \
  "v_max_i32_dpp v52, v52, v52 row_ror:4 row_mask:0xf bank_mask:0xf\n" \
  "s_nop 1\n" \
  "v_max_i32_dpp v52, v52, v52 row_ror:8 row_mask:0xf bank_mask:0xf\n" \
  "s_movk_i32 vcc_lo, 150\n" \
  "v_readlane_b32 s88, v52, 0\n" \
  "s_max_i32 vcc_lo, vcc_lo, s88\n" \
  "v_readlane_b32 s88, v52, 16\n" \
  "s_max_i32 vcc_lo, vcc_lo, s88\n" \
  "v_readlane_b32 s88, v52, 32\n" \
  "s_max_i32 vcc_lo, vcc_lo, s88\n" \
  "v_readlane_b32 s88, v52, 48\n" \
  "s_max_i32 vcc_lo, vcc_lo, s88\n" \
  "s_add_u32 s66, s66, 1024\n" \
  "s_add_u32 vcc_lo, vcc_lo, s39\n" \
  "s_add_u32 vcc_lo, vcc_lo, 23\n" \
  ".LCV_TW_%=:\n" \
  "s_memrealtime s[38:39]\n" \
  "s_waitcnt lgkmcnt(0)\n" \
  "s_sub_u32 s38, s38, vcc_lo\n" \
  "s_cmp_lt_i32 s38, 0\n" \
  "s_cbranch_scc1 .LCV_TW_%=\n" \
  "buffer_load_dwordx4 v[24:27], v44, s[40:43], s60 offen sc1\n" \
  "buffer_load_dwordx4 v[28:31], v44, s[40:43], s60 offen offset:2048 sc1\n" \
  "s_add_u32 s62, s62, 0x80000\n" \
  "s_addc_u32 s63, s63, 0\n" \
  "s_add_u32 s64, s64, 0x40000\n" \
  "s_addc_u32 s65, s65, 0\n" \
  "s_add_u32 s68, s68, 0x1000\n" \
  "s_add_u32 s69, s69, 0x10000\n" \
  "s_add_u32 s70, s70, 0x10000\n" \
  "s_sub_u32 s88, s73, 4\n" \
  "v_lshrrev_b32 v49, 2, v48\n" \
  "v_lshrrev_b32 v50, 4, v48\n" \
  "v_bitop3_b32 v50, v49, v50, 3 bitop3:0x28\n" \
  "v_lshl_add_u32 v4, v50, 3, s97\n" \
  "v_and_b32 v50, 3, v48\n" \
  "v_lshl_add_u32 v4, v50, 5, v4\n" \
  "v_and_b32 v49, 1, v49\n" \
  "v_lshl_add_u32 v4, v49, 8, v4\n" \
  "v_and_b32 v49, 8, v48\n" \
  "v_lshl_add_u32 v4, v49, 4, v4\n" \
  "v_and_b32 v51, 15, v48\n" \
  "v_bfe_u32 v49, v48, 4, 1\n" \
  "v_lshrrev_b32 v50, 5, v48\n" \
  "s_lshl_b32 s89, s88, 10\n" \
  "s_add_u32 s89, s89, s74\n" \
  "s_add_u32 s89, s89, 158720\n" \
  "v_lshrrev_b32 v9, 3, v51\n" \
  "v_mul_u32_u24 v6, 41, v9\n" \
  "v_xor_b32 v6, v6, v51\n" \
  "v_lshl_add_u32 v6, v6, 3, s89\n" \
  "v_lshl_add_u32 v6, v49, 6, v6\n" \
  "v_lshl_add_u32 v6, v50, 9, v6\n" \
  "v_xor_b32 v9, 0x90, v6\n" \
  "v_lshlrev_b32 v8, 4, v49\n" \
  "v_lshl_add_u32 v8, v51, 5, v8\n" \
  "v_lshl_add_u32 v8, v50, 3, v8\n" \
  "s_lshl_b32 s89, s88, 8\n" \
  "s_add_u32 s89, s89, s74\n" \
  "s_add_u32 s89, s89, 139264\n" \
  "v_and_b32 v49, 31, v48\n" \
  "v_lshl_add_u32 v5, v49, 3, s89\n" \
  "v_lshl_add_u32 v5, v50, 10, v5\n" \
  "v_lshrrev_b32 v49, 2, v51\n" \
  "v_and_b32 v50, 5, v51\n" \
  "v_and_or_b32 v49, v49, 2, v50\n" \
  "v_and_b32 v50, 48, v48\n" \
  "v_lshlrev_b32 v7, 3, v50\n" \
  "v_lshl_add_u32 v7, v49, 4, v7\n" \
  "v_bitop3_b32 v49, v51, 2, 2 bitop3:0x0c\n" \
  "v_lshl_add_u32 v7, v49, 29, v7\n" \
  "v_mov_b64 v[12:13], 0\n" \
  "v_mov_b64 v[14:15], 0\n" \
  "v_mov_b64 v[16:17], 0\n" \
  "v_mov_b64 v[18:19], 0\n" \
  "v_mov_b64 v[20:21], 0\n" \
  "v_mov_b64 v[22:23], 0\n" \
  "v_and_b32 v50, 3, v48\n" \
  "v_lshl_add_u32 v40, v50, 2, s98\n" \
  "v_lshrrev_b32 v50, 4, v48\n" \
  "v_mad_u32_u24 v40, v50, s95, v40\n" \
  "v_bfe_u32 v49, v48, 3, 1\n" \
  "v_lshl_add_u32 v40, v49, 6, v40\n" \
  "v_bfe_u32 v49, v48, 2, 1\n" \
  "v_lshl_add_u32 v40, v49, 7, v40\n" \
  "v_xor_b32 v41, 16, v40\n" \
  "v_xor_b32 v42, 32, v40\n" \
  "v_xor_b32 v43, 48, v40\n" \
  "s_add_i32 s80, s86, 0x35000000\n" \
  "v_mov_b32 v11, 0\n" \
  "s_mov_b32 s89, s60\n" \
  "s_bitcmp1_b32 s60, 7\n" \
  "s_cselect_b32 s88, 0x10000, 0\n" \
  "s_bitset1_b32 s88, 7\n" \
  "s_xor_b32 s56, s56, s88\n" \
  "s_xor_b32 s57, s57, s88\n" \
  "s_xor_b32 s58, s58, s88\n" \
  "s_xor_b32 s59, s59, s88\n" \
  "s_xor_b32 s60, s60, s88\n" \
  "s_sub_u32 s71, s71, 1\n" \
  ".LCV_SEE_%=:\n" \
  "s_waitcnt vmcnt(1)\n" \
  "v_cvt_f32_i32 v24, v24\n" \
  "v_cvt_f32_i32 v25, v25\n" \
  "v_cvt_f32_i32 v26, v26\n" \
  "v_cvt_f32_i32 v27, v27\n" \
  "v_max_f32 v45, |v24|, |v26|\n" \
  "v_pk_mul_f32 v[24:25], v[24:25], s[80:81] op_sel_hi:[1,0]\n" \
  "v_pk_mul_f32 v[26:27], v[26:27], s[80:81] op_sel_hi:[1,0]\n" \
  "s_nop 0\n" \
  "v_cvt_pk_bf16_f32 v32, v24, v26\n" \
  "v_cvt_pk_bf16_f32 v33, v25, v27\n" \
  "ds_write_b32 v40, v32\n" \
  "ds_write_b32 v41, v33\n" \
  "s_waitcnt vmcnt(0)\n" \
  "v_cvt_f32_i32 v28, v28\n" \
  "v_cvt_f32_i32 v30, v30\n" \
  "v_cvt_f32_i32 v29, v29\n" \
  "v_max3_f32 v45, v45, |v28|, |v30|\n" \
  "v_cvt_f32_i32 v31, v31\n" \
  "v_cmp_lt_f32 vcc, s91, v45\n" \
  "v_pk_mul_f32 v[28:29], v[28:29], s[80:81] op_sel_hi:[1,0]\n" \
  "v_pk_mul_f32 v[30:31], v[30:31], s[80:81] op_sel_hi:[1,0]\n" \
  "s_cmp_eq_u64 vcc, 0\n" \
  "s_cbranch_scc0 .LCV_POLL_%=\n" \
  "v_cvt_pk_bf16_f32 v36, v28, v30\n" \
  "v_cvt_pk_bf16_f32 v37, v29, v31\n" \
  "ds_write_b32 v42, v36\n" \
  "ds_write_b32 v43, v37\n" \
  "v_dot2c_f32_bf16 v11, v32, v32\n" \
  "v_dot2c_f32_bf16 v11, v33, v33\n" \
  "v_dot2c_f32_bf16 v11, v36, v36\n" \
  "v_dot2c_f32_bf16 v11, v37, v37\n" \
  "s_nop 1\n" \
  "s_nop 0\n" \
  "v_cmp_lt_f32 vcc, 4.0, v11\n" \
  "s_cbranch_vccz .LCV_TOP_%=\n" \
  "s_mov_b64 exec, vcc\n" \
  "ds_max_u32 v59, v11\n" \
  "s_mov_b64 exec, -1\n" \
  "s_branch .LCV_TOP_%=\n" \
  ".LCV_POLL_%=:\n" \
  "buffer_load_dwordx4 v[24:27], v44, s[40:43], s89 offen sc1\n" \
  "buffer_load_dwordx4 v[28:31], v44, s[40:43], s89 offen offset:2048 sc1\n" \
  "s_sub_u32 s87, s87, 1\n" \
  "s_cbranch_scc1 .LCV_END_%=\n" \
  "s_branch .LCV_SEE_%=\n" \
  ".LCV_LAST_%=:\n" \
  "s_waitcnt vmcnt(0)\n" \
  ".LCV_END_%=:\n" \
  "s_endpgm\n"
__device__ __forceinline__ void start_kgen(const MegaArgs& A, char* smem, const int kg) {
  const int w = A.wv, l = lane_id(), tid = 64 * w + l, c = l & 15, g = l >> 4;
  KGen G;
  unsigned tp = 0u;
#define TEXT_ASK()                                                                                                     \
  {                                                                                                                    \
    const char* pc;                                                                                                    \
    unsigned n;                                                                                                        \
    asm volatile("s_getpc_b64 %0\n.LKG_PC:\n\ts_mov_b32 %1, .LKG_LAST-.LKG_PC" : "=s"(pc), "=s"(n));                    \
    const unsigned o = (unsigned)(tid * 8 + ((blockIdx.x >> 3) & 7)) * IPS;                                            \
    if ((int)o < (int)n) asm volatile("global_load_dword %0, %1, %2" : "=v"(tp) : "v"(o), "s"(pc) : "memory");         \
  }
  if (IPF == 2) TEXT_ASK()
  v4u raw[2] = {};
#pragma unroll
  for (int i = 0; i < TP; ++i)
    if (kg < 32) raw[i] = *reinterpret_cast<const v4u*>(A.x0 + (size_t)(TP * (8 * (kg & 7) + w) + i) * DM + l * 8);
  G.load(A, 8 * kg + w, l);
  {
    const __amdgpu_buffer_rsrc_t rz = MKRS(A.ws + OFF_Z, Z_BYTES);
#pragma unroll
    for (int i = 0; i < 4; ++i) {
      const u64 z = i == 0 && kg < (int)(2 * SZ_ACC / 8192) ? ACC_INIT : 0ull;
      ST128((v4u{(unsigned)z, (unsigned)(z >> 32), (unsigned)z, (unsigned)(z >> 32)}), rz, tid * 16, (i * 64 + kg) * 8192);
    }
  }
  const u64 tag = call_tag(A);
  if (TP && kg < 32) {
    unsigned t = row_stats(raw[0]);
    if (TP > 1) {
      const unsigned t1 = row_stats(raw[1]);
      t = l < 2 ? t : t1;
    }
    if (l < 2 * TP) __hip_atomic_store((gu64*)(A.ws + OFF_GT) + 2 * (L_SEQ + TP * (8 * kg + w)) + l, tagged(tag, (l & 1) ? t : t << 16), RLX_AGENT);
  }
  DRAIN();
  __syncthreads();
  if (tid < 8) __hip_atomic_store((gu64*)(A.ws + OFF_RD) + tid * 64 + kg, tag, RLX_AGENT);
  asm volatile("" : "+v"(G.pa), "+v"(G.pb), "+v"(G.pe));
  G.setup();
  G.st = smem + w * 16640;
  const __amdgpu_buffer_rsrc_t rs = MKRS(A.ws + OFF_KF + (8 * kg + w) * 2048, 386 * 4);
  SST(1);
  unsigned tw = 0u;
#define TLB_ASK()                                                                                                      \
  if (tid < 384) {                                                                                                     \
    const unsigned o = (unsigned)OFF_XA + (unsigned)(tid * 8 + ((blockIdx.x >> 3) & 7)) * 65536u;                      \
    if (o < WS_TOTAL) asm volatile("global_load_dword %0, %1, %2" : "=v"(tw) : "v"(o), "s"(A.ws) : "memory");          \
  }
#define X2_CLEAR() \
  _Pragma("unroll") for (int i = 0; i < 4; ++i) ST128((v4u{0u, 0u, 0u, 0u}), MKRS(A.ws + OFF_X2, SZ_X), tid * 16, (4 * kg + i) * 8192);
  if (XZP == 1) { X2_CLEAR() }
  if (IPF == 1) TEXT_ASK()
  if (TLBW == 1) TLB_ASK()
  unsigned pf[6] = {};
#define PF_ASK(M)                                                                                                                  \
  if (kg < 32) {                                                                                                                   \
    if ((M) & 1) asm volatile("global_load_dword %0, %2, %3\n\tglobal_load_dword %1, %2, %3 offset:128" : "=&v"(pf[0]), "=&v"(pf[1]) : "v"(kg * 131072 + tid * 256), "s"(A.Wo)); \
    if ((M) & 2) asm volatile("global_load_dword %0, %2, %3\n\tglobal_load_dword %1, %2, %3 offset:128" : "=&v"(pf[2]), "=&v"(pf[3]) : "v"(kg * 256 + tid * 8192), "s"(A.Wq)); \
    if ((M) & 4) asm volatile("global_load_dword %0, %2, %3\n\tglobal_load_dword %1, %2, %3 offset:128" : "=&v"(pf[4]), "=&v"(pf[5]) : "v"(kg * 256 + tid * 8192), "s"(A.Wk)); \
  }
  PF_ASK(PFA)
  G.columns(l, 0, 16);
  G.get(G.bh, G.bl, l);
  if (XZP == 2) { X2_CLEAR() }
  PF_ASK(PFB)
  if (TLBW == 2) TLB_ASK()
  (void)G.scale();
  const Cx zb = G.power(256.f);
  Cx b = G.C, z = G.power(2.f);
#pragma unroll 1
  for (int f = 1; f < 9;) {
    char* p = G.st + l * 8;
    const int fe = f == 1 ? 4 : 9;
#pragma unroll 1
    for (; f < fe; ++f) {
      b = cmul(b, zb);
      if (f == 2 || f == 4) z = cmul(z, z);
      Cx y = b;
      p = G.rows(p, y, z, f == 1 ? 8 : f < 4 ? 4 : 2);
    }
    const f32x4 d = G.tile<false>(l);
    const v4u r = {pkh(d.x, d.x), pkh(d.y, d.y), pkh(d.z, d.z), pkh(d.w, d.w)};
    if (f == 4) {
      ST128(r, rs, (g < 2 ? 8 * c + 4 * g : 64 * g + 4 * c) * 4, 0);
    } else {
      ST64((v2u{r.x, r.y}), rs, (256 + 64 * g + 2 * c) * 4, 0);
      ST64((v2u{r.z, r.w}), rs, (288 + 64 * g + 2 * c) * 4, 0);
    }
  }
  SST(2);
  DRAIN();
  asm volatile("" ::"v"(tp), "v"(tw), "v"(pf[0]), "v"(pf[1]), "v"(pf[2]), "v"(pf[3]), "v"(pf[4]), "v"(pf[5]));
  if (l == 0) __hip_atomic_store((gu64*)(A.ws + OFF_KG) + kg * 16 + w, tag, RLX_AGENT);
  if (XZP == 3) { X2_CLEAR() }
  const u64 rv = (XZP & 3) && w == 0 ? ready_look(A) : 0;
  if (TLBW == 3) {
    TLB_ASK()
    DRAIN();
    asm volatile("" ::"v"(tw));
  }
  DRAIN();
  __syncthreads();
  if ((XZP & 3) && w == 0) {
    ready_wait(A, tag, rv);
    if (l < 8) set_flag((unsigned*)(A.ws + OFF_FL) + FL_FZ + l * 64 + kg);
  }
  SST(3);
}

constexpr int C_SM = 131072, C_SO = C_SM + 8192, C_KT = C_SO + 2048, C_X = C_KT + 16384, C_SQ = C_X + 1024;
static_assert(C_SQ + 4096 <= 163840 && C_SQ % 512 == 0, "lds");
__device__ __forceinline__ v4u mk_srd(const void* p, const unsigned bytes) {
  const unsigned long long a = (unsigned long long)p;
  return v4u{(unsigned)a, (unsigned)(a >> 32) & 0xffffu, bytes, 0x00020000u};
}
template <bool ATT>
__device__ __forceinline__ void role_chain_t(const MegaArgs& A, char* smem, const int L, const int h, const int rep) {
  const int w = A.wv;
  char* ws = A.ws;
  unsigned* FL = (unsigned*)(ws + OFF_FL);
  const int l = lane_id(), c = l & 15, g = l >> 4;
  unsigned* FQ = FL + FL_FQ + L * 64 * 32 * 32;
  const int wb = (w + ROT_H) & 7, tb = ROT_T;
  const unsigned sb = lds_base(smem);
  glds16(A.mem0, (unsigned)((c & 7) * DM + 64 * w + 16 * g + 8 * (c >> 3)) * 2, sb + C_SM + w * 1024);
#pragma unroll
  for (int i = 0; i < 16; ++i)
    glds16((const char*)A.Wo + (size_t)(h * DH + 32 * (i >> 2) + 4 * w + (i & 2)) * (DM * 2),
           (unsigned)((i & 1) * 1024 + ((l ^ (((i & 1) | ((w & 3) << 1)) << 1)) << 4)), sb + w * 16384 + i * 1024, WNT & 1);
  const u64 tag = call_tag(A);
  const char* gK = ws + OFF_QK + L * SZ_QK + ((size_t)h * 16 + 2 * w) * 1024;
  const char* gV = ws + OFF_VF + L * SZ_O + ((size_t)h * 8 + 2 * w - 8) * 1024;
  v8u pv, pw;
  {
    const unsigned voff = g * 256 + c * 8, wp = sb + w * 16384 + l * 16;
    pv[0] = (c & 8) ? 0xEEEE : 0x4444;
    pv[1] = sb + C_SM + l * 16;
    const unsigned ra = sb + C_SQ + (g >> 1) * 512 + (c ^ ((g & 1) * 18)) * 8;
    pv[2] = ATT ? ra : voff;
    pv[3] = ATT ? ra ^ 0x108 : wp;
    pv[4] = sb + ((c & 2) ? C_SQ : C_SO) + w * 256 + (((c & 5) | ((c >> 2) & 2)) + 8 * (g >> 1)) * 16 + 8 * ((g ^ (c >> 1)) & 1);
    pv[5] = voff;
    pv[6] = wp;
    pv[7] = sb + C_SO + (l >> 5) * 1024 + w * 256 + (l & 31) * 8;
    const int q = (l >> 2) & 3, p = l & 3;
    const int f = (q & 1) | ((g & 1) << 1) | ((q >> 1) << 2);
#pragma unroll
    for (int i = 0; i < 4; ++i) pw[i] = (4 * i + g) * (HD * 2) + ((c ^ (((g & 1) | (i << 1)) << 1)) << 4) - 1024 * i;
    pw[4] = sb + (g >> 1) * 16384 + (4 * (g & 1) + (q & 1) + 8 * (q >> 1)) * 256 + (w ^ f) * 32 + p * 8;
    pw[5] = sb + (4 * (g >> 1) + 2 * (q >> 1) + (g & 1)) * 16384 + (q & 1) * 1024 + p * 8 + ((4 * wb) ^ f ^ tb) * 32;
    pw[6] = pw[7] = 0u;
  }
  const size_t wo = (size_t)(16 * w) * HD + h * DH;
#define POL 2
#define PG(w) (POL == 2 ? (w) * 4096 : ((w) & 1) * 4096 + ((w) >> 1) * 16384)
  const v4u rsA = mk_srd(ws + OFF_ACC + L * SZ_ACC + (POL == 1 ? 8192 : 0), SZ_ACC - 8192);
  const v4u rsF = mk_srd(FQ, 64 * 32 * 32 * 4);
  const v4u rsMK = mk_srd(ws + OFF_MEMK + L * SZ_MEM, SZ_MEM);
  const v4u rsMV = mk_srd(ws + OFF_MEMV + L * SZ_MEM, SZ_MEM);
  const v4u pAB = (v4u{0u, 2048u, 3072u, 1024u} ^ (1536u * tb)) + PG(wb);
  const v4u rsT = mk_srd(FL + FL_FK + L * 32, 64 * 1024 - 128);
  unsigned sc[8];
  sc[0] = h * 128;
  sc[1] = (h * 4 + w - 4) * 512;
  sc[2] = (h * 4 + w - 4) * 512;
  sc[3] = NSEG - 1;
  sc[5] = w;
  sc[6] = sb;
  const char* dbg = ws;
#define CHAIN_OPS                                                                                                          \
  ::"{v[0:7]}"(pv), "{v[24:31]}"(pw), "{s[4:5]}"(A.Wv + wo), "{s[6:7]}"(A.Wq + wo), "{s[8:9]}"(A.Wk + wo), "{s[10:11]}"(tag),         \
      "{s[12:13]}"(ws + OFF_RD + (blockIdx.x & 7) * 512), "{s[14:15]}"(ws + OFF_G + (blockIdx.x & 7) * 256), "{s[16:17]}"(ws + OFF_G + h * 8), \
      "{s18}"(w == 4 && L == 0 ? 255 : 0), "{s19}"(sb + w * 16384), "{s[20:21]}"(ws + WS_TOTAL + (w == 0 ? 534 * 256 + A.vb : 610 * 256) * 8), \
      "{s[40:43]}"(rsA), "{s[44:47]}"(rsF), "{s[48:51]}"(rsMK), "{s[52:55]}"(rsMV), "{s[56:59]}"(pAB), "{s60}"(PG(w)), "{s61}"(ATT ? PG(h >> 2) + (h & 3) * 1024 : L), \
      "{s[62:63]}"(gK), "{s[64:65]}"(gV), "{s66}"(0), "{s67}"(w == 4 ? (h >> 3) * 256 + (h & 7) * 8 : 0x40000000), "{s68}"(sc[0]), "{s69}"(sc[1]), "{s70}"(sc[2]),    \
      "{s71}"(sc[3]), "{s73}"(sc[5]), "{s74}"(sc[6]), "{s[36:37]}"(dbg),                   \
      "{s[0:3]}"(rsT)                                                                                                      \
      : "memory", "vcc", "scc", "s99", "v255"
  if (ATT) asm volatile("s_mov_b64 exec, -1\ns_nop 4\n" CHAIN_ASM_ATT ".LKG_LAS2:\n" CHAIN_OPS);
  else asm volatile("s_mov_b64 exec, -1\ns_nop 4\n" CHAIN_ASM_VO CHAIN_OPS);
#undef CHAIN_OPS
}

__device__ __forceinline__ void role_chain(const MegaArgs& A, char* smem, const int L, const int h, const int rep) {
  if (A.wv < 4) role_chain_t<true>(A, smem, L, h, rep);
  else role_chain_t<false>(A, smem, L, h, rep);
}

constexpr int AW_SO = 0, AW_TK = 32768, AW_CNT = AW_TK + 98304;
__device__ __forceinline__ unsigned umin3(unsigned a, unsigned b, unsigned c) {
  return __builtin_elementwise_min(__builtin_elementwise_min(a, b), c);
}
#define PW_FREE(L_, s_, i_) \
  if (l == 0) set_flag(FL + FL_FD + ((s_) * 2 + (L_)) * 32 + (i_))
#define AW_ASM_X \
  "s_mov_b32 s61, 0\n" \
  "s_mov_b32 s67, 1\n" \
  "s_mov_b32 s68, 0\n" \
  "s_mov_b32 s70, 0\n" \
  "s_mov_b32 s73, 0\n" \
  "s_mov_b32 s71, 0x3e0293ee\n" \
  "s_mov_b32 s72, 0xbe0293ee\n" \
  "s_mov_b32 s65, 49152\n" \
  "s_bitcmp0_b32 s58, 2\n" \
  "s_cbranch_scc1 .LCX_OWN_%=\n" \
  "v_swap_b32 v192, v208\n" \
  "v_swap_b32 v193, v209\n" \
  "v_swap_b32 v194, v210\n" \
  "v_swap_b32 v195, v211\n" \
  "v_swap_b32 v196, v212\n" \
  "v_swap_b32 v197, v213\n" \
  "v_swap_b32 v198, v214\n" \
  "v_swap_b32 v199, v215\n" \
  "v_swap_b32 v200, v216\n" \
  "v_swap_b32 v201, v217\n" \
  "v_swap_b32 v202, v218\n" \
  "v_swap_b32 v203, v219\n" \
  "v_swap_b32 v204, v220\n" \
  "v_swap_b32 v205, v221\n" \
  "v_swap_b32 v206, v222\n" \
  "v_swap_b32 v207, v223\n" \
  "v_swap_b32 v224, v240\n" \
  "v_swap_b32 v225, v241\n" \
  "v_swap_b32 v226, v242\n" \
  "v_swap_b32 v227, v243\n" \
  "v_swap_b32 v228, v244\n" \
  "v_swap_b32 v229, v245\n" \
  "v_swap_b32 v230, v246\n" \
  "v_swap_b32 v231, v247\n" \
  "v_swap_b32 v232, v248\n" \
  "v_swap_b32 v233, v249\n" \
  "v_swap_b32 v234, v250\n" \
  "v_swap_b32 v235, v251\n" \
  "v_swap_b32 v236, v252\n" \
  "v_swap_b32 v237, v253\n" \
  "v_swap_b32 v238, v254\n" \
  "v_swap_b32 v239, v255\n" \
  ".LCX_OWN_%=:\n" \
  "v_mov_b32 v38, s58\n" \
  "s_memrealtime s[38:39]\n" \
  "s_waitcnt lgkmcnt(0)\n" \
  "s_mov_b32 s69, s38\n" \
  "s_setprio 3\n" \
  ".p2align 6\n" \
  ".LCX_TOP_%=:\n" \
  "s_mov_b32 s62, 0x200000\n" \
  "s_cmp_ge_u32 s73, s67\n" \
  "s_cbranch_scc1 .LCX_TOK_%=\n" \
  "s_cmp_eq_u32 s61, 0\n" \
  "s_cbranch_scc1 .LCX_SPIN_%=\n" \
  ".LCX_RE_%=:\n" \
  "ds_read_b32 v37, v38\n" \
  "s_add_u32 s62, s62, 1\n" \
  "s_waitcnt lgkmcnt(0)\n" \
  "v_readfirstlane_b32 s73, v37\n" \
  "s_bfe_u32 s64, s73, 0x80008\n" \
  "s_and_b32 s73, s73, 0xff\n" \
  "s_min_u32 s73, s73, s64\n" \
  "s_cmp_ge_u32 s73, s67\n" \
  "s_cbranch_scc1 .LCX_TOK_%=\n" \
  "s_sleep 1\n" \
  "s_cmp_lt_u32 s62, 2097158\n" \
  "s_cbranch_scc1 .LCX_RE_%=\n" \
  ".LCX_SPIN_%=:\n" \
  "s_sleep 2\n" \
  "s_memrealtime s[38:39]\n" \
  "ds_read_b32 v37, v38\n" \
  "s_sub_u32 s62, s62, 1\n" \
  "s_cbranch_scc1 .LCX_END_%=\n" \
  "s_waitcnt lgkmcnt(0)\n" \
  "v_readfirstlane_b32 s73, v37\n" \
  "s_add_u32 s38, s38, 80\n" \
  "s_bfe_u32 s64, s73, 0x80008\n" \
  "s_and_b32 s73, s73, 0xff\n" \
  "s_min_u32 s73, s73, s64\n" \
  "s_cmp_ge_u32 s73, s67\n" \
  "s_cbranch_scc0 .LCX_SPIN_%=\n" \
  "s_cmp_eq_u32 s70, 0\n" \
  "s_cbranch_scc1 .LCX_TOK_%=\n" \
  "s_sub_u32 s63, s38, s69\n" \
  "s_max_i32 s63, s63, 0\n" \
  "s_add_u32 s69, s69, s63\n" \
  ".LCX_TOK_%=:\n" \
  "ds_read_b128 v[48:51], v6 offset:0\n" \
  "ds_read_b128 v[64:67], v0 offset:8192\n" \
  "ds_read_b128 v[80:83], v0 offset:12288\n" \
  "ds_read_b128 v[52:55], v6 offset:1024\n" \
  "ds_read_b128 v[68:71], v0 offset:9216\n" \
  "ds_read_b128 v[84:87], v0 offset:13312\n" \
  "ds_read_b128 v[56:59], v6 offset:2048\n" \
  "ds_read_b128 v[72:75], v0 offset:10240\n" \
  "ds_read_b128 v[88:91], v0 offset:14336\n" \
  "ds_read_b128 v[60:63], v6 offset:3072\n" \
  "ds_read_b128 v[76:79], v0 offset:11264\n" \
  "ds_read_b128 v[92:95], v0 offset:15360\n" \
  "s_waitcnt lgkmcnt(9)\n" \
  "v_mfma_f32_16x16x32_bf16 v[20:23], v[64:67], v[48:51], v[8:11]\n" \
  "v_mfma_f32_16x16x32_bf16 v[24:27], v[80:83], v[48:51], v[12:15]\n" \
  "ds_read_b128 v[96:99], v0 offset:16384\n" \
  "ds_read_b128 v[100:103], v0 offset:17408\n" \
  "s_waitcnt lgkmcnt(8)\n" \
  "v_mfma_f32_16x16x32_bf16 v[20:23], v[68:71], v[52:55], v[20:23]\n" \
  "v_mfma_f32_16x16x32_bf16 v[24:27], v[84:87], v[52:55], v[24:27]\n" \
  "ds_read_b128 v[104:107], v0 offset:18432\n" \
  "ds_read_b128 v[108:111], v0 offset:19456\n" \
  "s_waitcnt lgkmcnt(7)\n" \
  "v_mfma_f32_16x16x32_bf16 v[20:23], v[72:75], v[56:59], v[20:23]\n" \
  "v_mfma_f32_16x16x32_bf16 v[24:27], v[88:91], v[56:59], v[24:27]\n" \
  "ds_read_b128 v[112:115], v0 offset:20480\n" \
  "ds_read_b128 v[116:119], v0 offset:21504\n" \
  "s_waitcnt lgkmcnt(6)\n" \
  "v_mfma_f32_16x16x32_bf16 v[20:23], v[76:79], v[60:63], v[20:23]\n" \
  "v_mfma_f32_16x16x32_bf16 v[24:27], v[92:95], v[60:63], v[24:27]\n" \
  "ds_read_b128 v[120:123], v0 offset:22528\n" \
  "ds_read_b128 v[124:127], v0 offset:23552\n" \
  "s_add_u32 s67, s67, 1\n" \
  "v_add_u32 v0, s65, v0\n" \
  "v_add_u32 v6, s65, v6\n" \
  "s_sub_u32 s65, 0, s65\n" \
  "s_nop 1\n" \
  "v_max3_f32 v32, v20, v21, v22\n" \
  "v_max3_f32 v33, v23, v24, v25\n" \
  "v_max3_f32 v32, v32, v26, v27\n" \
  "v_max_f32 v32, v32, v33\n" \
  "v_mov_b32 v33, v32\n" \
  "s_nop 1\n" \
  "v_permlane16_swap_b32 v32, v33\n" \
  "v_max_f32 v32, v32, v33\n" \
  "v_mov_b32 v33, v32\n" \
  "s_nop 1\n" \
  "v_permlane32_swap_b32 v32, v33\n" \
  "v_max_f32 v32, v32, v33\n" \
  "v_mul_f32 v33, s72, v32\n" \
  "v_fma_f32 v20, v20, s71, v33\n" \
  "v_fma_f32 v21, v21, s71, v33\n" \
  "v_fma_f32 v22, v22, s71, v33\n" \
  "v_fma_f32 v23, v23, s71, v33\n" \
  "v_fma_f32 v24, v24, s71, v33\n" \
  "v_fma_f32 v25, v25, s71, v33\n" \
  "v_fma_f32 v26, v26, s71, v33\n" \
  "v_fma_f32 v27, v27, s71, v33\n" \
  "v_exp_f32 v20, v20\n" \
  "v_exp_f32 v21, v21\n" \
  "v_exp_f32 v22, v22\n" \
  "v_exp_f32 v23, v23\n" \
  "v_exp_f32 v24, v24\n" \
  "v_exp_f32 v25, v25\n" \
  "v_exp_f32 v26, v26\n" \
  "v_exp_f32 v27, v27\n" \
  "v_add_f32 v34, v20, v21\n" \
  "v_add_f32 v33, v22, v23\n" \
  "v_add_f32 v36, v24, v25\n" \
  "v_add_f32 v34, v34, v33\n" \
  "v_add_f32 v33, v26, v27\n" \
  "v_add_f32 v34, v34, v36\n" \
  "v_add_f32 v34, v34, v33\n" \
  "v_mov_b64 v[28:29], 0\n" \
  "v_mov_b64 v[30:31], 0\n" \
  "s_waitcnt vmcnt(2) lgkmcnt(0)\n" \
  "s_cmp_gt_i32 s68, -2\n" \
  "s_cbranch_scc1 .LCX_NP_%=\n" \
  "s_waitcnt vmcnt(0)\n" \
  ".LCX_NP_%=:\n" \
  "s_cmp_lg_u32 s68, 0\n" \
  "s_cbranch_scc1 .LCX_TEST_%=\n" \
  ".LCX_WF_%=:\n" \
  "s_memrealtime s[38:39]\n" \
  "s_waitcnt lgkmcnt(0)\n" \
  "s_sub_u32 s63, s69, s38\n" \
  "s_cmp_le_i32 s63, 0\n" \
  "s_cbranch_scc1 .LCX_GO_%=\n" \
  "s_cmp_lt_i32 s63, 12\n" \
  "s_cbranch_scc1 .LCX_WF_%=\n" \
  "s_sleep 3\n" \
  "s_branch .LCX_WF_%=\n" \
  ".LCX_GO_%=:\n" \
  "v_mov_b32 v35, 0\n" \
  "global_load_dwordx4 v[128:131], v5, s[48:49] offset:0 sc1\n" \
  "global_load_dwordx4 v[132:135], v5, s[48:49] offset:1024 sc1\n" \
  "global_load_dwordx4 v[136:139], v3, s[48:49] offset:0 sc1\n" \
  "global_load_dwordx4 v[140:143], v3, s[48:49] offset:512 sc1\n" \
  "global_load_dword v35, v35, s[50:51] sc1\n" \
  "global_load_dword v146, v2, s[52:53] sc1\n" \
  "s_sub_u32 s62, s62, 8\n" \
  "s_cbranch_scc1 .LCX_END_%=\n" \
  "s_waitcnt vmcnt(0)\n" \
  ".LCX_TEST_%=:\n" \
  "s_setprio 3\n" \
  "v_smfmac_f32_16x16x64_bf16 v[28:31], v[128:131], v[48:55], v1\n" \
  "v_smfmac_f32_16x16x64_bf16 v[28:31], v[132:135], v[56:63], v1\n" \
  "v_min3_u32 v33, v128, v129, v130\n" \
  "v_min3_u32 v36, v131, v132, v133\n" \
  "v_min3_u32 v33, v33, v134, v135\n" \
  "v_min3_u32 v36, v36, v136, v137\n" \
  "v_min3_u32 v33, v33, v138, v139\n" \
  "v_min3_u32 v36, v36, v140, v141\n" \
  "v_min3_u32 v33, v33, v142, v143\n" \
  "v_min3_u32 v33, v33, v36, v146\n" \
  "v_cmp_eq_u32 vcc, 0, v33\n" \
  "v_readfirstlane_b32 s64, v35\n" \
  "s_mov_b32 s68, 0\n" \
  "s_cmp_eq_u64 vcc, 0\n" \
  "s_cbranch_scc0 .LCX_FAIL_%=\n" \
  "v_permlane32_swap_b32 v28, v30\n" \
  "v_permlane32_swap_b32 v29, v31\n" \
  "v_mov_b32 v33, s67\n" \
  "ds_write_b32 v38, v33 offset:16\n" \
  "v_add_f32 v28, v28, v30\n" \
  "v_add_f32 v29, v29, v31\n" \
  "v_max3_f32 v36, v28, v29, v32\n" \
  "v_mov_b32 v33, v36\n" \
  "s_cmp_eq_u32 s64, 0\n" \
  "s_cselect_b32 s64, s38, s64\n" \
  "v_permlane16_swap_b32 v36, v33\n" \
  "v_max_f32 v36, v36, v33\n" \
  "v_mov_b32 v33, v36\n" \
  "s_sub_u32 s63, s64, s70\n" \
  "s_mov_b32 s70, s64\n" \
  "v_permlane32_swap_b32 v36, v33\n" \
  "v_max_f32 v36, v36, v33\n" \
  "v_mul_f32 v36, s72, v36\n" \
  "v_fma_f32 v32, v32, s71, v36\n" \
  "v_fma_f32 v28, v28, s71, v36\n" \
  "v_fma_f32 v29, v29, s71, v36\n" \
  "v_exp_f32 v32, v32\n" \
  "v_exp_f32 v28, v28\n" \
  "v_exp_f32 v29, v29\n" \
  "v_fma_f32 v34, v34, v32, v28\n" \
  "v_add_f32 v34, v34, v29\n" \
  "v_mov_b32 v33, v34\n" \
  "s_min_u32 s63, s63, 250\n" \
  "s_add_u32 s69, s64, s63\n" \
  "v_permlane16_swap_b32 v34, v33\n" \
  "v_add_f32 v34, v34, v33\n" \
  "v_mov_b32 v33, v34\n" \
  "s_add_u32 s69, s69, -60\n" \
  "s_add_u32 s48, s48, 0x10000\n" \
  "s_addc_u32 s49, s49, 0\n" \
  "v_permlane32_swap_b32 v34, v33\n" \
  "v_add_f32 v34, v34, v33\n" \
  "v_rcp_f32 v34, v34\n" \
  "s_add_u32 s50, s50, 1024\n" \
  "s_addc_u32 s51, s51, 0\n" \
  "s_add_u32 s52, s52, 256\n" \
  "s_addc_u32 s53, s53, 0\n" \
  "v_mul_f32 v32, v32, v34\n" \
  "v_mul_f32 v20, v20, v32\n" \
  "v_mul_f32 v21, v21, v32\n" \
  "v_mul_f32 v22, v22, v32\n" \
  "v_mul_f32 v23, v23, v32\n" \
  "v_mul_f32 v24, v24, v32\n" \
  "v_mul_f32 v25, v25, v32\n" \
  "v_mul_f32 v26, v26, v32\n" \
  "v_mul_f32 v27, v27, v32\n" \
  "v_cvt_pk_bf16_f32 v40, v20, v21\n" \
  "v_cvt_pk_bf16_f32 v41, v22, v23\n" \
  "v_cvt_pk_bf16_f32 v42, v24, v25\n" \
  "v_cvt_pk_bf16_f32 v43, v26, v27\n" \
  "v_mul_f32 v28, v28, v34\n" \
  "v_mul_f32 v29, v29, v34\n" \
  "v_mfma_f32_16x16x32_bf16 v[64:67], v[96:99], v[40:43], 0\n" \
  "v_cvt_pk_bf16_f32 v44, v28, v29\n" \
  "v_mov_b32 v45, 0\n" \
  "v_mfma_f32_16x16x32_bf16 v[68:71], v[100:103], v[40:43], 0\n" \
  "v_cvt_pk_bf16_f32 v47, v28, v29\n" \
  "v_mov_b32 v46, 0\n" \
  "v_mfma_f32_16x16x32_bf16 v[72:75], v[104:107], v[40:43], 0\n" \
  "v_permlane32_swap_b32 v44, v45\n" \
  "v_mfma_f32_16x16x32_bf16 v[76:79], v[108:111], v[40:43], 0\n" \
  "v_permlane32_swap_b32 v46, v47\n" \
  "v_mfma_f32_16x16x32_bf16 v[80:83], v[112:115], v[40:43], 0\n" \
  "v_xor_b32 v16, 4096, v7\n" \
  "v_mfma_f32_16x16x32_bf16 v[84:87], v[116:119], v[40:43], 0\n" \
  "v_xor_b32 v17, 8192, v7\n" \
  "v_mfma_f32_16x16x32_bf16 v[88:91], v[120:123], v[40:43], 0\n" \
  "v_xor_b32 v18, 12288, v7\n" \
  "v_mfma_f32_16x16x32_bf16 v[92:95], v[124:127], v[40:43], 0\n" \
  "s_memrealtime s[38:39]\n" \
  "ds_read_b32 v37, v38\n" \
  "v_mfma_f32_16x16x16_bf16 v[64:67], v[136:137], v[44:45], v[64:67]\n" \
  "v_mfma_f32_16x16x16_bf16 v[68:71], v[140:141], v[44:45], v[68:71]\n" \
  "v_mfma_f32_16x16x16_bf16 v[72:75], v[136:137], v[46:47], v[72:75]\n" \
  "v_mfma_f32_16x16x16_bf16 v[76:79], v[140:141], v[46:47], v[76:79]\n" \
  "v_mfma_f32_16x16x16_bf16 v[80:83], v[138:139], v[44:45], v[80:83]\n" \
  "v_mfma_f32_16x16x16_bf16 v[84:87], v[142:143], v[44:45], v[84:87]\n" \
  "v_mfma_f32_16x16x16_bf16 v[88:91], v[138:139], v[46:47], v[88:91]\n" \
  "v_mfma_f32_16x16x16_bf16 v[92:95], v[142:143], v[46:47], v[92:95]\n" \
  "s_nop 0\n" \
  "v_cvt_pk_bf16_f32 v160, v64, v65\n" \
  "v_cvt_pk_bf16_f32 v161, v66, v67\n" \
  "v_cvt_pk_bf16_f32 v162, v68, v69\n" \
  "v_cvt_pk_bf16_f32 v163, v70, v71\n" \
  "ds_write_b128 v7, v[160:163] offset:0\n" \
  "v_cvt_pk_bf16_f32 v164, v72, v73\n" \
  "v_cvt_pk_bf16_f32 v165, v74, v75\n" \
  "v_cvt_pk_bf16_f32 v166, v76, v77\n" \
  "v_cvt_pk_bf16_f32 v167, v78, v79\n" \
  "ds_write_b128 v7, v[164:167] offset:1024\n" \
  "v_cvt_pk_bf16_f32 v168, v80, v81\n" \
  "v_cvt_pk_bf16_f32 v169, v82, v83\n" \
  "v_cvt_pk_bf16_f32 v170, v84, v85\n" \
  "v_cvt_pk_bf16_f32 v171, v86, v87\n" \
  "ds_write_b128 v7, v[168:171] offset:2048\n" \
  "v_cvt_pk_bf16_f32 v172, v88, v89\n" \
  "v_cvt_pk_bf16_f32 v173, v90, v91\n" \
  "v_cvt_pk_bf16_f32 v174, v92, v93\n" \
  "v_cvt_pk_bf16_f32 v175, v94, v95\n" \
  "ds_write_b128 v7, v[172:175] offset:3072\n" \
  "v_xor_b32 v7, 0x4000, v7\n" \
  "v_mfma_f32_16x16x32_bf16 v[112:115], v[192:195], v[160:163], 0\n" \
  "v_mfma_f32_16x16x32_bf16 v[116:119], v[224:227], v[160:163], 0\n" \
  "s_waitcnt lgkmcnt(0)\n" \
  "s_barrier\n" \
  "ds_read_b128 v[64:67], v16 offset:0\n" \
  "ds_read_b128 v[68:71], v16 offset:1024\n" \
  "ds_read_b128 v[72:75], v16 offset:2048\n" \
  "ds_read_b128 v[76:79], v16 offset:3072\n" \
  "ds_read_b128 v[80:83], v17 offset:0\n" \
  "ds_read_b128 v[84:87], v17 offset:1024\n" \
  "ds_read_b128 v[88:91], v17 offset:2048\n" \
  "ds_read_b128 v[92:95], v17 offset:3072\n" \
  "ds_read_b128 v[96:99], v18 offset:0\n" \
  "ds_read_b128 v[100:103], v18 offset:1024\n" \
  "ds_read_b128 v[104:107], v18 offset:2048\n" \
  "ds_read_b128 v[108:111], v18 offset:3072\n" \
  "s_sub_u32 s59, s60, 1\n" \
  "s_bfe_u32 s59, s59, 0x10003\n" \
  "v_readfirstlane_b32 s73, v37\n" \
  "s_sub_u32 s63, s38, s69\n" \
  "s_bfe_u32 s64, s73, 0x80010\n" \
  "s_sub_u32 s64, s64, s67\n" \
  "s_or_b32 s63, s63, s64\n" \
  "s_bfe_u32 s64, s73, 0x80008\n" \
  "s_and_b32 s73, s73, 0xff\n" \
  "s_min_u32 s73, s73, s64\n" \
  "s_or_b32 s63, s63, s68\n" \
  "s_cmp_lt_i32 s63, 0\n" \
  "s_cbranch_scc1 .LCX_E2_%=\n" \
  "v_mov_b32 v35, 0\n" \
  "global_load_dwordx4 v[128:131], v5, s[48:49] offset:0 sc1\n" \
  "global_load_dwordx4 v[132:135], v5, s[48:49] offset:1024 sc1\n" \
  "global_load_dwordx4 v[136:139], v3, s[48:49] offset:0 sc1\n" \
  "global_load_dwordx4 v[140:143], v3, s[48:49] offset:512 sc1\n" \
  "global_load_dword v35, v35, s[50:51] sc1\n" \
  "global_load_dword v146, v2, s[52:53] sc1\n" \
  "s_mov_b32 s68, -1\n" \
  ".LCX_E2_%=:\n" \
  "v_mfma_f32_16x16x32_bf16 v[112:115], v[196:199], v[164:167], v[112:115]\n" \
  "v_mfma_f32_16x16x32_bf16 v[116:119], v[228:231], v[164:167], v[116:119]\n" \
  "v_mfma_f32_16x16x32_bf16 v[112:115], v[200:203], v[168:171], v[112:115]\n" \
  "v_mfma_f32_16x16x32_bf16 v[116:119], v[232:235], v[168:171], v[116:119]\n" \
  "v_mfma_f32_16x16x32_bf16 v[112:115], v[204:207], v[172:175], v[112:115]\n" \
  "v_mfma_f32_16x16x32_bf16 v[116:119], v[236:239], v[172:175], v[116:119]\n" \
  "s_waitcnt lgkmcnt(11)\n" \
  "v_mfma_f32_16x16x32_bf16 v[120:123], v[192:195], v[64:67], 0\n" \
  "v_mfma_f32_16x16x32_bf16 v[124:127], v[224:227], v[64:67], 0\n" \
  "s_waitcnt lgkmcnt(10)\n" \
  "v_mfma_f32_16x16x32_bf16 v[120:123], v[196:199], v[68:71], v[120:123]\n" \
  "v_mfma_f32_16x16x32_bf16 v[124:127], v[228:231], v[68:71], v[124:127]\n" \
  "s_waitcnt lgkmcnt(9)\n" \
  "v_mfma_f32_16x16x32_bf16 v[120:123], v[200:203], v[72:75], v[120:123]\n" \
  "v_mfma_f32_16x16x32_bf16 v[124:127], v[232:235], v[72:75], v[124:127]\n" \
  "s_waitcnt lgkmcnt(8)\n" \
  "v_mfma_f32_16x16x32_bf16 v[120:123], v[204:207], v[76:79], v[120:123]\n" \
  "v_mfma_f32_16x16x32_bf16 v[124:127], v[236:239], v[76:79], v[124:127]\n" \
  "s_waitcnt lgkmcnt(7)\n" \
  "v_mfma_f32_16x16x32_bf16 v[112:115], v[208:211], v[80:83], v[112:115]\n" \
  "v_mfma_f32_16x16x32_bf16 v[116:119], v[240:243], v[80:83], v[116:119]\n" \
  "s_waitcnt lgkmcnt(6)\n" \
  "v_mfma_f32_16x16x32_bf16 v[112:115], v[212:215], v[84:87], v[112:115]\n" \
  "v_mfma_f32_16x16x32_bf16 v[116:119], v[244:247], v[84:87], v[116:119]\n" \
  "s_waitcnt lgkmcnt(5)\n" \
  "v_mfma_f32_16x16x32_bf16 v[112:115], v[216:219], v[88:91], v[112:115]\n" \
  "v_mfma_f32_16x16x32_bf16 v[116:119], v[248:251], v[88:91], v[116:119]\n" \
  "s_waitcnt lgkmcnt(4)\n" \
  "v_mfma_f32_16x16x32_bf16 v[112:115], v[220:223], v[92:95], v[112:115]\n" \
  "v_mfma_f32_16x16x32_bf16 v[116:119], v[252:255], v[92:95], v[116:119]\n" \
  "s_memrealtime s[38:39]\n" \
  "ds_read_b32 v37, v38\n" \
  "s_waitcnt lgkmcnt(4)\n" \
  "v_mfma_f32_16x16x32_bf16 v[120:123], v[208:211], v[96:99], v[120:123]\n" \
  "v_mfma_f32_16x16x32_bf16 v[124:127], v[240:243], v[96:99], v[124:127]\n" \
  "s_waitcnt lgkmcnt(3)\n" \
  "v_mfma_f32_16x16x32_bf16 v[120:123], v[212:215], v[100:103], v[120:123]\n" \
  "v_mfma_f32_16x16x32_bf16 v[124:127], v[244:247], v[100:103], v[124:127]\n" \
  "v_cvt_pk_bf16_f32 v20, v112, v113\n" \
  "v_cvt_pk_bf16_f32 v21, v114, v115\n" \
  "v_cvt_pk_bf16_f32 v22, v116, v117\n" \
  "v_cvt_pk_bf16_f32 v23, v118, v119\n" \
  "v_and_or_b32 v20, v20, -2, s59\n" \
  "s_waitcnt lgkmcnt(2)\n" \
  "v_mfma_f32_16x16x32_bf16 v[120:123], v[216:219], v[104:107], v[120:123]\n" \
  "v_mfma_f32_16x16x32_bf16 v[124:127], v[248:251], v[104:107], v[124:127]\n" \
  "buffer_store_dwordx4 v[20:23], v4, s[40:43], s54 offen sc1\n" \
  "s_waitcnt lgkmcnt(0)\n" \
  "v_mfma_f32_16x16x32_bf16 v[120:123], v[220:223], v[108:111], v[120:123]\n" \
  "v_mfma_f32_16x16x32_bf16 v[124:127], v[252:255], v[108:111], v[124:127]\n" \
  "s_xor_b32 s64, s54, 16384\n" \
  "s_mov_b32 s61, 1\n" \
  "s_nop 3\n" \
  "v_cvt_pk_bf16_f32 v24, v120, v121\n" \
  "v_cvt_pk_bf16_f32 v25, v122, v123\n" \
  "v_cvt_pk_bf16_f32 v26, v124, v125\n" \
  "v_cvt_pk_bf16_f32 v27, v126, v127\n" \
  "v_and_or_b32 v24, v24, -2, s59\n" \
  "buffer_store_dwordx4 v[24:27], v4, s[40:43], s64 offen sc1\n" \
  "v_readfirstlane_b32 s73, v37\n" \
  "s_sub_u32 s63, s38, s69\n" \
  "s_bfe_u32 s64, s73, 0x80010\n" \
  "s_sub_u32 s64, s64, s67\n" \
  "s_or_b32 s63, s63, s64\n" \
  "s_bfe_u32 s64, s73, 0x80008\n" \
  "s_and_b32 s73, s73, 0xff\n" \
  "s_min_u32 s73, s73, s64\n" \
  "s_or_b32 s63, s63, s68\n" \
  "s_cmp_lt_i32 s63, 0\n" \
  "s_cbranch_scc1 .LCX_E3_%=\n" \
  "v_mov_b32 v35, 0\n" \
  "global_load_dwordx4 v[128:131], v5, s[48:49] offset:0 sc1\n" \
  "global_load_dwordx4 v[132:135], v5, s[48:49] offset:1024 sc1\n" \
  "global_load_dwordx4 v[136:139], v3, s[48:49] offset:0 sc1\n" \
  "global_load_dwordx4 v[140:143], v3, s[48:49] offset:512 sc1\n" \
  "global_load_dword v35, v35, s[50:51] sc1\n" \
  "global_load_dword v146, v2, s[52:53] sc1\n" \
  "s_mov_b32 s68, -2\n" \
  ".LCX_E3_%=:\n" \
  "s_setprio 0\n" \
  "s_add_u32 s54, s54, 0x80000\n" \
  "s_bitset0_b32 s54, 22\n" \
  "s_sub_u32 s60, s60, 1\n" \
  "s_cmp_lg_u32 s60, 0\n" \
  "s_cbranch_scc1 .LCX_TOP_%=\n" \
  "s_waitcnt vmcnt(0)\n" \
  ".LCX_END_%=:\n" \
  "s_endpgm\n" \
  ".LCX_FAIL_%=:\n" \
  "v_mov_b64 v[28:29], 0\n" \
  "v_mov_b64 v[30:31], 0\n" \
  ".LCX_FAIL2_%=:\n" \
  "s_add_u32 s69, s64, -60\n" \
  "s_cmp_lg_u32 s64, 0\n" \
  "s_cbranch_scc1 .LCX_WF_%=\n" \
  "s_sleep 4\n" \
  "global_load_dword v35, v35, s[50:51] sc1\n" \
  "s_sub_u32 s62, s62, 8\n" \
  "s_cbranch_scc1 .LCX_END_%=\n" \
  "s_waitcnt vmcnt(0)\n" \
  "v_readfirstlane_b32 s64, v35\n" \
  "s_branch .LCX_FAIL2_%=\n"
#define AW_ASM_Y \
  "s_mov_b32 s72, 0\n" \
  "s_mov_b32 s67, 0\n" \
  "s_mov_b32 s68, 2\n" \
  "s_mov_b32 s69, 0\n" \
  "s_mov_b32 s65, 49152\n" \
  "s_mov_b32 s66, 16384\n" \
  "v_mov_b32 v5, s58\n" \
  "v_mov_b32 v60, v3\n" \
  "v_add_u32 v3, 4096, v2\n" \
  "s_mov_b32 s62, 0x40000\n" \
  "s_setprio 1\n" \
  "s_branch .LCY_ASK_%=\n" \
  ".p2align 6\n" \
  ".LCY_BAR_%=:\n" \
  "s_barrier\n" \
  "s_memrealtime s[38:39]\n" \
  "ds_read_b128 v[8:11], v0 offset:0\n" \
  "ds_read_b128 v[12:15], v0 offset:1024\n" \
  "ds_read_b128 v[16:19], v0 offset:2048\n" \
  "s_sub_u32 s59, s60, 1\n" \
  "s_bfe_u32 s59, s59, 0x10003\n" \
  "s_add_u32 s68, s68, 1\n" \
  "s_add_u32 s64, s54, 16384\n" \
  "s_cmp_eq_u32 s69, 0\n" \
  "s_cbranch_scc1 .LCY_NI_%=\n" \
  "s_waitcnt vmcnt(3)\n" \
  "s_cmp_lg_u32 s72, 0\n" \
  "s_cbranch_scc1 .LCY_NJ_%=\n" \
  "s_waitcnt vmcnt(0)\n" \
  ".LCY_NJ_%=:\n" \
  "v_mov_b32 v6, s55\n" \
  "s_mov_b64 exec, 1\n" \
  "ds_add_u32 v5, v6\n" \
  "s_mov_b64 exec, -1\n" \
  "s_mov_b32 s69, 0\n" \
  ".LCY_NI_%=:\n" \
  "s_waitcnt lgkmcnt(0)\n" \
  "s_add_u32 s63, s38, 215\n" \
  "s_mov_b32 s72, 0\n" \
  "v_or_b32 v7, 1, s63\n" \
  "s_mov_b64 exec, 1\n" \
  "global_store_dword v2, v7, s[52:53] sc1\n" \
  "s_mov_b64 exec, -1\n" \
  "s_add_u32 s52, s52, 2048\n" \
  "s_addc_u32 s53, s53, 0\n" \
  "v_cmp_eq_u32 vcc, 0, v4\n" \
  "s_cmp_eq_u64 vcc, 0\n" \
  "s_cbranch_scc0 .LCY_NS_%=\n" \
  "v_lshlrev_b32 v6, 16, s55\n" \
  "s_mov_b64 exec, 1\n" \
  "ds_add_u32 v5, v6\n" \
  "s_mov_b64 exec, -1\n" \
  "s_mov_b32 m0, s56\n" \
  "s_add_u32 s71, s56, 4096\n" \
  "global_load_lds_dwordx4 v2, s[48:49] offset:0 sc1\n" \
  "global_load_lds_dwordx4 v2, s[48:49] offset:1024 sc1\n" \
  "global_load_lds_dwordx4 v2, s[48:49] offset:2048 sc1\n" \
  ".LCY_NS_%=:\n" \
  "ds_read_b128 v[20:23], v0 offset:3072\n" \
  "v_mfma_f32_16x16x32_bf16 v[32:35], v[64:67], v[8:11], 0\n" \
  "v_mfma_f32_16x16x32_bf16 v[36:39], v[96:99], v[8:11], 0\n" \
  "v_mfma_f32_16x16x32_bf16 v[40:43], v[128:131], v[8:11], 0\n" \
  "v_mfma_f32_16x16x32_bf16 v[44:47], v[160:163], v[8:11], 0\n" \
  "v_mfma_f32_16x16x32_bf16 v[48:51], v[192:195], v[8:11], 0\n" \
  "v_mfma_f32_16x16x32_bf16 v[52:55], v[224:227], v[8:11], 0\n" \
  "ds_read_b128 v[8:11], v0 offset:8192\n" \
  "v_mfma_f32_16x16x32_bf16 v[32:35], v[68:71], v[12:15], v[32:35]\n" \
  "v_mfma_f32_16x16x32_bf16 v[36:39], v[100:103], v[12:15], v[36:39]\n" \
  "v_mfma_f32_16x16x32_bf16 v[40:43], v[132:135], v[12:15], v[40:43]\n" \
  "v_mfma_f32_16x16x32_bf16 v[44:47], v[164:167], v[12:15], v[44:47]\n" \
  "v_mfma_f32_16x16x32_bf16 v[48:51], v[196:199], v[12:15], v[48:51]\n" \
  "v_mfma_f32_16x16x32_bf16 v[52:55], v[228:231], v[12:15], v[52:55]\n" \
  "s_cbranch_vccnz .LCY_D1_%=\n" \
  "global_load_lds_dwordx4 v2, s[48:49] offset:3072 sc1\n" \
  ".LCY_D1_%=:\n" \
  "ds_read_b128 v[12:15], v0 offset:9216\n" \
  "v_mfma_f32_16x16x32_bf16 v[32:35], v[72:75], v[16:19], v[32:35]\n" \
  "v_mfma_f32_16x16x32_bf16 v[36:39], v[104:107], v[16:19], v[36:39]\n" \
  "v_mfma_f32_16x16x32_bf16 v[40:43], v[136:139], v[16:19], v[40:43]\n" \
  "v_mfma_f32_16x16x32_bf16 v[44:47], v[168:171], v[16:19], v[44:47]\n" \
  "v_mfma_f32_16x16x32_bf16 v[48:51], v[200:203], v[16:19], v[48:51]\n" \
  "v_mfma_f32_16x16x32_bf16 v[52:55], v[232:235], v[16:19], v[52:55]\n" \
  "s_cbranch_vccnz .LCY_D2_%=\n" \
  "s_mov_b32 m0, s71\n" \
  "s_add_u32 s56, s56, s65\n" \
  "global_load_lds_dwordx4 v3, s[48:49] offset:0 sc1\n" \
  ".LCY_D2_%=:\n" \
  "ds_read_b128 v[16:19], v0 offset:10240\n" \
  "s_waitcnt lgkmcnt(3)\n" \
  "v_mfma_f32_16x16x32_bf16 v[32:35], v[76:79], v[20:23], v[32:35]\n" \
  "v_mfma_f32_16x16x32_bf16 v[36:39], v[108:111], v[20:23], v[36:39]\n" \
  "v_mfma_f32_16x16x32_bf16 v[40:43], v[140:143], v[20:23], v[40:43]\n" \
  "v_mfma_f32_16x16x32_bf16 v[44:47], v[172:175], v[20:23], v[44:47]\n" \
  "v_mfma_f32_16x16x32_bf16 v[48:51], v[204:207], v[20:23], v[48:51]\n" \
  "v_mfma_f32_16x16x32_bf16 v[52:55], v[236:239], v[20:23], v[52:55]\n" \
  "s_cbranch_vccnz .LCY_D3_%=\n" \
  "global_load_lds_dwordx4 v3, s[48:49] offset:1024 sc1\n" \
  ".LCY_D3_%=:\n" \
  "ds_read_b128 v[20:23], v0 offset:11264\n" \
  "s_waitcnt lgkmcnt(3)\n" \
  "v_mfma_f32_16x16x32_bf16 v[32:35], v[80:83], v[8:11], v[32:35]\n" \
  "v_mfma_f32_16x16x32_bf16 v[36:39], v[112:115], v[8:11], v[36:39]\n" \
  "v_mfma_f32_16x16x32_bf16 v[40:43], v[144:147], v[8:11], v[40:43]\n" \
  "v_mfma_f32_16x16x32_bf16 v[44:47], v[176:179], v[8:11], v[44:47]\n" \
  "v_mfma_f32_16x16x32_bf16 v[48:51], v[208:211], v[8:11], v[48:51]\n" \
  "v_mfma_f32_16x16x32_bf16 v[52:55], v[240:243], v[8:11], v[52:55]\n" \
  "s_cbranch_vccnz .LCY_D4_%=\n" \
  "global_load_lds_dwordx4 v3, s[48:49] offset:2048 sc1\n" \
  ".LCY_D4_%=:\n" \
  "ds_read_b128 v[8:11], v0 offset:4096\n" \
  "s_waitcnt lgkmcnt(3)\n" \
  "v_mfma_f32_16x16x32_bf16 v[32:35], v[84:87], v[12:15], v[32:35]\n" \
  "v_mfma_f32_16x16x32_bf16 v[36:39], v[116:119], v[12:15], v[36:39]\n" \
  "v_mfma_f32_16x16x32_bf16 v[40:43], v[148:151], v[12:15], v[40:43]\n" \
  "v_mfma_f32_16x16x32_bf16 v[44:47], v[180:183], v[12:15], v[44:47]\n" \
  "v_mfma_f32_16x16x32_bf16 v[48:51], v[212:215], v[12:15], v[48:51]\n" \
  "v_mfma_f32_16x16x32_bf16 v[52:55], v[244:247], v[12:15], v[52:55]\n" \
  "s_cbranch_vccnz .LCY_D5_%=\n" \
  "global_load_lds_dwordx4 v3, s[48:49] offset:3072 sc1\n" \
  "s_add_u32 s48, s48, 0x80000\n" \
  "s_addc_u32 s49, s49, 0\n" \
  ".LCY_D5_%=:\n" \
  "ds_read_b128 v[12:15], v0 offset:5120\n" \
  "s_waitcnt lgkmcnt(3)\n" \
  "v_mfma_f32_16x16x32_bf16 v[32:35], v[88:91], v[16:19], v[32:35]\n" \
  "v_mfma_f32_16x16x32_bf16 v[36:39], v[120:123], v[16:19], v[36:39]\n" \
  "v_mfma_f32_16x16x32_bf16 v[40:43], v[152:155], v[16:19], v[40:43]\n" \
  "v_mfma_f32_16x16x32_bf16 v[44:47], v[184:187], v[16:19], v[44:47]\n" \
  "v_mfma_f32_16x16x32_bf16 v[48:51], v[216:219], v[16:19], v[48:51]\n" \
  "v_mfma_f32_16x16x32_bf16 v[52:55], v[248:251], v[16:19], v[52:55]\n" \
  "s_cbranch_vccnz .LCY_D6_%=\n" \
  "s_mov_b32 m0, s57\n" \
  "s_add_u32 s57, s57, s65\n" \
  "global_load_lds_dwordx4 v2, s[50:51] offset:0 sc1\n" \
  ".LCY_D6_%=:\n" \
  "ds_read_b128 v[16:19], v0 offset:6144\n" \
  "s_waitcnt lgkmcnt(3)\n" \
  "v_mfma_f32_16x16x32_bf16 v[32:35], v[92:95], v[20:23], v[32:35]\n" \
  "v_mfma_f32_16x16x32_bf16 v[36:39], v[124:127], v[20:23], v[36:39]\n" \
  "v_mfma_f32_16x16x32_bf16 v[40:43], v[156:159], v[20:23], v[40:43]\n" \
  "v_mfma_f32_16x16x32_bf16 v[44:47], v[188:191], v[20:23], v[44:47]\n" \
  "v_mfma_f32_16x16x32_bf16 v[48:51], v[220:223], v[20:23], v[48:51]\n" \
  "v_mfma_f32_16x16x32_bf16 v[52:55], v[252:255], v[20:23], v[52:55]\n" \
  "ds_read_b128 v[20:23], v0 offset:7168\n" \
  "s_waitcnt lgkmcnt(3)\n" \
  "s_nop 1\n" \
  "v_cvt_pk_bf16_f32 v24, v32, v33\n" \
  "v_cvt_pk_bf16_f32 v25, v34, v35\n" \
  "v_cvt_pk_bf16_f32 v26, v36, v37\n" \
  "v_cvt_pk_bf16_f32 v27, v38, v39\n" \
  "s_nop 0\n" \
  "v_mfma_f32_16x16x32_bf16 v[32:35], v[64:67], v[8:11], 0\n" \
  "v_mfma_f32_16x16x32_bf16 v[36:39], v[96:99], v[8:11], 0\n" \
  "v_and_or_b32 v24, v24, -2, s59\n" \
  "buffer_store_dwordx4 v[24:27], v1, s[40:43], s54 offen offset:0 sc1\n" \
  "v_cvt_pk_bf16_f32 v28, v40, v41\n" \
  "v_cvt_pk_bf16_f32 v29, v42, v43\n" \
  "v_cvt_pk_bf16_f32 v30, v44, v45\n" \
  "v_cvt_pk_bf16_f32 v31, v46, v47\n" \
  "s_nop 0\n" \
  "v_mfma_f32_16x16x32_bf16 v[40:43], v[128:131], v[8:11], 0\n" \
  "v_mfma_f32_16x16x32_bf16 v[44:47], v[160:163], v[8:11], 0\n" \
  "v_and_or_b32 v28, v28, -2, s59\n" \
  "buffer_store_dwordx4 v[28:31], v1, s[40:43], s54 offen offset:128 sc1\n" \
  "v_cvt_pk_bf16_f32 v56, v48, v49\n" \
  "v_cvt_pk_bf16_f32 v57, v50, v51\n" \
  "v_cvt_pk_bf16_f32 v58, v52, v53\n" \
  "v_cvt_pk_bf16_f32 v59, v54, v55\n" \
  "s_nop 0\n" \
  "v_mfma_f32_16x16x32_bf16 v[48:51], v[192:195], v[8:11], 0\n" \
  "v_mfma_f32_16x16x32_bf16 v[52:55], v[224:227], v[8:11], 0\n" \
  "v_and_or_b32 v56, v56, -2, s59\n" \
  "buffer_store_dwordx4 v[56:59], v1, s[40:43], s54 offen offset:256 sc1\n" \
  "ds_read_b128 v[8:11], v0 offset:12288\n" \
  "s_waitcnt lgkmcnt(3)\n" \
  "v_mfma_f32_16x16x32_bf16 v[32:35], v[68:71], v[12:15], v[32:35]\n" \
  "v_mfma_f32_16x16x32_bf16 v[36:39], v[100:103], v[12:15], v[36:39]\n" \
  "v_mfma_f32_16x16x32_bf16 v[40:43], v[132:135], v[12:15], v[40:43]\n" \
  "v_mfma_f32_16x16x32_bf16 v[44:47], v[164:167], v[12:15], v[44:47]\n" \
  "v_mfma_f32_16x16x32_bf16 v[48:51], v[196:199], v[12:15], v[48:51]\n" \
  "v_mfma_f32_16x16x32_bf16 v[52:55], v[228:231], v[12:15], v[52:55]\n" \
  "s_cbranch_vccnz .LCY_D7_%=\n" \
  "global_load_lds_dwordx4 v2, s[50:51] offset:1024 sc1\n" \
  "s_sub_u32 s65, 0, s65\n" \
  "s_add_u32 s67, s67, 4096\n" \
  ".LCY_D7_%=:\n" \
  "ds_read_b128 v[12:15], v0 offset:13312\n" \
  "s_waitcnt lgkmcnt(3)\n" \
  "v_mfma_f32_16x16x32_bf16 v[32:35], v[72:75], v[16:19], v[32:35]\n" \
  "v_mfma_f32_16x16x32_bf16 v[36:39], v[104:107], v[16:19], v[36:39]\n" \
  "v_mfma_f32_16x16x32_bf16 v[40:43], v[136:139], v[16:19], v[40:43]\n" \
  "v_mfma_f32_16x16x32_bf16 v[44:47], v[168:171], v[16:19], v[44:47]\n" \
  "v_mfma_f32_16x16x32_bf16 v[48:51], v[200:203], v[16:19], v[48:51]\n" \
  "v_mfma_f32_16x16x32_bf16 v[52:55], v[232:235], v[16:19], v[52:55]\n" \
  "s_cbranch_vccnz .LCY_D8_%=\n" \
  "global_load_lds_dwordx4 v2, s[50:51] offset:2048 sc1\n" \
  "s_sub_u32 s68, s68, 1\n" \
  "s_mov_b32 s69, 1\n" \
  ".LCY_D8_%=:\n" \
  "ds_read_b128 v[16:19], v0 offset:14336\n" \
  "s_waitcnt lgkmcnt(3)\n" \
  "v_mfma_f32_16x16x32_bf16 v[32:35], v[76:79], v[20:23], v[32:35]\n" \
  "v_mfma_f32_16x16x32_bf16 v[36:39], v[108:111], v[20:23], v[36:39]\n" \
  "v_mfma_f32_16x16x32_bf16 v[40:43], v[140:143], v[20:23], v[40:43]\n" \
  "v_mfma_f32_16x16x32_bf16 v[44:47], v[172:175], v[20:23], v[44:47]\n" \
  "v_mfma_f32_16x16x32_bf16 v[48:51], v[204:207], v[20:23], v[48:51]\n" \
  "v_mfma_f32_16x16x32_bf16 v[52:55], v[236:239], v[20:23], v[52:55]\n" \
  "s_cbranch_vccnz .LCY_D9_%=\n" \
  "global_load_lds_dwordx4 v2, s[50:51] offset:3072 sc1\n" \
  "s_add_u32 s50, s50, 0x40000\n" \
  "s_addc_u32 s51, s51, 0\n" \
  ".LCY_D9_%=:\n" \
  "ds_read_b128 v[20:23], v0 offset:15360\n" \
  "s_waitcnt lgkmcnt(3)\n" \
  "v_mfma_f32_16x16x32_bf16 v[32:35], v[80:83], v[8:11], v[32:35]\n" \
  "v_mfma_f32_16x16x32_bf16 v[36:39], v[112:115], v[8:11], v[36:39]\n" \
  "v_mfma_f32_16x16x32_bf16 v[40:43], v[144:147], v[8:11], v[40:43]\n" \
  "v_mfma_f32_16x16x32_bf16 v[44:47], v[176:179], v[8:11], v[44:47]\n" \
  "v_mfma_f32_16x16x32_bf16 v[48:51], v[208:211], v[8:11], v[48:51]\n" \
  "v_mfma_f32_16x16x32_bf16 v[52:55], v[240:243], v[8:11], v[52:55]\n" \
  "s_waitcnt lgkmcnt(2)\n" \
  "v_mfma_f32_16x16x32_bf16 v[32:35], v[84:87], v[12:15], v[32:35]\n" \
  "v_mfma_f32_16x16x32_bf16 v[36:39], v[116:119], v[12:15], v[36:39]\n" \
  "v_mfma_f32_16x16x32_bf16 v[40:43], v[148:151], v[12:15], v[40:43]\n" \
  "v_mfma_f32_16x16x32_bf16 v[44:47], v[180:183], v[12:15], v[44:47]\n" \
  "v_mfma_f32_16x16x32_bf16 v[48:51], v[212:215], v[12:15], v[48:51]\n" \
  "v_mfma_f32_16x16x32_bf16 v[52:55], v[244:247], v[12:15], v[52:55]\n" \
  "s_waitcnt lgkmcnt(1)\n" \
  "v_mfma_f32_16x16x32_bf16 v[32:35], v[88:91], v[16:19], v[32:35]\n" \
  "v_mfma_f32_16x16x32_bf16 v[36:39], v[120:123], v[16:19], v[36:39]\n" \
  "v_mfma_f32_16x16x32_bf16 v[40:43], v[152:155], v[16:19], v[40:43]\n" \
  "v_mfma_f32_16x16x32_bf16 v[44:47], v[184:187], v[16:19], v[44:47]\n" \
  "v_mfma_f32_16x16x32_bf16 v[48:51], v[216:219], v[16:19], v[48:51]\n" \
  "v_mfma_f32_16x16x32_bf16 v[52:55], v[248:251], v[16:19], v[52:55]\n" \
  "s_cbranch_vccnz .LCY_NT_%=\n" \
  "v_mov_b32 v4, 0\n" \
  "buffer_load_dword v4, v60, s[44:47], s67 offen sc1\n" \
  ".LCY_NT_%=:\n" \
  "s_waitcnt lgkmcnt(0)\n" \
  "v_mfma_f32_16x16x32_bf16 v[32:35], v[92:95], v[20:23], v[32:35]\n" \
  "v_mfma_f32_16x16x32_bf16 v[36:39], v[124:127], v[20:23], v[36:39]\n" \
  "v_mfma_f32_16x16x32_bf16 v[40:43], v[156:159], v[20:23], v[40:43]\n" \
  "v_mfma_f32_16x16x32_bf16 v[44:47], v[188:191], v[20:23], v[44:47]\n" \
  "v_mfma_f32_16x16x32_bf16 v[48:51], v[220:223], v[20:23], v[48:51]\n" \
  "v_mfma_f32_16x16x32_bf16 v[52:55], v[252:255], v[20:23], v[52:55]\n" \
  "v_add_u32 v0, s66, v0\n" \
  "s_sub_u32 s66, 0, s66\n" \
  "s_add_u32 s54, s54, 0x80000\n" \
  "s_bitset0_b32 s54, 22\n" \
  "s_mov_b32 s62, 0x40000\n" \
  "s_nop 0\n" \
  "v_cvt_pk_bf16_f32 v24, v32, v33\n" \
  "v_cvt_pk_bf16_f32 v25, v34, v35\n" \
  "v_cvt_pk_bf16_f32 v26, v36, v37\n" \
  "v_cvt_pk_bf16_f32 v27, v38, v39\n" \
  "v_and_or_b32 v24, v24, -2, s59\n" \
  "buffer_store_dwordx4 v[24:27], v1, s[40:43], s64 offen offset:0 sc1\n" \
  "v_cvt_pk_bf16_f32 v28, v40, v41\n" \
  "v_cvt_pk_bf16_f32 v29, v42, v43\n" \
  "v_cvt_pk_bf16_f32 v30, v44, v45\n" \
  "v_cvt_pk_bf16_f32 v31, v46, v47\n" \
  "v_and_or_b32 v28, v28, -2, s59\n" \
  "buffer_store_dwordx4 v[28:31], v1, s[40:43], s64 offen offset:128 sc1\n" \
  "v_cvt_pk_bf16_f32 v56, v48, v49\n" \
  "v_cvt_pk_bf16_f32 v57, v50, v51\n" \
  "v_cvt_pk_bf16_f32 v58, v52, v53\n" \
  "v_cvt_pk_bf16_f32 v59, v54, v55\n" \
  "v_and_or_b32 v56, v56, -2, s59\n" \
  "buffer_store_dwordx4 v[56:59], v1, s[40:43], s64 offen offset:256 sc1\n" \
  "s_cbranch_vccnz .LCY_ND_%=\n" \
  "s_cmp_eq_u32 s68, 0\n" \
  "s_cselect_b32 s72, 1, 0\n" \
  "s_cbranch_scc1 .LCY_DEF_%=\n" \
  "s_waitcnt vmcnt(0)\n" \
  "v_mov_b32 v6, s55\n" \
  "s_mov_b64 exec, 1\n" \
  "ds_add_u32 v5, v6\n" \
  "s_mov_b64 exec, -1\n" \
  "s_mov_b32 s69, 0\n" \
  "s_branch .LCY_WC_%=\n" \
  ".LCY_ND_%=:\n" \
  "v_mov_b32 v4, 0\n" \
  "buffer_load_dword v4, v60, s[44:47], s67 offen sc1\n" \
  "s_waitcnt vmcnt(0)\n" \
  ".LCY_WC_%=:\n" \
  ".LCY_DEF_%=:\n" \
  "s_sub_u32 s60, s60, 1\n" \
  "s_cmp_eq_u32 s60, 0\n" \
  "s_cbranch_scc1 .LCY_END_%=\n" \
  ".LCY_NEED_%=:\n" \
  "s_cmp_eq_u32 s68, 0\n" \
  "s_cbranch_scc1 .LCY_BAR_%=\n" \
  "v_cmp_eq_u32 vcc, 0, v4\n" \
  "s_cmp_eq_u64 vcc, 0\n" \
  "s_cbranch_scc1 .LCY_GET_%=\n" \
  "s_cmp_ge_u32 s67, 0x40000\n" \
  "s_cbranch_scc1 .LCY_BAR_%=\n" \
  "s_sleep 4\n" \
  "ds_read_b32 v6, v5 offset:16\n" \
  "s_waitcnt lgkmcnt(0)\n" \
  "v_readfirstlane_b32 s63, v6\n" \
  "s_add_u32 s63, s63, s60\n" \
  "s_cmp_ge_u32 s63, 66\n" \
  "s_cbranch_scc1 .LCY_BAR_%=\n" \
  ".LCY_ASK_%=:\n" \
  "v_mov_b32 v4, 0\n" \
  "buffer_load_dword v4, v60, s[44:47], s67 offen sc1\n" \
  "s_sub_u32 s62, s62, 1\n" \
  "s_cbranch_scc1 .LCY_END_%=\n" \
  "s_waitcnt vmcnt(0)\n" \
  "s_branch .LCY_NEED_%=\n" \
  ".LCY_GET_%=:\n" \
  "v_lshlrev_b32 v6, 16, s55\n" \
  "s_mov_b64 exec, 1\n" \
  "ds_add_u32 v5, v6\n" \
  "s_mov_b64 exec, -1\n" \
  "s_mov_b32 m0, s56\n" \
  "s_add_u32 s71, s56, 4096\n" \
  "global_load_lds_dwordx4 v2, s[48:49] offset:0 sc1\n" \
  "global_load_lds_dwordx4 v2, s[48:49] offset:1024 sc1\n" \
  "global_load_lds_dwordx4 v2, s[48:49] offset:2048 sc1\n" \
  "global_load_lds_dwordx4 v2, s[48:49] offset:3072 sc1\n" \
  "s_mov_b32 m0, s71\n" \
  "s_add_u32 s56, s56, s65\n" \
  "global_load_lds_dwordx4 v3, s[48:49] offset:0 sc1\n" \
  "global_load_lds_dwordx4 v3, s[48:49] offset:1024 sc1\n" \
  "global_load_lds_dwordx4 v3, s[48:49] offset:2048 sc1\n" \
  "global_load_lds_dwordx4 v3, s[48:49] offset:3072 sc1\n" \
  "s_add_u32 s48, s48, 0x80000\n" \
  "s_addc_u32 s49, s49, 0\n" \
  "s_mov_b32 m0, s57\n" \
  "s_add_u32 s57, s57, s65\n" \
  "global_load_lds_dwordx4 v2, s[50:51] offset:0 sc1\n" \
  "global_load_lds_dwordx4 v2, s[50:51] offset:1024 sc1\n" \
  "s_sub_u32 s65, 0, s65\n" \
  "s_add_u32 s67, s67, 4096\n" \
  "global_load_lds_dwordx4 v2, s[50:51] offset:2048 sc1\n" \
  "s_sub_u32 s68, s68, 1\n" \
  "s_mov_b32 s69, 1\n" \
  "global_load_lds_dwordx4 v2, s[50:51] offset:3072 sc1\n" \
  "s_add_u32 s50, s50, 0x40000\n" \
  "s_addc_u32 s51, s51, 0\n" \
  "v_mov_b32 v4, 0\n" \
  "buffer_load_dword v4, v60, s[44:47], s67 offen sc1\n" \
  "s_mov_b32 s61, 0\n" \
  "s_cmp_eq_u32 s68, 0\n" \
  "s_cbranch_scc1 .LCY_BAR_%=\n" \
  "s_waitcnt vmcnt(0)\n" \
  "v_mov_b32 v6, s55\n" \
  "s_mov_b64 exec, 1\n" \
  "ds_add_u32 v5, v6\n" \
  "s_mov_b64 exec, -1\n" \
  "s_mov_b32 s69, 0\n" \
  "s_branch .LCY_NEED_%=\n" \
  ".LCY_END_%=:\n" \
  "s_endpgm\n"
template <bool AT>
__device__ __forceinline__ void role_aw_t(const MegaArgs& A, char* smem, const int L, const int a, const u64 tag) {
  const int w = A.wv;
  const int l = lane_id(), c = l & 15, g = l >> 4;
  const int qt = w & 1, hh = (w >> 1) & 1, h = 2 * a + hh;
  char* ws = A.ws;
  unsigned* FL = (unsigned*)(ws + OFF_FL);
  const unsigned sb = lds_base(smem);
  if (w == 0 && l < 8) reinterpret_cast<unsigned*>(smem + AW_CNT)[l] = 0u;
  constexpr int NP = AT ? 1 : 3;
  const int cp0 = AT ? w : 3 * w - 8;
  v8u wo[2 * NP][4];
  {
    const int q = (l >> 2) & 3, p = l & 3, f = q | ((g & 1) << 2);
    const unsigned ra = sb + (4 * g + q) * 256 + (p & 1) * 16;
#define PW_CLEAR(n)                                                                                                         \
  _Pragma("unroll") for (int i = 0; i < 16; ++i)                                                                            \
    ST128((v4u{0u, 0u, 0u, 0u}), MKRS(ws + OFF_PW, SZ_PW), l * 16 + (i & 3) * 1024,                                         \
          ((((n) >> RGB) * 64 + ((n) & ((1 << RGB) - 1))) * 32 + 2 * a + qt) * 16384 + (i >> 2) * 4096);
    if (EF && AT && L && PC0 == 0) { PW_CLEAR(hh) }
#pragma unroll
    for (int r = 0; r < 2; ++r) {
#pragma unroll
      for (int k = 0; k < 2; ++k) {
        const char* base_ = (const char*)A.Wo + ((size_t)((2 * a + r) * DH + 64 * (w & 1) + 32 * k) * DM + 128 * (w >> 1)) * 2;
#pragma unroll
        for (int i = 0; i < 8; ++i) {
          const int fd = g | ((i & 1) << 2);
          glds16(base_, (unsigned)((4 * i + g) * (DM * 2) + ((c ^ (fd << 1)) << 4)), sb + w * 16384 + k * 8192 + i * 1024, WNT & 16);
        }
      }
      SST(4 + 3 * r);
      if (EF && AT && L && PC0 == 1 + r) { PW_CLEAR(hh) }
      if (r == 1) ready_wait(A, tag, ready_look(A));
      if (EF && AT && L && PC0 == 1 && r == 0) WAIT_VM(16);
      else WAIT_VM(0);
      if (EF && AT && r == (PC0 != 0) && L) {
        if (r == 0) ready_wait(A, tag, ready_look(A));
        PW_FREE(0, hh, 2 * a + qt);
      }
      RAW_BAR();
      SST(5 + 3 * r);
#pragma unroll
      for (int k2 = 0; k2 < 2; ++k2) {
#pragma unroll
        for (int ct = 0; ct < 2 * NP; ++ct) {
          const int cp = cp0 + (ct >> 1);
          const unsigned a_ = ra + (cp >> 2) * 32768 + k2 * 16384 + ((2 * (cp & 3) + (p >> 1)) ^ f) * 32 + (ct & 1) * 8;
          const v2u r0 = trd(a_), r1 = trd(a_ + 4096), r2 = trd(a_ + 8192), r3 = trd(a_ + 12288);
          wo[ct][2 * r + k2] = v8u{r0.x, r0.y, r1.x, r1.y, r2.x, r2.y, r3.x, r3.y};
          asm volatile("" : "+v"(wo[ct][2 * r + k2]));
        }
      }
      RAW_BAR();
      SST(6 + 3 * r);
    }
  }
  WSTAMP2();
  if (EF && L) {
    const unsigned t0 = now10();
#pragma unroll 1
    for (int n = 2 + (w >> 1); n < (2 << RGB); n += 4) {
      gate(t0 + PCT + (n - 2) * PCD);
      PW_CLEAR(n)
      DRAIN();
      PW_FREE(n >> RGB, n & ((1 << RGB) - 1), 2 * a + qt);
    }
  }
  const v4u rsW = mk_srd(ws + OFF_PW, SZ_PW);
  const int dp = (FL_FP - FL_FQ + (L * 16 + a - L * 64 * 32 - h) * 32 + 2 * hh) * 4;
  const v4u rsF = mk_srd(FL + FL_FQ + (L * 64 * 32 + h) * 32, (qt ? 0 : dp) + 64 * 4096 - 128);
  const char* p0 = AT ? ws + OFF_MEMK + L * SZ_MEM + h * 2048 : ws + OFF_QK + L * SZ_QK + ((size_t)h * 16 + 8 * qt) * 1024;
  const char* p1 = AT ? (const char*)(FL + FL_FK + L * 32 + (h >> 3) * 64 + (h & 7) * 2) : ws + OFF_VF + L * SZ_O + ((size_t)h * 8 + 4 * qt) * 1024;
  const unsigned* pFW = EF && AT ? FL + FL_FD + L * 32 : FL + FL_FW + (L * 64 * 16 + (a >> 2)) * 32 + (a & 3) + (EF ? (w & 3) * 128 : 0);
  const unsigned tk = sb + AW_TK + hh * 24 * 1024, voff = (c >> 1) * 2048 + (c & 1) * 64 + g * 16;
  const char* dbg = ws + WS_TOTAL + A.vb * 64 * 8;
#define AW_SOPS                                                                                                                 \
  "{s[40:43]}"(rsW), "{s[44:47]}"(rsF), "{s[48:49]}"(p0), "{s[50:51]}"(p1), "{s[52:53]}"(pFW),                                   \
      "{s54}"((L * 64 * 16 + a) * 32768 + cp0 * 128 + (AT ? qt * 16384 : 0)), "{s55}"(1 << (8 * qt)), "{s56}"(tk + qt * 8192), "{s57}"(tk + 16384 + qt * 4096),                  \
      "{s58}"(sb + AW_CNT + 8 + 4 * hh), "{s59}"(sb + AW_CNT), "{s60}"(NSEG), "{s[36:37]}"(dbg)                                  \
      : "memory", "vcc", "scc", "s80"
  if constexpr (AT) {
    const v8u pv = {tk + l * 16, (c & 8) ? 0xEEEEu : 0x4444u, (l & 31) * 4u, (unsigned)(OFF_MEMV - OFF_MEMK) + c * 32 + (g & 1) * 16 + (g >> 1) * 1024,
                    voff, (unsigned)l * 16, tk + qt * 4096 + l * 16, sb + AW_SO + w * 4096 + l * 16};
    const unsigned neg = 0xff61b1e6u;
    v8u bt;
#pragma unroll
    for (int i = 0; i < 4; ++i) {
      const unsigned d = 4 * g + i > c ? neg : 0u;
      bt[i] = qt ? 0u : d;
      bt[4 + i] = qt ? d : neg;
    }
    asm volatile("s_mov_b64 exec, -1\ns_waitcnt lgkmcnt(0)\ns_nop 4\n" AW_ASM_X ".LKG_LAST:\n"
                 ::"{v[192:199]}"(wo[0][0]), "{v[200:207]}"(wo[0][1]), "{v[208:215]}"(wo[0][2]), "{v[216:223]}"(wo[0][3]),
                 "{v[224:231]}"(wo[1][0]), "{v[232:239]}"(wo[1][1]), "{v[240:247]}"(wo[1][2]), "{v[248:255]}"(wo[1][3]),
                 "{v[0:7]}"(pv), "{v[8:15]}"(bt), AW_SOPS);
  } else {
    const v4u pv = {sb + AW_SO + l * 16, voff, (unsigned)l * 16, (l & 2) ? 16u + 8 * qt : qt ? 8u * (l & 1) : dp + 4 * (l & 1)};
    asm volatile("s_mov_b64 exec, -1\ns_waitcnt vmcnt(0) lgkmcnt(0)\ns_nop 4\n" AW_ASM_Y
                 ::"{v[64:71]}"(wo[0][0]), "{v[72:79]}"(wo[0][1]), "{v[80:87]}"(wo[0][2]), "{v[88:95]}"(wo[0][3]),
                 "{v[96:103]}"(wo[1][0]), "{v[104:111]}"(wo[1][1]), "{v[112:119]}"(wo[1][2]), "{v[120:127]}"(wo[1][3]),
                 "{v[128:135]}"(wo[2][0]), "{v[136:143]}"(wo[2][1]), "{v[144:151]}"(wo[2][2]), "{v[152:159]}"(wo[2][3]),
                 "{v[160:167]}"(wo[3][0]), "{v[168:175]}"(wo[3][1]), "{v[176:183]}"(wo[3][2]), "{v[184:191]}"(wo[3][3]),
                 "{v[192:199]}"(wo[4][0]), "{v[200:207]}"(wo[4][1]), "{v[208:215]}"(wo[4][2]), "{v[216:223]}"(wo[4][3]),
                 "{v[224:231]}"(wo[5][0]), "{v[232:239]}"(wo[5][1]), "{v[240:247]}"(wo[5][2]), "{v[248:255]}"(wo[5][3]),
                 "{v[0:3]}"(pv), AW_SOPS);
  }
#undef AW_SOPS
}
__device__ __forceinline__ void role_aw(const MegaArgs& A, char* smem, const int L, const int a, const u64 tag) {
  if (A.wv < 4) role_aw_t<true>(A, smem, L, a, tag);
  else role_aw_t<false>(A, smem, L, a, tag);
}

#define P_ASM \
  "s_mov_b32 s68, 0\n" \
  "s_mov_b32 s66, 3\n" \
  "s_mov_b32 s67, 2\n" \
  "s_mov_b32 s72, 0\n" \
  "v_mov_b32 v7, 1\n" \
  "v_mov_b32 v6, s70\n" \
  "v_mov_b32 v5, 0\n" \
  "v_add_u32 v10, 4096, v2\n" \
  ".p2align 6\n" \
  ".LCP_TOP_%=:\n" \
  "ds_read_b128 v[28:31], v1 offset:0\n" \
  "ds_read_b128 v[32:35], v1 offset:1024\n" \
  "s_add_u32 s66, s66, 3\n" \
  "s_add_u32 s67, s67, 2\n" \
  "s_mov_b32 s69, 0x400000\n" \
  "v_xor_b32 v0, 0x8000, v0\n" \
  "v_xor_b32 v3, 0x100, v3\n" \
  "v_readfirstlane_b32 s71, v5\n" \
  "s_cmp_ge_u32 s71, s66\n" \
  "s_cbranch_scc1 .LCP_GO_%=\n" \
  "s_cmp_eq_u32 s68, 1\n" \
  "s_cbranch_scc0 .LCP_X1_%=\n" \
  "s_waitcnt vmcnt(0)\n" \
  "s_mov_b64 exec, 1\n" \
  "buffer_store_dword v7, off, s[56:59], s72 sc1\n" \
  "s_mov_b64 exec, -1\n" \
  "s_add_u32 s72, s72, 0x1000\n" \
  "s_mov_b32 s68, 0\n" \
  ".LCP_X1_%=:\n" \
  ".LCP_S2_%=:\n" \
  "s_sleep 1\n" \
  "ds_read_b32 v5, v6 offset:0\n" \
  "s_sub_u32 s69, s69, 1\n" \
  "s_cbranch_scc1 .LCP_END_%=\n" \
  "s_waitcnt lgkmcnt(0)\n" \
  "v_readfirstlane_b32 s71, v5\n" \
  "s_cmp_ge_u32 s71, s66\n" \
  "s_cbranch_scc1 .LCP_GO_%=\n" \
  "s_branch .LCP_S2_%=\n" \
  ".LCP_GO_%=:\n" \
  "ds_read_b128 v[12:15], v0 offset:0\n" \
  "ds_read_b128 v[16:19], v0 offset:2048\n" \
  "ds_read_b128 v[20:23], v0 offset:4096\n" \
  "ds_read_b128 v[24:27], v0 offset:6144\n" \
  "ds_read_b128 v[36:39], v1 offset:2048\n" \
  "s_waitcnt lgkmcnt(4)\n" \
  "v_mfma_f32_16x16x32_bf16 v[48:51], v[64:67], v[12:15], 0\n" \
  "v_mfma_f32_16x16x32_bf16 v[52:55], v[128:131], v[12:15], 0\n" \
  "v_mfma_f32_16x16x32_bf16 v[56:59], v[192:195], v[12:15], 0\n" \
  "v_mfma_f32_16x16x32_bf16 v[60:63], v[28:31], v[12:15], 0\n" \
  "ds_read_b128 v[12:15], v0 offset:8192\n" \
  "ds_read_b128 v[28:31], v1 offset:3072\n" \
  "s_waitcnt lgkmcnt(5)\n" \
  "v_mfma_f32_16x16x32_bf16 v[48:51], v[68:71], v[16:19], v[48:51]\n" \
  "v_mfma_f32_16x16x32_bf16 v[52:55], v[132:135], v[16:19], v[52:55]\n" \
  "v_mfma_f32_16x16x32_bf16 v[56:59], v[196:199], v[16:19], v[56:59]\n" \
  "v_mfma_f32_16x16x32_bf16 v[60:63], v[32:35], v[16:19], v[60:63]\n" \
  "ds_read_b128 v[16:19], v0 offset:10240\n" \
  "ds_read_b128 v[32:35], v1 offset:4096\n" \
  "s_waitcnt lgkmcnt(6)\n" \
  "v_mfma_f32_16x16x32_bf16 v[48:51], v[72:75], v[20:23], v[48:51]\n" \
  "v_mfma_f32_16x16x32_bf16 v[52:55], v[136:139], v[20:23], v[52:55]\n" \
  "v_mfma_f32_16x16x32_bf16 v[56:59], v[200:203], v[20:23], v[56:59]\n" \
  "s_waitcnt lgkmcnt(4)\n" \
  "v_mfma_f32_16x16x32_bf16 v[60:63], v[36:39], v[20:23], v[60:63]\n" \
  "ds_read_b128 v[20:23], v0 offset:12288\n" \
  "ds_read_b128 v[36:39], v1 offset:5120\n" \
  "v_mfma_f32_16x16x32_bf16 v[48:51], v[76:79], v[24:27], v[48:51]\n" \
  "v_mfma_f32_16x16x32_bf16 v[52:55], v[140:143], v[24:27], v[52:55]\n" \
  "v_mfma_f32_16x16x32_bf16 v[56:59], v[204:207], v[24:27], v[56:59]\n" \
  "s_waitcnt lgkmcnt(4)\n" \
  "v_mfma_f32_16x16x32_bf16 v[60:63], v[28:31], v[24:27], v[60:63]\n" \
  "ds_read_b128 v[24:27], v0 offset:14336\n" \
  "ds_read_b128 v[28:31], v1 offset:6144\n" \
  "v_mfma_f32_16x16x32_bf16 v[48:51], v[80:83], v[12:15], v[48:51]\n" \
  "v_mfma_f32_16x16x32_bf16 v[52:55], v[144:147], v[12:15], v[52:55]\n" \
  "v_mfma_f32_16x16x32_bf16 v[56:59], v[208:211], v[12:15], v[56:59]\n" \
  "s_waitcnt lgkmcnt(4)\n" \
  "v_mfma_f32_16x16x32_bf16 v[60:63], v[32:35], v[12:15], v[60:63]\n" \
  "ds_read_b128 v[12:15], v0 offset:16384\n" \
  "ds_read_b128 v[32:35], v1 offset:7168\n" \
  "v_mfma_f32_16x16x32_bf16 v[48:51], v[84:87], v[16:19], v[48:51]\n" \
  "v_mfma_f32_16x16x32_bf16 v[52:55], v[148:151], v[16:19], v[52:55]\n" \
  "v_mfma_f32_16x16x32_bf16 v[56:59], v[212:215], v[16:19], v[56:59]\n" \
  "s_waitcnt lgkmcnt(4)\n" \
  "v_mfma_f32_16x16x32_bf16 v[60:63], v[36:39], v[16:19], v[60:63]\n" \
  "ds_read_b128 v[16:19], v0 offset:18432\n" \
  "ds_read_b128 v[36:39], v1 offset:8192\n" \
  "v_mfma_f32_16x16x32_bf16 v[48:51], v[88:91], v[20:23], v[48:51]\n" \
  "v_mfma_f32_16x16x32_bf16 v[52:55], v[152:155], v[20:23], v[52:55]\n" \
  "v_mfma_f32_16x16x32_bf16 v[56:59], v[216:219], v[20:23], v[56:59]\n" \
  "s_waitcnt lgkmcnt(4)\n" \
  "v_mfma_f32_16x16x32_bf16 v[60:63], v[28:31], v[20:23], v[60:63]\n" \
  "ds_read_b128 v[20:23], v0 offset:20480\n" \
  "ds_read_b128 v[28:31], v1 offset:9216\n" \
  "v_mfma_f32_16x16x32_bf16 v[48:51], v[92:95], v[24:27], v[48:51]\n" \
  "v_mfma_f32_16x16x32_bf16 v[52:55], v[156:159], v[24:27], v[52:55]\n" \
  "v_mfma_f32_16x16x32_bf16 v[56:59], v[220:223], v[24:27], v[56:59]\n" \
  "s_waitcnt lgkmcnt(4)\n" \
  "v_mfma_f32_16x16x32_bf16 v[60:63], v[32:35], v[24:27], v[60:63]\n" \
  "ds_read_b128 v[24:27], v0 offset:22528\n" \
  "ds_read_b128 v[32:35], v1 offset:10240\n" \
  "v_mfma_f32_16x16x32_bf16 v[48:51], v[96:99], v[12:15], v[48:51]\n" \
  "v_mfma_f32_16x16x32_bf16 v[52:55], v[160:163], v[12:15], v[52:55]\n" \
  "v_mfma_f32_16x16x32_bf16 v[56:59], v[224:227], v[12:15], v[56:59]\n" \
  "s_waitcnt lgkmcnt(4)\n" \
  "v_mfma_f32_16x16x32_bf16 v[60:63], v[36:39], v[12:15], v[60:63]\n" \
  "ds_read_b128 v[12:15], v0 offset:24576\n" \
  "ds_read_b128 v[36:39], v1 offset:11264\n" \
  "v_mfma_f32_16x16x32_bf16 v[48:51], v[100:103], v[16:19], v[48:51]\n" \
  "v_mfma_f32_16x16x32_bf16 v[52:55], v[164:167], v[16:19], v[52:55]\n" \
  "v_mfma_f32_16x16x32_bf16 v[56:59], v[228:231], v[16:19], v[56:59]\n" \
  "s_waitcnt lgkmcnt(4)\n" \
  "v_mfma_f32_16x16x32_bf16 v[60:63], v[28:31], v[16:19], v[60:63]\n" \
  "ds_read_b32 v5, v6 offset:16\n" \
  "ds_read_b128 v[16:19], v0 offset:26624\n" \
  "ds_read_b128 v[28:31], v1 offset:12288\n" \
  "v_mfma_f32_16x16x32_bf16 v[48:51], v[104:107], v[20:23], v[48:51]\n" \
  "v_mfma_f32_16x16x32_bf16 v[52:55], v[168:171], v[20:23], v[52:55]\n" \
  "v_mfma_f32_16x16x32_bf16 v[56:59], v[232:235], v[20:23], v[56:59]\n" \
  "s_waitcnt lgkmcnt(5)\n" \
  "v_mfma_f32_16x16x32_bf16 v[60:63], v[32:35], v[20:23], v[60:63]\n" \
  "ds_read_b128 v[20:23], v0 offset:28672\n" \
  "ds_read_b128 v[32:35], v1 offset:13312\n" \
  "v_mfma_f32_16x16x32_bf16 v[48:51], v[108:111], v[24:27], v[48:51]\n" \
  "v_mfma_f32_16x16x32_bf16 v[52:55], v[172:175], v[24:27], v[52:55]\n" \
  "v_mfma_f32_16x16x32_bf16 v[56:59], v[236:239], v[24:27], v[56:59]\n" \
  "s_waitcnt lgkmcnt(5)\n" \
  "v_mfma_f32_16x16x32_bf16 v[60:63], v[36:39], v[24:27], v[60:63]\n" \
  "s_cmp_eq_u32 s68, 1\n" \
  "s_cbranch_scc0 .LCP_X3_%=\n" \
  "s_waitcnt vmcnt(0)\n" \
  "s_mov_b64 exec, 1\n" \
  "buffer_store_dword v7, off, s[56:59], s72 sc1\n" \
  "s_mov_b64 exec, -1\n" \
  "s_add_u32 s72, s72, 0x1000\n" \
  "s_mov_b32 s68, 0\n" \
  ".LCP_X3_%=:\n" \
  "s_waitcnt lgkmcnt(4)\n" \
  "v_readfirstlane_b32 s71, v5\n" \
  "s_cmp_ge_u32 s71, s67\n" \
  "s_cbranch_scc1 .LCP_ST_%=\n" \
  ".LCP_S4_%=:\n" \
  "s_sleep 1\n" \
  "ds_read_b32 v5, v6 offset:16\n" \
  "s_sub_u32 s69, s69, 1\n" \
  "s_cbranch_scc1 .LCP_END_%=\n" \
  "s_waitcnt lgkmcnt(0)\n" \
  "v_readfirstlane_b32 s71, v5\n" \
  "s_cmp_ge_u32 s71, s67\n" \
  "s_cbranch_scc1 .LCP_ST_%=\n" \
  "s_branch .LCP_S4_%=\n" \
  ".LCP_ST_%=:\n" \
  "ds_read_b128 v[24:27], v0 offset:30720\n" \
  "ds_read_b128 v[36:39], v1 offset:14336\n" \
  "ds_read_b128 v[40:43], v4 offset:0\n" \
  "ds_read_b128 v[44:47], v4 offset:64\n" \
  "ds_read2_b32 v[8:9], v3 offset0:0 offset1:32\n" \
  "v_mfma_f32_16x16x32_bf16 v[48:51], v[112:115], v[12:15], v[48:51]\n" \
  "v_mfma_f32_16x16x32_bf16 v[52:55], v[176:179], v[12:15], v[52:55]\n" \
  "v_mfma_f32_16x16x32_bf16 v[56:59], v[240:243], v[12:15], v[56:59]\n" \
  "s_waitcnt lgkmcnt(7)\n" \
  "v_mfma_f32_16x16x32_bf16 v[60:63], v[28:31], v[12:15], v[60:63]\n" \
  "ds_read_b128 v[12:15], v0 offset:256\n" \
  "ds_read_b128 v[28:31], v1 offset:15360\n" \
  "v_mfma_f32_16x16x32_bf16 v[48:51], v[116:119], v[16:19], v[48:51]\n" \
  "v_mfma_f32_16x16x32_bf16 v[52:55], v[180:183], v[16:19], v[52:55]\n" \
  "v_mfma_f32_16x16x32_bf16 v[56:59], v[244:247], v[16:19], v[56:59]\n" \
  "s_waitcnt lgkmcnt(7)\n" \
  "v_mfma_f32_16x16x32_bf16 v[60:63], v[32:35], v[16:19], v[60:63]\n" \
  "ds_read_b128 v[16:19], v0 offset:2304\n" \
  "ds_read_b128 v[32:35], v4 offset:128\n" \
  "v_mfma_f32_16x16x32_bf16 v[48:51], v[120:123], v[20:23], v[48:51]\n" \
  "s_waitcnt lgkmcnt(4)\n" \
  "v_mul_f32 v40, v40, v8\n" \
  "v_mul_f32 v41, v41, v8\n" \
  "v_mfma_f32_16x16x32_bf16 v[52:55], v[184:187], v[20:23], v[52:55]\n" \
  "v_mul_f32 v42, v42, v8\n" \
  "v_mul_f32 v43, v43, v8\n" \
  "v_mfma_f32_16x16x32_bf16 v[56:59], v[248:251], v[20:23], v[56:59]\n" \
  "v_mul_f32 v44, v44, v8\n" \
  "v_mul_f32 v45, v45, v8\n" \
  "v_mfma_f32_16x16x32_bf16 v[60:63], v[36:39], v[20:23], v[60:63]\n" \
  "v_mul_f32 v46, v46, v8\n" \
  "v_mul_f32 v47, v47, v8\n" \
  "ds_read_b128 v[20:23], v0 offset:4352\n" \
  "ds_read_b128 v[36:39], v4 offset:192\n" \
  "v_mfma_f32_16x16x32_bf16 v[48:51], v[124:127], v[24:27], v[48:51]\n" \
  "v_mfma_f32_16x16x32_bf16 v[52:55], v[188:191], v[24:27], v[52:55]\n" \
  "v_mfma_f32_16x16x32_bf16 v[56:59], v[252:255], v[24:27], v[56:59]\n" \
  "s_waitcnt lgkmcnt(2)\n" \
  "v_mul_f32 v32, v32, v8\n" \
  "v_mul_f32 v33, v33, v8\n" \
  "v_mfma_f32_16x16x32_bf16 v[60:63], v[28:31], v[24:27], v[60:63]\n" \
  "v_mul_f32 v34, v34, v8\n" \
  "v_mul_f32 v35, v35, v8\n" \
  "ds_read_b128 v[28:31], v1 offset:0\n" \
  "ds_read_b128 v[24:27], v0 offset:6400\n" \
  "v_fmac_f32 v40, v48, v9\n" \
  "v_fmac_f32 v41, v49, v9\n" \
  "v_fmac_f32 v42, v50, v9\n" \
  "v_fmac_f32 v43, v51, v9\n" \
  "v_mfma_f32_16x16x32_bf16 v[48:51], v[64:67], v[12:15], 0\n" \
  "v_fmac_f32 v44, v52, v9\n" \
  "v_fmac_f32 v45, v53, v9\n" \
  "v_fmac_f32 v46, v54, v9\n" \
  "v_fmac_f32 v47, v55, v9\n" \
  "v_mfma_f32_16x16x32_bf16 v[52:55], v[128:131], v[12:15], 0\n" \
  "v_cvt_pk_bf16_f32 v40, v40, v41\n" \
  "v_cvt_pk_bf16_f32 v41, v42, v43\n" \
  "v_cvt_pk_bf16_f32 v42, v44, v45\n" \
  "v_cvt_pk_bf16_f32 v43, v46, v47\n" \
  "v_fmac_f32 v32, v56, v9\n" \
  "v_fmac_f32 v33, v57, v9\n" \
  "v_fmac_f32 v34, v58, v9\n" \
  "v_fmac_f32 v35, v59, v9\n" \
  "v_mfma_f32_16x16x32_bf16 v[56:59], v[192:195], v[12:15], 0\n" \
  "s_waitcnt lgkmcnt(2)\n" \
  "v_mul_f32 v36, v36, v8\n" \
  "v_mul_f32 v37, v37, v8\n" \
  "v_mul_f32 v38, v38, v8\n" \
  "v_mul_f32 v39, v39, v8\n" \
  "v_fmac_f32 v36, v60, v9\n" \
  "v_fmac_f32 v37, v61, v9\n" \
  "v_fmac_f32 v38, v62, v9\n" \
  "v_fmac_f32 v39, v63, v9\n" \
  "s_waitcnt lgkmcnt(1)\n" \
  "v_mfma_f32_16x16x32_bf16 v[60:63], v[28:31], v[12:15], 0\n" \
  "v_cvt_pk_bf16_f32 v44, v32, v33\n" \
  "v_cvt_pk_bf16_f32 v45, v34, v35\n" \
  "v_cvt_pk_bf16_f32 v46, v36, v37\n" \
  "v_cvt_pk_bf16_f32 v47, v38, v39\n" \
  "ds_read_b128 v[32:35], v1 offset:1024\n" \
  "ds_read_b128 v[36:39], v1 offset:2048\n" \
  "ds_read_b128 v[12:15], v0 offset:8448\n" \
  "ds_read_b128 v[28:31], v1 offset:3072\n" \
  "v_mfma_f32_16x16x32_bf16 v[48:51], v[68:71], v[16:19], v[48:51]\n" \
  "buffer_store_dwordx4 v[40:43], v2, s[40:43], s65 offen offset:0 sc1\n" \
  "v_mfma_f32_16x16x32_bf16 v[52:55], v[132:135], v[16:19], v[52:55]\n" \
  "buffer_store_dwordx4 v[44:47], v2, s[40:43], s65 offen offset:1024 sc1\n" \
  "v_mfma_f32_16x16x32_bf16 v[56:59], v[196:199], v[16:19], v[56:59]\n" \
  "s_waitcnt lgkmcnt(3)\n" \
  "v_mfma_f32_16x16x32_bf16 v[60:63], v[32:35], v[16:19], v[60:63]\n" \
  "ds_read_b128 v[16:19], v0 offset:10496\n" \
  "ds_read_b128 v[32:35], v1 offset:4096\n" \
  "v_mfma_f32_16x16x32_bf16 v[48:51], v[72:75], v[20:23], v[48:51]\n" \
  "v_mfma_f32_16x16x32_bf16 v[52:55], v[136:139], v[20:23], v[52:55]\n" \
  "v_mfma_f32_16x16x32_bf16 v[56:59], v[200:203], v[20:23], v[56:59]\n" \
  "s_waitcnt lgkmcnt(4)\n" \
  "v_mfma_f32_16x16x32_bf16 v[60:63], v[36:39], v[20:23], v[60:63]\n" \
  "ds_read_b128 v[20:23], v0 offset:12544\n" \
  "ds_read_b128 v[36:39], v1 offset:5120\n" \
  "v_mfma_f32_16x16x32_bf16 v[48:51], v[76:79], v[24:27], v[48:51]\n" \
  "v_mfma_f32_16x16x32_bf16 v[52:55], v[140:143], v[24:27], v[52:55]\n" \
  "v_mfma_f32_16x16x32_bf16 v[56:59], v[204:207], v[24:27], v[56:59]\n" \
  "s_waitcnt lgkmcnt(4)\n" \
  "v_mfma_f32_16x16x32_bf16 v[60:63], v[28:31], v[24:27], v[60:63]\n" \
  "ds_read_b128 v[24:27], v0 offset:14592\n" \
  "ds_read_b128 v[28:31], v1 offset:6144\n" \
  "v_mfma_f32_16x16x32_bf16 v[48:51], v[80:83], v[12:15], v[48:51]\n" \
  "v_mfma_f32_16x16x32_bf16 v[52:55], v[144:147], v[12:15], v[52:55]\n" \
  "v_mfma_f32_16x16x32_bf16 v[56:59], v[208:211], v[12:15], v[56:59]\n" \
  "s_waitcnt lgkmcnt(4)\n" \
  "v_mfma_f32_16x16x32_bf16 v[60:63], v[32:35], v[12:15], v[60:63]\n" \
  "ds_read_b128 v[12:15], v0 offset:16640\n" \
  "ds_read_b128 v[32:35], v1 offset:7168\n" \
  "v_mfma_f32_16x16x32_bf16 v[48:51], v[84:87], v[16:19], v[48:51]\n" \
  "v_mfma_f32_16x16x32_bf16 v[52:55], v[148:151], v[16:19], v[52:55]\n" \
  "v_mfma_f32_16x16x32_bf16 v[56:59], v[212:215], v[16:19], v[56:59]\n" \
  "s_waitcnt lgkmcnt(4)\n" \
  "v_mfma_f32_16x16x32_bf16 v[60:63], v[36:39], v[16:19], v[60:63]\n" \
  "ds_read_b128 v[16:19], v0 offset:18688\n" \
  "ds_read_b128 v[36:39], v1 offset:8192\n" \
  "v_mfma_f32_16x16x32_bf16 v[48:51], v[88:91], v[20:23], v[48:51]\n" \
  "v_mfma_f32_16x16x32_bf16 v[52:55], v[152:155], v[20:23], v[52:55]\n" \
  "v_mfma_f32_16x16x32_bf16 v[56:59], v[216:219], v[20:23], v[56:59]\n" \
  "s_waitcnt lgkmcnt(4)\n" \
  "v_mfma_f32_16x16x32_bf16 v[60:63], v[28:31], v[20:23], v[60:63]\n" \
  "ds_read_b128 v[20:23], v0 offset:20736\n" \
  "ds_read_b128 v[28:31], v1 offset:9216\n" \
  "v_mfma_f32_16x16x32_bf16 v[48:51], v[92:95], v[24:27], v[48:51]\n" \
  "v_mfma_f32_16x16x32_bf16 v[52:55], v[156:159], v[24:27], v[52:55]\n" \
  "v_mfma_f32_16x16x32_bf16 v[56:59], v[220:223], v[24:27], v[56:59]\n" \
  "s_waitcnt lgkmcnt(4)\n" \
  "v_mfma_f32_16x16x32_bf16 v[60:63], v[32:35], v[24:27], v[60:63]\n" \
  "ds_read_b128 v[24:27], v0 offset:22784\n" \
  "ds_read_b128 v[32:35], v1 offset:10240\n" \
  "v_mfma_f32_16x16x32_bf16 v[48:51], v[96:99], v[12:15], v[48:51]\n" \
  "v_mfma_f32_16x16x32_bf16 v[52:55], v[160:163], v[12:15], v[52:55]\n" \
  "v_mfma_f32_16x16x32_bf16 v[56:59], v[224:227], v[12:15], v[56:59]\n" \
  "s_waitcnt lgkmcnt(4)\n" \
  "v_mfma_f32_16x16x32_bf16 v[60:63], v[36:39], v[12:15], v[60:63]\n" \
  "ds_read_b128 v[12:15], v0 offset:24832\n" \
  "ds_read_b128 v[36:39], v1 offset:11264\n" \
  "v_mfma_f32_16x16x32_bf16 v[48:51], v[100:103], v[16:19], v[48:51]\n" \
  "v_mfma_f32_16x16x32_bf16 v[52:55], v[164:167], v[16:19], v[52:55]\n" \
  "v_mfma_f32_16x16x32_bf16 v[56:59], v[228:231], v[16:19], v[56:59]\n" \
  "s_waitcnt lgkmcnt(4)\n" \
  "v_mfma_f32_16x16x32_bf16 v[60:63], v[28:31], v[16:19], v[60:63]\n" \
  "ds_read_b128 v[16:19], v0 offset:26880\n" \
  "ds_read_b128 v[28:31], v1 offset:12288\n" \
  "v_mfma_f32_16x16x32_bf16 v[48:51], v[104:107], v[20:23], v[48:51]\n" \
  "v_mfma_f32_16x16x32_bf16 v[52:55], v[168:171], v[20:23], v[52:55]\n" \
  "v_mfma_f32_16x16x32_bf16 v[56:59], v[232:235], v[20:23], v[56:59]\n" \
  "s_waitcnt lgkmcnt(4)\n" \
  "v_mfma_f32_16x16x32_bf16 v[60:63], v[32:35], v[20:23], v[60:63]\n" \
  "ds_read_b128 v[20:23], v0 offset:28928\n" \
  "ds_read_b128 v[32:35], v1 offset:13312\n" \
  "v_mfma_f32_16x16x32_bf16 v[48:51], v[108:111], v[24:27], v[48:51]\n" \
  "v_mfma_f32_16x16x32_bf16 v[52:55], v[172:175], v[24:27], v[52:55]\n" \
  "v_mfma_f32_16x16x32_bf16 v[56:59], v[236:239], v[24:27], v[56:59]\n" \
  "s_waitcnt lgkmcnt(4)\n" \
  "v_mfma_f32_16x16x32_bf16 v[60:63], v[36:39], v[24:27], v[60:63]\n" \
  "ds_read_b128 v[24:27], v0 offset:30976\n" \
  "ds_read_b128 v[36:39], v1 offset:14336\n" \
  "ds_read_b128 v[40:43], v4 offset:0\n" \
  "ds_read_b128 v[44:47], v4 offset:64\n" \
  "ds_read2_b32 v[8:9], v3 offset0:16 offset1:48\n" \
  "v_mfma_f32_16x16x32_bf16 v[48:51], v[112:115], v[12:15], v[48:51]\n" \
  "v_mfma_f32_16x16x32_bf16 v[52:55], v[176:179], v[12:15], v[52:55]\n" \
  "v_mfma_f32_16x16x32_bf16 v[56:59], v[240:243], v[12:15], v[56:59]\n" \
  "s_waitcnt lgkmcnt(7)\n" \
  "v_mfma_f32_16x16x32_bf16 v[60:63], v[28:31], v[12:15], v[60:63]\n" \
  "ds_read_b128 v[28:31], v1 offset:15360\n" \
  "v_mfma_f32_16x16x32_bf16 v[48:51], v[116:119], v[16:19], v[48:51]\n" \
  "v_mfma_f32_16x16x32_bf16 v[52:55], v[180:183], v[16:19], v[52:55]\n" \
  "v_mfma_f32_16x16x32_bf16 v[56:59], v[244:247], v[16:19], v[56:59]\n" \
  "s_waitcnt lgkmcnt(6)\n" \
  "v_mfma_f32_16x16x32_bf16 v[60:63], v[32:35], v[16:19], v[60:63]\n" \
  "ds_read_b128 v[32:35], v4 offset:128\n" \
  "v_mfma_f32_16x16x32_bf16 v[48:51], v[120:123], v[20:23], v[48:51]\n" \
  "s_waitcnt lgkmcnt(2)\n" \
  "v_mul_f32 v40, v40, v8\n" \
  "v_mul_f32 v41, v41, v8\n" \
  "v_mfma_f32_16x16x32_bf16 v[52:55], v[184:187], v[20:23], v[52:55]\n" \
  "v_mul_f32 v42, v42, v8\n" \
  "v_mul_f32 v43, v43, v8\n" \
  "v_mfma_f32_16x16x32_bf16 v[56:59], v[248:251], v[20:23], v[56:59]\n" \
  "v_mul_f32 v44, v44, v8\n" \
  "v_mul_f32 v45, v45, v8\n" \
  "v_mfma_f32_16x16x32_bf16 v[60:63], v[36:39], v[20:23], v[60:63]\n" \
  "v_mul_f32 v46, v46, v8\n" \
  "v_mul_f32 v47, v47, v8\n" \
  "ds_read_b128 v[36:39], v4 offset:192\n" \
  "s_mov_b64 exec, 1\n" \
  "ds_add_u32 v6, v7 offset:8\n" \
  "s_mov_b64 exec, -1\n" \
  "v_xor_b32 v6, 4, v6\n" \
  "v_mfma_f32_16x16x32_bf16 v[48:51], v[124:127], v[24:27], v[48:51]\n" \
  "v_mfma_f32_16x16x32_bf16 v[52:55], v[188:191], v[24:27], v[52:55]\n" \
  "v_mfma_f32_16x16x32_bf16 v[56:59], v[252:255], v[24:27], v[56:59]\n" \
  "s_waitcnt lgkmcnt(2)\n" \
  "v_mul_f32 v32, v32, v8\n" \
  "v_mul_f32 v33, v33, v8\n" \
  "v_mfma_f32_16x16x32_bf16 v[60:63], v[28:31], v[24:27], v[60:63]\n" \
  "v_mul_f32 v34, v34, v8\n" \
  "v_mul_f32 v35, v35, v8\n" \
  "v_fmac_f32 v40, v48, v9\n" \
  "v_fmac_f32 v41, v49, v9\n" \
  "v_fmac_f32 v42, v50, v9\n" \
  "v_fmac_f32 v43, v51, v9\n" \
  "v_fmac_f32 v44, v52, v9\n" \
  "v_fmac_f32 v45, v53, v9\n" \
  "v_fmac_f32 v46, v54, v9\n" \
  "v_fmac_f32 v47, v55, v9\n" \
  "ds_read_b32 v5, v6 offset:0\n" \
  "v_cvt_pk_bf16_f32 v40, v40, v41\n" \
  "v_cvt_pk_bf16_f32 v41, v42, v43\n" \
  "v_cvt_pk_bf16_f32 v42, v44, v45\n" \
  "v_cvt_pk_bf16_f32 v43, v46, v47\n" \
  "buffer_store_dwordx4 v[40:43], v10, s[40:43], s65 offen offset:0 sc1\n" \
  "v_fmac_f32 v32, v56, v9\n" \
  "v_fmac_f32 v33, v57, v9\n" \
  "v_fmac_f32 v34, v58, v9\n" \
  "v_fmac_f32 v35, v59, v9\n" \
  "s_waitcnt lgkmcnt(2)\n" \
  "v_mul_f32 v36, v36, v8\n" \
  "v_mul_f32 v37, v37, v8\n" \
  "v_mul_f32 v38, v38, v8\n" \
  "v_mul_f32 v39, v39, v8\n" \
  "v_fmac_f32 v36, v60, v9\n" \
  "v_fmac_f32 v37, v61, v9\n" \
  "v_fmac_f32 v38, v62, v9\n" \
  "v_fmac_f32 v39, v63, v9\n" \
  "v_cvt_pk_bf16_f32 v44, v32, v33\n" \
  "v_cvt_pk_bf16_f32 v45, v34, v35\n" \
  "v_cvt_pk_bf16_f32 v46, v36, v37\n" \
  "v_cvt_pk_bf16_f32 v47, v38, v39\n" \
  "buffer_store_dwordx4 v[44:47], v10, s[40:43], s65 offen offset:1024 sc1\n" \
  "s_waitcnt lgkmcnt(0)\n" \
  "s_add_u32 s65, s65, 0x80000\n" \
  "s_sub_u32 s64, s64, 1\n" \
  "s_mov_b32 s68, 1\n" \
  "s_cmp_lg_u32 s64, 0\n" \
  "s_cbranch_scc1 .LCP_TOP_%=\n" \
  "s_waitcnt vmcnt(0)\n" \
  "s_mov_b64 exec, 1\n" \
  "buffer_store_dword v7, off, s[56:59], s72 sc1\n" \
  "s_mov_b64 exec, -1\n" \
  "s_add_u32 s72, s72, 0x1000\n" \
  "s_mov_b32 s68, 0\n" \
  ".LCP_END_%=:\n" \
  "s_endpgm\n"
#define V_ASM \
  "s_mov_b32 s68, 0\n" \
  "s_mov_b32 s66, 3\n" \
  "s_mov_b32 s67, 2\n" \
  "s_mov_b32 s72, 0\n" \
  "v_mov_b32 v7, 1\n" \
  "v_mov_b32 v6, s70\n" \
  "v_mov_b32 v5, 0\n" \
  "ds_read_b32 v8, v4 offset:0\n" \
  "ds_read_b32 v9, v4 offset:64\n" \
  "ds_read_b32 v10, v4 offset:128\n" \
  "ds_read_b32 v4, v4 offset:192\n" \
  "s_waitcnt lgkmcnt(0)\n" \
  ".p2align 6\n" \
  ".LCW_TOP_%=:\n" \
  "ds_read_b128 v[28:31], v1 offset:0\n" \
  "ds_read_b128 v[32:35], v1 offset:1024\n" \
  "s_add_u32 s66, s66, 3\n" \
  "s_add_u32 s67, s67, 2\n" \
  "s_mov_b32 s69, 0x400000\n" \
  "v_xor_b32 v0, 0x8000, v0\n" \
  "v_xor_b32 v3, 0x100, v3\n" \
  "v_readfirstlane_b32 s71, v5\n" \
  "s_cmp_ge_u32 s71, s66\n" \
  "s_cbranch_scc1 .LCW_GO_%=\n" \
  "s_cmp_eq_u32 s68, 1\n" \
  "s_cbranch_scc0 .LCW_X1_%=\n" \
  "s_waitcnt vmcnt(0)\n" \
  "s_mov_b64 exec, 1\n" \
  "buffer_store_dword v7, off, s[56:59], s72 sc1\n" \
  "s_mov_b64 exec, -1\n" \
  "s_add_u32 s72, s72, 0x1000\n" \
  "s_mov_b32 s68, 0\n" \
  ".LCW_X1_%=:\n" \
  ".LCW_S2_%=:\n" \
  "s_sleep 1\n" \
  "ds_read_b32 v5, v6 offset:0\n" \
  "s_sub_u32 s69, s69, 1\n" \
  "s_cbranch_scc1 .LCW_END_%=\n" \
  "s_waitcnt lgkmcnt(0)\n" \
  "v_readfirstlane_b32 s71, v5\n" \
  "s_cmp_ge_u32 s71, s66\n" \
  "s_cbranch_scc1 .LCW_GO_%=\n" \
  "s_branch .LCW_S2_%=\n" \
  ".LCW_GO_%=:\n" \
  "ds_read_b128 v[12:15], v0 offset:0\n" \
  "ds_read_b128 v[16:19], v0 offset:2048\n" \
  "ds_read_b128 v[20:23], v0 offset:4096\n" \
  "ds_read_b128 v[24:27], v0 offset:6144\n" \
  "ds_read_b128 v[36:39], v1 offset:2048\n" \
  "s_waitcnt lgkmcnt(4)\n" \
  "v_mfma_f32_16x16x32_bf16 v[42:45], v[12:15], v[64:67], 0\n" \
  "v_mfma_f32_16x16x32_bf16 v[48:51], v[12:15], v[128:131], 0\n" \
  "v_mfma_f32_16x16x32_bf16 v[54:57], v[12:15], v[192:195], 0\n" \
  "v_mfma_f32_16x16x32_bf16 v[60:63], v[12:15], v[28:31], 0\n" \
  "ds_read_b128 v[12:15], v0 offset:8192\n" \
  "ds_read_b128 v[28:31], v1 offset:3072\n" \
  "s_waitcnt lgkmcnt(5)\n" \
  "v_mfma_f32_16x16x32_bf16 v[42:45], v[16:19], v[68:71], v[42:45]\n" \
  "v_mfma_f32_16x16x32_bf16 v[48:51], v[16:19], v[132:135], v[48:51]\n" \
  "v_mfma_f32_16x16x32_bf16 v[54:57], v[16:19], v[196:199], v[54:57]\n" \
  "v_mfma_f32_16x16x32_bf16 v[60:63], v[16:19], v[32:35], v[60:63]\n" \
  "ds_read_b128 v[16:19], v0 offset:10240\n" \
  "ds_read_b128 v[32:35], v1 offset:4096\n" \
  "s_waitcnt lgkmcnt(6)\n" \
  "v_mfma_f32_16x16x32_bf16 v[42:45], v[20:23], v[72:75], v[42:45]\n" \
  "v_mfma_f32_16x16x32_bf16 v[48:51], v[20:23], v[136:139], v[48:51]\n" \
  "v_mfma_f32_16x16x32_bf16 v[54:57], v[20:23], v[200:203], v[54:57]\n" \
  "s_waitcnt lgkmcnt(4)\n" \
  "v_mfma_f32_16x16x32_bf16 v[60:63], v[20:23], v[36:39], v[60:63]\n" \
  "ds_read_b128 v[20:23], v0 offset:12288\n" \
  "ds_read_b128 v[36:39], v1 offset:5120\n" \
  "v_mfma_f32_16x16x32_bf16 v[42:45], v[24:27], v[76:79], v[42:45]\n" \
  "v_mfma_f32_16x16x32_bf16 v[48:51], v[24:27], v[140:143], v[48:51]\n" \
  "v_mfma_f32_16x16x32_bf16 v[54:57], v[24:27], v[204:207], v[54:57]\n" \
  "s_waitcnt lgkmcnt(4)\n" \
  "v_mfma_f32_16x16x32_bf16 v[60:63], v[24:27], v[28:31], v[60:63]\n" \
  "ds_read_b128 v[24:27], v0 offset:14336\n" \
  "ds_read_b128 v[28:31], v1 offset:6144\n" \
  "v_mfma_f32_16x16x32_bf16 v[42:45], v[12:15], v[80:83], v[42:45]\n" \
  "v_mfma_f32_16x16x32_bf16 v[48:51], v[12:15], v[144:147], v[48:51]\n" \
  "v_mfma_f32_16x16x32_bf16 v[54:57], v[12:15], v[208:211], v[54:57]\n" \
  "s_waitcnt lgkmcnt(4)\n" \
  "v_mfma_f32_16x16x32_bf16 v[60:63], v[12:15], v[32:35], v[60:63]\n" \
  "ds_read_b128 v[12:15], v0 offset:16384\n" \
  "ds_read_b128 v[32:35], v1 offset:7168\n" \
  "v_mfma_f32_16x16x32_bf16 v[42:45], v[16:19], v[84:87], v[42:45]\n" \
  "v_mfma_f32_16x16x32_bf16 v[48:51], v[16:19], v[148:151], v[48:51]\n" \
  "v_mfma_f32_16x16x32_bf16 v[54:57], v[16:19], v[212:215], v[54:57]\n" \
  "s_waitcnt lgkmcnt(4)\n" \
  "v_mfma_f32_16x16x32_bf16 v[60:63], v[16:19], v[36:39], v[60:63]\n" \
  "ds_read_b128 v[16:19], v0 offset:18432\n" \
  "ds_read_b128 v[36:39], v1 offset:8192\n" \
  "v_mfma_f32_16x16x32_bf16 v[42:45], v[20:23], v[88:91], v[42:45]\n" \
  "v_mfma_f32_16x16x32_bf16 v[48:51], v[20:23], v[152:155], v[48:51]\n" \
  "v_mfma_f32_16x16x32_bf16 v[54:57], v[20:23], v[216:219], v[54:57]\n" \
  "s_waitcnt lgkmcnt(4)\n" \
  "v_mfma_f32_16x16x32_bf16 v[60:63], v[20:23], v[28:31], v[60:63]\n" \
  "ds_read_b128 v[20:23], v0 offset:20480\n" \
  "ds_read_b128 v[28:31], v1 offset:9216\n" \
  "v_mfma_f32_16x16x32_bf16 v[42:45], v[24:27], v[92:95], v[42:45]\n" \
  "v_mfma_f32_16x16x32_bf16 v[48:51], v[24:27], v[156:159], v[48:51]\n" \
  "v_mfma_f32_16x16x32_bf16 v[54:57], v[24:27], v[220:223], v[54:57]\n" \
  "s_waitcnt lgkmcnt(4)\n" \
  "v_mfma_f32_16x16x32_bf16 v[60:63], v[24:27], v[32:35], v[60:63]\n" \
  "ds_read_b128 v[24:27], v0 offset:22528\n" \
  "ds_read_b128 v[32:35], v1 offset:10240\n" \
  "v_mfma_f32_16x16x32_bf16 v[42:45], v[12:15], v[96:99], v[42:45]\n" \
  "v_mfma_f32_16x16x32_bf16 v[48:51], v[12:15], v[160:163], v[48:51]\n" \
  "v_mfma_f32_16x16x32_bf16 v[54:57], v[12:15], v[224:227], v[54:57]\n" \
  "s_waitcnt lgkmcnt(4)\n" \
  "v_mfma_f32_16x16x32_bf16 v[60:63], v[12:15], v[36:39], v[60:63]\n" \
  "ds_read_b128 v[12:15], v0 offset:24576\n" \
  "ds_read_b128 v[36:39], v1 offset:11264\n" \
  "v_mfma_f32_16x16x32_bf16 v[42:45], v[16:19], v[100:103], v[42:45]\n" \
  "v_mfma_f32_16x16x32_bf16 v[48:51], v[16:19], v[164:167], v[48:51]\n" \
  "v_mfma_f32_16x16x32_bf16 v[54:57], v[16:19], v[228:231], v[54:57]\n" \
  "s_waitcnt lgkmcnt(4)\n" \
  "v_mfma_f32_16x16x32_bf16 v[60:63], v[16:19], v[28:31], v[60:63]\n" \
  "ds_read_b128 v[16:19], v0 offset:26624\n" \
  "ds_read_b128 v[28:31], v1 offset:12288\n" \
  "v_mfma_f32_16x16x32_bf16 v[42:45], v[20:23], v[104:107], v[42:45]\n" \
  "v_mfma_f32_16x16x32_bf16 v[48:51], v[20:23], v[168:171], v[48:51]\n" \
  "v_mfma_f32_16x16x32_bf16 v[54:57], v[20:23], v[232:235], v[54:57]\n" \
  "s_waitcnt lgkmcnt(4)\n" \
  "v_mfma_f32_16x16x32_bf16 v[60:63], v[20:23], v[32:35], v[60:63]\n" \
  "ds_read_b128 v[20:23], v0 offset:28672\n" \
  "ds_read_b128 v[32:35], v1 offset:13312\n" \
  "v_mfma_f32_16x16x32_bf16 v[42:45], v[24:27], v[108:111], v[42:45]\n" \
  "v_mfma_f32_16x16x32_bf16 v[48:51], v[24:27], v[172:175], v[48:51]\n" \
  "v_mfma_f32_16x16x32_bf16 v[54:57], v[24:27], v[236:239], v[54:57]\n" \
  "s_waitcnt lgkmcnt(4)\n" \
  "v_mfma_f32_16x16x32_bf16 v[60:63], v[24:27], v[36:39], v[60:63]\n" \
  "s_cmp_eq_u32 s68, 1\n" \
  "s_cbranch_scc0 .LCW_X3_%=\n" \
  "s_waitcnt vmcnt(0)\n" \
  "s_mov_b64 exec, 1\n" \
  "buffer_store_dword v7, off, s[56:59], s72 sc1\n" \
  "s_mov_b64 exec, -1\n" \
  "s_add_u32 s72, s72, 0x1000\n" \
  "s_mov_b32 s68, 0\n" \
  ".LCW_X3_%=:\n" \
  "ds_read_b32 v5, v6 offset:16\n" \
  "ds_read_b128 v[24:27], v0 offset:30720\n" \
  "ds_read_b128 v[36:39], v1 offset:14336\n" \
  "v_mfma_f32_16x16x32_bf16 v[42:45], v[12:15], v[112:115], v[42:45]\n" \
  "v_mfma_f32_16x16x32_bf16 v[48:51], v[12:15], v[176:179], v[48:51]\n" \
  "v_mfma_f32_16x16x32_bf16 v[54:57], v[12:15], v[240:243], v[54:57]\n" \
  "s_waitcnt lgkmcnt(5)\n" \
  "v_mfma_f32_16x16x32_bf16 v[60:63], v[12:15], v[28:31], v[60:63]\n" \
  "ds_read_b128 v[12:15], v0 offset:256\n" \
  "ds_read_b128 v[28:31], v1 offset:15360\n" \
  "v_mfma_f32_16x16x32_bf16 v[42:45], v[16:19], v[116:119], v[42:45]\n" \
  "v_mfma_f32_16x16x32_bf16 v[48:51], v[16:19], v[180:183], v[48:51]\n" \
  "v_mfma_f32_16x16x32_bf16 v[54:57], v[16:19], v[244:247], v[54:57]\n" \
  "s_waitcnt lgkmcnt(5)\n" \
  "v_mfma_f32_16x16x32_bf16 v[60:63], v[16:19], v[32:35], v[60:63]\n" \
  "s_waitcnt lgkmcnt(4)\n" \
  "v_readfirstlane_b32 s71, v5\n" \
  "s_cmp_ge_u32 s71, s67\n" \
  "s_cbranch_scc1 .LCW_ST_%=\n" \
  ".LCW_S4_%=:\n" \
  "s_sleep 1\n" \
  "ds_read_b32 v5, v6 offset:16\n" \
  "s_sub_u32 s69, s69, 1\n" \
  "s_cbranch_scc1 .LCW_END_%=\n" \
  "s_waitcnt lgkmcnt(0)\n" \
  "v_readfirstlane_b32 s71, v5\n" \
  "s_cmp_ge_u32 s71, s67\n" \
  "s_cbranch_scc1 .LCW_ST_%=\n" \
  "s_branch .LCW_S4_%=\n" \
  ".LCW_ST_%=:\n" \
  "ds_read_b128 v[16:19], v0 offset:2304\n" \
  "ds_read_b128 v[32:35], v3 offset:128\n" \
  "v_mfma_f32_16x16x32_bf16 v[42:45], v[20:23], v[120:123], v[42:45]\n" \
  "v_mfma_f32_16x16x32_bf16 v[48:51], v[20:23], v[184:187], v[48:51]\n" \
  "v_mfma_f32_16x16x32_bf16 v[54:57], v[20:23], v[248:251], v[54:57]\n" \
  "s_waitcnt lgkmcnt(4)\n" \
  "v_mfma_f32_16x16x32_bf16 v[60:63], v[20:23], v[36:39], v[60:63]\n" \
  "ds_read_b128 v[20:23], v0 offset:4352\n" \
  "ds_read_b128 v[36:39], v3 offset:0\n" \
  "v_mfma_f32_16x16x32_bf16 v[42:45], v[24:27], v[124:127], v[42:45]\n" \
  "v_mfma_f32_16x16x32_bf16 v[48:51], v[24:27], v[188:191], v[48:51]\n" \
  "v_mfma_f32_16x16x32_bf16 v[54:57], v[24:27], v[252:255], v[54:57]\n" \
  "s_waitcnt lgkmcnt(4)\n" \
  "v_mfma_f32_16x16x32_bf16 v[60:63], v[24:27], v[28:31], v[60:63]\n" \
  "ds_read_b128 v[28:31], v1 offset:0\n" \
  "ds_read_b128 v[24:27], v0 offset:6400\n" \
  "s_waitcnt lgkmcnt(4)\n" \
  "v_mul_f32 v42, v42, v32\n" \
  "v_mul_f32 v43, v43, v33\n" \
  "v_mul_f32 v44, v44, v34\n" \
  "v_mul_f32 v45, v45, v35\n" \
  "s_waitcnt lgkmcnt(2)\n" \
  "v_fmac_f32 v42, v36, v8\n" \
  "v_fmac_f32 v43, v37, v8\n" \
  "v_fmac_f32 v44, v38, v8\n" \
  "v_fmac_f32 v45, v39, v8\n" \
  "v_cvt_pk_bf16_f32 v40, v42, v43\n" \
  "v_cvt_pk_bf16_f32 v41, v44, v45\n" \
  "v_mfma_f32_16x16x32_bf16 v[42:45], v[12:15], v[64:67], 0\n" \
  "v_mul_f32 v48, v48, v32\n" \
  "v_mul_f32 v49, v49, v33\n" \
  "v_mul_f32 v50, v50, v34\n" \
  "v_mul_f32 v51, v51, v35\n" \
  "v_fmac_f32 v48, v36, v9\n" \
  "v_fmac_f32 v49, v37, v9\n" \
  "v_fmac_f32 v50, v38, v9\n" \
  "v_fmac_f32 v51, v39, v9\n" \
  "v_cvt_pk_bf16_f32 v46, v48, v49\n" \
  "v_cvt_pk_bf16_f32 v47, v50, v51\n" \
  "v_mfma_f32_16x16x32_bf16 v[48:51], v[12:15], v[128:131], 0\n" \
  "v_mul_f32 v54, v54, v32\n" \
  "v_mul_f32 v55, v55, v33\n" \
  "v_mul_f32 v56, v56, v34\n" \
  "v_mul_f32 v57, v57, v35\n" \
  "v_fmac_f32 v54, v36, v10\n" \
  "v_fmac_f32 v55, v37, v10\n" \
  "v_fmac_f32 v56, v38, v10\n" \
  "v_fmac_f32 v57, v39, v10\n" \
  "v_cvt_pk_bf16_f32 v52, v54, v55\n" \
  "v_cvt_pk_bf16_f32 v53, v56, v57\n" \
  "v_mfma_f32_16x16x32_bf16 v[54:57], v[12:15], v[192:195], 0\n" \
  "v_mul_f32 v60, v60, v32\n" \
  "v_mul_f32 v61, v61, v33\n" \
  "v_mul_f32 v62, v62, v34\n" \
  "v_mul_f32 v63, v63, v35\n" \
  "v_fmac_f32 v60, v36, v4\n" \
  "v_fmac_f32 v61, v37, v4\n" \
  "v_fmac_f32 v62, v38, v4\n" \
  "v_fmac_f32 v63, v39, v4\n" \
  "v_cvt_pk_bf16_f32 v58, v60, v61\n" \
  "v_cvt_pk_bf16_f32 v59, v62, v63\n" \
  "ds_read_b128 v[32:35], v1 offset:1024\n" \
  "ds_read_b128 v[36:39], v1 offset:2048\n" \
  "s_waitcnt lgkmcnt(3)\n" \
  "v_mfma_f32_16x16x32_bf16 v[60:63], v[12:15], v[28:31], 0\n" \
  "ds_read_b128 v[12:15], v0 offset:8448\n" \
  "ds_read_b128 v[28:31], v1 offset:3072\n" \
  "v_mfma_f32_16x16x32_bf16 v[42:45], v[16:19], v[68:71], v[42:45]\n" \
  "v_mfma_f32_16x16x32_bf16 v[48:51], v[16:19], v[132:135], v[48:51]\n" \
  "v_mfma_f32_16x16x32_bf16 v[54:57], v[16:19], v[196:199], v[54:57]\n" \
  "s_waitcnt lgkmcnt(3)\n" \
  "v_mfma_f32_16x16x32_bf16 v[60:63], v[16:19], v[32:35], v[60:63]\n" \
  "ds_read_b128 v[16:19], v0 offset:10496\n" \
  "ds_read_b128 v[32:35], v1 offset:4096\n" \
  "v_mfma_f32_16x16x32_bf16 v[42:45], v[20:23], v[72:75], v[42:45]\n" \
  "v_mfma_f32_16x16x32_bf16 v[48:51], v[20:23], v[136:139], v[48:51]\n" \
  "v_mfma_f32_16x16x32_bf16 v[54:57], v[20:23], v[200:203], v[54:57]\n" \
  "s_waitcnt lgkmcnt(4)\n" \
  "v_mfma_f32_16x16x32_bf16 v[60:63], v[20:23], v[36:39], v[60:63]\n" \
  "ds_read_b128 v[20:23], v0 offset:12544\n" \
  "ds_read_b128 v[36:39], v1 offset:5120\n" \
  "v_mfma_f32_16x16x32_bf16 v[42:45], v[24:27], v[76:79], v[42:45]\n" \
  "v_mfma_f32_16x16x32_bf16 v[48:51], v[24:27], v[140:143], v[48:51]\n" \
  "v_mfma_f32_16x16x32_bf16 v[54:57], v[24:27], v[204:207], v[54:57]\n" \
  "s_waitcnt lgkmcnt(4)\n" \
  "v_mfma_f32_16x16x32_bf16 v[60:63], v[24:27], v[28:31], v[60:63]\n" \
  "ds_read_b128 v[24:27], v0 offset:14592\n" \
  "ds_read_b128 v[28:31], v1 offset:6144\n" \
  "v_mfma_f32_16x16x32_bf16 v[42:45], v[12:15], v[80:83], v[42:45]\n" \
  "v_mfma_f32_16x16x32_bf16 v[48:51], v[12:15], v[144:147], v[48:51]\n" \
  "v_mfma_f32_16x16x32_bf16 v[54:57], v[12:15], v[208:211], v[54:57]\n" \
  "s_waitcnt lgkmcnt(4)\n" \
  "v_mfma_f32_16x16x32_bf16 v[60:63], v[12:15], v[32:35], v[60:63]\n" \
  "ds_read_b128 v[12:15], v0 offset:16640\n" \
  "ds_read_b128 v[32:35], v1 offset:7168\n" \
  "v_mfma_f32_16x16x32_bf16 v[42:45], v[16:19], v[84:87], v[42:45]\n" \
  "v_mfma_f32_16x16x32_bf16 v[48:51], v[16:19], v[148:151], v[48:51]\n" \
  "v_mfma_f32_16x16x32_bf16 v[54:57], v[16:19], v[212:215], v[54:57]\n" \
  "s_waitcnt lgkmcnt(4)\n" \
  "v_mfma_f32_16x16x32_bf16 v[60:63], v[16:19], v[36:39], v[60:63]\n" \
  "ds_read_b128 v[16:19], v0 offset:18688\n" \
  "ds_read_b128 v[36:39], v1 offset:8192\n" \
  "v_mfma_f32_16x16x32_bf16 v[42:45], v[20:23], v[88:91], v[42:45]\n" \
  "v_mfma_f32_16x16x32_bf16 v[48:51], v[20:23], v[152:155], v[48:51]\n" \
  "v_mfma_f32_16x16x32_bf16 v[54:57], v[20:23], v[216:219], v[54:57]\n" \
  "s_waitcnt lgkmcnt(4)\n" \
  "v_mfma_f32_16x16x32_bf16 v[60:63], v[20:23], v[28:31], v[60:63]\n" \
  "ds_read_b128 v[20:23], v0 offset:20736\n" \
  "ds_read_b128 v[28:31], v1 offset:9216\n" \
  "v_mfma_f32_16x16x32_bf16 v[42:45], v[24:27], v[92:95], v[42:45]\n" \
  "v_mfma_f32_16x16x32_bf16 v[48:51], v[24:27], v[156:159], v[48:51]\n" \
  "v_mfma_f32_16x16x32_bf16 v[54:57], v[24:27], v[220:223], v[54:57]\n" \
  "s_waitcnt lgkmcnt(4)\n" \
  "v_mfma_f32_16x16x32_bf16 v[60:63], v[24:27], v[32:35], v[60:63]\n" \
  "ds_read_b128 v[24:27], v0 offset:22784\n" \
  "ds_read_b128 v[32:35], v1 offset:10240\n" \
  "v_mfma_f32_16x16x32_bf16 v[42:45], v[12:15], v[96:99], v[42:45]\n" \
  "v_mfma_f32_16x16x32_bf16 v[48:51], v[12:15], v[160:163], v[48:51]\n" \
  "v_mfma_f32_16x16x32_bf16 v[54:57], v[12:15], v[224:227], v[54:57]\n" \
  "s_waitcnt lgkmcnt(4)\n" \
  "v_mfma_f32_16x16x32_bf16 v[60:63], v[12:15], v[36:39], v[60:63]\n" \
  "ds_read_b128 v[12:15], v0 offset:24832\n" \
  "ds_read_b128 v[36:39], v1 offset:11264\n" \
  "v_mfma_f32_16x16x32_bf16 v[42:45], v[16:19], v[100:103], v[42:45]\n" \
  "v_mfma_f32_16x16x32_bf16 v[48:51], v[16:19], v[164:167], v[48:51]\n" \
  "v_mfma_f32_16x16x32_bf16 v[54:57], v[16:19], v[228:231], v[54:57]\n" \
  "s_waitcnt lgkmcnt(4)\n" \
  "v_mfma_f32_16x16x32_bf16 v[60:63], v[16:19], v[28:31], v[60:63]\n" \
  "ds_read_b128 v[16:19], v0 offset:26880\n" \
  "ds_read_b128 v[28:31], v1 offset:12288\n" \
  "v_mfma_f32_16x16x32_bf16 v[42:45], v[20:23], v[104:107], v[42:45]\n" \
  "v_mfma_f32_16x16x32_bf16 v[48:51], v[20:23], v[168:171], v[48:51]\n" \
  "v_mfma_f32_16x16x32_bf16 v[54:57], v[20:23], v[232:235], v[54:57]\n" \
  "s_waitcnt lgkmcnt(4)\n" \
  "v_mfma_f32_16x16x32_bf16 v[60:63], v[20:23], v[32:35], v[60:63]\n" \
  "ds_read_b128 v[20:23], v0 offset:28928\n" \
  "ds_read_b128 v[32:35], v1 offset:13312\n" \
  "v_mfma_f32_16x16x32_bf16 v[42:45], v[24:27], v[108:111], v[42:45]\n" \
  "v_mfma_f32_16x16x32_bf16 v[48:51], v[24:27], v[172:175], v[48:51]\n" \
  "v_mfma_f32_16x16x32_bf16 v[54:57], v[24:27], v[236:239], v[54:57]\n" \
  "s_waitcnt lgkmcnt(4)\n" \
  "v_mfma_f32_16x16x32_bf16 v[60:63], v[24:27], v[36:39], v[60:63]\n" \
  "ds_read_b128 v[24:27], v0 offset:30976\n" \
  "ds_read_b128 v[36:39], v1 offset:14336\n" \
  "v_mfma_f32_16x16x32_bf16 v[42:45], v[12:15], v[112:115], v[42:45]\n" \
  "v_mfma_f32_16x16x32_bf16 v[48:51], v[12:15], v[176:179], v[48:51]\n" \
  "v_mfma_f32_16x16x32_bf16 v[54:57], v[12:15], v[240:243], v[54:57]\n" \
  "s_waitcnt lgkmcnt(4)\n" \
  "v_mfma_f32_16x16x32_bf16 v[60:63], v[12:15], v[28:31], v[60:63]\n" \
  "ds_read_b128 v[28:31], v1 offset:15360\n" \
  "v_mfma_f32_16x16x32_bf16 v[42:45], v[16:19], v[116:119], v[42:45]\n" \
  "v_mfma_f32_16x16x32_bf16 v[48:51], v[16:19], v[180:183], v[48:51]\n" \
  "v_mfma_f32_16x16x32_bf16 v[54:57], v[16:19], v[244:247], v[54:57]\n" \
  "s_waitcnt lgkmcnt(3)\n" \
  "v_mfma_f32_16x16x32_bf16 v[60:63], v[16:19], v[32:35], v[60:63]\n" \
  "ds_read_b128 v[32:35], v3 offset:192\n" \
  "v_mfma_f32_16x16x32_bf16 v[42:45], v[20:23], v[120:123], v[42:45]\n" \
  "v_mfma_f32_16x16x32_bf16 v[48:51], v[20:23], v[184:187], v[48:51]\n" \
  "v_mfma_f32_16x16x32_bf16 v[54:57], v[20:23], v[248:251], v[54:57]\n" \
  "s_waitcnt lgkmcnt(2)\n" \
  "v_mfma_f32_16x16x32_bf16 v[60:63], v[20:23], v[36:39], v[60:63]\n" \
  "ds_read_b128 v[36:39], v3 offset:64\n" \
  "s_mov_b64 exec, 1\n" \
  "ds_add_u32 v6, v7 offset:8\n" \
  "s_mov_b64 exec, -1\n" \
  "v_xor_b32 v6, 4, v6\n" \
  "v_mfma_f32_16x16x32_bf16 v[42:45], v[24:27], v[124:127], v[42:45]\n" \
  "v_mfma_f32_16x16x32_bf16 v[48:51], v[24:27], v[188:191], v[48:51]\n" \
  "v_mfma_f32_16x16x32_bf16 v[54:57], v[24:27], v[252:255], v[54:57]\n" \
  "s_waitcnt lgkmcnt(3)\n" \
  "v_mfma_f32_16x16x32_bf16 v[60:63], v[24:27], v[28:31], v[60:63]\n" \
  "s_waitcnt lgkmcnt(2)\n" \
  "v_mul_f32 v42, v42, v32\n" \
  "v_mul_f32 v43, v43, v33\n" \
  "v_mul_f32 v44, v44, v34\n" \
  "v_mul_f32 v45, v45, v35\n" \
  "s_waitcnt lgkmcnt(1)\n" \
  "v_fmac_f32 v42, v36, v8\n" \
  "v_fmac_f32 v43, v37, v8\n" \
  "v_fmac_f32 v44, v38, v8\n" \
  "v_fmac_f32 v45, v39, v8\n" \
  "v_cvt_pk_bf16_f32 v42, v42, v43\n" \
  "v_cvt_pk_bf16_f32 v43, v44, v45\n" \
  "buffer_store_dwordx4 v[40:43], v2, s[40:43], s65 offen offset:0 sc1\n" \
  "v_mul_f32 v48, v48, v32\n" \
  "v_mul_f32 v49, v49, v33\n" \
  "v_mul_f32 v50, v50, v34\n" \
  "v_mul_f32 v51, v51, v35\n" \
  "v_fmac_f32 v48, v36, v9\n" \
  "v_fmac_f32 v49, v37, v9\n" \
  "v_fmac_f32 v50, v38, v9\n" \
  "v_fmac_f32 v51, v39, v9\n" \
  "v_cvt_pk_bf16_f32 v48, v48, v49\n" \
  "v_cvt_pk_bf16_f32 v49, v50, v51\n" \
  "ds_read_b32 v5, v6 offset:0\n" \
  "buffer_store_dwordx4 v[46:49], v2, s[40:43], s65 offen offset:1024 sc1\n" \
  "v_mul_f32 v54, v54, v32\n" \
  "v_mul_f32 v55, v55, v33\n" \
  "v_mul_f32 v56, v56, v34\n" \
  "v_mul_f32 v57, v57, v35\n" \
  "v_fmac_f32 v54, v36, v10\n" \
  "v_fmac_f32 v55, v37, v10\n" \
  "v_fmac_f32 v56, v38, v10\n" \
  "v_fmac_f32 v57, v39, v10\n" \
  "v_cvt_pk_bf16_f32 v54, v54, v55\n" \
  "v_cvt_pk_bf16_f32 v55, v56, v57\n" \
  "buffer_store_dwordx4 v[52:55], v2, s[40:43], s65 offen offset:2048 sc1\n" \
  "v_mul_f32 v60, v60, v32\n" \
  "v_mul_f32 v61, v61, v33\n" \
  "v_mul_f32 v62, v62, v34\n" \
  "v_mul_f32 v63, v63, v35\n" \
  "v_fmac_f32 v60, v36, v4\n" \
  "v_fmac_f32 v61, v37, v4\n" \
  "v_fmac_f32 v62, v38, v4\n" \
  "v_fmac_f32 v63, v39, v4\n" \
  "v_cvt_pk_bf16_f32 v60, v60, v61\n" \
  "v_cvt_pk_bf16_f32 v61, v62, v63\n" \
  "buffer_store_dwordx4 v[58:61], v2, s[40:43], s65 offen offset:3072 sc1\n" \
  "s_waitcnt lgkmcnt(0)\n" \
  "s_add_u32 s65, s65, 0x40000\n" \
  "s_sub_u32 s64, s64, 1\n" \
  "s_mov_b32 s68, 1\n" \
  "s_cmp_lg_u32 s64, 0\n" \
  "s_cbranch_scc1 .LCW_TOP_%=\n" \
  "s_waitcnt vmcnt(0)\n" \
  "s_mov_b64 exec, 1\n" \
  "buffer_store_dword v7, off, s[56:59], s72 sc1\n" \
  "s_mov_b64 exec, -1\n" \
  "s_add_u32 s72, s72, 0x1000\n" \
  "s_mov_b32 s68, 0\n" \
  ".LCW_END_%=:\n" \
  "s_endpgm\n"
constexpr int RG = 2;
#define MARKS(s) __builtin_amdgcn_raw_buffer_load_b32(rm, (c >> 2) * 128 + (c & 3) * 4, (s) * 2048, AUX_SC1)
struct Yes { static constexpr bool value = true; };
struct No { static constexpr bool value = false; };
#define RFL(x) __builtin_amdgcn_readfirstlane(x)
#define HALF(x, i) BF8(((i) ? __builtin_shufflevector(x, x, 4, 5, 6, 7) : __builtin_shufflevector(x, x, 0, 1, 2, 3)))
constexpr int P_W = 65536, P_ST = 131072, P_SS = P_ST + 1024, P_CN = P_SS + 512;
template <bool PR>
__device__ __forceinline__ void role_proj_t(const MegaArgs& A, char* smem, const int L, const int idx, const bool kv) {
  const int w = A.wv;
  const int l = lane_id();
  const int c = l & 15, g = l >> 4;
  char* ws = A.ws;
  unsigned* FL = (unsigned*)(ws + OFF_FL);
  unsigned* TMO = FL + FL_TMO;
  const unsigned sb = lds_base(smem);
  auto cn = (volatile __attribute__((address_space(3))) unsigned*)(unsigned long)(sb + P_CN);
  u64 tag;
  const bool h4 = kv && L;
  const __amdgpu_buffer_rsrc_t rsM = MKRS(ws + OFF_MEMK + L * SZ_MEM, kv && w == 2 ? (4 - L) * SZ_MEM : 0);
  const bool mc = kv && w == 2 && (MCE < 2 || !L);
  constexpr int NMC = MCE == 2 ? 8 : 4;
  const int b0 = RFL(h4 ? (H4S ? 2 : 3) * w : w == 2 ? 0 : HX0),
            b1 = RFL(h4 ? (H4S ? 2 * w + 2 : w == 2 ? 8 : 3 * w + 3 * (w < 2)) : w == 2 ? HX0 : 8);
  const __amdgpu_buffer_rsrc_t rsF = MKRS(FL + FL_FC + L * 64 * 32 * 32, 64 * 4096);
  const int fo = ((blockIdx.x & 3) * 8 + ((H4S != 0 && h4) || w == 3 ? l >> 3 : min(b0 + (l >> 3), b1 - 1))) * 128 + (l & 7) * 4;
#define FC_LOOK(s) __builtin_amdgcn_raw_buffer_load_b32(rsF, fo, (s) * 4096, AUX_SC1)
#define MEM_CLEAR(s)                                                                                                          \
  _Pragma("unroll") for (int i = 0; i < NMC; ++i)                                                                             \
    ST128((v4u{0u, 0u, 0u, 0u}), rsM, l * 16 + (i & 1) * 1024, (s) * 65536 + idx * 2048 + (i >> 1) * (int)SZ_MEM * (MCE == 2 ? 1 : 2));
  const char* Xb = ws + (L ? OFF_X3 : OFF_XA);
  const __amdgpu_buffer_rsrc_t rsS = MKRS(ws + OFF_H + L * (SZ_X / 2), SZ_X / 2);
  unsigned nf = 0u;
  bool early = false;
  v4u t0[16];
#define X_ASK(s) \
  for (int i = b0; i < b1; ++i) glds64c(Xb + RFL((s) * 32768 + i * 4096), l * 16, RFL(sb + ((s) & 1) * 32768 + i * 4096));
#define S_ASK(s, t) _Pragma("unroll") for (int i = 0; i < 16; ++i) t[i] = LD128(rsS, l * 16, (s) * 16384 + i * 1024);
  {
    const int j = w & 3;
    const bool vt = kv && j >= 2;
    constexpr int NT = PR ? 3 : 1;
    v8u wq[NT][8];
    const u16* Wm = (kv ? (PR ? A.Wv : A.Wk) : A.Wq) - (PR ? 128 : 0) * HD;
    const int hm = kv ? idx : 2 * idx + PR;
    const int q = (l >> 2) & 3, p = l & 3, f = q | ((g & 1) << 2);
    unsigned ra[NT];
#pragma unroll
    for (int m = 0; m < NT; ++m) {
      const int ct = PR ? m : 3;
      ra[m] = sb + (j >> 1) * 65536 + (8 * g + q) * 256 + ((4 * (j & 1) + (vt ? ct : (ct & 2) + (p >> 1))) ^ f) * 32 + (vt ? p * 8 : (p & 1) * 16 + (ct & 1) * 8);
    }
#define W_ISSUE(n) wqkv_issue<false>(Wm + (128 * (n) - 256 * ((n) & 1)) * HD, (n) & 1, hm, w, l, sb, (WNT >> (kv ? 1 + PR : 3) & 1) && (L == 0 || (WNT & 32)))
#define W_STAGE(n, WAIT, NEXT)                                                                             \
  {                                                                                                        \
    WAIT;                                                                                                  \
    if (n == 3 && EL == 2 && !PR && L == 0 && w >= 2) nf = FC_LOOK(0);                                     \
    if (n == 3 && EL == 1 && !PR && L == 0 && w >= 2) {                                                    \
      early = __all(nf != 0u);                                                                             \
      if (early) {                                                                                         \
        X_ASK(0)                                                                                           \
        if (w == 3) { S_ASK(0, t0) }                                                                       \
      }                                                                                                    \
      nf = FC_LOOK(early);                                                                                 \
    }                                                                                                      \
    RAW_BAR();                                                                                             \
    if (n == 3 && EL == 3 && !PR && L == 0 && w >= 2) nf = FC_LOOK(0);                                     \
    _Pragma("unroll") for (int m = 0; m < NT; ++m) {                                                       \
      _Pragma("unroll") for (int t = 0; t < 2; ++t) {                                                      \
        const unsigned a_ = ra[m] + ((n) & 1) * 32768 + t * 16384;                                         \
        const v2u r0 = trd(a_), r1 = trd(a_ + 1024), r2 = trd(a_ + 8192), r3 = trd(a_ + 8192 + 1024);      \
        wq[m][2 * (n) + t] = v8u{r0.x, r0.y, r1.x, r1.y, r2.x, r2.y, r3.x, r3.y};                          \
      }                                                                                                    \
    }                                                                                                      \
    _Pragma("unroll") for (int m = 0; m < 2 * NT; ++m) asm volatile("" : "+v"(wq[m >> 1][2 * (n) + (m & 1)])); \
    RAW_BAR();                                                                                             \
    NEXT;                                                                                                  \
    _Pragma("unroll") for (int m = 0; m < 4 * NT; ++m)                                                     \
      cs[m % NT] = MFMA16(HALF(wq[m % NT][2 * (n) + m / NT / 2], (m / NT) & 1), BF8(one), cs[m % NT]);     \
    _Pragma("unroll") for (int m = 0; m < NT; ++m) asm volatile("" : "+v"(cs[m]));                         \
  }
    if (EL && !MC3 && mc) { MEM_CLEAR(0) }
    W_ISSUE(0);
    W_ISSUE(1);
    tag = call_tag(A);
    if (w == 0 && l < 8) cn[l] = l == 1 ? 3u : l == 5 ? 2u : 0u;
    const v4u one = {0x3f803f80u, 0x3f803f80u, 0x3f803f80u, 0x3f803f80u};
    f32x4 cs[NT] = {};
    u64 rv = 0;
    W_STAGE(0, WAIT_VM(8), W_ISSUE(2); if (EL) RD_ASK(rv));
    SST(4);
    W_STAGE(1, if (EL) WAIT_VM(9); else WAIT_VM(8), W_ISSUE(3); if (EL != 0 && MC3 != 0 && mc) { MEM_CLEAR(0) if (MC3 == 2) { MEM_CLEAR(1) } });
    SST(5);
    W_STAGE(2, if (EL != 0 && MC3 == 2 && mc) WAIT_VM(16); else if (EL != 0 && MC3 != 0 && mc) WAIT_VM(12); else WAIT_VM(8), (void)0);
    SST(6);
    asm volatile("" : "+v"(rv));
    if (!EL) ready_wait(A, tag, ready_look(A));
    for (unsigned spins = 0; EL && !__all(rv == tag) && spins < SPIN_CAP; ++spins) {
      __builtin_amdgcn_s_sleep(8);
      RD_ASK(rv);
      asm volatile("s_waitcnt vmcnt(0)" : "+v"(rv)::"memory");
    }
    if (EL == 1 && !PR && L == 0 && w >= 2) nf = FC_LOOK(0);
    W_STAGE(3, if (EL >= 2 && MC3 == 2 && mc) WAIT_VM(8); else if (EL >= 2 && MC3 == 1 && mc) WAIT_VM(4); else WAIT_VM(0), if (!PR) {
      _Pragma("unroll") for (int ks = 0; ks < 16; ++ks)
        *reinterpret_cast<bf16x8*>(smem + P_W + j * 16384 + ks * 1024 + l * 16) = HALF(wq[0][ks >> 1], ks & 1);
    });
    SST(7);
#undef W_STAGE
#undef W_ISSUE
#pragma unroll
    for (int m = 0; m < NT; ++m) *reinterpret_cast<f32x4*>(smem + P_ST + j * 256 + (PR ? m : 3) * 64 + g * 16) = cs[m];
    RAW_BAR();
    SST(8);
    if constexpr (PR) {
      const v4u rsO = mk_srd(ws + RFL((unsigned)(vt ? OFF_VF + L * SZ_O : OFF_QK + L * SZ_QK)), RFL((unsigned)(vt ? SZ_O : SZ_QK)));
      const v4u rsP = mk_srd(FL + RFL(kv ? FL_FQ + (L * 64 * 32 + idx) * 32 + 2 * j : FL_FP + (L * 16 + idx) * 32 + j), 63 * 4096 + 4);
      const int so = RFL(vt ? idx * 8 + 4 * (j & 1) : (kv ? idx * 16 + 8 : (2 * idx + (j >> 1)) * 16) + 2 * (j & 1));
      const v8u pv = {sb + 32768 + g * 512 + c * 16, sb + P_W + j * 16384 + l * 16, (unsigned)l * 16, sb + P_SS + 256 + (vt ? g * 16 : c * 4),
                      sb + P_ST + j * 256 + (vt ? c * 4 : g * 16), 0u, 0u, 0u};
      const char* dbg = nullptr;
#define P_OPS                                                                                                                               \
  ::"{v[64:71]}"(wq[0][0]), "{v[72:79]}"(wq[0][1]), "{v[80:87]}"(wq[0][2]), "{v[88:95]}"(wq[0][3]), "{v[96:103]}"(wq[0][4]),                \
      "{v[104:111]}"(wq[0][5]), "{v[112:119]}"(wq[0][6]), "{v[120:127]}"(wq[0][7]), "{v[128:135]}"(wq[1][0]), "{v[136:143]}"(wq[1][1]),     \
      "{v[144:151]}"(wq[1][2]), "{v[152:159]}"(wq[1][3]), "{v[160:167]}"(wq[1][4]), "{v[168:175]}"(wq[1][5]), "{v[176:183]}"(wq[1][6]),     \
      "{v[184:191]}"(wq[1][7]), "{v[192:199]}"(wq[2][0]), "{v[200:207]}"(wq[2][1]), "{v[208:215]}"(wq[2][2]), "{v[216:223]}"(wq[2][3]),     \
      "{v[224:231]}"(wq[2][4]), "{v[232:239]}"(wq[2][5]), "{v[240:247]}"(wq[2][6]), "{v[248:255]}"(wq[2][7]), "{v[0:7]}"(pv),               \
      "{s[40:43]}"(rsO), "{s[56:59]}"(rsP), "{s64}"(NSEG), "{s65}"(so * 1024), "{s70}"(sb + P_CN), "{s[36:37]}"(dbg)                        \
      : "memory", "vcc", "scc", "s80", "v63"
      if (vt) asm volatile("s_mov_b64 exec, -1\ns_waitcnt vmcnt(0) lgkmcnt(0)\ns_nop 4\n" V_ASM P_OPS);
      else asm volatile("s_mov_b64 exec, -1\ns_waitcnt vmcnt(0) lgkmcnt(0)\ns_nop 4\n" P_ASM P_OPS);
#undef P_OPS
      return;
    }
  }
  __builtin_amdgcn_s_setprio(RPRIO);
  if (h4 || w >= 2) {
    if (!EL && mc) { MEM_CLEAR(0) }
    auto stats = [&](const v4u (&t)[16], const int s) {
      float a = 0.f, b = 0.f, qa = 0.f, qb = 0.f;
#pragma unroll
      for (int i = 0; i < 16; ++i) {
        a = one(a + __uint_as_float(t[i].x));
        b = one(b + __uint_as_float(t[i].z));
        qa = one(qa + __uint_as_float(t[i].y));
        qb = one(qb + __uint_as_float(t[i].w));
      }
      a = xsum32(xsum16(a));
      b = xsum32(xsum16(b));
      float da = 0.f, db = 0.f;
#pragma unroll
      for (int i = 0; i < 16; ++i) {
        const float u = one(__uint_as_float(t[i].x) - a * (1.0f / 64)), v = one(__uint_as_float(t[i].z) - b * (1.0f / 64));
        da = one(da + u * u);
        db = one(db + v * v);
      }
      const float var = xsum32(swap_add16(qa + da * 0.125f, qb + db * 0.125f)) * (1.0f / DM);
      const float ri = 1.0f / bfr1(sqrtf(bfr1(bfr1(var) + 1e-5f)));
      reinterpret_cast<float*>(smem + P_SS)[(s & 1) * 64 + (g >> 1) * 32 + 2 * c + (g & 1)] = g < 2 ? bfr1(((g & 1) ? b : a) * (1.0f / DM)) * (EP ? -ri : 1.0f) : ri;
      asm volatile("" ::: "memory");
      if (l == 0) cn[4 + (s & 1)] = 2 * s + 4;
    };
    static_assert(EL != 1 || MC3 == 2, "clear");
    if (early) {
      if (w == 3) WAIT_VM(17);
      else WAIT_VM(1);
      if (l == 0) __hip_atomic_fetch_add((__attribute__((address_space(3))) unsigned*)cn, 3u, __ATOMIC_RELAXED, __HIP_MEMORY_SCOPE_WORKGROUP);
      SSX(10, w == 2);
      SSX(9, w == 2);
      if (w == 3) stats(t0, 0);
      SSX(11, w == 3);
    }
    if (L) {
      char* ws = A.ws;
      int w = A.wv, i1 = idx + 32 * kv;
      asm volatile("" : "+s"(ws), "+s"(w), "+s"(i1));
      const int idx = i1 & 31;
      const bool h4 = i1 >= 32, mc = h4 && w == 2;
      const int b0 = h4 ? 3 * w : w == 2 ? 0 : HX0, b1 = h4 ? (w == 2 ? 8 : 3 * w + 3 * (w < 2)) : w == 2 ? HX0 : 8;
      unsigned* FL = (unsigned*)(ws + OFF_FL);
      unsigned* TMO = FL + FL_TMO;
      const char* Xb = ws + OFF_X3;
      const __amdgpu_buffer_rsrc_t rsM = MKRS(ws + OFF_MEMK + SZ_MEM, 3 * SZ_MEM), rsF = MKRS(FL + FL_FC + 64 * 32 * 32, 64 * 4096),
                                   rsS = MKRS(ws + OFF_H + SZ_X / 2, SZ_X / 2);
      unsigned mk;
      int l16 = l * 16;
      asm volatile("" : "+v"(l16));
      const __amdgpu_buffer_rsrc_t rm = MKRS(FL + FL_FW + (blockIdx.x & 3) * 128, 63 * 2048 + 512);
      if (!wait_n<SLP_V, 32>(FL + FL_FZ + (blockIdx.x & 7) * 64, 64, TMO)) return;
#pragma unroll 1
      for (unsigned spins = 0;; ++spins) {
        if (spin_timeout(spins, TMO)) return;
        __builtin_amdgcn_s_sleep(32);
        mk = MARKS(0);
        if (__all(mk != 0u)) break;
        asm volatile("" ::: "memory");
      }
      gate(latest(mk, RFL(mk)) + KD + K0);
      auto feed = [&](auto ST_) {
        constexpr bool ST = ST_.value;
        bool due = true;
#pragma unroll 1
        for (int s = 0, n = 0; s < NSEG && n < 4 * NSEG; ++n) {
#pragma unroll 1
          for (unsigned spins = 0; !due; ++spins) {
            if (spin_timeout(spins, TMO)) return;
            if (spins) __builtin_amdgcn_s_sleep(SLP_H);
            due = __all(FC_LOOK(s) != 0u);
            asm volatile("" ::: "memory");
          }
          TS(4, s, 128);
#pragma unroll 1
          for (unsigned spins = 0; cn[2 + (s & 1)] < 4u * (s >> 1); ++spins) {
            if (spins > 64u * SPIN_CAP) return;
            __builtin_amdgcn_s_sleep(2);
          }
          TS(5, s, 128);
          X_ASK(s)
          v4u t[ST ? 16 : 1];
          if (ST) {
            int so = s * 16384;
#pragma unroll
            for (int i = 0; i < 16; ++i) {
              if ((i & 3) == 0) asm volatile("" : "+s"(so));
              t[i] = LD128(rsS, l16 + (i & 3) * 1024, so);
              if ((i & 3) == 3) so += 4096;
              __builtin_amdgcn_sched_barrier(0);
            }
          }
          mk = MARKS(s + 1);
          unsigned z = 1u;
          if (ST) WAIT_VM(17);
          else WAIT_VM(1);
#pragma unroll 1
          for (int k = b0; k < b1; k += 3) {
            const unsigned* q = reinterpret_cast<const unsigned*>(smem + (s & 1) * 32768 + k * 4096 + l16 + 4);
            const unsigned* q2 = q + min(b1 - k - 1, 2) * 1024;
#pragma unroll
            for (int i = 0; i < 12; i += 2) z = i < 8 ? umin3(z, q[256 * i], q[256 * i + 256]) : umin3(z, q2[256 * (i - 8)], q2[256 * (i - 7)]);
          }
          if (ST) {
#pragma unroll
            for (int i = 0; i < 16; ++i) z = umin3(z, t[i].y, t[i].w);
          }
          due = __all(z != 0u);
          if (!due) continue;
          asm volatile("" ::: "memory");
          if (l == 0 && !(ST && h4)) __hip_atomic_fetch_add((__attribute__((address_space(3))) unsigned*)cn + (s & 1), h4 ? 2u : 3u, __ATOMIC_RELAXED, __HIP_MEMORY_SCOPE_WORKGROUP);
          TS(6, s, 128);
          if constexpr (ST) {
            stats(t, s);
            TS(7, s, 192);
          }
          due = __all(mk != 0u);
          const unsigned td = latest(mk, RFL(mk)) + (h4 ? KD : PD);
          if (!ST && mc && s < NSEG - 1 && s) { MEM_CLEAR(s + 1) }
          if (due) gate(td);
          ++s;
        }
      };
      static_assert(HX0 == 6, "blocks");
      if (w == 3) feed(Yes{});
      else feed(No{});
      return;
    }
    unsigned tn = now10();
    for (int s = early; s < NSEG; ++s) {
      for (unsigned spins = 0; !__all(nf != 0u); ++spins) {
        if (spin_timeout(spins, TMO)) return;
        if (spins) __builtin_amdgcn_s_sleep(SLP_H);
        nf = FC_LOOK(s);
        asm volatile("" ::: "memory");
      }
      if (NFW) asm volatile("" ::"v"(nf));
      TS(4, s, 128);
      for (unsigned spins = 0; cn[2 + (s & 1)] < 4u * (s >> 1); ++spins) {
        if (spins > 64u * SPIN_CAP) return;
        __builtin_amdgcn_s_sleep(2);
      }
      if (s >= PACE0) gate(tn);
      else tn = now10();
      tn += PACE;
      TS(5, s, 128);
      X_ASK(s)
      if (w != 3) {
        nf = FC_LOOK(s + 1);
        if (mc && s < NSEG - 1 && (EL == 0 || MC3 != 2 || s)) {
          MEM_CLEAR(s + 1)
          if (NMC == 8) WAIT_VM(8);
          else WAIT_VM(4);
        } else {
          WAIT_VM(0);
        }
        if (l == 0) __hip_atomic_fetch_add((__attribute__((address_space(3))) unsigned*)cn + (s & 1), 3u, __ATOMIC_RELAXED, __HIP_MEMORY_SCOPE_WORKGROUP);
        TS(6, s, 128);
        SSX(10, s == 0 && w == 2);
      } else {
        v4u t[16];
        S_ASK(s, t)
        WAIT_VM(16);
        nf = FC_LOOK(s + 1);
        if (l == 0) __hip_atomic_fetch_add((__attribute__((address_space(3))) unsigned*)cn + (s & 1), 3u, __ATOMIC_RELAXED, __HIP_MEMORY_SCOPE_WORKGROUP);
        stats(t, s);
        SSX(11, s == 0);
        TS(7, s, 192);
      }
    }
    return;
  }
  const int RL = !kv, rp = idx & 15, u = RFL(kv ? idx >> 4 : L);
  const __amdgpu_buffer_rsrc_t rsW = MKRS(ws + OFF_PW, SZ_PW);
  const __amdgpu_buffer_rsrc_t rsX = MKRS(ws + (RL ? OFF_X3 : OFF_XA), SZ_X);
  const __amdgpu_buffer_rsrc_t rsY = RL ? MKRS(A.out, SZ_X) : MKRS(ws + OFF_X2, SZ_X);
  const int r = 2 * rp + (g >> 1), yo = r * (DM * 2) + (l & 31) * 16 + u * 512;
  const __amdgpu_buffer_rsrc_t rsC = MKRS(ws + OFF_H, OFF_QK - OFF_H);
#define X_CLEAR(s)                                                                                              \
  {                                                                                                             \
    ST128((v4u{0u, 0u, 0u, 0u}), rsY, yo, (s) * SEG * DM * 2);                                                  \
    if (KD) {                                                                                                   \
      ST128((v4u{0u, 0u, 0u, 0u}), rsC, l * 16, (int)(OFF_X3 - OFF_H) + ((s) * 32 + idx) * 1024);               \
      if (l < 32) ST128((v4u{0u, 0u, 0u, 0u}), rsC, l * 16, (int)(SZ_X / 2) + ((s) * 32 + idx) * 512);          \
    }                                                                                                           \
  }
  if (kv) {
    if (XZD) gate(now10() + XZD);
    if (XZP & 3) {
      if (!wait_n<SLP_V, 32>(FL + FL_FZ + (blockIdx.x & 7) * 64, 64, TMO)) return;
    } else {
      for (int s = w; s < (XZP ? 2 * RG : NSEG); s += RG) X_CLEAR(s)
      DRAIN();
      if (l < 8) set_flag(FL + FL_FZ + l * 64 + 2 * idx + w);
    }
  }
  v4u sel;
#pragma unroll
  for (int e = 0; e < 4; ++e) sel[e] = (g >> 1) == (c >> 3) && ((c & 7) >> 1) == e ? 0x3f80u << (16 * (c & 1)) : 0u;
  unsigned tl = now10();
  int po = (g & 1) * 32768 + (c >> 2) * 128 + (g >> 1) * 64 + (c & 3) * 16;
  asm volatile("" : "+v"(po));
  for (int s = w; s < NSEG; s += RG) {
    v4u xr;
    if (GF_R) {
      gate(tl);
      const gu32* f = (gu32*)(FL + FL_FW) + ((RL * 64 + s) * 4 + (blockIdx.x & 3)) * 4 * 32 + (c >> 2) * 32 + (c & 3);
      unsigned mk;
      for (unsigned spins = 0;; ++spins) {
        tl = now10() + GF_R;
        mk = __hip_atomic_load(f, RLX_AGENT);
        if (__all(mk != 0u) || spin_timeout(spins, TMO)) break;
        if (s < NSEG - RFA) __builtin_amdgcn_s_sleep(SLP_R);
      }
      if (EF) {
        const unsigned tp = latest(mk, tl);
        asm volatile("" ::: "memory");
        xr = LD128(rsX, (l & 31) * 512 + r * 16, s * 32768 + u * 16384);
        gate(tp - (RL && s >= NSEG - RLS ? RLEAD : 0));
        if (GR2) tl = tp + ((unsigned)(s - GR0) >= NSEG - 12 - GR0 ? GR3 : GR2);
      }
      asm volatile("" ::: "memory");
    } else
    grp_wait<SLP_R, 4, GATE_R>(true, nullptr, 0, FL + FL_FW + ((RL * 64 + s) * 4 + (blockIdx.x & 3)) * 4 * 32, 16, TMO, tl);
    if (s >= NSEG - 6 && !(EF != 0 && GR2 != 0)) tl -= GF_R - (s >= NSEG - 4 ? GF_RT : GF_RU);
    if (!EF) DRAIN();
    TS(0, s, w * 64);
    v4u p[8][2];
    if (!EF) xr = LD128(rsX, (l & 31) * 512 + r * 16, s * 32768 + u * 16384);
    f32x4 ac[2];
    const int sr = EF ? s & ((1 << RGB) - 1) : s;
    const unsigned old = (s >> RGB) & 1;
    for (unsigned spins = 0;; ++spins) {
      int so = (RL * 64 + sr) * 16 * 32768 + rp * 2048 + u * 1024;
#pragma unroll
      for (int k = 0; k < 8; ++k) {
        asm volatile("" : "+s"(so));
#pragma unroll
        for (int b = 0; b < 2; ++b) p[k][b] = __builtin_amdgcn_raw_buffer_load_b128(rsW, po + b * 512, so, RAUX);
        so += 65536;
      }
      __builtin_amdgcn_sched_barrier(0);
      unsigned z[2] = {1u, 1u};
      ac[0] = ac[1] = f32x4{};
#pragma unroll
      for (int k = 0; k < 8; ++k)
#pragma unroll
        for (int b = 0; b < 2; ++b) {
          ac[b] = MFMA16(BF8(sel), BF8(p[k][b]), ac[b]);
          z[b] = __builtin_amdgcn_bitop3_b32(z[b], p[k][b].x, old, 0x60);
          __builtin_amdgcn_sched_barrier(0);
        }
      if (!EF || !RVAL || __all((z[0] & z[1]) != 0u) || spin_timeout(spins, TMO)) break;
      if (SLP_D) __builtin_amdgcn_s_sleep(SLP_D);
      asm volatile("" ::: "memory");
    }
    v4u xo;
    {
      const auto x = __builtin_amdgcn_permlane16_swap(pk2s(ac[0][0], ac[0][1]), pk2s(ac[1][0], ac[1][1]), false, false);
      const auto y = __builtin_amdgcn_permlane16_swap(pk2s(ac[0][2], ac[0][3]), pk2s(ac[1][2], ac[1][3]), false, false);
      const v4u a = {x[0], y[0], x[1], y[1]};
#pragma unroll
      for (int e = 0; e < 4; ++e) {
        xo[e] = pk2s(lo16(xr[e]) + lo16(a[e]), hi16(xr[e]) + hi16(a[e]));
        if (kv) xo[e] = xo[e] ? xo[e] : 0x8000u;
      }
      ST128(xo, rsY, yo, s * SEG * DM * 2);
    }
    TS(1, s, w * 64);
    if (kv) {
      const unsigned p0 = __builtin_amdgcn_readlane(xo[0], 0), p1 = __builtin_amdgcn_readlane(xo[0], 32);
      const float pv = lo16(g < 2 ? p0 : p1);
      float sm = 0.f, q0 = 0.f, q1 = 0.f;
#pragma unroll
      for (int e = 0; e < 4; ++e) {
        const float a = one(lo16(xo[e]) - pv), b = one(hi16(xo[e]) - pv);
        sm = one(sm + one(a + b));
        q0 = one(q0 + a * a);
        q1 = one(q1 + b * b);
      }
      sm = half_sum(sm);
      const float q = fmaxf(one(half_sum(q0 + q1)) - sm * sm * (1.0f / 256), 0.f);
      const u64 st = (u64)__float_as_uint(sm + pv * 256.f) | (u64)(__float_as_uint(q) | 1u) << 32;
      if ((l & 31) < 4) __hip_atomic_store((gu64*)(FL + FL_FR + ((s * 4 + (l & 3)) * 4 + (r >> 3)) * 32 + (r & 7) * 4 + 2 * u), st, RLX_AGENT);
      if (XZP == 4 && s + 2 * RG < NSEG) X_CLEAR(s + 2 * RG)
    }
    if (EF && s + (1 << RGB) < NSEG) PW_FREE(RL, s + (1 << RGB), 2 * rp + u);
    TS(3, s, w * 64);
  }
  if (L == 1 && idx == 0 && w == 0 && l == 0) __hip_atomic_store((gu64*)(ws + OFF_EP), tag, RLX_AGENT);
}
__device__ __forceinline__ void role_proj(const MegaArgs& A, char* smem, const int L, const int idx, const bool kv) {
  if (A.wv < 4) role_proj_t<false>(A, smem, L, idx, kv);
  else role_proj_t<true>(A, smem, L, idx, kv);
}

constexpr int CUN = 2144;
__device__ __forceinline__ v2u part_stats(const v4u o) {
  float v[8];
#pragma unroll
  for (int e = 0; e < 4; ++e) { v[2 * e] = lo16(o[e]); v[2 * e + 1] = hi16(o[e]); }
  const float s = ((v[0] + v[1]) + (v[2] + v[3])) + ((v[4] + v[5]) + (v[6] + v[7])), m = s * 0.125f;
  float q0 = 0.f, q1 = 0.f;
#pragma unroll
  for (int e = 0; e < 4; ++e) {
    const float a = v[e] - m, b = v[4 + e] - m;
    q0 += a * a;
    q1 += b * b;
  }
  return v2u{__float_as_uint(s), __float_as_uint(q0 + q1) | 1u};
}
constexpr int CRN = 176;
constexpr int CTN = 528;
constexpr int CV_LDS = (8 * CKN + 8 * 2 * CUN + 64 + 2 * 32 * 8 + 16 * CRN + 64 + 8 * CTN) * 2 + 8 * 1568;
__device__ __forceinline__ void role_conv(const MegaArgs& A, char* smem, const int cv) {
  u16* Kr = reinterpret_cast<u16*>(smem);
  u16* Uc = Kr + 8 * CKN;
  u16* Us = Uc + 8 * CUN + 63;
  u16* To = Uc + 8 * 2 * CUN + 64;
  u16* Sx = To + 32 * 8;
  u16* Rc = Sx + 32 * 8;
  u16* Rs = Rc + 8 * CRN + 63;
  u16* Ta = Rc + 16 * CRN + 64;
  const int w = A.wv;
  const int l = lane_id(), tid = 64 * w + l, c = l & 15, g = l >> 4;
  const int d = 8 * cv + w;
  char* ws = A.ws;
  unsigned* FL = (unsigned*)(ws + OFF_FL);
  unsigned* TMO = FL + FL_TMO;
  u16* kr = Kr + w * CKN;
  const u16* ka = kr + 2080 - 16 * c + 8 * g;
#define KFRAG(D0) (*reinterpret_cast<const f16x8*>(ka - (D0)))
  typedef __attribute__((address_space(3))) const unsigned* lpu;
  typedef __attribute__((address_space(3))) u16* lph;
#define BFRAG(a) __builtin_bit_cast(f16x8, (v4u{((lpu)(unsigned long)(a))[0], ((lpu)(unsigned long)(a))[1], ((lpu)(unsigned long)(a))[2], ((lpu)(unsigned long)(a))[3]}))
  f32x4 acc[8], al[5];
  v4u tx[4];
  const __amdgpu_buffer_rsrc_t rz1 = MKRS(ws + OFF_XA, SZ_X), rps = MKRS(ws + OFF_H, SZ_X);
  u16* uc = Uc + w * CUN;
  const unsigned ub = lds_base(reinterpret_cast<char*>(((c & 1) ? Us : Uc) + w * CUN + c + 8 * g + 32));
  const int t0 = 16 * c + 4 * g;
  float dsk, ysc;
  unsigned mk = 0u;
  const __amdgpu_buffer_rsrc_t rm = MKRS(FL + FL_FW + (blockIdx.x & 3) * 128, 63 * 2048 + 512);
#define SKIP(T) (*reinterpret_cast<const v2u*>(uc + 32 + 256 * (T) + t0))
#define TILE_OUT(dst, a, uu)                                                                   \
  {                                                                                            \
    v2u v_;                                                                                    \
    _Pragma("unroll") for (int e = 0; e < 2; ++e) {                                            \
      const unsigned yy = pk2(a[2 * e] * ysc, a[2 * e + 1] * ysc), u_ = uu[e];                 \
      const unsigned k_ = pk2(h2f((u16)u_) * dsk, h2f((u16)(u_ >> 16)) * dsk);                 \
      v_[e] = pk2(lo16(yy) + lo16(k_), hi16(yy) + hi16(k_));                                   \
    }                                                                                          \
    *reinterpret_cast<v2u*>((dst) + t0) = v_;                                                  \
  }
#define CHUNK_ST(i, src, N) CHUNK_STX(w + 8 * (i), tx[i], src, N)
#define CHUNK_STX(i, X, src, N)                                                                \
  {                                                                                            \
    const unsigned p_ = lds_base((char*)((src) + (c >> 2) * (N) + (tid & ~15) + 4 * (c & 3))); \
    const v2u a0_ = trd(p_), a1_ = trd(p_ + 8 * (N));                                          \
    const v4u a_ = {a0_.x, a0_.y, a1_.x, a1_.y};                                               \
    v4u o;                                                                                     \
    _Pragma("unroll") for (int e = 0; e < 4; ++e) o[e] = pk2(lo16(X[e]) + lo16(a_[e]), hi16(X[e]) + hi16(a_[e])); \
    ST128(o, rz1, (l >> 5) * 32768 + (l & 31) * 16, (2 * (i) * 64 + cv) * 512);                \
    ST64(part_stats(o), rps, (l >> 5) * 16384 + (l & 31) * 8, (2 * (i) * 64 + cv) * 256);      \
  }
#define CHUNK_FLAG(i) set_flag(FL + FL_FC + ((8 * (i) + l) * 8 + (cv >> 3)) * 32 + (cv & 7))
  {
#define U_VAL(i)                                                                               \
  {                                                                                            \
    const float mb_ = lo16(st[i]), ri_ = 1024.0f / hi16(st[i]);                                 \
    _Pragma("unroll") for (int e = 0; e < 4; ++e) {                                            \
      const unsigned t_ = pk2(lo16(tx[i][e]) - mb_, hi16(tx[i][e]) - mb_);                     \
      const unsigned q_ = pk2(lo16(t_) * ri_, hi16(t_) * ri_);                                 \
      uv[e] = pkh(lo16(q_), hi16(q_));                                                         \
    }                                                                                          \
  }
    unsigned ta[3], k0p, st[4];
    v4u uv;
    v2u kn;
    u64 tag;
    const __amdgpu_buffer_rsrc_t rst = MKRS(ws + OFF_GT, (L_SEQ + 512) * 16);
    {
      KGen G;
      G.load(A, d, l);
      const u16 pk = A.Dskip[d];
      asm volatile("" ::: "memory");
      if (XL == 1) ask_slow(A, 16 * (w - 4) + (cv & 15), ta);
      if (XL) asm volatile("" : "+v"(G.pa), "+v"(G.pb), "+v"(G.pc), "+v"(G.pd), "+v"(G.pe)::"memory");
      tx[0] = *reinterpret_cast<const v4u*>(A.x0 + (size_t)tid * DM + 8 * cv);
      if (XL != 1) ask_slow(A, 16 * (w - 4) + (cv & 15), ta);
      tag = call_tag(A);
      G.setup();
      dsk = bf2f(pk) * (1.0f / 1024);
      G.st = smem + w * 16640;
      SST(0);
      ysc = G.scale();
      const Cx z = G.power(1.f);
      Cx y = cmul(G.C, z);
      G.rows(G.st + l * 8, y, z, 16);
      G.get(G.bh, G.bl, l);
      const float k0 = 2.0f * wave_sum(G.C.r.x + G.C.r.y);
      k0p = l == 0 ? pkh(k0, 0.f) : 0u;
      if (TP) {
        constexpr int R = 64 * TP, U0 = 2 * R + 32, U1 = 3 * R + 111, KN = 4 * R + 192;
        u16* k1 = reinterpret_cast<u16*>(smem + 8 * 16640) + w * KN;
        u16* t1 = reinterpret_cast<u16*>(smem + 8 * 16640) + 8 * KN;
        v4u s1 = {};
        u64 rv = 0;
        if (w < TP) {
          s1 = LD128(rst, tid * 16, (L_SEQ + R * (cv >> 4)) * 16);
          rv = ready_look(A);
        }
        if (l < R / 8 + 4) reinterpret_cast<v4u*>(k1)[l < R / 8 ? l + R / 8 + 4 : l - R / 8] = v4u{k0p, 0u, 0u, 0u};
        k1[U0 + (l < 32 ? l : R + l)] = k1[U1 + (l < 32 ? l : R + l)] = 0;
        G.columns(l, 0, R / 16);
        const f32x4 kt = G.tile<true>(l);
        if (c < R / 16) *reinterpret_cast<v2u*>(k1 + R + 28 - 16 * c - 4 * g) = v2u{pkh(kt.w, kt.z), pkh(kt.y, kt.x)};
        if (w < TP) {
          for (unsigned spins = 0; !__all(has_tag(tag, s1.x, s1.y) && has_tag(tag, s1.z, s1.w)) && spins < SPIN_CAP; ++spins) {
            __builtin_amdgcn_s_sleep(4);
            s1 = LD128(rst, tid * 16, (L_SEQ + R * (cv >> 4)) * 16);
          }
          st[0] = __builtin_amdgcn_perm(s1.w, s1.y, 0x07060302);
          U_VAL(0);
          u16* up = reinterpret_cast<u16*>(smem + 8 * 16640) + 32 + tid;
#pragma unroll
          for (int e = 0; e < 8; ++e) up[e * KN + U0] = up[e * KN + U1] = (u16)(uv[e >> 1] >> (16 * (e & 1)));
        }
        RAW_BAR();
        const unsigned u1 = lds_base(reinterpret_cast<char*>(k1 + ((c & 1) ? U1 : U0) + c + 8 * g));
        f32x4 a = {};
#pragma unroll
        for (int b = 0; b <= R / 32; ++b)
          a = MFMAH(BFRAG(u1 + 64 * b), *reinterpret_cast<const f16x8*>(k1 + R - 16 * (c & (R / 16 - 1)) + 8 * g + 32 * b), a);
        if (c < R / 16) {
          const v2u sk = *reinterpret_cast<const v2u*>(k1 + U0 + 32 + t0);
          TILE_OUT(t1 + w * (R + 16), a, sk);
        }
        RAW_BAR();
        SST(1);
        if (w < TP) CHUNK_ST(0, t1, R + 16);
        G.columns(l, R / 16, TPS);
        if (w < TP) {
          DRAIN();
          ready_wait(A, tag, rv);
          if (l < 8) CHUNK_FLAG(w);
        }
        SST(6);
        G.columns(l, TPS, 16);
      } else {
        G.columns(l, 0, 16);
      }
      const f32x4 kt = G.tile<true>(l);
      kn = v2u{pkh(kt.w, kt.z), pkh(kt.y, kt.x)};
    }
#define U_PUT(i)                                                                               \
  _Pragma("unroll") for (int e = 0; e < 4; ++e) {                                              \
    ((lph)(unsigned long)up0)[(2 * e) * CUN + 512 * (i)] = (u16)uv[e];                         \
    ((lph)(unsigned long)up0)[(2 * e + 1) * CUN + 512 * (i)] = (u16)(uv[e] >> 16);             \
    ((lph)(unsigned long)up1)[(2 * e) * CUN + 512 * (i)] = (u16)uv[e];                         \
    ((lph)(unsigned long)up1)[(2 * e + 1) * CUN + 512 * (i)] = (u16)(uv[e] >> 16);             \
  }
#define U_ROW(i) { U_VAL(i) U_PUT(i) }
    unsigned up0 = lds_base((char*)(Uc + 32 + tid)), up1 = lds_base((char*)(Us + 32 + tid));
    asm volatile("" : "+v"(up0), "+v"(up1), "+v"(kn), "+v"(k0p));
#define ST_GET(I0, I1)                                                                                                        \
  for (unsigned spins = 0; spins < SPIN_CAP; ++spins) {                                                                       \
    bool ok = true;                                                                                                           \
    v4u t_[4];                                                                                                                \
    _Pragma("unroll") for (int i = I0; i < I1; ++i) t_[i] = LD128(rst, (tid + 512 * i) * 16, 0);                              \
    _Pragma("unroll") for (int i = I0; i < I1; ++i) {                                                                         \
      ok = ok && has_tag(tag, t_[i].x, t_[i].y) && has_tag(tag, t_[i].z, t_[i].w);                                            \
      st[i] = __builtin_amdgcn_perm(t_[i].w, t_[i].y, 0x07060302);                                                            \
    }                                                                                                                         \
    if (__all(ok)) break;                                                                                                     \
    __builtin_amdgcn_s_sleep(4);                                                                                              \
    asm volatile("" ::: "memory");                                                                                            \
  }
    if (w < 4) {
      ST_GET(0, 2);
      U_VAL(0);
      asm volatile("" : "+v"(uv));
    }
#pragma unroll
    for (int i = 1; i < 4; ++i) tx[i] = *reinterpret_cast<const v4u*>(A.x0 + (size_t)(tid + 512 * i) * DM + 8 * cv);
    RAW_BAR();
    SST(2);
    if (w < 4) { U_PUT(0) }
    *reinterpret_cast<v2u*>(kr + 2076 - 16 * c - 4 * g) = kn;
    if (l < 38) reinterpret_cast<v4u*>(kr)[l < 32 ? 260 + l : l < 34 ? 194 + l : l - 34] = v4u{k0p, 0u, 0u, 0u};
    (l < 32 ? uc : Us + w * CUN - 32)[l] = 0;
    uc[32 + L_SEQ + l] = Us[w * CUN + 32 + L_SEQ + l] = 0;
    uc[32 + 256 + l] = Us[w * CUN + 32 + 256 + l] = 0;
    RAW_BAR();
    SST(11);
    {
      f32x4 a0 = {}, a1 = {};
      f16x8 bf[9], af[9];
#pragma unroll
      for (int b = 0; b < 9; ++b) {
        af[b] = KFRAG(32 - 32 * b);
        bf[b] = BFRAG(ub + 64 * (b - 1));
      }
      const v2u k0 = SKIP(0);
      __builtin_amdgcn_sched_barrier(0);
#pragma unroll
      for (int b = 0; b < 9; ++b) (b & 1 ? a1 : a0) = MFMAH(bf[b], af[b], (b & 1 ? a1 : a0));
      a0 += a1;
      TILE_OUT(Ta + w * CTN, a0, k0);
    }
    const u64 rv = ready_look(A);
    RAW_BAR();
    SST(3);
    if (w < 4) {
      if (w >= TP) CHUNK_ST(0, Ta, CTN);
      asm volatile("" : "+v"(tx[1]));
      U_ROW(1);
      DRAIN();
      ready_wait(A, tag, rv);
      if (l < 8 && w >= TP) CHUNK_FLAG(w);
      ST_GET(2, 4);
    } else {
      ST_GET(0, 4);
      ready_wait(A, tag, rv);
      asm volatile("" : "+v"(tx[0]), "+v"(tx[1]));
      U_ROW(0);
      U_ROW(1);
    }
    ASKED(ta);
    if (tid < (16 * CRN + 64) / 8) reinterpret_cast<v4u*>(Rc)[tid] = v4u{0u, 0u, 0u, 0u};
    SST(4);
    const gu64* fk = (gu64*)(ws + OFF_KG) + cv * 16 + w;
    u64 kg = __hip_atomic_load(fk, RLX_AGENT);
    U_ROW(2);
    for (unsigned spins = 0; !__all(kg == tag) && spins < SPIN_CAP; ++spins) {
      __builtin_amdgcn_s_sleep(8);
      kg = __hip_atomic_load(fk, RLX_AGENT);
    }
    asm volatile("" : "+v"(tx[3]), "+v"(st[3])::"memory");
    SST(5);
    const unsigned* kd = reinterpret_cast<const unsigned*>(Ta + 8 * CTN) + w * 392;
    glds16c(ws + OFF_KF + d * 2048, l * 16, lds_base((char*)kd));
    if (l < 34) glds16c(ws + OFF_KF + d * 2048 + 1024, l * 16, lds_base((char*)kd) + 1024);
    U_ROW(3);
    DRAIN();
    {
#define MIX(lo, hi) __builtin_amdgcn_perm(hi, lo, 0x07060100)
      const unsigned a0 = kd[384 - l], b0 = kd[383 - l], a1 = kd[320 - l], b1 = kd[319 - l];
      const unsigned a2 = kd[256 - 2 * l], m2 = kd[255 - 2 * l], b2 = kd[254 - 2 * l];
      const unsigned* k3 = kd + 124 - 4 * (l & 31);
      v4u* ko = reinterpret_cast<v4u*>(kr) + l;
      ko[4] = v4u{a0, a0, (l & 1) ? b0 : MIX(a0, b0), b0};
      ko[68] = v4u{a1, a1, (l & 1) ? b1 : MIX(a1, b1), b1};
      ko[132] = v4u{a2, MIX(a2, m2), m2, b2};
      if (l < 32) ko[196] = v4u{k3[4], MIX(k3[3], k3[2]), k3[2], MIX(k3[1], k3[0])};
#undef MIX
    }
    RAW_BAR();
    SST(7);
    {
      const f16x8 bm = BFRAG(ub - 64);
      f32x4 a1 = MFMAH(bm, KFRAG(256 + 32), (f32x4{})), a2 = MFMAH(bm, KFRAG(512 + 32), (f32x4{}));
#pragma unroll 2
      for (int r = 0; r < 8; ++r) {
        f16x8 a[3], bf[3];
#pragma unroll
        for (int e = 0; e < 3; ++e) {
          a[e] = KFRAG(256 * e - 32 * r);
          bf[e] = BFRAG(ub + 512 * e + 64 * r);
        }
#pragma unroll
        for (int e = 0; e < 3; ++e) {
          a2 = MFMAH(bf[2 - e], a[e], a2);
          if (e < 2) a1 = MFMAH(bf[1 - e], a[e], a1);
        }
      }
      SST(8);
      const v2u k1 = SKIP(1), k2 = SKIP(2);
      TILE_OUT(Ta + w * CTN + 256, a1, k1);
      TILE_OUT(Ta + w * CTN, a2, k2);
    }
    RAW_BAR();
    SST(10);
    if (w < 4) tx[0] = tx[1];
    CHUNK_STX(w < 4 ? w + 8 : w, tx[0], Ta, CTN);
    {
      const f16x8 bm = BFRAG(ub - 64);
#pragma unroll
      for (int T = 3; T < 8; ++T) al[T - 3] = MFMAH(bm, KFRAG(256 * T + 32), (f32x4{}));
    }
    SST(9);
#undef U_ROW
#undef U_VAL
#undef U_PUT
#undef ST_GET
  }
  const unsigned rb = lds_base(reinterpret_cast<char*>(((c & 1) ? Rs : Rc) + w * CRN + c + 8 * g));
#pragma unroll
  for (int j = 0; j < 8; ++j) acc[j] = f32x4{};
  const __amdgpu_buffer_rsrc_t rx = MKRS(ws + OFF_X2, SZ_X), rz = MKRS(ws + OFF_X3, SZ_X);
  const __amdgpu_buffer_rsrc_t rf = MKRS(FL + FL_FR, 64 * 16 * 128);
  v2u st = {0u, 0u}, xx = {0u, 0u};
#define X2_LOAD(ss, far)                                                                                                      \
  {                                                                                                                      \
    xx = LD64(rx, (l >> 1) * (DM * 2) + (l & 1) * 8, (far) ? (int)SZ_X : ((ss) * SEG * DM + 8 * cv) * 2);                 \
    st = LD64(rf, l * 8, ((ss) * 4 + (blockIdx.x & 3)) * 512);                                                           \
  }
  unsigned tl = now10();
  static_assert(R0 >= 2 && PP >= 8 - R0, "rounds");
  for (int s = -R0; s < NSEG; ++s) {
    const int q = s & 3, q8 = s & 7;
    const u16* kq = ka + 32 * q8;
    if (w == 1 && s >= 0) {
      gate(__all(mk != 0u) ? latest(mk, tl) + CE : tl);
      X2_LOAD(s, 0);
    }
    asm volatile("" ::: "memory");
    if (s < 8 - R0) {
      const int r = s + R0;
      const u16* kl = ka + 32 * r - 256 * 7;
      unsigned uq[4] = {ub + 64 * r, ub + 64 * r + 1024, ub + 64 * r + 2048, ub + 64 * r + 3072};
      asm volatile("" : "+v"(uq[0]), "+v"(uq[1]), "+v"(uq[2]), "+v"(uq[3]));
      f16x8 bf[8], a[8];
#pragma unroll
      for (int e = 7; e >= 0; --e) {
        a[e] = *reinterpret_cast<const f16x8*>(kl + 256 * (7 - e));
        bf[7 - e] = BFRAG(uq[(7 - e) >> 1] + 512 * ((7 - e) & 1));
      }
      __builtin_amdgcn_sched_barrier(0);
#pragma unroll
      for (int e = 7; e >= 0; --e)
#pragma unroll
        for (int T = (e > 3 ? e : 3); T < 8; ++T) al[T - 3] = MFMAH(bf[T - e], a[e], al[T - 3]);
      if (r == 1) {
        DRAIN();
        if (l < 8) CHUNK_FLAG(w < 4 ? w + 8 : w);
      }
      if (r == 7) {
        v2u k[5];
#pragma unroll
        for (int T = 3; T < 8; ++T) k[T - 3] = SKIP(T);
#pragma unroll
        for (int T = 3; T < 8; ++T) TILE_OUT(uc + 32 + 256 * T, al[T - 3], k[T - 3]);
      }
      if (s == -1 && w == 1) {
        (void)wait_n<SLP_V, 32>(FL + FL_FZ + (blockIdx.x & 7) * 64, 64, TMO);
#pragma unroll 1
        for (unsigned spins = 0; spins < SPIN_CAP; ++spins) {
          mk = MARKS(0);
          if (__all(mk != 0u)) break;
          __builtin_amdgcn_s_sleep(16);
          asm volatile("" ::: "memory");
        }
      }
      if (s < 0) continue;
    }
    f16x8 a0 = *reinterpret_cast<const f16x8*>(kq - 32), a1 = *reinterpret_cast<const f16x8*>(kq);
    asm volatile("" : "+v"(a0), "+v"(a1));
    if (w == 1) {
#pragma unroll 1
      for (unsigned spins = 0; !__all(umin3(xx.x, xx.y, st.y) != 0u); ++spins) {
        if (spin_timeout(spins, TMO)) break;
        __builtin_amdgcn_s_sleep(SLP_V);
        X2_LOAD(s, __builtin_popcountll(__ballot(st.y != 0u)) < CVT2);
      }
      TS(0, s, 64);
      const float s0 = __uint_as_float(st.x), s1 = dppr<0xb1>(s0), m2 = __uint_as_float(st.y);
      const float var = (m2 + dppr<0xb1>(m2) + (s0 - s1) * (s0 - s1) * (1.0f / DM)) * (1.0f / DM);
      const float mb = bfr1((s0 + s1) * (1.0f / DM)), ri = 1024.0f * __builtin_amdgcn_rcpf(bfr1(__builtin_amdgcn_sqrtf(bfr1(bfr1(var) + 1e-5f))));
      const unsigned t0 = pk2(lo16(xx.x) - mb, hi16(xx.x) - mb), t1 = pk2(lo16(xx.y) - mb, hi16(xx.y) - mb);
      const unsigned b0 = pk2(lo16(t0) * ri, hi16(t0) * ri), b1 = pk2(lo16(t1) * ri, hi16(t1) * ri);
      const unsigned u0 = pkh(lo16(b0), hi16(b0)), u1 = pkh(lo16(b1), hi16(b1));
      u16* rp = Rc + (l & 1) * 4 * CRN + 32 * q + (l >> 1);
#define R_PUT(o) { rp[o] = (u16)u0; rp[(o) + CRN] = (u16)(u0 >> 16); rp[(o) + 2 * CRN] = (u16)u1; rp[(o) + 3 * CRN] = (u16)(u1 >> 16); }
      R_PUT(0) R_PUT(8 * CRN + 63)
      if (q == 0) { R_PUT(128) R_PUT(8 * CRN + 63 + 128) }
#undef R_PUT
      *reinterpret_cast<v2u*>(Sx + l * 4) = xx;
    }
    unsigned ap = rb + 64 * ((q + 3) & 3), ac = rb + 64 * q, au = lds_base((char*)(Rc + w * CRN + 32 * q + c));
    asm volatile("" : "+v"(ap), "+v"(ac), "+v"(au));
    RAW_BAR();
    TS(1, s, 0);
    const f16x8 bp = BFRAG(ap), bc = BFRAG(ac);
    const u16 h0 = ((lph)(unsigned long)au)[0], h1 = ((lph)(unsigned long)au)[16], y0 = Sx[c * 8 + w], y1 = Sx[(16 + c) * 8 + w];
    __builtin_amdgcn_sched_barrier(0);
    const float u0 = h2f(h0), u1 = h2f(h1), x0 = bf2f(y0), x1 = bf2f(y1);
    acc[0] = MFMAH(a0, bp, (q8 == 0 ? acc[1] : acc[0]));
    const f32x4 tmp = MFMAH(a1, bc, acc[0]);
    const unsigned uu = pk2(u0 * dsk, u1 * dsk);
    const unsigned yy = pk2(((q & 1) ? tmp[2] : tmp[0]) * ysc, ((q & 1) ? tmp[3] : tmp[1]) * ysc);
    const unsigned aa = pk2(lo16(yy) + lo16(uu), hi16(yy) + hi16(uu)), oo = pk2(x0 + lo16(aa), x1 + hi16(aa));
    if (g == (q8 >> 1)) {
      To[c * 8 + w] = (u16)oo;
      To[(16 + c) * 8 + w] = (u16)(oo >> 16);
    }
    RAW_BAR();
    if (w == 0 && l < 32) {
      v4u o = *reinterpret_cast<const v4u*>(To + l * 8);
      o.y = o.y ? o.y : 0x8000u;
      ST128A(o, rz, l * 16, (s * 64 + cv) * 512, XAUX);
      ST64(part_stats(o), rps, l * 8, ((64 + s) * 64 + cv) * 256);
    }
    if (w == 1) {
      tl = now10() + GATE_V - 40;
      mk = MARKS(s + 1);
    }
    const int left = 7 - (s >> 3);
#define PUSH(J0, SH)                                                                                              \
  if (left >= (J0)) {                                                                                             \
    _Pragma("unroll") for (int j = (J0); j < (J0) + 2 && j < 8; ++j)                                              \
      acc[j] = MFMAH(*reinterpret_cast<const f16x8*>(kq - 32 - 256 * j), bp, (j + (SH) < 8 ? acc[(j + (SH)) & 7] : f32x4{})); \
  }
    TS(2, s, 0);
    if (w == 0) {
      DRAIN();
      if (l < 4) set_flag(FL + FL_FC + (((64 + s) * 4 + l) * 8 + (cv >> 3)) * 32 + (cv & 7));
    }
    TS(3, s, 0);
    if (q8 == 0) { PUSH(1, 1) PUSH(3, 1) PUSH(5, 1) PUSH(7, 1) }
    else { PUSH(1, 0) PUSH(3, 0) PUSH(5, 0) PUSH(7, 0) }
#undef PUSH
    if ((unsigned)(s - PP) < 4u * PS && (s - PP) % PS == 0) {
      const int i = 1 + (s - PP) / PS;
      if (i > 1) {
        DRAIN();
        if (l < 8) CHUNK_FLAG(w + 8 * i - 8);
        tx[1] = tx[2];
        tx[2] = tx[3];
      }
      if (i < 4 && (i > 1 || w >= 4)) CHUNK_STX(w + 8 * i, tx[1], Uc + 32 + 512 * i, CUN);
    }
    MSTAMP();
  }
#undef KFRAG
#undef BFRAG
#undef X2_LOAD
#undef TILE_OUT
#undef SKIP
#undef CHUNK_ST
#undef CHUNK_STX
#undef CHUNK_FLAG
}

__device__ __forceinline__ v4u ln_row_packed(const v4u xr) {
  float v[8];
#pragma unroll
  for (int e = 0; e < 4; ++e) { v[2 * e] = lo16(xr[e]); v[2 * e + 1] = hi16(xr[e]); }
  float s = ((v[0] + v[1]) + (v[2] + v[3])) + ((v[4] + v[5]) + (v[6] + v[7]));
  s = wave_sum(s);
  const float mean = s * (1.0f / DM);
  float q = 0.f;
#pragma unroll
  for (int j = 0; j < 8; ++j) { float t = v[j] - mean; q += t * t; }
  q = wave_sum(q);
  const float var = q * (1.0f / DM);
  const float mb = bfr(mean);
  const float sd = bfr(sqrtf(bfr(bfr(var) + 1e-5f)));
  const float ri = 1.0f / sd;
  v4u o;
#pragma unroll
  for (int e = 0; e < 4; ++e) {
    const unsigned t = pk2(v[2 * e] - mb, v[2 * e + 1] - mb);
    const float x0 = lo16(t), x1 = hi16(t);
    float q0 = x0 * ri, q1 = x1 * ri;
    q0 = __builtin_fmaf(__builtin_fmaf(-q0, sd, x0), ri, q0);
    q1 = __builtin_fmaf(__builtin_fmaf(-q1, sd, x1), ri, q1);
    o[e] = pk2(q0, q1);
  }
  return o;
}

__device__ __forceinline__ void ln4_packed(v4u (&x)[4]) {
  f32x2_t v[16];
#pragma unroll
  for (int e = 0; e < 16; ++e) v[e] = f32x2_t{lo16(x[e >> 2][e & 3]), hi16(x[e >> 2][e & 3])};
  const f32x2_t t = (((v[0] + v[1]) + (v[2] + v[3])) + ((v[4] + v[5]) + (v[6] + v[7]))) +
                    (((v[8] + v[9]) + (v[10] + v[11])) + ((v[12] + v[13]) + (v[14] + v[15])));
  const float mean = red_mod4(t.x + t.y) * (1.0f / DM);
  const float mb = bfr1(mean);
  f32x2_t q0 = {0.f, 0.f}, q1 = {0.f, 0.f};
#pragma unroll
  for (int e = 0; e < 16; e += 2) {
    v[e] -= mb;
    v[e + 1] -= mb;
    q0 += v[e] * v[e];
    q1 += v[e + 1] * v[e + 1];
  }
  q0 += q1;
  const float dm = mean - mb;
  const float var = fmaxf(red_mod4(q0.x + q0.y) * (1.0f / DM) - dm * dm, 0.f);
  const float sd = bfr1(sqrtf(bfr1(bfr1(var) + 1e-5f)));
  const float ri = 1.0f / sd;
#pragma unroll
  for (int e = 0; e < 16; ++e) {
    const unsigned t2 = pk2(v[e].x, v[e].y);
    const f32x2_t q = f32x2_t{lo16(t2), hi16(t2)} * ri;
    x[e >> 2][e & 3] = pk2(q.x, q.y);
  }
}

constexpr int MEGA_WGS = 256;
constexpr size_t MEGA_LDS = (size_t)(32 * 4 * 64 * 8 + 8 * SMS + 15 * QS + 128 + 16 * 64 * 8 + 8 * SOS + 512) * 2;
static_assert(MEGA_LDS <= 163840 && MEGA_LDS - 480 >= C_SQ + 4096, "lds");
static_assert(MEGA_LDS >= (size_t)AW_CNT + 128 && MEGA_LDS >= (size_t)P_CN + 512, "lds");
static_assert(MEGA_LDS >= (size_t)CV_LDS, "lds");
static_assert(MEGA_LDS >= (size_t)2 * 32 * SMS * 2 && MEGA_LDS >= 131072, "lds");
static_assert(MEGA_LDS >= (size_t)8 * (16640 + 2 * (704 + 144)), "lds");
__device__ __forceinline__ MegaArgs role_args(const int b, const int wv, const unsigned k0, const unsigned k1) {
  auto p = (const __attribute__((address_space(4))) u64*)__builtin_amdgcn_kernarg_segment_ptr();
  asm volatile("s_waitcnt lgkmcnt(0)" : "+s"(p) : "s"(k0), "s"(k1));
#define KA(i) ((u16*)(__attribute__((address_space(1))) u16*)p[i])
  return MegaArgs{KA(0), KA(1), (char*)KA(2), KA(3), KA(4), KA(5), KA(6), KA(7), KA(8), KA(9), KA(10), KA(11), KA(12), KA(13), b, wv};
#undef KA
}
__global__ __launch_bounds__(512, 2) void mega_kernel(const u16* mem0, const u16* Dskip, char* ws, u16* out, const u16* x0,
                                                      const u16* logAre, const u16* Aim, const u16* Cre, const u16* Cim,
                                                      const u16* logdt, const u16* Wq, const u16* Wk, const u16* Wv,
                                                      const u16* Wo) {
  extern __shared__ __attribute__((aligned(16))) char smem[];
  unsigned k0 = 0u, k1 = 0u;
  if (IPF == 3) {
    unsigned o;
    asm volatile(
        "s_load_dword %0, %3, 0\n\ts_load_dword %1, %3, 64\n\ts_getpc_b64 s[6:7]\n.LKG_PC:\n\ts_bfe_u32 s4, %4, 0x30003\n\t"
        "v_lshl_add_u32 %2, %5, 3, s4\n\tv_lshlrev_b32 %2, 7, %2\n\ts_mov_b32 m0, %6\n\ts_mov_b32 s4, .LKG_LAST-.LKG_PC\n\t"
        "s_max_i32 s4, s4, .LKG_LAS2-.LKG_PC\n\tv_cmp_gt_i32 vcc, s4, %2\n\t"
        "s_and_saveexec_b64 s[4:5], vcc\n\tglobal_load_lds_dword %2, s[6:7]\n\ts_mov_b64 exec, s[4:5]"
        : "=&s"(k0), "=&s"(k1), "=&v"(o)
        : "s"(__builtin_amdgcn_kernarg_segment_ptr()), "s"(blockIdx.x), "v"(threadIdx.x), "s"(lds_base(smem) + (unsigned)MEGA_LDS - 480)
        : "memory", "vcc", "scc", "s4", "s5", "s6", "s7");
  }
  const int b = vbid(), wv = __builtin_amdgcn_readfirstlane(threadIdx.x >> 6);
#define A role_args(b, wv, k0, k1)
  u64 tag = 0;
  if (__builtin_expect((0xde000ff0u >> (b >> 3)) & 1u, 0)) {
    const unsigned t0 = now10();
    if ((b >= 32 && b < 64) || (b >= 200 && b < 232)) start_kgen(A, smem, b < 64 ? b - 32 : b - 168);
    else if (b >= 64 && b < 96) tag = start_stats<4>(A, b - 64);
    else if (b >= 240) (void)start_stats<8>(A, b - 224);
    const unsigned dl = (b >= 32 && b < 64) ? DL_C : (b >= 64 && b < 80) ? DL_A : (b >= 80 && b < 96) ? DL_A2 : b >= 240 ? DL_R2 : ((b >= 96 && b < 104) || b >= 232) ? DL_R : (b >= 200 && b < 232) ? DL_Q : 0u;
    while (now10() - t0 < dl) __builtin_amdgcn_s_sleep(16);
    __builtin_amdgcn_s_waitcnt(0x0f70);
  }
  if ((EXP_OFF & 1) && b >= 64 && b < 80) return;
  if ((EXP_OFF & 8) && b >= 200 && b < 232) return;
  if ((EXP_OFF & 16) && b >= 32 && b < 64) return;
  if ((EXP_OFF & 32) && b >= 80 && b < 96) return;
  if (b < 64) role_chain(A, smem, b >> 5, b & 31, 0);
  else if (b < 96) role_aw(A, smem, (b >> 4) & 1, b & 15, tag);
  else if (b >= 104 && b < 168) role_conv(A, smem, b - 104);
  else {
    const bool kv = b >= 168 && b < 232;
    role_proj(A, smem, b >= (kv ? 200 : 240), kv ? (b - 168) & 31 : b & 15, kv);
  }
#undef A
}

extern "C" void kernel_launch(void* const* d_in, const int* in_sizes, int n_in, void* d_out, int out_size, void* d_ws,
                              size_t ws_size, hipStream_t stream) {
  MegaArgs A;
  A.x0 = (const u16*)d_in[0];
  A.logAre = (const u16*)d_in[1];
  A.Aim = (const u16*)d_in[2];
  A.Cre = (const u16*)d_in[3];
  A.Cim = (const u16*)d_in[4];
  A.logdt = (const u16*)d_in[5];
  A.Dskip = (const u16*)d_in[6];
  A.mem0 = (const u16*)d_in[8];
  A.Wq = (const u16*)d_in[9];
  A.Wk = (const u16*)d_in[10];
  A.Wv = (const u16*)d_in[11];
  A.Wo = (const u16*)d_in[12];
  A.ws = (char*)d_ws;
  A.out = (u16*)d_out;

  (void)hipFuncSetAttribute((const void*)mega_kernel, hipFuncAttributeMaxDynamicSharedMemorySize, (int)MEGA_LDS);
  mega_kernel<<<MEGA_WGS, 512, MEGA_LDS, stream>>>(A.mem0, A.Dskip, A.ws, A.out, A.x0, A.logAre, A.Aim, A.Cre, A.Cim, A.logdt,
                                                   A.Wq, A.Wk, A.Wv, A.Wo);
}
```

```cpp
#include <hip/hip_runtime.h>
#include <hip/hip_bf16.h>
#include <stdint.h>

constexpr int L_SEQ = 2048;
constexpr int DM    = 512;
constexpr int NST   = 128;
constexpr int NH    = 32;
constexpr int DH    = 128;
constexpr int HD    = NH * DH;
constexpr int MM    = 8;
constexpr int SEG   = 32;
constexpr int NSEG  = L_SEQ / SEG;
constexpr int LK    = MM + SEG;

typedef unsigned short u16;
using bf16x8 = __attribute__((ext_vector_type(8))) short;
using f32x4  = __attribute__((ext_vector_type(4))) float;
using f32x16 = __attribute__((ext_vector_type(16))) float;

static __device__ __forceinline__ u16 f2bf(float f) {
  unsigned u = __float_as_uint(f);
  u += 0x7fffu + ((u >> 16) & 1u);
  return (u16)(u >> 16);
}
static __device__ __forceinline__ float bf2f(u16 h) { return __uint_as_float(((unsigned)h) << 16); }
static __device__ __forceinline__ float bfr(float f) { return bf2f(f2bf(f)); }

constexpr size_t SZ_X      = (size_t)L_SEQ * DM * 2;
constexpr size_t OFF_KF    = 0;
constexpr size_t OFF_UT    = OFF_KF + (size_t)DM * L_SEQ * 4;
constexpr size_t OFF_YT    = OFF_UT + SZ_X;
constexpr size_t OFF_XA    = OFF_YT + SZ_X;
constexpr size_t OFF_H     = OFF_XA + SZ_X;
constexpr size_t OFF_X2    = OFF_H + SZ_X;
constexpr size_t OFF_U2    = OFF_X2 + SZ_X;
constexpr size_t OFF_X3    = OFF_U2 + SZ_X;
constexpr size_t SZ_QK     = (size_t)L_SEQ * 2 * HD * 2;
constexpr size_t OFF_QK    = OFF_X3 + SZ_X;
constexpr size_t SZ_O      = (size_t)L_SEQ * HD * 2;
constexpr size_t OFF_VF    = OFF_QK + 2 * SZ_QK;
constexpr size_t SZ_PW     = (size_t)2 * NSEG * 16 * SEG * DM * 2;
constexpr size_t OFF_PW    = OFF_VF + 2 * SZ_O;
constexpr size_t SZ_MEM    = (size_t)NSEG * MM * HD * 2;
constexpr size_t OFF_MEMK  = OFF_PW + SZ_PW;
constexpr size_t OFF_MEMV  = OFF_MEMK + 2 * SZ_MEM;
constexpr size_t OFF_GT    = OFF_MEMV + 2 * SZ_MEM;
constexpr size_t OFF_EP    = OFF_GT + 65536;
constexpr size_t OFF_RD    = OFF_EP + 4096;
constexpr size_t OFF_KG    = OFF_RD + 4096;
constexpr size_t OFF_G     = OFF_KG + 8192;
constexpr size_t SZ_ACC    = (size_t)2 * 65536;
constexpr size_t OFF_Z     = OFF_GT + (size_t)128 * DM * 4;
constexpr size_t OFF_ACC   = OFF_Z;
constexpr size_t OFF_FL    = OFF_ACC + 2 * SZ_ACC;
constexpr int FL_FK  = 0;
constexpr int FL_FW  = FL_FK + 64 * 8 * 32;
constexpr int FL_FR  = FL_FW + 2 * 64 * 16 * 32;
constexpr int FL_FC  = FL_FR + 64 * 16 * 32;
constexpr int FL_FQ  = FL_FC + 2 * 64 * 32 * 32;
constexpr int FL_FP  = FL_FQ + 2 * 64 * 32 * 32;
constexpr int FL_FD  = FL_FP + 64 * 32 * 32;
constexpr int FL_FZ  = FL_FD + 64 * 2 * 32;
constexpr int FL_TMO = FL_FZ + 16 * 32;
constexpr int FL_END = FL_TMO + 64;
constexpr size_t Z_BYTES   = OFF_FL + (size_t)FL_END * 4 - OFF_Z;
constexpr size_t WS_TOTAL  = OFF_Z + Z_BYTES;
static_assert(Z_BYTES % 16 == 0 && OFF_Z % 256 == 0 && Z_BYTES <= 4 * 64 * 8192 && OFF_G + 2048 <= OFF_Z, "zero region");
static_assert(WS_TOTAL + (4 << 20) <= (size_t)256 * 1024 * 1024, "workspace too large");

static_assert(WS_TOTAL - OFF_XA <= (size_t)384 * 8 * 65536, "translation warm-up");
constexpr int CKN = 2336;
static_assert((size_t)DM * 2 * CKN * 2 <= (size_t)DM * L_SEQ * 4 + SZ_X, "K buffer");
__device__ __forceinline__ float pos_of_index(int p) {
  if (p < 256) return (float)p;
  if (p < 384) return (float)(256 + 2 * (p - 256));
  if (p < 512) return (float)(512 + 4 * (p - 384));
  if (p < 640) return (float)(1024 + 8 * (p - 512));
  return 2048.0f;
}
__device__ __forceinline__ int index_of_l(int l) {
  float pf = bfr((float)l);
  int p = (int)pf;
  if (p < 256) return p;
  if (p < 512) return 256 + ((p - 256) >> 1);
  if (p < 1024) return 384 + ((p - 512) >> 2);
  if (p < 2048) return 512 + ((p - 1024) >> 3);
  return 640;
}

template <int CTRL> __device__ __forceinline__ float dppr(float x) {
  return __builtin_bit_cast(float, __builtin_amdgcn_update_dpp(0, __builtin_bit_cast(int, x), CTRL, 0xf, 0xf, false));
}
__device__ __forceinline__ float wave_sum(float v) {
  v += dppr<0x121>(v);
  v += dppr<0x122>(v);
  v += dppr<0x124>(v);
  v += dppr<0x128>(v);
  {
    auto r = __builtin_amdgcn_permlane16_swap(__float_as_uint(v), __float_as_uint(v), false, false);
    v = __uint_as_float(r[0]) + __uint_as_float(r[1]);
  }
  {
    auto r = __builtin_amdgcn_permlane32_swap(__float_as_uint(v), __float_as_uint(v), false, false);
    v = __uint_as_float(r[0]) + __uint_as_float(r[1]);
  }
  return v;
}
__device__ __forceinline__ void ln_row8(float (&v)[8]) {
  float s = 0.f;
#pragma unroll
  for (int j = 0; j < 8; ++j) s += v[j];
  s = wave_sum(s);
  const float mean = s * (1.0f / DM);
  float q = 0.f;
#pragma unroll
  for (int j = 0; j < 8; ++j) { float t = v[j] - mean; q += t * t; }
  q = wave_sum(q);
  const float var = q * (1.0f / DM);
  const float mb = bfr(mean);
  const float sd = bfr(sqrtf(bfr(bfr(var) + 1e-5f)));
#pragma unroll
  for (int j = 0; j < 8; ++j) v[j] = bfr(bfr(v[j] - mb) / sd);
}

#define RLX_AGENT __ATOMIC_RELAXED, __HIP_MEMORY_SCOPE_AGENT
typedef __attribute__((address_space(1))) unsigned gu32;
typedef unsigned v4u __attribute__((ext_vector_type(4)));
constexpr int AUX_SC1 = 16;
constexpr unsigned SPIN_CAP = 1u << 18;

__device__ __forceinline__ bool wait_flags(const unsigned* flags, int lane, int nfl, unsigned want, unsigned* tmo) {
  for (unsigned spins = 0;; ++spins) {
    unsigned v = want;
    if (lane < nfl) v = __hip_atomic_load((gu32*)flags + lane * 32, RLX_AGENT);
    if (__all(v >= want)) break;
    if (spins > SPIN_CAP) {
      if (lane == 0) __hip_atomic_store((gu32*)tmo, 1u, RLX_AGENT);
      return false;
    }
  }
  __builtin_amdgcn_fence(__ATOMIC_ACQUIRE, "wavefront");
  return true;
}

#define STAMP(i)
#define STAMP_DUMP()
#define WSTAMP(i)
template <int CTRL> __device__ __forceinline__ float dppf(float old, float x) {
  return __builtin_bit_cast(float, __builtin_amdgcn_update_dpp(__builtin_bit_cast(int, old), __builtin_bit_cast(int, x), CTRL, 0xf, 0xf, false));
}
__device__ __forceinline__ float wave_max_dpp(float x) {
  const float ninf = -3e38f;
  x = fmaxf(x, dppf<0x111>(ninf, x));
  x = fmaxf(x, dppf<0x112>(ninf, x));
  x = fmaxf(x, dppf<0x114>(ninf, x));
  x = fmaxf(x, dppf<0x118>(ninf, x));
  x = fmaxf(x, dppf<0x142>(ninf, x));
  x = fmaxf(x, dppf<0x143>(ninf, x));
  return __builtin_bit_cast(float, __builtin_amdgcn_readlane(__builtin_bit_cast(int, x), 63));
}
__device__ __forceinline__ float wave_sum_dpp(float x) {
  x += dppf<0x111>(0.f, x);
  x += dppf<0x112>(0.f, x);
  x += dppf<0x114>(0.f, x);
  x += dppf<0x118>(0.f, x);
  x += dppf<0x142>(0.f, x);
  x += dppf<0x143>(0.f, x);
  return __builtin_bit_cast(float, __builtin_amdgcn_readlane(__builtin_bit_cast(int, x), 63));
}
constexpr int SMS = 528;
constexpr int QS = 144;
constexpr int VS = 80;
constexpr int SOS = 144;
#define VAR_NREP 4
constexpr int NREP = VAR_NREP;
__device__ __forceinline__ float xsum16(float x) {
  auto r = __builtin_amdgcn_permlane16_swap(__float_as_uint(x), __float_as_uint(x), false, false);
  return __uint_as_float(r[0]) + __uint_as_float(r[1]);
}
__device__ __forceinline__ float xsum32(float x) {
  auto r = __builtin_amdgcn_permlane32_swap(__float_as_uint(x), __float_as_uint(x), false, false);
  return __uint_as_float(r[0]) + __uint_as_float(r[1]);
}
__device__ __forceinline__ float xmax16(float x) {
  auto r = __builtin_amdgcn_permlane16_swap(__float_as_uint(x), __float_as_uint(x), false, false);
  return fmaxf(__uint_as_float(r[0]), __uint_as_float(r[1]));
}
__device__ __forceinline__ float xmax32(float x) {
  auto r = __builtin_amdgcn_permlane32_swap(__float_as_uint(x), __float_as_uint(x), false, false);
  return fmaxf(__uint_as_float(r[0]), __uint_as_float(r[1]));
}
__device__ __forceinline__ float red_mod4(float x) {
  x += dppf<0x124>(0.f, x);
  x += dppf<0x128>(0.f, x);
  return xsum32(xsum16(x));
}
typedef __bf16 bf16x2_t __attribute__((ext_vector_type(2)));
typedef float f32x2_t __attribute__((ext_vector_type(2)));
__device__ __forceinline__ unsigned pk2(float a, float b) {
  f32x2_t v = {a, b};
  bf16x2_t r = __builtin_convertvector(v, bf16x2_t);
  return __builtin_bit_cast(unsigned, r);
}
typedef _Float16 f16x2_t __attribute__((ext_vector_type(2)));
typedef _Float16 f16x8 __attribute__((ext_vector_type(8)));
__device__ __forceinline__ unsigned pkh(float a, float b) {
  f32x2_t v = {a, b};
  return __builtin_bit_cast(unsigned, __builtin_convertvector(v, f16x2_t));
}
__device__ __forceinline__ float h2f(u16 h) { return (float)__builtin_bit_cast(_Float16, h); }
__device__ __forceinline__ float one(float x) {
  asm("" : "+v"(x));
  return x;
}
__device__ __forceinline__ unsigned pk2s(float a, float b) { return pk2(one(a), one(b)); }
__device__ __forceinline__ float dot2sq(const unsigned x, const float acc) {
  return __builtin_amdgcn_fdot2_f32_bf16(__builtin_bit_cast(bf16x2_t, x), __builtin_bit_cast(bf16x2_t, x), acc, false);
}
__device__ __forceinline__ float lo16(unsigned u) { return __uint_as_float(u << 16); }
__device__ __forceinline__ float hi16(unsigned u) { return __uint_as_float(u & 0xffff0000u); }
__device__ __forceinline__ float bfr1(float x) { return lo16(pk2(x, x)); }
__device__ __forceinline__ float half_sum(float v) {
  v += dppr<0x121>(v);
  v += dppr<0x122>(v);
  v += dppr<0x124>(v);
  v += dppr<0x128>(v);
  return xsum16(v);
}
__device__ __forceinline__ void scale_pair(float a, float b, float scale, float& oa, float& ob) {
  unsigned u = pk2(a, b);
  unsigned v = pk2s(lo16(u) * scale, hi16(u) * scale);
  oa = lo16(v);
  ob = hi16(v);
}

#define XMAP 2
__device__ __forceinline__ int vbid() {
  const int b = blockIdx.x, x = b & 7, t = b >> 3;
  if (XMAP == 2) {
    const int q = (t >> 2) + 2 * (x & 4);
    const unsigned long long m = (q & 8) ? 0xf0d85040cdc9ada9ull : 0x76726e6a25210501ull;
    const int e = (int)(m >> (8 * (q & 7))) & 255;
    return (e & 252) + ((x & 3) << (2 + (e & 3))) + (t & 3) + (q == 14 ? ((x & 2) ? 8 : -120) : 0);
  }
  if (XMAP == 0) {
    if (b < 64) return (b & 32) + 4 * x + (t & 3);
    if (b < 96) return (b & ~15) + 2 * x + (t & 1);
    if (b >= 104 && b < 168) return 104 + 8 * x + t - 13;
    if (b >= 168 && b < 232) return 168 + ((b - 168) & 32) + 4 * x + ((t - 21) & 3);
    return b;
  }
  if (XMAP == 3) {
    const int y = x & 3;
    if (t >= 16 && t < 24) return 104 + 16 * y + 2 * (x & 4) + t - 16;
    if (x < 4) return t < 16 ? 4 * (t & 8) + 8 * x + (t & 7) : 64 + 4 * (t & 4) + 4 * y + (t & 3);
    if (t >= 24) return (t >= 28 ? 240 : y < 2 ? 96 : 224) + 4 * y + (t & 3);
    return 168 + 4 * (t & 8) + 8 * y + (t & 7);
  }
  if (XMAP == 4) {
    const int y = x & 3;
    if (x < 4) return t < 16 ? 4 * (t & 8) + 8 * x + (t & 7) : t < 28 ? 104 + 16 * y + t - 16 : 64 + 4 * y + (t & 3);
    if (t >= 24) return (t >= 28 ? 240 : y < 2 ? 96 : 224) + 4 * y + (t & 3);
    if (t < 16) return 168 + 4 * (t & 8) + 8 * y + (t & 7);
    return t < 20 ? 104 + 16 * y + t - 4 : 80 + 4 * y + (t & 3);
  }
  if (XMAP == 2 || XMAP >= 5) {
    if (XMAP == 5 && x < 4 && t < 16) return 16 * x + t;
    if (XMAP == 6 && x < 4 && t < 16) return 32 * (x & 1) + 8 * (x & 2) + t;
    if (XMAP == 7 && x < 4) return (x & 1) ? 104 + 16 * (x & 2) + t : 16 * x + t;
    if (x < 4) return t < 16 ? 4 * (t & 8) + 8 * x + (t & 7) : 104 + 16 * x + t - 16;
    const int y = x - 4;
    if (t >= 24) return (t >= 28 ? 240 : y < 2 ? 96 : 224) + 4 * y + (t & 3);
    if (t < 16) return 168 + 4 * (t & 8) + 8 * y + (t & 7);
    return 64 + 4 * (t & 4) + 4 * y + (t & 3);
  }
  if (x < 2) return 2 * (t & 16) + 16 * x + (t & 15);
  const int i = 16 * (x - 2) + (t & 15);
  if (t < 16) {
    const int k = (i * 171) >> 9, r = i - 3 * k;
    return r == 0 ? 168 + k : r == 1 ? 200 + k : 64 + 16 * (k & 1) + (k >> 1);
  }
  const int g = i >> 3, e = i & 7;
  if (g == 3) return 96 + e;
  if (g == 7) return 232 + e;
  if (g == 9) return 240 + e;
  if (g == 11) return 248 + e;
  return 104 + 8 * (g - (g > 3) - (g > 7) - (g > 9)) + e;
}
#define MSTAMP()
#define WSTAMP2()
#define SST(k)
#define SSX(k, c)
#define SSW(k, v)
#define TS(k, ss, t)
#define LS(k, ss)
#define LS_DUMP()
struct MegaArgs {
  const u16* mem0;
  const u16* Dskip;
  char* ws;
  u16* out;
  const u16* x0;
  const u16 *logAre, *Aim, *Cre, *Cim, *logdt;
  const u16 *Wq, *Wk, *Wv, *Wo;
  int vb, wv;
};

__device__ __forceinline__ int lane_id() {
  int l;
  asm volatile("v_mbcnt_lo_u32_b32 %0, -1, 0\n\tv_mbcnt_hi_u32_b32 %0, -1, %0" : "=v"(l));
  __builtin_assume(l >= 0 && l < 64);
  return l;
}
__device__ __forceinline__ bool spin_timeout(unsigned spins, unsigned* tmo) {
  if (spins > SPIN_CAP) {
    if (lane_id() == 0) __hip_atomic_store((gu32*)tmo, 1u, RLX_AGENT);
    return true;
  }
  return false;
}
#define ROT_H (4 * (h & 1))
#define ROT_T (h & 2)
#define C2G 420
#define CVSP 40
#define PFA 0
#define PFB 0
#define WO1 1
#define AWH 0
#define PEEK0 1
#define SLP0 2
#define GF_R 400
#define GF_V 0
#define PP (9 - R0)
#define EL 1
#define IPF 3
#define XL 0
#define MC3 2
#define XZD 300
#define XZP 4
#define NFW 1
#define IPS 128u
#define WNT 17
#define OAUX 16
#define XAUX 16
#define GF_RT 0
#define GF_RU 0
#define EF 1
#define RGB 3
#define GR2 345
#define GR3 280
#define RLS 1
#define GT1 120
#define GT2 32
#define GT3 1
#define CE 190
#define KE 0
#define PE KE
#define KD 360
#define PD (KD + 20)
#define PC0 2
#define GR0 6
#define DL_A2 DL_A
#define PCT 0
#define EP 1
#define CQ 1
#define K0 40
#define PW 1
#define R0 3
#define PSW 1
#define RSK 0
#define KC 0
#define KCT 0
#define KR 1
#define K1 50
#define CVP 2
#define PCD 100
#define RAUX AUX_SC1
#define RVAL 1
#define RLEAD 60
#define SLP_D 0
#define RFA 1
#define TP 1
#define TPS (4 * TP + 4)
#define MCE 1
#define H4S 0
#define GF_H 150
#define CVT2 48
#define GF_P 0
#define GF0 8
#define SLP_H 12
#define HX0 6
#define CVSTEP 2
#define CVLATE 64
#define PACE 204
#define PACE0 12
#define RPRIO 3
#define DL_R2 1400
#define PNL 8
#define GSL 12
#define AWDIR 1
#define AWAT 0
#define AWRP 0
#define PEPS 4
#define FGATE 0
#define DL_C 0
#define DL_A 300
#define DL_R 0
#define DL_Q 0
#define KC0 0
#define CPRIO 1
#define P1PRIO 3
#define QLEAD 2
#define POLL_SLEEP 12
#define SLP_C POLL_SLEEP
#define SLP_A POLL_SLEEP
#define SLP_W POLL_SLEEP
#define SLP_R POLL_SLEEP
#define SLP_V POLL_SLEEP
#define SLP_Q POLL_SLEEP
#define SLP_M 4
#define AWPRIO 2
#define SLP_S 8
#define SPOS 4
#define CVPRE 12
#define QPD 3
#define QPRI 2
#define TLBW 3
#define QHV 0
#define QCHK 0
#define LEADN 99
#define GATE_V 170
#define GATE_R 380
#define GATE_N 380
#define AWAHEAD 1
#define GATE_M 150
#define PRI_A 3
#define PRI_WA 2
#define PRI_WB 1
#define AWSKIP 3
#define PRI_0 3
#define AWDEF 0
#define SLP_F 2
#define EXP_OFF 0
#define AWSPEC 1
#define CVTHR 48
#define NR_C 8
#define NR_R 8
#define NR_W 8
#define NR_N 2
#define QAHEAD 0
#define NS0 4
#define SLP_N POLL_SLEEP
template <int SLP, int WPL>
__device__ __forceinline__ bool wait_n(const unsigned* f, int n, unsigned* tmo) {
  const int l = lane_id();
  for (unsigned spins = 0;; ++spins) {
    unsigned v = 1u;
    if (l < n) v = __hip_atomic_load((gu32*)f + (l / WPL) * 32 + l % WPL, RLX_AGENT);
    if (__all(v != 0u)) break;
    if (spin_timeout(spins, tmo)) return false;
    __builtin_amdgcn_s_sleep(SLP);
  }
  asm volatile("" ::: "memory");
  return true;
}
__device__ __forceinline__ unsigned peek(const unsigned* base, const unsigned off) {
  unsigned r;
  asm volatile("global_atomic_or %0, %1, %2, %3 sc0\n\ts_waitcnt vmcnt(0)" : "=&v"(r) : "v"(off), "v"(0u), "s"(base) : "memory");
  return r;
}
__device__ __forceinline__ unsigned now10() { return (unsigned)__builtin_amdgcn_s_memrealtime(); }
__device__ __forceinline__ void gate(const unsigned t) {
#pragma unroll 1
  for (int i = 0; i < 64; ++i) {
    const int d = (int)(t - now10());
    if (d <= 0) break;
    if (d > GT1) __builtin_amdgcn_s_sleep(32);
    else if (d > GT2) __builtin_amdgcn_s_sleep(8);
    else __builtin_amdgcn_s_sleep(GT3);
  }
}
__device__ __forceinline__ unsigned latest(const unsigned mk, const unsigned t) {
  int d = (int)(mk - t);
  d = max(d, __builtin_amdgcn_update_dpp(0, d, 0x121, 0xf, 0xf, false));
  d = max(d, __builtin_amdgcn_update_dpp(0, d, 0x122, 0xf, 0xf, false));
  d = max(d, __builtin_amdgcn_update_dpp(0, d, 0x124, 0xf, 0xf, false));
  d = max(d, __builtin_amdgcn_update_dpp(0, d, 0x128, 0xf, 0xf, false));
  return t + __builtin_amdgcn_readfirstlane(d);
}
template <int SLP, int WPL, int GT>
__device__ __forceinline__ void grp_wait(const bool leader, volatile unsigned* sw, const unsigned val, const unsigned* f, int n,
                                         unsigned* tmo, unsigned& tl) {
  if (leader) {
    const int l = lane_id();
    for (unsigned spins = 0;; ++spins) {
      unsigned v = 1u;
      if (l < n) v = __hip_atomic_load((gu32*)f + (l / WPL) * 32 + l % WPL, RLX_AGENT);
      if (__all(v != 0u) || spin_timeout(spins, tmo)) break;
      if (GT && spins == 0) gate(tl);
      else __builtin_amdgcn_s_sleep(SLP);
    }
    if (GT) tl = now10() + GT;
    if (sw && l == 0) *sw = val;
  } else {
    for (unsigned spins = 0; *sw < val; ++spins) {
      if (spins > 64u * SPIN_CAP) break;
      if (FGATE && GT && spins == 0) gate(tl);
      __builtin_amdgcn_s_sleep(SLP_F);
    }
    if (FGATE && GT) tl = now10() + GT;
  }
  asm volatile("" ::: "memory");
}
template <int SLP, int WPL>
__device__ __forceinline__ bool wg_wait(const unsigned* f, int n, unsigned* tmo) {
  if (threadIdx.x < 64) (void)wait_n<SLP, WPL>(f, n, tmo);
  __syncthreads();
  asm volatile("" ::: "memory");
  return true;
}
__device__ __forceinline__ void set_flag(unsigned* f) { __hip_atomic_store((gu32*)f, 1u, RLX_AGENT); }
#define DRAIN() asm volatile("s_waitcnt vmcnt(0)" ::: "memory")
#define MKRS(ptr, bytes) __builtin_amdgcn_make_buffer_rsrc((void*)(ptr), 0, (int)(bytes), 0x00020000)
typedef unsigned v2u __attribute__((ext_vector_type(2)));
#define LD128(rs, voff, soff) __builtin_amdgcn_raw_buffer_load_b128(rs, voff, soff, AUX_SC1)
#define LD64(rs, voff, soff) __builtin_amdgcn_raw_buffer_load_b64(rs, voff, soff, AUX_SC1)
#define ST128(v, rs, voff, soff) ST128A(v, rs, voff, soff, AUX_SC1)
#define ST128A(v, rs, voff, soff, aux)                                        \
  do {                                                                        \
    const v4u st_d_ = (v);                                                    \
    __builtin_amdgcn_raw_buffer_store_b128(st_d_, rs, voff, soff, aux);       \
    asm volatile("s_nop 1" ::"v"(st_d_) : "memory");                          \
  } while (0)
#define ST64(v, rs, voff, soff) __builtin_amdgcn_raw_buffer_store_b64(v, rs, voff, soff, AUX_SC1)
#define BF8(x) __builtin_bit_cast(bf16x8, x)
#define MFMA16(a, b, c) __builtin_amdgcn_mfma_f32_16x16x32_bf16(a, b, c, 0, 0, 0)
typedef short bf16x4 __attribute__((ext_vector_type(4)));
#define MFMA16K(a, b, c) \
  __builtin_amdgcn_mfma_f32_16x16x16bf16_1k(__builtin_bit_cast(bf16x4, a), __builtin_bit_cast(bf16x4, b), c, 0, 0, 0)
__device__ __forceinline__ float fmax_nn(float a, float b) { return __builtin_elementwise_maximum(a, b); }
typedef __bf16 bfv8 __attribute__((ext_vector_type(8)));
typedef __bf16 bfv16 __attribute__((ext_vector_type(16)));
typedef unsigned v8u __attribute__((ext_vector_type(8)));
#define SMF(a, b, c, idx) \
  __builtin_amdgcn_smfmac_f32_16x16x64_bf16(__builtin_bit_cast(bfv8, a), __builtin_bit_cast(bfv16, b), c, idx, 0, 0)
__device__ __forceinline__ float swap_add32(float a, float b) {
  auto r = __builtin_amdgcn_permlane32_swap(__float_as_uint(a), __float_as_uint(b), false, false);
  return __uint_as_float(r[0]) + __uint_as_float(r[1]);
}
__device__ __forceinline__ float swap_add16(float a, float b) {
  auto r = __builtin_amdgcn_permlane16_swap(__float_as_uint(a), __float_as_uint(b), false, false);
  return __uint_as_float(r[0]) + __uint_as_float(r[1]);
}
__device__ __forceinline__ v4u smf_fix(const v4u d) {
  auto x = __builtin_amdgcn_permlane16_swap(d.x, d.z, false, false);
  auto y = __builtin_amdgcn_permlane16_swap(d.y, d.w, false, false);
  return v4u{x[0], x[1], y[0], y[1]};
}
__device__ __forceinline__ v8u smf_bfrag(const u16* p) {
  const v2u a = *reinterpret_cast<const v2u*>(p), b = *reinterpret_cast<const v2u*>(p + 8);
  const v2u c = *reinterpret_cast<const v2u*>(p + 32), d = *reinterpret_cast<const v2u*>(p + 40);
  return v8u{a.x, b.x, a.y, b.y, c.x, d.x, c.y, d.y};
}

#define VAR_PPA 0
#define VAR_PPB 0
#define VAR_THR 0
#define VAR_THR2 0
#define CV_LEAD 100
#define VAR_EARLY 1
#define VAR_DRAIN 1
#define VAR_D1 0
#define VAR_D2 0
#define VAR_DR2 1
#define VAR_S1 0
#define VAR_S2 0
typedef __attribute__((address_space(1))) unsigned long long gu64;
typedef __attribute__((address_space(1))) char gch;
__device__ __forceinline__ int cvt_rpi(float x) {
  int r;
  asm("v_cvt_rpi_i32_f32 %0, %1" : "=v"(r) : "v"(x));
  return r;
}
typedef unsigned long long u64;
__device__ __forceinline__ u64 call_tag(const MegaArgs& A) {
  u64 t = *(const __attribute__((address_space(4))) u64*)(A.ws + OFF_EP) * 6364136223846793005ull + 1442695040888963407ull;
  asm volatile("" : "+s"(t)::"memory");
  return t;
}
__device__ __forceinline__ u64 tagged(const u64 tag, const unsigned v) { return (tag & 0xffffffffffffull) | (u64)(v >> 16) << 48; }
__device__ __forceinline__ bool has_tag(const u64 tag, const unsigned lo, const unsigned hi) {
  return lo == (unsigned)tag && (hi & 0xffffu) == ((unsigned)(tag >> 32) & 0xffffu);
}
__device__ __forceinline__ u64 ready_look(const MegaArgs& A) {
  return __hip_atomic_load((gu64*)(A.ws + OFF_RD) + (blockIdx.x & 7) * 64 + lane_id(), RLX_AGENT);
}
#define RD_ASK(v) \
  asm volatile("global_load_dwordx2 %0, %1, %2 sc1" : "=v"(v) : "v"(lane_id() * 8), "s"(A.ws + OFF_RD + (blockIdx.x & 7) * 512) : "memory")
__device__ __forceinline__ void ready_wait(const MegaArgs& A, const u64 tag, u64 v) {
  for (unsigned spins = 0; !__all(v == tag) && spins < SPIN_CAP; ++spins) {
    __builtin_amdgcn_s_sleep(8);
    v = ready_look(A);
  }
  asm volatile("" ::: "memory");
}
typedef short v4s __attribute__((ext_vector_type(4)));
typedef __attribute__((address_space(3))) v4s* lp4s;
__device__ __forceinline__ void glds16(const void* sbase, const unsigned voff, const unsigned lds, const bool nt = false) {
  unsigned keep;
#define GLDS16(M)                                                                                                                     \
  asm volatile("s_nop 4\n\ts_mov_b32 %0, m0\n\ts_mov_b32 m0, %3\n\ts_nop 0\n\tglobal_load_lds_dwordx4 %1, %2" M "\n\ts_mov_b32 m0, %0" \
               : "=&s"(keep)                                                                                                          \
               : "v"(voff), "s"(sbase), "s"(lds)                                                                                      \
               : "memory")
  if (nt) GLDS16(" nt");
  else GLDS16("");
#undef GLDS16
}
__device__ __forceinline__ void glds16c(const void* sbase, const unsigned voff, const unsigned lds) {
  unsigned keep;
  asm volatile("s_nop 4\n\ts_mov_b32 %0, m0\n\ts_mov_b32 m0, %3\n\ts_nop 0\n\tglobal_load_lds_dwordx4 %1, %2 sc1\n\ts_mov_b32 m0, %0"
               : "=&s"(keep)
               : "v"(voff), "s"(sbase), "s"(lds)
               : "memory");
}
__device__ __forceinline__ void glds64c(const void* sbase, const unsigned voff, const unsigned lds) {
  unsigned keep;
  asm volatile("s_nop 4\n\ts_mov_b32 %0, m0\n\ts_mov_b32 m0, %3\n\ts_nop 0\n\tglobal_load_lds_dwordx4 %1, %2 sc1\n\t"
               "global_load_lds_dwordx4 %1, %2 offset:1024 sc1\n\tglobal_load_lds_dwordx4 %1, %2 offset:2048 sc1\n\t"
               "global_load_lds_dwordx4 %1, %2 offset:3072 sc1\n\ts_mov_b32 m0, %0"
               : "=&s"(keep)
               : "v"(voff), "s"(sbase), "s"(lds)
               : "memory");
}
__device__ __forceinline__ v2u trd(const unsigned a) {
  return __builtin_bit_cast(v2u, __builtin_amdgcn_ds_read_tr16_b64_v4i16((lp4s)(unsigned long)a));
}
__device__ __forceinline__ unsigned lds_base(char* smem) { return (unsigned)(unsigned long)(__attribute__((address_space(3))) char*)smem; }
#define RAW_BAR()                                           \
  do {                                                      \
    asm volatile("s_waitcnt lgkmcnt(0)" ::: "memory");      \
    __builtin_amdgcn_s_barrier();                           \
    asm volatile("" ::: "memory");                          \
  } while (0)
#define WAIT_VM(n) asm volatile("s_waitcnt vmcnt(" #n ")" ::: "memory")
template <bool SP>
__device__ __forceinline__ void wqkv_issue(const u16* Wm, const int hf, const int h, const int w, const int l, const unsigned sb, const bool nt) {
  const char* base = (const char*)Wm + (size_t)(256 * hf + 32 * w) * (HD * 2) + h * (DH * 2);
#define WQKV(NT_)                                                                                                       \
  _Pragma("unroll") for (int i = 0; i < 8; ++i) {                                                                      \
    const int r = l >> 4;                                                                                              \
    const int f = SP ? ((r & 1) | ((i & 1) << 1) | (((i >> 1) & 1) << 2)) : (r | (((i >> 1) & 1) << 2));               \
    glds16(base, (unsigned)((4 * i + r) * (HD * 2) + (((l & 15) ^ (f << 1)) << 4)),                                    \
           sb + (w >> 2) * 65536 + hf * 32768 + (w & 3) * 8192 + i * 1024, NT_);                                       \
  }
  if (nt) { WQKV(true) } else { WQKV(false) }
#undef WQKV
}

__device__ __forceinline__ unsigned row_stats(const v4u raw) {
  float v[8];
#pragma unroll
  for (int e = 0; e < 4; ++e) { v[2 * e] = lo16(raw[e]); v[2 * e + 1] = hi16(raw[e]); }
  float s = 0.f;
#pragma unroll
  for (int e = 0; e < 8; ++e) s += v[e];
  const float mean = wave_sum(s) * (1.0f / DM);
  float q = 0.f;
#pragma unroll
  for (int e = 0; e < 8; ++e) { const float t = v[e] - mean; q += t * t; }
  const float var = wave_sum(q) * (1.0f / DM);
  const float sd = bfr(sqrtf(bfr(bfr(var) + 1e-5f)));
  return (unsigned)f2bf(mean) | ((unsigned)f2bf(sd) << 16);
}
__device__ __forceinline__ void ask_slow(const MegaArgs& A, const int k, unsigned (&t)[3]) {
  const int l = lane_id(), x = blockIdx.x & 7;
  const char *wq = (const char*)A.Wq, *wv = (const char*)A.Wv;
  asm("" : "+s"(wq), "+s"(wv));
#pragma unroll
  for (int m = 0; m < 3; ++m) {
    const char* W = m == 1 ? (const char*)A.Wk : ((m == 0) == (x < 4)) ? wv : wq;
    const int a = (int)((unsigned long)W >> 8) + 8 * (x & 3), h = k < 32 ? ((1 - a) & 3) + 4 * (k & 1) : (31 - a) & 31;
    const int r = k < 32 ? k >> 1 : k - 32;
    t[m] = __builtin_amdgcn_raw_buffer_load_b32(MKRS(W, (unsigned)k < 48u && h < 8 ? DM * HD * 2 : 0), (l >> 1) * (HD * 2) + (l & 1) * 128,
                                                (32 * (r & 15)) * (HD * 2) + (8 * (x & 3) + (h & 7)) * 256, 0);
  }
}
#define ASKED(t) asm volatile("" ::"v"(t[0]), "v"(t[1]), "v"(t[2]))
template <int N>
__device__ __forceinline__ u64 start_stats(const MegaArgs& A, const int idx) {
  const int w = A.wv, l = lane_id();
  const int r0 = N * (8 * idx + w);
  unsigned ta[3], tb[3];
  v4u raw[N];
#pragma unroll
  for (int i = 0; i < N; ++i) raw[i] = *reinterpret_cast<const v4u*>(A.x0 + (size_t)(r0 + i) * DM + l * 8);
  asm volatile("" ::: "memory");
  ask_slow(A, 16 * w + (idx & 3) + (idx >> 4) * N, ta);
  if (N > 4) ask_slow(A, 16 * w + (idx & 3) + 12, tb);
  unsigned tc[2] = {};
  if (N == 4 && AWH > 0) tc[0] = *reinterpret_cast<const unsigned*>(A.Wo + (size_t)idx * 65536 + 32768 + (64 * w + l) * 64);
  if (N == 4 && AWH > 1 && w >= 4) tc[1] = *reinterpret_cast<const unsigned*>(A.Wo + (size_t)idx * 65536 + (64 * w + l) * 64);
  const u64 tag = call_tag(A);
  unsigned stw = 0u;
#pragma unroll
  for (int i = 0; i < N; ++i) {
    const unsigned t = row_stats(raw[i]);
    if ((l >> 1) == i) stw = t;
  }
  if (l < 2 * N) __hip_atomic_store((gu64*)(A.ws + OFF_GT) + 2 * r0 + l, tagged(tag, (l & 1) ? stw : stw << 16), RLX_AGENT);
  SST(0);
  ASKED(ta);
  if (N > 4) ASKED(tb);
  asm volatile("" ::"v"(tc[0]), "v"(tc[1]));
  return tag;
}
#define MFMAH(a, b, c) __builtin_amdgcn_mfma_f32_16x16x32_f16(a, b, c, 0, 0, 0)
struct Cx {
  f32x2_t r, i;
};
__device__ __forceinline__ Cx cmul(const Cx a, const Cx b) { return Cx{a.r * b.r - a.i * b.i, a.r * b.i + a.i * b.r}; }
struct KGen {
  unsigned pa, pb, pc, pd;
  u16 pe;
  f32x2_t ar, ai;
  Cx C;
  char* st;
  v4u bh[8], bl[8];
  __device__ __forceinline__ void load(const MegaArgs& A, const int d, const int l) {
    pa = *reinterpret_cast<const unsigned*>(A.logAre + d * NST + 2 * l);
    pb = *reinterpret_cast<const unsigned*>(A.Aim + d * NST + 2 * l);
    pc = *reinterpret_cast<const unsigned*>(A.Cre + d * NST + 2 * l);
    pd = *reinterpret_cast<const unsigned*>(A.Cim + d * NST + 2 * l);
    pe = A.logdt[d];
  }
  __device__ __forceinline__ void setup() {
    const float dt = bfr(expf(bf2f(pe)));
    ar = f32x2_t{-bfr(expf(lo16(pa))) * dt, -bfr(expf(hi16(pa))) * dt};
    ai = f32x2_t{lo16(pb) * dt, hi16(pb) * dt};
    C = Cx{f32x2_t{lo16(pc) * dt, hi16(pc) * dt}, f32x2_t{lo16(pd) * dt, hi16(pd) * dt}};
  }
  __device__ __forceinline__ float scale() {
    const float b = 2.0f * wave_sum((fabsf(C.r.x) + fabsf(C.r.y)) + (fabsf(C.i.x) + fabsf(C.i.y)));
    const unsigned eb = max(__float_as_uint(b) >> 23, 25u);
    const float ks = __uint_as_float((268u - eb) << 23);
    C.r *= ks;
    C.i *= ks;
    return __uint_as_float((eb - 24u) << 23);
  }
  __device__ __forceinline__ Cx power(const float p) const {
    Cx z;
#pragma unroll
    for (int e = 0; e < 2; ++e) {
      const float x = ai[e] * p;
      const float f = __builtin_fmaf(x, 6.4206382e-9f, __builtin_fmaf(x, 0.15915494f, -__builtin_rintf(x * 0.15915494f)));
      const float m = __expf(ar[e] * p);
      z.r[e] = m * __builtin_amdgcn_cosf(f);
      z.i[e] = m * __builtin_amdgcn_sinf(f);
    }
    return z;
  }
  static __device__ __forceinline__ unsigned rest(const f32x2_t y, const unsigned h) {
    unsigned r;
    asm("v_fma_mixlo_f16 %0, %1, -1.0, %2 op_sel_hi:[1,0,0]" : "=v"(r) : "v"(h), "v"(y.x));
    asm("v_fma_mixhi_f16 %0, %1, -1.0, %2 op_sel:[1,0,0] op_sel_hi:[1,0,0]" : "+v"(r) : "v"(h), "v"(y.y));
    return r;
  }
  __device__ __forceinline__ char* rows(char* p, Cx& y, const Cx s, const int n) const {
#pragma unroll 1
    for (int i = 0; i < n; i += 2) {
#pragma unroll
      for (int k = 0; k < 2; ++k) {
        const unsigned h0 = pkh(y.r.x, y.r.y), h1 = pkh(y.i.x, y.i.y);
        *reinterpret_cast<v2u*>(p + 1040 * k) = v2u{h0, h1};
        *reinterpret_cast<v2u*>(p + 1040 * k + 512) = v2u{rest(y.r, h0), rest(y.i, h1)};
        y = cmul(y, s);
      }
      p += 2080;
    }
    return p;
  }
  __device__ __forceinline__ void get(v4u (&h)[8], v4u (&o)[8], const int l) const {
    const char* p = st + (l & 15) * 1040 + (l >> 4) * 16;
    asm volatile("" ::: "memory");
#pragma unroll
    for (int k = 0; k < 8; ++k) {
      h[k] = *reinterpret_cast<const v4u*>(p + 64 * k);
      o[k] = *reinterpret_cast<const v4u*>(p + 512 + 64 * k);
    }
    asm volatile("" ::: "memory");
  }
  Cx cy, cs;
  __device__ __forceinline__ void columns(const int l, const int j0, const int j1) {
    if (j0 == 0) {
      cs = power(16.f);
      cs.i = -cs.i;
      cy = Cx{f32x2_t{16384.f, 16384.f}, f32x2_t{0.f, 0.f}};
    }
    rows(st + l * 8 + 1040 * j0, cy, cs, j1 - j0);
  }
  template <bool COL>
  __device__ __forceinline__ f32x4 tile(const int l) const {
    v4u h[8], o[8];
    get(h, o, l);
    f32x4 d = {}, e = {}, f = {};
#define H8(x) __builtin_bit_cast(f16x8, x)
#define KMM(a, b, d) (COL ? MFMAH(H8(b), H8(a), d) : MFMAH(H8(a), H8(b), d))
#pragma unroll
    for (int k = 0; k < 8; ++k) {
      d = KMM(h[k], bh[k], d);
      e = KMM(o[k], bh[k], e);
      f = KMM(h[k], bl[k], f);
    }
#undef KMM
#undef H8
    return (d + (e + f)) * (1.0f / 8192);
  }
};
#define ACC_INIT 0xc9b26c86c9b26ca0ull
#define CHAIN_ASM_ATT \
  "s_mov_b32 s95, 272\n" \
  "s_mov_b32 s91, 0x4c7c2000\n" \
  "s_mov_b32 s85, 0xc9b26c9b\n" \
  "s_mov_b32 s92, 0xc9326c9b\n" \
  "s_mov_b32 s93, 0xc9326c9b\n" \
  "s_mov_b32 s78, 0x3e0293ee\n" \
  "s_mov_b32 s76, 0x33333333\n" \
  "s_mov_b32 s77, 0x33333333\n" \
  "s_lshl_b32 s98, s73, 10\n" \
  "s_add_u32 s98, s98, s74\n" \
  "s_add_u32 s98, s98, 131072\n" \
  "s_lshl_b32 s97, s73, 9\n" \
  "s_add_u32 s97, s97, s74\n" \
  "s_add_u32 s97, s97, 158720\n" \
  "s_lshl_b32 s99, s73, 10\n" \
  "v_mbcnt_lo_u32_b32 v48, -1, 0\n" \
  "v_mbcnt_hi_u32_b32 v48, -1, v48\n" \
  "s_add_u32 s88, s74, 157760\n" \
  "v_mov_b32 v59, s88\n" \
  "v_lshrrev_b32 v49, 2, v48\n" \
  "v_lshrrev_b32 v50, 4, v48\n" \
  "v_bitop3_b32 v50, v49, v50, 3 bitop3:0x28\n" \
  "v_lshl_add_u32 v8, v50, 3, s97\n" \
  "v_and_b32 v50, 3, v48\n" \
  "v_lshl_add_u32 v8, v50, 5, v8\n" \
  "v_and_b32 v49, 1, v49\n" \
  "v_lshl_add_u32 v8, v49, 8, v8\n" \
  "v_and_b32 v49, 8, v48\n" \
  "v_lshl_add_u32 v8, v49, 4, v8\n" \
  "v_lshrrev_b32 v49, 5, v48\n" \
  "v_add_u32 v10, s99, v1\n" \
  "s_lshr_b32 s88, s99, 2\n" \
  "s_add_u32 s88, s88, s74\n" \
  "s_add_u32 s88, s88, 139264\n" \
  "v_lshl_add_u32 v7, v49, 10, s88\n" \
  "v_and_b32 v49, 31, v48\n" \
  "v_lshl_add_u32 v7, v49, 3, v7\n" \
  "v_mov_b64 v[12:13], 0\n" \
  "v_mov_b64 v[14:15], 0\n" \
  "v_mov_b64 v[16:17], 0\n" \
  "v_mov_b64 v[18:19], 0\n" \
  "v_mov_b64 v[20:21], 0\n" \
  "v_mov_b64 v[22:23], 0\n" \
  "v_mov_b32 v34, 0xc9b26ca0\n" \
  "v_mov_b32 v35, 0xc9b26c86\n" \
  "s_mov_b32 s87, 0x40000\n" \
  "v_subrev_u32 v30, 64, v59\n" \
  "v_add_u32 v17, 0x10000, v28\n" \
  "v_xor_b32 v18, 128, v28\n" \
  "v_xor_b32 v19, 128, v17\n" \
  "ds_write_b32 v30, v12 offset:0\n" \
  "ds_write_b32 v30, v12 offset:32\n" \
  "ds_write_b32 v30, v12 offset:64\n" \
  "v_xor_b32 v20, 0, v29\n" \
  "v_xor_b32 v21, 32, v29\n" \
  "v_xor_b32 v22, 64, v29\n" \
  "v_xor_b32 v23, 96, v29\n" \
  "s_waitcnt vmcnt(12) lgkmcnt(0)\n" \
  "s_barrier\n" \
  "ds_read_b64_tr_b16 v[160:161], v20 offset:0\n" \
  "ds_read_b64_tr_b16 v[162:163], v20 offset:2048\n" \
  "ds_read_b64_tr_b16 v[168:169], v21 offset:0\n" \
  "ds_read_b64_tr_b16 v[170:171], v21 offset:2048\n" \
  "ds_read_b64_tr_b16 v[176:177], v22 offset:0\n" \
  "ds_read_b64_tr_b16 v[178:179], v22 offset:2048\n" \
  "ds_read_b64_tr_b16 v[184:185], v23 offset:0\n" \
  "ds_read_b64_tr_b16 v[186:187], v23 offset:2048\n" \
  "s_lshl_b32 s88, s73, 10\n" \
  "v_add_u32 v48, s88, v1\n" \
  "ds_read_b128 v[52:55], v48 offset:0\n" \
  "s_waitcnt lgkmcnt(0)\n" \
  "v_mov_b32 v11, 0\n" \
  "v_dot2c_f32_bf16 v11, v52, v52\n" \
  "v_dot2c_f32_bf16 v11, v53, v53\n" \
  "v_dot2c_f32_bf16 v11, v54, v54\n" \
  "v_dot2c_f32_bf16 v11, v55, v55\n" \
  "s_nop 0\n" \
  "s_nop 1\n" \
  "v_max_u32_dpp v11, v11, v11 row_ror:1 row_mask:0xf bank_mask:0xf\n" \
  "s_nop 1\n" \
  "v_max_u32_dpp v11, v11, v11 row_ror:2 row_mask:0xf bank_mask:0xf\n" \
  "s_nop 1\n" \
  "v_max_u32_dpp v11, v11, v11 row_ror:4 row_mask:0xf bank_mask:0xf\n" \
  "s_nop 1\n" \
  "v_max_u32_dpp v11, v11, v11 row_ror:8 row_mask:0xf bank_mask:0xf\n" \
  "v_mov_b32 v31, v11\n" \
  "s_nop 1\n" \
  "v_permlane16_swap_b32 v11, v31\n" \
  "v_max_u32 v11, v11, v31\n" \
  "v_mov_b32 v31, v11\n" \
  "s_nop 1\n" \
  "v_permlane32_swap_b32 v11, v31\n" \
  "v_max_u32 v11, v11, v31\n" \
  "s_mov_b64 exec, 1\n" \
  "ds_max_u32 v59, v11\n" \
  "s_mov_b64 exec, -1\n" \
  "s_waitcnt vmcnt(8) lgkmcnt(0)\n" \
  "s_barrier\n" \
  "ds_read_b64_tr_b16 v[164:165], v20 offset:4096\n" \
  "ds_read_b64_tr_b16 v[166:167], v20 offset:6144\n" \
  "ds_read_b64_tr_b16 v[172:173], v21 offset:4096\n" \
  "ds_read_b64_tr_b16 v[174:175], v21 offset:6144\n" \
  "ds_read_b64_tr_b16 v[180:181], v22 offset:4096\n" \
  "ds_read_b64_tr_b16 v[182:183], v22 offset:6144\n" \
  "ds_read_b64_tr_b16 v[188:189], v23 offset:4096\n" \
  "ds_read_b64_tr_b16 v[190:191], v23 offset:6144\n" \
  "s_add_u32 m0, s19, 0\n" \
  "s_nop 0\n" \
  "global_load_lds_dwordx4 v24, s[4:5] offset:0\n" \
  "global_load_lds_dwordx4 v25, s[4:5] offset:1024\n" \
  "global_load_lds_dwordx4 v26, s[4:5] offset:2048\n" \
  "global_load_lds_dwordx4 v27, s[4:5] offset:3072\n" \
  "s_add_u32 s4, s4, 0x100000\n" \
  "s_addc_u32 s5, s5, 0\n" \
  "v_mfma_f32_16x16x32_bf16 v[64:67], v[160:163], v[160:163], 0\n" \
  "v_mfma_f32_16x16x32_bf16 v[68:71], v[168:171], v[168:171], 0\n" \
  "v_mfma_f32_16x16x32_bf16 v[72:75], v[176:179], v[176:179], 0\n" \
  "v_mfma_f32_16x16x32_bf16 v[76:79], v[184:187], v[184:187], 0\n" \
  "s_waitcnt vmcnt(8) lgkmcnt(0)\n" \
  "s_barrier\n" \
  "ds_read_b64_tr_b16 v[32:33], v20 offset:8192\n" \
  "ds_read_b64_tr_b16 v[34:35], v20 offset:10240\n" \
  "ds_read_b64_tr_b16 v[40:41], v21 offset:8192\n" \
  "ds_read_b64_tr_b16 v[42:43], v21 offset:10240\n" \
  "ds_read_b64_tr_b16 v[48:49], v22 offset:8192\n" \
  "ds_read_b64_tr_b16 v[50:51], v22 offset:10240\n" \
  "ds_read_b64_tr_b16 v[56:57], v23 offset:8192\n" \
  "ds_read_b64_tr_b16 v[58:59], v23 offset:10240\n" \
  "s_add_u32 m0, s19, 4096\n" \
  "s_nop 0\n" \
  "global_load_lds_dwordx4 v24, s[4:5] offset:0\n" \
  "global_load_lds_dwordx4 v25, s[4:5] offset:1024\n" \
  "global_load_lds_dwordx4 v26, s[4:5] offset:2048\n" \
  "global_load_lds_dwordx4 v27, s[4:5] offset:3072\n" \
  "s_add_u32 s4, s4, 0x100000\n" \
  "s_addc_u32 s5, s5, 0\n" \
  "v_mfma_f32_16x16x32_bf16 v[64:67], v[164:167], v[164:167], v[64:67]\n" \
  "v_mfma_f32_16x16x32_bf16 v[68:71], v[172:175], v[172:175], v[68:71]\n" \
  "v_mfma_f32_16x16x32_bf16 v[72:75], v[180:183], v[180:183], v[72:75]\n" \
  "v_mfma_f32_16x16x32_bf16 v[76:79], v[188:191], v[188:191], v[76:79]\n" \
  "s_waitcnt vmcnt(8) lgkmcnt(0)\n" \
  "s_barrier\n" \
  "ds_read_b64_tr_b16 v[36:37], v20 offset:12288\n" \
  "ds_read_b64_tr_b16 v[38:39], v20 offset:14336\n" \
  "ds_read_b64_tr_b16 v[44:45], v21 offset:12288\n" \
  "ds_read_b64_tr_b16 v[46:47], v21 offset:14336\n" \
  "ds_read_b64_tr_b16 v[52:53], v22 offset:12288\n" \
  "ds_read_b64_tr_b16 v[54:55], v22 offset:14336\n" \
  "ds_read_b64_tr_b16 v[60:61], v23 offset:12288\n" \
  "ds_read_b64_tr_b16 v[62:63], v23 offset:14336\n" \
  "s_add_u32 m0, s19, 8192\n" \
  "s_nop 0\n" \
  "global_load_lds_dwordx4 v24, s[4:5] offset:0\n" \
  "global_load_lds_dwordx4 v25, s[4:5] offset:1024\n" \
  "global_load_lds_dwordx4 v26, s[4:5] offset:2048\n" \
  "global_load_lds_dwordx4 v27, s[4:5] offset:3072\n" \
  "s_add_u32 s4, s4, 0x100000\n" \
  "s_addc_u32 s5, s5, 0\n" \
  "v_mfma_f32_16x16x32_bf16 v[64:67], v[32:35], v[32:35], v[64:67]\n" \
  "v_mfma_f32_16x16x32_bf16 v[68:71], v[40:43], v[40:43], v[68:71]\n" \
  "v_mfma_f32_16x16x32_bf16 v[72:75], v[48:51], v[48:51], v[72:75]\n" \
  "v_mfma_f32_16x16x32_bf16 v[76:79], v[56:59], v[56:59], v[76:79]\n" \
  "s_waitcnt vmcnt(8) lgkmcnt(0)\n" \
  "s_barrier\n" \
  "s_add_u32 m0, s19, 12288\n" \
  "s_nop 0\n" \
  "global_load_lds_dwordx4 v24, s[4:5] offset:0\n" \
  "global_load_lds_dwordx4 v25, s[4:5] offset:1024\n" \
  "global_load_lds_dwordx4 v26, s[4:5] offset:2048\n" \
  "global_load_lds_dwordx4 v27, s[4:5] offset:3072\n" \
  "s_add_u32 s4, s4, 0x100000\n" \
  "s_addc_u32 s5, s5, 0\n" \
  "v_mfma_f32_16x16x32_bf16 v[64:67], v[36:39], v[36:39], v[64:67]\n" \
  "v_mfma_f32_16x16x32_bf16 v[68:71], v[44:47], v[44:47], v[68:71]\n" \
  "v_mfma_f32_16x16x32_bf16 v[72:75], v[52:55], v[52:55], v[72:75]\n" \
  "v_mfma_f32_16x16x32_bf16 v[76:79], v[60:63], v[60:63], v[76:79]\n" \
  "s_nop 7\n" \
  "v_max3_f32 v13, v64, v65, v66\n" \
  "v_max3_f32 v13, v13, v67, v68\n" \
  "v_max3_f32 v13, v13, v69, v70\n" \
  "v_max3_f32 v13, v13, v71, v72\n" \
  "v_max3_f32 v13, v13, v73, v74\n" \
  "v_max3_f32 v13, v13, v75, v76\n" \
  "v_max3_f32 v13, v13, v77, v78\n" \
  "v_max_f32 v13, v13, v79\n" \
  "s_nop 1\n" \
  "v_max_f32_dpp v13, v13, v13 row_ror:1 row_mask:0xf bank_mask:0xf\n" \
  "s_nop 1\n" \
  "v_max_f32_dpp v13, v13, v13 row_ror:2 row_mask:0xf bank_mask:0xf\n" \
  "s_nop 1\n" \
  "v_max_f32_dpp v13, v13, v13 row_ror:4 row_mask:0xf bank_mask:0xf\n" \
  "s_nop 1\n" \
  "v_max_f32_dpp v13, v13, v13 row_ror:8 row_mask:0xf bank_mask:0xf\n" \
  "v_mov_b32 v31, v13\n" \
  "s_nop 1\n" \
  "v_permlane16_swap_b32 v13, v31\n" \
  "v_max_f32 v13, v13, v31\n" \
  "v_mov_b32 v31, v13\n" \
  "s_nop 1\n" \
  "v_permlane32_swap_b32 v13, v31\n" \
  "v_max_f32 v13, v13, v31\n" \
  "s_mov_b64 exec, 1\n" \
  "ds_max_u32 v30, v13 offset:32\n" \
  "s_mov_b64 exec, -1\n" \
  "s_waitcnt vmcnt(8) lgkmcnt(0)\n" \
  "s_barrier\n" \
  "s_add_u32 m0, s19, 0\n" \
  "s_nop 0\n" \
  "global_load_lds_dwordx4 v24, s[6:7] offset:0\n" \
  "global_load_lds_dwordx4 v25, s[6:7] offset:1024\n" \
  "global_load_lds_dwordx4 v26, s[6:7] offset:2048\n" \
  "global_load_lds_dwordx4 v27, s[6:7] offset:3072\n" \
  "s_add_u32 s6, s6, 0x100000\n" \
  "s_addc_u32 s7, s7, 0\n" \
  "s_waitcnt vmcnt(8) lgkmcnt(0)\n" \
  "s_barrier\n" \
  "s_add_u32 m0, s19, 4096\n" \
  "s_nop 0\n" \
  "global_load_lds_dwordx4 v24, s[6:7] offset:0\n" \
  "global_load_lds_dwordx4 v25, s[6:7] offset:1024\n" \
  "global_load_lds_dwordx4 v26, s[6:7] offset:2048\n" \
  "global_load_lds_dwordx4 v27, s[6:7] offset:3072\n" \
  "s_add_u32 s6, s6, 0x100000\n" \
  "s_addc_u32 s7, s7, 0\n" \
  "s_waitcnt vmcnt(8) lgkmcnt(0)\n" \
  "s_barrier\n" \
  "s_add_u32 m0, s19, 8192\n" \
  "s_nop 0\n" \
  "global_load_lds_dwordx4 v24, s[6:7] offset:0\n" \
  "global_load_lds_dwordx4 v25, s[6:7] offset:1024\n" \
  "global_load_lds_dwordx4 v26, s[6:7] offset:2048\n" \
  "global_load_lds_dwordx4 v27, s[6:7] offset:3072\n" \
  "s_add_u32 s6, s6, 0x100000\n" \
  "s_addc_u32 s7, s7, 0\n" \
  "s_waitcnt vmcnt(8) lgkmcnt(0)\n" \
  "s_barrier\n" \
  "ds_read_b64_tr_b16 v[64:65], v28 offset:0\n" \
  "ds_read_b64_tr_b16 v[66:67], v28 offset:512\n" \
  "ds_read_b64_tr_b16 v[68:69], v28 offset:32768\n" \
  "ds_read_b64_tr_b16 v[70:71], v28 offset:33280\n" \
  "ds_read_b64_tr_b16 v[72:73], v17 offset:0\n" \
  "ds_read_b64_tr_b16 v[74:75], v17 offset:512\n" \
  "ds_read_b64_tr_b16 v[76:77], v17 offset:32768\n" \
  "ds_read_b64_tr_b16 v[78:79], v17 offset:33280\n" \
  "ds_read_b64_tr_b16 v[192:193], v18 offset:0\n" \
  "ds_read_b64_tr_b16 v[194:195], v18 offset:512\n" \
  "ds_read_b64_tr_b16 v[196:197], v18 offset:32768\n" \
  "ds_read_b64_tr_b16 v[198:199], v18 offset:33280\n" \
  "ds_read_b64_tr_b16 v[200:201], v19 offset:0\n" \
  "ds_read_b64_tr_b16 v[202:203], v19 offset:512\n" \
  "ds_read_b64_tr_b16 v[204:205], v19 offset:32768\n" \
  "ds_read_b64_tr_b16 v[206:207], v19 offset:33280\n" \
  "s_add_u32 m0, s19, 12288\n" \
  "s_nop 0\n" \
  "global_load_lds_dwordx4 v24, s[6:7] offset:0\n" \
  "global_load_lds_dwordx4 v25, s[6:7] offset:1024\n" \
  "global_load_lds_dwordx4 v26, s[6:7] offset:2048\n" \
  "global_load_lds_dwordx4 v27, s[6:7] offset:3072\n" \
  "s_add_u32 s6, s6, 0x100000\n" \
  "s_addc_u32 s7, s7, 0\n" \
  "s_waitcnt vmcnt(8) lgkmcnt(0)\n" \
  "s_barrier\n" \
  "ds_read_b64_tr_b16 v[80:81], v28 offset:4096\n" \
  "ds_read_b64_tr_b16 v[82:83], v28 offset:4608\n" \
  "ds_read_b64_tr_b16 v[84:85], v28 offset:36864\n" \
  "ds_read_b64_tr_b16 v[86:87], v28 offset:37376\n" \
  "ds_read_b64_tr_b16 v[88:89], v17 offset:4096\n" \
  "ds_read_b64_tr_b16 v[90:91], v17 offset:4608\n" \
  "ds_read_b64_tr_b16 v[92:93], v17 offset:36864\n" \
  "ds_read_b64_tr_b16 v[94:95], v17 offset:37376\n" \
  "ds_read_b64_tr_b16 v[208:209], v18 offset:4096\n" \
  "ds_read_b64_tr_b16 v[210:211], v18 offset:4608\n" \
  "ds_read_b64_tr_b16 v[212:213], v18 offset:36864\n" \
  "ds_read_b64_tr_b16 v[214:215], v18 offset:37376\n" \
  "ds_read_b64_tr_b16 v[216:217], v19 offset:4096\n" \
  "ds_read_b64_tr_b16 v[218:219], v19 offset:4608\n" \
  "ds_read_b64_tr_b16 v[220:221], v19 offset:36864\n" \
  "ds_read_b64_tr_b16 v[222:223], v19 offset:37376\n" \
  "s_add_u32 m0, s19, 0\n" \
  "s_nop 0\n" \
  "global_load_lds_dwordx4 v24, s[8:9] offset:0\n" \
  "global_load_lds_dwordx4 v25, s[8:9] offset:1024\n" \
  "global_load_lds_dwordx4 v26, s[8:9] offset:2048\n" \
  "global_load_lds_dwordx4 v27, s[8:9] offset:3072\n" \
  "s_add_u32 s8, s8, 0x100000\n" \
  "s_addc_u32 s9, s9, 0\n" \
  "s_waitcnt vmcnt(8) lgkmcnt(0)\n" \
  "s_barrier\n" \
  "ds_read_b64_tr_b16 v[96:97], v28 offset:8192\n" \
  "ds_read_b64_tr_b16 v[98:99], v28 offset:8704\n" \
  "ds_read_b64_tr_b16 v[100:101], v28 offset:40960\n" \
  "ds_read_b64_tr_b16 v[102:103], v28 offset:41472\n" \
  "ds_read_b64_tr_b16 v[104:105], v17 offset:8192\n" \
  "ds_read_b64_tr_b16 v[106:107], v17 offset:8704\n" \
  "ds_read_b64_tr_b16 v[108:109], v17 offset:40960\n" \
  "ds_read_b64_tr_b16 v[110:111], v17 offset:41472\n" \
  "ds_read_b64_tr_b16 v[224:225], v18 offset:8192\n" \
  "ds_read_b64_tr_b16 v[226:227], v18 offset:8704\n" \
  "ds_read_b64_tr_b16 v[228:229], v18 offset:40960\n" \
  "ds_read_b64_tr_b16 v[230:231], v18 offset:41472\n" \
  "ds_read_b64_tr_b16 v[232:233], v19 offset:8192\n" \
  "ds_read_b64_tr_b16 v[234:235], v19 offset:8704\n" \
  "ds_read_b64_tr_b16 v[236:237], v19 offset:40960\n" \
  "ds_read_b64_tr_b16 v[238:239], v19 offset:41472\n" \
  "s_add_u32 m0, s19, 4096\n" \
  "s_nop 0\n" \
  "global_load_lds_dwordx4 v24, s[8:9] offset:0\n" \
  "global_load_lds_dwordx4 v25, s[8:9] offset:1024\n" \
  "global_load_lds_dwordx4 v26, s[8:9] offset:2048\n" \
  "global_load_lds_dwordx4 v27, s[8:9] offset:3072\n" \
  "s_add_u32 s8, s8, 0x100000\n" \
  "s_addc_u32 s9, s9, 0\n" \
  "s_waitcnt vmcnt(8) lgkmcnt(0)\n" \
  "s_barrier\n" \
  "ds_read_b64_tr_b16 v[112:113], v28 offset:12288\n" \
  "ds_read_b64_tr_b16 v[114:115], v28 offset:12800\n" \
  "ds_read_b64_tr_b16 v[116:117], v28 offset:45056\n" \
  "ds_read_b64_tr_b16 v[118:119], v28 offset:45568\n" \
  "ds_read_b64_tr_b16 v[120:121], v17 offset:12288\n" \
  "ds_read_b64_tr_b16 v[122:123], v17 offset:12800\n" \
  "ds_read_b64_tr_b16 v[124:125], v17 offset:45056\n" \
  "ds_read_b64_tr_b16 v[126:127], v17 offset:45568\n" \
  "ds_read_b64_tr_b16 v[240:241], v18 offset:12288\n" \
  "ds_read_b64_tr_b16 v[242:243], v18 offset:12800\n" \
  "ds_read_b64_tr_b16 v[244:245], v18 offset:45056\n" \
  "ds_read_b64_tr_b16 v[246:247], v18 offset:45568\n" \
  "ds_read_b64_tr_b16 v[248:249], v19 offset:12288\n" \
  "ds_read_b64_tr_b16 v[250:251], v19 offset:12800\n" \
  "ds_read_b64_tr_b16 v[252:253], v19 offset:45056\n" \
  "ds_read_b64_tr_b16 v[254:255], v19 offset:45568\n" \
  "s_add_u32 m0, s19, 8192\n" \
  "s_nop 0\n" \
  "global_load_lds_dwordx4 v24, s[8:9] offset:0\n" \
  "global_load_lds_dwordx4 v25, s[8:9] offset:1024\n" \
  "global_load_lds_dwordx4 v26, s[8:9] offset:2048\n" \
  "global_load_lds_dwordx4 v27, s[8:9] offset:3072\n" \
  "s_add_u32 s8, s8, 0x100000\n" \
  "s_addc_u32 s9, s9, 0\n" \
  "s_waitcnt vmcnt(8) lgkmcnt(0)\n" \
  "s_barrier\n" \
  "ds_read_b64_tr_b16 v[128:129], v28 offset:0\n" \
  "ds_read_b64_tr_b16 v[130:131], v28 offset:512\n" \
  "ds_read_b64_tr_b16 v[132:133], v28 offset:32768\n" \
  "ds_read_b64_tr_b16 v[134:135], v28 offset:33280\n" \
  "ds_read_b64_tr_b16 v[136:137], v17 offset:0\n" \
  "ds_read_b64_tr_b16 v[138:139], v17 offset:512\n" \
  "ds_read_b64_tr_b16 v[140:141], v17 offset:32768\n" \
  "ds_read_b64_tr_b16 v[142:143], v17 offset:33280\n" \
  "s_add_u32 m0, s19, 12288\n" \
  "s_nop 0\n" \
  "global_load_lds_dwordx4 v24, s[8:9] offset:0\n" \
  "global_load_lds_dwordx4 v25, s[8:9] offset:1024\n" \
  "global_load_lds_dwordx4 v26, s[8:9] offset:2048\n" \
  "global_load_lds_dwordx4 v27, s[8:9] offset:3072\n" \
  "s_add_u32 s8, s8, 0x100000\n" \
  "s_addc_u32 s9, s9, 0\n" \
  "s_waitcnt vmcnt(8) lgkmcnt(0)\n" \
  "s_barrier\n" \
  "ds_write_b128 v6, v[160:163] offset:0\n" \
  "ds_write_b128 v6, v[164:167] offset:1024\n" \
  "ds_write_b128 v6, v[168:171] offset:2048\n" \
  "ds_write_b128 v6, v[172:175] offset:3072\n" \
  "ds_read_b64_tr_b16 v[144:145], v28 offset:4096\n" \
  "ds_read_b64_tr_b16 v[146:147], v28 offset:4608\n" \
  "ds_read_b64_tr_b16 v[148:149], v28 offset:36864\n" \
  "ds_read_b64_tr_b16 v[150:151], v28 offset:37376\n" \
  "ds_read_b64_tr_b16 v[152:153], v17 offset:4096\n" \
  "ds_read_b64_tr_b16 v[154:155], v17 offset:4608\n" \
  "ds_read_b64_tr_b16 v[156:157], v17 offset:36864\n" \
  "ds_read_b64_tr_b16 v[158:159], v17 offset:37376\n" \
  "s_waitcnt vmcnt(4) lgkmcnt(0)\n" \
  "s_barrier\n" \
  "ds_write_b128 v6, v[176:179] offset:4096\n" \
  "ds_write_b128 v6, v[180:183] offset:5120\n" \
  "ds_write_b128 v6, v[184:187] offset:6144\n" \
  "ds_write_b128 v6, v[188:191] offset:7168\n" \
  "ds_read_b64_tr_b16 v[160:161], v28 offset:8192\n" \
  "ds_read_b64_tr_b16 v[162:163], v28 offset:8704\n" \
  "ds_read_b64_tr_b16 v[164:165], v28 offset:40960\n" \
  "ds_read_b64_tr_b16 v[166:167], v28 offset:41472\n" \
  "ds_read_b64_tr_b16 v[168:169], v17 offset:8192\n" \
  "ds_read_b64_tr_b16 v[170:171], v17 offset:8704\n" \
  "ds_read_b64_tr_b16 v[172:173], v17 offset:40960\n" \
  "ds_read_b64_tr_b16 v[174:175], v17 offset:41472\n" \
  "s_waitcnt vmcnt(0) lgkmcnt(0)\n" \
  "s_barrier\n" \
  "ds_write_b128 v6, v[32:35] offset:8192\n" \
  "ds_write_b128 v6, v[36:39] offset:9216\n" \
  "ds_write_b128 v6, v[40:43] offset:10240\n" \
  "ds_write_b128 v6, v[44:47] offset:11264\n" \
  "ds_read_b64_tr_b16 v[176:177], v28 offset:12288\n" \
  "ds_read_b64_tr_b16 v[178:179], v28 offset:12800\n" \
  "ds_read_b64_tr_b16 v[180:181], v28 offset:45056\n" \
  "ds_read_b64_tr_b16 v[182:183], v28 offset:45568\n" \
  "ds_read_b64_tr_b16 v[184:185], v17 offset:12288\n" \
  "ds_read_b64_tr_b16 v[186:187], v17 offset:12800\n" \
  "ds_read_b64_tr_b16 v[188:189], v17 offset:45056\n" \
  "ds_read_b64_tr_b16 v[190:191], v17 offset:45568\n" \
  "ds_read_b32 v31, v30 offset:0\n" \
  "s_waitcnt lgkmcnt(0)\n" \
  "s_barrier\n" \
  "ds_write_b128 v6, v[48:51] offset:12288\n" \
  "ds_write_b128 v6, v[52:55] offset:13312\n" \
  "ds_write_b128 v6, v[56:59] offset:14336\n" \
  "ds_write_b128 v6, v[60:63] offset:15360\n" \
  "v_mul_f32 v31, 0x3f8e353f, v31\n" \
  "v_mul_f32 v30, 0x44870000, v31\n" \
  "v_mov_b64 v[12:13], 0\n" \
  "v_mov_b64 v[14:15], 0\n" \
  "v_mov_b64 v[16:17], 0\n" \
  "v_mov_b64 v[18:19], 0\n" \
  "v_mov_b64 v[20:21], 0\n" \
  "v_mov_b64 v[22:23], 0\n" \
  "v_readfirstlane_b32 s72, v31\n" \
  "v_readfirstlane_b32 s86, v30\n" \
  "s_lshr_b32 s86, s86, 23\n" \
  "s_addk_i32 s86, 0xff83\n" \
  "s_ashr_i32 s86, s86, 1\n" \
  "s_max_i32 s86, s86, -20\n" \
  "s_min_i32 s86, s86, 100\n" \
  "s_mov_b32 s82, s86\n" \
  "s_lshr_b32 s83, s72, 23\n" \
  "s_sub_i32 s83, s83, 243\n" \
  "s_waitcnt lgkmcnt(0)\n" \
  "s_add_u32 s88, s74, 157760\n" \
  "v_mov_b32 v59, s88\n" \
  "v_mov_b32 v34, 0xc9b26ca0\n" \
  "v_mov_b32 v35, 0xc9b26c86\n" \
  ".p2align 6\n" \
  "s_nop 0\n" \
  "s_nop 0\n" \
  "s_nop 0\n" \
  ".LCA_TOP_%=:\n" \
  "s_waitcnt lgkmcnt(0)\n" \
  "s_barrier\n" \
  "ds_read_b128 v[40:43], v1 offset:0\n" \
  "ds_read_b128 v[44:47], v1 offset:1024\n" \
  "ds_read_b128 v[48:51], v1 offset:2048\n" \
  "ds_read_b128 v[52:55], v1 offset:3072\n" \
  "ds_read_b128 v[56:59], v1 offset:4096\n" \
  "ds_read_b128 v[60:63], v1 offset:5120\n" \
  "s_cmp_lg_u32 s73, 0\n" \
  "s_cbranch_scc1 .LCA_NOZ_%=\n" \
  "s_xor_b32 s88, s60, 0x10000\n" \
  "s_add_u32 s88, s88, s61\n" \
  "buffer_store_dwordx2 v[34:35], v5, s[40:43], s88 offen sc1\n" \
  ".LCA_NOZ_%=:\n" \
  "s_waitcnt lgkmcnt(5)\n" \
  "v_smfmac_f32_16x16x64_bf16 v[20:23], v[40:43], v[192:199], v0\n" \
  "v_smfmac_f32_16x16x64_bf16 v[12:15], v[40:43], v[64:71], v0\n" \
  "v_smfmac_f32_16x16x64_bf16 v[16:19], v[40:43], v[128:135], v0\n" \
  "ds_read_b128 v[40:43], v1 offset:6144\n" \
  "s_waitcnt lgkmcnt(5)\n" \
  "v_smfmac_f32_16x16x64_bf16 v[20:23], v[44:47], v[200:207], v0\n" \
  "v_smfmac_f32_16x16x64_bf16 v[12:15], v[44:47], v[72:79], v0\n" \
  "v_smfmac_f32_16x16x64_bf16 v[16:19], v[44:47], v[136:143], v0\n" \
  "ds_read_b128 v[44:47], v1 offset:7168\n" \
  "s_waitcnt lgkmcnt(5)\n" \
  "v_smfmac_f32_16x16x64_bf16 v[20:23], v[48:51], v[208:215], v0\n" \
  "v_smfmac_f32_16x16x64_bf16 v[12:15], v[48:51], v[80:87], v0\n" \
  "v_smfmac_f32_16x16x64_bf16 v[16:19], v[48:51], v[144:151], v0\n" \
  "ds_read_b128 v[24:27], v1 offset:10240\n" \
  "ds_read_b128 v[28:31], v1 offset:14336\n" \
  "s_waitcnt lgkmcnt(6)\n" \
  "v_smfmac_f32_16x16x64_bf16 v[20:23], v[52:55], v[216:223], v0\n" \
  "v_smfmac_f32_16x16x64_bf16 v[12:15], v[52:55], v[88:95], v0\n" \
  "v_smfmac_f32_16x16x64_bf16 v[16:19], v[52:55], v[152:159], v0\n" \
  "ds_read_b128 v[32:35], v1 offset:11264\n" \
  "ds_read_b128 v[36:39], v1 offset:15360\n" \
  "s_waitcnt lgkmcnt(7)\n" \
  "v_smfmac_f32_16x16x64_bf16 v[20:23], v[56:59], v[224:231], v0\n" \
  "v_smfmac_f32_16x16x64_bf16 v[12:15], v[56:59], v[96:103], v0\n" \
  "v_smfmac_f32_16x16x64_bf16 v[16:19], v[56:59], v[160:167], v0\n" \
  "ds_read_b128 v[56:59], v1 offset:12288\n" \
  "s_waitcnt lgkmcnt(7)\n" \
  "v_smfmac_f32_16x16x64_bf16 v[20:23], v[60:63], v[232:239], v0\n" \
  "v_smfmac_f32_16x16x64_bf16 v[12:15], v[60:63], v[104:111], v0\n" \
  "v_smfmac_f32_16x16x64_bf16 v[16:19], v[60:63], v[168:175], v0\n" \
  "ds_read_b128 v[60:63], v1 offset:16384\n" \
  "s_waitcnt lgkmcnt(7)\n" \
  "v_smfmac_f32_16x16x64_bf16 v[20:23], v[40:43], v[240:247], v0\n" \
  "v_smfmac_f32_16x16x64_bf16 v[12:15], v[40:43], v[112:119], v0\n" \
  "v_smfmac_f32_16x16x64_bf16 v[16:19], v[40:43], v[176:183], v0\n" \
  "s_waitcnt lgkmcnt(6)\n" \
  "v_smfmac_f32_16x16x64_bf16 v[20:23], v[44:47], v[248:255], v0\n" \
  "v_smfmac_f32_16x16x64_bf16 v[12:15], v[44:47], v[120:127], v0\n" \
  "v_smfmac_f32_16x16x64_bf16 v[16:19], v[44:47], v[184:191], v0\n" \
  "s_cmp_eq_u32 s71, 0\n" \
  "v_permlane32_swap_b32 v20, v22\n" \
  "v_permlane32_swap_b32 v21, v23\n" \
  "v_pk_add_f32 v[20:21], v[20:21], v[22:23]\n" \
  "s_mov_b64 vcc, s[76:77]\n" \
  "v_permlane32_swap_b32 v12, v14\n" \
  "v_permlane32_swap_b32 v13, v15\n" \
  "v_cvt_pk_bf16_f32 v20, v20, v21\n" \
  "v_pk_add_f32 v[12:13], v[12:13], v[14:15]\n" \
  "v_permlane32_swap_b32 v16, v18\n" \
  "v_permlane32_swap_b32 v17, v19\n" \
  "ds_write_b32 v8, v20 offset:2048\n" \
  "v_cvt_pk_bf16_f32 v12, v12, v13\n" \
  "v_pk_add_f32 v[16:17], v[16:17], v[18:19]\n" \
  "s_nop 0\n" \
  "v_cvt_pk_bf16_f32 v13, v16, v17\n" \
  "ds_write_b64 v8, v[12:13]\n" \
  "s_setprio 1\n" \
  "s_waitcnt lgkmcnt(0)\n" \
  "s_barrier\n" \
  "s_cbranch_scc1 .LCA_LAST_%=\n" \
  "ds_read_b64_tr_b16 v[40:41], v2 offset:0\n" \
  "ds_read_b64_tr_b16 v[42:43], v3 offset:0\n" \
  "ds_read_b64_tr_b16 v[44:45], v2 offset:1024\n" \
  "ds_read_b64_tr_b16 v[46:47], v3 offset:1024\n" \
  "ds_read_b64_tr_b16 v[48:49], v2 offset:2048\n" \
  "ds_read_b64_tr_b16 v[50:51], v3 offset:2048\n" \
  "ds_read_b64_tr_b16 v[52:53], v2 offset:3072\n" \
  "ds_read_b64_tr_b16 v[54:55], v3 offset:3072\n" \
  "ds_read_b32 v11, v1 offset:26688\n" \
  "s_waitcnt lgkmcnt(7)\n" \
  "v_mfma_f32_16x16x32_bf16 v[12:15], v[24:27], v[40:43], 0\n" \
  "v_mfma_f32_16x16x32_bf16 v[16:19], v[28:31], v[40:43], 0\n" \
  "ds_read_b128 v[24:27], v1 offset:13312\n" \
  "ds_read_b128 v[28:31], v1 offset:17408\n" \
  "v_mfma_f32_16x16x32_bf16 v[20:23], v[40:43], v[40:43], 0\n" \
  "s_waitcnt lgkmcnt(7)\n" \
  "v_mfma_f32_16x16x32_bf16 v[12:15], v[32:35], v[44:47], v[12:15]\n" \
  "v_mfma_f32_16x16x32_bf16 v[16:19], v[36:39], v[44:47], v[16:19]\n" \
  "v_mfma_f32_16x16x32_bf16 v[20:23], v[44:47], v[44:47], v[20:23]\n" \
  "s_waitcnt lgkmcnt(2)\n" \
  "v_mfma_f32_16x16x32_bf16 v[12:15], v[56:59], v[48:51], v[12:15]\n" \
  "v_readfirstlane_b32 s86, v11\n" \
  "v_mfma_f32_16x16x32_bf16 v[16:19], v[60:63], v[48:51], v[16:19]\n" \
  "ds_read_b128 v[32:35], v6 offset:8192\n" \
  "ds_read_b128 v[36:39], v6 offset:9216\n" \
  "v_mfma_f32_16x16x32_bf16 v[20:23], v[48:51], v[48:51], v[20:23]\n" \
  "s_lshr_b32 s86, s86, 23\n" \
  "s_add_i32 s86, s86, s83\n" \
  "s_ashr_i32 s86, s86, 1\n" \
  "ds_read_b128 v[48:51], v10 offset:18432\n" \
  "v_mfma_f32_16x16x32_bf16 v[20:23], v[52:55], v[52:55], v[20:23]\n" \
  "s_max_i32 s86, s86, s82\n" \
  "s_min_i32 s86, s86, 100\n" \
  "s_lshl_b32 s86, s86, 23\n" \
  "s_sub_i32 s84, 0xc8800000, s86\n" \
  "s_waitcnt lgkmcnt(3)\n" \
  "v_mfma_f32_16x16x32_bf16 v[12:15], v[24:27], v[52:55], v[12:15]\n" \
  "v_mfma_f32_16x16x32_bf16 v[16:19], v[28:31], v[52:55], v[16:19]\n" \
  "ds_read_b128 v[52:55], v10 offset:22528\n" \
  "ds_read_b128 v[56:59], v6 offset:0\n" \
  "ds_read_b128 v[60:63], v6 offset:1024\n" \
  "ds_read_b128 v[24:27], v6 offset:2048\n" \
  "ds_read_b128 v[28:31], v6 offset:3072\n" \
  "v_cndmask_b32_dpp v20, v23, v22, vcc quad_perm:[2,3,0,1] row_mask:0xf bank_mask:0xf\n" \
  "v_mov_b32 v41, 0\n" \
  "v_mov_b32 v42, 0\n" \
  "v_cndmask_b32_dpp v12, v16, v12, vcc quad_perm:[2,3,0,1] row_mask:0xf bank_mask:0xf\n" \
  "v_cndmask_b32_dpp v13, v17, v13, vcc quad_perm:[2,3,0,1] row_mask:0xf bank_mask:0xf\n" \
  "v_cndmask_b32_dpp v14, v18, v14, vcc quad_perm:[2,3,0,1] row_mask:0xf bank_mask:0xf\n" \
  "v_cndmask_b32_dpp v15, v19, v15, vcc quad_perm:[2,3,0,1] row_mask:0xf bank_mask:0xf\n" \
  "v_maximum3_f32 v16, v20, v12, v13\n" \
  "v_maximum3_f32 v16, v16, v14, v15\n" \
  "s_nop 1\n" \
  "v_max_f32_dpp v16, v16, v16 quad_perm:[2,3,0,1] row_mask:0xf bank_mask:0xf\n" \
  "v_mov_b32 v17, v16\n" \
  "s_nop 1\n" \
  "v_permlane16_swap_b32 v16, v17\n" \
  "v_max_f32 v16, v16, v17\n" \
  "v_mov_b32 v17, v16\n" \
  "s_nop 1\n" \
  "v_permlane32_swap_b32 v16, v17\n" \
  "v_max_f32 v16, v16, v17\n" \
  "v_mul_f32 v16, s78, v16\n" \
  "v_pk_fma_f32 v[12:13], v[12:13], s[78:79], v[16:17] op_sel_hi:[1,0,0] neg_lo:[0,0,1] neg_hi:[0,0,1]\n" \
  "v_pk_fma_f32 v[14:15], v[14:15], s[78:79], v[16:17] op_sel_hi:[1,0,0] neg_lo:[0,0,1] neg_hi:[0,0,1]\n" \
  "v_fma_f32 v20, v20, s78, -v16\n" \
  "v_exp_f32 v12, v12\n" \
  "v_exp_f32 v13, v13\n" \
  "v_exp_f32 v14, v14\n" \
  "v_exp_f32 v15, v15\n" \
  "v_exp_f32 v20, v20\n" \
  "s_waitcnt lgkmcnt(4)\n" \
  "v_cvt_pk_bf16_f32 v44, v12, v13\n" \
  "v_cvt_pk_bf16_f32 v45, v14, v15\n" \
  "v_pk_add_f32 v[8:9], v[12:13], v[14:15]\n" \
  "v_mov_b32_dpp v46, v44 quad_perm:[2,3,0,1] row_mask:0xf bank_mask:0xf\n" \
  "v_mov_b32_dpp v47, v45 quad_perm:[2,3,0,1] row_mask:0xf bank_mask:0xf\n" \
  "v_add_f32 v10, v8, v20\n" \
  "v_add_f32 v10, v10, v9\n" \
  "v_mfma_f32_16x16x32_bf16 v[12:15], v[48:51], v[44:47], 0\n" \
  "v_mov_b32_dpp v21, v20 quad_perm:[2,3,0,1] row_mask:0xf bank_mask:0xf\n" \
  "v_add_f32_dpp v10, v10, v10 quad_perm:[2,3,0,1] row_mask:0xf bank_mask:0xf\n" \
  "v_mov_b32 v11, v10\n" \
  "v_mfma_f32_16x16x32_bf16 v[16:19], v[52:55], v[44:47], 0\n" \
  "s_setprio 3\n" \
  "s_barrier\n" \
  "ds_read_b64 v[22:23], v7 offset:0\n" \
  "s_nop 0\n" \
  "v_permlane16_swap_b32 v10, v11\n" \
  "v_add_f32 v10, v10, v11\n" \
  "v_mov_b32 v11, v10\n" \
  "v_mov_b64 v[8:9], s[92:93]\n" \
  "s_nop 0\n" \
  "v_permlane32_swap_b32 v10, v11\n" \
  "v_add_f32 v10, v10, v11\n" \
  "v_rcp_f32 v10, v10\n" \
  "s_nop 0\n" \
  "v_mul_f32 v10, s84, v10\n" \
  "v_pk_mul_f32 v[20:21], v[20:21], v[10:11] op_sel_hi:[1,0]\n" \
  "v_pk_mul_f32 v[12:13], v[12:13], v[10:11] op_sel_hi:[1,0]\n" \
  "v_pk_mul_f32 v[14:15], v[14:15], v[10:11] op_sel_hi:[1,0]\n" \
  "v_cvt_pk_bf16_f32 v40, v20, v21\n" \
  "v_pk_mul_f32 v[16:17], v[16:17], v[10:11] op_sel_hi:[1,0]\n" \
  "v_cvt_pk_bf16_f32 v43, v20, v21\n" \
  "v_pk_mul_f32 v[18:19], v[18:19], v[10:11] op_sel_hi:[1,0]\n" \
  "s_waitcnt lgkmcnt(0)\n" \
  "v_mfma_f32_16x16x16_bf16 v[12:15], v[22:23], v[40:41], v[12:15]\n" \
  "v_mfma_f32_16x16x16_bf16 v[16:19], v[22:23], v[42:43], v[16:19]\n" \
  "v_mov_b64 v[10:11], s[92:93]\n" \
  "ds_read_b128 v[48:51], v6 offset:10240\n" \
  "ds_read_b128 v[52:55], v6 offset:11264\n" \
  "v_mov_b64 v[20:21], s[92:93]\n" \
  "v_mov_b64 v[22:23], s[92:93]\n" \
  "s_nop 1\n" \
  "v_cvt_pk_bf16_f32 v40, v12, v13\n" \
  "v_cvt_pk_bf16_f32 v41, v14, v15\n" \
  "v_cvt_pk_bf16_f32 v42, v16, v17\n" \
  "ds_write_b64 v4, v[40:41]\n" \
  "v_cvt_pk_bf16_f32 v43, v18, v19\n" \
  "ds_write_b64 v4, v[42:43] offset:1024\n" \
  "v_mov_b64 v[12:13], s[92:93]\n" \
  "v_mov_b64 v[14:15], s[92:93]\n" \
  "v_mov_b64 v[16:17], s[92:93]\n" \
  "v_mov_b64 v[18:19], s[92:93]\n" \
  "s_setprio 0\n" \
  "s_waitcnt lgkmcnt(0)\n" \
  "s_barrier\n" \
  "ds_read_b128 v[40:43], v1 offset:8192\n" \
  "ds_read_b128 v[44:47], v1 offset:9216\n" \
  "s_waitcnt lgkmcnt(1)\n" \
  "v_smfmac_f32_16x16x64_bf16 v[12:15], v[40:43], v[56:63], v0\n" \
  "ds_read_b128 v[56:59], v6 offset:4096\n" \
  "ds_read_b128 v[60:63], v6 offset:5120\n" \
  "v_smfmac_f32_16x16x64_bf16 v[16:19], v[40:43], v[24:31], v0\n" \
  "ds_read_b128 v[24:27], v6 offset:6144\n" \
  "ds_read_b128 v[28:31], v6 offset:7168\n" \
  "s_waitcnt lgkmcnt(4)\n" \
  "v_smfmac_f32_16x16x64_bf16 v[12:15], v[44:47], v[32:39], v0\n" \
  "ds_read_b128 v[32:35], v6 offset:12288\n" \
  "ds_read_b128 v[36:39], v6 offset:13312\n" \
  "v_smfmac_f32_16x16x64_bf16 v[16:19], v[44:47], v[48:55], v0\n" \
  "ds_read_b128 v[48:51], v6 offset:14336\n" \
  "ds_read_b128 v[52:55], v6 offset:15360\n" \
  "s_nop 5\n" \
  "v_permlane32_swap_b32 v12, v17\n" \
  "v_permlane32_swap_b32 v13, v16\n" \
  "v_permlane32_swap_b32 v14, v19\n" \
  "v_permlane32_swap_b32 v15, v18\n" \
  "v_pk_add_f32 v[12:13], v[12:13], v[16:17] op_sel:[0,1] op_sel_hi:[1,0]\n" \
  "v_pk_add_f32 v[14:15], v[14:15], v[18:19] op_sel:[0,1] op_sel_hi:[1,0]\n" \
  "buffer_atomic_add_x2 v[12:13], v5, s[40:43], s56 offen\n" \
  "buffer_atomic_add_x2 v[14:15], v5, s[40:43], s57 offen\n" \
  "s_waitcnt lgkmcnt(6)\n" \
  "v_smfmac_f32_16x16x64_bf16 v[20:23], v[40:43], v[56:63], v0\n" \
  "s_waitcnt lgkmcnt(4)\n" \
  "v_smfmac_f32_16x16x64_bf16 v[8:11], v[40:43], v[24:31], v0\n" \
  "s_waitcnt lgkmcnt(2)\n" \
  "v_smfmac_f32_16x16x64_bf16 v[20:23], v[44:47], v[32:39], v0\n" \
  "s_waitcnt lgkmcnt(0)\n" \
  "v_smfmac_f32_16x16x64_bf16 v[8:11], v[44:47], v[48:55], v0\n" \
  "s_nop 7\n" \
  "v_permlane32_swap_b32 v20, v9\n" \
  "v_permlane32_swap_b32 v21, v8\n" \
  "v_permlane32_swap_b32 v22, v11\n" \
  "v_permlane32_swap_b32 v23, v10\n" \
  "v_pk_add_f32 v[20:21], v[20:21], v[8:9] op_sel:[0,1] op_sel_hi:[1,0]\n" \
  "v_pk_add_f32 v[22:23], v[22:23], v[10:11] op_sel:[0,1] op_sel_hi:[1,0]\n" \
  "buffer_atomic_add_x2 v[20:21], v5, s[40:43], s58 offen\n" \
  "buffer_atomic_add_x2 v[22:23], v5, s[40:43], s59 offen\n" \
  "s_memrealtime s[38:39]\n" \
  "v_mbcnt_lo_u32_b32 v48, -1, 0\n" \
  "v_mbcnt_hi_u32_b32 v48, -1, v48\n" \
  "s_add_u32 s88, s74, 157760\n" \
  "v_mov_b32 v59, s88\n" \
  "v_lshl_add_u32 v52, v48, 2, v59\n" \
  "ds_read_b32 v52, v52 offset:192\n" \
  "v_lshrrev_b32 v49, 3, v48\n" \
  "v_and_b32 v50, 7, v48\n" \
  "v_lshlrev_b32 v50, 4, v50\n" \
  "v_lshl_or_b32 v44, v49, 8, v50\n" \
  "s_mov_b32 s87, 0x40000\n" \
  "v_lshrrev_b32 v49, 2, v48\n" \
  "v_lshrrev_b32 v50, 4, v48\n" \
  "v_bitop3_b32 v50, v49, v50, 3 bitop3:0x28\n" \
  "v_lshl_add_u32 v8, v50, 3, s97\n" \
  "v_and_b32 v50, 3, v48\n" \
  "v_lshl_add_u32 v8, v50, 5, v8\n" \
  "v_and_b32 v49, 1, v49\n" \
  "v_lshl_add_u32 v8, v49, 8, v8\n" \
  "v_and_b32 v49, 8, v48\n" \
  "v_lshl_add_u32 v8, v49, 4, v8\n" \
  "v_lshrrev_b32 v49, 5, v48\n" \
  "v_add_u32 v10, s99, v1\n" \
  "s_lshr_b32 s88, s99, 2\n" \
  "s_add_u32 s88, s88, s74\n" \
  "s_add_u32 s88, s88, 139264\n" \
  "v_lshl_add_u32 v7, v49, 10, s88\n" \
  "v_and_b32 v49, 31, v48\n" \
  "v_lshl_add_u32 v7, v49, 3, v7\n" \
  "v_mov_b64 v[12:13], 0\n" \
  "v_mov_b64 v[14:15], 0\n" \
  "v_mov_b64 v[16:17], 0\n" \
  "v_mov_b64 v[18:19], 0\n" \
  "v_mov_b64 v[20:21], 0\n" \
  "v_mov_b64 v[22:23], 0\n" \
  "v_mov_b32 v34, 0xc9b26ca0\n" \
  "v_mov_b32 v35, 0xc9b26c86\n" \
  "v_and_b32 v50, 3, v48\n" \
  "v_lshl_add_u32 v40, v50, 2, s98\n" \
  "v_lshrrev_b32 v50, 4, v48\n" \
  "v_mad_u32_u24 v40, v50, s95, v40\n" \
  "v_bfe_u32 v49, v48, 3, 1\n" \
  "v_lshl_add_u32 v40, v49, 6, v40\n" \
  "v_bfe_u32 v49, v48, 2, 1\n" \
  "v_lshl_add_u32 v40, v49, 7, v40\n" \
  "v_xor_b32 v41, 16, v40\n" \
  "v_xor_b32 v42, 32, v40\n" \
  "v_xor_b32 v43, 48, v40\n" \
  "s_add_i32 s80, s86, 0x35000000\n" \
  "v_mov_b32 v11, 0\n" \
  "s_mov_b32 s89, s60\n" \
  "s_bitcmp1_b32 s60, 7\n" \
  "s_cselect_b32 s88, 0x10000, 0\n" \
  "s_bitset1_b32 s88, 7\n" \
  "s_xor_b32 s56, s56, s88\n" \
  "s_xor_b32 s57, s57, s88\n" \
  "s_xor_b32 s58, s58, s88\n" \
  "s_xor_b32 s59, s59, s88\n" \
  "s_xor_b32 s60, s60, s88\n" \
  "s_sub_u32 s71, s71, 1\n" \
  "s_waitcnt lgkmcnt(0)\n" \
  "s_sub_u32 s39, s38, 150\n" \
  "v_subrev_u32 v52, s39, v52\n" \
  "v_cmp_lt_u32 vcc, 0x1000, v52\n" \
  "s_cmp_eq_u64 vcc, 0\n" \
  "s_cbranch_scc1 .LCA_OKM_%=\n" \
  ".LCA_AGN_%=:\n" \
  "buffer_load_dword v52, v5, s[0:3], s66 offen sc1\n" \
  "s_sub_u32 s87, s87, 1\n" \
  "s_cbranch_scc1 .LCA_END_%=\n" \
  "s_waitcnt vmcnt(0)\n" \
  "v_cmp_eq_u32 vcc, 0, v52\n" \
  "v_subrev_u32 v52, s39, v52\n" \
  "s_cmp_eq_u64 vcc, 0\n" \
  "s_cbranch_scc1 .LCA_OKM_%=\n" \
  "s_sleep 4\n" \
  "s_branch .LCA_AGN_%=\n" \
  ".LCA_OKM_%=:\n" \
  "s_nop 1\n" \
  "v_max_i32_dpp v52, v52, v52 row_ror:1 row_mask:0xf bank_mask:0xf\n" \
  "s_nop 1\n" \
  "v_max_i32_dpp v52, v52, v52 row_ror:2 row_mask:0xf bank_mask:0xf\n" \
  "s_nop 1\n" \
  "v_max_i32_dpp v52, v52, v52 row_ror:4 row_mask:0xf bank_mask:0xf\n" \
  "s_nop 1\n" \
  "v_max_i32_dpp v52, v52, v52 row_ror:8 row_mask:0xf bank_mask:0xf\n" \
  "s_movk_i32 vcc_lo, 150\n" \
  "v_readlane_b32 s88, v52, 0\n" \
  "s_max_i32 vcc_lo, vcc_lo, s88\n" \
  "v_readlane_b32 s88, v52, 16\n" \
  "s_max_i32 vcc_lo, vcc_lo, s88\n" \
  "v_readlane_b32 s88, v52, 32\n" \
  "s_max_i32 vcc_lo, vcc_lo, s88\n" \
  "v_readlane_b32 s88, v52, 48\n" \
  "s_max_i32 vcc_lo, vcc_lo, s88\n" \
  "s_add_u32 s66, s66, 1024\n" \
  "s_add_u32 vcc_lo, vcc_lo, s39\n" \
  "s_add_u32 vcc_lo, vcc_lo, 23\n" \
  ".LCA_TW_%=:\n" \
  "s_memrealtime s[38:39]\n" \
  "s_waitcnt lgkmcnt(0)\n" \
  "s_sub_u32 s38, s38, vcc_lo\n" \
  "s_cmp_lt_i32 s38, 0\n" \
  "s_cbranch_scc1 .LCA_TW_%=\n" \
  "buffer_load_dwordx4 v[24:27], v44, s[40:43], s89 offen sc1\n" \
  "buffer_load_dwordx4 v[28:31], v44, s[40:43], s89 offen offset:2048 sc1\n" \
  ".LCA_SEE_%=:\n" \
  "s_waitcnt vmcnt(1)\n" \
  "v_cvt_f32_i32 v24, v24\n" \
  "v_cvt_f32_i32 v25, v25\n" \
  "v_cvt_f32_i32 v26, v26\n" \
  "v_cvt_f32_i32 v27, v27\n" \
  "v_max_f32 v45, |v24|, |v26|\n" \
  "v_pk_mul_f32 v[24:25], v[24:25], s[80:81] op_sel_hi:[1,0]\n" \
  "v_pk_mul_f32 v[26:27], v[26:27], s[80:81] op_sel_hi:[1,0]\n" \
  "s_nop 0\n" \
  "v_cvt_pk_bf16_f32 v32, v24, v26\n" \
  "v_cvt_pk_bf16_f32 v33, v25, v27\n" \
  "ds_write_b32 v40, v32\n" \
  "ds_write_b32 v41, v33\n" \
  "s_waitcnt vmcnt(0)\n" \
  "v_cvt_f32_i32 v28, v28\n" \
  "v_cvt_f32_i32 v30, v30\n" \
  "v_cvt_f32_i32 v29, v29\n" \
  "v_max3_f32 v45, v45, |v28|, |v30|\n" \
  "v_cvt_f32_i32 v31, v31\n" \
  "v_cmp_lt_f32 vcc, s91, v45\n" \
  "v_pk_mul_f32 v[28:29], v[28:29], s[80:81] op_sel_hi:[1,0]\n" \
  "v_pk_mul_f32 v[30:31], v[30:31], s[80:81] op_sel_hi:[1,0]\n" \
  "s_cmp_eq_u64 vcc, 0\n" \
  "s_cbranch_scc0 .LCA_POLL_%=\n" \
  "v_cvt_pk_bf16_f32 v36, v28, v30\n" \
  "v_cvt_pk_bf16_f32 v37, v29, v31\n" \
  "ds_write_b32 v42, v36\n" \
  "ds_write_b32 v43, v37\n" \
  "v_dot2c_f32_bf16 v11, v32, v32\n" \
  "v_dot2c_f32_bf16 v11, v33, v33\n" \
  "v_dot2c_f32_bf16 v11, v36, v36\n" \
  "v_dot2c_f32_bf16 v11, v37, v37\n" \
  "s_nop 1\n" \
  "s_nop 0\n" \
  "v_cmp_lt_f32 vcc, 4.0, v11\n" \
  "s_cbranch_vccz .LCA_TOP_%=\n" \
  "s_mov_b64 exec, vcc\n" \
  "ds_max_u32 v59, v11\n" \
  "s_mov_b64 exec, -1\n" \
  "s_branch .LCA_TOP_%=\n" \
  ".LCA_POLL_%=:\n" \
  "buffer_load_dwordx4 v[24:27], v44, s[40:43], s89 offen sc1\n" \
  "buffer_load_dwordx4 v[28:31], v44, s[40:43], s89 offen offset:2048 sc1\n" \
  "s_sub_u32 s87, s87, 1\n" \
  "s_cbranch_scc1 .LCA_END_%=\n" \
  "s_branch .LCA_SEE_%=\n" \
  ".LCA_LAST_%=:\n" \
  ".LCA_END_%=:\n" \
  "s_endpgm\n"
#define CHAIN_ASM_VO \
  "s_mov_b32 s95, 272\n" \
  "s_mov_b32 s91, 0x4c7c2000\n" \
  "s_mov_b32 s85, 0xc9b26c9b\n" \
  "s_mov_b32 s92, 0xc9326c9b\n" \
  "s_mov_b32 s93, 0xc9326c9b\n" \
  "s_mov_b32 s78, 0x3e0293ee\n" \
  "s_mov_b32 s76, 0x33333333\n" \
  "s_mov_b32 s77, 0x33333333\n" \
  "s_lshl_b32 s98, s73, 10\n" \
  "s_add_u32 s98, s98, s74\n" \
  "s_add_u32 s98, s98, 131072\n" \
  "s_lshl_b32 s97, s73, 9\n" \
  "s_add_u32 s97, s97, s74\n" \
  "s_add_u32 s97, s97, 158724\n" \
  "s_lshl_b32 s99, s73, 11\n" \
  "s_add_u32 s99, s99, s74\n" \
  "s_add_u32 s99, s99, 133120\n" \
  "s_lshl_b32 s94, s73, 4\n" \
  "s_add_u32 s94, s94, s74\n" \
  "s_add_u32 s94, s94, 157760\n" \
  "v_mbcnt_lo_u32_b32 v48, -1, 0\n" \
  "v_mbcnt_hi_u32_b32 v48, -1, v48\n" \
  "s_add_u32 s88, s74, 157760\n" \
  "v_mov_b32 v59, s88\n" \
  "s_sub_u32 s88, s73, 4\n" \
  "v_lshrrev_b32 v49, 2, v48\n" \
  "v_lshrrev_b32 v50, 4, v48\n" \
  "v_bitop3_b32 v50, v49, v50, 3 bitop3:0x28\n" \
  "v_lshl_add_u32 v4, v50, 3, s97\n" \
  "v_and_b32 v50, 3, v48\n" \
  "v_lshl_add_u32 v4, v50, 5, v4\n" \
  "v_and_b32 v49, 1, v49\n" \
  "v_lshl_add_u32 v4, v49, 8, v4\n" \
  "v_and_b32 v49, 8, v48\n" \
  "v_lshl_add_u32 v4, v49, 4, v4\n" \
  "v_and_b32 v51, 15, v48\n" \
  "v_bfe_u32 v49, v48, 4, 1\n" \
  "v_lshrrev_b32 v50, 5, v48\n" \
  "s_lshl_b32 s89, s88, 10\n" \
  "s_add_u32 s89, s89, s74\n" \
  "s_add_u32 s89, s89, 158720\n" \
  "v_lshrrev_b32 v9, 3, v51\n" \
  "v_mul_u32_u24 v6, 41, v9\n" \
  "v_xor_b32 v6, v6, v51\n" \
  "v_lshl_add_u32 v6, v6, 3, s89\n" \
  "v_lshl_add_u32 v6, v49, 6, v6\n" \
  "v_lshl_add_u32 v6, v50, 9, v6\n" \
  "v_xor_b32 v9, 0x90, v6\n" \
  "v_lshlrev_b32 v8, 4, v49\n" \
  "v_lshl_add_u32 v8, v51, 5, v8\n" \
  "v_lshl_add_u32 v8, v50, 3, v8\n" \
  "s_lshl_b32 s89, s88, 8\n" \
  "s_add_u32 s89, s89, s74\n" \
  "s_add_u32 s89, s89, 139264\n" \
  "v_and_b32 v49, 31, v48\n" \
  "v_lshl_add_u32 v5, v49, 3, s89\n" \
  "v_lshl_add_u32 v5, v50, 10, v5\n" \
  "v_lshrrev_b32 v49, 2, v51\n" \
  "v_and_b32 v50, 5, v51\n" \
  "v_and_or_b32 v49, v49, 2, v50\n" \
  "v_and_b32 v50, 48, v48\n" \
  "v_lshlrev_b32 v7, 3, v50\n" \
  "v_lshl_add_u32 v7, v49, 4, v7\n" \
  "v_bitop3_b32 v49, v51, 2, 2 bitop3:0x0c\n" \
  "v_lshl_add_u32 v7, v49, 29, v7\n" \
  "v_mov_b64 v[12:13], 0\n" \
  "v_mov_b64 v[14:15], 0\n" \
  "v_mov_b64 v[16:17], 0\n" \
  "v_mov_b64 v[18:19], 0\n" \
  "v_mov_b64 v[20:21], 0\n" \
  "v_mov_b64 v[22:23], 0\n" \
  "v_lshlrev_b32 v10, 4, v48\n" \
  "s_add_u32 s78, s74, 157952\n" \
  "v_lshl_add_u32 v52, v48, 2, v59\n" \
  "ds_write_b32 v52, v12 offset:192\n" \
  "s_movk_i32 s79, 105\n" \
  "s_movk_i32 s96, 25\n" \
  "s_mov_b32 s87, 0x40000\n" \
  "v_subrev_u32 v30, 64, v59\n" \
  "v_add_u32 v17, 0x10000, v28\n" \
  "v_xor_b32 v18, 128, v28\n" \
  "v_xor_b32 v19, 128, v17\n" \
  "ds_write_b32 v30, v12 offset:0\n" \
  "ds_write_b32 v30, v12 offset:32\n" \
  "ds_write_b32 v30, v12 offset:64\n" \
  "v_xor_b32 v20, 0, v29\n" \
  "v_xor_b32 v21, 32, v29\n" \
  "v_xor_b32 v22, 64, v29\n" \
  "v_xor_b32 v23, 96, v29\n" \
  "s_waitcnt vmcnt(12) lgkmcnt(0)\n" \
  "s_barrier\n" \
  "ds_read_b64_tr_b16 v[96:97], v20 offset:0\n" \
  "ds_read_b64_tr_b16 v[98:99], v20 offset:2048\n" \
  "ds_read_b64_tr_b16 v[104:105], v21 offset:0\n" \
  "ds_read_b64_tr_b16 v[106:107], v21 offset:2048\n" \
  "ds_read_b64_tr_b16 v[112:113], v22 offset:0\n" \
  "ds_read_b64_tr_b16 v[114:115], v22 offset:2048\n" \
  "ds_read_b64_tr_b16 v[120:121], v23 offset:0\n" \
  "ds_read_b64_tr_b16 v[122:123], v23 offset:2048\n" \
  "s_lshl_b32 s88, s73, 10\n" \
  "v_add_u32 v48, s88, v1\n" \
  "ds_read_b128 v[52:55], v48 offset:0\n" \
  "s_waitcnt lgkmcnt(0)\n" \
  "v_mov_b32 v11, 0\n" \
  "v_dot2c_f32_bf16 v11, v52, v52\n" \
  "v_dot2c_f32_bf16 v11, v53, v53\n" \
  "v_dot2c_f32_bf16 v11, v54, v54\n" \
  "v_dot2c_f32_bf16 v11, v55, v55\n" \
  "s_nop 0\n" \
  "s_nop 1\n" \
  "v_max_u32_dpp v11, v11, v11 row_ror:1 row_mask:0xf bank_mask:0xf\n" \
  "s_nop 1\n" \
  "v_max_u32_dpp v11, v11, v11 row_ror:2 row_mask:0xf bank_mask:0xf\n" \
  "s_nop 1\n" \
  "v_max_u32_dpp v11, v11, v11 row_ror:4 row_mask:0xf bank_mask:0xf\n" \
  "s_nop 1\n" \
  "v_max_u32_dpp v11, v11, v11 row_ror:8 row_mask:0xf bank_mask:0xf\n" \
  "v_mov_b32 v31, v11\n" \
  "s_nop 1\n" \
  "v_permlane16_swap_b32 v11, v31\n" \
  "v_max_u32 v11, v11, v31\n" \
  "v_mov_b32 v31, v11\n" \
  "s_nop 1\n" \
  "v_permlane32_swap_b32 v11, v31\n" \
  "v_max_u32 v11, v11, v31\n" \
  "s_mov_b64 exec, 1\n" \
  "ds_max_u32 v59, v11\n" \
  "s_mov_b64 exec, -1\n" \
  "s_waitcnt vmcnt(8) lgkmcnt(0)\n" \
  "s_barrier\n" \
  "ds_read_b64_tr_b16 v[100:101], v20 offset:4096\n" \
  "ds_read_b64_tr_b16 v[102:103], v20 offset:6144\n" \
  "ds_read_b64_tr_b16 v[108:109], v21 offset:4096\n" \
  "ds_read_b64_tr_b16 v[110:111], v21 offset:6144\n" \
  "ds_read_b64_tr_b16 v[116:117], v22 offset:4096\n" \
  "ds_read_b64_tr_b16 v[118:119], v22 offset:6144\n" \
  "ds_read_b64_tr_b16 v[124:125], v23 offset:4096\n" \
  "ds_read_b64_tr_b16 v[126:127], v23 offset:6144\n" \
  "s_add_u32 m0, s19, 0\n" \
  "s_nop 0\n" \
  "global_load_lds_dwordx4 v24, s[4:5] offset:0\n" \
  "global_load_lds_dwordx4 v25, s[4:5] offset:1024\n" \
  "global_load_lds_dwordx4 v26, s[4:5] offset:2048\n" \
  "global_load_lds_dwordx4 v27, s[4:5] offset:3072\n" \
  "s_add_u32 s4, s4, 0x100000\n" \
  "s_addc_u32 s5, s5, 0\n" \
  "v_mfma_f32_16x16x32_bf16 v[64:67], v[96:99], v[96:99], 0\n" \
  "v_mfma_f32_16x16x32_bf16 v[68:71], v[104:107], v[104:107], 0\n" \
  "v_mfma_f32_16x16x32_bf16 v[72:75], v[112:115], v[112:115], 0\n" \
  "v_mfma_f32_16x16x32_bf16 v[76:79], v[120:123], v[120:123], 0\n" \
  "s_waitcnt vmcnt(8) lgkmcnt(0)\n" \
  "s_barrier\n" \
  "ds_read_b64_tr_b16 v[32:33], v20 offset:8192\n" \
  "ds_read_b64_tr_b16 v[34:35], v20 offset:10240\n" \
  "ds_read_b64_tr_b16 v[40:41], v21 offset:8192\n" \
  "ds_read_b64_tr_b16 v[42:43], v21 offset:10240\n" \
  "ds_read_b64_tr_b16 v[48:49], v22 offset:8192\n" \
  "ds_read_b64_tr_b16 v[50:51], v22 offset:10240\n" \
  "ds_read_b64_tr_b16 v[56:57], v23 offset:8192\n" \
  "ds_read_b64_tr_b16 v[58:59], v23 offset:10240\n" \
  "s_add_u32 m0, s19, 4096\n" \
  "s_nop 0\n" \
  "global_load_lds_dwordx4 v24, s[4:5] offset:0\n" \
  "global_load_lds_dwordx4 v25, s[4:5] offset:1024\n" \
  "global_load_lds_dwordx4 v26, s[4:5] offset:2048\n" \
  "global_load_lds_dwordx4 v27, s[4:5] offset:3072\n" \
  "s_add_u32 s4, s4, 0x100000\n" \
  "s_addc_u32 s5, s5, 0\n" \
  "v_mfma_f32_16x16x32_bf16 v[64:67], v[100:103], v[100:103], v[64:67]\n" \
  "v_mfma_f32_16x16x32_bf16 v[68:71], v[108:111], v[108:111], v[68:71]\n" \
  "v_mfma_f32_16x16x32_bf16 v[72:75], v[116:119], v[116:119], v[72:75]\n" \
  "v_mfma_f32_16x16x32_bf16 v[76:79], v[124:127], v[124:127], v[76:79]\n" \
  "s_waitcnt vmcnt(8) lgkmcnt(0)\n" \
  "s_barrier\n" \
  "ds_read_b64_tr_b16 v[36:37], v20 offset:12288\n" \
  "ds_read_b64_tr_b16 v[38:39], v20 offset:14336\n" \
  "ds_read_b64_tr_b16 v[44:45], v21 offset:12288\n" \
  "ds_read_b64_tr_b16 v[46:47], v21 offset:14336\n" \
  "ds_read_b64_tr_b16 v[52:53], v22 offset:12288\n" \
  "ds_read_b64_tr_b16 v[54:55], v22 offset:14336\n" \
  "ds_read_b64_tr_b16 v[60:61], v23 offset:12288\n" \
  "ds_read_b64_tr_b16 v[62:63], v23 offset:14336\n" \
  "s_add_u32 m0, s19, 8192\n" \
  "s_nop 0\n" \
  "global_load_lds_dwordx4 v24, s[4:5] offset:0\n" \
  "global_load_lds_dwordx4 v25, s[4:5] offset:1024\n" \
  "global_load_lds_dwordx4 v26, s[4:5] offset:2048\n" \
  "global_load_lds_dwordx4 v27, s[4:5] offset:3072\n" \
  "s_add_u32 s4, s4, 0x100000\n" \
  "s_addc_u32 s5, s5, 0\n" \
  "v_mfma_f32_16x16x32_bf16 v[64:67], v[32:35], v[32:35], v[64:67]\n" \
  "v_mfma_f32_16x16x32_bf16 v[68:71], v[40:43], v[40:43], v[68:71]\n" \
  "v_mfma_f32_16x16x32_bf16 v[72:75], v[48:51], v[48:51], v[72:75]\n" \
  "v_mfma_f32_16x16x32_bf16 v[76:79], v[56:59], v[56:59], v[76:79]\n" \
  "s_waitcnt vmcnt(8) lgkmcnt(0)\n" \
  "s_barrier\n" \
  "ds_read_b64_tr_b16 v[128:129], v18 offset:0\n" \
  "ds_read_b64_tr_b16 v[130:131], v18 offset:512\n" \
  "ds_read_b64_tr_b16 v[132:133], v18 offset:32768\n" \
  "ds_read_b64_tr_b16 v[134:135], v18 offset:33280\n" \
  "ds_read_b64_tr_b16 v[136:137], v19 offset:0\n" \
  "ds_read_b64_tr_b16 v[138:139], v19 offset:512\n" \
  "ds_read_b64_tr_b16 v[140:141], v19 offset:32768\n" \
  "ds_read_b64_tr_b16 v[142:143], v19 offset:33280\n" \
  "ds_read_b64_tr_b16 v[192:193], v28 offset:0\n" \
  "ds_read_b64_tr_b16 v[194:195], v28 offset:512\n" \
  "ds_read_b64_tr_b16 v[196:197], v28 offset:32768\n" \
  "ds_read_b64_tr_b16 v[198:199], v28 offset:33280\n" \
  "ds_read_b64_tr_b16 v[200:201], v17 offset:0\n" \
  "ds_read_b64_tr_b16 v[202:203], v17 offset:512\n" \
  "ds_read_b64_tr_b16 v[204:205], v17 offset:32768\n" \
  "ds_read_b64_tr_b16 v[206:207], v17 offset:33280\n" \
  "s_add_u32 m0, s19, 12288\n" \
  "s_nop 0\n" \
  "global_load_lds_dwordx4 v24, s[4:5] offset:0\n" \
  "global_load_lds_dwordx4 v25, s[4:5] offset:1024\n" \
  "global_load_lds_dwordx4 v26, s[4:5] offset:2048\n" \
  "global_load_lds_dwordx4 v27, s[4:5] offset:3072\n" \
  "s_add_u32 s4, s4, 0x100000\n" \
  "s_addc_u32 s5, s5, 0\n" \
  "v_mfma_f32_16x16x32_bf16 v[64:67], v[36:39], v[36:39], v[64:67]\n" \
  "v_mfma_f32_16x16x32_bf16 v[68:71], v[44:47], v[44:47], v[68:71]\n" \
  "v_mfma_f32_16x16x32_bf16 v[72:75], v[52:55], v[52:55], v[72:75]\n" \
  "v_mfma_f32_16x16x32_bf16 v[76:79], v[60:63], v[60:63], v[76:79]\n" \
  "s_nop 7\n" \
  "v_max3_f32 v13, v64, v65, v66\n" \
  "v_max3_f32 v13, v13, v67, v68\n" \
  "v_max3_f32 v13, v13, v69, v70\n" \
  "v_max3_f32 v13, v13, v71, v72\n" \
  "v_max3_f32 v13, v13, v73, v74\n" \
  "v_max3_f32 v13, v13, v75, v76\n" \
  "v_max3_f32 v13, v13, v77, v78\n" \
  "v_max_f32 v13, v13, v79\n" \
  "s_nop 1\n" \
  "v_max_f32_dpp v13, v13, v13 row_ror:1 row_mask:0xf bank_mask:0xf\n" \
  "s_nop 1\n" \
  "v_max_f32_dpp v13, v13, v13 row_ror:2 row_mask:0xf bank_mask:0xf\n" \
  "s_nop 1\n" \
  "v_max_f32_dpp v13, v13, v13 row_ror:4 row_mask:0xf bank_mask:0xf\n" \
  "s_nop 1\n" \
  "v_max_f32_dpp v13, v13, v13 row_ror:8 row_mask:0xf bank_mask:0xf\n" \
  "v_mov_b32 v31, v13\n" \
  "s_nop 1\n" \
  "v_permlane16_swap_b32 v13, v31\n" \
  "v_max_f32 v13, v13, v31\n" \
  "v_mov_b32 v31, v13\n" \
  "s_nop 1\n" \
  "v_permlane32_swap_b32 v13, v31\n" \
  "v_max_f32 v13, v13, v31\n" \
  "s_mov_b64 exec, 1\n" \
  "ds_max_u32 v30, v13 offset:32\n" \
  "s_mov_b64 exec, -1\n" \
  "s_waitcnt vmcnt(8) lgkmcnt(0)\n" \
  "s_barrier\n" \
  "ds_read_b64_tr_b16 v[144:145], v18 offset:4096\n" \
  "ds_read_b64_tr_b16 v[146:147], v18 offset:4608\n" \
  "ds_read_b64_tr_b16 v[148:149], v18 offset:36864\n" \
  "ds_read_b64_tr_b16 v[150:151], v18 offset:37376\n" \
  "ds_read_b64_tr_b16 v[152:153], v19 offset:4096\n" \
  "ds_read_b64_tr_b16 v[154:155], v19 offset:4608\n" \
  "ds_read_b64_tr_b16 v[156:157], v19 offset:36864\n" \
  "ds_read_b64_tr_b16 v[158:159], v19 offset:37376\n" \
  "ds_read_b64_tr_b16 v[208:209], v28 offset:4096\n" \
  "ds_read_b64_tr_b16 v[210:211], v28 offset:4608\n" \
  "ds_read_b64_tr_b16 v[212:213], v28 offset:36864\n" \
  "ds_read_b64_tr_b16 v[214:215], v28 offset:37376\n" \
  "ds_read_b64_tr_b16 v[216:217], v17 offset:4096\n" \
  "ds_read_b64_tr_b16 v[218:219], v17 offset:4608\n" \
  "ds_read_b64_tr_b16 v[220:221], v17 offset:36864\n" \
  "ds_read_b64_tr_b16 v[222:223], v17 offset:37376\n" \
  "s_add_u32 m0, s19, 0\n" \
  "s_nop 0\n" \
  "global_load_lds_dwordx4 v24, s[6:7] offset:0\n" \
  "global_load_lds_dwordx4 v25, s[6:7] offset:1024\n" \
  "global_load_lds_dwordx4 v26, s[6:7] offset:2048\n" \
  "global_load_lds_dwordx4 v27, s[6:7] offset:3072\n" \
  "s_add_u32 s6, s6, 0x100000\n" \
  "s_addc_u32 s7, s7, 0\n" \
  "v_mfma_f32_16x16x32_bf16 v[64:67], v[128:131], v[128:131], 0\n" \
  "v_mfma_f32_16x16x32_bf16 v[68:71], v[192:195], v[192:195], 0\n" \
  "v_mfma_f32_16x16x32_bf16 v[64:67], v[132:135], v[132:135], v[64:67]\n" \
  "v_mfma_f32_16x16x32_bf16 v[68:71], v[196:199], v[196:199], v[68:71]\n" \
  "v_mfma_f32_16x16x32_bf16 v[64:67], v[136:139], v[136:139], v[64:67]\n" \
  "v_mfma_f32_16x16x32_bf16 v[68:71], v[200:203], v[200:203], v[68:71]\n" \
  "v_mfma_f32_16x16x32_bf16 v[64:67], v[140:143], v[140:143], v[64:67]\n" \
  "v_mfma_f32_16x16x32_bf16 v[68:71], v[204:207], v[204:207], v[68:71]\n" \
  "s_waitcnt vmcnt(8) lgkmcnt(0)\n" \
  "s_barrier\n" \
  "ds_read_b64_tr_b16 v[160:161], v18 offset:8192\n" \
  "ds_read_b64_tr_b16 v[162:163], v18 offset:8704\n" \
  "ds_read_b64_tr_b16 v[164:165], v18 offset:40960\n" \
  "ds_read_b64_tr_b16 v[166:167], v18 offset:41472\n" \
  "ds_read_b64_tr_b16 v[168:169], v19 offset:8192\n" \
  "ds_read_b64_tr_b16 v[170:171], v19 offset:8704\n" \
  "ds_read_b64_tr_b16 v[172:173], v19 offset:40960\n" \
  "ds_read_b64_tr_b16 v[174:175], v19 offset:41472\n" \
  "ds_read_b64_tr_b16 v[224:225], v28 offset:8192\n" \
  "ds_read_b64_tr_b16 v[226:227], v28 offset:8704\n" \
  "ds_read_b64_tr_b16 v[228:229], v28 offset:40960\n" \
  "ds_read_b64_tr_b16 v[230:231], v28 offset:41472\n" \
  "ds_read_b64_tr_b16 v[232:233], v17 offset:8192\n" \
  "ds_read_b64_tr_b16 v[234:235], v17 offset:8704\n" \
  "ds_read_b64_tr_b16 v[236:237], v17 offset:40960\n" \
  "ds_read_b64_tr_b16 v[238:239], v17 offset:41472\n" \
  "s_add_u32 m0, s19, 4096\n" \
  "s_nop 0\n" \
  "global_load_lds_dwordx4 v24, s[6:7] offset:0\n" \
  "global_load_lds_dwordx4 v25, s[6:7] offset:1024\n" \
  "global_load_lds_dwordx4 v26, s[6:7] offset:2048\n" \
  "global_load_lds_dwordx4 v27, s[6:7] offset:3072\n" \
  "s_add_u32 s6, s6, 0x100000\n" \
  "s_addc_u32 s7, s7, 0\n" \
  "v_mfma_f32_16x16x32_bf16 v[64:67], v[144:147], v[144:147], v[64:67]\n" \
  "v_mfma_f32_16x16x32_bf16 v[68:71], v[208:211], v[208:211], v[68:71]\n" \
  "v_mfma_f32_16x16x32_bf16 v[64:67], v[148:151], v[148:151], v[64:67]\n" \
  "v_mfma_f32_16x16x32_bf16 v[68:71], v[212:215], v[212:215], v[68:71]\n" \
  "v_mfma_f32_16x16x32_bf16 v[64:67], v[152:155], v[152:155], v[64:67]\n" \
  "v_mfma_f32_16x16x32_bf16 v[68:71], v[216:219], v[216:219], v[68:71]\n" \
  "v_mfma_f32_16x16x32_bf16 v[64:67], v[156:159], v[156:159], v[64:67]\n" \
  "v_mfma_f32_16x16x32_bf16 v[68:71], v[220:223], v[220:223], v[68:71]\n" \
  "s_waitcnt vmcnt(8) lgkmcnt(0)\n" \
  "s_barrier\n" \
  "ds_read_b64_tr_b16 v[176:177], v18 offset:12288\n" \
  "ds_read_b64_tr_b16 v[178:179], v18 offset:12800\n" \
  "ds_read_b64_tr_b16 v[180:181], v18 offset:45056\n" \
  "ds_read_b64_tr_b16 v[182:183], v18 offset:45568\n" \
  "ds_read_b64_tr_b16 v[184:185], v19 offset:12288\n" \
  "ds_read_b64_tr_b16 v[186:187], v19 offset:12800\n" \
  "ds_read_b64_tr_b16 v[188:189], v19 offset:45056\n" \
  "ds_read_b64_tr_b16 v[190:191], v19 offset:45568\n" \
  "ds_read_b64_tr_b16 v[240:241], v28 offset:12288\n" \
  "ds_read_b64_tr_b16 v[242:243], v28 offset:12800\n" \
  "ds_read_b64_tr_b16 v[244:245], v28 offset:45056\n" \
  "ds_read_b64_tr_b16 v[246:247], v28 offset:45568\n" \
  "ds_read_b64_tr_b16 v[248:249], v17 offset:12288\n" \
  "ds_read_b64_tr_b16 v[250:251], v17 offset:12800\n" \
  "ds_read_b64_tr_b16 v[252:253], v17 offset:45056\n" \
  "ds_read_b64_tr_b16 v[254:255], v17 offset:45568\n" \
  "s_add_u32 m0, s19, 8192\n" \
  "s_nop 0\n" \
  "global_load_lds_dwordx4 v24, s[6:7] offset:0\n" \
  "global_load_lds_dwordx4 v25, s[6:7] offset:1024\n" \
  "global_load_lds_dwordx4 v26, s[6:7] offset:2048\n" \
  "global_load_lds_dwordx4 v27, s[6:7] offset:3072\n" \
  "s_add_u32 s6, s6, 0x100000\n" \
  "s_addc_u32 s7, s7, 0\n" \
  "v_mfma_f32_16x16x32_bf16 v[64:67], v[160:163], v[160:163], v[64:67]\n" \
  "v_mfma_f32_16x16x32_bf16 v[68:71], v[224:227], v[224:227], v[68:71]\n" \
  "v_mfma_f32_16x16x32_bf16 v[64:67], v[164:167], v[164:167], v[64:67]\n" \
  "v_mfma_f32_16x16x32_bf16 v[68:71], v[228:231], v[228:231], v[68:71]\n" \
  "v_mfma_f32_16x16x32_bf16 v[64:67], v[168:171], v[168:171], v[64:67]\n" \
  "v_mfma_f32_16x16x32_bf16 v[68:71], v[232:235], v[232:235], v[68:71]\n" \
  "v_mfma_f32_16x16x32_bf16 v[64:67], v[172:175], v[172:175], v[64:67]\n" \
  "v_mfma_f32_16x16x32_bf16 v[68:71], v[236:239], v[236:239], v[68:71]\n" \
  "s_waitcnt vmcnt(8) lgkmcnt(0)\n" \
  "s_barrier\n" \
  "s_add_u32 m0, s19, 12288\n" \
  "s_nop 0\n" \
  "global_load_lds_dwordx4 v24, s[6:7] offset:0\n" \
  "global_load_lds_dwordx4 v25, s[6:7] offset:1024\n" \
  "global_load_lds_dwordx4 v26, s[6:7] offset:2048\n" \
  "global_load_lds_dwordx4 v27, s[6:7] offset:3072\n" \
  "s_add_u32 s6, s6, 0x100000\n" \
  "s_addc_u32 s7, s7, 0\n" \
  "v_mfma_f32_16x16x32_bf16 v[64:67], v[176:179], v[176:179], v[64:67]\n" \
  "v_mfma_f32_16x16x32_bf16 v[68:71], v[240:243], v[240:243], v[68:71]\n" \
  "v_mfma_f32_16x16x32_bf16 v[64:67], v[180:183], v[180:183], v[64:67]\n" \
  "v_mfma_f32_16x16x32_bf16 v[68:71], v[244:247], v[244:247], v[68:71]\n" \
  "v_mfma_f32_16x16x32_bf16 v[64:67], v[184:187], v[184:187], v[64:67]\n" \
  "v_mfma_f32_16x16x32_bf16 v[68:71], v[248:251], v[248:251], v[68:71]\n" \
  "v_mfma_f32_16x16x32_bf16 v[64:67], v[188:191], v[188:191], v[64:67]\n" \
  "v_mfma_f32_16x16x32_bf16 v[68:71], v[252:255], v[252:255], v[68:71]\n" \
  "s_nop 7\n" \
  "s_mov_b32 exec_lo, 0x100001\n" \
  "s_mov_b32 exec_hi, 0x10000100\n" \
  "v_add_f32 v12, v64, v68\n" \
  "s_mov_b32 exec_lo, 0x200002\n" \
  "s_mov_b32 exec_hi, 0x20000200\n" \
  "v_add_f32 v12, v65, v69\n" \
  "s_mov_b32 exec_lo, 0x400004\n" \
  "s_mov_b32 exec_hi, 0x40000400\n" \
  "v_add_f32 v12, v66, v70\n" \
  "s_mov_b32 exec_lo, 0x800008\n" \
  "s_mov_b32 exec_hi, 0x80000800\n" \
  "v_add_f32 v12, v67, v71\n" \
  "s_mov_b64 exec, -1\n" \
  "s_nop 1\n" \
  "v_add_f32_dpp v12, v12, v12 row_ror:1 row_mask:0xf bank_mask:0xf\n" \
  "s_nop 1\n" \
  "v_add_f32_dpp v12, v12, v12 row_ror:2 row_mask:0xf bank_mask:0xf\n" \
  "s_nop 1\n" \
  "v_add_f32_dpp v12, v12, v12 row_ror:4 row_mask:0xf bank_mask:0xf\n" \
  "s_nop 1\n" \
  "v_add_f32_dpp v12, v12, v12 row_ror:8 row_mask:0xf bank_mask:0xf\n" \
  "v_mov_b32 v31, v12\n" \
  "s_nop 1\n" \
  "v_permlane16_swap_b32 v12, v31\n" \
  "v_add_f32 v12, v12, v31\n" \
  "v_mov_b32 v31, v12\n" \
  "s_nop 1\n" \
  "v_permlane32_swap_b32 v12, v31\n" \
  "v_add_f32 v12, v12, v31\n" \
  "v_lshl_add_u32 v31, s73, 2, v30\n" \
  "s_mov_b64 exec, 1\n" \
  "ds_write_b32 v31, v12\n" \
  "s_mov_b64 exec, -1\n" \
  "s_waitcnt vmcnt(8) lgkmcnt(0)\n" \
  "s_barrier\n" \
  "s_add_u32 m0, s19, 0\n" \
  "s_nop 0\n" \
  "global_load_lds_dwordx4 v24, s[8:9] offset:0\n" \
  "global_load_lds_dwordx4 v25, s[8:9] offset:1024\n" \
  "global_load_lds_dwordx4 v26, s[8:9] offset:2048\n" \
  "global_load_lds_dwordx4 v27, s[8:9] offset:3072\n" \
  "s_add_u32 s8, s8, 0x100000\n" \
  "s_addc_u32 s9, s9, 0\n" \
  "ds_read_b128 v[20:23], v30 offset:16\n" \
  "ds_read_b32 v31, v30 offset:32\n" \
  "v_mbcnt_lo_u32_b32 v16, -1, 0\n" \
  "v_mbcnt_hi_u32_b32 v16, -1, v16\n" \
  "v_lshlrev_b32 v15, 8, v16\n" \
  "s_waitcnt lgkmcnt(0)\n" \
  "v_add_f32 v20, v20, v21\n" \
  "v_add_f32 v22, v22, v23\n" \
  "v_add_f32 v20, v20, v22\n" \
  "v_mul_f32 v31, v20, v31\n" \
  "v_mul_f32 v31, 0x3f8147ae, v31\n" \
  "v_mov_b32 v12, s10\n" \
  "v_mov_b32 v13, 0xffff\n" \
  "v_bfi_b32 v13, v13, s11, v31\n" \
  "s_mov_b32 exec_hi, 0\n" \
  "s_mov_b32 exec_lo, s18\n" \
  "global_store_dwordx2 v15, v[12:13], s[16:17] sc1\n" \
  "s_mov_b64 exec, -1\n" \
  "v_lshlrev_b32 v16, 3, v16\n" \
  "s_waitcnt vmcnt(8) lgkmcnt(0)\n" \
  "s_barrier\n" \
  "s_add_u32 m0, s19, 4096\n" \
  "s_nop 0\n" \
  "global_load_lds_dwordx4 v24, s[8:9] offset:0\n" \
  "global_load_lds_dwordx4 v25, s[8:9] offset:1024\n" \
  "global_load_lds_dwordx4 v26, s[8:9] offset:2048\n" \
  "global_load_lds_dwordx4 v27, s[8:9] offset:3072\n" \
  "s_add_u32 s8, s8, 0x100000\n" \
  "s_addc_u32 s9, s9, 0\n" \
  "global_load_dwordx2 v[20:21], v16, s[12:13] sc1\n" \
  "s_waitcnt vmcnt(9) lgkmcnt(0)\n" \
  "s_barrier\n" \
  "s_add_u32 m0, s19, 8192\n" \
  "s_nop 0\n" \
  "global_load_lds_dwordx4 v24, s[8:9] offset:0\n" \
  "global_load_lds_dwordx4 v25, s[8:9] offset:1024\n" \
  "global_load_lds_dwordx4 v26, s[8:9] offset:2048\n" \
  "global_load_lds_dwordx4 v27, s[8:9] offset:3072\n" \
  "s_add_u32 s8, s8, 0x100000\n" \
  "s_addc_u32 s9, s9, 0\n" \
  "s_waitcnt vmcnt(9) lgkmcnt(0)\n" \
  "s_barrier\n" \
  "ds_read_b64_tr_b16 v[64:65], v28 offset:0\n" \
  "ds_read_b64_tr_b16 v[66:67], v28 offset:512\n" \
  "ds_read_b64_tr_b16 v[68:69], v28 offset:32768\n" \
  "ds_read_b64_tr_b16 v[70:71], v28 offset:33280\n" \
  "ds_read_b64_tr_b16 v[72:73], v17 offset:0\n" \
  "ds_read_b64_tr_b16 v[74:75], v17 offset:512\n" \
  "ds_read_b64_tr_b16 v[76:77], v17 offset:32768\n" \
  "ds_read_b64_tr_b16 v[78:79], v17 offset:33280\n" \
  "s_add_u32 m0, s19, 12288\n" \
  "s_nop 0\n" \
  "global_load_lds_dwordx4 v24, s[8:9] offset:0\n" \
  "global_load_lds_dwordx4 v25, s[8:9] offset:1024\n" \
  "global_load_lds_dwordx4 v26, s[8:9] offset:2048\n" \
  "global_load_lds_dwordx4 v27, s[8:9] offset:3072\n" \
  "s_add_u32 s8, s8, 0x100000\n" \
  "s_addc_u32 s9, s9, 0\n" \
  "s_waitcnt vmcnt(8)\n" \
  ".LCV_RD0_%=:\n" \
  "v_cmp_ne_u32 vcc, s10, v20\n" \
  "v_xor_b32 v31, s11, v21\n" \
  "v_cmp_ne_u32 s[22:23], 0, v31\n" \
  "s_or_b64 vcc, vcc, s[22:23]\n" \
  "s_cbranch_scc0 .LCV_RD1_%=\n" \
  "s_sleep 8\n" \
  "global_load_dwordx2 v[20:21], v16, s[12:13] sc1\n" \
  "s_sub_u32 s87, s87, 1\n" \
  "s_cbranch_scc1 .LCV_END_%=\n" \
  "s_waitcnt vmcnt(0)\n" \
  "s_branch .LCV_RD0_%=\n" \
  ".LCV_RD1_%=:\n" \
  "s_waitcnt vmcnt(8) lgkmcnt(0)\n" \
  "s_barrier\n" \
  "ds_write_b128 v3, v[96:99] offset:0\n" \
  "ds_write_b128 v3, v[100:103] offset:1024\n" \
  "ds_write_b128 v3, v[104:107] offset:2048\n" \
  "ds_write_b128 v3, v[108:111] offset:3072\n" \
  "ds_read_b64_tr_b16 v[80:81], v28 offset:4096\n" \
  "ds_read_b64_tr_b16 v[82:83], v28 offset:4608\n" \
  "ds_read_b64_tr_b16 v[84:85], v28 offset:36864\n" \
  "ds_read_b64_tr_b16 v[86:87], v28 offset:37376\n" \
  "ds_read_b64_tr_b16 v[88:89], v17 offset:4096\n" \
  "ds_read_b64_tr_b16 v[90:91], v17 offset:4608\n" \
  "ds_read_b64_tr_b16 v[92:93], v17 offset:36864\n" \
  "ds_read_b64_tr_b16 v[94:95], v17 offset:37376\n" \
  "v_and_b32 v16, 0xff, v16\n" \
  "global_load_dwordx2 v[22:23], v16, s[14:15] sc1\n" \
  "s_mov_b64 exec, 15\n" \
  "buffer_load_dword v13, v2, s[44:47], s68 offen sc1\n" \
  "s_mov_b64 exec, -1\n" \
  "s_waitcnt vmcnt(6) lgkmcnt(0)\n" \
  "s_barrier\n" \
  "ds_write_b128 v3, v[112:115] offset:4096\n" \
  "ds_write_b128 v3, v[116:119] offset:5120\n" \
  "ds_write_b128 v3, v[120:123] offset:6144\n" \
  "ds_write_b128 v3, v[124:127] offset:7168\n" \
  "ds_read_b64_tr_b16 v[96:97], v28 offset:8192\n" \
  "ds_read_b64_tr_b16 v[98:99], v28 offset:8704\n" \
  "ds_read_b64_tr_b16 v[100:101], v28 offset:40960\n" \
  "ds_read_b64_tr_b16 v[102:103], v28 offset:41472\n" \
  "ds_read_b64_tr_b16 v[104:105], v17 offset:8192\n" \
  "ds_read_b64_tr_b16 v[106:107], v17 offset:8704\n" \
  "ds_read_b64_tr_b16 v[108:109], v17 offset:40960\n" \
  "ds_read_b64_tr_b16 v[110:111], v17 offset:41472\n" \
  "s_waitcnt vmcnt(0)\n" \
  "v_cmp_eq_u32 vcc, 0, v13\n" \
  "s_and_b32 s90, vcc_lo, 15\n" \
  "s_cbranch_scc1 .LCV_GL0_%=\n" \
  "s_mov_b32 m0, s99\n" \
  "s_add_u32 s88, s99, 8192\n" \
  "global_load_lds_dwordx4 v10, s[62:63] sc1\n" \
  "global_load_lds_dwordx4 v10, s[62:63] offset:1024 sc1\n" \
  "s_mov_b32 m0, s88\n" \
  "s_nop 0\n" \
  "global_load_lds_dwordx4 v10, s[64:65] sc1\n" \
  "global_load_lds_dwordx4 v10, s[64:65] offset:1024 sc1\n" \
  ".LCV_GL0_%=:\n" \
  "v_cmp_ne_u32 vcc, s10, v22\n" \
  "v_xor_b32 v31, s11, v23\n" \
  "v_and_b32 v31, 0xffff, v31\n" \
  "v_cmp_ne_u32 s[22:23], 0, v31\n" \
  "s_or_b64 vcc, vcc, s[22:23]\n" \
  "s_cbranch_scc0 .LCV_GL1_%=\n" \
  "s_sleep 12\n" \
  "global_load_dwordx2 v[22:23], v16, s[14:15] sc1\n" \
  "s_sub_u32 s87, s87, 1\n" \
  "s_cbranch_scc1 .LCV_END_%=\n" \
  "s_waitcnt vmcnt(0)\n" \
  "s_branch .LCV_GL0_%=\n" \
  ".LCV_GL1_%=:\n" \
  "v_and_b32 v31, 0xffff0000, v23\n" \
  "s_nop 1\n" \
  "v_max_u32_dpp v31, v31, v31 row_ror:1 row_mask:0xf bank_mask:0xf\n" \
  "s_nop 1\n" \
  "v_max_u32_dpp v31, v31, v31 row_ror:2 row_mask:0xf bank_mask:0xf\n" \
  "s_nop 1\n" \
  "v_max_u32_dpp v31, v31, v31 row_ror:4 row_mask:0xf bank_mask:0xf\n" \
  "s_nop 1\n" \
  "v_max_u32_dpp v31, v31, v31 row_ror:8 row_mask:0xf bank_mask:0xf\n" \
  "v_mov_b32 v12, v31\n" \
  "s_nop 1\n" \
  "v_permlane16_swap_b32 v31, v12\n" \
  "v_max_u32 v31, v31, v12\n" \
  "v_mov_b32 v12, v31\n" \
  "s_nop 1\n" \
  "v_permlane32_swap_b32 v31, v12\n" \
  "v_max_u32 v31, v31, v12\n" \
  "s_mov_b64 exec, 1\n" \
  "ds_write_b32 v30, v31\n" \
  "s_mov_b64 exec, -1\n" \
  "s_waitcnt vmcnt(4) lgkmcnt(0)\n" \
  "s_barrier\n" \
  "ds_write_b128 v3, v[32:35] offset:8192\n" \
  "ds_write_b128 v3, v[36:39] offset:9216\n" \
  "ds_write_b128 v3, v[40:43] offset:10240\n" \
  "ds_write_b128 v3, v[44:47] offset:11264\n" \
  "ds_read_b64_tr_b16 v[112:113], v28 offset:12288\n" \
  "ds_read_b64_tr_b16 v[114:115], v28 offset:12800\n" \
  "ds_read_b64_tr_b16 v[116:117], v28 offset:45056\n" \
  "ds_read_b64_tr_b16 v[118:119], v28 offset:45568\n" \
  "ds_read_b64_tr_b16 v[120:121], v17 offset:12288\n" \
  "ds_read_b64_tr_b16 v[122:123], v17 offset:12800\n" \
  "ds_read_b64_tr_b16 v[124:125], v17 offset:45056\n" \
  "ds_read_b64_tr_b16 v[126:127], v17 offset:45568\n" \
  "ds_read_b32 v31, v30 offset:0\n" \
  "s_waitcnt lgkmcnt(0)\n" \
  "s_barrier\n" \
  "ds_write_b128 v3, v[48:51] offset:12288\n" \
  "ds_write_b128 v3, v[52:55] offset:13312\n" \
  "ds_write_b128 v3, v[56:59] offset:14336\n" \
  "ds_write_b128 v3, v[60:63] offset:15360\n" \
  "v_mul_f32 v31, 0x3f8e353f, v31\n" \
  "v_mul_f32 v30, 0x44870000, v31\n" \
  "v_mov_b64 v[12:13], 0\n" \
  "v_mov_b64 v[14:15], 0\n" \
  "v_mov_b64 v[16:17], 0\n" \
  "v_mov_b64 v[18:19], 0\n" \
  "v_mov_b64 v[20:21], 0\n" \
  "v_mov_b64 v[22:23], 0\n" \
  "v_readfirstlane_b32 s72, v31\n" \
  "v_readfirstlane_b32 s86, v30\n" \
  "s_lshr_b32 s86, s86, 23\n" \
  "s_addk_i32 s86, 0xff83\n" \
  "s_ashr_i32 s86, s86, 1\n" \
  "s_max_i32 s86, s86, -20\n" \
  "s_min_i32 s86, s86, 100\n" \
  "s_mov_b32 s82, s86\n" \
  "s_lshr_b32 s83, s72, 23\n" \
  "s_sub_i32 s83, s83, 243\n" \
  "s_waitcnt lgkmcnt(0)\n" \
  "s_add_u32 s88, s74, 157760\n" \
  "v_mov_b32 v59, s88\n" \
  "s_cmp_lg_u32 s61, 0\n" \
  "s_movk_i32 s76, -6976\n" \
  "s_cmovk_i32 s76, 8128\n" \
  "s_movk_i32 s77, 90\n" \
  "s_cmovk_i32 s77, 90\n" \
  "s_cselect_b32 s72, -1, 56\n" \
  "s_cmp_eq_u32 s61, 0\n" \
  "s_cbranch_scc1 .LCV_W7_%=\n" \
  "s_movk_i32 s87, 100\n" \
  ".LCV_W5_%=:\n" \
  "buffer_load_dword v58, off, s[0:3], 0 offset:896 sc1\n" \
  "s_waitcnt vmcnt(0)\n" \
  "v_readfirstlane_b32 vcc_lo, v58\n" \
  ".LCV_W6_%=:\n" \
  "s_sleep 16\n" \
  "s_sub_u32 s87, s87, 1\n" \
  "s_cbranch_scc1 .LCV_W7_%=\n" \
  "s_cmp_eq_u32 vcc_lo, 0\n" \
  "s_cbranch_scc1 .LCV_W5_%=\n" \
  "s_memrealtime s[38:39]\n" \
  "s_waitcnt lgkmcnt(0)\n" \
  "s_sub_u32 s38, s38, vcc_lo\n" \
  "s_cmp_lt_i32 s38, 800\n" \
  "s_cbranch_scc1 .LCV_W6_%=\n" \
  ".LCV_W7_%=:\n" \
  "s_mov_b32 s87, 0x40000\n" \
  "s_cmp_eq_u32 s90, 0\n" \
  "s_cbranch_scc1 .LCV_W1_%=\n" \
  "s_branch .LCV_W2_%=\n" \
  ".LCV_W0_%=:\n" \
  "s_sleep 2\n" \
  "s_cmp_eq_u32 s61, 0\n" \
  "s_cbranch_scc1 .LCV_W2_%=\n" \
  "s_sleep 8\n" \
  ".LCV_W2_%=:\n" \
  "s_mov_b64 exec, 15\n" \
  "buffer_load_dword v58, v2, s[44:47], s68 offen sc1\n" \
  "s_mov_b64 exec, -1\n" \
  "s_sub_u32 s87, s87, 1\n" \
  "s_cbranch_scc1 .LCV_END_%=\n" \
  "s_waitcnt vmcnt(0)\n" \
  "v_cmp_eq_u32 vcc, 0, v58\n" \
  "s_and_b32 vcc_lo, vcc_lo, 15\n" \
  "s_cbranch_scc1 .LCV_W0_%=\n" \
  "s_mov_b32 m0, s99\n" \
  "s_add_u32 s88, s99, 8192\n" \
  "global_load_lds_dwordx4 v10, s[62:63] sc1\n" \
  "global_load_lds_dwordx4 v10, s[62:63] offset:1024 sc1\n" \
  "s_mov_b32 m0, s88\n" \
  "s_nop 0\n" \
  "global_load_lds_dwordx4 v10, s[64:65] sc1\n" \
  "global_load_lds_dwordx4 v10, s[64:65] offset:1024 sc1\n" \
  ".LCV_W1_%=:\n" \
  "s_add_u32 s62, s62, 0x80000\n" \
  "s_addc_u32 s63, s63, 0\n" \
  "s_add_u32 s64, s64, 0x40000\n" \
  "s_addc_u32 s65, s65, 0\n" \
  "s_add_u32 s68, s68, 0x1000\n" \
  "s_cmp_eq_u32 s61, 0\n" \
  "s_cbranch_scc1 .LCV_W4_%=\n" \
  "s_add_u32 s38, s67, 3968\n" \
  "v_mov_b32 v58, s38\n" \
  "buffer_load_dword v58, v58, s[0:3], 0 offen sc1\n" \
  "s_waitcnt vmcnt(0)\n" \
  "v_readfirstlane_b32 vcc_lo, v58\n" \
  "s_movk_i32 s87, 200\n" \
  "s_cmp_eq_u32 vcc_lo, 0\n" \
  "s_cbranch_scc1 .LCV_W4_%=\n" \
  "s_add_u32 vcc_lo, vcc_lo, 590\n" \
  ".LCV_W3_%=:\n" \
  "s_sleep 2\n" \
  "s_memrealtime s[38:39]\n" \
  "s_sub_u32 s87, s87, 1\n" \
  "s_cbranch_scc1 .LCV_W4_%=\n" \
  "s_waitcnt lgkmcnt(0)\n" \
  "s_sub_u32 s38, s38, vcc_lo\n" \
  "s_cmp_lt_i32 s38, 0\n" \
  "s_cbranch_scc1 .LCV_W3_%=\n" \
  ".LCV_W4_%=:\n" \
  "s_waitcnt vmcnt(0)\n" \
  ".p2align 6\n" \
  ".LCV_TOP_%=:\n" \
  "s_waitcnt lgkmcnt(0)\n" \
  "s_barrier\n" \
  "s_setprio 3\n" \
  "ds_read_b128 v[24:27], v1 offset:0\n" \
  "ds_read_b128 v[28:31], v1 offset:1024\n" \
  "ds_read_b128 v[32:35], v1 offset:2048\n" \
  "ds_read_b128 v[36:39], v1 offset:3072\n" \
  "ds_read_b128 v[40:43], v1 offset:4096\n" \
  "ds_read_b128 v[44:47], v1 offset:5120\n" \
  "ds_read_b128 v[48:51], v1 offset:6144\n" \
  "ds_read_b128 v[52:55], v1 offset:7168\n" \
  "s_memrealtime s[38:39]\n" \
  "s_waitcnt lgkmcnt(7)\n" \
  "v_smfmac_f32_16x16x64_bf16 v[12:15], v[24:27], v[64:71], v0\n" \
  "s_waitcnt lgkmcnt(6)\n" \
  "v_smfmac_f32_16x16x64_bf16 v[12:15], v[28:31], v[72:79], v0\n" \
  "s_waitcnt lgkmcnt(5)\n" \
  "v_smfmac_f32_16x16x64_bf16 v[12:15], v[32:35], v[80:87], v0\n" \
  "s_waitcnt lgkmcnt(4)\n" \
  "v_smfmac_f32_16x16x64_bf16 v[12:15], v[36:39], v[88:95], v0\n" \
  "s_waitcnt lgkmcnt(3)\n" \
  "v_smfmac_f32_16x16x64_bf16 v[12:15], v[40:43], v[96:103], v0\n" \
  "s_waitcnt lgkmcnt(2)\n" \
  "v_smfmac_f32_16x16x64_bf16 v[12:15], v[44:47], v[104:111], v0\n" \
  "s_waitcnt lgkmcnt(1)\n" \
  "v_smfmac_f32_16x16x64_bf16 v[12:15], v[48:51], v[112:119], v0\n" \
  "s_waitcnt lgkmcnt(0)\n" \
  "v_smfmac_f32_16x16x64_bf16 v[12:15], v[52:55], v[120:127], v0\n" \
  "ds_read_b32 v11, v1 offset:26688\n" \
  "s_mov_b32 s61, s38\n" \
  "s_add_u32 s38, s38, s79\n" \
  "s_add_u32 s38, s38, s96\n" \
  "s_mov_b32 s96, 0\n" \
  "v_or_b32 v57, 1, s38\n" \
  "v_add_u32 v56, s67, v10\n" \
  "s_add_u32 s39, s67, s76\n" \
  "s_add_u32 s38, s38, s77\n" \
  "s_bitset1_b32 s38, 0\n" \
  "v_writelane_b32 v56, s39, 1\n" \
  "v_writelane_b32 v57, s38, 1\n" \
  "s_sub_u32 s39, s71, s72\n" \
  "s_cmp_lt_u32 s39, 8\n" \
  "s_cselect_b64 exec, 19, 3\n" \
  "buffer_store_dword v57, v56, s[0:3], 0 offen sc1\n" \
  "s_mov_b64 exec, -1\n" \
  "v_permlane32_swap_b32 v12, v14\n" \
  "v_permlane32_swap_b32 v13, v15\n" \
  "v_pk_add_f32 v[12:13], v[12:13], v[14:15]\n" \
  "s_nop 0\n" \
  "v_cvt_pk_bf16_f32 v12, v12, v13\n" \
  "ds_write_b32 v4, v12\n" \
  "s_setprio 2\n" \
  "v_mov_b64 v[12:13], s[92:93]\n" \
  "v_mov_b64 v[14:15], s[92:93]\n" \
  "s_waitcnt lgkmcnt(1)\n" \
  "v_readfirstlane_b32 s86, v11\n" \
  "s_lshr_b32 s86, s86, 23\n" \
  "s_add_i32 s86, s86, s83\n" \
  "s_ashr_i32 s86, s86, 1\n" \
  "s_max_i32 s86, s86, s82\n" \
  "s_min_i32 s86, s86, 100\n" \
  "s_lshl_b32 s86, s86, 23\n" \
  "s_sub_i32 s84, 0xc8800000, s86\n" \
  "s_cmp_eq_u32 s73, 5\n" \
  "v_smfmac_f32_16x16x64_bf16 v[16:19], v[24:27], v[128:135], v0\n" \
  "v_smfmac_f32_16x16x64_bf16 v[16:19], v[28:31], v[136:143], v0\n" \
  "v_smfmac_f32_16x16x64_bf16 v[16:19], v[32:35], v[144:151], v0\n" \
  "v_smfmac_f32_16x16x64_bf16 v[16:19], v[36:39], v[152:159], v0\n" \
  "v_smfmac_f32_16x16x64_bf16 v[16:19], v[40:43], v[160:167], v0\n" \
  "v_smfmac_f32_16x16x64_bf16 v[16:19], v[44:47], v[168:175], v0\n" \
  "s_waitcnt lgkmcnt(0)\n" \
  "s_barrier\n" \
  "ds_read_b64_tr_b16 v[60:61], v6 offset:0\n" \
  "ds_read_b64_tr_b16 v[62:63], v9 offset:0\n" \
  "v_smfmac_f32_16x16x64_bf16 v[16:19], v[48:51], v[176:183], v0\n" \
  "v_smfmac_f32_16x16x64_bf16 v[16:19], v[52:55], v[184:191], v0\n" \
  "v_smfmac_f32_16x16x64_bf16 v[20:23], v[24:27], v[192:199], v0\n" \
  "v_smfmac_f32_16x16x64_bf16 v[20:23], v[28:31], v[200:207], v0\n" \
  "v_smfmac_f32_16x16x64_bf16 v[20:23], v[32:35], v[208:215], v0\n" \
  "v_smfmac_f32_16x16x64_bf16 v[20:23], v[36:39], v[216:223], v0\n" \
  "v_smfmac_f32_16x16x64_bf16 v[20:23], v[40:43], v[224:231], v0\n" \
  "v_smfmac_f32_16x16x64_bf16 v[20:23], v[44:47], v[232:239], v0\n" \
  "v_smfmac_f32_16x16x64_bf16 v[20:23], v[48:51], v[240:247], v0\n" \
  "v_smfmac_f32_16x16x64_bf16 v[20:23], v[52:55], v[248:255], v0\n" \
  "s_cmp_eq_u32 s71, 0\n" \
  "v_permlane32_swap_b32 v16, v18\n" \
  "v_permlane32_swap_b32 v17, v19\n" \
  "v_add_f32 v16, v16, v18\n" \
  "v_add_f32 v17, v17, v19\n" \
  "v_cvt_pk_bf16_f32 v16, v16, v17\n" \
  "s_waitcnt lgkmcnt(0)\n" \
  "v_max_u32 v60, 1, v60\n" \
  "v_max_u32 v61, 1, v61\n" \
  "v_max_u32 v62, 1, v62\n" \
  "v_max_u32 v63, 1, v63\n" \
  "buffer_store_dwordx4 v[60:63], v7, s[48:51], s69 offen sc1\n" \
  "v_permlane32_swap_b32 v20, v22\n" \
  "v_permlane32_swap_b32 v21, v23\n" \
  "v_pk_add_f32 v[20:21], v[20:21], v[22:23]\n" \
  "v_max_u32 v18, 1, v16\n" \
  "v_cvt_pk_bf16_f32 v17, v20, v21\n" \
  "ds_write_b64 v5, v[16:17]\n" \
  "v_max_u32 v19, 1, v17\n" \
  "s_nop 1\n" \
  "v_permlane32_swap_b32 v18, v19\n" \
  "buffer_store_dwordx2 v[18:19], v8, s[52:55], s70 offen sc1\n" \
  "ds_read_b128 v[24:27], v3 offset:0\n" \
  "ds_read_b128 v[28:31], v3 offset:1024\n" \
  "ds_read_b128 v[32:35], v3 offset:2048\n" \
  "ds_read_b128 v[36:39], v3 offset:3072\n" \
  "ds_read_b128 v[40:43], v3 offset:8192\n" \
  "ds_read_b128 v[44:47], v3 offset:9216\n" \
  "ds_read_b128 v[48:51], v3 offset:10240\n" \
  "ds_read_b128 v[52:55], v3 offset:11264\n" \
  "s_cbranch_scc1 .LCV_LAST_%=\n" \
  "s_cmp_eq_u32 s73, 5\n" \
  "s_cbranch_scc0 .LCV_NMK_%=\n" \
  "s_mov_b32 m0, s78\n" \
  "s_nop 0\n" \
  "buffer_load_dword v2, s[0:3], s66 offen sc1 lds\n" \
  ".LCV_NMK_%=:\n" \
  "s_setprio 0\n" \
  "s_waitcnt lgkmcnt(8)\n" \
  "s_barrier\n" \
  "s_add_u32 s67, s67, 1024\n" \
  "v_mov_b32 v11, 0\n" \
  "ds_write_b32 v59, v11\n" \
  "ds_read_b128 v[56:59], v3 offset:4096\n" \
  "ds_read_b128 v[60:63], v3 offset:5120\n" \
  "v_mov_b64 v[20:21], s[92:93]\n" \
  "v_mov_b64 v[22:23], s[92:93]\n" \
  "v_mov_b64 v[16:17], s[92:93]\n" \
  "v_mov_b64 v[18:19], s[92:93]\n" \
  "s_mov_b32 m0, s94\n" \
  "s_mov_b64 exec, 15\n" \
  "buffer_load_dword v2, s[44:47], s68 offen sc1 lds\n" \
  "s_mov_b64 exec, -1\n" \
  "s_setprio 2\n" \
  "s_waitcnt lgkmcnt(0)\n" \
  "s_barrier\n" \
  "ds_read_b128 v[4:7], v1 offset:8192\n" \
  "ds_read_b128 v[8:11], v1 offset:9216\n" \
  "s_waitcnt lgkmcnt(1)\n" \
  "v_smfmac_f32_16x16x64_bf16 v[12:15], v[4:7], v[24:31], v0\n" \
  "ds_read_b128 v[24:27], v3 offset:6144\n" \
  "ds_read_b128 v[28:31], v3 offset:7168\n" \
  "v_smfmac_f32_16x16x64_bf16 v[16:19], v[4:7], v[32:39], v0\n" \
  "ds_read_b128 v[32:35], v3 offset:12288\n" \
  "ds_read_b128 v[36:39], v3 offset:13312\n" \
  "s_waitcnt lgkmcnt(4)\n" \
  "v_smfmac_f32_16x16x64_bf16 v[12:15], v[8:11], v[40:47], v0\n" \
  "v_smfmac_f32_16x16x64_bf16 v[16:19], v[8:11], v[48:55], v0\n" \
  "v_smfmac_f32_16x16x64_bf16 v[20:23], v[4:7], v[56:63], v0\n" \
  "ds_read_b128 v[56:59], v3 offset:14336\n" \
  "ds_read_b128 v[60:63], v3 offset:15360\n" \
  "v_mov_b64 v[40:41], s[92:93]\n" \
  "v_mov_b64 v[42:43], s[92:93]\n" \
  "s_nop 2\n" \
  "v_permlane32_swap_b32 v12, v17\n" \
  "v_permlane32_swap_b32 v13, v16\n" \
  "v_permlane32_swap_b32 v14, v19\n" \
  "v_permlane32_swap_b32 v15, v18\n" \
  "v_pk_add_f32 v[12:13], v[12:13], v[16:17] op_sel:[0,1] op_sel_hi:[1,0]\n" \
  "v_pk_add_f32 v[14:15], v[14:15], v[18:19] op_sel:[0,1] op_sel_hi:[1,0]\n" \
  "buffer_atomic_add_x2 v[12:13], v2, s[40:43], s56 offen\n" \
  "buffer_atomic_add_x2 v[14:15], v2, s[40:43], s57 offen\n" \
  "s_waitcnt lgkmcnt(2)\n" \
  "v_smfmac_f32_16x16x64_bf16 v[20:23], v[8:11], v[32:39], v0\n" \
  "v_smfmac_f32_16x16x64_bf16 v[40:43], v[4:7], v[24:31], v0\n" \
  "s_waitcnt lgkmcnt(0)\n" \
  "v_smfmac_f32_16x16x64_bf16 v[40:43], v[8:11], v[56:63], v0\n" \
  "s_nop 7\n" \
  "v_permlane32_swap_b32 v20, v41\n" \
  "v_permlane32_swap_b32 v21, v40\n" \
  "v_permlane32_swap_b32 v22, v43\n" \
  "v_permlane32_swap_b32 v23, v42\n" \
  "v_pk_add_f32 v[20:21], v[20:21], v[40:41] op_sel:[0,1] op_sel_hi:[1,0]\n" \
  "v_pk_add_f32 v[22:23], v[22:23], v[42:43] op_sel:[0,1] op_sel_hi:[1,0]\n" \
  "buffer_atomic_add_x2 v[20:21], v2, s[40:43], s58 offen\n" \
  "buffer_atomic_add_x2 v[22:23], v2, s[40:43], s59 offen\n" \
  "s_setprio 0\n" \
  "s_memrealtime s[38:39]\n" \
  "v_mbcnt_lo_u32_b32 v48, -1, 0\n" \
  "v_mbcnt_hi_u32_b32 v48, -1, v48\n" \
  "s_add_u32 s88, s74, 157760\n" \
  "v_mov_b32 v59, s88\n" \
  "v_lshl_add_u32 v52, v48, 2, v59\n" \
  "ds_read_b32 v52, v52 offset:192\n" \
  "v_lshrrev_b32 v49, 3, v48\n" \
  "v_and_b32 v50, 7, v48\n" \
  "v_lshlrev_b32 v50, 4, v50\n" \
  "v_lshl_or_b32 v44, v49, 8, v50\n" \
  "s_mov_b32 s87, 0x40000\n" \
  "v_lshlrev_b32 v10, 4, v48\n" \
  "v_lshl_add_u32 v58, v48, 2, s94\n" \
  "s_waitcnt vmcnt(4)\n" \
  "ds_read_b32 v58, v58\n" \
  "s_waitcnt lgkmcnt(0)\n" \
  "v_cmp_eq_u32 vcc, 0, v58\n" \
  "s_and_b32 vcc_lo, vcc_lo, 15\n" \
  "s_cbranch_scc0 .LCV_GOT_%=\n" \
  "s_branch .LCV_ASK_%=\n" \
  ".LCV_LATE_%=:\n" \
  "v_cmp_eq_u32 vcc, 0, v58\n" \
  "s_and_b32 vcc_lo, vcc_lo, 15\n" \
  "s_cbranch_scc0 .LCV_GOT_%=\n" \
  "s_sleep 12\n" \
  ".LCV_ASK_%=:\n" \
  "s_mov_b64 exec, 15\n" \
  "buffer_load_dword v58, v2, s[44:47], s68 offen sc1\n" \
  "s_mov_b64 exec, -1\n" \
  "s_sub_u32 s87, s87, 1\n" \
  "s_cbranch_scc1 .LCV_END_%=\n" \
  "s_waitcnt vmcnt(0)\n" \
  "s_branch .LCV_LATE_%=\n" \
  ".LCV_GOT_%=:\n" \
  "s_mov_b32 m0, s99\n" \
  "s_add_u32 s88, s99, 8192\n" \
  "global_load_lds_dwordx4 v10, s[62:63] sc1\n" \
  "global_load_lds_dwordx4 v10, s[62:63] offset:1024 sc1\n" \
  "s_mov_b32 m0, s88\n" \
  "s_nop 0\n" \
  "global_load_lds_dwordx4 v10, s[64:65] sc1\n" \
  "global_load_lds_dwordx4 v10, s[64:65] offset:1024 sc1\n" \
  ".LCV_HAVE_%=:\n" \
  "s_waitcnt lgkmcnt(0)\n" \
  "s_sub_u32 s61, s38, s61\n" \
  "s_add_u32 s79, s79, 2\n" \
  "s_min_u32 s79, s79, s61\n" \
  "s_sub_u32 s39, s38, 150\n" \
  "v_subrev_u32 v52, s39, v52\n" \
  "v_cmp_lt_u32 vcc, 0x1000, v52\n" \
  "s_cmp_eq_u64 vcc, 0\n" \
  "s_cbranch_scc1 .LCV_OKM_%=\n" \
  ".LCV_AGN_%=:\n" \
  "buffer_load_dword v52, v2, s[0:3], s66 offen sc1\n" \
  "s_sub_u32 s87, s87, 1\n" \
  "s_cbranch_scc1 .LCV_END_%=\n" \
  "s_waitcnt vmcnt(0)\n" \
  "v_cmp_eq_u32 vcc, 0, v52\n" \
  "v_subrev_u32 v52, s39, v52\n" \
  "s_cmp_eq_u64 vcc, 0\n" \
  "s_cbranch_scc1 .LCV_OKM_%=\n" \
  "s_sleep 4\n" \
  "s_branch .LCV_AGN_%=\n" \
  ".LCV_OKM_%=:\n" \
  "s_nop 1\n" \
  "v_max_i32_dpp v52, v52, v52 row_ror:1 row_mask:0xf bank_mask:0xf\n" \
  "s_nop 1\n" \
  "v_max_i32_dpp v52, v52, v52 row_ror:2 row_mask:0xf bank_mask:0xf\n" \
  "s_nop 1\n" \
  "v_max_i32_dpp v52, v52, v52 row_ror:4 row_mask:0xf bank_mask:0xf\n" \
  "s_nop 1\n" \
  "v_max_i32_dpp v52, v52, v52 row_ror:8 row_mask:0xf bank_mask:0xf\n" \
  "s_movk_i32 vcc_lo, 150\n" \
  "v_readlane_b32 s88, v52, 0\n" \
  "s_max_i32 vcc_lo, vcc_lo, s88\n" \
  "v_readlane_b32 s88, v52, 16\n" \
  "s_max_i32 vcc_lo, vcc_lo, s88\n" \
  "v_readlane_b32 s88, v52, 32\n" \
  "s_max_i32 vcc_lo, vcc_lo, s88\n" \
  "v_readlane_b32 s88, v52, 48\n" \
  "s_max_i32 vcc_lo, vcc_lo, s88\n" \
  "s_add_u32 s66, s66, 1024\n" \
  "s_add_u32 vcc_lo, vcc_lo, s39\n" \
  "s_add_u32 vcc_lo, vcc_lo, 23\n" \
  ".LCV_TW_%=:\n" \
  "s_memrealtime s[38:39]\n" \
  "s_waitcnt lgkmcnt(0)\n" \
  "s_sub_u32 s38, s38, vcc_lo\n" \
  "s_cmp_lt_i32 s38, 0\n" \
  "s_cbranch_scc1 .LCV_TW_%=\n" \
  "buffer_load_dwordx4 v[24:27], v44, s[40:43], s60 offen sc1\n" \
  "buffer_load_dwordx4 v[28:31], v44, s[40:43], s60 offen offset:2048 sc1\n" \
  "s_add_u32 s62, s62, 0x80000\n" \
  "s_addc_u32 s63, s63, 0\n" \
  "s_add_u32 s64, s64, 0x40000\n" \
  "s_addc_u32 s65, s65, 0\n" \
  "s_add_u32 s68, s68, 0x1000\n" \
  "s_add_u32 s69, s69, 0x10000\n" \
  "s_add_u32 s70, s70, 0x10000\n" \
  "s_sub_u32 s88, s73, 4\n" \
  "v_lshrrev_b32 v49, 2, v48\n" \
  "v_lshrrev_b32 v50, 4, v48\n" \
  "v_bitop3_b32 v50, v49, v50, 3 bitop3:0x28\n" \
  "v_lshl_add_u32 v4, v50, 3, s97\n" \
  "v_and_b32 v50, 3, v48\n" \
  "v_lshl_add_u32 v4, v50, 5, v4\n" \
  "v_and_b32 v49, 1, v49\n" \
  "v_lshl_add_u32 v4, v49, 8, v4\n" \
  "v_and_b32 v49, 8, v48\n" \
  "v_lshl_add_u32 v4, v49, 4, v4\n" \
  "v_and_b32 v51, 15, v48\n" \
  "v_bfe_u32 v49, v48, 4, 1\n" \
  "v_lshrrev_b32 v50, 5, v48\n" \
  "s_lshl_b32 s89, s88, 10\n" \
  "s_add_u32 s89, s89, s74\n" \
  "s_add_u32 s89, s89, 158720\n" \
  "v_lshrrev_b32 v9, 3, v51\n" \
  "v_mul_u32_u24 v6, 41, v9\n" \
  "v_xor_b32 v6, v6, v51\n" \
  "v_lshl_add_u32 v6, v6, 3, s89\n" \
  "v_lshl_add_u32 v6, v49, 6, v6\n" \
  "v_lshl_add_u32 v6, v50, 9, v6\n" \
  "v_xor_b32 v9, 0x90, v6\n" \
  "v_lshlrev_b32 v8, 4, v49\n" \
  "v_lshl_add_u32 v8, v51, 5, v8\n" \
  "v_lshl_add_u32 v8, v50, 3, v8\n" \
  "s_lshl_b32 s89, s88, 8\n" \
  "s_add_u32 s89, s89, s74\n" \
  "s_add_u32 s89, s89, 139264\n" \
  "v_and_b32 v49, 31, v48\n" \
  "v_lshl_add_u32 v5, v49, 3, s89\n" \
  "v_lshl_add_u32 v5, v50, 10, v5\n" \
  "v_lshrrev_b32 v49, 2, v51\n" \
  "v_and_b32 v50, 5, v51\n" \
  "v_and_or_b32 v49, v49, 2, v50\n" \
  "v_and_b32 v50, 48, v48\n" \
  "v_lshlrev_b32 v7, 3, v50\n" \
  "v_lshl_add_u32 v7, v49, 4, v7\n" \
  "v_bitop3_b32 v49, v51, 2, 2 bitop3:0x0c\n" \
  "v_lshl_add_u32 v7, v49, 29, v7\n" \
  "v_mov_b64 v[12:13], 0\n" \
  "v_mov_b64 v[14:15], 0\n" \
  "v_mov_b64 v[16:17], 0\n" \
  "v_mov_b64 v[18:19], 0\n" \
  "v_mov_b64 v[20:21], 0\n" \
  "v_mov_b64 v[22:23], 0\n" \
  "v_and_b32 v50, 3, v48\n" \
  "v_lshl_add_u32 v40, v50, 2, s98\n" \
  "v_lshrrev_b32 v50, 4, v48\n" \
  "v_mad_u32_u24 v40, v50, s95, v40\n" \
  "v_bfe_u32 v49, v48, 3, 1\n" \
  "v_lshl_add_u32 v40, v49, 6, v40\n" \
  "v_bfe_u32 v49, v48, 2, 1\n" \
  "v_lshl_add_u32 v40, v49, 7, v40\n" \
  "v_xor_b32 v41, 16, v40\n" \
  "v_xor_b32 v42, 32, v40\n" \
  "v_xor_b32 v43, 48, v40\n" \
  "s_add_i32 s80, s86, 0x35000000\n" \
  "v_mov_b32 v11, 0\n" \
  "s_mov_b32 s89, s60\n" \
  "s_bitcmp1_b32 s60, 7\n" \
  "s_cselect_b32 s88, 0x10000, 0\n" \
  "s_bitset1_b32 s88, 7\n" \
  "s_xor_b32 s56, s56, s88\n" \
  "s_xor_b32 s57, s57, s88\n" \
  "s_xor_b32 s58, s58, s88\n" \
  "s_xor_b32 s59, s59, s88\n" \
  "s_xor_b32 s60, s60, s88\n" \
  "s_sub_u32 s71, s71, 1\n" \
  ".LCV_SEE_%=:\n" \
  "s_waitcnt vmcnt(1)\n" \
  "v_cvt_f32_i32 v24, v24\n" \
  "v_cvt_f32_i32 v25, v25\n" \
  "v_cvt_f32_i32 v26, v26\n" \
  "v_cvt_f32_i32 v27, v27\n" \
  "v_max_f32 v45, |v24|, |v26|\n" \
  "v_pk_mul_f32 v[24:25], v[24:25], s[80:81] op_sel_hi:[1,0]\n" \
  "v_pk_mul_f32 v[26:27], v[26:27], s[80:81] op_sel_hi:[1,0]\n" \
  "s_nop 0\n" \
  "v_cvt_pk_bf16_f32 v32, v24, v26\n" \
  "v_cvt_pk_bf16_f32 v33, v25, v27\n" \
  "ds_write_b32 v40, v32\n" \
  "ds_write_b32 v41, v33\n" \
  "s_waitcnt vmcnt(0)\n" \
  "v_cvt_f32_i32 v28, v28\n" \
  "v_cvt_f32_i32 v30, v30\n" \
  "v_cvt_f32_i32 v29, v29\n" \
  "v_max3_f32 v45, v45, |v28|, |v30|\n" \
  "v_cvt_f32_i32 v31, v31\n" \
  "v_cmp_lt_f32 vcc, s91, v45\n" \
  "v_pk_mul_f32 v[28:29], v[28:29], s[80:81] op_sel_hi:[1,0]\n" \
  "v_pk_mul_f32 v[30:31], v[30:31], s[80:81] op_sel_hi:[1,0]\n" \
  "s_cmp_eq_u64 vcc, 0\n" \
  "s_cbranch_scc0 .LCV_POLL_%=\n" \
  "v_cvt_pk_bf16_f32 v36, v28, v30\n" \
  "v_cvt_pk_bf16_f32 v37, v29, v31\n" \
  "ds_write_b32 v42, v36\n" \
  "ds_write_b32 v43, v37\n" \
  "v_dot2c_f32_bf16 v11, v32, v32\n" \
  "v_dot2c_f32_bf16 v11, v33, v33\n" \
  "v_dot2c_f32_bf16 v11, v36, v36\n" \
  "v_dot2c_f32_bf16 v11, v37, v37\n" \
  "s_nop 1\n" \
  "s_nop 0\n" \
  "v_cmp_lt_f32 vcc, 4.0, v11\n" \
  "s_cbranch_vccz .LCV_TOP_%=\n" \
  "s_mov_b64 exec, vcc\n" \
  "ds_max_u32 v59, v11\n" \
  "s_mov_b64 exec, -1\n" \
  "s_branch .LCV_TOP_%=\n" \
  ".LCV_POLL_%=:\n" \
  "buffer_load_dwordx4 v[24:27], v44, s[40:43], s89 offen sc1\n" \
  "buffer_load_dwordx4 v[28:31], v44, s[40:43], s89 offen offset:2048 sc1\n" \
  "s_sub_u32 s87, s87, 1\n" \
  "s_cbranch_scc1 .LCV_END_%=\n" \
  "s_branch .LCV_SEE_%=\n" \
  ".LCV_LAST_%=:\n" \
  "s_waitcnt vmcnt(0)\n" \
  ".LCV_END_%=:\n" \
  "s_endpgm\n"
__device__ __forceinline__ void start_kgen(const MegaArgs& A, char* smem, const int kg) {
  const int w = A.wv, l = lane_id(), tid = 64 * w + l, c = l & 15, g = l >> 4;
  KGen G;
  unsigned tp = 0u;
#define TEXT_ASK()                                                                                                     \
  {                                                                                                                    \
    const char* pc;                                                                                                    \
    unsigned n;                                                                                                        \
    asm volatile("s_getpc_b64 %0\n.LKG_PC:\n\ts_mov_b32 %1, .LKG_LAST-.LKG_PC" : "=s"(pc), "=s"(n));                    \
    const unsigned o = (unsigned)(tid * 8 + ((blockIdx.x >> 3) & 7)) * IPS;                                            \
    if ((int)o < (int)n) asm volatile("global_load_dword %0, %1, %2" : "=v"(tp) : "v"(o), "s"(pc) : "memory");         \
  }
  if (IPF == 2) TEXT_ASK()
  v4u raw[2] = {};
#pragma unroll
  for (int i = 0; i < TP; ++i)
    if (kg < 32) raw[i] = *reinterpret_cast<const v4u*>(A.x0 + (size_t)(TP * (8 * (kg & 7) + w) + i) * DM + l * 8);
  G.load(A, 8 * kg + w, l);
  {
    const __amdgpu_buffer_rsrc_t rz = MKRS(A.ws + OFF_Z, Z_BYTES);
#pragma unroll
    for (int i = 0; i < 4; ++i) {
      const u64 z = i == 0 && kg < (int)(2 * SZ_ACC / 8192) ? ACC_INIT : 0ull;
      ST128((v4u{(unsigned)z, (unsigned)(z >> 32), (unsigned)z, (unsigned)(z >> 32)}), rz, tid * 16, (i * 64 + kg) * 8192);
    }
  }
  const u64 tag = call_tag(A);
  if (TP && kg < 32) {
    unsigned t = row_stats(raw[0]);
    if (TP > 1) {
      const unsigned t1 = row_stats(raw[1]);
      t = l < 2 ? t : t1;
    }
    if (l < 2 * TP) __hip_atomic_store((gu64*)(A.ws + OFF_GT) + 2 * (L_SEQ + TP * (8 * kg + w)) + l, tagged(tag, (l & 1) ? t : t << 16), RLX_AGENT);
  }
  DRAIN();
  __syncthreads();
  if (tid < 8) __hip_atomic_store((gu64*)(A.ws + OFF_RD) + tid * 64 + kg, tag, RLX_AGENT);
  asm volatile("" : "+v"(G.pa), "+v"(G.pb), "+v"(G.pe));
  G.setup();
  G.st = smem + w * 16640;
  const __amdgpu_buffer_rsrc_t rs = MKRS(A.ws + OFF_KF + (8 * kg + w) * 2048, 386 * 4);
  SST(1);
  unsigned tw = 0u;
#define TLB_ASK()                                                                                                      \
  if (tid < 384) {                                                                                                     \
    const unsigned o = (unsigned)OFF_XA + (unsigned)(tid * 8 + ((blockIdx.x >> 3) & 7)) * 65536u;                      \
    if (o < WS_TOTAL) asm volatile("global_load_dword %0, %1, %2" : "=v"(tw) : "v"(o), "s"(A.ws) : "memory");          \
  }
#define X2_CLEAR() \
  _Pragma("unroll") for (int i = 0; i < 4; ++i) ST128((v4u{0u, 0u, 0u, 0u}), MKRS(A.ws + OFF_X2, SZ_X), tid * 16, (4 * kg + i) * 8192);
  if (XZP == 1) { X2_CLEAR() }
  if (IPF == 1) TEXT_ASK()
  if (TLBW == 1) TLB_ASK()
  unsigned pf[6] = {};
#define PF_ASK(M)                                                                                                                  \
  if (kg < 32) {                                                                                                                   \
    if ((M) & 1) asm volatile("global_load_dword %0, %2, %3\n\tglobal_load_dword %1, %2, %3 offset:128" : "=&v"(pf[0]), "=&v"(pf[1]) : "v"(kg * 131072 + tid * 256), "s"(A.Wo)); \
    if ((M) & 2) asm volatile("global_load_dword %0, %2, %3\n\tglobal_load_dword %1, %2, %3 offset:128" : "=&v"(pf[2]), "=&v"(pf[3]) : "v"(kg * 256 + tid * 8192), "s"(A.Wq)); \
    if ((M) & 4) asm volatile("global_load_dword %0, %2, %3\n\tglobal_load_dword %1, %2, %3 offset:128" : "=&v"(pf[4]), "=&v"(pf[5]) : "v"(kg * 256 + tid * 8192), "s"(A.Wk)); \
  }
  PF_ASK(PFA)
  G.columns(l, 0, 16);
  G.get(G.bh, G.bl, l);
  if (XZP == 2) { X2_CLEAR() }
  PF_ASK(PFB)
  if (TLBW == 2) TLB_ASK()
  (void)G.scale();
  const Cx zb = G.power(256.f);
  Cx b = G.C, z = G.power(2.f);
#pragma unroll 1
  for (int f = 1; f < 9;) {
    char* p = G.st + l * 8;
    const int fe = f == 1 ? 4 : 9;
#pragma unroll 1
    for (; f < fe; ++f) {
      b = cmul(b, zb);
      if (f == 2 || f == 4) z = cmul(z, z);
      Cx y = b;
      p = G.rows(p, y, z, f == 1 ? 8 : f < 4 ? 4 : 2);
    }
    const f32x4 d = G.tile<false>(l);
    const v4u r = {pkh(d.x, d.x), pkh(d.y, d.y), pkh(d.z, d.z), pkh(d.w, d.w)};
    if (f == 4) {
      ST128(r, rs, (g < 2 ? 8 * c + 4 * g : 64 * g + 4 * c) * 4, 0);
    } else {
      ST64((v2u{r.x, r.y}), rs, (256 + 64 * g + 2 * c) * 4, 0);
      ST64((v2u{r.z, r.w}), rs, (288 + 64 * g + 2 * c) * 4, 0);
    }
  }
  SST(2);
  DRAIN();
  asm volatile("" ::"v"(tp), "v"(tw), "v"(pf[0]), "v"(pf[1]), "v"(pf[2]), "v"(pf[3]), "v"(pf[4]), "v"(pf[5]));
  if (l == 0) __hip_atomic_store((gu64*)(A.ws + OFF_KG) + kg * 16 + w, tag, RLX_AGENT);
  if (XZP == 3) { X2_CLEAR() }
  const u64 rv = (XZP & 3) && w == 0 ? ready_look(A) : 0;
  if (TLBW == 3) {
    TLB_ASK()
    DRAIN();
    asm volatile("" ::"v"(tw));
  }
  DRAIN();
  __syncthreads();
  if ((XZP & 3) && w == 0) {
    ready_wait(A, tag, rv);
    if (l < 8) set_flag((unsigned*)(A.ws + OFF_FL) + FL_FZ + l * 64 + kg);
  }
  SST(3);
}

constexpr int C_SM = 131072, C_SO = C_SM + 8192, C_KT = C_SO + 2048, C_X = C_KT + 16384, C_SQ = C_X + 1024;
static_assert(C_SQ + 4096 <= 163840 && C_SQ % 512 == 0, "lds");
__device__ __forceinline__ v4u mk_srd(const void* p, const unsigned bytes) {
  const unsigned long long a = (unsigned long long)p;
  return v4u{(unsigned)a, (unsigned)(a >> 32) & 0xffffu, bytes, 0x00020000u};
}
template <bool ATT>
__device__ __forceinline__ void role_chain_t(const MegaArgs& A, char* smem, const int L, const int h, const int rep) {
  const int w = A.wv;
  char* ws = A.ws;
  unsigned* FL = (unsigned*)(ws + OFF_FL);
  const int l = lane_id(), c = l & 15, g = l >> 4;
  unsigned* FQ = FL + FL_FQ + L * 64 * 32 * 32;
  const int wb = (w + ROT_H) & 7, tb = ROT_T;
  const unsigned sb = lds_base(smem);
  glds16(A.mem0, (unsigned)((c & 7) * DM + 64 * w + 16 * g + 8 * (c >> 3)) * 2, sb + C_SM + w * 1024);
#pragma unroll
  for (int i = 0; i < 16; ++i)
    glds16((const char*)A.Wo + (size_t)(h * DH + 32 * (i >> 2) + 4 * w + (i & 2)) * (DM * 2),
           (unsigned)((i & 1) * 1024 + ((l ^ (((i & 1) | ((w & 3) << 1)) << 1)) << 4)), sb + w * 16384 + i * 1024, WNT & 1);
  const u64 tag = call_tag(A);
  const char* gK = ws + OFF_QK + L * SZ_QK + ((size_t)h * 16 + 2 * w) * 1024;
  const char* gV = ws + OFF_VF + L * SZ_O + ((size_t)h * 8 + 2 * w - 8) * 1024;
  v8u pv, pw;
  {
    const unsigned voff = g * 256 + c * 8, wp = sb + w * 16384 + l * 16;
    pv[0] = (c & 8) ? 0xEEEE : 0x4444;
    pv[1] = sb + C_SM + l * 16;
    const unsigned ra = sb + C_SQ + (g >> 1) * 512 + (c ^ ((g & 1) * 18)) * 8;
    pv[2] = ATT ? ra : voff;
    pv[3] = ATT ? ra ^ 0x108 : wp;
    pv[4] = sb + ((c & 2) ? C_SQ : C_SO) + w * 256 + (((c & 5) | ((c >> 2) & 2)) + 8 * (g >> 1)) * 16 + 8 * ((g ^ (c >> 1)) & 1);
    pv[5] = voff;
    pv[6] = wp;
    pv[7] = sb + C_SO + (l >> 5) * 1024 + w * 256 + (l & 31) * 8;
    const int q = (l >> 2) & 3, p = l & 3;
    const int f = (q & 1) | ((g & 1) << 1) | ((q >> 1) << 2);
#pragma unroll
    for (int i = 0; i < 4; ++i) pw[i] = (4 * i + g) * (HD * 2) + ((c ^ (((g & 1) | (i << 1)) << 1)) << 4) - 1024 * i;
    pw[4] = sb + (g >> 1) * 16384 + (4 * (g & 1) + (q & 1) + 8 * (q >> 1)) * 256 + (w ^ f) * 32 + p * 8;
    pw[5] = sb + (4 * (g >> 1) + 2 * (q >> 1) + (g & 1)) * 16384 + (q & 1) * 1024 + p * 8 + ((4 * wb) ^ f ^ tb) * 32;
    pw[6] = pw[7] = 0u;
  }
  const size_t wo = (size_t)(16 * w) * HD + h * DH;
#define POL 2
#define PG(w) (POL == 2 ? (w) * 4096 : ((w) & 1) * 4096 + ((w) >> 1) * 16384)
  const v4u rsA = mk_srd(ws + OFF_ACC + L * SZ_ACC + (POL == 1 ? 8192 : 0), SZ_ACC - 8192);
  const v4u rsF = mk_srd(FQ, 64 * 32 * 32 * 4);
  const v4u rsMK = mk_srd(ws + OFF_MEMK + L * SZ_MEM, SZ_MEM);
  const v4u rsMV = mk_srd(ws + OFF_MEMV + L * SZ_MEM, SZ_MEM);
  const v4u pAB = (v4u{0u, 2048u, 3072u, 1024u} ^ (1536u * tb)) + PG(wb);
  const v4u rsT = mk_srd(FL + FL_FK + L * 32, 64 * 1024 - 128);
  unsigned sc[8];
  sc[0] = h * 128;
  sc[1] = (h * 4 + w - 4) * 512;
  sc[2] = (h * 4 + w - 4) * 512;
  sc[3] = NSEG - 1;
  sc[5] = w;
  sc[6] = sb;
  const char* dbg = ws;
#define CHAIN_OPS                                                                                                          \
  ::"{v[0:7]}"(pv), "{v[24:31]}"(pw), "{s[4:5]}"(A.Wv + wo), "{s[6:7]}"(A.Wq + wo), "{s[8:9]}"(A.Wk + wo), "{s[10:11]}"(tag),         \
      "{s[12:13]}"(ws + OFF_RD + (blockIdx.x & 7) * 512), "{s[14:15]}"(ws + OFF_G + (blockIdx.x & 7) * 256), "{s[16:17]}"(ws + OFF_G + h * 8), \
      "{s18}"(w == 4 && L == 0 ? 255 : 0), "{s19}"(sb + w * 16384), "{s[20:21]}"(ws + WS_TOTAL + (w == 0 ? 534 * 256 + A.vb : 610 * 256) * 8), \
      "{s[40:43]}"(rsA), "{s[44:47]}"(rsF), "{s[48:51]}"(rsMK), "{s[52:55]}"(rsMV), "{s[56:59]}"(pAB), "{s60}"(PG(w)), "{s61}"(ATT ? PG(h >> 2) + (h & 3) * 1024 : L), \
      "{s[62:63]}"(gK), "{s[64:65]}"(gV), "{s66}"(0), "{s67}"(w == 4 ? (h >> 3) * 256 + (h & 7) * 8 : 0x40000000), "{s68}"(sc[0]), "{s69}"(sc[1]), "{s70}"(sc[2]),    \
      "{s71}"(sc[3]), "{s73}"(sc[5]), "{s74}"(sc[6]), "{s[36:37]}"(dbg),                   \
      "{s[0:3]}"(rsT)                                                                                                      \
      : "memory", "vcc", "scc", "s99", "v255"
  if (ATT) asm volatile("s_mov_b64 exec, -1\ns_nop 4\n" CHAIN_ASM_ATT ".LKG_LAS2:\n" CHAIN_OPS);
  else asm volatile("s_mov_b64 exec, -1\ns_nop 4\n" CHAIN_ASM_VO CHAIN_OPS);
#undef CHAIN_OPS
}

__device__ __forceinline__ void role_chain(const MegaArgs& A, char* smem, const int L, const int h, const int rep) {
  if (A.wv < 4) role_chain_t<true>(A, smem, L, h, rep);
  else role_chain_t<false>(A, smem, L, h, rep);
}

constexpr int AW_SO = 0, AW_TK = 32768, AW_CNT = AW_TK + 98304;
__device__ __forceinline__ unsigned umin3(unsigned a, unsigned b, unsigned c) {
  return __builtin_elementwise_min(__builtin_elementwise_min(a, b), c);
}
#define PW_FREE(L_, s_, i_) \
  if (l == 0) set_flag(FL + FL_FD + ((s_) * 2 + (L_)) * 32 + (i_))
#define AW_ASM_X \
  "s_mov_b32 s61, 0\n" \
  "s_mov_b32 s67, 1\n" \
  "s_mov_b32 s68, 0\n" \
  "s_mov_b32 s70, 0\n" \
  "s_mov_b32 s73, 0\n" \
  "s_mov_b32 s71, 0x3e0293ee\n" \
  "s_mov_b32 s72, 0xbe0293ee\n" \
  "s_mov_b32 s65, 49152\n" \
  "s_bitcmp0_b32 s58, 2\n" \
  "s_cbranch_scc1 .LCX_OWN_%=\n" \
  "v_swap_b32 v192, v208\n" \
  "v_swap_b32 v193, v209\n" \
  "v_swap_b32 v194, v210\n" \
  "v_swap_b32 v195, v211\n" \
  "v_swap_b32 v196, v212\n" \
  "v_swap_b32 v197, v213\n" \
  "v_swap_b32 v198, v214\n" \
  "v_swap_b32 v199, v215\n" \
  "v_swap_b32 v200, v216\n" \
  "v_swap_b32 v201, v217\n" \
  "v_swap_b32 v202, v218\n" \
  "v_swap_b32 v203, v219\n" \
  "v_swap_b32 v204, v220\n" \
  "v_swap_b32 v205, v221\n" \
  "v_swap_b32 v206, v222\n" \
  "v_swap_b32 v207, v223\n" \
  "v_swap_b32 v224, v240\n" \
  "v_swap_b32 v225, v241\n" \
  "v_swap_b32 v226, v242\n" \
  "v_swap_b32 v227, v243\n" \
  "v_swap_b32 v228, v244\n" \
  "v_swap_b32 v229, v245\n" \
  "v_swap_b32 v230, v246\n" \
  "v_swap_b32 v231, v247\n" \
  "v_swap_b32 v232, v248\n" \
  "v_swap_b32 v233, v249\n" \
  "v_swap_b32 v234, v250\n" \
  "v_swap_b32 v235, v251\n" \
  "v_swap_b32 v236, v252\n" \
  "v_swap_b32 v237, v253\n" \
  "v_swap_b32 v238, v254\n" \
  "v_swap_b32 v239, v255\n" \
  ".LCX_OWN_%=:\n" \
  "v_mov_b32 v38, s58\n" \
  "s_memrealtime s[38:39]\n" \
  "s_waitcnt lgkmcnt(0)\n" \
  "s_mov_b32 s69, s38\n" \
  "s_setprio 3\n" \
  ".p2align 6\n" \
  ".LCX_TOP_%=:\n" \
  "s_mov_b32 s62, 0x200000\n" \
  "s_cmp_ge_u32 s73, s67\n" \
  "s_cbranch_scc1 .LCX_TOK_%=\n" \
  "s_cmp_eq_u32 s61, 0\n" \
  "s_cbranch_scc1 .LCX_SPIN_%=\n" \
  ".LCX_RE_%=:\n" \
  "ds_read_b32 v37, v38\n" \
  "s_add_u32 s62, s62, 1\n" \
  "s_waitcnt lgkmcnt(0)\n" \
  "v_readfirstlane_b32 s73, v37\n" \
  "s_bfe_u32 s64, s73, 0x80008\n" \
  "s_and_b32 s73, s73, 0xff\n" \
  "s_min_u32 s73, s73, s64\n" \
  "s_cmp_ge_u32 s73, s67\n" \
  "s_cbranch_scc1 .LCX_TOK_%=\n" \
  "s_sleep 1\n" \
  "s_cmp_lt_u32 s62, 2097158\n" \
  "s_cbranch_scc1 .LCX_RE_%=\n" \
  ".LCX_SPIN_%=:\n" \
  "s_sleep 2\n" \
  "s_memrealtime s[38:39]\n" \
  "ds_read_b32 v37, v38\n" \
  "s_sub_u32 s62, s62, 1\n" \
  "s_cbranch_scc1 .LCX_END_%=\n" \
  "s_waitcnt lgkmcnt(0)\n" \
  "v_readfirstlane_b32 s73, v37\n" \
  "s_add_u32 s38, s38, 80\n" \
  "s_bfe_u32 s64, s73, 0x80008\n" \
  "s_and_b32 s73, s73, 0xff\n" \
  "s_min_u32 s73, s73, s64\n" \
  "s_cmp_ge_u32 s73, s67\n" \
  "s_cbranch_scc0 .LCX_SPIN_%=\n" \
  "s_cmp_eq_u32 s70, 0\n" \
  "s_cbranch_scc1 .LCX_TOK_%=\n" \
  "s_sub_u32 s63, s38, s69\n" \
  "s_max_i32 s63, s63, 0\n" \
  "s_add_u32 s69, s69, s63\n" \
  ".LCX_TOK_%=:\n" \
  "ds_read_b128 v[48:51], v6 offset:0\n" \
  "ds_read_b128 v[64:67], v0 offset:8192\n" \
  "ds_read_b128 v[80:83], v0 offset:12288\n" \
  "ds_read_b128 v[52:55], v6 offset:1024\n" \
  "ds_read_b128 v[68:71], v0 offset:9216\n" \
  "ds_read_b128 v[84:87], v0 offset:13312\n" \
  "ds_read_b128 v[56:59], v6 offset:2048\n" \
  "ds_read_b128 v[72:75], v0 offset:10240\n" \
  "ds_read_b128 v[88:91], v0 offset:14336\n" \
  "ds_read_b128 v[60:63], v6 offset:3072\n" \
  "ds_read_b128 v[76:79], v0 offset:11264\n" \
  "ds_read_b128 v[92:95], v0 offset:15360\n" \
  "s_waitcnt lgkmcnt(9)\n" \
  "v_mfma_f32_16x16x32_bf16 v[20:23], v[64:67], v[48:51], v[8:11]\n" \
  "v_mfma_f32_16x16x32_bf16 v[24:27], v[80:83], v[48:51], v[12:15]\n" \
  "s_memrealtime s[38:39]\n" \
  "s_waitcnt lgkmcnt(6)\n" \
  "v_mfma_f32_16x16x32_bf16 v[20:23], v[68:71], v[52:55], v[20:23]\n" \
  "v_mfma_f32_16x16x32_bf16 v[24:27], v[84:87], v[52:55], v[24:27]\n" \
  "s_waitcnt lgkmcnt(3)\n" \
  "v_mfma_f32_16x16x32_bf16 v[20:23], v[72:75], v[56:59], v[20:23]\n" \
  "v_mfma_f32_16x16x32_bf16 v[24:27], v[88:91], v[56:59], v[24:27]\n" \
  "s_waitcnt lgkmcnt(0)\n" \
  "v_mfma_f32_16x16x32_bf16 v[20:23], v[76:79], v[60:63], v[20:23]\n" \
  "v_mfma_f32_16x16x32_bf16 v[24:27], v[92:95], v[60:63], v[24:27]\n" \
  "s_sub_u32 s63, s38, s69\n" \
  "s_add_u32 s63, s63, 8\n" \
  "s_or_b32 s63, s63, s68\n" \
  "s_cmp_lt_i32 s63, 0\n" \
  "s_cbranch_scc1 .LCX_D2_%=\n" \
  "s_setprio 3\n" \
  "v_mov_b32 v35, 0\n" \
  "global_load_dwordx4 v[128:131], v5, s[48:49] offset:0 sc1\n" \
  "global_load_dwordx4 v[132:135], v5, s[48:49] offset:1024 sc1\n" \
  "global_load_dwordx4 v[136:139], v3, s[48:49] offset:0 sc1\n" \
  "global_load_dwordx4 v[140:143], v3, s[48:49] offset:512 sc1\n" \
  "global_load_dword v35, v35, s[50:51] sc1\n" \
  "global_load_dword v146, v2, s[52:53] sc1\n" \
  "s_setprio 0\n" \
  "s_mov_b32 s68, -2\n" \
  ".LCX_D2_%=:\n" \
  "ds_read_b128 v[96:99], v0 offset:16384\n" \
  "ds_read_b128 v[100:103], v0 offset:17408\n" \
  "ds_read_b128 v[104:107], v0 offset:18432\n" \
  "ds_read_b128 v[108:111], v0 offset:19456\n" \
  "ds_read_b128 v[112:115], v0 offset:20480\n" \
  "ds_read_b128 v[116:119], v0 offset:21504\n" \
  "ds_read_b128 v[120:123], v0 offset:22528\n" \
  "ds_read_b128 v[124:127], v0 offset:23552\n" \
  "s_add_u32 s67, s67, 1\n" \
  "v_add_u32 v0, s65, v0\n" \
  "v_add_u32 v6, s65, v6\n" \
  "s_sub_u32 s65, 0, s65\n" \
  "s_nop 1\n" \
  "v_max3_f32 v32, v20, v21, v22\n" \
  "v_max3_f32 v33, v23, v24, v25\n" \
  "v_max3_f32 v32, v32, v26, v27\n" \
  "v_max_f32 v32, v32, v33\n" \
  "s_memrealtime s[38:39]\n" \
  "v_mov_b32 v33, v32\n" \
  "s_nop 1\n" \
  "v_permlane16_swap_b32 v32, v33\n" \
  "v_max_f32 v32, v32, v33\n" \
  "v_mov_b32 v33, v32\n" \
  "s_nop 1\n" \
  "v_permlane32_swap_b32 v32, v33\n" \
  "v_max_f32 v32, v32, v33\n" \
  "s_waitcnt lgkmcnt(0)\n" \
  "s_sub_u32 s63, s38, s69\n" \
  "s_add_u32 s63, s63, 8\n" \
  "s_or_b32 s63, s63, s68\n" \
  "s_cmp_lt_i32 s63, 0\n" \
  "s_cbranch_scc1 .LCX_D3_%=\n" \
  "s_setprio 3\n" \
  "v_mov_b32 v35, 0\n" \
  "global_load_dwordx4 v[128:131], v5, s[48:49] offset:0 sc1\n" \
  "global_load_dwordx4 v[132:135], v5, s[48:49] offset:1024 sc1\n" \
  "global_load_dwordx4 v[136:139], v3, s[48:49] offset:0 sc1\n" \
  "global_load_dwordx4 v[140:143], v3, s[48:49] offset:512 sc1\n" \
  "global_load_dword v35, v35, s[50:51] sc1\n" \
  "global_load_dword v146, v2, s[52:53] sc1\n" \
  "s_setprio 0\n" \
  "s_mov_b32 s68, -2\n" \
  ".LCX_D3_%=:\n" \
  "v_mul_f32 v33, s72, v32\n" \
  "v_fma_f32 v20, v20, s71, v33\n" \
  "v_fma_f32 v21, v21, s71, v33\n" \
  "v_fma_f32 v22, v22, s71, v33\n" \
  "v_fma_f32 v23, v23, s71, v33\n" \
  "v_fma_f32 v24, v24, s71, v33\n" \
  "v_fma_f32 v25, v25, s71, v33\n" \
  "v_fma_f32 v26, v26, s71, v33\n" \
  "v_fma_f32 v27, v27, s71, v33\n" \
  "v_exp_f32 v20, v20\n" \
  "v_exp_f32 v21, v21\n" \
  "v_exp_f32 v22, v22\n" \
  "v_exp_f32 v23, v23\n" \
  "v_exp_f32 v24, v24\n" \
  "v_exp_f32 v25, v25\n" \
  "v_exp_f32 v26, v26\n" \
  "v_exp_f32 v27, v27\n" \
  "v_add_f32 v34, v20, v21\n" \
  "v_add_f32 v33, v22, v23\n" \
  "v_add_f32 v36, v24, v25\n" \
  "v_add_f32 v34, v34, v33\n" \
  "v_add_f32 v33, v26, v27\n" \
  "v_add_f32 v34, v34, v36\n" \
  "v_add_f32 v34, v34, v33\n" \
  "v_mov_b64 v[28:29], 0\n" \
  "v_mov_b64 v[30:31], 0\n" \
  "s_waitcnt vmcnt(2) lgkmcnt(0)\n" \
  "s_cmp_gt_i32 s68, -2\n" \
  "s_cbranch_scc1 .LCX_NP_%=\n" \
  "s_waitcnt vmcnt(0)\n" \
  ".LCX_NP_%=:\n" \
  "s_cmp_lg_u32 s68, 0\n" \
  "s_cbranch_scc1 .LCX_TEST_%=\n" \
  ".LCX_WF_%=:\n" \
  "s_memrealtime s[38:39]\n" \
  "s_waitcnt lgkmcnt(0)\n" \
  "s_sub_u32 s63, s69, s38\n" \
  "s_cmp_le_i32 s63, 0\n" \
  "s_cbranch_scc1 .LCX_GO_%=\n" \
  "s_cmp_lt_i32 s63, 12\n" \
  "s_cbranch_scc1 .LCX_WF_%=\n" \
  "s_sleep 3\n" \
  "s_branch .LCX_WF_%=\n" \
  ".LCX_GO_%=:\n" \
  "v_mov_b32 v35, 0\n" \
  "global_load_dwordx4 v[128:131], v5, s[48:49] offset:0 sc1\n" \
  "global_load_dwordx4 v[132:135], v5, s[48:49] offset:1024 sc1\n" \
  "global_load_dwordx4 v[136:139], v3, s[48:49] offset:0 sc1\n" \
  "global_load_dwordx4 v[140:143], v3, s[48:49] offset:512 sc1\n" \
  "global_load_dword v35, v35, s[50:51] sc1\n" \
  "global_load_dword v146, v2, s[52:53] sc1\n" \
  "s_sub_u32 s62, s62, 8\n" \
  "s_cbranch_scc1 .LCX_END_%=\n" \
  "s_waitcnt vmcnt(0)\n" \
  ".LCX_TEST_%=:\n" \
  "s_setprio 3\n" \
  "v_smfmac_f32_16x16x64_bf16 v[28:31], v[128:131], v[48:55], v1\n" \
  "v_smfmac_f32_16x16x64_bf16 v[28:31], v[132:135], v[56:63], v1\n" \
  "v_min3_u32 v33, v128, v129, v130\n" \
  "v_min3_u32 v36, v131, v132, v133\n" \
  "v_min3_u32 v33, v33, v134, v135\n" \
  "v_min3_u32 v36, v36, v136, v137\n" \
  "v_min3_u32 v33, v33, v138, v139\n" \
  "v_min3_u32 v36, v36, v140, v141\n" \
  "v_min3_u32 v33, v33, v142, v143\n" \
  "v_min3_u32 v33, v33, v36, v146\n" \
  "v_cmp_eq_u32 vcc, 0, v33\n" \
  "v_readfirstlane_b32 s64, v35\n" \
  "s_mov_b32 s68, 0\n" \
  "s_cmp_eq_u64 vcc, 0\n" \
  "s_cbranch_scc0 .LCX_FAIL_%=\n" \
  "v_permlane32_swap_b32 v28, v30\n" \
  "v_permlane32_swap_b32 v29, v31\n" \
  "v_mov_b32 v33, s67\n" \
  "ds_write_b32 v38, v33 offset:16\n" \
  "v_add_f32 v28, v28, v30\n" \
  "v_add_f32 v29, v29, v31\n" \
  "v_max3_f32 v36, v28, v29, v32\n" \
  "v_mov_b32 v33, v36\n" \
  "s_cmp_eq_u32 s64, 0\n" \
  "s_cselect_b32 s64, s38, s64\n" \
  "v_permlane16_swap_b32 v36, v33\n" \
  "v_max_f32 v36, v36, v33\n" \
  "v_mov_b32 v33, v36\n" \
  "s_sub_u32 s63, s64, s70\n" \
  "s_mov_b32 s70, s64\n" \
  "v_permlane32_swap_b32 v36, v33\n" \
  "v_max_f32 v36, v36, v33\n" \
  "v_mul_f32 v36, s72, v36\n" \
  "v_fma_f32 v32, v32, s71, v36\n" \
  "v_fma_f32 v28, v28, s71, v36\n" \
  "v_fma_f32 v29, v29, s71, v36\n" \
  "v_exp_f32 v32, v32\n" \
  "v_exp_f32 v28, v28\n" \
  "v_exp_f32 v29, v29\n" \
  "v_fma_f32 v34, v34, v32, v28\n" \
  "v_add_f32 v34, v34, v29\n" \
  "v_mov_b32 v33, v34\n" \
  "s_min_u32 s63, s63, 250\n" \
  "s_add_u32 s69, s64, s63\n" \
  "v_permlane16_swap_b32 v34, v33\n" \
  "v_add_f32 v34, v34, v33\n" \
  "v_mov_b32 v33, v34\n" \
  "s_add_u32 s69, s69, -52\n" \
  "s_add_u32 s48, s48, 0x10000\n" \
  "s_addc_u32 s49, s49, 0\n" \
  "v_permlane32_swap_b32 v34, v33\n" \
  "v_add_f32 v34, v34, v33\n" \
  "v_rcp_f32 v34, v34\n" \
  "s_add_u32 s50, s50, 1024\n" \
  "s_addc_u32 s51, s51, 0\n" \
  "s_add_u32 s52, s52, 256\n" \
  "s_addc_u32 s53, s53, 0\n" \
  "v_mul_f32 v32, v32, v34\n" \
  "v_mul_f32 v20, v20, v32\n" \
  "v_mul_f32 v21, v21, v32\n" \
  "v_mul_f32 v22, v22, v32\n" \
  "v_mul_f32 v23, v23, v32\n" \
  "v_mul_f32 v24, v24, v32\n" \
  "v_mul_f32 v25, v25, v32\n" \
  "v_mul_f32 v26, v26, v32\n" \
  "v_mul_f32 v27, v27, v32\n" \
  "v_cvt_pk_bf16_f32 v40, v20, v21\n" \
  "v_cvt_pk_bf16_f32 v41, v22, v23\n" \
  "v_cvt_pk_bf16_f32 v42, v24, v25\n" \
  "v_cvt_pk_bf16_f32 v43, v26, v27\n" \
  "v_mul_f32 v28, v28, v34\n" \
  "v_mul_f32 v29, v29, v34\n" \
  "v_mfma_f32_16x16x32_bf16 v[64:67], v[96:99], v[40:43], 0\n" \
  "v_cvt_pk_bf16_f32 v44, v28, v29\n" \
  "v_mov_b32 v45, 0\n" \
  "v_mfma_f32_16x16x32_bf16 v[68:71], v[100:103], v[40:43], 0\n" \
  "v_cvt_pk_bf16_f32 v47, v28, v29\n" \
  "v_mov_b32 v46, 0\n" \
  "v_mfma_f32_16x16x32_bf16 v[72:75], v[104:107], v[40:43], 0\n" \
  "v_permlane32_swap_b32 v44, v45\n" \
  "v_mfma_f32_16x16x32_bf16 v[76:79], v[108:111], v[40:43], 0\n" \
  "v_permlane32_swap_b32 v46, v47\n" \
  "v_mfma_f32_16x16x32_bf16 v[80:83], v[112:115], v[40:43], 0\n" \
  "v_xor_b32 v16, 4096, v7\n" \
  "v_mfma_f32_16x16x32_bf16 v[84:87], v[116:119], v[40:43], 0\n" \
  "v_xor_b32 v17, 8192, v7\n" \
  "v_mfma_f32_16x16x32_bf16 v[88:91], v[120:123], v[40:43], 0\n" \
  "v_xor_b32 v18, 12288, v7\n" \
  "v_mfma_f32_16x16x32_bf16 v[92:95], v[124:127], v[40:43], 0\n" \
  "s_memrealtime s[38:39]\n" \
  "ds_read_b32 v37, v38\n" \
  "v_mfma_f32_16x16x16_bf16 v[64:67], v[136:137], v[44:45], v[64:67]\n" \
  "v_mfma_f32_16x16x16_bf16 v[68:71], v[140:141], v[44:45], v[68:71]\n" \
  "v_mfma_f32_16x16x16_bf16 v[72:75], v[136:137], v[46:47], v[72:75]\n" \
  "v_mfma_f32_16x16x16_bf16 v[76:79], v[140:141], v[46:47], v[76:79]\n" \
  "v_mfma_f32_16x16x16_bf16 v[80:83], v[138:139], v[44:45], v[80:83]\n" \
  "v_mfma_f32_16x16x16_bf16 v[84:87], v[142:143], v[44:45], v[84:87]\n" \
  "v_mfma_f32_16x16x16_bf16 v[88:91], v[138:139], v[46:47], v[88:91]\n" \
  "v_mfma_f32_16x16x16_bf16 v[92:95], v[142:143], v[46:47], v[92:95]\n" \
  "s_nop 0\n" \
  "v_cvt_pk_bf16_f32 v160, v64, v65\n" \
  "v_cvt_pk_bf16_f32 v161, v66, v67\n" \
  "v_cvt_pk_bf16_f32 v162, v68, v69\n" \
  "v_cvt_pk_bf16_f32 v163, v70, v71\n" \
  "ds_write_b128 v7, v[160:163] offset:0\n" \
  "v_cvt_pk_bf16_f32 v164, v72, v73\n" \
  "v_cvt_pk_bf16_f32 v165, v74, v75\n" \
  "v_cvt_pk_bf16_f32 v166, v76, v77\n" \
  "v_cvt_pk_bf16_f32 v167, v78, v79\n" \
  "ds_write_b128 v7, v[164:167] offset:1024\n" \
  "v_cvt_pk_bf16_f32 v168, v80, v81\n" \
  "v_cvt_pk_bf16_f32 v169, v82, v83\n" \
  "v_cvt_pk_bf16_f32 v170, v84, v85\n" \
  "v_cvt_pk_bf16_f32 v171, v86, v87\n" \
  "ds_write_b128 v7, v[168:171] offset:2048\n" \
  "v_cvt_pk_bf16_f32 v172, v88, v89\n" \
  "v_cvt_pk_bf16_f32 v173, v90, v91\n" \
  "v_cvt_pk_bf16_f32 v174, v92, v93\n" \
  "v_cvt_pk_bf16_f32 v175, v94, v95\n" \
  "ds_write_b128 v7, v[172:175] offset:3072\n" \
  "v_xor_b32 v7, 0x4000, v7\n" \
  "v_mfma_f32_16x16x32_bf16 v[112:115], v[192:195], v[160:163], 0\n" \
  "v_mfma_f32_16x16x32_bf16 v[116:119], v[224:227], v[160:163], 0\n" \
  "s_waitcnt lgkmcnt(0)\n" \
  "s_barrier\n" \
  "v_mfma_f32_16x16x32_bf16 v[112:115], v[196:199], v[164:167], v[112:115]\n" \
  "ds_read_b128 v[64:67], v16 offset:0\n" \
  "ds_read_b128 v[68:71], v16 offset:1024\n" \
  "ds_read_b128 v[72:75], v16 offset:2048\n" \
  "ds_read_b128 v[76:79], v16 offset:3072\n" \
  "s_sub_u32 s59, s60, 1\n" \
  "s_bfe_u32 s59, s59, 0x10003\n" \
  "v_readfirstlane_b32 s73, v37\n" \
  "s_sub_u32 s63, s38, s69\n" \
  "s_bfe_u32 s64, s73, 0x80010\n" \
  "s_sub_u32 s64, s64, s67\n" \
  "s_or_b32 s63, s63, s64\n" \
  "s_bfe_u32 s64, s73, 0x80008\n" \
  "s_and_b32 s73, s73, 0xff\n" \
  "s_min_u32 s73, s73, s64\n" \
  "s_or_b32 s63, s63, s68\n" \
  "s_cmp_lt_i32 s63, 0\n" \
  "s_cbranch_scc1 .LCX_E4_%=\n" \
  "v_mov_b32 v35, 0\n" \
  "global_load_dwordx4 v[128:131], v5, s[48:49] offset:0 sc1\n" \
  "global_load_dwordx4 v[132:135], v5, s[48:49] offset:1024 sc1\n" \
  "global_load_dwordx4 v[136:139], v3, s[48:49] offset:0 sc1\n" \
  "global_load_dwordx4 v[140:143], v3, s[48:49] offset:512 sc1\n" \
  "global_load_dword v35, v35, s[50:51] sc1\n" \
  "global_load_dword v146, v2, s[52:53] sc1\n" \
  "s_mov_b32 s68, -1\n" \
  ".LCX_E4_%=:\n" \
  "v_mfma_f32_16x16x32_bf16 v[116:119], v[228:231], v[164:167], v[116:119]\n" \
  "v_mfma_f32_16x16x32_bf16 v[112:115], v[200:203], v[168:171], v[112:115]\n" \
  "v_mfma_f32_16x16x32_bf16 v[116:119], v[232:235], v[168:171], v[116:119]\n" \
  "v_mfma_f32_16x16x32_bf16 v[112:115], v[204:207], v[172:175], v[112:115]\n" \
  "v_mfma_f32_16x16x32_bf16 v[116:119], v[236:239], v[172:175], v[116:119]\n" \
  "s_waitcnt lgkmcnt(3)\n" \
  "v_mfma_f32_16x16x32_bf16 v[120:123], v[192:195], v[64:67], 0\n" \
  "v_mfma_f32_16x16x32_bf16 v[124:127], v[224:227], v[64:67], 0\n" \
  "ds_read_b128 v[80:83], v17 offset:0\n" \
  "s_waitcnt lgkmcnt(3)\n" \
  "v_mfma_f32_16x16x32_bf16 v[120:123], v[196:199], v[68:71], v[120:123]\n" \
  "v_mfma_f32_16x16x32_bf16 v[124:127], v[228:231], v[68:71], v[124:127]\n" \
  "ds_read_b128 v[84:87], v17 offset:1024\n" \
  "s_waitcnt lgkmcnt(3)\n" \
  "v_mfma_f32_16x16x32_bf16 v[120:123], v[200:203], v[72:75], v[120:123]\n" \
  "v_mfma_f32_16x16x32_bf16 v[124:127], v[232:235], v[72:75], v[124:127]\n" \
  "ds_read_b128 v[88:91], v17 offset:2048\n" \
  "s_waitcnt lgkmcnt(3)\n" \
  "v_mfma_f32_16x16x32_bf16 v[120:123], v[204:207], v[76:79], v[120:123]\n" \
  "v_mfma_f32_16x16x32_bf16 v[124:127], v[236:239], v[76:79], v[124:127]\n" \
  "ds_read_b128 v[92:95], v17 offset:3072\n" \
  "s_waitcnt lgkmcnt(3)\n" \
  "v_mfma_f32_16x16x32_bf16 v[112:115], v[208:211], v[80:83], v[112:115]\n" \
  "v_mfma_f32_16x16x32_bf16 v[116:119], v[240:243], v[80:83], v[116:119]\n" \
  "ds_read_b128 v[96:99], v18 offset:0\n" \
  "s_waitcnt lgkmcnt(3)\n" \
  "v_mfma_f32_16x16x32_bf16 v[112:115], v[212:215], v[84:87], v[112:115]\n" \
  "v_mfma_f32_16x16x32_bf16 v[116:119], v[244:247], v[84:87], v[116:119]\n" \
  "ds_read_b128 v[100:103], v18 offset:1024\n" \
  "s_waitcnt lgkmcnt(3)\n" \
  "v_mfma_f32_16x16x32_bf16 v[112:115], v[216:219], v[88:91], v[112:115]\n" \
  "v_mfma_f32_16x16x32_bf16 v[116:119], v[248:251], v[88:91], v[116:119]\n" \
  "ds_read_b128 v[104:107], v18 offset:2048\n" \
  "s_waitcnt lgkmcnt(3)\n" \
  "v_mfma_f32_16x16x32_bf16 v[112:115], v[220:223], v[92:95], v[112:115]\n" \
  "v_mfma_f32_16x16x32_bf16 v[116:119], v[252:255], v[92:95], v[116:119]\n" \
  "ds_read_b128 v[108:111], v18 offset:3072\n" \
  "s_waitcnt lgkmcnt(3)\n" \
  "v_mfma_f32_16x16x32_bf16 v[120:123], v[208:211], v[96:99], v[120:123]\n" \
  "v_mfma_f32_16x16x32_bf16 v[124:127], v[240:243], v[96:99], v[124:127]\n" \
  "s_memrealtime s[38:39]\n" \
  "ds_read_b32 v37, v38\n" \
  "s_waitcnt lgkmcnt(3)\n" \
  "v_mfma_f32_16x16x32_bf16 v[120:123], v[212:215], v[100:103], v[120:123]\n" \
  "v_mfma_f32_16x16x32_bf16 v[124:127], v[244:247], v[100:103], v[124:127]\n" \
  "v_cvt_pk_bf16_f32 v20, v112, v113\n" \
  "v_cvt_pk_bf16_f32 v21, v114, v115\n" \
  "v_cvt_pk_bf16_f32 v22, v116, v117\n" \
  "v_cvt_pk_bf16_f32 v23, v118, v119\n" \
  "v_and_or_b32 v20, v20, -2, s59\n" \
  "s_waitcnt lgkmcnt(2)\n" \
  "v_mfma_f32_16x16x32_bf16 v[120:123], v[216:219], v[104:107], v[120:123]\n" \
  "v_mfma_f32_16x16x32_bf16 v[124:127], v[248:251], v[104:107], v[124:127]\n" \
  "buffer_store_dwordx4 v[20:23], v4, s[40:43], s54 offen sc1\n" \
  "s_waitcnt lgkmcnt(0)\n" \
  "v_mfma_f32_16x16x32_bf16 v[120:123], v[220:223], v[108:111], v[120:123]\n" \
  "v_mfma_f32_16x16x32_bf16 v[124:127], v[252:255], v[108:111], v[124:127]\n" \
  "s_xor_b32 s64, s54, 16384\n" \
  "s_mov_b32 s61, 1\n" \
  "s_nop 3\n" \
  "v_cvt_pk_bf16_f32 v24, v120, v121\n" \
  "v_cvt_pk_bf16_f32 v25, v122, v123\n" \
  "v_cvt_pk_bf16_f32 v26, v124, v125\n" \
  "v_cvt_pk_bf16_f32 v27, v126, v127\n" \
  "v_and_or_b32 v24, v24, -2, s59\n" \
  "buffer_store_dwordx4 v[24:27], v4, s[40:43], s64 offen sc1\n" \
  "v_readfirstlane_b32 s73, v37\n" \
  "s_sub_u32 s63, s38, s69\n" \
  "s_bfe_u32 s64, s73, 0x80010\n" \
  "s_sub_u32 s64, s64, s67\n" \
  "s_or_b32 s63, s63, s64\n" \
  "s_bfe_u32 s64, s73, 0x80008\n" \
  "s_and_b32 s73, s73, 0xff\n" \
  "s_min_u32 s73, s73, s64\n" \
  "s_or_b32 s63, s63, s68\n" \
  "s_cmp_lt_i32 s63, 0\n" \
  "s_cbranch_scc1 .LCX_E5_%=\n" \
  "v_mov_b32 v35, 0\n" \
  "global_load_dwordx4 v[128:131], v5, s[48:49] offset:0 sc1\n" \
  "global_load_dwordx4 v[132:135], v5, s[48:49] offset:1024 sc1\n" \
  "global_load_dwordx4 v[136:139], v3, s[48:49] offset:0 sc1\n" \
  "global_load_dwordx4 v[140:143], v3, s[48:49] offset:512 sc1\n" \
  "global_load_dword v35, v35, s[50:51] sc1\n" \
  "global_load_dword v146, v2, s[52:53] sc1\n" \
  "s_mov_b32 s68, -2\n" \
  ".LCX_E5_%=:\n" \
  "s_setprio 0\n" \
  "s_add_u32 s54, s54, 0x80000\n" \
  "s_bitset0_b32 s54, 22\n" \
  "s_sub_u32 s60, s60, 1\n" \
  "s_cmp_lg_u32 s60, 0\n" \
  "s_cbranch_scc1 .LCX_TOP_%=\n" \
  "s_waitcnt vmcnt(0)\n" \
  ".LCX_END_%=:\n" \
  "s_endpgm\n" \
  ".LCX_FAIL_%=:\n" \
  "v_mov_b64 v[28:29], 0\n" \
  "v_mov_b64 v[30:31], 0\n" \
  ".LCX_FAIL2_%=:\n" \
  "s_add_u32 s69, s64, -52\n" \
  "s_cmp_lg_u32 s64, 0\n" \
  "s_cbranch_scc1 .LCX_WF_%=\n" \
  "s_sleep 4\n" \
  "global_load_dword v35, v35, s[50:51] sc1\n" \
  "s_sub_u32 s62, s62, 8\n" \
  "s_cbranch_scc1 .LCX_END_%=\n" \
  "s_waitcnt vmcnt(0)\n" \
  "v_readfirstlane_b32 s64, v35\n" \
  "s_branch .LCX_FAIL2_%=\n"
#define AW_ASM_Y \
  "s_mov_b32 s72, 0\n" \
  "s_mov_b32 s67, 0\n" \
  "s_mov_b32 s68, 2\n" \
  "s_mov_b32 s69, 0\n" \
  "s_mov_b32 s65, 49152\n" \
  "s_mov_b32 s66, 16384\n" \
  "v_mov_b32 v5, s58\n" \
  "v_mov_b32 v60, v3\n" \
  "v_add_u32 v3, 4096, v2\n" \
  "s_mov_b32 s62, 0x40000\n" \
  "s_setprio 1\n" \
  "s_branch .LCY_ASK_%=\n" \
  ".p2align 6\n" \
  ".LCY_BAR_%=:\n" \
  "s_barrier\n" \
  "s_memrealtime s[38:39]\n" \
  "ds_read_b128 v[8:11], v0 offset:0\n" \
  "ds_read_b128 v[12:15], v0 offset:1024\n" \
  "ds_read_b128 v[16:19], v0 offset:2048\n" \
  "s_sub_u32 s59, s60, 1\n" \
  "s_bfe_u32 s59, s59, 0x10003\n" \
  "s_add_u32 s68, s68, 1\n" \
  "s_add_u32 s64, s54, 16384\n" \
  "s_cmp_eq_u32 s69, 0\n" \
  "s_cbranch_scc1 .LCY_NI_%=\n" \
  "s_waitcnt vmcnt(3)\n" \
  "s_cmp_lg_u32 s72, 0\n" \
  "s_cbranch_scc1 .LCY_NJ_%=\n" \
  "s_waitcnt vmcnt(0)\n" \
  ".LCY_NJ_%=:\n" \
  "v_mov_b32 v6, s55\n" \
  "s_mov_b64 exec, 1\n" \
  "ds_add_u32 v5, v6\n" \
  "s_mov_b64 exec, -1\n" \
  "s_mov_b32 s69, 0\n" \
  ".LCY_NI_%=:\n" \
  "s_waitcnt lgkmcnt(0)\n" \
  "s_add_u32 s63, s38, 205\n" \
  "s_mov_b32 s72, 0\n" \
  "v_or_b32 v7, 1, s63\n" \
  "s_mov_b64 exec, 1\n" \
  "global_store_dword v2, v7, s[52:53] sc1\n" \
  "s_mov_b64 exec, -1\n" \
  "s_add_u32 s52, s52, 2048\n" \
  "s_addc_u32 s53, s53, 0\n" \
  "v_cmp_eq_u32 vcc, 0, v4\n" \
  "s_cmp_eq_u64 vcc, 0\n" \
  "s_cbranch_scc0 .LCY_NS_%=\n" \
  "v_lshlrev_b32 v6, 16, s55\n" \
  "s_mov_b64 exec, 1\n" \
  "ds_add_u32 v5, v6\n" \
  "s_mov_b64 exec, -1\n" \
  "s_mov_b32 m0, s56\n" \
  "s_add_u32 s71, s56, 4096\n" \
  "global_load_lds_dwordx4 v2, s[48:49] offset:0 sc1\n" \
  "global_load_lds_dwordx4 v2, s[48:49] offset:1024 sc1\n" \
  "global_load_lds_dwordx4 v2, s[48:49] offset:2048 sc1\n" \
  "global_load_lds_dwordx4 v2, s[48:49] offset:3072 sc1\n" \
  ".LCY_NS_%=:\n" \
  "ds_read_b128 v[20:23], v0 offset:3072\n" \
  "v_mfma_f32_16x16x32_bf16 v[32:35], v[64:67], v[8:11], 0\n" \
  "v_mfma_f32_16x16x32_bf16 v[36:39], v[96:99], v[8:11], 0\n" \
  "v_mfma_f32_16x16x32_bf16 v[40:43], v[128:131], v[8:11], 0\n" \
  "v_mfma_f32_16x16x32_bf16 v[44:47], v[160:163], v[8:11], 0\n" \
  "v_mfma_f32_16x16x32_bf16 v[48:51], v[192:195], v[8:11], 0\n" \
  "v_mfma_f32_16x16x32_bf16 v[52:55], v[224:227], v[8:11], 0\n" \
  "ds_read_b128 v[8:11], v0 offset:8192\n" \
  "v_mfma_f32_16x16x32_bf16 v[32:35], v[68:71], v[12:15], v[32:35]\n" \
  "v_mfma_f32_16x16x32_bf16 v[36:39], v[100:103], v[12:15], v[36:39]\n" \
  "v_mfma_f32_16x16x32_bf16 v[40:43], v[132:135], v[12:15], v[40:43]\n" \
  "v_mfma_f32_16x16x32_bf16 v[44:47], v[164:167], v[12:15], v[44:47]\n" \
  "v_mfma_f32_16x16x32_bf16 v[48:51], v[196:199], v[12:15], v[48:51]\n" \
  "v_mfma_f32_16x16x32_bf16 v[52:55], v[228:231], v[12:15], v[52:55]\n" \
  "s_cbranch_vccnz .LCY_D1_%=\n" \
  "s_mov_b32 m0, s71\n" \
  "s_add_u32 s56, s56, s65\n" \
  "global_load_lds_dwordx4 v3, s[48:49] offset:0 sc1\n" \
  ".LCY_D1_%=:\n" \
  "ds_read_b128 v[12:15], v0 offset:9216\n" \
  "v_mfma_f32_16x16x32_bf16 v[32:35], v[72:75], v[16:19], v[32:35]\n" \
  "v_mfma_f32_16x16x32_bf16 v[36:39], v[104:107], v[16:19], v[36:39]\n" \
  "v_mfma_f32_16x16x32_bf16 v[40:43], v[136:139], v[16:19], v[40:43]\n" \
  "v_mfma_f32_16x16x32_bf16 v[44:47], v[168:171], v[16:19], v[44:47]\n" \
  "v_mfma_f32_16x16x32_bf16 v[48:51], v[200:203], v[16:19], v[48:51]\n" \
  "v_mfma_f32_16x16x32_bf16 v[52:55], v[232:235], v[16:19], v[52:55]\n" \
  "s_cbranch_vccnz .LCY_D2_%=\n" \
  "global_load_lds_dwordx4 v3, s[48:49] offset:1024 sc1\n" \
  ".LCY_D2_%=:\n" \
  "ds_read_b128 v[16:19], v0 offset:10240\n" \
  "s_waitcnt lgkmcnt(3)\n" \
  "v_mfma_f32_16x16x32_bf16 v[32:35], v[76:79], v[20:23], v[32:35]\n" \
  "v_mfma_f32_16x16x32_bf16 v[36:39], v[108:111], v[20:23], v[36:39]\n" \
  "v_mfma_f32_16x16x32_bf16 v[40:43], v[140:143], v[20:23], v[40:43]\n" \
  "v_mfma_f32_16x16x32_bf16 v[44:47], v[172:175], v[20:23], v[44:47]\n" \
  "v_mfma_f32_16x16x32_bf16 v[48:51], v[204:207], v[20:23], v[48:51]\n" \
  "v_mfma_f32_16x16x32_bf16 v[52:55], v[236:239], v[20:23], v[52:55]\n" \
  "s_cbranch_vccnz .LCY_D3_%=\n" \
  "global_load_lds_dwordx4 v3, s[48:49] offset:2048 sc1\n" \
  ".LCY_D3_%=:\n" \
  "ds_read_b128 v[20:23], v0 offset:11264\n" \
  "s_waitcnt lgkmcnt(3)\n" \
  "v_mfma_f32_16x16x32_bf16 v[32:35], v[80:83], v[8:11], v[32:35]\n" \
  "v_mfma_f32_16x16x32_bf16 v[36:39], v[112:115], v[8:11], v[36:39]\n" \
  "v_mfma_f32_16x16x32_bf16 v[40:43], v[144:147], v[8:11], v[40:43]\n" \
  "v_mfma_f32_16x16x32_bf16 v[44:47], v[176:179], v[8:11], v[44:47]\n" \
  "v_mfma_f32_16x16x32_bf16 v[48:51], v[208:211], v[8:11], v[48:51]\n" \
  "v_mfma_f32_16x16x32_bf16 v[52:55], v[240:243], v[8:11], v[52:55]\n" \
  "s_cbranch_vccnz .LCY_D4_%=\n" \
  "global_load_lds_dwordx4 v3, s[48:49] offset:3072 sc1\n" \
  "s_add_u32 s48, s48, 0x80000\n" \
  "s_addc_u32 s49, s49, 0\n" \
  ".LCY_D4_%=:\n" \
  "ds_read_b128 v[8:11], v0 offset:4096\n" \
  "s_waitcnt lgkmcnt(3)\n" \
  "v_mfma_f32_16x16x32_bf16 v[32:35], v[84:87], v[12:15], v[32:35]\n" \
  "v_mfma_f32_16x16x32_bf16 v[36:39], v[116:119], v[12:15], v[36:39]\n" \
  "v_mfma_f32_16x16x32_bf16 v[40:43], v[148:151], v[12:15], v[40:43]\n" \
  "v_mfma_f32_16x16x32_bf16 v[44:47], v[180:183], v[12:15], v[44:47]\n" \
  "v_mfma_f32_16x16x32_bf16 v[48:51], v[212:215], v[12:15], v[48:51]\n" \
  "v_mfma_f32_16x16x32_bf16 v[52:55], v[244:247], v[12:15], v[52:55]\n" \
  "s_cbranch_vccnz .LCY_D5_%=\n" \
  "s_mov_b32 m0, s57\n" \
  "s_add_u32 s57, s57, s65\n" \
  "global_load_lds_dwordx4 v2, s[50:51] offset:0 sc1\n" \
  ".LCY_D5_%=:\n" \
  "ds_read_b128 v[12:15], v0 offset:5120\n" \
  "s_waitcnt lgkmcnt(3)\n" \
  "v_mfma_f32_16x16x32_bf16 v[32:35], v[88:91], v[16:19], v[32:35]\n" \
  "v_mfma_f32_16x16x32_bf16 v[36:39], v[120:123], v[16:19], v[36:39]\n" \
  "v_mfma_f32_16x16x32_bf16 v[40:43], v[152:155], v[16:19], v[40:43]\n" \
  "v_mfma_f32_16x16x32_bf16 v[44:47], v[184:187], v[16:19], v[44:47]\n" \
  "v_mfma_f32_16x16x32_bf16 v[48:51], v[216:219], v[16:19], v[48:51]\n" \
  "v_mfma_f32_16x16x32_bf16 v[52:55], v[248:251], v[16:19], v[52:55]\n" \
  "s_cbranch_vccnz .LCY_D6_%=\n" \
  "global_load_lds_dwordx4 v2, s[50:51] offset:1024 sc1\n" \
  "s_sub_u32 s65, 0, s65\n" \
  "s_add_u32 s67, s67, 4096\n" \
  ".LCY_D6_%=:\n" \
  "ds_read_b128 v[16:19], v0 offset:6144\n" \
  "s_waitcnt lgkmcnt(3)\n" \
  "v_mfma_f32_16x16x32_bf16 v[32:35], v[92:95], v[20:23], v[32:35]\n" \
  "v_mfma_f32_16x16x32_bf16 v[36:39], v[124:127], v[20:23], v[36:39]\n" \
  "v_mfma_f32_16x16x32_bf16 v[40:43], v[156:159], v[20:23], v[40:43]\n" \
  "v_mfma_f32_16x16x32_bf16 v[44:47], v[188:191], v[20:23], v[44:47]\n" \
  "v_mfma_f32_16x16x32_bf16 v[48:51], v[220:223], v[20:23], v[48:51]\n" \
  "v_mfma_f32_16x16x32_bf16 v[52:55], v[252:255], v[20:23], v[52:55]\n" \
  "ds_read_b128 v[20:23], v0 offset:7168\n" \
  "s_waitcnt lgkmcnt(3)\n" \
  "s_nop 1\n" \
  "v_cvt_pk_bf16_f32 v24, v32, v33\n" \
  "v_cvt_pk_bf16_f32 v25, v34, v35\n" \
  "v_cvt_pk_bf16_f32 v26, v36, v37\n" \
  "v_cvt_pk_bf16_f32 v27, v38, v39\n" \
  "s_nop 0\n" \
  "v_mfma_f32_16x16x32_bf16 v[32:35], v[64:67], v[8:11], 0\n" \
  "v_mfma_f32_16x16x32_bf16 v[36:39], v[96:99], v[8:11], 0\n" \
  "v_and_or_b32 v24, v24, -2, s59\n" \
  "buffer_store_dwordx4 v[24:27], v1, s[40:43], s54 offen offset:0 sc1\n" \
  "v_cvt_pk_bf16_f32 v28, v40, v41\n" \
  "v_cvt_pk_bf16_f32 v29, v42, v43\n" \
  "v_cvt_pk_bf16_f32 v30, v44, v45\n" \
  "v_cvt_pk_bf16_f32 v31, v46, v47\n" \
  "s_nop 0\n" \
  "v_mfma_f32_16x16x32_bf16 v[40:43], v[128:131], v[8:11], 0\n" \
  "v_mfma_f32_16x16x32_bf16 v[44:47], v[160:163], v[8:11], 0\n" \
  "v_and_or_b32 v28, v28, -2, s59\n" \
  "buffer_store_dwordx4 v[28:31], v1, s[40:43], s54 offen offset:128 sc1\n" \
  "v_cvt_pk_bf16_f32 v56, v48, v49\n" \
  "v_cvt_pk_bf16_f32 v57, v50, v51\n" \
  "v_cvt_pk_bf16_f32 v58, v52, v53\n" \
  "v_cvt_pk_bf16_f32 v59, v54, v55\n" \
  "s_nop 0\n" \
  "v_mfma_f32_16x16x32_bf16 v[48:51], v[192:195], v[8:11], 0\n" \
  "v_mfma_f32_16x16x32_bf16 v[52:55], v[224:227], v[8:11], 0\n" \
  "v_and_or_b32 v56, v56, -2, s59\n" \
  "buffer_store_dwordx4 v[56:59], v1, s[40:43], s54 offen offset:256 sc1\n" \
  "ds_read_b128 v[8:11], v0 offset:12288\n" \
  "s_waitcnt lgkmcnt(3)\n" \
  "v_mfma_f32_16x16x32_bf16 v[32:35], v[68:71], v[12:15], v[32:35]\n" \
  "v_mfma_f32_16x16x32_bf16 v[36:39], v[100:103], v[12:15], v[36:39]\n" \
  "v_mfma_f32_16x16x32_bf16 v[40:43], v[132:135], v[12:15], v[40:43]\n" \
  "v_mfma_f32_16x16x32_bf16 v[44:47], v[164:167], v[12:15], v[44:47]\n" \
  "v_mfma_f32_16x16x32_bf16 v[48:51], v[196:199], v[12:15], v[48:51]\n" \
  "v_mfma_f32_16x16x32_bf16 v[52:55], v[228:231], v[12:15], v[52:55]\n" \
  "s_cbranch_vccnz .LCY_D7_%=\n" \
  "global_load_lds_dwordx4 v2, s[50:51] offset:2048 sc1\n" \
  "s_sub_u32 s68, s68, 1\n" \
  "s_mov_b32 s69, 1\n" \
  ".LCY_D7_%=:\n" \
  "ds_read_b128 v[12:15], v0 offset:13312\n" \
  "s_waitcnt lgkmcnt(3)\n" \
  "v_mfma_f32_16x16x32_bf16 v[32:35], v[72:75], v[16:19], v[32:35]\n" \
  "v_mfma_f32_16x16x32_bf16 v[36:39], v[104:107], v[16:19], v[36:39]\n" \
  "v_mfma_f32_16x16x32_bf16 v[40:43], v[136:139], v[16:19], v[40:43]\n" \
  "v_mfma_f32_16x16x32_bf16 v[44:47], v[168:171], v[16:19], v[44:47]\n" \
  "v_mfma_f32_16x16x32_bf16 v[48:51], v[200:203], v[16:19], v[48:51]\n" \
  "v_mfma_f32_16x16x32_bf16 v[52:55], v[232:235], v[16:19], v[52:55]\n" \
  "s_cbranch_vccnz .LCY_D8_%=\n" \
  "global_load_lds_dwordx4 v2, s[50:51] offset:3072 sc1\n" \
  "s_add_u32 s50, s50, 0x40000\n" \
  "s_addc_u32 s51, s51, 0\n" \
  ".LCY_D8_%=:\n" \
  "ds_read_b128 v[16:19], v0 offset:14336\n" \
  "s_waitcnt lgkmcnt(3)\n" \
  "v_mfma_f32_16x16x32_bf16 v[32:35], v[76:79], v[20:23], v[32:35]\n" \
  "v_mfma_f32_16x16x32_bf16 v[36:39], v[108:111], v[20:23], v[36:39]\n" \
  "v_mfma_f32_16x16x32_bf16 v[40:43], v[140:143], v[20:23], v[40:43]\n" \
  "v_mfma_f32_16x16x32_bf16 v[44:47], v[172:175], v[20:23], v[44:47]\n" \
  "v_mfma_f32_16x16x32_bf16 v[48:51], v[204:207], v[20:23], v[48:51]\n" \
  "v_mfma_f32_16x16x32_bf16 v[52:55], v[236:239], v[20:23], v[52:55]\n" \
  "ds_read_b128 v[20:23], v0 offset:15360\n" \
  "s_waitcnt lgkmcnt(3)\n" \
  "v_mfma_f32_16x16x32_bf16 v[32:35], v[80:83], v[8:11], v[32:35]\n" \
  "v_mfma_f32_16x16x32_bf16 v[36:39], v[112:115], v[8:11], v[36:39]\n" \
  "v_mfma_f32_16x16x32_bf16 v[40:43], v[144:147], v[8:11], v[40:43]\n" \
  "v_mfma_f32_16x16x32_bf16 v[44:47], v[176:179], v[8:11], v[44:47]\n" \
  "v_mfma_f32_16x16x32_bf16 v[48:51], v[208:211], v[8:11], v[48:51]\n" \
  "v_mfma_f32_16x16x32_bf16 v[52:55], v[240:243], v[8:11], v[52:55]\n" \
  "s_waitcnt lgkmcnt(2)\n" \
  "v_mfma_f32_16x16x32_bf16 v[32:35], v[84:87], v[12:15], v[32:35]\n" \
  "v_mfma_f32_16x16x32_bf16 v[36:39], v[116:119], v[12:15], v[36:39]\n" \
  "v_mfma_f32_16x16x32_bf16 v[40:43], v[148:151], v[12:15], v[40:43]\n" \
  "v_mfma_f32_16x16x32_bf16 v[44:47], v[180:183], v[12:15], v[44:47]\n" \
  "v_mfma_f32_16x16x32_bf16 v[48:51], v[212:215], v[12:15], v[48:51]\n" \
  "v_mfma_f32_16x16x32_bf16 v[52:55], v[244:247], v[12:15], v[52:55]\n" \
  "s_waitcnt lgkmcnt(1)\n" \
  "v_mfma_f32_16x16x32_bf16 v[32:35], v[88:91], v[16:19], v[32:35]\n" \
  "v_mfma_f32_16x16x32_bf16 v[36:39], v[120:123], v[16:19], v[36:39]\n" \
  "v_mfma_f32_16x16x32_bf16 v[40:43], v[152:155], v[16:19], v[40:43]\n" \
  "v_mfma_f32_16x16x32_bf16 v[44:47], v[184:187], v[16:19], v[44:47]\n" \
  "v_mfma_f32_16x16x32_bf16 v[48:51], v[216:219], v[16:19], v[48:51]\n" \
  "v_mfma_f32_16x16x32_bf16 v[52:55], v[248:251], v[16:19], v[52:55]\n" \
  "s_cbranch_vccnz .LCY_NT_%=\n" \
  "v_mov_b32 v4, 0\n" \
  "buffer_load_dword v4, v60, s[44:47], s67 offen sc1\n" \
  ".LCY_NT_%=:\n" \
  "s_waitcnt lgkmcnt(0)\n" \
  "v_mfma_f32_16x16x32_bf16 v[32:35], v[92:95], v[20:23], v[32:35]\n" \
  "v_mfma_f32_16x16x32_bf16 v[36:39], v[124:127], v[20:23], v[36:39]\n" \
  "v_mfma_f32_16x16x32_bf16 v[40:43], v[156:159], v[20:23], v[40:43]\n" \
  "v_mfma_f32_16x16x32_bf16 v[44:47], v[188:191], v[20:23], v[44:47]\n" \
  "v_mfma_f32_16x16x32_bf16 v[48:51], v[220:223], v[20:23], v[48:51]\n" \
  "v_mfma_f32_16x16x32_bf16 v[52:55], v[252:255], v[20:23], v[52:55]\n" \
  "v_add_u32 v0, s66, v0\n" \
  "s_sub_u32 s66, 0, s66\n" \
  "s_add_u32 s54, s54, 0x80000\n" \
  "s_bitset0_b32 s54, 22\n" \
  "s_mov_b32 s62, 0x40000\n" \
  "s_nop 0\n" \
  "v_cvt_pk_bf16_f32 v24, v32, v33\n" \
  "v_cvt_pk_bf16_f32 v25, v34, v35\n" \
  "v_cvt_pk_bf16_f32 v26, v36, v37\n" \
  "v_cvt_pk_bf16_f32 v27, v38, v39\n" \
  "v_and_or_b32 v24, v24, -2, s59\n" \
  "buffer_store_dwordx4 v[24:27], v1, s[40:43], s64 offen offset:0 sc1\n" \
  "v_cvt_pk_bf16_f32 v28, v40, v41\n" \
  "v_cvt_pk_bf16_f32 v29, v42, v43\n" \
  "v_cvt_pk_bf16_f32 v30, v44, v45\n" \
  "v_cvt_pk_bf16_f32 v31, v46, v47\n" \
  "v_and_or_b32 v28, v28, -2, s59\n" \
  "buffer_store_dwordx4 v[28:31], v1, s[40:43], s64 offen offset:128 sc1\n" \
  "v_cvt_pk_bf16_f32 v56, v48, v49\n" \
  "v_cvt_pk_bf16_f32 v57, v50, v51\n" \
  "v_cvt_pk_bf16_f32 v58, v52, v53\n" \
  "v_cvt_pk_bf16_f32 v59, v54, v55\n" \
  "v_and_or_b32 v56, v56, -2, s59\n" \
  "buffer_store_dwordx4 v[56:59], v1, s[40:43], s64 offen offset:256 sc1\n" \
  "s_cbranch_vccnz .LCY_ND_%=\n" \
  "s_cmp_eq_u32 s68, 0\n" \
  "s_cselect_b32 s72, 1, 0\n" \
  "s_cbranch_scc1 .LCY_DEF_%=\n" \
  "s_waitcnt vmcnt(0)\n" \
  "v_mov_b32 v6, s55\n" \
  "s_mov_b64 exec, 1\n" \
  "ds_add_u32 v5, v6\n" \
  "s_mov_b64 exec, -1\n" \
  "s_mov_b32 s69, 0\n" \
  "s_branch .LCY_WC_%=\n" \
  ".LCY_ND_%=:\n" \
  "v_mov_b32 v4, 0\n" \
  "buffer_load_dword v4, v60, s[44:47], s67 offen sc1\n" \
  "s_waitcnt vmcnt(0)\n" \
  ".LCY_WC_%=:\n" \
  ".LCY_DEF_%=:\n" \
  "s_sub_u32 s60, s60, 1\n" \
  "s_cmp_eq_u32 s60, 0\n" \
  "s_cbranch_scc1 .LCY_END_%=\n" \
  ".LCY_NEED_%=:\n" \
  "s_cmp_eq_u32 s68, 0\n" \
  "s_cbranch_scc1 .LCY_BAR_%=\n" \
  "v_cmp_eq_u32 vcc, 0, v4\n" \
  "s_cmp_eq_u64 vcc, 0\n" \
  "s_cbranch_scc1 .LCY_GET_%=\n" \
  "s_cmp_ge_u32 s67, 0x40000\n" \
  "s_cbranch_scc1 .LCY_BAR_%=\n" \
  "s_sleep 4\n" \
  "ds_read_b32 v6, v5 offset:16\n" \
  "s_waitcnt lgkmcnt(0)\n" \
  "v_readfirstlane_b32 s63, v6\n" \
  "s_add_u32 s63, s63, s60\n" \
  "s_cmp_ge_u32 s63, 66\n" \
  "s_cbranch_scc1 .LCY_BAR_%=\n" \
  ".LCY_ASK_%=:\n" \
  "v_mov_b32 v4, 0\n" \
  "buffer_load_dword v4, v60, s[44:47], s67 offen sc1\n" \
  "s_sub_u32 s62, s62, 1\n" \
  "s_cbranch_scc1 .LCY_END_%=\n" \
  "s_waitcnt vmcnt(0)\n" \
  "s_branch .LCY_NEED_%=\n" \
  ".LCY_GET_%=:\n" \
  "v_lshlrev_b32 v6, 16, s55\n" \
  "s_mov_b64 exec, 1\n" \
  "ds_add_u32 v5, v6\n" \
  "s_mov_b64 exec, -1\n" \
  "s_mov_b32 m0, s56\n" \
  "s_add_u32 s71, s56, 4096\n" \
  "global_load_lds_dwordx4 v2, s[48:49] offset:0 sc1\n" \
  "global_load_lds_dwordx4 v2, s[48:49] offset:1024 sc1\n" \
  "global_load_lds_dwordx4 v2, s[48:49] offset:2048 sc1\n" \
  "global_load_lds_dwordx4 v2, s[48:49] offset:3072 sc1\n" \
  "s_mov_b32 m0, s71\n" \
  "s_add_u32 s56, s56, s65\n" \
  "global_load_lds_dwordx4 v3, s[48:49] offset:0 sc1\n" \
  "global_load_lds_dwordx4 v3, s[48:49] offset:1024 sc1\n" \
  "global_load_lds_dwordx4 v3, s[48:49] offset:2048 sc1\n" \
  "global_load_lds_dwordx4 v3, s[48:49] offset:3072 sc1\n" \
  "s_add_u32 s48, s48, 0x80000\n" \
  "s_addc_u32 s49, s49, 0\n" \
  "s_mov_b32 m0, s57\n" \
  "s_add_u32 s57, s57, s65\n" \
  "global_load_lds_dwordx4 v2, s[50:51] offset:0 sc1\n" \
  "global_load_lds_dwordx4 v2, s[50:51] offset:1024 sc1\n" \
  "s_sub_u32 s65, 0, s65\n" \
  "s_add_u32 s67, s67, 4096\n" \
  "global_load_lds_dwordx4 v2, s[50:51] offset:2048 sc1\n" \
  "s_sub_u32 s68, s68, 1\n" \
  "s_mov_b32 s69, 1\n" \
  "global_load_lds_dwordx4 v2, s[50:51] offset:3072 sc1\n" \
  "s_add_u32 s50, s50, 0x40000\n" \
  "s_addc_u32 s51, s51, 0\n" \
  "v_mov_b32 v4, 0\n" \
  "buffer_load_dword v4, v60, s[44:47], s67 offen sc1\n" \
  "s_mov_b32 s61, 0\n" \
  "s_cmp_eq_u32 s68, 0\n" \
  "s_cbranch_scc1 .LCY_BAR_%=\n" \
  "s_waitcnt vmcnt(0)\n" \
  "v_mov_b32 v6, s55\n" \
  "s_mov_b64 exec, 1\n" \
  "ds_add_u32 v5, v6\n" \
  "s_mov_b64 exec, -1\n" \
  "s_mov_b32 s69, 0\n" \
  "s_branch .LCY_NEED_%=\n" \
  ".LCY_END_%=:\n" \
  "s_endpgm\n"
template <bool AT>
__device__ __forceinline__ void role_aw_t(const MegaArgs& A, char* smem, const int L, const int a, const u64 tag) {
  const int w = A.wv;
  const int l = lane_id(), c = l & 15, g = l >> 4;
  const int qt = w & 1, hh = (w >> 1) & 1, h = 2 * a + hh;
  char* ws = A.ws;
  unsigned* FL = (unsigned*)(ws + OFF_FL);
  const unsigned sb = lds_base(smem);
  if (w == 0 && l < 8) reinterpret_cast<unsigned*>(smem + AW_CNT)[l] = 0u;
  constexpr int NP = AT ? 1 : 3;
  const int cp0 = AT ? w : 3 * w - 8;
  v8u wo[2 * NP][4];
  {
    const int q = (l >> 2) & 3, p = l & 3, f = q | ((g & 1) << 2);
    const unsigned ra = sb + (4 * g + q) * 256 + (p & 1) * 16;
#define PW_CLEAR(n)                                                                                                         \
  _Pragma("unroll") for (int i = 0; i < 16; ++i)                                                                            \
    ST128((v4u{0u, 0u, 0u, 0u}), MKRS(ws + OFF_PW, SZ_PW), l * 16 + (i & 3) * 1024,                                         \
          ((((n) >> RGB) * 64 + ((n) & ((1 << RGB) - 1))) * 32 + 2 * a + qt) * 16384 + (i >> 2) * 4096);
    if (EF && AT && L && PC0 == 0) { PW_CLEAR(hh) }
#pragma unroll
    for (int r = 0; r < 2; ++r) {
#pragma unroll
      for (int k = 0; k < 2; ++k) {
        const char* base_ = (const char*)A.Wo + ((size_t)((2 * a + r) * DH + 64 * (w & 1) + 32 * k) * DM + 128 * (w >> 1)) * 2;
#pragma unroll
        for (int i = 0; i < 8; ++i) {
          const int fd = g | ((i & 1) << 2);
          glds16(base_, (unsigned)((4 * i + g) * (DM * 2) + ((c ^ (fd << 1)) << 4)), sb + w * 16384 + k * 8192 + i * 1024, WNT & 16);
        }
      }
      SST(4 + 3 * r);
      if (EF && AT && L && PC0 == 1 + r) { PW_CLEAR(hh) }
      if (r == 1) ready_wait(A, tag, ready_look(A));
      if (EF && AT && L && PC0 == 1 && r == 0) WAIT_VM(16);
      else WAIT_VM(0);
      if (EF && AT && r == (PC0 != 0) && L) {
        if (r == 0) ready_wait(A, tag, ready_look(A));
        PW_FREE(0, hh, 2 * a + qt);
      }
      RAW_BAR();
      SST(5 + 3 * r);
#pragma unroll
      for (int k2 = 0; k2 < 2; ++k2) {
#pragma unroll
        for (int ct = 0; ct < 2 * NP; ++ct) {
          const int cp = cp0 + (ct >> 1);
          const unsigned a_ = ra + (cp >> 2) * 32768 + k2 * 16384 + ((2 * (cp & 3) + (p >> 1)) ^ f) * 32 + (ct & 1) * 8;
          const v2u r0 = trd(a_), r1 = trd(a_ + 4096), r2 = trd(a_ + 8192), r3 = trd(a_ + 12288);
          wo[ct][2 * r + k2] = v8u{r0.x, r0.y, r1.x, r1.y, r2.x, r2.y, r3.x, r3.y};
          asm volatile("" : "+v"(wo[ct][2 * r + k2]));
        }
      }
      RAW_BAR();
      SST(6 + 3 * r);
    }
  }
  WSTAMP2();
  if (EF && L) {
    const unsigned t0 = now10();
#pragma unroll 1
    for (int n = 2 + (w >> 1); n < (2 << RGB); n += 4) {
      gate(t0 + PCT + (n - 2) * PCD);
      PW_CLEAR(n)
      DRAIN();
      PW_FREE(n >> RGB, n & ((1 << RGB) - 1), 2 * a + qt);
    }
  }
  const v4u rsW = mk_srd(ws + OFF_PW, SZ_PW);
  const int dp = (FL_FP - FL_FQ + (L * 16 + a - L * 64 * 32 - h) * 32 + 2 * hh) * 4;
  const v4u rsF = mk_srd(FL + FL_FQ + (L * 64 * 32 + h) * 32, (qt ? 0 : dp) + 64 * 4096 - 128);
  const char* p0 = AT ? ws + OFF_MEMK + L * SZ_MEM + h * 2048 : ws + OFF_QK + L * SZ_QK + ((size_t)h * 16 + 8 * qt) * 1024;
  const char* p1 = AT ? (const char*)(FL + FL_FK + L * 32 + (h >> 3) * 64 + (h & 7) * 2) : ws + OFF_VF + L * SZ_O + ((size_t)h * 8 + 4 * qt) * 1024;
  const unsigned* pFW = EF && AT ? FL + FL_FD + L * 32 : FL + FL_FW + (L * 64 * 16 + (a >> 2)) * 32 + (a & 3) + (EF ? (w & 3) * 128 : 0);
  const unsigned tk = sb + AW_TK + hh * 24 * 1024, voff = (c >> 1) * 2048 + (c & 1) * 64 + g * 16;
  const char* dbg = ws + WS_TOTAL + A.vb * 64 * 8;
#define AW_SOPS                                                                                                                 \
  "{s[40:43]}"(rsW), "{s[44:47]}"(rsF), "{s[48:49]}"(p0), "{s[50:51]}"(p1), "{s[52:53]}"(pFW),                                   \
      "{s54}"((L * 64 * 16 + a) * 32768 + cp0 * 128 + (AT ? qt * 16384 : 0)), "{s55}"(1 << (8 * qt)), "{s56}"(tk + qt * 8192), "{s57}"(tk + 16384 + qt * 4096),                  \
      "{s58}"(sb + AW_CNT + 8 + 4 * hh), "{s59}"(sb + AW_CNT), "{s60}"(NSEG), "{s[36:37]}"(dbg)                                  \
      : "memory", "vcc", "scc", "s80"
  if constexpr (AT) {
    const v8u pv = {tk + l * 16, (c & 8) ? 0xEEEEu : 0x4444u, (l & 31) * 4u, (unsigned)(OFF_MEMV - OFF_MEMK) + c * 32 + (g & 1) * 16 + (g >> 1) * 1024,
                    voff, (unsigned)l * 16, tk + qt * 4096 + l * 16, sb + AW_SO + w * 4096 + l * 16};
    const unsigned neg = 0xff61b1e6u;
    v8u bt;
#pragma unroll
    for (int i = 0; i < 4; ++i) {
      const unsigned d = 4 * g + i > c ? neg : 0u;
      bt[i] = qt ? 0u : d;
      bt[4 + i] = qt ? d : neg;
    }
    asm volatile("s_mov_b64 exec, -1\ns_waitcnt lgkmcnt(0)\ns_nop 4\n" AW_ASM_X ".LKG_LAST:\n"
                 ::"{v[192:199]}"(wo[0][0]), "{v[200:207]}"(wo[0][1]), "{v[208:215]}"(wo[0][2]), "{v[216:223]}"(wo[0][3]),
                 "{v[224:231]}"(wo[1][0]), "{v[232:239]}"(wo[1][1]), "{v[240:247]}"(wo[1][2]), "{v[248:255]}"(wo[1][3]),
                 "{v[0:7]}"(pv), "{v[8:15]}"(bt), AW_SOPS);
  } else {
    const v4u pv = {sb + AW_SO + l * 16, voff, (unsigned)l * 16, (l & 2) ? 16u + 8 * qt : qt ? 8u * (l & 1) : dp + 4 * (l & 1)};
    asm volatile("s_mov_b64 exec, -1\ns_waitcnt vmcnt(0) lgkmcnt(0)\ns_nop 4\n" AW_ASM_Y
                 ::"{v[64:71]}"(wo[0][0]), "{v[72:79]}"(wo[0][1]), "{v[80:87]}"(wo[0][2]), "{v[88:95]}"(wo[0][3]),
                 "{v[96:103]}"(wo[1][0]), "{v[104:111]}"(wo[1][1]), "{v[112:119]}"(wo[1][2]), "{v[120:127]}"(wo[1][3]),
                 "{v[128:135]}"(wo[2][0]), "{v[136:143]}"(wo[2][1]), "{v[144:151]}"(wo[2][2]), "{v[152:159]}"(wo[2][3]),
                 "{v[160:167]}"(wo[3][0]), "{v[168:175]}"(wo[3][1]), "{v[176:183]}"(wo[3][2]), "{v[184:191]}"(wo[3][3]),
                 "{v[192:199]}"(wo[4][0]), "{v[200:207]}"(wo[4][1]), "{v[208:215]}"(wo[4][2]), "{v[216:223]}"(wo[4][3]),
                 "{v[224:231]}"(wo[5][0]), "{v[232:239]}"(wo[5][1]), "{v[240:247]}"(wo[5][2]), "{v[248:255]}"(wo[5][3]),
                 "{v[0:3]}"(pv), AW_SOPS);
  }
#undef AW_SOPS
}
__device__ __forceinline__ void role_aw(const MegaArgs& A, char* smem, const int L, const int a, const u64 tag) {
  if (A.wv < 4) role_aw_t<true>(A, smem, L, a, tag);
  else role_aw_t<false>(A, smem, L, a, tag);
}

#define P_ASM \
  "s_mov_b32 s68, 0\n" \
  "s_mov_b32 s66, 3\n" \
  "s_mov_b32 s67, 2\n" \
  "s_mov_b32 s72, 0\n" \
  "v_mov_b32 v7, 1\n" \
  "v_mov_b32 v6, s70\n" \
  "v_mov_b32 v5, 0\n" \
  "v_add_u32 v10, 4096, v2\n" \
  ".p2align 6\n" \
  ".LCP_TOP_%=:\n" \
  "ds_read_b128 v[28:31], v1 offset:0\n" \
  "ds_read_b128 v[32:35], v1 offset:1024\n" \
  "s_add_u32 s66, s66, 3\n" \
  "s_add_u32 s67, s67, 2\n" \
  "s_mov_b32 s69, 0x400000\n" \
  "v_xor_b32 v0, 0x8000, v0\n" \
  "v_xor_b32 v3, 0x100, v3\n" \
  "v_readfirstlane_b32 s71, v5\n" \
  "s_cmp_ge_u32 s71, s66\n" \
  "s_cbranch_scc1 .LCP_GO_%=\n" \
  "s_cmp_eq_u32 s68, 1\n" \
  "s_cbranch_scc0 .LCP_X1_%=\n" \
  "s_waitcnt vmcnt(0)\n" \
  "s_mov_b64 exec, 1\n" \
  "buffer_store_dword v7, off, s[56:59], s72 sc1\n" \
  "s_mov_b64 exec, -1\n" \
  "s_add_u32 s72, s72, 0x1000\n" \
  "s_mov_b32 s68, 0\n" \
  ".LCP_X1_%=:\n" \
  ".LCP_S2_%=:\n" \
  "s_sleep 1\n" \
  "ds_read_b32 v5, v6 offset:0\n" \
  "s_sub_u32 s69, s69, 1\n" \
  "s_cbranch_scc1 .LCP_END_%=\n" \
  "s_waitcnt lgkmcnt(0)\n" \
  "v_readfirstlane_b32 s71, v5\n" \
  "s_cmp_ge_u32 s71, s66\n" \
  "s_cbranch_scc1 .LCP_GO_%=\n" \
  "s_branch .LCP_S2_%=\n" \
  ".LCP_GO_%=:\n" \
  "ds_read_b128 v[12:15], v0 offset:0\n" \
  "ds_read_b128 v[16:19], v0 offset:2048\n" \
  "ds_read_b128 v[20:23], v0 offset:4096\n" \
  "ds_read_b128 v[24:27], v0 offset:6144\n" \
  "ds_read_b128 v[36:39], v1 offset:2048\n" \
  "s_waitcnt lgkmcnt(4)\n" \
  "v_mfma_f32_16x16x32_bf16 v[48:51], v[64:67], v[12:15], 0\n" \
  "v_mfma_f32_16x16x32_bf16 v[52:55], v[128:131], v[12:15], 0\n" \
  "v_mfma_f32_16x16x32_bf16 v[56:59], v[192:195], v[12:15], 0\n" \
  "v_mfma_f32_16x16x32_bf16 v[60:63], v[28:31], v[12:15], 0\n" \
  "ds_read_b128 v[12:15], v0 offset:8192\n" \
  "ds_read_b128 v[28:31], v1 offset:3072\n" \
  "s_waitcnt lgkmcnt(5)\n" \
  "v_mfma_f32_16x16x32_bf16 v[48:51], v[68:71], v[16:19], v[48:51]\n" \
  "v_mfma_f32_16x16x32_bf16 v[52:55], v[132:135], v[16:19], v[52:55]\n" \
  "v_mfma_f32_16x16x32_bf16 v[56:59], v[196:199], v[16:19], v[56:59]\n" \
  "v_mfma_f32_16x16x32_bf16 v[60:63], v[32:35], v[16:19], v[60:63]\n" \
  "ds_read_b128 v[16:19], v0 offset:10240\n" \
  "ds_read_b128 v[32:35], v1 offset:4096\n" \
  "s_waitcnt lgkmcnt(6)\n" \
  "v_mfma_f32_16x16x32_bf16 v[48:51], v[72:75], v[20:23], v[48:51]\n" \
  "v_mfma_f32_16x16x32_bf16 v[52:55], v[136:139], v[20:23], v[52:55]\n" \
  "v_mfma_f32_16x16x32_bf16 v[56:59], v[200:203], v[20:23], v[56:59]\n" \
  "s_waitcnt lgkmcnt(4)\n" \
  "v_mfma_f32_16x16x32_bf16 v[60:63], v[36:39], v[20:23], v[60:63]\n" \
  "ds_read_b128 v[20:23], v0 offset:12288\n" \
  "ds_read_b128 v[36:39], v1 offset:5120\n" \
  "v_mfma_f32_16x16x32_bf16 v[48:51], v[76:79], v[24:27], v[48:51]\n" \
  "v_mfma_f32_16x16x32_bf16 v[52:55], v[140:143], v[24:27], v[52:55]\n" \
  "v_mfma_f32_16x16x32_bf16 v[56:59], v[204:207], v[24:27], v[56:59]\n" \
  "s_waitcnt lgkmcnt(4)\n" \
  "v_mfma_f32_16x16x32_bf16 v[60:63], v[28:31], v[24:27], v[60:63]\n" \
  "ds_read_b128 v[24:27], v0 offset:14336\n" \
  "ds_read_b128 v[28:31], v1 offset:6144\n" \
  "v_mfma_f32_16x16x32_bf16 v[48:51], v[80:83], v[12:15], v[48:51]\n" \
  "v_mfma_f32_16x16x32_bf16 v[52:55], v[144:147], v[12:15], v[52:55]\n" \
  "v_mfma_f32_16x16x32_bf16 v[56:59], v[208:211], v[12:15], v[56:59]\n" \
  "s_waitcnt lgkmcnt(4)\n" \
  "v_mfma_f32_16x16x32_bf16 v[60:63], v[32:35], v[12:15], v[60:63]\n" \
  "ds_read_b128 v[12:15], v0 offset:16384\n" \
  "ds_read_b128 v[32:35], v1 offset:7168\n" \
  "v_mfma_f32_16x16x32_bf16 v[48:51], v[84:87], v[16:19], v[48:51]\n" \
  "v_mfma_f32_16x16x32_bf16 v[52:55], v[148:151], v[16:19], v[52:55]\n" \
  "v_mfma_f32_16x16x32_bf16 v[56:59], v[212:215], v[16:19], v[56:59]\n" \
  "s_waitcnt lgkmcnt(4)\n" \
  "v_mfma_f32_16x16x32_bf16 v[60:63], v[36:39], v[16:19], v[60:63]\n" \
  "ds_read_b128 v[16:19], v0 offset:18432\n" \
  "ds_read_b128 v[36:39], v1 offset:8192\n" \
  "v_mfma_f32_16x16x32_bf16 v[48:51], v[88:91], v[20:23], v[48:51]\n" \
  "v_mfma_f32_16x16x32_bf16 v[52:55], v[152:155], v[20:23], v[52:55]\n" \
  "v_mfma_f32_16x16x32_bf16 v[56:59], v[216:219], v[20:23], v[56:59]\n" \
  "s_waitcnt lgkmcnt(4)\n" \
  "v_mfma_f32_16x16x32_bf16 v[60:63], v[28:31], v[20:23], v[60:63]\n" \
  "ds_read_b128 v[20:23], v0 offset:20480\n" \
  "ds_read_b128 v[28:31], v1 offset:9216\n" \
  "v_mfma_f32_16x16x32_bf16 v[48:51], v[92:95], v[24:27], v[48:51]\n" \
  "v_mfma_f32_16x16x32_bf16 v[52:55], v[156:159], v[24:27], v[52:55]\n" \
  "v_mfma_f32_16x16x32_bf16 v[56:59], v[220:223], v[24:27], v[56:59]\n" \
  "s_waitcnt lgkmcnt(4)\n" \
  "v_mfma_f32_16x16x32_bf16 v[60:63], v[32:35], v[24:27], v[60:63]\n" \
  "ds_read_b128 v[24:27], v0 offset:22528\n" \
  "ds_read_b128 v[32:35], v1 offset:10240\n" \
  "v_mfma_f32_16x16x32_bf16 v[48:51], v[96:99], v[12:15], v[48:51]\n" \
  "v_mfma_f32_16x16x32_bf16 v[52:55], v[160:163], v[12:15], v[52:55]\n" \
  "v_mfma_f32_16x16x32_bf16 v[56:59], v[224:227], v[12:15], v[56:59]\n" \
  "s_waitcnt lgkmcnt(4)\n" \
  "v_mfma_f32_16x16x32_bf16 v[60:63], v[36:39], v[12:15], v[60:63]\n" \
  "ds_read_b128 v[12:15], v0 offset:24576\n" \
  "ds_read_b128 v[36:39], v1 offset:11264\n" \
  "v_mfma_f32_16x16x32_bf16 v[48:51], v[100:103], v[16:19], v[48:51]\n" \
  "v_mfma_f32_16x16x32_bf16 v[52:55], v[164:167], v[16:19], v[52:55]\n" \
  "v_mfma_f32_16x16x32_bf16 v[56:59], v[228:231], v[16:19], v[56:59]\n" \
  "s_waitcnt lgkmcnt(4)\n" \
  "v_mfma_f32_16x16x32_bf16 v[60:63], v[28:31], v[16:19], v[60:63]\n" \
  "ds_read_b32 v5, v6 offset:16\n" \
  "ds_read_b128 v[16:19], v0 offset:26624\n" \
  "ds_read_b128 v[28:31], v1 offset:12288\n" \
  "v_mfma_f32_16x16x32_bf16 v[48:51], v[104:107], v[20:23], v[48:51]\n" \
  "v_mfma_f32_16x16x32_bf16 v[52:55], v[168:171], v[20:23], v[52:55]\n" \
  "v_mfma_f32_16x16x32_bf16 v[56:59], v[232:235], v[20:23], v[56:59]\n" \
  "s_waitcnt lgkmcnt(5)\n" \
  "v_mfma_f32_16x16x32_bf16 v[60:63], v[32:35], v[20:23], v[60:63]\n" \
  "ds_read_b128 v[20:23], v0 offset:28672\n" \
  "ds_read_b128 v[32:35], v1 offset:13312\n" \
  "v_mfma_f32_16x16x32_bf16 v[48:51], v[108:111], v[24:27], v[48:51]\n" \
  "v_mfma_f32_16x16x32_bf16 v[52:55], v[172:175], v[24:27], v[52:55]\n" \
  "v_mfma_f32_16x16x32_bf16 v[56:59], v[236:239], v[24:27], v[56:59]\n" \
  "s_waitcnt lgkmcnt(5)\n" \
  "v_mfma_f32_16x16x32_bf16 v[60:63], v[36:39], v[24:27], v[60:63]\n" \
  "s_cmp_eq_u32 s68, 1\n" \
  "s_cbranch_scc0 .LCP_X3_%=\n" \
  "s_waitcnt vmcnt(0)\n" \
  "s_mov_b64 exec, 1\n" \
  "buffer_store_dword v7, off, s[56:59], s72 sc1\n" \
  "s_mov_b64 exec, -1\n" \
  "s_add_u32 s72, s72, 0x1000\n" \
  "s_mov_b32 s68, 0\n" \
  ".LCP_X3_%=:\n" \
  "s_waitcnt lgkmcnt(4)\n" \
  "v_readfirstlane_b32 s71, v5\n" \
  "s_cmp_ge_u32 s71, s67\n" \
  "s_cbranch_scc1 .LCP_ST_%=\n" \
  ".LCP_S4_%=:\n" \
  "s_sleep 1\n" \
  "ds_read_b32 v5, v6 offset:16\n" \
  "s_sub_u32 s69, s69, 1\n" \
  "s_cbranch_scc1 .LCP_END_%=\n" \
  "s_waitcnt lgkmcnt(0)\n" \
  "v_readfirstlane_b32 s71, v5\n" \
  "s_cmp_ge_u32 s71, s67\n" \
  "s_cbranch_scc1 .LCP_ST_%=\n" \
  "s_branch .LCP_S4_%=\n" \
  ".LCP_ST_%=:\n" \
  "ds_read_b128 v[24:27], v0 offset:30720\n" \
  "ds_read_b128 v[36:39], v1 offset:14336\n" \
  "ds_read_b128 v[40:43], v4 offset:0\n" \
  "ds_read_b128 v[44:47], v4 offset:64\n" \
  "ds_read2_b32 v[8:9], v3 offset0:0 offset1:32\n" \
  "v_mfma_f32_16x16x32_bf16 v[48:51], v[112:115], v[12:15], v[48:51]\n" \
  "v_mfma_f32_16x16x32_bf16 v[52:55], v[176:179], v[12:15], v[52:55]\n" \
  "v_mfma_f32_16x16x32_bf16 v[56:59], v[240:243], v[12:15], v[56:59]\n" \
  "s_waitcnt lgkmcnt(7)\n" \
  "v_mfma_f32_16x16x32_bf16 v[60:63], v[28:31], v[12:15], v[60:63]\n" \
  "ds_read_b128 v[12:15], v0 offset:256\n" \
  "ds_read_b128 v[28:31], v1 offset:15360\n" \
  "v_mfma_f32_16x16x32_bf16 v[48:51], v[116:119], v[16:19], v[48:51]\n" \
  "v_mfma_f32_16x16x32_bf16 v[52:55], v[180:183], v[16:19], v[52:55]\n" \
  "v_mfma_f32_16x16x32_bf16 v[56:59], v[244:247], v[16:19], v[56:59]\n" \
  "s_waitcnt lgkmcnt(7)\n" \
  "v_mfma_f32_16x16x32_bf16 v[60:63], v[32:35], v[16:19], v[60:63]\n" \
  "ds_read_b128 v[16:19], v0 offset:2304\n" \
  "ds_read_b128 v[32:35], v4 offset:128\n" \
  "v_mfma_f32_16x16x32_bf16 v[48:51], v[120:123], v[20:23], v[48:51]\n" \
  "s_waitcnt lgkmcnt(4)\n" \
  "v_mul_f32 v40, v40, v8\n" \
  "v_mul_f32 v41, v41, v8\n" \
  "v_mfma_f32_16x16x32_bf16 v[52:55], v[184:187], v[20:23], v[52:55]\n" \
  "v_mul_f32 v42, v42, v8\n" \
  "v_mul_f32 v43, v43, v8\n" \
  "v_mfma_f32_16x16x32_bf16 v[56:59], v[248:251], v[20:23], v[56:59]\n" \
  "v_mul_f32 v44, v44, v8\n" \
  "v_mul_f32 v45, v45, v8\n" \
  "v_mfma_f32_16x16x32_bf16 v[60:63], v[36:39], v[20:23], v[60:63]\n" \
  "v_mul_f32 v46, v46, v8\n" \
  "v_mul_f32 v47, v47, v8\n" \
  "ds_read_b128 v[20:23], v0 offset:4352\n" \
  "ds_read_b128 v[36:39], v4 offset:192\n" \
  "v_mfma_f32_16x16x32_bf16 v[48:51], v[124:127], v[24:27], v[48:51]\n" \
  "v_mfma_f32_16x16x32_bf16 v[52:55], v[188:191], v[24:27], v[52:55]\n" \
  "v_mfma_f32_16x16x32_bf16 v[56:59], v[252:255], v[24:27], v[56:59]\n" \
  "s_waitcnt lgkmcnt(2)\n" \
  "v_mul_f32 v32, v32, v8\n" \
  "v_mul_f32 v33, v33, v8\n" \
  "v_mfma_f32_16x16x32_bf16 v[60:63], v[28:31], v[24:27], v[60:63]\n" \
  "v_mul_f32 v34, v34, v8\n" \
  "v_mul_f32 v35, v35, v8\n" \
  "ds_read_b128 v[28:31], v1 offset:0\n" \
  "ds_read_b128 v[24:27], v0 offset:6400\n" \
  "v_fmac_f32 v40, v48, v9\n" \
  "v_fmac_f32 v41, v49, v9\n" \
  "v_fmac_f32 v42, v50, v9\n" \
  "v_fmac_f32 v43, v51, v9\n" \
  "v_mfma_f32_16x16x32_bf16 v[48:51], v[64:67], v[12:15], 0\n" \
  "v_fmac_f32 v44, v52, v9\n" \
  "v_fmac_f32 v45, v53, v9\n" \
  "v_fmac_f32 v46, v54, v9\n" \
  "v_fmac_f32 v47, v55, v9\n" \
  "v_mfma_f32_16x16x32_bf16 v[52:55], v[128:131], v[12:15], 0\n" \
  "v_cvt_pk_bf16_f32 v40, v40, v41\n" \
  "v_cvt_pk_bf16_f32 v41, v42, v43\n" \
  "v_cvt_pk_bf16_f32 v42, v44, v45\n" \
  "v_cvt_pk_bf16_f32 v43, v46, v47\n" \
  "v_fmac_f32 v32, v56, v9\n" \
  "v_fmac_f32 v33, v57, v9\n" \
  "v_fmac_f32 v34, v58, v9\n" \
  "v_fmac_f32 v35, v59, v9\n" \
  "v_mfma_f32_16x16x32_bf16 v[56:59], v[192:195], v[12:15], 0\n" \
  "s_waitcnt lgkmcnt(2)\n" \
  "v_mul_f32 v36, v36, v8\n" \
  "v_mul_f32 v37, v37, v8\n" \
  "v_mul_f32 v38, v38, v8\n" \
  "v_mul_f32 v39, v39, v8\n" \
  "v_fmac_f32 v36, v60, v9\n" \
  "v_fmac_f32 v37, v61, v9\n" \
  "v_fmac_f32 v38, v62, v9\n" \
  "v_fmac_f32 v39, v63, v9\n" \
  "s_waitcnt lgkmcnt(1)\n" \
  "v_mfma_f32_16x16x32_bf16 v[60:63], v[28:31], v[12:15], 0\n" \
  "v_cvt_pk_bf16_f32 v44, v32, v33\n" \
  "v_cvt_pk_bf16_f32 v45, v34, v35\n" \
  "v_cvt_pk_bf16_f32 v46, v36, v37\n" \
  "v_cvt_pk_bf16_f32 v47, v38, v39\n" \
  "ds_read_b128 v[32:35], v1 offset:1024\n" \
  "ds_read_b128 v[36:39], v1 offset:2048\n" \
  "ds_read_b128 v[12:15], v0 offset:8448\n" \
  "ds_read_b128 v[28:31], v1 offset:3072\n" \
  "v_mfma_f32_16x16x32_bf16 v[48:51], v[68:71], v[16:19], v[48:51]\n" \
  "buffer_store_dwordx4 v[40:43], v2, s[40:43], s65 offen offset:0 sc1\n" \
  "v_mfma_f32_16x16x32_bf16 v[52:55], v[132:135], v[16:19], v[52:55]\n" \
  "buffer_store_dwordx4 v[44:47], v2, s[40:43], s65 offen offset:1024 sc1\n" \
  "v_mfma_f32_16x16x32_bf16 v[56:59], v[196:199], v[16:19], v[56:59]\n" \
  "s_waitcnt lgkmcnt(3)\n" \
  "v_mfma_f32_16x16x32_bf16 v[60:63], v[32:35], v[16:19], v[60:63]\n" \
  "ds_read_b128 v[16:19], v0 offset:10496\n" \
  "ds_read_b128 v[32:35], v1 offset:4096\n" \
  "v_mfma_f32_16x16x32_bf16 v[48:51], v[72:75], v[20:23], v[48:51]\n" \
  "v_mfma_f32_16x16x32_bf16 v[52:55], v[136:139], v[20:23], v[52:55]\n" \
  "v_mfma_f32_16x16x32_bf16 v[56:59], v[200:203], v[20:23], v[56:59]\n" \
  "s_waitcnt lgkmcnt(4)\n" \
  "v_mfma_f32_16x16x32_bf16 v[60:63], v[36:39], v[20:23], v[60:63]\n" \
  "ds_read_b128 v[20:23], v0 offset:12544\n" \
  "ds_read_b128 v[36:39], v1 offset:5120\n" \
  "v_mfma_f32_16x16x32_bf16 v[48:51], v[76:79], v[24:27], v[48:51]\n" \
  "v_mfma_f32_16x16x32_bf16 v[52:55], v[140:143], v[24:27], v[52:55]\n" \
  "v_mfma_f32_16x16x32_bf16 v[56:59], v[204:207], v[24:27], v[56:59]\n" \
  "s_waitcnt lgkmcnt(4)\n" \
  "v_mfma_f32_16x16x32_bf16 v[60:63], v[28:31], v[24:27], v[60:63]\n" \
  "ds_read_b128 v[24:27], v0 offset:14592\n" \
  "ds_read_b128 v[28:31], v1 offset:6144\n" \
  "v_mfma_f32_16x16x32_bf16 v[48:51], v[80:83], v[12:15], v[48:51]\n" \
  "v_mfma_f32_16x16x32_bf16 v[52:55], v[144:147], v[12:15], v[52:55]\n" \
  "v_mfma_f32_16x16x32_bf16 v[56:59], v[208:211], v[12:15], v[56:59]\n" \
  "s_waitcnt lgkmcnt(4)\n" \
  "v_mfma_f32_16x16x32_bf16 v[60:63], v[32:35], v[12:15], v[60:63]\n" \
  "ds_read_b128 v[12:15], v0 offset:16640\n" \
  "ds_read_b128 v[32:35], v1 offset:7168\n" \
  "v_mfma_f32_16x16x32_bf16 v[48:51], v[84:87], v[16:19], v[48:51]\n" \
  "v_mfma_f32_16x16x32_bf16 v[52:55], v[148:151], v[16:19], v[52:55]\n" \
  "v_mfma_f32_16x16x32_bf16 v[56:59], v[212:215], v[16:19], v[56:59]\n" \
  "s_waitcnt lgkmcnt(4)\n" \
  "v_mfma_f32_16x16x32_bf16 v[60:63], v[36:39], v[16:19], v[60:63]\n" \
  "ds_read_b128 v[16:19], v0 offset:18688\n" \
  "ds_read_b128 v[36:39], v1 offset:8192\n" \
  "v_mfma_f32_16x16x32_bf16 v[48:51], v[88:91], v[20:23], v[48:51]\n" \
  "v_mfma_f32_16x16x32_bf16 v[52:55], v[152:155], v[20:23], v[52:55]\n" \
  "v_mfma_f32_16x16x32_bf16 v[56:59], v[216:219], v[20:23], v[56:59]\n" \
  "s_waitcnt lgkmcnt(4)\n" \
  "v_mfma_f32_16x16x32_bf16 v[60:63], v[28:31], v[20:23], v[60:63]\n" \
  "ds_read_b128 v[20:23], v0 offset:20736\n" \
  "ds_read_b128 v[28:31], v1 offset:9216\n" \
  "v_mfma_f32_16x16x32_bf16 v[48:51], v[92:95], v[24:27], v[48:51]\n" \
  "v_mfma_f32_16x16x32_bf16 v[52:55], v[156:159], v[24:27], v[52:55]\n" \
  "v_mfma_f32_16x16x32_bf16 v[56:59], v[220:223], v[24:27], v[56:59]\n" \
  "s_waitcnt lgkmcnt(4)\n" \
  "v_mfma_f32_16x16x32_bf16 v[60:63], v[32:35], v[24:27], v[60:63]\n" \
  "ds_read_b128 v[24:27], v0 offset:22784\n" \
  "ds_read_b128 v[32:35], v1 offset:10240\n" \
  "v_mfma_f32_16x16x32_bf16 v[48:51], v[96:99], v[12:15], v[48:51]\n" \
  "v_mfma_f32_16x16x32_bf16 v[52:55], v[160:163], v[12:15], v[52:55]\n" \
  "v_mfma_f32_16x16x32_bf16 v[56:59], v[224:227], v[12:15], v[56:59]\n" \
  "s_waitcnt lgkmcnt(4)\n" \
  "v_mfma_f32_16x16x32_bf16 v[60:63], v[36:39], v[12:15], v[60:63]\n" \
  "ds_read_b128 v[12:15], v0 offset:24832\n" \
  "ds_read_b128 v[36:39], v1 offset:11264\n" \
  "v_mfma_f32_16x16x32_bf16 v[48:51], v[100:103], v[16:19], v[48:51]\n" \
  "v_mfma_f32_16x16x32_bf16 v[52:55], v[164:167], v[16:19], v[52:55]\n" \
  "v_mfma_f32_16x16x32_bf16 v[56:59], v[228:231], v[16:19], v[56:59]\n" \
  "s_waitcnt lgkmcnt(4)\n" \
  "v_mfma_f32_16x16x32_bf16 v[60:63], v[28:31], v[16:19], v[60:63]\n" \
  "ds_read_b128 v[16:19], v0 offset:26880\n" \
  "ds_read_b128 v[28:31], v1 offset:12288\n" \
  "v_mfma_f32_16x16x32_bf16 v[48:51], v[104:107], v[20:23], v[48:51]\n" \
  "v_mfma_f32_16x16x32_bf16 v[52:55], v[168:171], v[20:23], v[52:55]\n" \
  "v_mfma_f32_16x16x32_bf16 v[56:59], v[232:235], v[20:23], v[56:59]\n" \
  "s_waitcnt lgkmcnt(4)\n" \
  "v_mfma_f32_16x16x32_bf16 v[60:63], v[32:35], v[20:23], v[60:63]\n" \
  "ds_read_b128 v[20:23], v0 offset:28928\n" \
  "ds_read_b128 v[32:35], v1 offset:13312\n" \
  "v_mfma_f32_16x16x32_bf16 v[48:51], v[108:111], v[24:27], v[48:51]\n" \
  "v_mfma_f32_16x16x32_bf16 v[52:55], v[172:175], v[24:27], v[52:55]\n" \
  "v_mfma_f32_16x16x32_bf16 v[56:59], v[236:239], v[24:27], v[56:59]\n" \
  "s_waitcnt lgkmcnt(4)\n" \
  "v_mfma_f32_16x16x32_bf16 v[60:63], v[36:39], v[24:27], v[60:63]\n" \
  "ds_read_b128 v[24:27], v0 offset:30976\n" \
  "ds_read_b128 v[36:39], v1 offset:14336\n" \
  "ds_read_b128 v[40:43], v4 offset:0\n" \
  "ds_read_b128 v[44:47], v4 offset:64\n" \
  "ds_read2_b32 v[8:9], v3 offset0:16 offset1:48\n" \
  "v_mfma_f32_16x16x32_bf16 v[48:51], v[112:115], v[12:15], v[48:51]\n" \
  "v_mfma_f32_16x16x32_bf16 v[52:55], v[176:179], v[12:15], v[52:55]\n" \
  "v_mfma_f32_16x16x32_bf16 v[56:59], v[240:243], v[12:15], v[56:59]\n" \
  "s_waitcnt lgkmcnt(7)\n" \
  "v_mfma_f32_16x16x32_bf16 v[60:63], v[28:31], v[12:15], v[60:63]\n" \
  "ds_read_b128 v[28:31], v1 offset:15360\n" \
  "v_mfma_f32_16x16x32_bf16 v[48:51], v[116:119], v[16:19], v[48:51]\n" \
  "v_mfma_f32_16x16x32_bf16 v[52:55], v[180:183], v[16:19], v[52:55]\n" \
  "v_mfma_f32_16x16x32_bf16 v[56:59], v[244:247], v[16:19], v[56:59]\n" \
  "s_waitcnt lgkmcnt(6)\n" \
  "v_mfma_f32_16x16x32_bf16 v[60:63], v[32:35], v[16:19], v[60:63]\n" \
  "ds_read_b128 v[32:35], v4 offset:128\n" \
  "v_mfma_f32_16x16x32_bf16 v[48:51], v[120:123], v[20:23], v[48:51]\n" \
  "s_waitcnt lgkmcnt(2)\n" \
  "v_mul_f32 v40, v40, v8\n" \
  "v_mul_f32 v41, v41, v8\n" \
  "v_mfma_f32_16x16x32_bf16 v[52:55], v[184:187], v[20:23], v[52:55]\n" \
  "v_mul_f32 v42, v42, v8\n" \
  "v_mul_f32 v43, v43, v8\n" \
  "v_mfma_f32_16x16x32_bf16 v[56:59], v[248:251], v[20:23], v[56:59]\n" \
  "v_mul_f32 v44, v44, v8\n" \
  "v_mul_f32 v45, v45, v8\n" \
  "v_mfma_f32_16x16x32_bf16 v[60:63], v[36:39], v[20:23], v[60:63]\n" \
  "v_mul_f32 v46, v46, v8\n" \
  "v_mul_f32 v47, v47, v8\n" \
  "ds_read_b128 v[36:39], v4 offset:192\n" \
  "s_mov_b64 exec, 1\n" \
  "ds_add_u32 v6, v7 offset:8\n" \
  "s_mov_b64 exec, -1\n" \
  "v_xor_b32 v6, 4, v6\n" \
  "v_mfma_f32_16x16x32_bf16 v[48:51], v[124:127], v[24:27], v[48:51]\n" \
  "v_mfma_f32_16x16x32_bf16 v[52:55], v[188:191], v[24:27], v[52:55]\n" \
  "v_mfma_f32_16x16x32_bf16 v[56:59], v[252:255], v[24:27], v[56:59]\n" \
  "s_waitcnt lgkmcnt(2)\n" \
  "v_mul_f32 v32, v32, v8\n" \
  "v_mul_f32 v33, v33, v8\n" \
  "v_mfma_f32_16x16x32_bf16 v[60:63], v[28:31], v[24:27], v[60:63]\n" \
  "v_mul_f32 v34, v34, v8\n" \
  "v_mul_f32 v35, v35, v8\n" \
  "v_fmac_f32 v40, v48, v9\n" \
  "v_fmac_f32 v41, v49, v9\n" \
  "v_fmac_f32 v42, v50, v9\n" \
  "v_fmac_f32 v43, v51, v9\n" \
  "v_fmac_f32 v44, v52, v9\n" \
  "v_fmac_f32 v45, v53, v9\n" \
  "v_fmac_f32 v46, v54, v9\n" \
  "v_fmac_f32 v47, v55, v9\n" \
  "ds_read_b32 v5, v6 offset:0\n" \
  "v_cvt_pk_bf16_f32 v40, v40, v41\n" \
  "v_cvt_pk_bf16_f32 v41, v42, v43\n" \
  "v_cvt_pk_bf16_f32 v42, v44, v45\n" \
  "v_cvt_pk_bf16_f32 v43, v46, v47\n" \
  "buffer_store_dwordx4 v[40:43], v10, s[40:43], s65 offen offset:0 sc1\n" \
  "v_fmac_f32 v32, v56, v9\n" \
  "v_fmac_f32 v33, v57, v9\n" \
  "v_fmac_f32 v34, v58, v9\n" \
  "v_fmac_f32 v35, v59, v9\n" \
  "s_waitcnt lgkmcnt(2)\n" \
  "v_mul_f32 v36, v36, v8\n" \
  "v_mul_f32 v37, v37, v8\n" \
  "v_mul_f32 v38, v38, v8\n" \
  "v_mul_f32 v39, v39, v8\n" \
  "v_fmac_f32 v36, v60, v9\n" \
  "v_fmac_f32 v37, v61, v9\n" \
  "v_fmac_f32 v38, v62, v9\n" \
  "v_fmac_f32 v39, v63, v9\n" \
  "v_cvt_pk_bf16_f32 v44, v32, v33\n" \
  "v_cvt_pk_bf16_f32 v45, v34, v35\n" \
  "v_cvt_pk_bf16_f32 v46, v36, v37\n" \
  "v_cvt_pk_bf16_f32 v47, v38, v39\n" \
  "buffer_store_dwordx4 v[44:47], v10, s[40:43], s65 offen offset:1024 sc1\n" \
  "s_waitcnt lgkmcnt(0)\n" \
  "s_add_u32 s65, s65, 0x80000\n" \
  "s_sub_u32 s64, s64, 1\n" \
  "s_mov_b32 s68, 1\n" \
  "s_cmp_lg_u32 s64, 0\n" \
  "s_cbranch_scc1 .LCP_TOP_%=\n" \
  "s_waitcnt vmcnt(0)\n" \
  "s_mov_b64 exec, 1\n" \
  "buffer_store_dword v7, off, s[56:59], s72 sc1\n" \
  "s_mov_b64 exec, -1\n" \
  "s_add_u32 s72, s72, 0x1000\n" \
  "s_mov_b32 s68, 0\n" \
  ".LCP_END_%=:\n" \
  "s_endpgm\n"
#define V_ASM \
  "s_mov_b32 s68, 0\n" \
  "s_mov_b32 s66, 3\n" \
  "s_mov_b32 s67, 2\n" \
  "s_mov_b32 s72, 0\n" \
  "v_mov_b32 v7, 1\n" \
  "v_mov_b32 v6, s70\n" \
  "v_mov_b32 v5, 0\n" \
  "ds_read_b32 v8, v4 offset:0\n" \
  "ds_read_b32 v9, v4 offset:64\n" \
  "ds_read_b32 v10, v4 offset:128\n" \
  "ds_read_b32 v4, v4 offset:192\n" \
  "s_waitcnt lgkmcnt(0)\n" \
  ".p2align 6\n" \
  ".LCW_TOP_%=:\n" \
  "ds_read_b128 v[28:31], v1 offset:0\n" \
  "ds_read_b128 v[32:35], v1 offset:1024\n" \
  "s_add_u32 s66, s66, 3\n" \
  "s_add_u32 s67, s67, 2\n" \
  "s_mov_b32 s69, 0x400000\n" \
  "v_xor_b32 v0, 0x8000, v0\n" \
  "v_xor_b32 v3, 0x100, v3\n" \
  "v_readfirstlane_b32 s71, v5\n" \
  "s_cmp_ge_u32 s71, s66\n" \
  "s_cbranch_scc1 .LCW_GO_%=\n" \
  "s_cmp_eq_u32 s68, 1\n" \
  "s_cbranch_scc0 .LCW_X1_%=\n" \
  "s_waitcnt vmcnt(0)\n" \
  "s_mov_b64 exec, 1\n" \
  "buffer_store_dword v7, off, s[56:59], s72 sc1\n" \
  "s_mov_b64 exec, -1\n" \
  "s_add_u32 s72, s72, 0x1000\n" \
  "s_mov_b32 s68, 0\n" \
  ".LCW_X1_%=:\n" \
  ".LCW_S2_%=:\n" \
  "s_sleep 1\n" \
  "ds_read_b32 v5, v6 offset:0\n" \
  "s_sub_u32 s69, s69, 1\n" \
  "s_cbranch_scc1 .LCW_END_%=\n" \
  "s_waitcnt lgkmcnt(0)\n" \
  "v_readfirstlane_b32 s71, v5\n" \
  "s_cmp_ge_u32 s71, s66\n" \
  "s_cbranch_scc1 .LCW_GO_%=\n" \
  "s_branch .LCW_S2_%=\n" \
  ".LCW_GO_%=:\n" \
  "ds_read_b128 v[12:15], v0 offset:0\n" \
  "ds_read_b128 v[16:19], v0 offset:2048\n" \
  "ds_read_b128 v[20:23], v0 offset:4096\n" \
  "ds_read_b128 v[24:27], v0 offset:6144\n" \
  "ds_read_b128 v[36:39], v1 offset:2048\n" \
  "s_waitcnt lgkmcnt(4)\n" \
  "v_mfma_f32_16x16x32_bf16 v[42:45], v[12:15], v[64:67], 0\n" \
  "v_mfma_f32_16x16x32_bf16 v[48:51], v[12:15], v[128:131], 0\n" \
  "v_mfma_f32_16x16x32_bf16 v[54:57], v[12:15], v[192:195], 0\n" \
  "v_mfma_f32_16x16x32_bf16 v[60:63], v[12:15], v[28:31], 0\n" \
  "ds_read_b128 v[12:15], v0 offset:8192\n" \
  "ds_read_b128 v[28:31], v1 offset:3072\n" \
  "s_waitcnt lgkmcnt(5)\n" \
  "v_mfma_f32_16x16x32_bf16 v[42:45], v[16:19], v[68:71], v[42:45]\n" \
  "v_mfma_f32_16x16x32_bf16 v[48:51], v[16:19], v[132:135], v[48:51]\n" \
  "v_mfma_f32_16x16x32_bf16 v[54:57], v[16:19], v[196:199], v[54:57]\n" \
  "v_mfma_f32_16x16x32_bf16 v[60:63], v[16:19], v[32:35], v[60:63]\n" \
  "ds_read_b128 v[16:19], v0 offset:10240\n" \
  "ds_read_b128 v[32:35], v1 offset:4096\n" \
  "s_waitcnt lgkmcnt(6)\n" \
  "v_mfma_f32_16x16x32_bf16 v[42:45], v[20:23], v[72:75], v[42:45]\n" \
  "v_mfma_f32_16x16x32_bf16 v[48:51], v[20:23], v[136:139], v[48:51]\n" \
  "v_mfma_f32_16x16x32_bf16 v[54:57], v[20:23], v[200:203], v[54:57]\n" \
  "s_waitcnt lgkmcnt(4)\n" \
  "v_mfma_f32_16x16x32_bf16 v[60:63], v[20:23], v[36:39], v[60:63]\n" \
  "ds_read_b128 v[20:23], v0 offset:12288\n" \
  "ds_read_b128 v[36:39], v1 offset:5120\n" \
  "v_mfma_f32_16x16x32_bf16 v[42:45], v[24:27], v[76:79], v[42:45]\n" \
  "v_mfma_f32_16x16x32_bf16 v[48:51], v[24:27], v[140:143], v[48:51]\n" \
  "v_mfma_f32_16x16x32_bf16 v[54:57], v[24:27], v[204:207], v[54:57]\n" \
  "s_waitcnt lgkmcnt(4)\n" \
  "v_mfma_f32_16x16x32_bf16 v[60:63], v[24:27], v[28:31], v[60:63]\n" \
  "ds_read_b128 v[24:27], v0 offset:14336\n" \
  "ds_read_b128 v[28:31], v1 offset:6144\n" \
  "v_mfma_f32_16x16x32_bf16 v[42:45], v[12:15], v[80:83], v[42:45]\n" \
  "v_mfma_f32_16x16x32_bf16 v[48:51], v[12:15], v[144:147], v[48:51]\n" \
  "v_mfma_f32_16x16x32_bf16 v[54:57], v[12:15], v[208:211], v[54:57]\n" \
  "s_waitcnt lgkmcnt(4)\n" \
  "v_mfma_f32_16x16x32_bf16 v[60:63], v[12:15], v[32:35], v[60:63]\n" \
  "ds_read_b128 v[12:15], v0 offset:16384\n" \
  "ds_read_b128 v[32:35], v1 offset:7168\n" \
  "v_mfma_f32_16x16x32_bf16 v[42:45], v[16:19], v[84:87], v[42:45]\n" \
  "v_mfma_f32_16x16x32_bf16 v[48:51], v[16:19], v[148:151], v[48:51]\n" \
  "v_mfma_f32_16x16x32_bf16 v[54:57], v[16:19], v[212:215], v[54:57]\n" \
  "s_waitcnt lgkmcnt(4)\n" \
  "v_mfma_f32_16x16x32_bf16 v[60:63], v[16:19], v[36:39], v[60:63]\n" \
  "ds_read_b128 v[16:19], v0 offset:18432\n" \
  "ds_read_b128 v[36:39], v1 offset:8192\n" \
  "v_mfma_f32_16x16x32_bf16 v[42:45], v[20:23], v[88:91], v[42:45]\n" \
  "v_mfma_f32_16x16x32_bf16 v[48:51], v[20:23], v[152:155], v[48:51]\n" \
  "v_mfma_f32_16x16x32_bf16 v[54:57], v[20:23], v[216:219], v[54:57]\n" \
  "s_waitcnt lgkmcnt(4)\n" \
  "v_mfma_f32_16x16x32_bf16 v[60:63], v[20:23], v[28:31], v[60:63]\n" \
  "ds_read_b128 v[20:23], v0 offset:20480\n" \
  "ds_read_b128 v[28:31], v1 offset:9216\n" \
  "v_mfma_f32_16x16x32_bf16 v[42:45], v[24:27], v[92:95], v[42:45]\n" \
  "v_mfma_f32_16x16x32_bf16 v[48:51], v[24:27], v[156:159], v[48:51]\n" \
  "v_mfma_f32_16x16x32_bf16 v[54:57], v[24:27], v[220:223], v[54:57]\n" \
  "s_waitcnt lgkmcnt(4)\n" \
  "v_mfma_f32_16x16x32_bf16 v[60:63], v[24:27], v[32:35], v[60:63]\n" \
  "ds_read_b128 v[24:27], v0 offset:22528\n" \
  "ds_read_b128 v[32:35], v1 offset:10240\n" \
  "v_mfma_f32_16x16x32_bf16 v[42:45], v[12:15], v[96:99], v[42:45]\n" \
  "v_mfma_f32_16x16x32_bf16 v[48:51], v[12:15], v[160:163], v[48:51]\n" \
  "v_mfma_f32_16x16x32_bf16 v[54:57], v[12:15], v[224:227], v[54:57]\n" \
  "s_waitcnt lgkmcnt(4)\n" \
  "v_mfma_f32_16x16x32_bf16 v[60:63], v[12:15], v[36:39], v[60:63]\n" \
  "ds_read_b128 v[12:15], v0 offset:24576\n" \
  "ds_read_b128 v[36:39], v1 offset:11264\n" \
  "v_mfma_f32_16x16x32_bf16 v[42:45], v[16:19], v[100:103], v[42:45]\n" \
  "v_mfma_f32_16x16x32_bf16 v[48:51], v[16:19], v[164:167], v[48:51]\n" \
  "v_mfma_f32_16x16x32_bf16 v[54:57], v[16:19], v[228:231], v[54:57]\n" \
  "s_waitcnt lgkmcnt(4)\n" \
  "v_mfma_f32_16x16x32_bf16 v[60:63], v[16:19], v[28:31], v[60:63]\n" \
  "ds_read_b128 v[16:19], v0 offset:26624\n" \
  "ds_read_b128 v[28:31], v1 offset:12288\n" \
  "v_mfma_f32_16x16x32_bf16 v[42:45], v[20:23], v[104:107], v[42:45]\n" \
  "v_mfma_f32_16x16x32_bf16 v[48:51], v[20:23], v[168:171], v[48:51]\n" \
  "v_mfma_f32_16x16x32_bf16 v[54:57], v[20:23], v[232:235], v[54:57]\n" \
  "s_waitcnt lgkmcnt(4)\n" \
  "v_mfma_f32_16x16x32_bf16 v[60:63], v[20:23], v[32:35], v[60:63]\n" \
  "ds_read_b128 v[20:23], v0 offset:28672\n" \
  "ds_read_b128 v[32:35], v1 offset:13312\n" \
  "v_mfma_f32_16x16x32_bf16 v[42:45], v[24:27], v[108:111], v[42:45]\n" \
  "v_mfma_f32_16x16x32_bf16 v[48:51], v[24:27], v[172:175], v[48:51]\n" \
  "v_mfma_f32_16x16x32_bf16 v[54:57], v[24:27], v[236:239], v[54:57]\n" \
  "s_waitcnt lgkmcnt(4)\n" \
  "v_mfma_f32_16x16x32_bf16 v[60:63], v[24:27], v[36:39], v[60:63]\n" \
  "s_cmp_eq_u32 s68, 1\n" \
  "s_cbranch_scc0 .LCW_X3_%=\n" \
  "s_waitcnt vmcnt(0)\n" \
  "s_mov_b64 exec, 1\n" \
  "buffer_store_dword v7, off, s[56:59], s72 sc1\n" \
  "s_mov_b64 exec, -1\n" \
  "s_add_u32 s72, s72, 0x1000\n" \
  "s_mov_b32 s68, 0\n" \
  ".LCW_X3_%=:\n" \
  "ds_read_b32 v5, v6 offset:16\n" \
  "ds_read_b128 v[24:27], v0 offset:30720\n" \
  "ds_read_b128 v[36:39], v1 offset:14336\n" \
  "v_mfma_f32_16x16x32_bf16 v[42:45], v[12:15], v[112:115], v[42:45]\n" \
  "v_mfma_f32_16x16x32_bf16 v[48:51], v[12:15], v[176:179], v[48:51]\n" \
  "v_mfma_f32_16x16x32_bf16 v[54:57], v[12:15], v[240:243], v[54:57]\n" \
  "s_waitcnt lgkmcnt(5)\n" \
  "v_mfma_f32_16x16x32_bf16 v[60:63], v[12:15], v[28:31], v[60:63]\n" \
  "ds_read_b128 v[12:15], v0 offset:256\n" \
  "ds_read_b128 v[28:31], v1 offset:15360\n" \
  "v_mfma_f32_16x16x32_bf16 v[42:45], v[16:19], v[116:119], v[42:45]\n" \
  "v_mfma_f32_16x16x32_bf16 v[48:51], v[16:19], v[180:183], v[48:51]\n" \
  "v_mfma_f32_16x16x32_bf16 v[54:57], v[16:19], v[244:247], v[54:57]\n" \
  "s_waitcnt lgkmcnt(5)\n" \
  "v_mfma_f32_16x16x32_bf16 v[60:63], v[16:19], v[32:35], v[60:63]\n" \
  "s_waitcnt lgkmcnt(4)\n" \
  "v_readfirstlane_b32 s71, v5\n" \
  "s_cmp_ge_u32 s71, s67\n" \
  "s_cbranch_scc1 .LCW_ST_%=\n" \
  ".LCW_S4_%=:\n" \
  "s_sleep 1\n" \
  "ds_read_b32 v5, v6 offset:16\n" \
  "s_sub_u32 s69, s69, 1\n" \
  "s_cbranch_scc1 .LCW_END_%=\n" \
  "s_waitcnt lgkmcnt(0)\n" \
  "v_readfirstlane_b32 s71, v5\n" \
  "s_cmp_ge_u32 s71, s67\n" \
  "s_cbranch_scc1 .LCW_ST_%=\n" \
  "s_branch .LCW_S4_%=\n" \
  ".LCW_ST_%=:\n" \
  "ds_read_b128 v[16:19], v0 offset:2304\n" \
  "ds_read_b128 v[32:35], v3 offset:128\n" \
  "v_mfma_f32_16x16x32_bf16 v[42:45], v[20:23], v[120:123], v[42:45]\n" \
  "v_mfma_f32_16x16x32_bf16 v[48:51], v[20:23], v[184:187], v[48:51]\n" \
  "v_mfma_f32_16x16x32_bf16 v[54:57], v[20:23], v[248:251], v[54:57]\n" \
  "s_waitcnt lgkmcnt(4)\n" \
  "v_mfma_f32_16x16x32_bf16 v[60:63], v[20:23], v[36:39], v[60:63]\n" \
  "ds_read_b128 v[20:23], v0 offset:4352\n" \
  "ds_read_b128 v[36:39], v3 offset:0\n" \
  "v_mfma_f32_16x16x32_bf16 v[42:45], v[24:27], v[124:127], v[42:45]\n" \
  "v_mfma_f32_16x16x32_bf16 v[48:51], v[24:27], v[188:191], v[48:51]\n" \
  "v_mfma_f32_16x16x32_bf16 v[54:57], v[24:27], v[252:255], v[54:57]\n" \
  "s_waitcnt lgkmcnt(4)\n" \
  "v_mfma_f32_16x16x32_bf16 v[60:63], v[24:27], v[28:31], v[60:63]\n" \
  "ds_read_b128 v[28:31], v1 offset:0\n" \
  "ds_read_b128 v[24:27], v0 offset:6400\n" \
  "s_waitcnt lgkmcnt(4)\n" \
  "v_mul_f32 v42, v42, v32\n" \
  "v_mul_f32 v43, v43, v33\n" \
  "v_mul_f32 v44, v44, v34\n" \
  "v_mul_f32 v45, v45, v35\n" \
  "s_waitcnt lgkmcnt(2)\n" \
  "v_fmac_f32 v42, v36, v8\n" \
  "v_fmac_f32 v43, v37, v8\n" \
  "v_fmac_f32 v44, v38, v8\n" \
  "v_fmac_f32 v45, v39, v8\n" \
  "v_cvt_pk_bf16_f32 v40, v42, v43\n" \
  "v_cvt_pk_bf16_f32 v41, v44, v45\n" \
  "v_mfma_f32_16x16x32_bf16 v[42:45], v[12:15], v[64:67], 0\n" \
  "v_mul_f32 v48, v48, v32\n" \
  "v_mul_f32 v49, v49, v33\n" \
  "v_mul_f32 v50, v50, v34\n" \
  "v_mul_f32 v51, v51, v35\n" \
  "v_fmac_f32 v48, v36, v9\n" \
  "v_fmac_f32 v49, v37, v9\n" \
  "v_fmac_f32 v50, v38, v9\n" \
  "v_fmac_f32 v51, v39, v9\n" \
  "v_cvt_pk_bf16_f32 v46, v48, v49\n" \
  "v_cvt_pk_bf16_f32 v47, v50, v51\n" \
  "v_mfma_f32_16x16x32_bf16 v[48:51], v[12:15], v[128:131], 0\n" \
  "v_mul_f32 v54, v54, v32\n" \
  "v_mul_f32 v55, v55, v33\n" \
  "v_mul_f32 v56, v56, v34\n" \
  "v_mul_f32 v57, v57, v35\n" \
  "v_fmac_f32 v54, v36, v10\n" \
  "v_fmac_f32 v55, v37, v10\n" \
  "v_fmac_f32 v56, v38, v10\n" \
  "v_fmac_f32 v57, v39, v10\n" \
  "v_cvt_pk_bf16_f32 v52, v54, v55\n" \
  "v_cvt_pk_bf16_f32 v53, v56, v57\n" \
  "v_mfma_f32_16x16x32_bf16 v[54:57], v[12:15], v[192:195], 0\n" \
  "v_mul_f32 v60, v60, v32\n" \
  "v_mul_f32 v61, v61, v33\n" \
  "v_mul_f32 v62, v62, v34\n" \
  "v_mul_f32 v63, v63, v35\n" \
  "v_fmac_f32 v60, v36, v4\n" \
  "v_fmac_f32 v61, v37, v4\n" \
  "v_fmac_f32 v62, v38, v4\n" \
  "v_fmac_f32 v63, v39, v4\n" \
  "v_cvt_pk_bf16_f32 v58, v60, v61\n" \
  "v_cvt_pk_bf16_f32 v59, v62, v63\n" \
  "ds_read_b128 v[32:35], v1 offset:1024\n" \
  "ds_read_b128 v[36:39], v1 offset:2048\n" \
  "s_waitcnt lgkmcnt(3)\n" \
  "v_mfma_f32_16x16x32_bf16 v[60:63], v[12:15], v[28:31], 0\n" \
  "ds_read_b128 v[12:15], v0 offset:8448\n" \
  "ds_read_b128 v[28:31], v1 offset:3072\n" \
  "v_mfma_f32_16x16x32_bf16 v[42:45], v[16:19], v[68:71], v[42:45]\n" \
  "v_mfma_f32_16x16x32_bf16 v[48:51], v[16:19], v[132:135], v[48:51]\n" \
  "v_mfma_f32_16x16x32_bf16 v[54:57], v[16:19], v[196:199], v[54:57]\n" \
  "s_waitcnt lgkmcnt(3)\n" \
  "v_mfma_f32_16x16x32_bf16 v[60:63], v[16:19], v[32:35], v[60:63]\n" \
  "ds_read_b128 v[16:19], v0 offset:10496\n" \
  "ds_read_b128 v[32:35], v1 offset:4096\n" \
  "v_mfma_f32_16x16x32_bf16 v[42:45], v[20:23], v[72:75], v[42:45]\n" \
  "v_mfma_f32_16x16x32_bf16 v[48:51], v[20:23], v[136:139], v[48:51]\n" \
  "v_mfma_f32_16x16x32_bf16 v[54:57], v[20:23], v[200:203], v[54:57]\n" \
  "s_waitcnt lgkmcnt(4)\n" \
  "v_mfma_f32_16x16x32_bf16 v[60:63], v[20:23], v[36:39], v[60:63]\n" \
  "ds_read_b128 v[20:23], v0 offset:12544\n" \
  "ds_read_b128 v[36:39], v1 offset:5120\n" \
  "v_mfma_f32_16x16x32_bf16 v[42:45], v[24:27], v[76:79], v[42:45]\n" \
  "v_mfma_f32_16x16x32_bf16 v[48:51], v[24:27], v[140:143], v[48:51]\n" \
  "v_mfma_f32_16x16x32_bf16 v[54:57], v[24:27], v[204:207], v[54:57]\n" \
  "s_waitcnt lgkmcnt(4)\n" \
  "v_mfma_f32_16x16x32_bf16 v[60:63], v[24:27], v[28:31], v[60:63]\n" \
  "ds_read_b128 v[24:27], v0 offset:14592\n" \
  "ds_read_b128 v[28:31], v1 offset:6144\n" \
  "v_mfma_f32_16x16x32_bf16 v[42:45], v[12:15], v[80:83], v[42:45]\n" \
  "v_mfma_f32_16x16x32_bf16 v[48:51], v[12:15], v[144:147], v[48:51]\n" \
  "v_mfma_f32_16x16x32_bf16 v[54:57], v[12:15], v[208:211], v[54:57]\n" \
  "s_waitcnt lgkmcnt(4)\n" \
  "v_mfma_f32_16x16x32_bf16 v[60:63], v[12:15], v[32:35], v[60:63]\n" \
  "ds_read_b128 v[12:15], v0 offset:16640\n" \
  "ds_read_b128 v[32:35], v1 offset:7168\n" \
  "v_mfma_f32_16x16x32_bf16 v[42:45], v[16:19], v[84:87], v[42:45]\n" \
  "v_mfma_f32_16x16x32_bf16 v[48:51], v[16:19], v[148:151], v[48:51]\n" \
  "v_mfma_f32_16x16x32_bf16 v[54:57], v[16:19], v[212:215], v[54:57]\n" \
  "s_waitcnt lgkmcnt(4)\n" \
  "v_mfma_f32_16x16x32_bf16 v[60:63], v[16:19], v[36:39], v[60:63]\n" \
  "ds_read_b128 v[16:19], v0 offset:18688\n" \
  "ds_read_b128 v[36:39], v1 offset:8192\n" \
  "v_mfma_f32_16x16x32_bf16 v[42:45], v[20:23], v[88:91], v[42:45]\n" \
  "v_mfma_f32_16x16x32_bf16 v[48:51], v[20:23], v[152:155], v[48:51]\n" \
  "v_mfma_f32_16x16x32_bf16 v[54:57], v[20:23], v[216:219], v[54:57]\n" \
  "s_waitcnt lgkmcnt(4)\n" \
  "v_mfma_f32_16x16x32_bf16 v[60:63], v[20:23], v[28:31], v[60:63]\n" \
  "ds_read_b128 v[20:23], v0 offset:20736\n" \
  "ds_read_b128 v[28:31], v1 offset:9216\n" \
  "v_mfma_f32_16x16x32_bf16 v[42:45], v[24:27], v[92:95], v[42:45]\n" \
  "v_mfma_f32_16x16x32_bf16 v[48:51], v[24:27], v[156:159], v[48:51]\n" \
  "v_mfma_f32_16x16x32_bf16 v[54:57], v[24:27], v[220:223], v[54:57]\n" \
  "s_waitcnt lgkmcnt(4)\n" \
  "v_mfma_f32_16x16x32_bf16 v[60:63], v[24:27], v[32:35], v[60:63]\n" \
  "ds_read_b128 v[24:27], v0 offset:22784\n" \
  "ds_read_b128 v[32:35], v1 offset:10240\n" \
  "v_mfma_f32_16x16x32_bf16 v[42:45], v[12:15], v[96:99], v[42:45]\n" \
  "v_mfma_f32_16x16x32_bf16 v[48:51], v[12:15], v[160:163], v[48:51]\n" \
  "v_mfma_f32_16x16x32_bf16 v[54:57], v[12:15], v[224:227], v[54:57]\n" \
  "s_waitcnt lgkmcnt(4)\n" \
  "v_mfma_f32_16x16x32_bf16 v[60:63], v[12:15], v[36:39], v[60:63]\n" \
  "ds_read_b128 v[12:15], v0 offset:24832\n" \
  "ds_read_b128 v[36:39], v1 offset:11264\n" \
  "v_mfma_f32_16x16x32_bf16 v[42:45], v[16:19], v[100:103], v[42:45]\n" \
  "v_mfma_f32_16x16x32_bf16 v[48:51], v[16:19], v[164:167], v[48:51]\n" \
  "v_mfma_f32_16x16x32_bf16 v[54:57], v[16:19], v[228:231], v[54:57]\n" \
  "s_waitcnt lgkmcnt(4)\n" \
  "v_mfma_f32_16x16x32_bf16 v[60:63], v[16:19], v[28:31], v[60:63]\n" \
  "ds_read_b128 v[16:19], v0 offset:26880\n" \
  "ds_read_b128 v[28:31], v1 offset:12288\n" \
  "v_mfma_f32_16x16x32_bf16 v[42:45], v[20:23], v[104:107], v[42:45]\n" \
  "v_mfma_f32_16x16x32_bf16 v[48:51], v[20:23], v[168:171], v[48:51]\n" \
  "v_mfma_f32_16x16x32_bf16 v[54:57], v[20:23], v[232:235], v[54:57]\n" \
  "s_waitcnt lgkmcnt(4)\n" \
  "v_mfma_f32_16x16x32_bf16 v[60:63], v[20:23], v[32:35], v[60:63]\n" \
  "ds_read_b128 v[20:23], v0 offset:28928\n" \
  "ds_read_b128 v[32:35], v1 offset:13312\n" \
  "v_mfma_f32_16x16x32_bf16 v[42:45], v[24:27], v[108:111], v[42:45]\n" \
  "v_mfma_f32_16x16x32_bf16 v[48:51], v[24:27], v[172:175], v[48:51]\n" \
  "v_mfma_f32_16x16x32_bf16 v[54:57], v[24:27], v[236:239], v[54:57]\n" \
  "s_waitcnt lgkmcnt(4)\n" \
  "v_mfma_f32_16x16x32_bf16 v[60:63], v[24:27], v[36:39], v[60:63]\n" \
  "ds_read_b128 v[24:27], v0 offset:30976\n" \
  "ds_read_b128 v[36:39], v1 offset:14336\n" \
  "v_mfma_f32_16x16x32_bf16 v[42:45], v[12:15], v[112:115], v[42:45]\n" \
  "v_mfma_f32_16x16x32_bf16 v[48:51], v[12:15], v[176:179], v[48:51]\n" \
  "v_mfma_f32_16x16x32_bf16 v[54:57], v[12:15], v[240:243], v[54:57]\n" \
  "s_waitcnt lgkmcnt(4)\n" \
  "v_mfma_f32_16x16x32_bf16 v[60:63], v[12:15], v[28:31], v[60:63]\n" \
  "ds_read_b128 v[28:31], v1 offset:15360\n" \
  "v_mfma_f32_16x16x32_bf16 v[42:45], v[16:19], v[116:119], v[42:45]\n" \
  "v_mfma_f32_16x16x32_bf16 v[48:51], v[16:19], v[180:183], v[48:51]\n" \
  "v_mfma_f32_16x16x32_bf16 v[54:57], v[16:19], v[244:247], v[54:57]\n" \
  "s_waitcnt lgkmcnt(3)\n" \
  "v_mfma_f32_16x16x32_bf16 v[60:63], v[16:19], v[32:35], v[60:63]\n" \
  "ds_read_b128 v[32:35], v3 offset:192\n" \
  "v_mfma_f32_16x16x32_bf16 v[42:45], v[20:23], v[120:123], v[42:45]\n" \
  "v_mfma_f32_16x16x32_bf16 v[48:51], v[20:23], v[184:187], v[48:51]\n" \
  "v_mfma_f32_16x16x32_bf16 v[54:57], v[20:23], v[248:251], v[54:57]\n" \
  "s_waitcnt lgkmcnt(2)\n" \
  "v_mfma_f32_16x16x32_bf16 v[60:63], v[20:23], v[36:39], v[60:63]\n" \
  "ds_read_b128 v[36:39], v3 offset:64\n" \
  "s_mov_b64 exec, 1\n" \
  "ds_add_u32 v6, v7 offset:8\n" \
  "s_mov_b64 exec, -1\n" \
  "v_xor_b32 v6, 4, v6\n" \
  "v_mfma_f32_16x16x32_bf16 v[42:45], v[24:27], v[124:127], v[42:45]\n" \
  "v_mfma_f32_16x16x32_bf16 v[48:51], v[24:27], v[188:191], v[48:51]\n" \
  "v_mfma_f32_16x16x32_bf16 v[54:57], v[24:27], v[252:255], v[54:57]\n" \
  "s_waitcnt lgkmcnt(3)\n" \
  "v_mfma_f32_16x16x32_bf16 v[60:63], v[24:27], v[28:31], v[60:63]\n" \
  "s_waitcnt lgkmcnt(2)\n" \
  "v_mul_f32 v42, v42, v32\n" \
  "v_mul_f32 v43, v43, v33\n" \
  "v_mul_f32 v44, v44, v34\n" \
  "v_mul_f32 v45, v45, v35\n" \
  "s_waitcnt lgkmcnt(1)\n" \
  "v_fmac_f32 v42, v36, v8\n" \
  "v_fmac_f32 v43, v37, v8\n" \
  "v_fmac_f32 v44, v38, v8\n" \
  "v_fmac_f32 v45, v39, v8\n" \
  "v_cvt_pk_bf16_f32 v42, v42, v43\n" \
  "v_cvt_pk_bf16_f32 v43, v44, v45\n" \
  "buffer_store_dwordx4 v[40:43], v2, s[40:43], s65 offen offset:0 sc1\n" \
  "v_mul_f32 v48, v48, v32\n" \
  "v_mul_f32 v49, v49, v33\n" \
  "v_mul_f32 v50, v50, v34\n" \
  "v_mul_f32 v51, v51, v35\n" \
  "v_fmac_f32 v48, v36, v9\n" \
  "v_fmac_f32 v49, v37, v9\n" \
  "v_fmac_f32 v50, v38, v9\n" \
  "v_fmac_f32 v51, v39, v9\n" \
  "v_cvt_pk_bf16_f32 v48, v48, v49\n" \
  "v_cvt_pk_bf16_f32 v49, v50, v51\n" \
  "ds_read_b32 v5, v6 offset:0\n" \
  "buffer_store_dwordx4 v[46:49], v2, s[40:43], s65 offen offset:1024 sc1\n" \
  "v_mul_f32 v54, v54, v32\n" \
  "v_mul_f32 v55, v55, v33\n" \
  "v_mul_f32 v56, v56, v34\n" \
  "v_mul_f32 v57, v57, v35\n" \
  "v_fmac_f32 v54, v36, v10\n" \
  "v_fmac_f32 v55, v37, v10\n" \
  "v_fmac_f32 v56, v38, v10\n" \
  "v_fmac_f32 v57, v39, v10\n" \
  "v_cvt_pk_bf16_f32 v54, v54, v55\n" \
  "v_cvt_pk_bf16_f32 v55, v56, v57\n" \
  "buffer_store_dwordx4 v[52:55], v2, s[40:43], s65 offen offset:2048 sc1\n" \
  "v_mul_f32 v60, v60, v32\n" \
  "v_mul_f32 v61, v61, v33\n" \
  "v_mul_f32 v62, v62, v34\n" \
  "v_mul_f32 v63, v63, v35\n" \
  "v_fmac_f32 v60, v36, v4\n" \
  "v_fmac_f32 v61, v37, v4\n" \
  "v_fmac_f32 v62, v38, v4\n" \
  "v_fmac_f32 v63, v39, v4\n" \
  "v_cvt_pk_bf16_f32 v60, v60, v61\n" \
  "v_cvt_pk_bf16_f32 v61, v62, v63\n" \
  "buffer_store_dwordx4 v[58:61], v2, s[40:43], s65 offen offset:3072 sc1\n" \
  "s_waitcnt lgkmcnt(0)\n" \
  "s_add_u32 s65, s65, 0x40000\n" \
  "s_sub_u32 s64, s64, 1\n" \
  "s_mov_b32 s68, 1\n" \
  "s_cmp_lg_u32 s64, 0\n" \
  "s_cbranch_scc1 .LCW_TOP_%=\n" \
  "s_waitcnt vmcnt(0)\n" \
  "s_mov_b64 exec, 1\n" \
  "buffer_store_dword v7, off, s[56:59], s72 sc1\n" \
  "s_mov_b64 exec, -1\n" \
  "s_add_u32 s72, s72, 0x1000\n" \
  "s_mov_b32 s68, 0\n" \
  ".LCW_END_%=:\n" \
  "s_endpgm\n"
constexpr int RG = 2;
#define MARKS(s) __builtin_amdgcn_raw_buffer_load_b32(rm, (c >> 2) * 128 + (c & 3) * 4, (s) * 2048, AUX_SC1)
struct Yes { static constexpr bool value = true; };
struct No { static constexpr bool value = false; };
#define RFL(x) __builtin_amdgcn_readfirstlane(x)
#define HALF(x, i) BF8(((i) ? __builtin_shufflevector(x, x, 4, 5, 6, 7) : __builtin_shufflevector(x, x, 0, 1, 2, 3)))
constexpr int P_W = 65536, P_ST = 131072, P_SS = P_ST + 1024, P_CN = P_SS + 512;
template <bool PR>
__device__ __forceinline__ void role_proj_t(const MegaArgs& A, char* smem, const int L, const int idx, const bool kv) {
  const int w = A.wv;
  const int l = lane_id();
  const int c = l & 15, g = l >> 4;
  char* ws = A.ws;
  unsigned* FL = (unsigned*)(ws + OFF_FL);
  unsigned* TMO = FL + FL_TMO;
  const unsigned sb = lds_base(smem);
  auto cn = (volatile __attribute__((address_space(3))) unsigned*)(unsigned long)(sb + P_CN);
  u64 tag;
  const bool h4 = kv && L;
  const __amdgpu_buffer_rsrc_t rsM = MKRS(ws + OFF_MEMK + L * SZ_MEM, kv && w == 2 ? (4 - L) * SZ_MEM : 0);
  const bool mc = kv && w == 2 && (MCE < 2 || !L);
  constexpr int NMC = MCE == 2 ? 8 : 4;
  const int b0 = RFL(h4 ? (H4S ? 2 : 3) * w : w == 2 ? 0 : HX0),
            b1 = RFL(h4 ? (H4S ? 2 * w + 2 : w == 2 ? 8 : 3 * w + 3 * (w < 2)) : w == 2 ? HX0 : 8);
  const __amdgpu_buffer_rsrc_t rsF = MKRS(FL + FL_FC + L * 64 * 32 * 32, 64 * 4096);
  const int fo = ((blockIdx.x & 3) * 8 + ((H4S != 0 && h4) || w == 3 ? l >> 3 : min(b0 + (l >> 3), b1 - 1))) * 128 + (l & 7) * 4;
#define FC_LOOK(s) __builtin_amdgcn_raw_buffer_load_b32(rsF, fo, (s) * 4096, AUX_SC1)
#define MEM_CLEAR(s)                                                                                                          \
  _Pragma("unroll") for (int i = 0; i < NMC; ++i)                                                                             \
    ST128((v4u{0u, 0u, 0u, 0u}), rsM, l * 16 + (i & 1) * 1024, (s) * 65536 + idx * 2048 + (i >> 1) * (int)SZ_MEM * (MCE == 2 ? 1 : 2));
  const char* Xb = ws + (L ? OFF_X3 : OFF_XA);
  const __amdgpu_buffer_rsrc_t rsS = MKRS(ws + OFF_H + L * (SZ_X / 2), SZ_X / 2);
  unsigned nf = 0u;
  bool early = false;
  v4u t0[16];
#define X_ASK(s) \
  for (int i = b0; i < b1; ++i) glds64c(Xb + RFL((s) * 32768 + i * 4096), l * 16, RFL(sb + ((s) & 1) * 32768 + i * 4096));
#define S_ASK(s, t) _Pragma("unroll") for (int i = 0; i < 16; ++i) t[i] = LD128(rsS, l * 16, (s) * 16384 + i * 1024);
  {
    const int j = w & 3;
    const bool vt = kv && j >= 2;
    constexpr int NT = PR ? 3 : 1;
    v8u wq[NT][8];
    const u16* Wm = (kv ? (PR ? A.Wv : A.Wk) : A.Wq) - (PR ? 128 : 0) * HD;
    const int hm = kv ? idx : 2 * idx + PR;
    const int q = (l >> 2) & 3, p = l & 3, f = q | ((g & 1) << 2);
    unsigned ra[NT];
#pragma unroll
    for (int m = 0; m < NT; ++m) {
      const int ct = PR ? m : 3;
      ra[m] = sb + (j >> 1) * 65536 + (8 * g + q) * 256 + ((4 * (j & 1) + (vt ? ct : (ct & 2) + (p >> 1))) ^ f) * 32 + (vt ? p * 8 : (p & 1) * 16 + (ct & 1) * 8);
    }
#define W_ISSUE(n) wqkv_issue<false>(Wm + (128 * (n) - 256 * ((n) & 1)) * HD, (n) & 1, hm, w, l, sb, (WNT >> (kv ? 1 + PR : 3) & 1) && (L == 0 || (WNT & 32)))
#define W_STAGE(n, WAIT, NEXT)                                                                             \
  {                                                                                                        \
    WAIT;                                                                                                  \
    if (n == 3 && EL == 2 && !PR && L == 0 && w >= 2) nf = FC_LOOK(0);                                     \
    if (n == 3 && EL == 1 && !PR && L == 0 && w >= 2) {                                                    \
      early = __all(nf != 0u);                                                                             \
      if (early) {                                                                                         \
        X_ASK(0)                                                                                           \
        if (w == 3) { S_ASK(0, t0) }                                                                       \
      }                                                                                                    \
      nf = FC_LOOK(early);                                                                                 \
    }                                                                                                      \
    RAW_BAR();                                                                                             \
    if (n == 3 && EL == 3 && !PR && L == 0 && w >= 2) nf = FC_LOOK(0);                                     \
    _Pragma("unroll") for (int m = 0; m < NT; ++m) {                                                       \
      _Pragma("unroll") for (int t = 0; t < 2; ++t) {                                                      \
        const unsigned a_ = ra[m] + ((n) & 1) * 32768 + t * 16384;                                         \
        const v2u r0 = trd(a_), r1 = trd(a_ + 1024), r2 = trd(a_ + 8192), r3 = trd(a_ + 8192 + 1024);      \
        wq[m][2 * (n) + t] = v8u{r0.x, r0.y, r1.x, r1.y, r2.x, r2.y, r3.x, r3.y};                          \
      }                                                                                                    \
    }                                                                                                      \
    _Pragma("unroll") for (int m = 0; m < 2 * NT; ++m) asm volatile("" : "+v"(wq[m >> 1][2 * (n) + (m & 1)])); \
    RAW_BAR();                                                                                             \
    NEXT;                                                                                                  \
    _Pragma("unroll") for (int m = 0; m < 4 * NT; ++m)                                                     \
      cs[m % NT] = MFMA16(HALF(wq[m % NT][2 * (n) + m / NT / 2], (m / NT) & 1), BF8(one), cs[m % NT]);     \
    _Pragma("unroll") for (int m = 0; m < NT; ++m) asm volatile("" : "+v"(cs[m]));                         \
  }
    if (EL && !MC3 && mc) { MEM_CLEAR(0) }
    W_ISSUE(0);
    W_ISSUE(1);
    tag = call_tag(A);
    if (w == 0 && l < 8) cn[l] = l == 1 ? 3u : l == 5 ? 2u : 0u;
    const v4u one = {0x3f803f80u, 0x3f803f80u, 0x3f803f80u, 0x3f803f80u};
    f32x4 cs[NT] = {};
    u64 rv = 0;
    W_STAGE(0, WAIT_VM(8), W_ISSUE(2); if (EL) RD_ASK(rv));
    SST(4);
    W_STAGE(1, if (EL) WAIT_VM(9); else WAIT_VM(8), W_ISSUE(3); if (EL != 0 && MC3 != 0 && mc) { MEM_CLEAR(0) if (MC3 == 2) { MEM_CLEAR(1) } });
    SST(5);
    W_STAGE(2, if (EL != 0 && MC3 == 2 && mc) WAIT_VM(16); else if (EL != 0 && MC3 != 0 && mc) WAIT_VM(12); else WAIT_VM(8), (void)0);
    SST(6);
    asm volatile("" : "+v"(rv));
    if (!EL) ready_wait(A, tag, ready_look(A));
    for (unsigned spins = 0; EL && !__all(rv == tag) && spins < SPIN_CAP; ++spins) {
      __builtin_amdgcn_s_sleep(8);
      RD_ASK(rv);
      asm volatile("s_waitcnt vmcnt(0)" : "+v"(rv)::"memory");
    }
    if (EL == 1 && !PR && L == 0 && w >= 2) nf = FC_LOOK(0);
    W_STAGE(3, if (EL >= 2 && MC3 == 2 && mc) WAIT_VM(8); else if (EL >= 2 && MC3 == 1 && mc) WAIT_VM(4); else WAIT_VM(0), if (!PR) {
      _Pragma("unroll") for (int ks = 0; ks < 16; ++ks)
        *reinterpret_cast<bf16x8*>(smem + P_W + j * 16384 + ks * 1024 + l * 16) = HALF(wq[0][ks >> 1], ks & 1);
    });
    SST(7);
#undef W_STAGE
#undef W_ISSUE
#pragma unroll
    for (int m = 0; m < NT; ++m) *reinterpret_cast<f32x4*>(smem + P_ST + j * 256 + (PR ? m : 3) * 64 + g * 16) = cs[m];
    RAW_BAR();
    SST(8);
    if constexpr (PR) {
      const v4u rsO = mk_srd(ws + RFL((unsigned)(vt ? OFF_VF + L * SZ_O : OFF_QK + L * SZ_QK)), RFL((unsigned)(vt ? SZ_O : SZ_QK)));
      const v4u rsP = mk_srd(FL + RFL(kv ? FL_FQ + (L * 64 * 32 + idx) * 32 + 2 * j : FL_FP + (L * 16 + idx) * 32 + j), 63 * 4096 + 4);
      const int so = RFL(vt ? idx * 8 + 4 * (j & 1) : (kv ? idx * 16 + 8 : (2 * idx + (j >> 1)) * 16) + 2 * (j & 1));
      const v8u pv = {sb + 32768 + g * 512 + c * 16, sb + P_W + j * 16384 + l * 16, (unsigned)l * 16, sb + P_SS + 256 + (vt ? g * 16 : c * 4),
                      sb + P_ST + j * 256 + (vt ? c * 4 : g * 16), 0u, 0u, 0u};
      const char* dbg = nullptr;
#define P_OPS                                                                                                                               \
  ::"{v[64:71]}"(wq[0][0]), "{v[72:79]}"(wq[0][1]), "{v[80:87]}"(wq[0][2]), "{v[88:95]}"(wq[0][3]), "{v[96:103]}"(wq[0][4]),                \
      "{v[104:111]}"(wq[0][5]), "{v[112:119]}"(wq[0][6]), "{v[120:127]}"(wq[0][7]), "{v[128:135]}"(wq[1][0]), "{v[136:143]}"(wq[1][1]),     \
      "{v[144:151]}"(wq[1][2]), "{v[152:159]}"(wq[1][3]), "{v[160:167]}"(wq[1][4]), "{v[168:175]}"(wq[1][5]), "{v[176:183]}"(wq[1][6]),     \
      "{v[184:191]}"(wq[1][7]), "{v[192:199]}"(wq[2][0]), "{v[200:207]}"(wq[2][1]), "{v[208:215]}"(wq[2][2]), "{v[216:223]}"(wq[2][3]),     \
      "{v[224:231]}"(wq[2][4]), "{v[232:239]}"(wq[2][5]), "{v[240:247]}"(wq[2][6]), "{v[248:255]}"(wq[2][7]), "{v[0:7]}"(pv),               \
      "{s[40:43]}"(rsO), "{s[56:59]}"(rsP), "{s64}"(NSEG), "{s65}"(so * 1024), "{s70}"(sb + P_CN), "{s[36:37]}"(dbg)                        \
      : "memory", "vcc", "scc", "s80", "v63"
      if (vt) asm volatile("s_mov_b64 exec, -1\ns_waitcnt vmcnt(0) lgkmcnt(0)\ns_nop 4\n" V_ASM P_OPS);
      else asm volatile("s_mov_b64 exec, -1\ns_waitcnt vmcnt(0) lgkmcnt(0)\ns_nop 4\n" P_ASM P_OPS);
#undef P_OPS
      return;
    }
  }
  __builtin_amdgcn_s_setprio(RPRIO);
  if (h4 || w >= 2) {
    if (!EL && mc) { MEM_CLEAR(0) }
    auto stats = [&](const v4u (&t)[16], const int s) {
      float a = 0.f, b = 0.f, qa = 0.f, qb = 0.f;
#pragma unroll
      for (int i = 0; i < 16; ++i) {
        a = one(a + __uint_as_float(t[i].x));
        b = one(b + __uint_as_float(t[i].z));
        qa = one(qa + __uint_as_float(t[i].y));
        qb = one(qb + __uint_as_float(t[i].w));
      }
      a = xsum32(xsum16(a));
      b = xsum32(xsum16(b));
      float da = 0.f, db = 0.f;
#pragma unroll
      for (int i = 0; i < 16; ++i) {
        const float u = one(__uint_as_float(t[i].x) - a * (1.0f / 64)), v = one(__uint_as_float(t[i].z) - b * (1.0f / 64));
        da = one(da + u * u);
        db = one(db + v * v);
      }
      const float var = xsum32(swap_add16(qa + da * 0.125f, qb + db * 0.125f)) * (1.0f / DM);
      const float ri = 1.0f / bfr1(sqrtf(bfr1(bfr1(var) + 1e-5f)));
      reinterpret_cast<float*>(smem + P_SS)[(s & 1) * 64 + (g >> 1) * 32 + 2 * c + (g & 1)] = g < 2 ? bfr1(((g & 1) ? b : a) * (1.0f / DM)) * (EP ? -ri : 1.0f) : ri;
      asm volatile("" ::: "memory");
      if (l == 0) cn[4 + (s & 1)] = 2 * s + 4;
    };
    static_assert(EL != 1 || MC3 == 2, "clear");
    if (early) {
      if (w == 3) WAIT_VM(17);
      else WAIT_VM(1);
      if (l == 0) __hip_atomic_fetch_add((__attribute__((address_space(3))) unsigned*)cn, 3u, __ATOMIC_RELAXED, __HIP_MEMORY_SCOPE_WORKGROUP);
      SSX(10, w == 2);
      SSX(9, w == 2);
      if (w == 3) stats(t0, 0);
      SSX(11, w == 3);
    }
    if (L) {
      char* ws = A.ws;
      int w = A.wv, i1 = idx + 32 * kv;
      asm volatile("" : "+s"(ws), "+s"(w), "+s"(i1));
      const int idx = i1 & 31;
      const bool h4 = i1 >= 32, mc = h4 && w == 2;
      const int b0 = h4 ? 3 * w : w == 2 ? 0 : HX0, b1 = h4 ? (w == 2 ? 8 : 3 * w + 3 * (w < 2)) : w == 2 ? HX0 : 8;
      unsigned* FL = (unsigned*)(ws + OFF_FL);
      unsigned* TMO = FL + FL_TMO;
      const char* Xb = ws + OFF_X3;
      const __amdgpu_buffer_rsrc_t rsM = MKRS(ws + OFF_MEMK + SZ_MEM, 3 * SZ_MEM), rsF = MKRS(FL + FL_FC + 64 * 32 * 32, 64 * 4096),
                                   rsS = MKRS(ws + OFF_H + SZ_X / 2, SZ_X / 2);
      unsigned mk;
      int l16 = l * 16;
      asm volatile("" : "+v"(l16));
      const __amdgpu_buffer_rsrc_t rm = MKRS(FL + FL_FW + (blockIdx.x & 3) * 128, 63 * 2048 + 512);
      const __amdgpu_buffer_rsrc_t rsK = MKRS(ws + (w < 2 ? OFF_QK + SZ_QK + idx * 16384 + 8192 : OFF_VF + SZ_O + idx * 8192) + (w & 1) * 4096,
                                              KC && h4 ? 63 * (w < 2 ? 0x80000 : 0x40000) + 4096 : 0);
#define KV_CLEAR(s) \
  _Pragma("unroll") for (int i = 0; i < 4 * KC; ++i) ST128((v4u{0u, 0u, 0u, 0u}), rsK, l16 + i * 1024, (s) << (w < 2 ? 19 : 18));
      KV_CLEAR(1)
      if (!wait_n<SLP_V, 32>(FL + FL_FZ + (blockIdx.x & 7) * 64, 64, TMO)) return;
#pragma unroll 1
      for (unsigned spins = 0;; ++spins) {
        if (spin_timeout(spins, TMO)) return;
        __builtin_amdgcn_s_sleep(32);
        mk = MARKS(0);
        if (__all(mk != 0u)) break;
        asm volatile("" ::: "memory");
      }
      gate(latest(mk, RFL(mk)) + KD + K0);
      auto feed = [&](auto ST_) {
        constexpr bool ST = ST_.value;
        bool due = true;
        int sf = -1;
#pragma unroll 1
        for (int s = 0, n = 0; s < NSEG && n < 4 * NSEG; ++n) {
#pragma unroll 1
          for (unsigned spins = 0; !due; ++spins) {
            if (spin_timeout(spins, TMO)) return;
            if (spins) __builtin_amdgcn_s_sleep(SLP_H);
            due = __all(FC_LOOK(s) != 0u);
            asm volatile("" ::: "memory");
          }
          TS(4, s, 128);
#pragma unroll 1
          for (unsigned spins = 0; cn[2 + (s & 1)] < 4u * (s >> 1); ++spins) {
            if (spins > 64u * SPIN_CAP) return;
            __builtin_amdgcn_s_sleep(2);
          }
          TS(5, s, 128);
          X_ASK(s)
          v4u t[ST ? 16 : 1];
          if (ST) {
            int so = s * 16384;
#pragma unroll
            for (int i = 0; i < 16; ++i) {
              if ((i & 3) == 0) asm volatile("" : "+s"(so));
              t[i] = LD128(rsS, l16 + (i & 3) * 1024, so);
              if ((i & 3) == 3) so += 4096;
              __builtin_amdgcn_sched_barrier(0);
            }
          }
          mk = MARKS(s + 1);
          unsigned z = 1u;
          if (ST) WAIT_VM(17);
          else WAIT_VM(1);
#pragma unroll 1
          for (int k = b0; k < b1; k += 3) {
            const unsigned* q = reinterpret_cast<const unsigned*>(smem + (s & 1) * 32768 + k * 4096 + l16 + 4);
            const unsigned* q2 = q + min(b1 - k - 1, 2) * 1024;
#pragma unroll
            for (int i = 0; i < 12; i += 2) z = i < 8 ? umin3(z, q[256 * i], q[256 * i + 256]) : umin3(z, q2[256 * (i - 8)], q2[256 * (i - 7)]);
          }
          if (ST) {
#pragma unroll
            for (int i = 0; i < 16; ++i) z = umin3(z, t[i].y, t[i].w);
          }
          due = __all(z != 0u);
          if (!due) {
            due = KR && sf != s;
            sf = s;
            continue;
          }
          asm volatile("" ::: "memory");
          if (l == 0 && !(ST && h4)) __hip_atomic_fetch_add((__attribute__((address_space(3))) unsigned*)cn + (s & 1), h4 ? 2u : 3u, __ATOMIC_RELAXED, __HIP_MEMORY_SCOPE_WORKGROUP);
          TS(6, s, 128);
          if constexpr (ST) {
            stats(t, s);
            TS(7, s, 192);
          }
          due = __all(mk != 0u);
          const unsigned td = latest(mk, RFL(mk)) + (h4 ? KD : PD) + (s < 7 - R0 ? K1 : 0);
          if (!ST && mc && s < NSEG - 1 && s) { MEM_CLEAR(s + 1) }
          if (h4) {
            if (KCT && due) gate(td - KCT);
            KV_CLEAR(s + 2)
          }
          if (due) gate(td);
          ++s;
        }
      };
      static_assert(HX0 == 6, "blocks");
      if (w == 3) feed(Yes{});
      else feed(No{});
      return;
    }
    unsigned tn = now10();
    for (int s = RFL((int)early); s < NSEG; ++s) {
#pragma unroll 1
      for (unsigned spins = 0; !__all(nf != 0u); ++spins) {
        if (spin_timeout(spins, TMO)) return;
        if (spins) __builtin_amdgcn_s_sleep(SLP_H);
        nf = FC_LOOK(s);
        asm volatile("" ::: "memory");
      }
      if (NFW) asm volatile("" ::"v"(nf));
      TS(4, s, 128);
#pragma unroll 1
      for (unsigned spins = 0; cn[2 + (s & 1)] < 4u * (s >> 1); ++spins) {
        if (spins > 64u * SPIN_CAP) return;
        __builtin_amdgcn_s_sleep(2);
      }
      if (s >= PACE0) gate(tn);
      else tn = now10();
      tn += PACE;
      TS(5, s, 128);
      X_ASK(s)
      if (w != 3) {
        nf = FC_LOOK(s + 1);
        if (mc && s < NSEG - 1 && (EL == 0 || MC3 != 2 || s)) {
          MEM_CLEAR(s + 1)
          if (NMC == 8) WAIT_VM(8);
          else WAIT_VM(4);
        } else {
          WAIT_VM(0);
        }
        if (l == 0) __hip_atomic_fetch_add((__attribute__((address_space(3))) unsigned*)cn + (s & 1), 3u, __ATOMIC_RELAXED, __HIP_MEMORY_SCOPE_WORKGROUP);
        TS(6, s, 128);
      } else {
        v4u t[16];
        S_ASK(s, t)
        WAIT_VM(16);
        nf = FC_LOOK(s + 1);
        if (l == 0) __hip_atomic_fetch_add((__attribute__((address_space(3))) unsigned*)cn + (s & 1), 3u, __ATOMIC_RELAXED, __HIP_MEMORY_SCOPE_WORKGROUP);
        stats(t, s);
        TS(7, s, 192);
      }
    }
    return;
  }
  const int RL = !kv, rp = idx & 15, u = RFL(kv ? idx >> 4 : L);
  const __amdgpu_buffer_rsrc_t rsW = MKRS(ws + OFF_PW, SZ_PW);
  const __amdgpu_buffer_rsrc_t rsX = MKRS(ws + (RL ? OFF_X3 : OFF_XA), SZ_X);
  const __amdgpu_buffer_rsrc_t rsY = RL ? MKRS(A.out, SZ_X) : MKRS(ws + OFF_X2, SZ_X);
  const int r = 2 * rp + (g >> 1), yo = r * (DM * 2) + (l & 31) * 16 + u * 512;
  const __amdgpu_buffer_rsrc_t rsC = MKRS(ws + OFF_H, OFF_QK - OFF_H);
#define X_CLEAR(s)                                                                                              \
  {                                                                                                             \
    ST128((v4u{0u, 0u, 0u, 0u}), rsY, yo, (s) * SEG * DM * 2);                                                  \
    if (KD) {                                                                                                   \
      ST128((v4u{0u, 0u, 0u, 0u}), rsC, l * 16, (int)(OFF_X3 - OFF_H) + ((s) * 32 + idx) * 1024);               \
      if (l < 32) ST128((v4u{0u, 0u, 0u, 0u}), rsC, l * 16, (int)(SZ_X / 2) + ((s) * 32 + idx) * 512);          \
    }                                                                                                           \
  }
  if (kv) {
    if (XZD) gate(now10() + XZD);
    if (XZP & 3) {
      if (!wait_n<SLP_V, 32>(FL + FL_FZ + (blockIdx.x & 7) * 64, 64, TMO)) return;
    } else {
      for (int s = w; s < (XZP ? 2 * RG : NSEG); s += RG) X_CLEAR(s)
      DRAIN();
      if (l < 8) set_flag(FL + FL_FZ + l * 64 + 2 * idx + w);
    }
  }
  v4u sel;
#pragma unroll
  for (int e = 0; e < 4; ++e) sel[e] = (g >> 1) == (c >> 3) && ((c & 7) >> 1) == e ? 0x3f80u << (16 * (c & 1)) : 0u;
  unsigned tl = now10();
  int po = (g & 1) * 32768 + (c >> 2) * 128 + (g >> 1) * 64 + (c & 3) * 16;
  asm volatile("" : "+v"(po));
  for (int s = w; s < NSEG; s += RG) {
    v4u xr;
    if (GF_R) {
      gate(tl);
      const gu32* f = (gu32*)(FL + FL_FW) + ((RL * 64 + s) * 4 + (blockIdx.x & 3)) * 4 * 32 + (c >> 2) * 32 + (c & 3);
      unsigned mk;
      for (unsigned spins = 0;; ++spins) {
        tl = now10() + GF_R;
        mk = __hip_atomic_load(f, RLX_AGENT);
        if (__all(mk != 0u) || spin_timeout(spins, TMO)) break;
        if (s < NSEG - RFA) __builtin_amdgcn_s_sleep(SLP_R);
      }
      if (EF) {
        const unsigned tp = latest(mk, tl);
        asm volatile("" ::: "memory");
        xr = LD128(rsX, (l & 31) * 512 + r * 16, s * 32768 + u * 16384);
        gate(tp - (RL && s >= NSEG - RLS ? RLEAD : 0));
        if (GR2) tl = tp + ((unsigned)(s - GR0) >= NSEG - 12 - GR0 ? GR3 : GR2);
      }
      asm volatile("" ::: "memory");
    } else
    grp_wait<SLP_R, 4, GATE_R>(true, nullptr, 0, FL + FL_FW + ((RL * 64 + s) * 4 + (blockIdx.x & 3)) * 4 * 32, 16, TMO, tl);
    if (s >= NSEG - 6 && !(EF != 0 && GR2 != 0)) tl -= GF_R - (s >= NSEG - 4 ? GF_RT : GF_RU);
    if (!EF) DRAIN();
    TS(0, s, w * 64);
    v4u p[8][2];
    if (!EF) xr = LD128(rsX, (l & 31) * 512 + r * 16, s * 32768 + u * 16384);
    f32x4 ac[2];
    const int sr = EF ? s & ((1 << RGB) - 1) : s;
    const unsigned old = (s >> RGB) & 1;
    for (unsigned spins = 0;; ++spins) {
      int so = (RL * 64 + sr) * 16 * 32768 + rp * 2048 + u * 1024;
#pragma unroll
      for (int k = 0; k < 8; ++k) {
        asm volatile("" : "+s"(so));
#pragma unroll
        for (int b = 0; b < 2; ++b) p[k][b] = __builtin_amdgcn_raw_buffer_load_b128(rsW, po + b * 512, so, RAUX);
        so += 65536;
      }
      __builtin_amdgcn_sched_barrier(0);
      unsigned z[2] = {1u, 1u};
      ac[0] = ac[1] = f32x4{};
#pragma unroll
      for (int k = 0; k < 8; ++k)
#pragma unroll
        for (int b = 0; b < 2; ++b) {
          ac[b] = MFMA16(BF8(sel), BF8(p[k][b]), ac[b]);
          z[b] = __builtin_amdgcn_bitop3_b32(z[b], p[k][b].x, old, 0x60);
          __builtin_amdgcn_sched_barrier(0);
        }
      if (!EF || !RVAL || __all((z[0] & z[1]) != 0u) || spin_timeout(spins, TMO)) break;
      if (SLP_D) __builtin_amdgcn_s_sleep(SLP_D);
      asm volatile("" ::: "memory");
    }
    v4u xo;
    {
      const auto x = __builtin_amdgcn_permlane16_swap(pk2s(ac[0][0], ac[0][1]), pk2s(ac[1][0], ac[1][1]), false, false);
      const auto y = __builtin_amdgcn_permlane16_swap(pk2s(ac[0][2], ac[0][3]), pk2s(ac[1][2], ac[1][3]), false, false);
      const v4u a = {x[0], y[0], x[1], y[1]};
#pragma unroll
      for (int e = 0; e < 4; ++e) {
        xo[e] = pk2s(lo16(xr[e]) + lo16(a[e]), hi16(xr[e]) + hi16(a[e]));
        if (kv) xo[e] = xo[e] ? xo[e] : 0x8000u;
      }
      ST128(xo, rsY, yo, s * SEG * DM * 2);
    }
    TS(1, s, w * 64);
    if (kv) {
      const unsigned p0 = __builtin_amdgcn_readlane(xo[0], 0), p1 = __builtin_amdgcn_readlane(xo[0], 32);
      const float pv = lo16(g < 2 ? p0 : p1);
      float sm = 0.f, q0 = 0.f, q1 = 0.f;
#pragma unroll
      for (int e = 0; e < 4; ++e) {
        const float a = one(lo16(xo[e]) - pv), b = one(hi16(xo[e]) - pv);
        sm = one(sm + one(a + b));
        q0 = one(q0 + a * a);
        q1 = one(q1 + b * b);
      }
      sm = half_sum(sm);
      const float q = fmaxf(one(half_sum(q0 + q1)) - sm * sm * (1.0f / 256), 0.f);
      const u64 st = (u64)__float_as_uint(sm + pv * 256.f) | (u64)(__float_as_uint(q) | 1u) << 32;
      if ((l & 31) < 4) __hip_atomic_store((gu64*)(FL + FL_FR + ((s * 4 + (l & 3)) * 4 + (r >> 3)) * 32 + (r & 7) * 4 + 2 * u), st, RLX_AGENT);
      if (XZP == 4 && s + 2 * RG < NSEG) X_CLEAR(s + 2 * RG)
    }
    if (EF && s + (1 << RGB) < NSEG) PW_FREE(RL, s + (1 << RGB), 2 * rp + u);
    TS(3, s, w * 64);
  }
  if (L == 1 && idx == 0 && w == 0 && l == 0) __hip_atomic_store((gu64*)(ws + OFF_EP), tag, RLX_AGENT);
}
__device__ __forceinline__ void role_proj(const MegaArgs& A, char* smem, const int L, const int idx, const bool kv) {
  if (A.wv < 4) role_proj_t<false>(A, smem, L, idx, kv);
  else role_proj_t<true>(A, smem, L, idx, kv);
}

constexpr int CUN = 2144;
__device__ __forceinline__ v2u part_stats(const v4u o) {
  float v[8];
#pragma unroll
  for (int e = 0; e < 4; ++e) { v[2 * e] = lo16(o[e]); v[2 * e + 1] = hi16(o[e]); }
  const float s = ((v[0] + v[1]) + (v[2] + v[3])) + ((v[4] + v[5]) + (v[6] + v[7])), m = s * 0.125f;
  float q0 = 0.f, q1 = 0.f;
#pragma unroll
  for (int e = 0; e < 4; ++e) {
    const float a = v[e] - m, b = v[4 + e] - m;
    q0 += a * a;
    q1 += b * b;
  }
  return v2u{__float_as_uint(s), __float_as_uint(q0 + q1) | 1u};
}
#define PS_ST(o, voff, soff)                                                                                              \
  {                                                                                                                       \
    const v2u p_ = part_stats(o);                                                                                         \
    if (PSW) {                                                                                                            \
      const v4u q_ = {p_.x, p_.y, (unsigned)__builtin_amdgcn_update_dpp(0, (int)p_.x, 0xb1, 0xf, 0xf, true),              \
                      (unsigned)__builtin_amdgcn_update_dpp(0, (int)p_.y, 0xb1, 0xf, 0xf, true)};                         \
      if (!(l & 1)) ST128(q_, rps, voff, soff);                                                                           \
    } else {                                                                                                              \
      ST64(p_, rps, voff, soff);                                                                                          \
    }                                                                                                                     \
  }
constexpr int CRN = 176;
constexpr int CTN = 528;
constexpr int CV_LDS = (8 * CKN + 8 * 2 * CUN + 64 + 2 * 32 * 8 + 16 * CRN + 64 + 8 * CTN) * 2 + 8 * 1568;
__device__ __forceinline__ void role_conv(const MegaArgs& A, char* smem, const int cv) {
  u16* Kr = reinterpret_cast<u16*>(smem);
  u16* Uc = Kr + 8 * CKN;
  u16* Us = Uc + 8 * CUN + 63;
  u16* To = Uc + 8 * 2 * CUN + 64;
  u16* Sx = To + 32 * 8;
  u16* Rc = Sx + 32 * 8;
  u16* Rs = Rc + 8 * CRN + 63;
  u16* Ta = Rc + 16 * CRN + 64;
  const int w = A.wv;
  const int l = lane_id(), tid = 64 * w + l, c = l & 15, g = l >> 4;
  const int d = 8 * cv + w;
  char* ws = A.ws;
  unsigned* FL = (unsigned*)(ws + OFF_FL);
  unsigned* TMO = FL + FL_TMO;
  u16* kr = Kr + w * CKN;
  const u16* ka = kr + 2080 - 16 * c + 8 * g;
#define KFRAG(D0) (*reinterpret_cast<const f16x8*>(ka - (D0)))
  typedef __attribute__((address_space(3))) const unsigned* lpu;
  typedef __attribute__((address_space(3))) u16* lph;
#define BFRAG(a) __builtin_bit_cast(f16x8, (v4u{((lpu)(unsigned long)(a))[0], ((lpu)(unsigned long)(a))[1], ((lpu)(unsigned long)(a))[2], ((lpu)(unsigned long)(a))[3]}))
  f32x4 acc[8], al[5];
  v4u tx[4];
  const __amdgpu_buffer_rsrc_t rz1 = MKRS(ws + OFF_XA, SZ_X), rps = MKRS(ws + OFF_H, SZ_X);
  u16* uc = Uc + w * CUN;
  const unsigned ub = lds_base(reinterpret_cast<char*>(((c & 1) ? Us : Uc) + w * CUN + c + 8 * g + 32));
  const int t0 = 16 * c + 4 * g;
  float dsk, ysc;
  unsigned mk = 0u;
  const __amdgpu_buffer_rsrc_t rm = MKRS(FL + FL_FW + (blockIdx.x & 3) * 128, 63 * 2048 + 512);
#define SKIP(T) (*reinterpret_cast<const v2u*>(uc + 32 + 256 * (T) + t0))
#define TILE_OUT(dst, a, uu)                                                                   \
  {                                                                                            \
    v2u v_;                                                                                    \
    _Pragma("unroll") for (int e = 0; e < 2; ++e) {                                            \
      const unsigned yy = pk2(a[2 * e] * ysc, a[2 * e + 1] * ysc), u_ = uu[e];                 \
      const unsigned k_ = pk2(h2f((u16)u_) * dsk, h2f((u16)(u_ >> 16)) * dsk);                 \
      v_[e] = pk2(lo16(yy) + lo16(k_), hi16(yy) + hi16(k_));                                   \
    }                                                                                          \
    *reinterpret_cast<v2u*>((dst) + t0) = v_;                                                  \
  }
#define CHUNK_ST(i, src, N) CHUNK_STX(w + 8 * (i), tx[i], src, N)
#define CHUNK_STX(i, X, src, N)                                                                \
  {                                                                                            \
    const unsigned p_ = lds_base((char*)((src) + (c >> 2) * (N) + (tid & ~15) + 4 * (c & 3))); \
    const v2u a0_ = trd(p_), a1_ = trd(p_ + 8 * (N));                                          \
    const v4u a_ = {a0_.x, a0_.y, a1_.x, a1_.y};                                               \
    v4u o;                                                                                     \
    _Pragma("unroll") for (int e = 0; e < 4; ++e) o[e] = pk2(lo16(X[e]) + lo16(a_[e]), hi16(X[e]) + hi16(a_[e])); \
    ST128(o, rz1, (l >> 5) * 32768 + (l & 31) * 16, (2 * (i) * 64 + cv) * 512);                \
    PS_ST(o, (l >> 5) * 16384 + (l & 31) * 8, (2 * (i) * 64 + cv) * 256)                        \
  }
#define CHUNK_FLAG(i) set_flag(FL + FL_FC + ((8 * (i) + l) * 8 + (cv >> 3)) * 32 + (cv & 7))
  {
#define U_VAL(i)                                                                               \
  {                                                                                            \
    const float mb_ = lo16(st[i]), ri_ = 1024.0f / hi16(st[i]);                                 \
    _Pragma("unroll") for (int e = 0; e < 4; ++e) {                                            \
      const unsigned t_ = pk2(lo16(tx[i][e]) - mb_, hi16(tx[i][e]) - mb_);                     \
      const unsigned q_ = pk2(lo16(t_) * ri_, hi16(t_) * ri_);                                 \
      uv[e] = pkh(lo16(q_), hi16(q_));                                                         \
    }                                                                                          \
  }
    unsigned ta[3], k0p, st[4];
    v4u uv;
    v2u kn;
    u64 tag;
    const __amdgpu_buffer_rsrc_t rst = MKRS(ws + OFF_GT, (L_SEQ + 512) * 16);
    {
      KGen G;
      G.load(A, d, l);
      const u16 pk = A.Dskip[d];
      asm volatile("" ::: "memory");
      if (XL == 1) ask_slow(A, 16 * (w - 4) + (cv & 15), ta);
      if (XL) asm volatile("" : "+v"(G.pa), "+v"(G.pb), "+v"(G.pc), "+v"(G.pd), "+v"(G.pe)::"memory");
      tx[0] = *reinterpret_cast<const v4u*>(A.x0 + (size_t)tid * DM + 8 * cv);
      if (XL != 1) ask_slow(A, 16 * (w - 4) + (cv & 15), ta);
      tag = call_tag(A);
      G.setup();
      dsk = bf2f(pk) * (1.0f / 1024);
      G.st = smem + w * 16640;
      SST(0);
      ysc = G.scale();
      const Cx z = G.power(1.f);
      Cx y = cmul(G.C, z);
      G.rows(G.st + l * 8, y, z, 16);
      G.get(G.bh, G.bl, l);
      const float k0 = 2.0f * wave_sum(G.C.r.x + G.C.r.y);
      k0p = l == 0 ? pkh(k0, 0.f) : 0u;
      if (TP) {
        constexpr int R = 64 * TP, U0 = 2 * R + 32, U1 = 3 * R + 111, KN = 4 * R + 192;
        u16* k1 = reinterpret_cast<u16*>(smem + 8 * 16640) + w * KN;
        u16* t1 = reinterpret_cast<u16*>(smem + 8 * 16640) + 8 * KN;
        v4u s1 = {};
        u64 rv = 0;
        if (w < TP) {
          s1 = LD128(rst, tid * 16, (L_SEQ + R * (cv >> 4)) * 16);
          rv = ready_look(A);
        }
        if (l < R / 8 + 4) reinterpret_cast<v4u*>(k1)[l < R / 8 ? l + R / 8 + 4 : l - R / 8] = v4u{k0p, 0u, 0u, 0u};
        k1[U0 + (l < 32 ? l : R + l)] = k1[U1 + (l < 32 ? l : R + l)] = 0;
        G.columns(l, 0, R / 16);
        const f32x4 kt = G.tile<true>(l);
        if (c < R / 16) *reinterpret_cast<v2u*>(k1 + R + 28 - 16 * c - 4 * g) = v2u{pkh(kt.w, kt.z), pkh(kt.y, kt.x)};
        if (w < TP) {
          for (unsigned spins = 0; !__all(has_tag(tag, s1.x, s1.y) && has_tag(tag, s1.z, s1.w)) && spins < SPIN_CAP; ++spins) {
            __builtin_amdgcn_s_sleep(4);
            s1 = LD128(rst, tid * 16, (L_SEQ + R * (cv >> 4)) * 16);
          }
          st[0] = __builtin_amdgcn_perm(s1.w, s1.y, 0x07060302);
          U_VAL(0);
          u16* up = reinterpret_cast<u16*>(smem + 8 * 16640) + 32 + tid;
#pragma unroll
          for (int e = 0; e < 8; ++e) up[e * KN + U0] = up[e * KN + U1] = (u16)(uv[e >> 1] >> (16 * (e & 1)));
        }
        RAW_BAR();
        const unsigned u1 = lds_base(reinterpret_cast<char*>(k1 + ((c & 1) ? U1 : U0) + c + 8 * g));
        f32x4 a = {};
#pragma unroll
        for (int b = 0; b <= R / 32; ++b)
          a = MFMAH(BFRAG(u1 + 64 * b), *reinterpret_cast<const f16x8*>(k1 + R - 16 * (c & (R / 16 - 1)) + 8 * g + 32 * b), a);
        if (c < R / 16) {
          const v2u sk = *reinterpret_cast<const v2u*>(k1 + U0 + 32 + t0);
          TILE_OUT(t1 + w * (R + 16), a, sk);
        }
        RAW_BAR();
        SST(1);
        if (w < TP) CHUNK_ST(0, t1, R + 16);
        G.columns(l, R / 16, TPS);
        if (w < TP) {
          DRAIN();
          ready_wait(A, tag, rv);
          if (l < 8) CHUNK_FLAG(w);
        }
        SST(6);
        G.columns(l, TPS, 16);
      } else {
        G.columns(l, 0, 16);
      }
      const f32x4 kt = G.tile<true>(l);
      kn = v2u{pkh(kt.w, kt.z), pkh(kt.y, kt.x)};
    }
#define U_PUT(i)                                                                               \
  _Pragma("unroll") for (int e = 0; e < 4; ++e) {                                              \
    ((lph)(unsigned long)up0)[(2 * e) * CUN + 512 * (i)] = (u16)uv[e];                         \
    ((lph)(unsigned long)up0)[(2 * e + 1) * CUN + 512 * (i)] = (u16)(uv[e] >> 16);             \
    ((lph)(unsigned long)up1)[(2 * e) * CUN + 512 * (i)] = (u16)uv[e];                         \
    ((lph)(unsigned long)up1)[(2 * e + 1) * CUN + 512 * (i)] = (u16)(uv[e] >> 16);             \
  }
#define U_ROW(i) { U_VAL(i) U_PUT(i) }
    unsigned up0 = lds_base((char*)(Uc + 32 + tid)), up1 = lds_base((char*)(Us + 32 + tid));
    asm volatile("" : "+v"(up0), "+v"(up1), "+v"(kn), "+v"(k0p));
#define ST_GET(I0, I1)                                                                                                        \
  for (unsigned spins = 0; spins < SPIN_CAP; ++spins) {                                                                       \
    bool ok = true;                                                                                                           \
    v4u t_[4];                                                                                                                \
    _Pragma("unroll") for (int i = I0; i < I1; ++i) t_[i] = LD128(rst, (tid + 512 * i) * 16, 0);                              \
    _Pragma("unroll") for (int i = I0; i < I1; ++i) {                                                                         \
      ok = ok && has_tag(tag, t_[i].x, t_[i].y) && has_tag(tag, t_[i].z, t_[i].w);                                            \
      st[i] = __builtin_amdgcn_perm(t_[i].w, t_[i].y, 0x07060302);                                                            \
    }                                                                                                                         \
    if (__all(ok)) break;                                                                                                     \
    __builtin_amdgcn_s_sleep(4);                                                                                              \
    asm volatile("" ::: "memory");                                                                                            \
  }
    if (w < 4) {
      ST_GET(0, 2);
      U_VAL(0);
      asm volatile("" : "+v"(uv));
    }
#pragma unroll
    for (int i = 1; i < 4; ++i) tx[i] = *reinterpret_cast<const v4u*>(A.x0 + (size_t)(tid + 512 * i) * DM + 8 * cv);
    RAW_BAR();
    SST(2);
    if (w < 4) { U_PUT(0) }
    *reinterpret_cast<v2u*>(kr + 2076 - 16 * c - 4 * g) = kn;
    if (l < 38) reinterpret_cast<v4u*>(kr)[l < 32 ? 260 + l : l < 34 ? 194 + l : l - 34] = v4u{k0p, 0u, 0u, 0u};
    (l < 32 ? uc : Us + w * CUN - 32)[l] = 0;
    uc[32 + L_SEQ + l] = Us[w * CUN + 32 + L_SEQ + l] = 0;
    uc[32 + 256 + l] = Us[w * CUN + 32 + 256 + l] = 0;
    RAW_BAR();
    SST(11);
    {
      f32x4 a0 = {}, a1 = {};
      f16x8 bf[9], af[9];
#pragma unroll
      for (int b = 0; b < 9; ++b) {
        af[b] = KFRAG(32 - 32 * b);
        bf[b] = BFRAG(ub + 64 * (b - 1));
      }
      const v2u k0 = SKIP(0);
      __builtin_amdgcn_sched_barrier(0);
#pragma unroll
      for (int b = 0; b < 9; ++b) (b & 1 ? a1 : a0) = MFMAH(bf[b], af[b], (b & 1 ? a1 : a0));
      a0 += a1;
      TILE_OUT(Ta + w * CTN, a0, k0);
    }
    const u64 rv = ready_look(A);
    RAW_BAR();
    SST(3);
    if (w < 4) {
      if (w >= TP) CHUNK_ST(0, Ta, CTN);
      asm volatile("" : "+v"(tx[1]));
      U_ROW(1);
      DRAIN();
      ready_wait(A, tag, rv);
      if (l < 8 && w >= TP) CHUNK_FLAG(w);
      ST_GET(2, 4);
    } else {
      ST_GET(0, 4);
      ready_wait(A, tag, rv);
      asm volatile("" : "+v"(tx[0]), "+v"(tx[1]));
      U_ROW(0);
      U_ROW(1);
    }
    ASKED(ta);
    if (tid < (16 * CRN + 64) / 8) reinterpret_cast<v4u*>(Rc)[tid] = v4u{0u, 0u, 0u, 0u};
    SST(4);
    const gu64* fk = (gu64*)(ws + OFF_KG) + cv * 16 + w;
    u64 kg = __hip_atomic_load(fk, RLX_AGENT);
    U_ROW(2);
    for (unsigned spins = 0; !__all(kg == tag) && spins < SPIN_CAP; ++spins) {
      __builtin_amdgcn_s_sleep(8);
      kg = __hip_atomic_load(fk, RLX_AGENT);
    }
    asm volatile("" : "+v"(tx[3]), "+v"(st[3])::"memory");
    SST(5);
    const unsigned* kd = reinterpret_cast<const unsigned*>(Ta + 8 * CTN) + w * 392;
    glds16c(ws + OFF_KF + d * 2048, l * 16, lds_base((char*)kd));
    if (l < 34) glds16c(ws + OFF_KF + d * 2048 + 1024, l * 16, lds_base((char*)kd) + 1024);
    U_ROW(3);
    DRAIN();
    {
#define MIX(lo, hi) __builtin_amdgcn_perm(hi, lo, 0x07060100)
      const unsigned a0 = kd[384 - l], b0 = kd[383 - l], a1 = kd[320 - l], b1 = kd[319 - l];
      const unsigned a2 = kd[256 - 2 * l], m2 = kd[255 - 2 * l], b2 = kd[254 - 2 * l];
      const unsigned* k3 = kd + 124 - 4 * (l & 31);
      v4u* ko = reinterpret_cast<v4u*>(kr) + l;
      ko[4] = v4u{a0, a0, (l & 1) ? b0 : MIX(a0, b0), b0};
      ko[68] = v4u{a1, a1, (l & 1) ? b1 : MIX(a1, b1), b1};
      ko[132] = v4u{a2, MIX(a2, m2), m2, b2};
      if (l < 32) ko[196] = v4u{k3[4], MIX(k3[3], k3[2]), k3[2], MIX(k3[1], k3[0])};
#undef MIX
    }
    RAW_BAR();
    {
      const f16x8 bm = BFRAG(ub - 64);
      f32x4 a1 = MFMAH(bm, KFRAG(256 + 32), (f32x4{})), a2 = MFMAH(bm, KFRAG(512 + 32), (f32x4{}));
#pragma unroll 2
      for (int r = 0; r < 8; ++r) {
        f16x8 a[3], bf[3];
#pragma unroll
        for (int e = 0; e < 3; ++e) {
          a[e] = KFRAG(256 * e - 32 * r);
          bf[e] = BFRAG(ub + 512 * e + 64 * r);
        }
#pragma unroll
        for (int e = 0; e < 3; ++e) {
          a2 = MFMAH(bf[2 - e], a[e], a2);
          if (e < 2) a1 = MFMAH(bf[1 - e], a[e], a1);
        }
      }
      SST(8);
      const v2u k1 = SKIP(1), k2 = SKIP(2);
      TILE_OUT(Ta + w * CTN + 256, a1, k1);
      TILE_OUT(Ta + w * CTN, a2, k2);
    }
    RAW_BAR();
    SST(10);
    if (w < 4) {
      tx[0] = tx[1];
      tx[1] = tx[2];
      tx[2] = tx[3];
    }
    CHUNK_STX(w < 4 ? w + 8 : w, tx[0], Ta, CTN);
    {
      const f16x8 bm = BFRAG(ub - 64);
#pragma unroll
      for (int T = 3; T < 8; ++T) al[T - 3] = MFMAH(bm, KFRAG(256 * T + 32), (f32x4{}));
    }
    SST(7);
    if (RSK && w >= 4) __builtin_amdgcn_s_sleep(RSK);
#undef U_ROW
#undef U_VAL
#undef U_PUT
#undef ST_GET
  }
  const unsigned rb = lds_base(reinterpret_cast<char*>(((c & 1) ? Rs : Rc) + w * CRN + c + 8 * g));
#pragma unroll
  for (int j = 0; j < 8; ++j) acc[j] = f32x4{};
  const __amdgpu_buffer_rsrc_t rx = MKRS(ws + OFF_X2, SZ_X), rz = MKRS(ws + OFF_X3, SZ_X);
  const __amdgpu_buffer_rsrc_t rf = MKRS(FL + FL_FR, 64 * 16 * 128);
  v2u st = {0u, 0u}, xx = {0u, 0u};
#define X2_LOAD(ss, far)                                                                                                      \
  {                                                                                                                      \
    xx = LD64(rx, (l >> 1) * (DM * 2) + (l & 1) * 8, (far) ? (int)SZ_X : ((ss) * SEG * DM + 8 * cv) * 2);                 \
    st = LD64(rf, l * 8, ((ss) * 4 + (blockIdx.x & 3)) * 512);                                                           \
  }
  unsigned tl = now10();
  static_assert(R0 >= 2 && PP > 8 - R0, "rounds");
  if (CVP && w == 1) __builtin_amdgcn_s_setprio(CVP);
  for (int s = -R0; s < NSEG; ++s) {
    const int q = s & 3, q8 = s & 7;
    const u16* kq = ka + 32 * q8;
    if (w == 1 && s >= 0) {
      gate(tl);
      X2_LOAD(s, 0);
    }
    asm volatile("" ::: "memory");
    if (s < 8 - R0) {
      const int r = s + R0;
      const u16* kl = ka + 32 * r - 256 * 7;
      unsigned uq[4] = {ub + 64 * r, ub + 64 * r + 1024, ub + 64 * r + 2048, ub + 64 * r + 3072};
      asm volatile("" : "+v"(uq[0]), "+v"(uq[1]), "+v"(uq[2]), "+v"(uq[3]));
      f16x8 bf[8], a[8];
#pragma unroll
      for (int e = 7; e >= 0; --e) {
        a[e] = *reinterpret_cast<const f16x8*>(kl + 256 * (7 - e));
        bf[7 - e] = BFRAG(uq[(7 - e) >> 1] + 512 * ((7 - e) & 1));
      }
      __builtin_amdgcn_sched_barrier(0);
#pragma unroll
      for (int e = 7; e >= 0; --e)
#pragma unroll
        for (int T = (e > 3 ? e : 3); T < 8; ++T) al[T - 3] = MFMAH(bf[T - e], a[e], al[T - 3]);
      if (r == 1) {
        DRAIN();
        if (l < 8) CHUNK_FLAG(w < 4 ? w + 8 : w);
      }
      if (s == -1) SST(9);
      if (s == -1 && w == 1) {
        (void)wait_n<SLP_V, 32>(FL + FL_FZ + (blockIdx.x & 7) * 64, 64, TMO);
#pragma unroll 1
        for (unsigned spins = 0; spins < SPIN_CAP; ++spins) {
          mk = MARKS(0);
          if (__all(mk != 0u)) break;
          __builtin_amdgcn_s_sleep(16);
          asm volatile("" ::: "memory");
        }
        tl = latest(mk, tl) + CE;
      }
      if (s < 0) continue;
    }
    f16x8 a0 = *reinterpret_cast<const f16x8*>(kq - 32), a1 = *reinterpret_cast<const f16x8*>(kq);
    asm volatile("" : "+v"(a0), "+v"(a1));
    if (w == 1) {
#pragma unroll 1
      for (unsigned spins = 0; !__all(umin3(xx.x, xx.y, st.y) != 0u); ++spins) {
        if (spin_timeout(spins, TMO)) break;
        __builtin_amdgcn_s_sleep(SLP_V);
        X2_LOAD(s, __builtin_popcountll(__ballot(st.y != 0u)) < CVT2);
      }
      TS(0, s, 64);
      const float s0 = __uint_as_float(st.x), s1 = dppr<0xb1>(s0), m2 = __uint_as_float(st.y);
      const float var = (m2 + dppr<0xb1>(m2) + (s0 - s1) * (s0 - s1) * (1.0f / DM)) * (1.0f / DM);
      const float mb = bfr1((s0 + s1) * (1.0f / DM)), ri = 1024.0f * __builtin_amdgcn_rcpf(bfr1(__builtin_amdgcn_sqrtf(bfr1(bfr1(var) + 1e-5f))));
      const unsigned t0 = pk2(lo16(xx.x) - mb, hi16(xx.x) - mb), t1 = pk2(lo16(xx.y) - mb, hi16(xx.y) - mb);
      const unsigned b0 = pk2(lo16(t0) * ri, hi16(t0) * ri), b1 = pk2(lo16(t1) * ri, hi16(t1) * ri);
      const unsigned u0 = pkh(lo16(b0), hi16(b0)), u1 = pkh(lo16(b1), hi16(b1));
      u16* rp = Rc + (l & 1) * 4 * CRN + 32 * q + (l >> 1);
#define R_PUT(o) { rp[o] = (u16)u0; rp[(o) + CRN] = (u16)(u0 >> 16); rp[(o) + 2 * CRN] = (u16)u1; rp[(o) + 3 * CRN] = (u16)(u1 >> 16); }
      R_PUT(0) R_PUT(8 * CRN + 63)
      if (q == 0) { R_PUT(128) R_PUT(8 * CRN + 63 + 128) }
#undef R_PUT
      *reinterpret_cast<v2u*>(Sx + l * 4) = xx;
    }
    unsigned ap = rb + 64 * ((q + 3) & 3), ac = rb + 64 * q, au = lds_base((char*)(Rc + w * CRN + 32 * q + c));
    asm volatile("" : "+v"(ap), "+v"(ac), "+v"(au));
    RAW_BAR();
    TS(1, s, 0);
    const f16x8 bp = BFRAG(ap), bc = BFRAG(ac);
    const u16 h0 = ((lph)(unsigned long)au)[0], h1 = ((lph)(unsigned long)au)[16], y0 = Sx[c * 8 + w], y1 = Sx[(16 + c) * 8 + w];
    __builtin_amdgcn_sched_barrier(0);
    const float u0 = h2f(h0), u1 = h2f(h1), x0 = bf2f(y0), x1 = bf2f(y1);
    acc[0] = MFMAH(a0, bp, (q8 == 0 ? acc[1] : acc[0]));
    const f32x4 tmp = MFMAH(a1, bc, acc[0]);
    const unsigned uu = pk2(u0 * dsk, u1 * dsk);
    const unsigned yy = pk2(((q & 1) ? tmp[2] : tmp[0]) * ysc, ((q & 1) ? tmp[3] : tmp[1]) * ysc);
    const unsigned aa = pk2(lo16(yy) + lo16(uu), hi16(yy) + hi16(uu)), oo = pk2(x0 + lo16(aa), x1 + hi16(aa));
    if (g == (q8 >> 1)) {
      To[c * 8 + w] = (u16)oo;
      To[(16 + c) * 8 + w] = (u16)(oo >> 16);
    }
    RAW_BAR();
    if (w == 0 && l < 32) {
      v4u o = *reinterpret_cast<const v4u*>(To + l * 8);
      o.y = o.y ? o.y : 0x8000u;
      ST128A(o, rz, l * 16, (s * 64 + cv) * 512, XAUX);
      PS_ST(o, l * 8, ((64 + s) * 64 + cv) * 256)
    }
    if (w == 1) {
      tl = now10() + GATE_V - 40;
      mk = MARKS(s + 1);
    }
    const int left = 7 - (s >> 3);
#define PUSH(J0, SH)                                                                                              \
  if (left >= (J0)) {                                                                                             \
    _Pragma("unroll") for (int j = (J0); j < (J0) + 2 && j < 8; ++j)                                              \
      acc[j] = MFMAH(*reinterpret_cast<const f16x8*>(kq - 32 - 256 * j), bp, (j + (SH) < 8 ? acc[(j + (SH)) & 7] : f32x4{})); \
  }
    TS(2, s, 0);
    if (w == 0) {
      DRAIN();
      if (l < 4) set_flag(FL + FL_FC + (((64 + s) * 4 + l) * 8 + (cv >> 3)) * 32 + (cv & 7));
    }
    TS(3, s, 0);
    if (q8 == 0) { PUSH(1, 1) PUSH(3, 1) PUSH(5, 1) PUSH(7, 1) }
    else { PUSH(1, 0) PUSH(3, 0) PUSH(5, 0) PUSH(7, 0) }
#undef PUSH
    if (w == 1 && __all(mk != 0u)) tl = latest(mk, tl) + CE;
    if (s == 8 - R0) {
      v2u k[5];
#pragma unroll
      for (int T = 3; T < 8; ++T) k[T - 3] = SKIP(T);
#pragma unroll
      for (int T = 3; T < 8; ++T) TILE_OUT(uc + 32 + 256 * T, al[T - 3], k[T - 3]);
    }
    {
      const int n = 12 + PW * (s - PP), wi = n + ((w - n) & 7), wf = wi - 8 * (PW < 8);
      if (s > PP && wf < 32 && wf >= n - PW && wf < n) {
        DRAIN();
        if (l < 8) CHUNK_FLAG(wf);
      }
      if (s >= PP && wi < 32 && wi < n + PW) {
        CHUNK_STX(wi, tx[1], Uc + 32 + 64 * (wi & 24), CUN);
        tx[1] = tx[2];
        tx[2] = tx[3];
      }
    }
    MSTAMP();
  }
#undef KFRAG
#undef BFRAG
#undef X2_LOAD
#undef TILE_OUT
#undef SKIP
#undef CHUNK_ST
#undef CHUNK_STX
#undef CHUNK_FLAG
}

__device__ __forceinline__ v4u ln_row_packed(const v4u xr) {
  float v[8];
#pragma unroll
  for (int e = 0; e < 4; ++e) { v[2 * e] = lo16(xr[e]); v[2 * e + 1] = hi16(xr[e]); }
  float s = ((v[0] + v[1]) + (v[2] + v[3])) + ((v[4] + v[5]) + (v[6] + v[7]));
  s = wave_sum(s);
  const float mean = s * (1.0f / DM);
  float q = 0.f;
#pragma unroll
  for (int j = 0; j < 8; ++j) { float t = v[j] - mean; q += t * t; }
  q = wave_sum(q);
  const float var = q * (1.0f / DM);
  const float mb = bfr(mean);
  const float sd = bfr(sqrtf(bfr(bfr(var) + 1e-5f)));
  const float ri = 1.0f / sd;
  v4u o;
#pragma unroll
  for (int e = 0; e < 4; ++e) {
    const unsigned t = pk2(v[2 * e] - mb, v[2 * e + 1] - mb);
    const float x0 = lo16(t), x1 = hi16(t);
    float q0 = x0 * ri, q1 = x1 * ri;
    q0 = __builtin_fmaf(__builtin_fmaf(-q0, sd, x0), ri, q0);
    q1 = __builtin_fmaf(__builtin_fmaf(-q1, sd, x1), ri, q1);
    o[e] = pk2(q0, q1);
  }
  return o;
}

__device__ __forceinline__ void ln4_packed(v4u (&x)[4]) {
  f32x2_t v[16];
#pragma unroll
  for (int e = 0; e < 16; ++e) v[e] = f32x2_t{lo16(x[e >> 2][e & 3]), hi16(x[e >> 2][e & 3])};
  const f32x2_t t = (((v[0] + v[1]) + (v[2] + v[3])) + ((v[4] + v[5]) + (v[6] + v[7]))) +
                    (((v[8] + v[9]) + (v[10] + v[11])) + ((v[12] + v[13]) + (v[14] + v[15])));
  const float mean = red_mod4(t.x + t.y) * (1.0f / DM);
  const float mb = bfr1(mean);
  f32x2_t q0 = {0.f, 0.f}, q1 = {0.f, 0.f};
#pragma unroll
  for (int e = 0; e < 16; e += 2) {
    v[e] -= mb;
    v[e + 1] -= mb;
    q0 += v[e] * v[e];
    q1 += v[e + 1] * v[e + 1];
  }
  q0 += q1;
  const float dm = mean - mb;
  const float var = fmaxf(red_mod4(q0.x + q0.y) * (1.0f / DM) - dm * dm, 0.f);
  const float sd = bfr1(sqrtf(bfr1(bfr1(var) + 1e-5f)));
  const float ri = 1.0f / sd;
#pragma unroll
  for (int e = 0; e < 16; ++e) {
    const unsigned t2 = pk2(v[e].x, v[e].y);
    const f32x2_t q = f32x2_t{lo16(t2), hi16(t2)} * ri;
    x[e >> 2][e & 3] = pk2(q.x, q.y);
  }
}

constexpr int MEGA_WGS = 256;
constexpr size_t MEGA_LDS = (size_t)(32 * 4 * 64 * 8 + 8 * SMS + 15 * QS + 128 + 16 * 64 * 8 + 8 * SOS + 512) * 2;
static_assert(MEGA_LDS <= 163840 && MEGA_LDS - 480 >= C_SQ + 4096, "lds");
static_assert(MEGA_LDS >= (size_t)AW_CNT + 128 && MEGA_LDS >= (size_t)P_CN + 512, "lds");
static_assert(MEGA_LDS >= (size_t)CV_LDS, "lds");
static_assert(MEGA_LDS >= (size_t)2 * 32 * SMS * 2 && MEGA_LDS >= 131072, "lds");
static_assert(MEGA_LDS >= (size_t)8 * (16640 + 2 * (704 + 144)), "lds");
__device__ __forceinline__ MegaArgs role_args(const int b, const int wv, const unsigned k0, const unsigned k1) {
  auto p = (const __attribute__((address_space(4))) u64*)__builtin_amdgcn_kernarg_segment_ptr();
  asm volatile("s_waitcnt lgkmcnt(0)" : "+s"(p) : "s"(k0), "s"(k1));
#define KA(i) ((u16*)(__attribute__((address_space(1))) u16*)p[i])
  return MegaArgs{KA(0), KA(1), (char*)KA(2), KA(3), KA(4), KA(5), KA(6), KA(7), KA(8), KA(9), KA(10), KA(11), KA(12), KA(13), b, wv};
#undef KA
}
__global__ __launch_bounds__(512, 2) void mega_kernel(const u16* mem0, const u16* Dskip, char* ws, u16* out, const u16* x0,
                                                      const u16* logAre, const u16* Aim, const u16* Cre, const u16* Cim,
                                                      const u16* logdt, const u16* Wq, const u16* Wk, const u16* Wv,
                                                      const u16* Wo) {
  extern __shared__ __attribute__((aligned(16))) char smem[];
  unsigned k0 = 0u, k1 = 0u;
  if (IPF == 3) {
    unsigned o;
    asm volatile(
        "s_load_dword %0, %3, 0\n\ts_load_dword %1, %3, 64\n\ts_getpc_b64 s[6:7]\n.LKG_PC:\n\ts_bfe_u32 s4, %4, 0x30003\n\t"
        "v_lshl_add_u32 %2, %5, 3, s4\n\tv_lshlrev_b32 %2, 7, %2\n\ts_mov_b32 m0, %6\n\ts_mov_b32 s4, .LKG_LAST-.LKG_PC\n\t"
        "s_max_i32 s4, s4, .LKG_LAS2-.LKG_PC\n\tv_cmp_gt_i32 vcc, s4, %2\n\t"
        "s_and_saveexec_b64 s[4:5], vcc\n\tglobal_load_lds_dword %2, s[6:7]\n\ts_mov_b64 exec, s[4:5]"
        : "=&s"(k0), "=&s"(k1), "=&v"(o)
        : "s"(__builtin_amdgcn_kernarg_segment_ptr()), "s"(blockIdx.x), "v"(threadIdx.x), "s"(lds_base(smem) + (unsigned)MEGA_LDS - 480)
        : "memory", "vcc", "scc", "s4", "s5", "s6", "s7");
  }
  const int b = vbid(), wv = __builtin_amdgcn_readfirstlane(threadIdx.x >> 6);
#define A role_args(b, wv, k0, k1)
  u64 tag = 0;
  if (__builtin_expect((0xde000ff0u >> (b >> 3)) & 1u, 0)) {
    const unsigned t0 = now10();
    if ((b >= 32 && b < 64) || (b >= 200 && b < 232)) start_kgen(A, smem, b < 64 ? b - 32 : b - 168);
    else if (b >= 64 && b < 96) tag = start_stats<4>(A, b - 64);
    else if (b >= 240) (void)start_stats<8>(A, b - 224);
    const unsigned dl = (b >= 32 && b < 64) ? DL_C : (b >= 64 && b < 80) ? DL_A : (b >= 80 && b < 96) ? DL_A2 : b >= 240 ? DL_R2 : ((b >= 96 && b < 104) || b >= 232) ? DL_R : (b >= 200 && b < 232) ? DL_Q : 0u;
    while (now10() - t0 < dl) __builtin_amdgcn_s_sleep(16);
    __builtin_amdgcn_s_waitcnt(0x0f70);
  }
  if ((EXP_OFF & 1) && b >= 64 && b < 80) return;
  if ((EXP_OFF & 8) && b >= 200 && b < 232) return;
  if ((EXP_OFF & 16) && b >= 32 && b < 64) return;
  if ((EXP_OFF & 32) && b >= 80 && b < 96) return;
  if (b < 64) role_chain(A, smem, b >> 5, b & 31, 0);
  else if (b < 96) role_aw(A, smem, (b >> 4) & 1, b & 15, tag);
  else if (b >= 104 && b < 168) role_conv(A, smem, b - 104);
  else {
    const bool kv = b >= 168 && b < 232;
    role_proj(A, smem, b >= (kv ? 200 : 240), kv ? (b - 168) & 31 : b & 15, kv);
  }
#undef A
}

extern "C" void kernel_launch(void* const* d_in, const int* in_sizes, int n_in, void* d_out, int out_size, void* d_ws,
                              size_t ws_size, hipStream_t stream) {
  MegaArgs A;
  A.x0 = (const u16*)d_in[0];
  A.logAre = (const u16*)d_in[1];
  A.Aim = (const u16*)d_in[2];
  A.Cre = (const u16*)d_in[3];
  A.Cim = (const u16*)d_in[4];
  A.logdt = (const u16*)d_in[5];
  A.Dskip = (const u16*)d_in[6];
  A.mem0 = (const u16*)d_in[8];
  A.Wq = (const u16*)d_in[9];
  A.Wk = (const u16*)d_in[10];
  A.Wv = (const u16*)d_in[11];
  A.Wo = (const u16*)d_in[12];
  A.ws = (char*)d_ws;
  A.out = (u16*)d_out;

  (void)hipFuncSetAttribute((const void*)mega_kernel, hipFuncAttributeMaxDynamicSharedMemorySize, (int)MEGA_LDS);
  mega_kernel<<<MEGA_WGS, 512, MEGA_LDS, stream>>>(A.mem0, A.Dskip, A.ws, A.out, A.x0, A.logAre, A.Aim, A.Cre, A.Cim, A.logdt,
                                                   A.Wq, A.Wk, A.Wv, A.Wo);
}
```

```cpp
#include <hip/hip_runtime.h>
#include <hip/hip_bf16.h>
#include <stdint.h>

constexpr int L_SEQ = 2048;
constexpr int DM    = 512;
constexpr int NST   = 128;
constexpr int NH    = 32;
constexpr int DH    = 128;
constexpr int HD    = NH * DH;
constexpr int MM    = 8;
constexpr int SEG   = 32;
constexpr int NSEG  = L_SEQ / SEG;
constexpr int LK    = MM + SEG;

typedef unsigned short u16;
using bf16x8 = __attribute__((ext_vector_type(8))) short;
using f32x4  = __attribute__((ext_vector_type(4))) float;
using f32x16 = __attribute__((ext_vector_type(16))) float;

static __device__ __forceinline__ u16 f2bf(float f) {
  unsigned u = __float_as_uint(f);
  u += 0x7fffu + ((u >> 16) & 1u);
  return (u16)(u >> 16);
}
static __device__ __forceinline__ float bf2f(u16 h) { return __uint_as_float(((unsigned)h) << 16); }
static __device__ __forceinline__ float bfr(float f) { return bf2f(f2bf(f)); }

constexpr size_t SZ_X      = (size_t)L_SEQ * DM * 2;
constexpr size_t OFF_KF    = 0;
constexpr size_t OFF_UT    = OFF_KF + (size_t)DM * L_SEQ * 4;
constexpr size_t OFF_YT    = OFF_UT + SZ_X;
constexpr size_t OFF_XA    = OFF_YT + SZ_X;
constexpr size_t OFF_H     = OFF_XA + SZ_X;
constexpr size_t OFF_X2    = OFF_H + SZ_X;
constexpr size_t OFF_U2    = OFF_X2 + SZ_X;
constexpr size_t OFF_X3    = OFF_U2 + SZ_X;
constexpr size_t SZ_QK     = (size_t)L_SEQ * 2 * HD * 2;
constexpr size_t OFF_QK    = OFF_X3 + SZ_X;
constexpr size_t SZ_O      = (size_t)L_SEQ * HD * 2;
constexpr size_t OFF_VF    = OFF_QK + 2 * SZ_QK;
constexpr size_t SZ_PW     = (size_t)2 * NSEG * 16 * SEG * DM * 2;
constexpr size_t OFF_PW    = OFF_VF + 2 * SZ_O;
constexpr size_t SZ_MEM    = (size_t)NSEG * MM * HD * 2;
constexpr size_t OFF_MEMK  = OFF_PW + SZ_PW;
constexpr size_t OFF_MEMV  = OFF_MEMK + 2 * SZ_MEM;
constexpr size_t OFF_GT    = OFF_MEMV + 2 * SZ_MEM;
constexpr size_t OFF_EP    = OFF_GT + 65536;
constexpr size_t OFF_RD    = OFF_EP + 4096;
constexpr size_t OFF_KG    = OFF_RD + 4096;
constexpr size_t OFF_G     = OFF_KG + 8192;
constexpr size_t SZ_ACC    = (size_t)2 * 65536;
constexpr size_t OFF_Z     = OFF_GT + (size_t)128 * DM * 4;
constexpr size_t OFF_ACC   = OFF_Z;
constexpr size_t OFF_FL    = OFF_ACC + 2 * SZ_ACC;
constexpr int FL_FK  = 0;
constexpr int FL_FW  = FL_FK + 64 * 8 * 32;
constexpr int FL_FR  = FL_FW + 2 * 64 * 16 * 32;
constexpr int FL_FC  = FL_FR + 64 * 16 * 32;
constexpr int FL_FQ  = FL_FC + 2 * 64 * 32 * 32;
constexpr int FL_FP  = FL_FQ + 2 * 64 * 32 * 32;
constexpr int FL_FD  = FL_FP + 64 * 32 * 32;
constexpr int FL_FZ  = FL_FD + 64 * 2 * 32;
constexpr int FL_TMO = FL_FZ + 16 * 32;
constexpr int FL_END = FL_TMO + 64;
constexpr size_t Z_BYTES   = OFF_FL + (size_t)FL_END * 4 - OFF_Z;
constexpr size_t WS_TOTAL  = OFF_Z + Z_BYTES;
static_assert(Z_BYTES % 16 == 0 && OFF_Z % 256 == 0 && Z_BYTES <= 4 * 64 * 8192 && OFF_G + 2048 <= OFF_Z, "zero region");
static_assert(WS_TOTAL + (4 << 20) <= (size_t)256 * 1024 * 1024, "workspace too large");

static_assert(WS_TOTAL - OFF_XA <= (size_t)384 * 8 * 65536, "translation warm-up");
constexpr int CKN = 2336;
static_assert((size_t)DM * 2 * CKN * 2 <= (size_t)DM * L_SEQ * 4 + SZ_X, "K buffer");
__device__ __forceinline__ float pos_of_index(int p) {
  if (p < 256) return (float)p;
  if (p < 384) return (float)(256 + 2 * (p - 256));
  if (p < 512) return (float)(512 + 4 * (p - 384));
  if (p < 640) return (float)(1024 + 8 * (p - 512));
  return 2048.0f;
}
__device__ __forceinline__ int index_of_l(int l) {
  float pf = bfr((float)l);
  int p = (int)pf;
  if (p < 256) return p;
  if (p < 512) return 256 + ((p - 256) >> 1);
  if (p < 1024) return 384 + ((p - 512) >> 2);
  if (p < 2048) return 512 + ((p - 1024) >> 3);
  return 640;
}

template <int CTRL> __device__ __forceinline__ float dppr(float x) {
  return __builtin_bit_cast(float, __builtin_amdgcn_update_dpp(0, __builtin_bit_cast(int, x), CTRL, 0xf, 0xf, false));
}
__device__ __forceinline__ float wave_sum(float v) {
  v += dppr<0x121>(v);
  v += dppr<0x122>(v);
  v += dppr<0x124>(v);
  v += dppr<0x128>(v);
  {
    auto r = __builtin_amdgcn_permlane16_swap(__float_as_uint(v), __float_as_uint(v), false, false);
    v = __uint_as_float(r[0]) + __uint_as_float(r[1]);
  }
  {
    auto r = __builtin_amdgcn_permlane32_swap(__float_as_uint(v), __float_as_uint(v), false, false);
    v = __uint_as_float(r[0]) + __uint_as_float(r[1]);
  }
  return v;
}
__device__ __forceinline__ void ln_row8(float (&v)[8]) {
  float s = 0.f;
#pragma unroll
  for (int j = 0; j < 8; ++j) s += v[j];
  s = wave_sum(s);
  const float mean = s * (1.0f / DM);
  float q = 0.f;
#pragma unroll
  for (int j = 0; j < 8; ++j) { float t = v[j] - mean; q += t * t; }
  q = wave_sum(q);
  const float var = q * (1.0f / DM);
  const float mb = bfr(mean);
  const float sd = bfr(sqrtf(bfr(bfr(var) + 1e-5f)));
#pragma unroll
  for (int j = 0; j < 8; ++j) v[j] = bfr(bfr(v[j] - mb) / sd);
}

#define RLX_AGENT __ATOMIC_RELAXED, __HIP_MEMORY_SCOPE_AGENT
typedef __attribute__((address_space(1))) unsigned gu32;
typedef unsigned v4u __attribute__((ext_vector_type(4)));
constexpr int AUX_SC1 = 16;
constexpr unsigned SPIN_CAP = 1u << 18;

__device__ __forceinline__ bool wait_flags(const unsigned* flags, int lane, int nfl, unsigned want, unsigned* tmo) {
  for (unsigned spins = 0;; ++spins) {
    unsigned v = want;
    if (lane < nfl) v = __hip_atomic_load((gu32*)flags + lane * 32, RLX_AGENT);
    if (__all(v >= want)) break;
    if (spins > SPIN_CAP) {
      if (lane == 0) __hip_atomic_store((gu32*)tmo, 1u, RLX_AGENT);
      return false;
    }
  }
  __builtin_amdgcn_fence(__ATOMIC_ACQUIRE, "wavefront");
  return true;
}

#define STAMP(i)
#define STAMP_DUMP()
#define WSTAMP(i)
template <int CTRL> __device__ __forceinline__ float dppf(float old, float x) {
  return __builtin_bit_cast(float, __builtin_amdgcn_update_dpp(__builtin_bit_cast(int, old), __builtin_bit_cast(int, x), CTRL, 0xf, 0xf, false));
}
__device__ __forceinline__ float wave_max_dpp(float x) {
  const float ninf = -3e38f;
  x = fmaxf(x, dppf<0x111>(ninf, x));
  x = fmaxf(x, dppf<0x112>(ninf, x));
  x = fmaxf(x, dppf<0x114>(ninf, x));
  x = fmaxf(x, dppf<0x118>(ninf, x));
  x = fmaxf(x, dppf<0x142>(ninf, x));
  x = fmaxf(x, dppf<0x143>(ninf, x));
  return __builtin_bit_cast(float, __builtin_amdgcn_readlane(__builtin_bit_cast(int, x), 63));
}
__device__ __forceinline__ float wave_sum_dpp(float x) {
  x += dppf<0x111>(0.f, x);
  x += dppf<0x112>(0.f, x);
  x += dppf<0x114>(0.f, x);
  x += dppf<0x118>(0.f, x);
  x += dppf<0x142>(0.f, x);
  x += dppf<0x143>(0.f, x);
  return __builtin_bit_cast(float, __builtin_amdgcn_readlane(__builtin_bit_cast(int, x), 63));
}
constexpr int SMS = 528;
constexpr int QS = 144;
constexpr int VS = 80;
constexpr int SOS = 144;
#define VAR_NREP 4
constexpr int NREP = VAR_NREP;
__device__ __forceinline__ float xsum16(float x) {
  auto r = __builtin_amdgcn_permlane16_swap(__float_as_uint(x), __float_as_uint(x), false, false);
  return __uint_as_float(r[0]) + __uint_as_float(r[1]);
}
__device__ __forceinline__ float xsum32(float x) {
  auto r = __builtin_amdgcn_permlane32_swap(__float_as_uint(x), __float_as_uint(x), false, false);
  return __uint_as_float(r[0]) + __uint_as_float(r[1]);
}
__device__ __forceinline__ float xmax16(float x) {
  auto r = __builtin_amdgcn_permlane16_swap(__float_as_uint(x), __float_as_uint(x), false, false);
  return fmaxf(__uint_as_float(r[0]), __uint_as_float(r[1]));
}
__device__ __forceinline__ float xmax32(float x) {
  auto r = __builtin_amdgcn_permlane32_swap(__float_as_uint(x), __float_as_uint(x), false, false);
  return fmaxf(__uint_as_float(r[0]), __uint_as_float(r[1]));
}
__device__ __forceinline__ float red_mod4(float x) {
  x += dppf<0x124>(0.f, x);
  x += dppf<0x128>(0.f, x);
  return xsum32(xsum16(x));
}
typedef __bf16 bf16x2_t __attribute__((ext_vector_type(2)));
typedef float f32x2_t __attribute__((ext_vector_type(2)));
__device__ __forceinline__ unsigned pk2(float a, float b) {
  f32x2_t v = {a, b};
  bf16x2_t r = __builtin_convertvector(v, bf16x2_t);
  return __builtin_bit_cast(unsigned, r);
}
typedef _Float16 f16x2_t __attribute__((ext_vector_type(2)));
typedef _Float16 f16x8 __attribute__((ext_vector_type(8)));
__device__ __forceinline__ unsigned pkh(float a, float b) {
  f32x2_t v = {a, b};
  return __builtin_bit_cast(unsigned, __builtin_convertvector(v, f16x2_t));
}
__device__ __forceinline__ float h2f(u16 h) { return (float)__builtin_bit_cast(_Float16, h); }
__device__ __forceinline__ float one(float x) {
  asm("" : "+v"(x));
  return x;
}
__device__ __forceinline__ unsigned pk2s(float a, float b) { return pk2(one(a), one(b)); }
__device__ __forceinline__ float dot2sq(const unsigned x, const float acc) {
  return __builtin_amdgcn_fdot2_f32_bf16(__builtin_bit_cast(bf16x2_t, x), __builtin_bit_cast(bf16x2_t, x), acc, false);
}
__device__ __forceinline__ float lo16(unsigned u) { return __uint_as_float(u << 16); }
__device__ __forceinline__ float hi16(unsigned u) { return __uint_as_float(u & 0xffff0000u); }
__device__ __forceinline__ float bfr1(float x) { return lo16(pk2(x, x)); }
__device__ __forceinline__ float half_sum(float v) {
  v += dppr<0x121>(v);
  v += dppr<0x122>(v);
  v += dppr<0x124>(v);
  v += dppr<0x128>(v);
  return xsum16(v);
}
__device__ __forceinline__ void scale_pair(float a, float b, float scale, float& oa, float& ob) {
  unsigned u = pk2(a, b);
  unsigned v = pk2s(lo16(u) * scale, hi16(u) * scale);
  oa = lo16(v);
  ob = hi16(v);
}

#define XMAP 2
__device__ __forceinline__ int vbid() {
  const int b = blockIdx.x, x = b & 7, t = b >> 3;
  if (XMAP == 2) {
    const int q = (t >> 2) + 2 * (x & 4);
    const unsigned long long m = (q & 8) ? 0xf0d85040cdc9ada9ull : 0x76726e6a25210501ull;
    const int e = (int)(m >> (8 * (q & 7))) & 255;
    return (e & 252) + ((x & 3) << (2 + (e & 3))) + (t & 3) + (q == 14 ? ((x & 2) ? 8 : -120) : 0);
  }
  if (XMAP == 0) {
    if (b < 64) return (b & 32) + 4 * x + (t & 3);
    if (b < 96) return (b & ~15) + 2 * x + (t & 1);
    if (b >= 104 && b < 168) return 104 + 8 * x + t - 13;
    if (b >= 168 && b < 232) return 168 + ((b - 168) & 32) + 4 * x + ((t - 21) & 3);
    return b;
  }
  if (XMAP == 3) {
    const int y = x & 3;
    if (t >= 16 && t < 24) return 104 + 16 * y + 2 * (x & 4) + t - 16;
    if (x < 4) return t < 16 ? 4 * (t & 8) + 8 * x + (t & 7) : 64 + 4 * (t & 4) + 4 * y + (t & 3);
    if (t >= 24) return (t >= 28 ? 240 : y < 2 ? 96 : 224) + 4 * y + (t & 3);
    return 168 + 4 * (t & 8) + 8 * y + (t & 7);
  }
  if (XMAP == 4) {
    const int y = x & 3;
    if (x < 4) return t < 16 ? 4 * (t & 8) + 8 * x + (t & 7) : t < 28 ? 104 + 16 * y + t - 16 : 64 + 4 * y + (t & 3);
    if (t >= 24) return (t >= 28 ? 240 : y < 2 ? 96 : 224) + 4 * y + (t & 3);
    if (t < 16) return 168 + 4 * (t & 8) + 8 * y + (t & 7);
    return t < 20 ? 104 + 16 * y + t - 4 : 80 + 4 * y + (t & 3);
  }
  if (XMAP == 2 || XMAP >= 5) {
    if (XMAP == 5 && x < 4 && t < 16) return 16 * x + t;
    if (XMAP == 6 && x < 4 && t < 16) return 32 * (x & 1) + 8 * (x & 2) + t;
    if (XMAP == 7 && x < 4) return (x & 1) ? 104 + 16 * (x & 2) + t : 16 * x + t;
    if (x < 4) return t < 16 ? 4 * (t & 8) + 8 * x + (t & 7) : 104 + 16 * x + t - 16;
    const int y = x - 4;
    if (t >= 24) return (t >= 28 ? 240 : y < 2 ? 96 : 224) + 4 * y + (t & 3);
    if (t < 16) return 168 + 4 * (t & 8) + 8 * y + (t & 7);
    return 64 + 4 * (t & 4) + 4 * y + (t & 3);
  }
  if (x < 2) return 2 * (t & 16) + 16 * x + (t & 15);
  const int i = 16 * (x - 2) + (t & 15);
  if (t < 16) {
    const int k = (i * 171) >> 9, r = i - 3 * k;
    return r == 0 ? 168 + k : r == 1 ? 200 + k : 64 + 16 * (k & 1) + (k >> 1);
  }
  const int g = i >> 3, e = i & 7;
  if (g == 3) return 96 + e;
  if (g == 7) return 232 + e;
  if (g == 9) return 240 + e;
  if (g == 11) return 248 + e;
  return 104 + 8 * (g - (g > 3) - (g > 7) - (g > 9)) + e;
}
#define MSTAMP()
#define WSTAMP2()
#define SST(k)
#define SSX(k, c)
#define SSW(k, v)
#define TS(k, ss, t)
#define LS(k, ss)
#define LS_DUMP()
struct MegaArgs {
  const u16* mem0;
  const u16* Dskip;
  char* ws;
  u16* out;
  const u16* x0;
  const u16 *logAre, *Aim, *Cre, *Cim, *logdt;
  const u16 *Wq, *Wk, *Wv, *Wo;
  int vb, wv;
};

__device__ __forceinline__ int lane_id() {
  int l;
  asm volatile("v_mbcnt_lo_u32_b32 %0, -1, 0\n\tv_mbcnt_hi_u32_b32 %0, -1, %0" : "=v"(l));
  __builtin_assume(l >= 0 && l < 64);
  return l;
}
__device__ __forceinline__ bool spin_timeout(unsigned spins, unsigned* tmo) {
  if (spins > SPIN_CAP) {
    if (lane_id() == 0) __hip_atomic_store((gu32*)tmo, 1u, RLX_AGENT);
    return true;
  }
  return false;
}
#define ROT_H (4 * (h & 1))
#define ROT_T (h & 2)
#define C2G 420
#define CVSP 40
#define PFA 0
#define PFB 0
#define WO1 1
#define AWH 0
#define PEEK0 1
#define SLP0 2
#define GF_R 400
#define GF_V 0
#define PP (9 - R0)
#define EL 1
#define IPF 3
#define XL 0
#define MC3 2
#define XZD 300
#define XZP 4
#define NFW 1
#define IPS 128u
#define WNT 17
#define OAUX 16
#define XAUX 16
#define GF_RT 0
#define GF_RU 0
#define EF 1
#define RGB 3
#define GR2 345
#define GR3 280
#define RLS 1
#define GT1 120
#define GT2 32
#define GT3 1
#define CE 190
#define KE 0
#define PE KE
#define KD 360
#define PD (KD + 20)
#define PC0 2
#define GR0 6
#define DL_A2 DL_A
#define PCT 0
#define EP 1
#define CQ 1
#define K0 40
#define PW 1
#define R0 3
#define PSW 1
#define RSK 0
#define KC 1
#define AWD 2200
#define P2D 0
#define TS1 3
#define TL1 20
#define TL2 30
#define TL3 50
#define KCT 20
#define KR 1
#define K1 50
#define CVP 2
#define PCD 100
#define RAUX AUX_SC1
#define RVAL 1
#define RLEAD 60
#define SLP_D 0
#define RFA 1
#define TP 1
#define TPS (4 * TP + 4)
#define MCE 1
#define H4S 0
#define GF_H 150
#define CVT2 48
#define GF_P 0
#define GF0 8
#define SLP_H 12
#define HX0 6
#define CVSTEP 2
#define CVLATE 64
#define PACE 204
#define PACE0 12
#define RPRIO 3
#define DL_R2 1400
#define PNL 8
#define GSL 12
#define AWDIR 1
#define AWAT 0
#define AWRP 0
#define PEPS 4
#define FGATE 0
#define DL_C 0
#define DL_A 300
#define DL_R 0
#define DL_Q 0
#define KC0 0
#define CPRIO 1
#define P1PRIO 3
#define QLEAD 2
#define POLL_SLEEP 12
#define SLP_C POLL_SLEEP
#define SLP_A POLL_SLEEP
#define SLP_W POLL_SLEEP
#define SLP_R POLL_SLEEP
#define SLP_V POLL_SLEEP
#define SLP_Q POLL_SLEEP
#define SLP_M 4
#define AWPRIO 2
#define SLP_S 8
#define SPOS 4
#define CVPRE 12
#define QPD 3
#define QPRI 2
#define TLBW 3
#define QHV 0
#define QCHK 0
#define LEADN 99
#define GATE_V 170
#define GATE_R 380
#define GATE_N 380
#define AWAHEAD 1
#define GATE_M 150
#define PRI_A 3
#define PRI_WA 2
#define PRI_WB 1
#define AWSKIP 3
#define PRI_0 3
#define AWDEF 0
#define SLP_F 2
#define EXP_OFF 0
#define AWSPEC 1
#define CVTHR 48
#define NR_C 8
#define NR_R 8
#define NR_W 8
#define NR_N 2
#define QAHEAD 0
#define NS0 4
#define SLP_N POLL_SLEEP
template <int SLP, int WPL>
__device__ __forceinline__ bool wait_n(const unsigned* f, int n, unsigned* tmo) {
  const int l = lane_id();
  for (unsigned spins = 0;; ++spins) {
    unsigned v = 1u;
    if (l < n) v = __hip_atomic_load((gu32*)f + (l / WPL) * 32 + l % WPL, RLX_AGENT);
    if (__all(v != 0u)) break;
    if (spin_timeout(spins, tmo)) return false;
    __builtin_amdgcn_s_sleep(SLP);
  }
  asm volatile("" ::: "memory");
  return true;
}
__device__ __forceinline__ unsigned peek(const unsigned* base, const unsigned off) {
  unsigned r;
  asm volatile("global_atomic_or %0, %1, %2, %3 sc0\n\ts_waitcnt vmcnt(0)" : "=&v"(r) : "v"(off), "v"(0u), "s"(base) : "memory");
  return r;
}
__device__ __forceinline__ unsigned now10() { return (unsigned)__builtin_amdgcn_s_memrealtime(); }
__device__ __forceinline__ void gate(const unsigned t) {
#pragma unroll 1
  for (int i = 0; i < 64; ++i) {
    const int d = (int)(t - now10());
    if (d <= 0) break;
    if (d > GT1) __builtin_amdgcn_s_sleep(32);
    else if (d > GT2) __builtin_amdgcn_s_sleep(8);
    else __builtin_amdgcn_s_sleep(GT3);
  }
}
__device__ __forceinline__ unsigned latest(const unsigned mk, const unsigned t) {
  int d = (int)(mk - t);
  d = max(d, __builtin_amdgcn_update_dpp(0, d, 0x121, 0xf, 0xf, false));
  d = max(d, __builtin_amdgcn_update_dpp(0, d, 0x122, 0xf, 0xf, false));
  d = max(d, __builtin_amdgcn_update_dpp(0, d, 0x124, 0xf, 0xf, false));
  d = max(d, __builtin_amdgcn_update_dpp(0, d, 0x128, 0xf, 0xf, false));
  return t + __builtin_amdgcn_readfirstlane(d);
}
template <int SLP, int WPL, int GT>
__device__ __forceinline__ void grp_wait(const bool leader, volatile unsigned* sw, const unsigned val, const unsigned* f, int n,
                                         unsigned* tmo, unsigned& tl) {
  if (leader) {
    const int l = lane_id();
    for (unsigned spins = 0;; ++spins) {
      unsigned v = 1u;
      if (l < n) v = __hip_atomic_load((gu32*)f + (l / WPL) * 32 + l % WPL, RLX_AGENT);
      if (__all(v != 0u) || spin_timeout(spins, tmo)) break;
      if (GT && spins == 0) gate(tl);
      else __builtin_amdgcn_s_sleep(SLP);
    }
    if (GT) tl = now10() + GT;
    if (sw && l == 0) *sw = val;
  } else {
    for (unsigned spins = 0; *sw < val; ++spins) {
      if (spins > 64u * SPIN_CAP) break;
      if (FGATE && GT && spins == 0) gate(tl);
      __builtin_amdgcn_s_sleep(SLP_F);
    }
    if (FGATE && GT) tl = now10() + GT;
  }
  asm volatile("" ::: "memory");
}
template <int SLP, int WPL>
__device__ __forceinline__ bool wg_wait(const unsigned* f, int n, unsigned* tmo) {
  if (threadIdx.x < 64) (void)wait_n<SLP, WPL>(f, n, tmo);
  __syncthreads();
  asm volatile("" ::: "memory");
  return true;
}
__device__ __forceinline__ void set_flag(unsigned* f) { __hip_atomic_store((gu32*)f, 1u, RLX_AGENT); }
#define DRAIN() asm volatile("s_waitcnt vmcnt(0)" ::: "memory")
#define MKRS(ptr, bytes) __builtin_amdgcn_make_buffer_rsrc((void*)(ptr), 0, (int)(bytes), 0x00020000)
typedef unsigned v2u __attribute__((ext_vector_type(2)));
#define LD128(rs, voff, soff) __builtin_amdgcn_raw_buffer_load_b128(rs, voff, soff, AUX_SC1)
#define LD64(rs, voff, soff) __builtin_amdgcn_raw_buffer_load_b64(rs, voff, soff, AUX_SC1)
#define ST128(v, rs, voff, soff) ST128A(v, rs, voff, soff, AUX_SC1)
#define ST128A(v, rs, voff, soff, aux)                                        \
  do {                                                                        \
    const v4u st_d_ = (v);                                                    \
    __builtin_amdgcn_raw_buffer_store_b128(st_d_, rs, voff, soff, aux);       \
    asm volatile("s_nop 1" ::"v"(st_d_) : "memory");                          \
  } while (0)
#define ST64(v, rs, voff, soff) __builtin_amdgcn_raw_buffer_store_b64(v, rs, voff, soff, AUX_SC1)
#define BF8(x) __builtin_bit_cast(bf16x8, x)
#define MFMA16(a, b, c) __builtin_amdgcn_mfma_f32_16x16x32_bf16(a, b, c, 0, 0, 0)
typedef short bf16x4 __attribute__((ext_vector_type(4)));
#define MFMA16K(a, b, c) \
  __builtin_amdgcn_mfma_f32_16x16x16bf16_1k(__builtin_bit_cast(bf16x4, a), __builtin_bit_cast(bf16x4, b), c, 0, 0, 0)
__device__ __forceinline__ float fmax_nn(float a, float b) { return __builtin_elementwise_maximum(a, b); }
typedef __bf16 bfv8 __attribute__((ext_vector_type(8)));
typedef __bf16 bfv16 __attribute__((ext_vector_type(16)));
typedef unsigned v8u __attribute__((ext_vector_type(8)));
#define SMF(a, b, c, idx) \
  __builtin_amdgcn_smfmac_f32_16x16x64_bf16(__builtin_bit_cast(bfv8, a), __builtin_bit_cast(bfv16, b), c, idx, 0, 0)
__device__ __forceinline__ float swap_add32(float a, float b) {
  auto r = __builtin_amdgcn_permlane32_swap(__float_as_uint(a), __float_as_uint(b), false, false);
  return __uint_as_float(r[0]) + __uint_as_float(r[1]);
}
__device__ __forceinline__ float swap_add16(float a, float b) {
  auto r = __builtin_amdgcn_permlane16_swap(__float_as_uint(a), __float_as_uint(b), false, false);
  return __uint_as_float(r[0]) + __uint_as_float(r[1]);
}
__device__ __forceinline__ v4u smf_fix(const v4u d) {
  auto x = __builtin_amdgcn_permlane16_swap(d.x, d.z, false, false);
  auto y = __builtin_amdgcn_permlane16_swap(d.y, d.w, false, false);
  return v4u{x[0], x[1], y[0], y[1]};
}
__device__ __forceinline__ v8u smf_bfrag(const u16* p) {
  const v2u a = *reinterpret_cast<const v2u*>(p), b = *reinterpret_cast<const v2u*>(p + 8);
  const v2u c = *reinterpret_cast<const v2u*>(p + 32), d = *reinterpret_cast<const v2u*>(p + 40);
  return v8u{a.x, b.x, a.y, b.y, c.x, d.x, c.y, d.y};
}

#define VAR_PPA 0
#define VAR_PPB 0
#define VAR_THR 0
#define VAR_THR2 0
#define CV_LEAD 100
#define VAR_EARLY 1
#define VAR_DRAIN 1
#define VAR_D1 0
#define VAR_D2 0
#define VAR_DR2 1
#define VAR_S1 0
#define VAR_S2 0
typedef __attribute__((address_space(1))) unsigned long long gu64;
typedef __attribute__((address_space(1))) char gch;
__device__ __forceinline__ int cvt_rpi(float x) {
  int r;
  asm("v_cvt_rpi_i32_f32 %0, %1" : "=v"(r) : "v"(x));
  return r;
}
typedef unsigned long long u64;
__device__ __forceinline__ u64 call_tag(const MegaArgs& A) {
  u64 t = *(const __attribute__((address_space(4))) u64*)(A.ws + OFF_EP) * 6364136223846793005ull + 1442695040888963407ull;
  asm volatile("" : "+s"(t)::"memory");
  return t;
}
__device__ __forceinline__ u64 tagged(const u64 tag, const unsigned v) { return (tag & 0xffffffffffffull) | (u64)(v >> 16) << 48; }
__device__ __forceinline__ bool has_tag(const u64 tag, const unsigned lo, const unsigned hi) {
  return lo == (unsigned)tag && (hi & 0xffffu) == ((unsigned)(tag >> 32) & 0xffffu);
}
__device__ __forceinline__ u64 ready_look(const MegaArgs& A) {
  return __hip_atomic_load((gu64*)(A.ws + OFF_RD) + (blockIdx.x & 7) * 64 + lane_id(), RLX_AGENT);
}
#define RD_ASK(v) \
  asm volatile("global_load_dwordx2 %0, %1, %2 sc1" : "=v"(v) : "v"(lane_id() * 8), "s"(A.ws + OFF_RD + (blockIdx.x & 7) * 512) : "memory")
__device__ __forceinline__ void ready_wait(const MegaArgs& A, const u64 tag, u64 v) {
  for (unsigned spins = 0; !__all(v == tag) && spins < SPIN_CAP; ++spins) {
    __builtin_amdgcn_s_sleep(8);
    v = ready_look(A);
  }
  asm volatile("" ::: "memory");
}
typedef short v4s __attribute__((ext_vector_type(4)));
typedef __attribute__((address_space(3))) v4s* lp4s;
__device__ __forceinline__ void glds16(const void* sbase, const unsigned voff, const unsigned lds, const bool nt = false) {
  unsigned keep;
#define GLDS16(M)                                                                                                                     \
  asm volatile("s_nop 4\n\ts_mov_b32 %0, m0\n\ts_mov_b32 m0, %3\n\ts_nop 0\n\tglobal_load_lds_dwordx4 %1, %2" M "\n\ts_mov_b32 m0, %0" \
               : "=&s"(keep)                                                                                                          \
               : "v"(voff), "s"(sbase), "s"(lds)                                                                                      \
               : "memory")
  if (nt) GLDS16(" nt");
  else GLDS16("");
#undef GLDS16
}
__device__ __forceinline__ void glds16c(const void* sbase, const unsigned voff, const unsigned lds) {
  unsigned keep;
  asm volatile("s_nop 4\n\ts_mov_b32 %0, m0\n\ts_mov_b32 m0, %3\n\ts_nop 0\n\tglobal_load_lds_dwordx4 %1, %2 sc1\n\ts_mov_b32 m0, %0"
               : "=&s"(keep)
               : "v"(voff), "s"(sbase), "s"(lds)
               : "memory");
}
__device__ __forceinline__ void glds64c(const void* sbase, const unsigned voff, const unsigned lds) {
  unsigned keep;
  asm volatile("s_nop 4\n\ts_mov_b32 %0, m0\n\ts_mov_b32 m0, %3\n\ts_nop 0\n\tglobal_load_lds_dwordx4 %1, %2 sc1\n\t"
               "global_load_lds_dwordx4 %1, %2 offset:1024 sc1\n\tglobal_load_lds_dwordx4 %1, %2 offset:2048 sc1\n\t"
               "global_load_lds_dwordx4 %1, %2 offset:3072 sc1\n\ts_mov_b32 m0, %0"
               : "=&s"(keep)
               : "v"(voff), "s"(sbase), "s"(lds)
               : "memory");
}
__device__ __forceinline__ v2u trd(const unsigned a) {
  return __builtin_bit_cast(v2u, __builtin_amdgcn_ds_read_tr16_b64_v4i16((lp4s)(unsigned long)a));
}
__device__ __forceinline__ unsigned lds_base(char* smem) { return (unsigned)(unsigned long)(__attribute__((address_space(3))) char*)smem; }
#define RAW_BAR()                                           \
  do {                                                      \
    asm volatile("s_waitcnt lgkmcnt(0)" ::: "memory");      \
    __builtin_amdgcn_s_barrier();                           \
    asm volatile("" ::: "memory");                          \
  } while (0)
#define WAIT_VM(n) asm volatile("s_waitcnt vmcnt(" #n ")" ::: "memory")
template <bool SP>
__device__ __forceinline__ void wqkv_issue(const u16* Wm, const int hf, const int h, const int w, const int l, const unsigned sb, const bool nt) {
  const char* base = (const char*)Wm + (size_t)(256 * hf + 32 * w) * (HD * 2) + h * (DH * 2);
#define WQKV(NT_)                                                                                                       \
  _Pragma("unroll") for (int i = 0; i < 8; ++i) {                                                                      \
    const int r = l >> 4;                                                                                              \
    const int f = SP ? ((r & 1) | ((i & 1) << 1) | (((i >> 1) & 1) << 2)) : (r | (((i >> 1) & 1) << 2));               \
    glds16(base, (unsigned)((4 * i + r) * (HD * 2) + (((l & 15) ^ (f << 1)) << 4)),                                    \
           sb + (w >> 2) * 65536 + hf * 32768 + (w & 3) * 8192 + i * 1024, NT_);                                       \
  }
  if (nt) { WQKV(true) } else { WQKV(false) }
#undef WQKV
}

__device__ __forceinline__ unsigned row_stats(const v4u raw) {
  float v[8];
#pragma unroll
  for (int e = 0; e < 4; ++e) { v[2 * e] = lo16(raw[e]); v[2 * e + 1] = hi16(raw[e]); }
  float s = 0.f;
#pragma unroll
  for (int e = 0; e < 8; ++e) s += v[e];
  const float mean = wave_sum(s) * (1.0f / DM);
  float q = 0.f;
#pragma unroll
  for (int e = 0; e < 8; ++e) { const float t = v[e] - mean; q += t * t; }
  const float var = wave_sum(q) * (1.0f / DM);
  const float sd = bfr(sqrtf(bfr(bfr(var) + 1e-5f)));
  return (unsigned)f2bf(mean) | ((unsigned)f2bf(sd) << 16);
}
__device__ __forceinline__ void ask_slow(const MegaArgs& A, const int k, unsigned (&t)[3]) {
  const int l = lane_id(), x = blockIdx.x & 7;
  const char *wq = (const char*)A.Wq, *wv = (const char*)A.Wv;
  asm("" : "+s"(wq), "+s"(wv));
#pragma unroll
  for (int m = 0; m < 3; ++m) {
    const char* W = m == 1 ? (const char*)A.Wk : ((m == 0) == (x < 4)) ? wv : wq;
    const int a = (int)((unsigned long)W >> 8) + 8 * (x & 3), h = k < 32 ? ((1 - a) & 3) + 4 * (k & 1) : (31 - a) & 31;
    const int r = k < 32 ? k >> 1 : k - 32;
    t[m] = __builtin_amdgcn_raw_buffer_load_b32(MKRS(W, (unsigned)k < 48u && h < 8 ? DM * HD * 2 : 0), (l >> 1) * (HD * 2) + (l & 1) * 128,
                                                (32 * (r & 15)) * (HD * 2) + (8 * (x & 3) + (h & 7)) * 256, 0);
  }
}
#define ASKED(t) asm volatile("" ::"v"(t[0]), "v"(t[1]), "v"(t[2]))
template <int N>
__device__ __forceinline__ u64 start_stats(const MegaArgs& A, const int idx) {
  const int w = A.wv, l = lane_id();
  const int r0 = N * (8 * idx + w);
  unsigned ta[3], tb[3];
  v4u raw[N];
#pragma unroll
  for (int i = 0; i < N; ++i) raw[i] = *reinterpret_cast<const v4u*>(A.x0 + (size_t)(r0 + i) * DM + l * 8);
  asm volatile("" ::: "memory");
  ask_slow(A, 16 * w + (idx & 3) + (idx >> 4) * N, ta);
  if (N > 4) ask_slow(A, 16 * w + (idx & 3) + 12, tb);
  unsigned tc[2] = {};
  if (N == 4 && AWH > 0) tc[0] = *reinterpret_cast<const unsigned*>(A.Wo + (size_t)idx * 65536 + 32768 + (64 * w + l) * 64);
  if (N == 4 && AWH > 1 && w >= 4) tc[1] = *reinterpret_cast<const unsigned*>(A.Wo + (size_t)idx * 65536 + (64 * w + l) * 64);
  const u64 tag = call_tag(A);
  unsigned stw = 0u;
#pragma unroll
  for (int i = 0; i < N; ++i) {
    const unsigned t = row_stats(raw[i]);
    if ((l >> 1) == i) stw = t;
  }
  if (l < 2 * N) __hip_atomic_store((gu64*)(A.ws + OFF_GT) + 2 * r0 + l, tagged(tag, (l & 1) ? stw : stw << 16), RLX_AGENT);
  SST(0);
  ASKED(ta);
  if (N > 4) ASKED(tb);
  asm volatile("" ::"v"(tc[0]), "v"(tc[1]));
  return tag;
}
#define MFMAH(a, b, c) __builtin_amdgcn_mfma_f32_16x16x32_f16(a, b, c, 0, 0, 0)
struct Cx {
  f32x2_t r, i;
};
__device__ __forceinline__ Cx cmul(const Cx a, const Cx b) { return Cx{a.r * b.r - a.i * b.i, a.r * b.i + a.i * b.r}; }
struct KGen {
  unsigned pa, pb, pc, pd;
  u16 pe;
  f32x2_t ar, ai;
  Cx C;
  char* st;
  v4u bh[8], bl[8];
  __device__ __forceinline__ void load(const MegaArgs& A, const int d, const int l) {
    pa = *reinterpret_cast<const unsigned*>(A.logAre + d * NST + 2 * l);
    pb = *reinterpret_cast<const unsigned*>(A.Aim + d * NST + 2 * l);
    pc = *reinterpret_cast<const unsigned*>(A.Cre + d * NST + 2 * l);
    pd = *reinterpret_cast<const unsigned*>(A.Cim + d * NST + 2 * l);
    pe = A.logdt[d];
  }
  __device__ __forceinline__ void setup() {
    const float dt = bfr(expf(bf2f(pe)));
    ar = f32x2_t{-bfr(expf(lo16(pa))) * dt, -bfr(expf(hi16(pa))) * dt};
    ai = f32x2_t{lo16(pb) * dt, hi16(pb) * dt};
    C = Cx{f32x2_t{lo16(pc) * dt, hi16(pc) * dt}, f32x2_t{lo16(pd) * dt, hi16(pd) * dt}};
  }
  __device__ __forceinline__ float scale() {
    const float b = 2.0f * wave_sum((fabsf(C.r.x) + fabsf(C.r.y)) + (fabsf(C.i.x) + fabsf(C.i.y)));
    const unsigned eb = max(__float_as_uint(b) >> 23, 25u);
    const float ks = __uint_as_float((268u - eb) << 23);
    C.r *= ks;
    C.i *= ks;
    return __uint_as_float((eb - 24u) << 23);
  }
  __device__ __forceinline__ Cx power(const float p) const {
    Cx z;
#pragma unroll
    for (int e = 0; e < 2; ++e) {
      const float x = ai[e] * p;
      const float f = __builtin_fmaf(x, 6.4206382e-9f, __builtin_fmaf(x, 0.15915494f, -__builtin_rintf(x * 0.15915494f)));
      const float m = __expf(ar[e] * p);
      z.r[e] = m * __builtin_amdgcn_cosf(f);
      z.i[e] = m * __builtin_amdgcn_sinf(f);
    }
    return z;
  }
  static __device__ __forceinline__ unsigned rest(const f32x2_t y, const unsigned h) {
    unsigned r;
    asm("v_fma_mixlo_f16 %0, %1, -1.0, %2 op_sel_hi:[1,0,0]" : "=v"(r) : "v"(h), "v"(y.x));
    asm("v_fma_mixhi_f16 %0, %1, -1.0, %2 op_sel:[1,0,0] op_sel_hi:[1,0,0]" : "+v"(r) : "v"(h), "v"(y.y));
    return r;
  }
  __device__ __forceinline__ char* rows(char* p, Cx& y, const Cx s, const int n) const {
#pragma unroll 1
    for (int i = 0; i < n; i += 2) {
#pragma unroll
      for (int k = 0; k < 2; ++k) {
        const unsigned h0 = pkh(y.r.x, y.r.y), h1 = pkh(y.i.x, y.i.y);
        *reinterpret_cast<v2u*>(p + 1040 * k) = v2u{h0, h1};
        *reinterpret_cast<v2u*>(p + 1040 * k + 512) = v2u{rest(y.r, h0), rest(y.i, h1)};
        y = cmul(y, s);
      }
      p += 2080;
    }
    return p;
  }
  __device__ __forceinline__ void get(v4u (&h)[8], v4u (&o)[8], const int l) const {
    const char* p = st + (l & 15) * 1040 + (l >> 4) * 16;
    asm volatile("" ::: "memory");
#pragma unroll
    for (int k = 0; k < 8; ++k) {
      h[k] = *reinterpret_cast<const v4u*>(p + 64 * k);
      o[k] = *reinterpret_cast<const v4u*>(p + 512 + 64 * k);
    }
    asm volatile("" ::: "memory");
  }
  Cx cy, cs;
  __device__ __forceinline__ void columns(const int l, const int j0, const int j1) {
    if (j0 == 0) {
      cs = power(16.f);
      cs.i = -cs.i;
      cy = Cx{f32x2_t{16384.f, 16384.f}, f32x2_t{0.f, 0.f}};
    }
    rows(st + l * 8 + 1040 * j0, cy, cs, j1 - j0);
  }
  template <bool COL>
  __device__ __forceinline__ f32x4 tile(const int l) const {
    v4u h[8], o[8];
    get(h, o, l);
    f32x4 d = {}, e = {}, f = {};
#define H8(x) __builtin_bit_cast(f16x8, x)
#define KMM(a, b, d) (COL ? MFMAH(H8(b), H8(a), d) : MFMAH(H8(a), H8(b), d))
#pragma unroll
    for (int k = 0; k < 8; ++k) {
      d = KMM(h[k], bh[k], d);
      e = KMM(o[k], bh[k], e);
      f = KMM(h[k], bl[k], f);
    }
#undef KMM
#undef H8
    return (d + (e + f)) * (1.0f / 8192);
  }
};
#define ACC_INIT 0xc9b26c86c9b26ca0ull
#define GEN_TL 2
#define GEN_TR 4
#define CHAIN_ASM_ATT \
  "s_mov_b32 s95, 272\n" \
  "s_mov_b32 s91, 0x4c7c2000\n" \
  "s_mov_b32 s85, 0xc9b26c9b\n" \
  "s_mov_b32 s92, 0xc9326c9b\n" \
  "s_mov_b32 s93, 0xc9326c9b\n" \
  "s_mov_b32 s78, 0x3e0293ee\n" \
  "s_mov_b32 s76, 0x33333333\n" \
  "s_mov_b32 s77, 0x33333333\n" \
  "s_lshl_b32 s98, s73, 10\n" \
  "s_add_u32 s98, s98, s74\n" \
  "s_add_u32 s98, s98, 131072\n" \
  "s_lshl_b32 s97, s73, 9\n" \
  "s_add_u32 s97, s97, s74\n" \
  "s_add_u32 s97, s97, 158720\n" \
  "s_lshl_b32 s99, s73, 10\n" \
  "v_mbcnt_lo_u32_b32 v48, -1, 0\n" \
  "v_mbcnt_hi_u32_b32 v48, -1, v48\n" \
  "s_add_u32 s88, s74, 157760\n" \
  "v_mov_b32 v59, s88\n" \
  "v_lshrrev_b32 v49, 2, v48\n" \
  "v_lshrrev_b32 v50, 4, v48\n" \
  "v_bitop3_b32 v50, v49, v50, 3 bitop3:0x28\n" \
  "v_lshl_add_u32 v8, v50, 3, s97\n" \
  "v_and_b32 v50, 3, v48\n" \
  "v_lshl_add_u32 v8, v50, 5, v8\n" \
  "v_and_b32 v49, 1, v49\n" \
  "v_lshl_add_u32 v8, v49, 8, v8\n" \
  "v_and_b32 v49, 8, v48\n" \
  "v_lshl_add_u32 v8, v49, 4, v8\n" \
  "v_lshrrev_b32 v49, 5, v48\n" \
  "v_add_u32 v10, s99, v1\n" \
  "s_lshr_b32 s88, s99, 2\n" \
  "s_add_u32 s88, s88, s74\n" \
  "s_add_u32 s88, s88, 139264\n" \
  "v_lshl_add_u32 v7, v49, 10, s88\n" \
  "v_and_b32 v49, 31, v48\n" \
  "v_lshl_add_u32 v7, v49, 3, v7\n" \
  "v_mov_b64 v[12:13], 0\n" \
  "v_mov_b64 v[14:15], 0\n" \
  "v_mov_b64 v[16:17], 0\n" \
  "v_mov_b64 v[18:19], 0\n" \
  "v_mov_b64 v[20:21], 0\n" \
  "v_mov_b64 v[22:23], 0\n" \
  "v_mov_b32 v34, 0xc9b26ca0\n" \
  "v_mov_b32 v35, 0xc9b26c86\n" \
  "s_mov_b32 s87, 0x40000\n" \
  "v_subrev_u32 v30, 64, v59\n" \
  "v_add_u32 v17, 0x10000, v28\n" \
  "v_xor_b32 v18, 128, v28\n" \
  "v_xor_b32 v19, 128, v17\n" \
  "ds_write_b32 v30, v12 offset:0\n" \
  "ds_write_b32 v30, v12 offset:32\n" \
  "ds_write_b32 v30, v12 offset:64\n" \
  "v_xor_b32 v20, 0, v29\n" \
  "v_xor_b32 v21, 32, v29\n" \
  "v_xor_b32 v22, 64, v29\n" \
  "v_xor_b32 v23, 96, v29\n" \
  "s_waitcnt vmcnt(12) lgkmcnt(0)\n" \
  "s_barrier\n" \
  "ds_read_b64_tr_b16 v[160:161], v20 offset:0\n" \
  "ds_read_b64_tr_b16 v[162:163], v20 offset:2048\n" \
  "ds_read_b64_tr_b16 v[168:169], v21 offset:0\n" \
  "ds_read_b64_tr_b16 v[170:171], v21 offset:2048\n" \
  "ds_read_b64_tr_b16 v[176:177], v22 offset:0\n" \
  "ds_read_b64_tr_b16 v[178:179], v22 offset:2048\n" \
  "ds_read_b64_tr_b16 v[184:185], v23 offset:0\n" \
  "ds_read_b64_tr_b16 v[186:187], v23 offset:2048\n" \
  "s_lshl_b32 s88, s73, 10\n" \
  "v_add_u32 v48, s88, v1\n" \
  "ds_read_b128 v[52:55], v48 offset:0\n" \
  "s_waitcnt lgkmcnt(0)\n" \
  "v_mov_b32 v11, 0\n" \
  "v_dot2c_f32_bf16 v11, v52, v52\n" \
  "v_dot2c_f32_bf16 v11, v53, v53\n" \
  "v_dot2c_f32_bf16 v11, v54, v54\n" \
  "v_dot2c_f32_bf16 v11, v55, v55\n" \
  "s_nop 0\n" \
  "s_nop 1\n" \
  "v_max_u32_dpp v11, v11, v11 row_ror:1 row_mask:0xf bank_mask:0xf\n" \
  "s_nop 1\n" \
  "v_max_u32_dpp v11, v11, v11 row_ror:2 row_mask:0xf bank_mask:0xf\n" \
  "s_nop 1\n" \
  "v_max_u32_dpp v11, v11, v11 row_ror:4 row_mask:0xf bank_mask:0xf\n" \
  "s_nop 1\n" \
  "v_max_u32_dpp v11, v11, v11 row_ror:8 row_mask:0xf bank_mask:0xf\n" \
  "v_mov_b32 v31, v11\n" \
  "s_nop 1\n" \
  "v_permlane16_swap_b32 v11, v31\n" \
  "v_max_u32 v11, v11, v31\n" \
  "v_mov_b32 v31, v11\n" \
  "s_nop 1\n" \
  "v_permlane32_swap_b32 v11, v31\n" \
  "v_max_u32 v11, v11, v31\n" \
  "s_mov_b64 exec, 1\n" \
  "ds_max_u32 v59, v11\n" \
  "s_mov_b64 exec, -1\n" \
  "s_waitcnt vmcnt(8) lgkmcnt(0)\n" \
  "s_barrier\n" \
  "ds_read_b64_tr_b16 v[164:165], v20 offset:4096\n" \
  "ds_read_b64_tr_b16 v[166:167], v20 offset:6144\n" \
  "ds_read_b64_tr_b16 v[172:173], v21 offset:4096\n" \
  "ds_read_b64_tr_b16 v[174:175], v21 offset:6144\n" \
  "ds_read_b64_tr_b16 v[180:181], v22 offset:4096\n" \
  "ds_read_b64_tr_b16 v[182:183], v22 offset:6144\n" \
  "ds_read_b64_tr_b16 v[188:189], v23 offset:4096\n" \
  "ds_read_b64_tr_b16 v[190:191], v23 offset:6144\n" \
  "s_add_u32 m0, s19, 0\n" \
  "s_nop 0\n" \
  "global_load_lds_dwordx4 v24, s[4:5] offset:0\n" \
  "global_load_lds_dwordx4 v25, s[4:5] offset:1024\n" \
  "global_load_lds_dwordx4 v26, s[4:5] offset:2048\n" \
  "global_load_lds_dwordx4 v27, s[4:5] offset:3072\n" \
  "s_add_u32 s4, s4, 0x100000\n" \
  "s_addc_u32 s5, s5, 0\n" \
  "v_mfma_f32_16x16x32_bf16 v[64:67], v[160:163], v[160:163], 0\n" \
  "v_mfma_f32_16x16x32_bf16 v[68:71], v[168:171], v[168:171], 0\n" \
  "v_mfma_f32_16x16x32_bf16 v[72:75], v[176:179], v[176:179], 0\n" \
  "v_mfma_f32_16x16x32_bf16 v[76:79], v[184:187], v[184:187], 0\n" \
  "s_waitcnt vmcnt(8) lgkmcnt(0)\n" \
  "s_barrier\n" \
  "ds_read_b64_tr_b16 v[32:33], v20 offset:8192\n" \
  "ds_read_b64_tr_b16 v[34:35], v20 offset:10240\n" \
  "ds_read_b64_tr_b16 v[40:41], v21 offset:8192\n" \
  "ds_read_b64_tr_b16 v[42:43], v21 offset:10240\n" \
  "ds_read_b64_tr_b16 v[48:49], v22 offset:8192\n" \
  "ds_read_b64_tr_b16 v[50:51], v22 offset:10240\n" \
  "ds_read_b64_tr_b16 v[56:57], v23 offset:8192\n" \
  "ds_read_b64_tr_b16 v[58:59], v23 offset:10240\n" \
  "s_add_u32 m0, s19, 4096\n" \
  "s_nop 0\n" \
  "global_load_lds_dwordx4 v24, s[4:5] offset:0\n" \
  "global_load_lds_dwordx4 v25, s[4:5] offset:1024\n" \
  "global_load_lds_dwordx4 v26, s[4:5] offset:2048\n" \
  "global_load_lds_dwordx4 v27, s[4:5] offset:3072\n" \
  "s_add_u32 s4, s4, 0x100000\n" \
  "s_addc_u32 s5, s5, 0\n" \
  "v_mfma_f32_16x16x32_bf16 v[64:67], v[164:167], v[164:167], v[64:67]\n" \
  "v_mfma_f32_16x16x32_bf16 v[68:71], v[172:175], v[172:175], v[68:71]\n" \
  "v_mfma_f32_16x16x32_bf16 v[72:75], v[180:183], v[180:183], v[72:75]\n" \
  "v_mfma_f32_16x16x32_bf16 v[76:79], v[188:191], v[188:191], v[76:79]\n" \
  "s_waitcnt vmcnt(8) lgkmcnt(0)\n" \
  "s_barrier\n" \
  "ds_read_b64_tr_b16 v[36:37], v20 offset:12288\n" \
  "ds_read_b64_tr_b16 v[38:39], v20 offset:14336\n" \
  "ds_read_b64_tr_b16 v[44:45], v21 offset:12288\n" \
  "ds_read_b64_tr_b16 v[46:47], v21 offset:14336\n" \
  "ds_read_b64_tr_b16 v[52:53], v22 offset:12288\n" \
  "ds_read_b64_tr_b16 v[54:55], v22 offset:14336\n" \
  "ds_read_b64_tr_b16 v[60:61], v23 offset:12288\n" \
  "ds_read_b64_tr_b16 v[62:63], v23 offset:14336\n" \
  "s_add_u32 m0, s19, 8192\n" \
  "s_nop 0\n" \
  "global_load_lds_dwordx4 v24, s[4:5] offset:0\n" \
  "global_load_lds_dwordx4 v25, s[4:5] offset:1024\n" \
  "global_load_lds_dwordx4 v26, s[4:5] offset:2048\n" \
  "global_load_lds_dwordx4 v27, s[4:5] offset:3072\n" \
  "s_add_u32 s4, s4, 0x100000\n" \
  "s_addc_u32 s5, s5, 0\n" \
  "v_mfma_f32_16x16x32_bf16 v[64:67], v[32:35], v[32:35], v[64:67]\n" \
  "v_mfma_f32_16x16x32_bf16 v[68:71], v[40:43], v[40:43], v[68:71]\n" \
  "v_mfma_f32_16x16x32_bf16 v[72:75], v[48:51], v[48:51], v[72:75]\n" \
  "v_mfma_f32_16x16x32_bf16 v[76:79], v[56:59], v[56:59], v[76:79]\n" \
  "s_waitcnt vmcnt(8) lgkmcnt(0)\n" \
  "s_barrier\n" \
  "s_add_u32 m0, s19, 12288\n" \
  "s_nop 0\n" \
  "global_load_lds_dwordx4 v24, s[4:5] offset:0\n" \
  "global_load_lds_dwordx4 v25, s[4:5] offset:1024\n" \
  "global_load_lds_dwordx4 v26, s[4:5] offset:2048\n" \
  "global_load_lds_dwordx4 v27, s[4:5] offset:3072\n" \
  "s_add_u32 s4, s4, 0x100000\n" \
  "s_addc_u32 s5, s5, 0\n" \
  "v_mfma_f32_16x16x32_bf16 v[64:67], v[36:39], v[36:39], v[64:67]\n" \
  "v_mfma_f32_16x16x32_bf16 v[68:71], v[44:47], v[44:47], v[68:71]\n" \
  "v_mfma_f32_16x16x32_bf16 v[72:75], v[52:55], v[52:55], v[72:75]\n" \
  "v_mfma_f32_16x16x32_bf16 v[76:79], v[60:63], v[60:63], v[76:79]\n" \
  "s_nop 7\n" \
  "v_max3_f32 v13, v64, v65, v66\n" \
  "v_max3_f32 v13, v13, v67, v68\n" \
  "v_max3_f32 v13, v13, v69, v70\n" \
  "v_max3_f32 v13, v13, v71, v72\n" \
  "v_max3_f32 v13, v13, v73, v74\n" \
  "v_max3_f32 v13, v13, v75, v76\n" \
  "v_max3_f32 v13, v13, v77, v78\n" \
  "v_max_f32 v13, v13, v79\n" \
  "s_nop 1\n" \
  "v_max_f32_dpp v13, v13, v13 row_ror:1 row_mask:0xf bank_mask:0xf\n" \
  "s_nop 1\n" \
  "v_max_f32_dpp v13, v13, v13 row_ror:2 row_mask:0xf bank_mask:0xf\n" \
  "s_nop 1\n" \
  "v_max_f32_dpp v13, v13, v13 row_ror:4 row_mask:0xf bank_mask:0xf\n" \
  "s_nop 1\n" \
  "v_max_f32_dpp v13, v13, v13 row_ror:8 row_mask:0xf bank_mask:0xf\n" \
  "v_mov_b32 v31, v13\n" \
  "s_nop 1\n" \
  "v_permlane16_swap_b32 v13, v31\n" \
  "v_max_f32 v13, v13, v31\n" \
  "v_mov_b32 v31, v13\n" \
  "s_nop 1\n" \
  "v_permlane32_swap_b32 v13, v31\n" \
  "v_max_f32 v13, v13, v31\n" \
  "s_mov_b64 exec, 1\n" \
  "ds_max_u32 v30, v13 offset:32\n" \
  "s_mov_b64 exec, -1\n" \
  "s_waitcnt vmcnt(8) lgkmcnt(0)\n" \
  "s_barrier\n" \
  "s_add_u32 m0, s19, 0\n" \
  "s_nop 0\n" \
  "global_load_lds_dwordx4 v24, s[6:7] offset:0\n" \
  "global_load_lds_dwordx4 v25, s[6:7] offset:1024\n" \
  "global_load_lds_dwordx4 v26, s[6:7] offset:2048\n" \
  "global_load_lds_dwordx4 v27, s[6:7] offset:3072\n" \
  "s_add_u32 s6, s6, 0x100000\n" \
  "s_addc_u32 s7, s7, 0\n" \
  "s_waitcnt vmcnt(8) lgkmcnt(0)\n" \
  "s_barrier\n" \
  "s_add_u32 m0, s19, 4096\n" \
  "s_nop 0\n" \
  "global_load_lds_dwordx4 v24, s[6:7] offset:0\n" \
  "global_load_lds_dwordx4 v25, s[6:7] offset:1024\n" \
  "global_load_lds_dwordx4 v26, s[6:7] offset:2048\n" \
  "global_load_lds_dwordx4 v27, s[6:7] offset:3072\n" \
  "s_add_u32 s6, s6, 0x100000\n" \
  "s_addc_u32 s7, s7, 0\n" \
  "s_waitcnt vmcnt(8) lgkmcnt(0)\n" \
  "s_barrier\n" \
  "s_add_u32 m0, s19, 8192\n" \
  "s_nop 0\n" \
  "global_load_lds_dwordx4 v24, s[6:7] offset:0\n" \
  "global_load_lds_dwordx4 v25, s[6:7] offset:1024\n" \
  "global_load_lds_dwordx4 v26, s[6:7] offset:2048\n" \
  "global_load_lds_dwordx4 v27, s[6:7] offset:3072\n" \
  "s_add_u32 s6, s6, 0x100000\n" \
  "s_addc_u32 s7, s7, 0\n" \
  "s_waitcnt vmcnt(8) lgkmcnt(0)\n" \
  "s_barrier\n" \
  "ds_read_b64_tr_b16 v[64:65], v28 offset:0\n" \
  "ds_read_b64_tr_b16 v[66:67], v28 offset:512\n" \
  "ds_read_b64_tr_b16 v[68:69], v28 offset:32768\n" \
  "ds_read_b64_tr_b16 v[70:71], v28 offset:33280\n" \
  "ds_read_b64_tr_b16 v[72:73], v17 offset:0\n" \
  "ds_read_b64_tr_b16 v[74:75], v17 offset:512\n" \
  "ds_read_b64_tr_b16 v[76:77], v17 offset:32768\n" \
  "ds_read_b64_tr_b16 v[78:79], v17 offset:33280\n" \
  "ds_read_b64_tr_b16 v[192:193], v18 offset:0\n" \
  "ds_read_b64_tr_b16 v[194:195], v18 offset:512\n" \
  "ds_read_b64_tr_b16 v[196:197], v18 offset:32768\n" \
  "ds_read_b64_tr_b16 v[198:199], v18 offset:33280\n" \
  "ds_read_b64_tr_b16 v[200:201], v19 offset:0\n" \
  "ds_read_b64_tr_b16 v[202:203], v19 offset:512\n" \
  "ds_read_b64_tr_b16 v[204:205], v19 offset:32768\n" \
  "ds_read_b64_tr_b16 v[206:207], v19 offset:33280\n" \
  "s_add_u32 m0, s19, 12288\n" \
  "s_nop 0\n" \
  "global_load_lds_dwordx4 v24, s[6:7] offset:0\n" \
  "global_load_lds_dwordx4 v25, s[6:7] offset:1024\n" \
  "global_load_lds_dwordx4 v26, s[6:7] offset:2048\n" \
  "global_load_lds_dwordx4 v27, s[6:7] offset:3072\n" \
  "s_add_u32 s6, s6, 0x100000\n" \
  "s_addc_u32 s7, s7, 0\n" \
  "s_waitcnt vmcnt(8) lgkmcnt(0)\n" \
  "s_barrier\n" \
  "ds_read_b64_tr_b16 v[80:81], v28 offset:4096\n" \
  "ds_read_b64_tr_b16 v[82:83], v28 offset:4608\n" \
  "ds_read_b64_tr_b16 v[84:85], v28 offset:36864\n" \
  "ds_read_b64_tr_b16 v[86:87], v28 offset:37376\n" \
  "ds_read_b64_tr_b16 v[88:89], v17 offset:4096\n" \
  "ds_read_b64_tr_b16 v[90:91], v17 offset:4608\n" \
  "ds_read_b64_tr_b16 v[92:93], v17 offset:36864\n" \
  "ds_read_b64_tr_b16 v[94:95], v17 offset:37376\n" \
  "ds_read_b64_tr_b16 v[208:209], v18 offset:4096\n" \
  "ds_read_b64_tr_b16 v[210:211], v18 offset:4608\n" \
  "ds_read_b64_tr_b16 v[212:213], v18 offset:36864\n" \
  "ds_read_b64_tr_b16 v[214:215], v18 offset:37376\n" \
  "ds_read_b64_tr_b16 v[216:217], v19 offset:4096\n" \
  "ds_read_b64_tr_b16 v[218:219], v19 offset:4608\n" \
  "ds_read_b64_tr_b16 v[220:221], v19 offset:36864\n" \
  "ds_read_b64_tr_b16 v[222:223], v19 offset:37376\n" \
  "s_add_u32 m0, s19, 0\n" \
  "s_nop 0\n" \
  "global_load_lds_dwordx4 v24, s[8:9] offset:0\n" \
  "global_load_lds_dwordx4 v25, s[8:9] offset:1024\n" \
  "global_load_lds_dwordx4 v26, s[8:9] offset:2048\n" \
  "global_load_lds_dwordx4 v27, s[8:9] offset:3072\n" \
  "s_add_u32 s8, s8, 0x100000\n" \
  "s_addc_u32 s9, s9, 0\n" \
  "s_waitcnt vmcnt(8) lgkmcnt(0)\n" \
  "s_barrier\n" \
  "ds_read_b64_tr_b16 v[96:97], v28 offset:8192\n" \
  "ds_read_b64_tr_b16 v[98:99], v28 offset:8704\n" \
  "ds_read_b64_tr_b16 v[100:101], v28 offset:40960\n" \
  "ds_read_b64_tr_b16 v[102:103], v28 offset:41472\n" \
  "ds_read_b64_tr_b16 v[104:105], v17 offset:8192\n" \
  "ds_read_b64_tr_b16 v[106:107], v17 offset:8704\n" \
  "ds_read_b64_tr_b16 v[108:109], v17 offset:40960\n" \
  "ds_read_b64_tr_b16 v[110:111], v17 offset:41472\n" \
  "ds_read_b64_tr_b16 v[224:225], v18 offset:8192\n" \
  "ds_read_b64_tr_b16 v[226:227], v18 offset:8704\n" \
  "ds_read_b64_tr_b16 v[228:229], v18 offset:40960\n" \
  "ds_read_b64_tr_b16 v[230:231], v18 offset:41472\n" \
  "ds_read_b64_tr_b16 v[232:233], v19 offset:8192\n" \
  "ds_read_b64_tr_b16 v[234:235], v19 offset:8704\n" \
  "ds_read_b64_tr_b16 v[236:237], v19 offset:40960\n" \
  "ds_read_b64_tr_b16 v[238:239], v19 offset:41472\n" \
  "s_add_u32 m0, s19, 4096\n" \
  "s_nop 0\n" \
  "global_load_lds_dwordx4 v24, s[8:9] offset:0\n" \
  "global_load_lds_dwordx4 v25, s[8:9] offset:1024\n" \
  "global_load_lds_dwordx4 v26, s[8:9] offset:2048\n" \
  "global_load_lds_dwordx4 v27, s[8:9] offset:3072\n" \
  "s_add_u32 s8, s8, 0x100000\n" \
  "s_addc_u32 s9, s9, 0\n" \
  "s_waitcnt vmcnt(8) lgkmcnt(0)\n" \
  "s_barrier\n" \
  "ds_read_b64_tr_b16 v[112:113], v28 offset:12288\n" \
  "ds_read_b64_tr_b16 v[114:115], v28 offset:12800\n" \
  "ds_read_b64_tr_b16 v[116:117], v28 offset:45056\n" \
  "ds_read_b64_tr_b16 v[118:119], v28 offset:45568\n" \
  "ds_read_b64_tr_b16 v[120:121], v17 offset:12288\n" \
  "ds_read_b64_tr_b16 v[122:123], v17 offset:12800\n" \
  "ds_read_b64_tr_b16 v[124:125], v17 offset:45056\n" \
  "ds_read_b64_tr_b16 v[126:127], v17 offset:45568\n" \
  "ds_read_b64_tr_b16 v[240:241], v18 offset:12288\n" \
  "ds_read_b64_tr_b16 v[242:243], v18 offset:12800\n" \
  "ds_read_b64_tr_b16 v[244:245], v18 offset:45056\n" \
  "ds_read_b64_tr_b16 v[246:247], v18 offset:45568\n" \
  "ds_read_b64_tr_b16 v[248:249], v19 offset:12288\n" \
  "ds_read_b64_tr_b16 v[250:251], v19 offset:12800\n" \
  "ds_read_b64_tr_b16 v[252:253], v19 offset:45056\n" \
  "ds_read_b64_tr_b16 v[254:255], v19 offset:45568\n" \
  "s_add_u32 m0, s19, 8192\n" \
  "s_nop 0\n" \
  "global_load_lds_dwordx4 v24, s[8:9] offset:0\n" \
  "global_load_lds_dwordx4 v25, s[8:9] offset:1024\n" \
  "global_load_lds_dwordx4 v26, s[8:9] offset:2048\n" \
  "global_load_lds_dwordx4 v27, s[8:9] offset:3072\n" \
  "s_add_u32 s8, s8, 0x100000\n" \
  "s_addc_u32 s9, s9, 0\n" \
  "s_waitcnt vmcnt(8) lgkmcnt(0)\n" \
  "s_barrier\n" \
  "ds_read_b64_tr_b16 v[128:129], v28 offset:0\n" \
  "ds_read_b64_tr_b16 v[130:131], v28 offset:512\n" \
  "ds_read_b64_tr_b16 v[132:133], v28 offset:32768\n" \
  "ds_read_b64_tr_b16 v[134:135], v28 offset:33280\n" \
  "ds_read_b64_tr_b16 v[136:137], v17 offset:0\n" \
  "ds_read_b64_tr_b16 v[138:139], v17 offset:512\n" \
  "ds_read_b64_tr_b16 v[140:141], v17 offset:32768\n" \
  "ds_read_b64_tr_b16 v[142:143], v17 offset:33280\n" \
  "s_add_u32 m0, s19, 12288\n" \
  "s_nop 0\n" \
  "global_load_lds_dwordx4 v24, s[8:9] offset:0\n" \
  "global_load_lds_dwordx4 v25, s[8:9] offset:1024\n" \
  "global_load_lds_dwordx4 v26, s[8:9] offset:2048\n" \
  "global_load_lds_dwordx4 v27, s[8:9] offset:3072\n" \
  "s_add_u32 s8, s8, 0x100000\n" \
  "s_addc_u32 s9, s9, 0\n" \
  "s_waitcnt vmcnt(8) lgkmcnt(0)\n" \
  "s_barrier\n" \
  "ds_write_b128 v6, v[160:163] offset:0\n" \
  "ds_write_b128 v6, v[164:167] offset:1024\n" \
  "ds_write_b128 v6, v[168:171] offset:2048\n" \
  "ds_write_b128 v6, v[172:175] offset:3072\n" \
  "ds_read_b64_tr_b16 v[144:145], v28 offset:4096\n" \
  "ds_read_b64_tr_b16 v[146:147], v28 offset:4608\n" \
  "ds_read_b64_tr_b16 v[148:149], v28 offset:36864\n" \
  "ds_read_b64_tr_b16 v[150:151], v28 offset:37376\n" \
  "ds_read_b64_tr_b16 v[152:153], v17 offset:4096\n" \
  "ds_read_b64_tr_b16 v[154:155], v17 offset:4608\n" \
  "ds_read_b64_tr_b16 v[156:157], v17 offset:36864\n" \
  "ds_read_b64_tr_b16 v[158:159], v17 offset:37376\n" \
  "s_waitcnt vmcnt(4) lgkmcnt(0)\n" \
  "s_barrier\n" \
  "ds_write_b128 v6, v[176:179] offset:4096\n" \
  "ds_write_b128 v6, v[180:183] offset:5120\n" \
  "ds_write_b128 v6, v[184:187] offset:6144\n" \
  "ds_write_b128 v6, v[188:191] offset:7168\n" \
  "ds_read_b64_tr_b16 v[160:161], v28 offset:8192\n" \
  "ds_read_b64_tr_b16 v[162:163], v28 offset:8704\n" \
  "ds_read_b64_tr_b16 v[164:165], v28 offset:40960\n" \
  "ds_read_b64_tr_b16 v[166:167], v28 offset:41472\n" \
  "ds_read_b64_tr_b16 v[168:169], v17 offset:8192\n" \
  "ds_read_b64_tr_b16 v[170:171], v17 offset:8704\n" \
  "ds_read_b64_tr_b16 v[172:173], v17 offset:40960\n" \
  "ds_read_b64_tr_b16 v[174:175], v17 offset:41472\n" \
  "s_waitcnt vmcnt(0) lgkmcnt(0)\n" \
  "s_barrier\n" \
  "ds_write_b128 v6, v[32:35] offset:8192\n" \
  "ds_write_b128 v6, v[36:39] offset:9216\n" \
  "ds_write_b128 v6, v[40:43] offset:10240\n" \
  "ds_write_b128 v6, v[44:47] offset:11264\n" \
  "ds_read_b64_tr_b16 v[176:177], v28 offset:12288\n" \
  "ds_read_b64_tr_b16 v[178:179], v28 offset:12800\n" \
  "ds_read_b64_tr_b16 v[180:181], v28 offset:45056\n" \
  "ds_read_b64_tr_b16 v[182:183], v28 offset:45568\n" \
  "ds_read_b64_tr_b16 v[184:185], v17 offset:12288\n" \
  "ds_read_b64_tr_b16 v[186:187], v17 offset:12800\n" \
  "ds_read_b64_tr_b16 v[188:189], v17 offset:45056\n" \
  "ds_read_b64_tr_b16 v[190:191], v17 offset:45568\n" \
  "ds_read_b32 v31, v30 offset:0\n" \
  "s_waitcnt lgkmcnt(0)\n" \
  "s_barrier\n" \
  "ds_write_b128 v6, v[48:51] offset:12288\n" \
  "ds_write_b128 v6, v[52:55] offset:13312\n" \
  "ds_write_b128 v6, v[56:59] offset:14336\n" \
  "ds_write_b128 v6, v[60:63] offset:15360\n" \
  "v_mul_f32 v31, 0x3f8e353f, v31\n" \
  "v_mul_f32 v30, 0x44870000, v31\n" \
  "v_mov_b64 v[12:13], 0\n" \
  "v_mov_b64 v[14:15], 0\n" \
  "v_mov_b64 v[16:17], 0\n" \
  "v_mov_b64 v[18:19], 0\n" \
  "v_mov_b64 v[20:21], 0\n" \
  "v_mov_b64 v[22:23], 0\n" \
  "v_readfirstlane_b32 s72, v31\n" \
  "v_readfirstlane_b32 s86, v30\n" \
  "s_lshr_b32 s86, s86, 23\n" \
  "s_addk_i32 s86, 0xff83\n" \
  "s_ashr_i32 s86, s86, 1\n" \
  "s_max_i32 s86, s86, -20\n" \
  "s_min_i32 s86, s86, 100\n" \
  "s_mov_b32 s82, s86\n" \
  "s_lshr_b32 s83, s72, 23\n" \
  "s_sub_i32 s83, s83, 243\n" \
  "s_waitcnt lgkmcnt(0)\n" \
  "s_add_u32 s88, s74, 157760\n" \
  "v_mov_b32 v59, s88\n" \
  "v_mov_b32 v34, 0xc9b26ca0\n" \
  "v_mov_b32 v35, 0xc9b26c86\n" \
  ".p2align 6\n" \
  "s_nop 0\n" \
  "s_nop 0\n" \
  "s_nop 0\n" \
  ".LCA_TOP_%=:\n" \
  "s_waitcnt lgkmcnt(0)\n" \
  "s_barrier\n" \
  "ds_read_b128 v[40:43], v1 offset:0\n" \
  "ds_read_b128 v[44:47], v1 offset:1024\n" \
  "ds_read_b128 v[48:51], v1 offset:2048\n" \
  "ds_read_b128 v[52:55], v1 offset:3072\n" \
  "ds_read_b128 v[56:59], v1 offset:4096\n" \
  "ds_read_b128 v[60:63], v1 offset:5120\n" \
  "s_cmp_lg_u32 s73, 0\n" \
  "s_cbranch_scc1 .LCA_NOZ_%=\n" \
  "s_xor_b32 s88, s60, 0x10000\n" \
  "s_add_u32 s88, s88, s61\n" \
  "buffer_store_dwordx2 v[34:35], v5, s[40:43], s88 offen sc1\n" \
  ".LCA_NOZ_%=:\n" \
  "s_waitcnt lgkmcnt(5)\n" \
  "v_smfmac_f32_16x16x64_bf16 v[20:23], v[40:43], v[192:199], v0\n" \
  "v_smfmac_f32_16x16x64_bf16 v[12:15], v[40:43], v[64:71], v0\n" \
  "v_smfmac_f32_16x16x64_bf16 v[16:19], v[40:43], v[128:135], v0\n" \
  "ds_read_b128 v[40:43], v1 offset:6144\n" \
  "s_waitcnt lgkmcnt(5)\n" \
  "v_smfmac_f32_16x16x64_bf16 v[20:23], v[44:47], v[200:207], v0\n" \
  "v_smfmac_f32_16x16x64_bf16 v[12:15], v[44:47], v[72:79], v0\n" \
  "v_smfmac_f32_16x16x64_bf16 v[16:19], v[44:47], v[136:143], v0\n" \
  "ds_read_b128 v[44:47], v1 offset:7168\n" \
  "s_waitcnt lgkmcnt(5)\n" \
  "v_smfmac_f32_16x16x64_bf16 v[20:23], v[48:51], v[208:215], v0\n" \
  "v_smfmac_f32_16x16x64_bf16 v[12:15], v[48:51], v[80:87], v0\n" \
  "v_smfmac_f32_16x16x64_bf16 v[16:19], v[48:51], v[144:151], v0\n" \
  "ds_read_b128 v[24:27], v1 offset:10240\n" \
  "ds_read_b128 v[28:31], v1 offset:14336\n" \
  "s_waitcnt lgkmcnt(6)\n" \
  "v_smfmac_f32_16x16x64_bf16 v[20:23], v[52:55], v[216:223], v0\n" \
  "v_smfmac_f32_16x16x64_bf16 v[12:15], v[52:55], v[88:95], v0\n" \
  "v_smfmac_f32_16x16x64_bf16 v[16:19], v[52:55], v[152:159], v0\n" \
  "ds_read_b128 v[32:35], v1 offset:11264\n" \
  "ds_read_b128 v[36:39], v1 offset:15360\n" \
  "s_waitcnt lgkmcnt(7)\n" \
  "v_smfmac_f32_16x16x64_bf16 v[20:23], v[56:59], v[224:231], v0\n" \
  "v_smfmac_f32_16x16x64_bf16 v[12:15], v[56:59], v[96:103], v0\n" \
  "v_smfmac_f32_16x16x64_bf16 v[16:19], v[56:59], v[160:167], v0\n" \
  "ds_read_b128 v[56:59], v1 offset:12288\n" \
  "s_waitcnt lgkmcnt(7)\n" \
  "v_smfmac_f32_16x16x64_bf16 v[20:23], v[60:63], v[232:239], v0\n" \
  "v_smfmac_f32_16x16x64_bf16 v[12:15], v[60:63], v[104:111], v0\n" \
  "v_smfmac_f32_16x16x64_bf16 v[16:19], v[60:63], v[168:175], v0\n" \
  "ds_read_b128 v[60:63], v1 offset:16384\n" \
  "s_waitcnt lgkmcnt(7)\n" \
  "v_smfmac_f32_16x16x64_bf16 v[20:23], v[40:43], v[240:247], v0\n" \
  "v_smfmac_f32_16x16x64_bf16 v[12:15], v[40:43], v[112:119], v0\n" \
  "v_smfmac_f32_16x16x64_bf16 v[16:19], v[40:43], v[176:183], v0\n" \
  "s_waitcnt lgkmcnt(6)\n" \
  "v_smfmac_f32_16x16x64_bf16 v[20:23], v[44:47], v[248:255], v0\n" \
  "v_smfmac_f32_16x16x64_bf16 v[12:15], v[44:47], v[120:127], v0\n" \
  "v_smfmac_f32_16x16x64_bf16 v[16:19], v[44:47], v[184:191], v0\n" \
  "s_cmp_eq_u32 s71, 0\n" \
  "v_permlane32_swap_b32 v20, v22\n" \
  "v_permlane32_swap_b32 v21, v23\n" \
  "v_pk_add_f32 v[20:21], v[20:21], v[22:23]\n" \
  "s_mov_b64 vcc, s[76:77]\n" \
  "v_permlane32_swap_b32 v12, v14\n" \
  "v_permlane32_swap_b32 v13, v15\n" \
  "v_cvt_pk_bf16_f32 v20, v20, v21\n" \
  "v_pk_add_f32 v[12:13], v[12:13], v[14:15]\n" \
  "v_permlane32_swap_b32 v16, v18\n" \
  "v_permlane32_swap_b32 v17, v19\n" \
  "ds_write_b32 v8, v20 offset:2048\n" \
  "v_cvt_pk_bf16_f32 v12, v12, v13\n" \
  "v_pk_add_f32 v[16:17], v[16:17], v[18:19]\n" \
  "s_nop 0\n" \
  "v_cvt_pk_bf16_f32 v13, v16, v17\n" \
  "ds_write_b64 v8, v[12:13]\n" \
  "s_setprio 1\n" \
  "s_waitcnt lgkmcnt(0)\n" \
  "s_barrier\n" \
  "s_cbranch_scc1 .LCA_LAST_%=\n" \
  "ds_read_b64_tr_b16 v[40:41], v2 offset:0\n" \
  "ds_read_b64_tr_b16 v[42:43], v3 offset:0\n" \
  "ds_read_b64_tr_b16 v[44:45], v2 offset:1024\n" \
  "ds_read_b64_tr_b16 v[46:47], v3 offset:1024\n" \
  "ds_read_b64_tr_b16 v[48:49], v2 offset:2048\n" \
  "ds_read_b64_tr_b16 v[50:51], v3 offset:2048\n" \
  "ds_read_b64_tr_b16 v[52:53], v2 offset:3072\n" \
  "ds_read_b64_tr_b16 v[54:55], v3 offset:3072\n" \
  "ds_read_b32 v11, v1 offset:26688\n" \
  "s_waitcnt lgkmcnt(7)\n" \
  "v_mfma_f32_16x16x32_bf16 v[12:15], v[24:27], v[40:43], 0\n" \
  "v_mfma_f32_16x16x32_bf16 v[16:19], v[28:31], v[40:43], 0\n" \
  "ds_read_b128 v[24:27], v1 offset:13312\n" \
  "ds_read_b128 v[28:31], v1 offset:17408\n" \
  "v_mfma_f32_16x16x32_bf16 v[20:23], v[40:43], v[40:43], 0\n" \
  "s_waitcnt lgkmcnt(7)\n" \
  "v_mfma_f32_16x16x32_bf16 v[12:15], v[32:35], v[44:47], v[12:15]\n" \
  "v_mfma_f32_16x16x32_bf16 v[16:19], v[36:39], v[44:47], v[16:19]\n" \
  "v_mfma_f32_16x16x32_bf16 v[20:23], v[44:47], v[44:47], v[20:23]\n" \
  "s_waitcnt lgkmcnt(2)\n" \
  "v_mfma_f32_16x16x32_bf16 v[12:15], v[56:59], v[48:51], v[12:15]\n" \
  "v_readfirstlane_b32 s86, v11\n" \
  "v_mfma_f32_16x16x32_bf16 v[16:19], v[60:63], v[48:51], v[16:19]\n" \
  "ds_read_b128 v[32:35], v6 offset:8192\n" \
  "ds_read_b128 v[36:39], v6 offset:9216\n" \
  "v_mfma_f32_16x16x32_bf16 v[20:23], v[48:51], v[48:51], v[20:23]\n" \
  "s_lshr_b32 s86, s86, 23\n" \
  "s_add_i32 s86, s86, s83\n" \
  "s_ashr_i32 s86, s86, 1\n" \
  "ds_read_b128 v[48:51], v10 offset:18432\n" \
  "v_mfma_f32_16x16x32_bf16 v[20:23], v[52:55], v[52:55], v[20:23]\n" \
  "s_max_i32 s86, s86, s82\n" \
  "s_min_i32 s86, s86, 100\n" \
  "s_lshl_b32 s86, s86, 23\n" \
  "s_sub_i32 s84, 0xc8800000, s86\n" \
  "s_waitcnt lgkmcnt(3)\n" \
  "v_mfma_f32_16x16x32_bf16 v[12:15], v[24:27], v[52:55], v[12:15]\n" \
  "v_mfma_f32_16x16x32_bf16 v[16:19], v[28:31], v[52:55], v[16:19]\n" \
  "ds_read_b128 v[52:55], v10 offset:22528\n" \
  "ds_read_b128 v[56:59], v6 offset:0\n" \
  "ds_read_b128 v[60:63], v6 offset:1024\n" \
  "ds_read_b128 v[24:27], v6 offset:2048\n" \
  "ds_read_b128 v[28:31], v6 offset:3072\n" \
  "v_cndmask_b32_dpp v20, v23, v22, vcc quad_perm:[2,3,0,1] row_mask:0xf bank_mask:0xf\n" \
  "v_mov_b32 v41, 0\n" \
  "v_mov_b32 v42, 0\n" \
  "v_cndmask_b32_dpp v12, v16, v12, vcc quad_perm:[2,3,0,1] row_mask:0xf bank_mask:0xf\n" \
  "v_cndmask_b32_dpp v13, v17, v13, vcc quad_perm:[2,3,0,1] row_mask:0xf bank_mask:0xf\n" \
  "v_cndmask_b32_dpp v14, v18, v14, vcc quad_perm:[2,3,0,1] row_mask:0xf bank_mask:0xf\n" \
  "v_cndmask_b32_dpp v15, v19, v15, vcc quad_perm:[2,3,0,1] row_mask:0xf bank_mask:0xf\n" \
  "v_maximum3_f32 v16, v20, v12, v13\n" \
  "v_maximum3_f32 v16, v16, v14, v15\n" \
  "s_nop 1\n" \
  "v_max_f32_dpp v16, v16, v16 quad_perm:[2,3,0,1] row_mask:0xf bank_mask:0xf\n" \
  "v_mov_b32 v17, v16\n" \
  "s_nop 1\n" \
  "v_permlane16_swap_b32 v16, v17\n" \
  "v_max_f32 v16, v16, v17\n" \
  "v_mov_b32 v17, v16\n" \
  "s_nop 1\n" \
  "v_permlane32_swap_b32 v16, v17\n" \
  "v_max_f32 v16, v16, v17\n" \
  "v_mul_f32 v16, s78, v16\n" \
  "v_pk_fma_f32 v[12:13], v[12:13], s[78:79], v[16:17] op_sel_hi:[1,0,0] neg_lo:[0,0,1] neg_hi:[0,0,1]\n" \
  "v_pk_fma_f32 v[14:15], v[14:15], s[78:79], v[16:17] op_sel_hi:[1,0,0] neg_lo:[0,0,1] neg_hi:[0,0,1]\n" \
  "v_fma_f32 v20, v20, s78, -v16\n" \
  "v_exp_f32 v12, v12\n" \
  "v_exp_f32 v13, v13\n" \
  "v_exp_f32 v14, v14\n" \
  "v_exp_f32 v15, v15\n" \
  "v_exp_f32 v20, v20\n" \
  "s_waitcnt lgkmcnt(4)\n" \
  "v_cvt_pk_bf16_f32 v44, v12, v13\n" \
  "v_cvt_pk_bf16_f32 v45, v14, v15\n" \
  "v_pk_add_f32 v[8:9], v[12:13], v[14:15]\n" \
  "v_mov_b32_dpp v46, v44 quad_perm:[2,3,0,1] row_mask:0xf bank_mask:0xf\n" \
  "v_mov_b32_dpp v47, v45 quad_perm:[2,3,0,1] row_mask:0xf bank_mask:0xf\n" \
  "v_add_f32 v10, v8, v20\n" \
  "v_add_f32 v10, v10, v9\n" \
  "v_mfma_f32_16x16x32_bf16 v[12:15], v[48:51], v[44:47], 0\n" \
  "v_mov_b32_dpp v21, v20 quad_perm:[2,3,0,1] row_mask:0xf bank_mask:0xf\n" \
  "v_add_f32_dpp v10, v10, v10 quad_perm:[2,3,0,1] row_mask:0xf bank_mask:0xf\n" \
  "v_mov_b32 v11, v10\n" \
  "v_mfma_f32_16x16x32_bf16 v[16:19], v[52:55], v[44:47], 0\n" \
  "s_setprio 3\n" \
  "s_barrier\n" \
  "ds_read_b64 v[22:23], v7 offset:0\n" \
  "s_nop 0\n" \
  "v_permlane16_swap_b32 v10, v11\n" \
  "v_add_f32 v10, v10, v11\n" \
  "v_mov_b32 v11, v10\n" \
  "v_mov_b64 v[8:9], s[92:93]\n" \
  "s_nop 0\n" \
  "v_permlane32_swap_b32 v10, v11\n" \
  "v_add_f32 v10, v10, v11\n" \
  "v_rcp_f32 v10, v10\n" \
  "s_nop 0\n" \
  "v_mul_f32 v10, s84, v10\n" \
  "v_pk_mul_f32 v[20:21], v[20:21], v[10:11] op_sel_hi:[1,0]\n" \
  "v_pk_mul_f32 v[12:13], v[12:13], v[10:11] op_sel_hi:[1,0]\n" \
  "v_pk_mul_f32 v[14:15], v[14:15], v[10:11] op_sel_hi:[1,0]\n" \
  "v_cvt_pk_bf16_f32 v40, v20, v21\n" \
  "v_pk_mul_f32 v[16:17], v[16:17], v[10:11] op_sel_hi:[1,0]\n" \
  "v_cvt_pk_bf16_f32 v43, v20, v21\n" \
  "v_pk_mul_f32 v[18:19], v[18:19], v[10:11] op_sel_hi:[1,0]\n" \
  "s_waitcnt lgkmcnt(0)\n" \
  "v_mfma_f32_16x16x16_bf16 v[12:15], v[22:23], v[40:41], v[12:15]\n" \
  "v_mfma_f32_16x16x16_bf16 v[16:19], v[22:23], v[42:43], v[16:19]\n" \
  "v_mov_b64 v[10:11], s[92:93]\n" \
  "ds_read_b128 v[48:51], v6 offset:10240\n" \
  "ds_read_b128 v[52:55], v6 offset:11264\n" \
  "v_mov_b64 v[20:21], s[92:93]\n" \
  "v_mov_b64 v[22:23], s[92:93]\n" \
  "s_nop 1\n" \
  "v_cvt_pk_bf16_f32 v40, v12, v13\n" \
  "v_cvt_pk_bf16_f32 v41, v14, v15\n" \
  "v_cvt_pk_bf16_f32 v42, v16, v17\n" \
  "ds_write_b64 v4, v[40:41]\n" \
  "v_cvt_pk_bf16_f32 v43, v18, v19\n" \
  "ds_write_b64 v4, v[42:43] offset:1024\n" \
  "v_mov_b64 v[12:13], s[92:93]\n" \
  "v_mov_b64 v[14:15], s[92:93]\n" \
  "v_mov_b64 v[16:17], s[92:93]\n" \
  "v_mov_b64 v[18:19], s[92:93]\n" \
  "s_setprio 0\n" \
  "s_waitcnt lgkmcnt(0)\n" \
  "s_barrier\n" \
  "ds_read_b128 v[40:43], v1 offset:8192\n" \
  "ds_read_b128 v[44:47], v1 offset:9216\n" \
  "s_waitcnt lgkmcnt(1)\n" \
  "v_smfmac_f32_16x16x64_bf16 v[12:15], v[40:43], v[56:63], v0\n" \
  "ds_read_b128 v[56:59], v6 offset:4096\n" \
  "ds_read_b128 v[60:63], v6 offset:5120\n" \
  "v_smfmac_f32_16x16x64_bf16 v[16:19], v[40:43], v[24:31], v0\n" \
  "ds_read_b128 v[24:27], v6 offset:6144\n" \
  "ds_read_b128 v[28:31], v6 offset:7168\n" \
  "s_waitcnt lgkmcnt(4)\n" \
  "v_smfmac_f32_16x16x64_bf16 v[12:15], v[44:47], v[32:39], v0\n" \
  "ds_read_b128 v[32:35], v6 offset:12288\n" \
  "ds_read_b128 v[36:39], v6 offset:13312\n" \
  "v_smfmac_f32_16x16x64_bf16 v[16:19], v[44:47], v[48:55], v0\n" \
  "ds_read_b128 v[48:51], v6 offset:14336\n" \
  "ds_read_b128 v[52:55], v6 offset:15360\n" \
  "s_nop 5\n" \
  "v_permlane32_swap_b32 v12, v17\n" \
  "v_permlane32_swap_b32 v13, v16\n" \
  "v_permlane32_swap_b32 v14, v19\n" \
  "v_permlane32_swap_b32 v15, v18\n" \
  "v_pk_add_f32 v[12:13], v[12:13], v[16:17] op_sel:[0,1] op_sel_hi:[1,0]\n" \
  "v_pk_add_f32 v[14:15], v[14:15], v[18:19] op_sel:[0,1] op_sel_hi:[1,0]\n" \
  "buffer_atomic_add_x2 v[12:13], v5, s[40:43], s56 offen\n" \
  "buffer_atomic_add_x2 v[14:15], v5, s[40:43], s57 offen\n" \
  "s_waitcnt lgkmcnt(6)\n" \
  "v_smfmac_f32_16x16x64_bf16 v[20:23], v[40:43], v[56:63], v0\n" \
  "s_waitcnt lgkmcnt(4)\n" \
  "v_smfmac_f32_16x16x64_bf16 v[8:11], v[40:43], v[24:31], v0\n" \
  "s_waitcnt lgkmcnt(2)\n" \
  "v_smfmac_f32_16x16x64_bf16 v[20:23], v[44:47], v[32:39], v0\n" \
  "s_waitcnt lgkmcnt(0)\n" \
  "v_smfmac_f32_16x16x64_bf16 v[8:11], v[44:47], v[48:55], v0\n" \
  "s_nop 7\n" \
  "v_permlane32_swap_b32 v20, v9\n" \
  "v_permlane32_swap_b32 v21, v8\n" \
  "v_permlane32_swap_b32 v22, v11\n" \
  "v_permlane32_swap_b32 v23, v10\n" \
  "v_pk_add_f32 v[20:21], v[20:21], v[8:9] op_sel:[0,1] op_sel_hi:[1,0]\n" \
  "v_pk_add_f32 v[22:23], v[22:23], v[10:11] op_sel:[0,1] op_sel_hi:[1,0]\n" \
  "buffer_atomic_add_x2 v[20:21], v5, s[40:43], s58 offen\n" \
  "buffer_atomic_add_x2 v[22:23], v5, s[40:43], s59 offen\n" \
  "s_memrealtime s[38:39]\n" \
  "v_mbcnt_lo_u32_b32 v48, -1, 0\n" \
  "v_mbcnt_hi_u32_b32 v48, -1, v48\n" \
  "s_add_u32 s88, s74, 157760\n" \
  "v_mov_b32 v59, s88\n" \
  "v_lshl_add_u32 v52, v48, 2, v59\n" \
  "ds_read_b32 v52, v52 offset:192\n" \
  "v_lshrrev_b32 v49, 3, v48\n" \
  "v_and_b32 v50, 7, v48\n" \
  "v_lshlrev_b32 v50, 4, v50\n" \
  "v_lshl_or_b32 v44, v49, 8, v50\n" \
  "s_mov_b32 s87, 0x40000\n" \
  "v_lshrrev_b32 v49, 2, v48\n" \
  "v_lshrrev_b32 v50, 4, v48\n" \
  "v_bitop3_b32 v50, v49, v50, 3 bitop3:0x28\n" \
  "v_lshl_add_u32 v8, v50, 3, s97\n" \
  "v_and_b32 v50, 3, v48\n" \
  "v_lshl_add_u32 v8, v50, 5, v8\n" \
  "v_and_b32 v49, 1, v49\n" \
  "v_lshl_add_u32 v8, v49, 8, v8\n" \
  "v_and_b32 v49, 8, v48\n" \
  "v_lshl_add_u32 v8, v49, 4, v8\n" \
  "v_lshrrev_b32 v49, 5, v48\n" \
  "v_add_u32 v10, s99, v1\n" \
  "s_lshr_b32 s88, s99, 2\n" \
  "s_add_u32 s88, s88, s74\n" \
  "s_add_u32 s88, s88, 139264\n" \
  "v_lshl_add_u32 v7, v49, 10, s88\n" \
  "v_and_b32 v49, 31, v48\n" \
  "v_lshl_add_u32 v7, v49, 3, v7\n" \
  "v_mov_b64 v[12:13], 0\n" \
  "v_mov_b64 v[14:15], 0\n" \
  "v_mov_b64 v[16:17], 0\n" \
  "v_mov_b64 v[18:19], 0\n" \
  "v_mov_b64 v[20:21], 0\n" \
  "v_mov_b64 v[22:23], 0\n" \
  "v_mov_b32 v34, 0xc9b26ca0\n" \
  "v_mov_b32 v35, 0xc9b26c86\n" \
  "v_and_b32 v50, 3, v48\n" \
  "v_lshl_add_u32 v40, v50, 2, s98\n" \
  "v_lshrrev_b32 v50, 4, v48\n" \
  "v_mad_u32_u24 v40, v50, s95, v40\n" \
  "v_bfe_u32 v49, v48, 3, 1\n" \
  "v_lshl_add_u32 v40, v49, 6, v40\n" \
  "v_bfe_u32 v49, v48, 2, 1\n" \
  "v_lshl_add_u32 v40, v49, 7, v40\n" \
  "v_xor_b32 v41, 16, v40\n" \
  "v_xor_b32 v42, 32, v40\n" \
  "v_xor_b32 v43, 48, v40\n" \
  "s_add_i32 s80, s86, 0x35000000\n" \
  "v_mov_b32 v11, 0\n" \
  "s_mov_b32 s89, s60\n" \
  "s_bitcmp1_b32 s60, 7\n" \
  "s_cselect_b32 s88, 0x10000, 0\n" \
  "s_bitset1_b32 s88, 7\n" \
  "s_xor_b32 s56, s56, s88\n" \
  "s_xor_b32 s57, s57, s88\n" \
  "s_xor_b32 s58, s58, s88\n" \
  "s_xor_b32 s59, s59, s88\n" \
  "s_xor_b32 s60, s60, s88\n" \
  "s_sub_u32 s71, s71, 1\n" \
  "s_waitcnt lgkmcnt(0)\n" \
  "s_sub_u32 s39, s38, 150\n" \
  "v_subrev_u32 v52, s39, v52\n" \
  "v_cmp_lt_u32 vcc, 0x1000, v52\n" \
  "s_cmp_eq_u64 vcc, 0\n" \
  "s_cbranch_scc1 .LCA_OKM_%=\n" \
  ".LCA_AGN_%=:\n" \
  "buffer_load_dword v52, v5, s[0:3], s66 offen sc1\n" \
  "s_sub_u32 s87, s87, 1\n" \
  "s_cbranch_scc1 .LCA_END_%=\n" \
  "s_waitcnt vmcnt(0)\n" \
  "v_cmp_eq_u32 vcc, 0, v52\n" \
  "v_subrev_u32 v52, s39, v52\n" \
  "s_cmp_eq_u64 vcc, 0\n" \
  "s_cbranch_scc1 .LCA_OKM_%=\n" \
  "s_sleep 4\n" \
  "s_branch .LCA_AGN_%=\n" \
  ".LCA_OKM_%=:\n" \
  "s_nop 1\n" \
  "v_max_i32_dpp v52, v52, v52 row_ror:1 row_mask:0xf bank_mask:0xf\n" \
  "s_nop 1\n" \
  "v_max_i32_dpp v52, v52, v52 row_ror:2 row_mask:0xf bank_mask:0xf\n" \
  "s_nop 1\n" \
  "v_max_i32_dpp v52, v52, v52 row_ror:4 row_mask:0xf bank_mask:0xf\n" \
  "s_nop 1\n" \
  "v_max_i32_dpp v52, v52, v52 row_ror:8 row_mask:0xf bank_mask:0xf\n" \
  "s_movk_i32 vcc_lo, 150\n" \
  "v_readlane_b32 s88, v52, 0\n" \
  "s_max_i32 vcc_lo, vcc_lo, s88\n" \
  "v_readlane_b32 s88, v52, 16\n" \
  "s_max_i32 vcc_lo, vcc_lo, s88\n" \
  "v_readlane_b32 s88, v52, 32\n" \
  "s_max_i32 vcc_lo, vcc_lo, s88\n" \
  "v_readlane_b32 s88, v52, 48\n" \
  "s_max_i32 vcc_lo, vcc_lo, s88\n" \
  "s_add_u32 s66, s66, 1024\n" \
  "s_add_u32 vcc_lo, vcc_lo, s39\n" \
  "s_add_u32 vcc_lo, vcc_lo, 23\n" \
  ".LCA_TW_%=:\n" \
  "s_memrealtime s[38:39]\n" \
  "s_waitcnt lgkmcnt(0)\n" \
  "s_sub_u32 s38, s38, vcc_lo\n" \
  "s_cmp_lt_i32 s38, 0\n" \
  "s_cbranch_scc1 .LCA_TW_%=\n" \
  "buffer_load_dwordx4 v[24:27], v44, s[40:43], s89 offen sc1\n" \
  "buffer_load_dwordx4 v[28:31], v44, s[40:43], s89 offen offset:2048 sc1\n" \
  ".LCA_SEE_%=:\n" \
  "s_waitcnt vmcnt(1)\n" \
  "v_cvt_f32_i32 v24, v24\n" \
  "v_cvt_f32_i32 v25, v25\n" \
  "v_cvt_f32_i32 v26, v26\n" \
  "v_cvt_f32_i32 v27, v27\n" \
  "v_max_f32 v45, |v24|, |v26|\n" \
  "v_pk_mul_f32 v[24:25], v[24:25], s[80:81] op_sel_hi:[1,0]\n" \
  "v_pk_mul_f32 v[26:27], v[26:27], s[80:81] op_sel_hi:[1,0]\n" \
  "s_nop 0\n" \
  "v_cvt_pk_bf16_f32 v32, v24, v26\n" \
  "v_cvt_pk_bf16_f32 v33, v25, v27\n" \
  "ds_write_b32 v40, v32\n" \
  "ds_write_b32 v41, v33\n" \
  "s_waitcnt vmcnt(0)\n" \
  "v_cvt_f32_i32 v28, v28\n" \
  "v_cvt_f32_i32 v30, v30\n" \
  "v_cvt_f32_i32 v29, v29\n" \
  "v_max3_f32 v45, v45, |v28|, |v30|\n" \
  "v_cvt_f32_i32 v31, v31\n" \
  "v_cmp_lt_f32 vcc, s91, v45\n" \
  "v_pk_mul_f32 v[28:29], v[28:29], s[80:81] op_sel_hi:[1,0]\n" \
  "v_pk_mul_f32 v[30:31], v[30:31], s[80:81] op_sel_hi:[1,0]\n" \
  "s_cmp_eq_u64 vcc, 0\n" \
  "s_cbranch_scc0 .LCA_POLL_%=\n" \
  "v_cvt_pk_bf16_f32 v36, v28, v30\n" \
  "v_cvt_pk_bf16_f32 v37, v29, v31\n" \
  "ds_write_b32 v42, v36\n" \
  "ds_write_b32 v43, v37\n" \
  "v_dot2c_f32_bf16 v11, v32, v32\n" \
  "v_dot2c_f32_bf16 v11, v33, v33\n" \
  "v_dot2c_f32_bf16 v11, v36, v36\n" \
  "v_dot2c_f32_bf16 v11, v37, v37\n" \
  "s_nop 1\n" \
  "s_nop 0\n" \
  "v_cmp_lt_f32 vcc, 4.0, v11\n" \
  "s_cbranch_vccz .LCA_TOP_%=\n" \
  "s_mov_b64 exec, vcc\n" \
  "ds_max_u32 v59, v11\n" \
  "s_mov_b64 exec, -1\n" \
  "s_branch .LCA_TOP_%=\n" \
  ".LCA_POLL_%=:\n" \
  "buffer_load_dwordx4 v[24:27], v44, s[40:43], s89 offen sc1\n" \
  "buffer_load_dwordx4 v[28:31], v44, s[40:43], s89 offen offset:2048 sc1\n" \
  "s_sub_u32 s87, s87, 1\n" \
  "s_cbranch_scc1 .LCA_END_%=\n" \
  "s_branch .LCA_SEE_%=\n" \
  ".LCA_LAST_%=:\n" \
  ".LCA_END_%=:\n" \
  "s_endpgm\n"
#define CHAIN_ASM_VO \
  "s_mov_b32 s95, 272\n" \
  "s_mov_b32 s91, 0x4c7c2000\n" \
  "s_mov_b32 s85, 0xc9b26c9b\n" \
  "s_mov_b32 s92, 0xc9326c9b\n" \
  "s_mov_b32 s93, 0xc9326c9b\n" \
  "s_mov_b32 s78, 0x3e0293ee\n" \
  "s_mov_b32 s76, 0x33333333\n" \
  "s_mov_b32 s77, 0x33333333\n" \
  "s_lshl_b32 s98, s73, 10\n" \
  "s_add_u32 s98, s98, s74\n" \
  "s_add_u32 s98, s98, 131072\n" \
  "s_lshl_b32 s97, s73, 9\n" \
  "s_add_u32 s97, s97, s74\n" \
  "s_add_u32 s97, s97, 158724\n" \
  "s_lshl_b32 s99, s73, 11\n" \
  "s_add_u32 s99, s99, s74\n" \
  "s_add_u32 s99, s99, 133120\n" \
  "s_lshl_b32 s94, s73, 4\n" \
  "s_add_u32 s94, s94, s74\n" \
  "s_add_u32 s94, s94, 157760\n" \
  "v_mbcnt_lo_u32_b32 v48, -1, 0\n" \
  "v_mbcnt_hi_u32_b32 v48, -1, v48\n" \
  "s_add_u32 s88, s74, 157760\n" \
  "v_mov_b32 v59, s88\n" \
  "s_sub_u32 s88, s73, 4\n" \
  "v_lshrrev_b32 v49, 2, v48\n" \
  "v_lshrrev_b32 v50, 4, v48\n" \
  "v_bitop3_b32 v50, v49, v50, 3 bitop3:0x28\n" \
  "v_lshl_add_u32 v4, v50, 3, s97\n" \
  "v_and_b32 v50, 3, v48\n" \
  "v_lshl_add_u32 v4, v50, 5, v4\n" \
  "v_and_b32 v49, 1, v49\n" \
  "v_lshl_add_u32 v4, v49, 8, v4\n" \
  "v_and_b32 v49, 8, v48\n" \
  "v_lshl_add_u32 v4, v49, 4, v4\n" \
  "v_and_b32 v51, 15, v48\n" \
  "v_bfe_u32 v49, v48, 4, 1\n" \
  "v_lshrrev_b32 v50, 5, v48\n" \
  "s_lshl_b32 s89, s88, 10\n" \
  "s_add_u32 s89, s89, s74\n" \
  "s_add_u32 s89, s89, 158720\n" \
  "v_lshrrev_b32 v9, 3, v51\n" \
  "v_mul_u32_u24 v6, 41, v9\n" \
  "v_xor_b32 v6, v6, v51\n" \
  "v_lshl_add_u32 v6, v6, 3, s89\n" \
  "v_lshl_add_u32 v6, v49, 6, v6\n" \
  "v_lshl_add_u32 v6, v50, 9, v6\n" \
  "v_xor_b32 v9, 0x90, v6\n" \
  "v_lshlrev_b32 v8, 4, v49\n" \
  "v_lshl_add_u32 v8, v51, 5, v8\n" \
  "v_lshl_add_u32 v8, v50, 3, v8\n" \
  "s_lshl_b32 s89, s88, 8\n" \
  "s_add_u32 s89, s89, s74\n" \
  "s_add_u32 s89, s89, 139264\n" \
  "v_and_b32 v49, 31, v48\n" \
  "v_lshl_add_u32 v5, v49, 3, s89\n" \
  "v_lshl_add_u32 v5, v50, 10, v5\n" \
  "v_lshrrev_b32 v49, 2, v51\n" \
  "v_and_b32 v50, 5, v51\n" \
  "v_and_or_b32 v49, v49, 2, v50\n" \
  "v_and_b32 v50, 48, v48\n" \
  "v_lshlrev_b32 v7, 3, v50\n" \
  "v_lshl_add_u32 v7, v49, 4, v7\n" \
  "v_bitop3_b32 v49, v51, 2, 2 bitop3:0x0c\n" \
  "v_lshl_add_u32 v7, v49, 29, v7\n" \
  "v_mov_b64 v[12:13], 0\n" \
  "v_mov_b64 v[14:15], 0\n" \
  "v_mov_b64 v[16:17], 0\n" \
  "v_mov_b64 v[18:19], 0\n" \
  "v_mov_b64 v[20:21], 0\n" \
  "v_mov_b64 v[22:23], 0\n" \
  "v_lshlrev_b32 v10, 4, v48\n" \
  "s_add_u32 s78, s74, 157952\n" \
  "v_lshl_add_u32 v52, v48, 2, v59\n" \
  "ds_write_b32 v52, v12 offset:192\n" \
  "s_movk_i32 s79, 105\n" \
  "s_movk_i32 s96, 25\n" \
  "s_mov_b32 s87, 0x40000\n" \
  "v_subrev_u32 v30, 64, v59\n" \
  "v_add_u32 v17, 0x10000, v28\n" \
  "v_xor_b32 v18, 128, v28\n" \
  "v_xor_b32 v19, 128, v17\n" \
  "ds_write_b32 v30, v12 offset:0\n" \
  "ds_write_b32 v30, v12 offset:32\n" \
  "ds_write_b32 v30, v12 offset:64\n" \
  "v_xor_b32 v20, 0, v29\n" \
  "v_xor_b32 v21, 32, v29\n" \
  "v_xor_b32 v22, 64, v29\n" \
  "v_xor_b32 v23, 96, v29\n" \
  "s_waitcnt vmcnt(12) lgkmcnt(0)\n" \
  "s_barrier\n" \
  "ds_read_b64_tr_b16 v[96:97], v20 offset:0\n" \
  "ds_read_b64_tr_b16 v[98:99], v20 offset:2048\n" \
  "ds_read_b64_tr_b16 v[104:105], v21 offset:0\n" \
  "ds_read_b64_tr_b16 v[106:107], v21 offset:2048\n" \
  "ds_read_b64_tr_b16 v[112:113], v22 offset:0\n" \
  "ds_read_b64_tr_b16 v[114:115], v22 offset:2048\n" \
  "ds_read_b64_tr_b16 v[120:121], v23 offset:0\n" \
  "ds_read_b64_tr_b16 v[122:123], v23 offset:2048\n" \
  "s_lshl_b32 s88, s73, 10\n" \
  "v_add_u32 v48, s88, v1\n" \
  "ds_read_b128 v[52:55], v48 offset:0\n" \
  "s_waitcnt lgkmcnt(0)\n" \
  "v_mov_b32 v11, 0\n" \
  "v_dot2c_f32_bf16 v11, v52, v52\n" \
  "v_dot2c_f32_bf16 v11, v53, v53\n" \
  "v_dot2c_f32_bf16 v11, v54, v54\n" \
  "v_dot2c_f32_bf16 v11, v55, v55\n" \
  "s_nop 0\n" \
  "s_nop 1\n" \
  "v_max_u32_dpp v11, v11, v11 row_ror:1 row_mask:0xf bank_mask:0xf\n" \
  "s_nop 1\n" \
  "v_max_u32_dpp v11, v11, v11 row_ror:2 row_mask:0xf bank_mask:0xf\n" \
  "s_nop 1\n" \
  "v_max_u32_dpp v11, v11, v11 row_ror:4 row_mask:0xf bank_mask:0xf\n" \
  "s_nop 1\n" \
  "v_max_u32_dpp v11, v11, v11 row_ror:8 row_mask:0xf bank_mask:0xf\n" \
  "v_mov_b32 v31, v11\n" \
  "s_nop 1\n" \
  "v_permlane16_swap_b32 v11, v31\n" \
  "v_max_u32 v11, v11, v31\n" \
  "v_mov_b32 v31, v11\n" \
  "s_nop 1\n" \
  "v_permlane32_swap_b32 v11, v31\n" \
  "v_max_u32 v11, v11, v31\n" \
  "s_mov_b64 exec, 1\n" \
  "ds_max_u32 v59, v11\n" \
  "s_mov_b64 exec, -1\n" \
  "s_waitcnt vmcnt(8) lgkmcnt(0)\n" \
  "s_barrier\n" \
  "ds_read_b64_tr_b16 v[100:101], v20 offset:4096\n" \
  "ds_read_b64_tr_b16 v[102:103], v20 offset:6144\n" \
  "ds_read_b64_tr_b16 v[108:109], v21 offset:4096\n" \
  "ds_read_b64_tr_b16 v[110:111], v21 offset:6144\n" \
  "ds_read_b64_tr_b16 v[116:117], v22 offset:4096\n" \
  "ds_read_b64_tr_b16 v[118:119], v22 offset:6144\n" \
  "ds_read_b64_tr_b16 v[124:125], v23 offset:4096\n" \
  "ds_read_b64_tr_b16 v[126:127], v23 offset:6144\n" \
  "s_add_u32 m0, s19, 0\n" \
  "s_nop 0\n" \
  "global_load_lds_dwordx4 v24, s[4:5] offset:0\n" \
  "global_load_lds_dwordx4 v25, s[4:5] offset:1024\n" \
  "global_load_lds_dwordx4 v26, s[4:5] offset:2048\n" \
  "global_load_lds_dwordx4 v27, s[4:5] offset:3072\n" \
  "s_add_u32 s4, s4, 0x100000\n" \
  "s_addc_u32 s5, s5, 0\n" \
  "v_mfma_f32_16x16x32_bf16 v[64:67], v[96:99], v[96:99], 0\n" \
  "v_mfma_f32_16x16x32_bf16 v[68:71], v[104:107], v[104:107], 0\n" \
  "v_mfma_f32_16x16x32_bf16 v[72:75], v[112:115], v[112:115], 0\n" \
  "v_mfma_f32_16x16x32_bf16 v[76:79], v[120:123], v[120:123], 0\n" \
  "s_waitcnt vmcnt(8) lgkmcnt(0)\n" \
  "s_barrier\n" \
  "ds_read_b64_tr_b16 v[32:33], v20 offset:8192\n" \
  "ds_read_b64_tr_b16 v[34:35], v20 offset:10240\n" \
  "ds_read_b64_tr_b16 v[40:41], v21 offset:8192\n" \
  "ds_read_b64_tr_b16 v[42:43], v21 offset:10240\n" \
  "ds_read_b64_tr_b16 v[48:49], v22 offset:8192\n" \
  "ds_read_b64_tr_b16 v[50:51], v22 offset:10240\n" \
  "ds_read_b64_tr_b16 v[56:57], v23 offset:8192\n" \
  "ds_read_b64_tr_b16 v[58:59], v23 offset:10240\n" \
  "s_add_u32 m0, s19, 4096\n" \
  "s_nop 0\n" \
  "global_load_lds_dwordx4 v24, s[4:5] offset:0\n" \
  "global_load_lds_dwordx4 v25, s[4:5] offset:1024\n" \
  "global_load_lds_dwordx4 v26, s[4:5] offset:2048\n" \
  "global_load_lds_dwordx4 v27, s[4:5] offset:3072\n" \
  "s_add_u32 s4, s4, 0x100000\n" \
  "s_addc_u32 s5, s5, 0\n" \
  "v_mfma_f32_16x16x32_bf16 v[64:67], v[100:103], v[100:103], v[64:67]\n" \
  "v_mfma_f32_16x16x32_bf16 v[68:71], v[108:111], v[108:111], v[68:71]\n" \
  "v_mfma_f32_16x16x32_bf16 v[72:75], v[116:119], v[116:119], v[72:75]\n" \
  "v_mfma_f32_16x16x32_bf16 v[76:79], v[124:127], v[124:127], v[76:79]\n" \
  "s_waitcnt vmcnt(8) lgkmcnt(0)\n" \
  "s_barrier\n" \
  "ds_read_b64_tr_b16 v[36:37], v20 offset:12288\n" \
  "ds_read_b64_tr_b16 v[38:39], v20 offset:14336\n" \
  "ds_read_b64_tr_b16 v[44:45], v21 offset:12288\n" \
  "ds_read_b64_tr_b16 v[46:47], v21 offset:14336\n" \
  "ds_read_b64_tr_b16 v[52:53], v22 offset:12288\n" \
  "ds_read_b64_tr_b16 v[54:55], v22 offset:14336\n" \
  "ds_read_b64_tr_b16 v[60:61], v23 offset:12288\n" \
  "ds_read_b64_tr_b16 v[62:63], v23 offset:14336\n" \
  "s_add_u32 m0, s19, 8192\n" \
  "s_nop 0\n" \
  "global_load_lds_dwordx4 v24, s[4:5] offset:0\n" \
  "global_load_lds_dwordx4 v25, s[4:5] offset:1024\n" \
  "global_load_lds_dwordx4 v26, s[4:5] offset:2048\n" \
  "global_load_lds_dwordx4 v27, s[4:5] offset:3072\n" \
  "s_add_u32 s4, s4, 0x100000\n" \
  "s_addc_u32 s5, s5, 0\n" \
  "v_mfma_f32_16x16x32_bf16 v[64:67], v[32:35], v[32:35], v[64:67]\n" \
  "v_mfma_f32_16x16x32_bf16 v[68:71], v[40:43], v[40:43], v[68:71]\n" \
  "v_mfma_f32_16x16x32_bf16 v[72:75], v[48:51], v[48:51], v[72:75]\n" \
  "v_mfma_f32_16x16x32_bf16 v[76:79], v[56:59], v[56:59], v[76:79]\n" \
  "s_waitcnt vmcnt(8) lgkmcnt(0)\n" \
  "s_barrier\n" \
  "ds_read_b64_tr_b16 v[128:129], v18 offset:0\n" \
  "ds_read_b64_tr_b16 v[130:131], v18 offset:512\n" \
  "ds_read_b64_tr_b16 v[132:133], v18 offset:32768\n" \
  "ds_read_b64_tr_b16 v[134:135], v18 offset:33280\n" \
  "ds_read_b64_tr_b16 v[136:137], v19 offset:0\n" \
  "ds_read_b64_tr_b16 v[138:139], v19 offset:512\n" \
  "ds_read_b64_tr_b16 v[140:141], v19 offset:32768\n" \
  "ds_read_b64_tr_b16 v[142:143], v19 offset:33280\n" \
  "ds_read_b64_tr_b16 v[192:193], v28 offset:0\n" \
  "ds_read_b64_tr_b16 v[194:195], v28 offset:512\n" \
  "ds_read_b64_tr_b16 v[196:197], v28 offset:32768\n" \
  "ds_read_b64_tr_b16 v[198:199], v28 offset:33280\n" \
  "ds_read_b64_tr_b16 v[200:201], v17 offset:0\n" \
  "ds_read_b64_tr_b16 v[202:203], v17 offset:512\n" \
  "ds_read_b64_tr_b16 v[204:205], v17 offset:32768\n" \
  "ds_read_b64_tr_b16 v[206:207], v17 offset:33280\n" \
  "s_add_u32 m0, s19, 12288\n" \
  "s_nop 0\n" \
  "global_load_lds_dwordx4 v24, s[4:5] offset:0\n" \
  "global_load_lds_dwordx4 v25, s[4:5] offset:1024\n" \
  "global_load_lds_dwordx4 v26, s[4:5] offset:2048\n" \
  "global_load_lds_dwordx4 v27, s[4:5] offset:3072\n" \
  "s_add_u32 s4, s4, 0x100000\n" \
  "s_addc_u32 s5, s5, 0\n" \
  "v_mfma_f32_16x16x32_bf16 v[64:67], v[36:39], v[36:39], v[64:67]\n" \
  "v_mfma_f32_16x16x32_bf16 v[68:71], v[44:47], v[44:47], v[68:71]\n" \
  "v_mfma_f32_16x16x32_bf16 v[72:75], v[52:55], v[52:55], v[72:75]\n" \
  "v_mfma_f32_16x16x32_bf16 v[76:79], v[60:63], v[60:63], v[76:79]\n" \
  "s_nop 7\n" \
  "v_max3_f32 v13, v64, v65, v66\n" \
  "v_max3_f32 v13, v13, v67, v68\n" \
  "v_max3_f32 v13, v13, v69, v70\n" \
  "v_max3_f32 v13, v13, v71, v72\n" \
  "v_max3_f32 v13, v13, v73, v74\n" \
  "v_max3_f32 v13, v13, v75, v76\n" \
  "v_max3_f32 v13, v13, v77, v78\n" \
  "v_max_f32 v13, v13, v79\n" \
  "s_nop 1\n" \
  "v_max_f32_dpp v13, v13, v13 row_ror:1 row_mask:0xf bank_mask:0xf\n" \
  "s_nop 1\n" \
  "v_max_f32_dpp v13, v13, v13 row_ror:2 row_mask:0xf bank_mask:0xf\n" \
  "s_nop 1\n" \
  "v_max_f32_dpp v13, v13, v13 row_ror:4 row_mask:0xf bank_mask:0xf\n" \
  "s_nop 1\n" \
  "v_max_f32_dpp v13, v13, v13 row_ror:8 row_mask:0xf bank_mask:0xf\n" \
  "v_mov_b32 v31, v13\n" \
  "s_nop 1\n" \
  "v_permlane16_swap_b32 v13, v31\n" \
  "v_max_f32 v13, v13, v31\n" \
  "v_mov_b32 v31, v13\n" \
  "s_nop 1\n" \
  "v_permlane32_swap_b32 v13, v31\n" \
  "v_max_f32 v13, v13, v31\n" \
  "s_mov_b64 exec, 1\n" \
  "ds_max_u32 v30, v13 offset:32\n" \
  "s_mov_b64 exec, -1\n" \
  "s_waitcnt vmcnt(8) lgkmcnt(0)\n" \
  "s_barrier\n" \
  "ds_read_b64_tr_b16 v[144:145], v18 offset:4096\n" \
  "ds_read_b64_tr_b16 v[146:147], v18 offset:4608\n" \
  "ds_read_b64_tr_b16 v[148:149], v18 offset:36864\n" \
  "ds_read_b64_tr_b16 v[150:151], v18 offset:37376\n" \
  "ds_read_b64_tr_b16 v[152:153], v19 offset:4096\n" \
  "ds_read_b64_tr_b16 v[154:155], v19 offset:4608\n" \
  "ds_read_b64_tr_b16 v[156:157], v19 offset:36864\n" \
  "ds_read_b64_tr_b16 v[158:159], v19 offset:37376\n" \
  "ds_read_b64_tr_b16 v[208:209], v28 offset:4096\n" \
  "ds_read_b64_tr_b16 v[210:211], v28 offset:4608\n" \
  "ds_read_b64_tr_b16 v[212:213], v28 offset:36864\n" \
  "ds_read_b64_tr_b16 v[214:215], v28 offset:37376\n" \
  "ds_read_b64_tr_b16 v[216:217], v17 offset:4096\n" \
  "ds_read_b64_tr_b16 v[218:219], v17 offset:4608\n" \
  "ds_read_b64_tr_b16 v[220:221], v17 offset:36864\n" \
  "ds_read_b64_tr_b16 v[222:223], v17 offset:37376\n" \
  "s_add_u32 m0, s19, 0\n" \
  "s_nop 0\n" \
  "global_load_lds_dwordx4 v24, s[6:7] offset:0\n" \
  "global_load_lds_dwordx4 v25, s[6:7] offset:1024\n" \
  "global_load_lds_dwordx4 v26, s[6:7] offset:2048\n" \
  "global_load_lds_dwordx4 v27, s[6:7] offset:3072\n" \
  "s_add_u32 s6, s6, 0x100000\n" \
  "s_addc_u32 s7, s7, 0\n" \
  "v_mfma_f32_16x16x32_bf16 v[64:67], v[128:131], v[128:131], 0\n" \
  "v_mfma_f32_16x16x32_bf16 v[68:71], v[192:195], v[192:195], 0\n" \
  "v_mfma_f32_16x16x32_bf16 v[64:67], v[132:135], v[132:135], v[64:67]\n" \
  "v_mfma_f32_16x16x32_bf16 v[68:71], v[196:199], v[196:199], v[68:71]\n" \
  "v_mfma_f32_16x16x32_bf16 v[64:67], v[136:139], v[136:139], v[64:67]\n" \
  "v_mfma_f32_16x16x32_bf16 v[68:71], v[200:203], v[200:203], v[68:71]\n" \
  "v_mfma_f32_16x16x32_bf16 v[64:67], v[140:143], v[140:143], v[64:67]\n" \
  "v_mfma_f32_16x16x32_bf16 v[68:71], v[204:207], v[204:207], v[68:71]\n" \
  "s_waitcnt vmcnt(8) lgkmcnt(0)\n" \
  "s_barrier\n" \
  "ds_read_b64_tr_b16 v[160:161], v18 offset:8192\n" \
  "ds_read_b64_tr_b16 v[162:163], v18 offset:8704\n" \
  "ds_read_b64_tr_b16 v[164:165], v18 offset:40960\n" \
  "ds_read_b64_tr_b16 v[166:167], v18 offset:41472\n" \
  "ds_read_b64_tr_b16 v[168:169], v19 offset:8192\n" \
  "ds_read_b64_tr_b16 v[170:171], v19 offset:8704\n" \
  "ds_read_b64_tr_b16 v[172:173], v19 offset:40960\n" \
  "ds_read_b64_tr_b16 v[174:175], v19 offset:41472\n" \
  "ds_read_b64_tr_b16 v[224:225], v28 offset:8192\n" \
  "ds_read_b64_tr_b16 v[226:227], v28 offset:8704\n" \
  "ds_read_b64_tr_b16 v[228:229], v28 offset:40960\n" \
  "ds_read_b64_tr_b16 v[230:231], v28 offset:41472\n" \
  "ds_read_b64_tr_b16 v[232:233], v17 offset:8192\n" \
  "ds_read_b64_tr_b16 v[234:235], v17 offset:8704\n" \
  "ds_read_b64_tr_b16 v[236:237], v17 offset:40960\n" \
  "ds_read_b64_tr_b16 v[238:239], v17 offset:41472\n" \
  "s_add_u32 m0, s19, 4096\n" \
  "s_nop 0\n" \
  "global_load_lds_dwordx4 v24, s[6:7] offset:0\n" \
  "global_load_lds_dwordx4 v25, s[6:7] offset:1024\n" \
  "global_load_lds_dwordx4 v26, s[6:7] offset:2048\n" \
  "global_load_lds_dwordx4 v27, s[6:7] offset:3072\n" \
  "s_add_u32 s6, s6, 0x100000\n" \
  "s_addc_u32 s7, s7, 0\n" \
  "v_mfma_f32_16x16x32_bf16 v[64:67], v[144:147], v[144:147], v[64:67]\n" \
  "v_mfma_f32_16x16x32_bf16 v[68:71], v[208:211], v[208:211], v[68:71]\n" \
  "v_mfma_f32_16x16x32_bf16 v[64:67], v[148:151], v[148:151], v[64:67]\n" \
  "v_mfma_f32_16x16x32_bf16 v[68:71], v[212:215], v[212:215], v[68:71]\n" \
  "v_mfma_f32_16x16x32_bf16 v[64:67], v[152:155], v[152:155], v[64:67]\n" \
  "v_mfma_f32_16x16x32_bf16 v[68:71], v[216:219], v[216:219], v[68:71]\n" \
  "v_mfma_f32_16x16x32_bf16 v[64:67], v[156:159], v[156:159], v[64:67]\n" \
  "v_mfma_f32_16x16x32_bf16 v[68:71], v[220:223], v[220:223], v[68:71]\n" \
  "s_waitcnt vmcnt(8) lgkmcnt(0)\n" \
  "s_barrier\n" \
  "ds_read_b64_tr_b16 v[176:177], v18 offset:12288\n" \
  "ds_read_b64_tr_b16 v[178:179], v18 offset:12800\n" \
  "ds_read_b64_tr_b16 v[180:181], v18 offset:45056\n" \
  "ds_read_b64_tr_b16 v[182:183], v18 offset:45568\n" \
  "ds_read_b64_tr_b16 v[184:185], v19 offset:12288\n" \
  "ds_read_b64_tr_b16 v[186:187], v19 offset:12800\n" \
  "ds_read_b64_tr_b16 v[188:189], v19 offset:45056\n" \
  "ds_read_b64_tr_b16 v[190:191], v19 offset:45568\n" \
  "ds_read_b64_tr_b16 v[240:241], v28 offset:12288\n" \
  "ds_read_b64_tr_b16 v[242:243], v28 offset:12800\n" \
  "ds_read_b64_tr_b16 v[244:245], v28 offset:45056\n" \
  "ds_read_b64_tr_b16 v[246:247], v28 offset:45568\n" \
  "ds_read_b64_tr_b16 v[248:249], v17 offset:12288\n" \
  "ds_read_b64_tr_b16 v[250:251], v17 offset:12800\n" \
  "ds_read_b64_tr_b16 v[252:253], v17 offset:45056\n" \
  "ds_read_b64_tr_b16 v[254:255], v17 offset:45568\n" \
  "s_add_u32 m0, s19, 8192\n" \
  "s_nop 0\n" \
  "global_load_lds_dwordx4 v24, s[6:7] offset:0\n" \
  "global_load_lds_dwordx4 v25, s[6:7] offset:1024\n" \
  "global_load_lds_dwordx4 v26, s[6:7] offset:2048\n" \
  "global_load_lds_dwordx4 v27, s[6:7] offset:3072\n" \
  "s_add_u32 s6, s6, 0x100000\n" \
  "s_addc_u32 s7, s7, 0\n" \
  "v_mfma_f32_16x16x32_bf16 v[64:67], v[160:163], v[160:163], v[64:67]\n" \
  "v_mfma_f32_16x16x32_bf16 v[68:71], v[224:227], v[224:227], v[68:71]\n" \
  "v_mfma_f32_16x16x32_bf16 v[64:67], v[164:167], v[164:167], v[64:67]\n" \
  "v_mfma_f32_16x16x32_bf16 v[68:71], v[228:231], v[228:231], v[68:71]\n" \
  "v_mfma_f32_16x16x32_bf16 v[64:67], v[168:171], v[168:171], v[64:67]\n" \
  "v_mfma_f32_16x16x32_bf16 v[68:71], v[232:235], v[232:235], v[68:71]\n" \
  "v_mfma_f32_16x16x32_bf16 v[64:67], v[172:175], v[172:175], v[64:67]\n" \
  "v_mfma_f32_16x16x32_bf16 v[68:71], v[236:239], v[236:239], v[68:71]\n" \
  "s_waitcnt vmcnt(8) lgkmcnt(0)\n" \
  "s_barrier\n" \
  "s_add_u32 m0, s19, 12288\n" \
  "s_nop 0\n" \
  "global_load_lds_dwordx4 v24, s[6:7] offset:0\n" \
  "global_load_lds_dwordx4 v25, s[6:7] offset:1024\n" \
  "global_load_lds_dwordx4 v26, s[6:7] offset:2048\n" \
  "global_load_lds_dwordx4 v27, s[6:7] offset:3072\n" \
  "s_add_u32 s6, s6, 0x100000\n" \
  "s_addc_u32 s7, s7, 0\n" \
  "v_mfma_f32_16x16x32_bf16 v[64:67], v[176:179], v[176:179], v[64:67]\n" \
  "v_mfma_f32_16x16x32_bf16 v[68:71], v[240:243], v[240:243], v[68:71]\n" \
  "v_mfma_f32_16x16x32_bf16 v[64:67], v[180:183], v[180:183], v[64:67]\n" \
  "v_mfma_f32_16x16x32_bf16 v[68:71], v[244:247], v[244:247], v[68:71]\n" \
  "v_mfma_f32_16x16x32_bf16 v[64:67], v[184:187], v[184:187], v[64:67]\n" \
  "v_mfma_f32_16x16x32_bf16 v[68:71], v[248:251], v[248:251], v[68:71]\n" \
  "v_mfma_f32_16x16x32_bf16 v[64:67], v[188:191], v[188:191], v[64:67]\n" \
  "v_mfma_f32_16x16x32_bf16 v[68:71], v[252:255], v[252:255], v[68:71]\n" \
  "s_nop 7\n" \
  "s_mov_b32 exec_lo, 0x100001\n" \
  "s_mov_b32 exec_hi, 0x10000100\n" \
  "v_add_f32 v12, v64, v68\n" \
  "s_mov_b32 exec_lo, 0x200002\n" \
  "s_mov_b32 exec_hi, 0x20000200\n" \
  "v_add_f32 v12, v65, v69\n" \
  "s_mov_b32 exec_lo, 0x400004\n" \
  "s_mov_b32 exec_hi, 0x40000400\n" \
  "v_add_f32 v12, v66, v70\n" \
  "s_mov_b32 exec_lo, 0x800008\n" \
  "s_mov_b32 exec_hi, 0x80000800\n" \
  "v_add_f32 v12, v67, v71\n" \
  "s_mov_b64 exec, -1\n" \
  "s_nop 1\n" \
  "v_add_f32_dpp v12, v12, v12 row_ror:1 row_mask:0xf bank_mask:0xf\n" \
  "s_nop 1\n" \
  "v_add_f32_dpp v12, v12, v12 row_ror:2 row_mask:0xf bank_mask:0xf\n" \
  "s_nop 1\n" \
  "v_add_f32_dpp v12, v12, v12 row_ror:4 row_mask:0xf bank_mask:0xf\n" \
  "s_nop 1\n" \
  "v_add_f32_dpp v12, v12, v12 row_ror:8 row_mask:0xf bank_mask:0xf\n" \
  "v_mov_b32 v31, v12\n" \
  "s_nop 1\n" \
  "v_permlane16_swap_b32 v12, v31\n" \
  "v_add_f32 v12, v12, v31\n" \
  "v_mov_b32 v31, v12\n" \
  "s_nop 1\n" \
  "v_permlane32_swap_b32 v12, v31\n" \
  "v_add_f32 v12, v12, v31\n" \
  "v_lshl_add_u32 v31, s73, 2, v30\n" \
  "s_mov_b64 exec, 1\n" \
  "ds_write_b32 v31, v12\n" \
  "s_mov_b64 exec, -1\n" \
  "s_waitcnt vmcnt(8) lgkmcnt(0)\n" \
  "s_barrier\n" \
  "s_add_u32 m0, s19, 0\n" \
  "s_nop 0\n" \
  "global_load_lds_dwordx4 v24, s[8:9] offset:0\n" \
  "global_load_lds_dwordx4 v25, s[8:9] offset:1024\n" \
  "global_load_lds_dwordx4 v26, s[8:9] offset:2048\n" \
  "global_load_lds_dwordx4 v27, s[8:9] offset:3072\n" \
  "s_add_u32 s8, s8, 0x100000\n" \
  "s_addc_u32 s9, s9, 0\n" \
  "ds_read_b128 v[20:23], v30 offset:16\n" \
  "ds_read_b32 v31, v30 offset:32\n" \
  "v_mbcnt_lo_u32_b32 v16, -1, 0\n" \
  "v_mbcnt_hi_u32_b32 v16, -1, v16\n" \
  "v_lshlrev_b32 v15, 8, v16\n" \
  "s_waitcnt lgkmcnt(0)\n" \
  "v_add_f32 v20, v20, v21\n" \
  "v_add_f32 v22, v22, v23\n" \
  "v_add_f32 v20, v20, v22\n" \
  "v_mul_f32 v31, v20, v31\n" \
  "v_mul_f32 v31, 0x3f8147ae, v31\n" \
  "v_mov_b32 v12, s10\n" \
  "v_mov_b32 v13, 0xffff\n" \
  "v_bfi_b32 v13, v13, s11, v31\n" \
  "s_mov_b32 exec_hi, 0\n" \
  "s_mov_b32 exec_lo, s18\n" \
  "global_store_dwordx2 v15, v[12:13], s[16:17] sc1\n" \
  "s_mov_b64 exec, -1\n" \
  "v_lshlrev_b32 v16, 3, v16\n" \
  "s_waitcnt vmcnt(8) lgkmcnt(0)\n" \
  "s_barrier\n" \
  "s_add_u32 m0, s19, 4096\n" \
  "s_nop 0\n" \
  "global_load_lds_dwordx4 v24, s[8:9] offset:0\n" \
  "global_load_lds_dwordx4 v25, s[8:9] offset:1024\n" \
  "global_load_lds_dwordx4 v26, s[8:9] offset:2048\n" \
  "global_load_lds_dwordx4 v27, s[8:9] offset:3072\n" \
  "s_add_u32 s8, s8, 0x100000\n" \
  "s_addc_u32 s9, s9, 0\n" \
  "global_load_dwordx2 v[20:21], v16, s[12:13] sc1\n" \
  "s_waitcnt vmcnt(9) lgkmcnt(0)\n" \
  "s_barrier\n" \
  "s_add_u32 m0, s19, 8192\n" \
  "s_nop 0\n" \
  "global_load_lds_dwordx4 v24, s[8:9] offset:0\n" \
  "global_load_lds_dwordx4 v25, s[8:9] offset:1024\n" \
  "global_load_lds_dwordx4 v26, s[8:9] offset:2048\n" \
  "global_load_lds_dwordx4 v27, s[8:9] offset:3072\n" \
  "s_add_u32 s8, s8, 0x100000\n" \
  "s_addc_u32 s9, s9, 0\n" \
  "s_waitcnt vmcnt(9) lgkmcnt(0)\n" \
  "s_barrier\n" \
  "ds_read_b64_tr_b16 v[64:65], v28 offset:0\n" \
  "ds_read_b64_tr_b16 v[66:67], v28 offset:512\n" \
  "ds_read_b64_tr_b16 v[68:69], v28 offset:32768\n" \
  "ds_read_b64_tr_b16 v[70:71], v28 offset:33280\n" \
  "ds_read_b64_tr_b16 v[72:73], v17 offset:0\n" \
  "ds_read_b64_tr_b16 v[74:75], v17 offset:512\n" \
  "ds_read_b64_tr_b16 v[76:77], v17 offset:32768\n" \
  "ds_read_b64_tr_b16 v[78:79], v17 offset:33280\n" \
  "s_add_u32 m0, s19, 12288\n" \
  "s_nop 0\n" \
  "global_load_lds_dwordx4 v24, s[8:9] offset:0\n" \
  "global_load_lds_dwordx4 v25, s[8:9] offset:1024\n" \
  "global_load_lds_dwordx4 v26, s[8:9] offset:2048\n" \
  "global_load_lds_dwordx4 v27, s[8:9] offset:3072\n" \
  "s_add_u32 s8, s8, 0x100000\n" \
  "s_addc_u32 s9, s9, 0\n" \
  "s_waitcnt vmcnt(8)\n" \
  ".LCV_RD0_%=:\n" \
  "v_cmp_ne_u32 vcc, s10, v20\n" \
  "v_xor_b32 v31, s11, v21\n" \
  "v_cmp_ne_u32 s[22:23], 0, v31\n" \
  "s_or_b64 vcc, vcc, s[22:23]\n" \
  "s_cbranch_scc0 .LCV_RD1_%=\n" \
  "s_sleep 8\n" \
  "global_load_dwordx2 v[20:21], v16, s[12:13] sc1\n" \
  "s_sub_u32 s87, s87, 1\n" \
  "s_cbranch_scc1 .LCV_END_%=\n" \
  "s_waitcnt vmcnt(0)\n" \
  "s_branch .LCV_RD0_%=\n" \
  ".LCV_RD1_%=:\n" \
  "s_waitcnt vmcnt(8) lgkmcnt(0)\n" \
  "s_barrier\n" \
  "ds_write_b128 v3, v[96:99] offset:0\n" \
  "ds_write_b128 v3, v[100:103] offset:1024\n" \
  "ds_write_b128 v3, v[104:107] offset:2048\n" \
  "ds_write_b128 v3, v[108:111] offset:3072\n" \
  "ds_read_b64_tr_b16 v[80:81], v28 offset:4096\n" \
  "ds_read_b64_tr_b16 v[82:83], v28 offset:4608\n" \
  "ds_read_b64_tr_b16 v[84:85], v28 offset:36864\n" \
  "ds_read_b64_tr_b16 v[86:87], v28 offset:37376\n" \
  "ds_read_b64_tr_b16 v[88:89], v17 offset:4096\n" \
  "ds_read_b64_tr_b16 v[90:91], v17 offset:4608\n" \
  "ds_read_b64_tr_b16 v[92:93], v17 offset:36864\n" \
  "ds_read_b64_tr_b16 v[94:95], v17 offset:37376\n" \
  "v_and_b32 v16, 0xff, v16\n" \
  "global_load_dwordx2 v[22:23], v16, s[14:15] sc1\n" \
  "s_mov_b64 exec, 15\n" \
  "buffer_load_dword v13, v2, s[44:47], s68 offen sc1\n" \
  "s_mov_b64 exec, -1\n" \
  "s_waitcnt vmcnt(6) lgkmcnt(0)\n" \
  "s_barrier\n" \
  "ds_write_b128 v3, v[112:115] offset:4096\n" \
  "ds_write_b128 v3, v[116:119] offset:5120\n" \
  "ds_write_b128 v3, v[120:123] offset:6144\n" \
  "ds_write_b128 v3, v[124:127] offset:7168\n" \
  "ds_read_b64_tr_b16 v[96:97], v28 offset:8192\n" \
  "ds_read_b64_tr_b16 v[98:99], v28 offset:8704\n" \
  "ds_read_b64_tr_b16 v[100:101], v28 offset:40960\n" \
  "ds_read_b64_tr_b16 v[102:103], v28 offset:41472\n" \
  "ds_read_b64_tr_b16 v[104:105], v17 offset:8192\n" \
  "ds_read_b64_tr_b16 v[106:107], v17 offset:8704\n" \
  "ds_read_b64_tr_b16 v[108:109], v17 offset:40960\n" \
  "ds_read_b64_tr_b16 v[110:111], v17 offset:41472\n" \
  "s_waitcnt vmcnt(0)\n" \
  "v_cmp_eq_u32 vcc, 0, v13\n" \
  "s_and_b32 s90, vcc_lo, 15\n" \
  "s_cbranch_scc1 .LCV_GL0_%=\n" \
  "s_mov_b32 m0, s99\n" \
  "s_add_u32 s88, s99, 8192\n" \
  "global_load_lds_dwordx4 v10, s[62:63] sc1\n" \
  "global_load_lds_dwordx4 v10, s[62:63] offset:1024 sc1\n" \
  "s_mov_b32 m0, s88\n" \
  "s_nop 0\n" \
  "global_load_lds_dwordx4 v10, s[64:65] sc1\n" \
  "global_load_lds_dwordx4 v10, s[64:65] offset:1024 sc1\n" \
  ".LCV_GL0_%=:\n" \
  "v_cmp_ne_u32 vcc, s10, v22\n" \
  "v_xor_b32 v31, s11, v23\n" \
  "v_and_b32 v31, 0xffff, v31\n" \
  "v_cmp_ne_u32 s[22:23], 0, v31\n" \
  "s_or_b64 vcc, vcc, s[22:23]\n" \
  "s_cbranch_scc0 .LCV_GL1_%=\n" \
  "s_sleep 12\n" \
  "global_load_dwordx2 v[22:23], v16, s[14:15] sc1\n" \
  "s_sub_u32 s87, s87, 1\n" \
  "s_cbranch_scc1 .LCV_END_%=\n" \
  "s_waitcnt vmcnt(0)\n" \
  "s_branch .LCV_GL0_%=\n" \
  ".LCV_GL1_%=:\n" \
  "v_and_b32 v31, 0xffff0000, v23\n" \
  "s_nop 1\n" \
  "v_max_u32_dpp v31, v31, v31 row_ror:1 row_mask:0xf bank_mask:0xf\n" \
  "s_nop 1\n" \
  "v_max_u32_dpp v31, v31, v31 row_ror:2 row_mask:0xf bank_mask:0xf\n" \
  "s_nop 1\n" \
  "v_max_u32_dpp v31, v31, v31 row_ror:4 row_mask:0xf bank_mask:0xf\n" \
  "s_nop 1\n" \
  "v_max_u32_dpp v31, v31, v31 row_ror:8 row_mask:0xf bank_mask:0xf\n" \
  "v_mov_b32 v12, v31\n" \
  "s_nop 1\n" \
  "v_permlane16_swap_b32 v31, v12\n" \
  "v_max_u32 v31, v31, v12\n" \
  "v_mov_b32 v12, v31\n" \
  "s_nop 1\n" \
  "v_permlane32_swap_b32 v31, v12\n" \
  "v_max_u32 v31, v31, v12\n" \
  "s_mov_b64 exec, 1\n" \
  "ds_write_b32 v30, v31\n" \
  "s_mov_b64 exec, -1\n" \
  "s_waitcnt vmcnt(4) lgkmcnt(0)\n" \
  "s_barrier\n" \
  "ds_write_b128 v3, v[32:35] offset:8192\n" \
  "ds_write_b128 v3, v[36:39] offset:9216\n" \
  "ds_write_b128 v3, v[40:43] offset:10240\n" \
  "ds_write_b128 v3, v[44:47] offset:11264\n" \
  "ds_read_b64_tr_b16 v[112:113], v28 offset:12288\n" \
  "ds_read_b64_tr_b16 v[114:115], v28 offset:12800\n" \
  "ds_read_b64_tr_b16 v[116:117], v28 offset:45056\n" \
  "ds_read_b64_tr_b16 v[118:119], v28 offset:45568\n" \
  "ds_read_b64_tr_b16 v[120:121], v17 offset:12288\n" \
  "ds_read_b64_tr_b16 v[122:123], v17 offset:12800\n" \
  "ds_read_b64_tr_b16 v[124:125], v17 offset:45056\n" \
  "ds_read_b64_tr_b16 v[126:127], v17 offset:45568\n" \
  "ds_read_b32 v31, v30 offset:0\n" \
  "s_waitcnt lgkmcnt(0)\n" \
  "s_barrier\n" \
  "ds_write_b128 v3, v[48:51] offset:12288\n" \
  "ds_write_b128 v3, v[52:55] offset:13312\n" \
  "ds_write_b128 v3, v[56:59] offset:14336\n" \
  "ds_write_b128 v3, v[60:63] offset:15360\n" \
  "v_mul_f32 v31, 0x3f8e353f, v31\n" \
  "v_mul_f32 v30, 0x44870000, v31\n" \
  "v_mov_b64 v[12:13], 0\n" \
  "v_mov_b64 v[14:15], 0\n" \
  "v_mov_b64 v[16:17], 0\n" \
  "v_mov_b64 v[18:19], 0\n" \
  "v_mov_b64 v[20:21], 0\n" \
  "v_mov_b64 v[22:23], 0\n" \
  "v_readfirstlane_b32 s72, v31\n" \
  "v_readfirstlane_b32 s86, v30\n" \
  "s_lshr_b32 s86, s86, 23\n" \
  "s_addk_i32 s86, 0xff83\n" \
  "s_ashr_i32 s86, s86, 1\n" \
  "s_max_i32 s86, s86, -20\n" \
  "s_min_i32 s86, s86, 100\n" \
  "s_mov_b32 s82, s86\n" \
  "s_lshr_b32 s83, s72, 23\n" \
  "s_sub_i32 s83, s83, 243\n" \
  "s_waitcnt lgkmcnt(0)\n" \
  "s_add_u32 s88, s74, 157760\n" \
  "v_mov_b32 v59, s88\n" \
  "s_cmp_lg_u32 s61, 0\n" \
  "s_movk_i32 s76, -5952\n" \
  "s_cmovk_i32 s76, 7104\n" \
  "s_movk_i32 s77, 90\n" \
  "s_cmovk_i32 s77, 90\n" \
  "s_cselect_b32 s72, -1, 57\n" \
  "s_cmp_eq_u32 s61, 0\n" \
  "s_cbranch_scc1 .LCV_W7_%=\n" \
  "s_movk_i32 s87, 100\n" \
  ".LCV_W5_%=:\n" \
  "buffer_load_dword v58, off, s[0:3], 0 offset:896 sc1\n" \
  "s_waitcnt vmcnt(0)\n" \
  "v_readfirstlane_b32 vcc_lo, v58\n" \
  ".LCV_W6_%=:\n" \
  "s_sleep 16\n" \
  "s_sub_u32 s87, s87, 1\n" \
  "s_cbranch_scc1 .LCV_W7_%=\n" \
  "s_cmp_eq_u32 vcc_lo, 0\n" \
  "s_cbranch_scc1 .LCV_W5_%=\n" \
  "s_memrealtime s[38:39]\n" \
  "s_waitcnt lgkmcnt(0)\n" \
  "s_sub_u32 s38, s38, vcc_lo\n" \
  "s_cmp_lt_i32 s38, 800\n" \
  "s_cbranch_scc1 .LCV_W6_%=\n" \
  ".LCV_W7_%=:\n" \
  "s_mov_b32 s87, 0x40000\n" \
  "s_cmp_eq_u32 s90, 0\n" \
  "s_cbranch_scc1 .LCV_W1_%=\n" \
  "s_branch .LCV_W2_%=\n" \
  ".LCV_W0_%=:\n" \
  "s_sleep 2\n" \
  "s_cmp_eq_u32 s61, 0\n" \
  "s_cbranch_scc1 .LCV_W2_%=\n" \
  "s_sleep 8\n" \
  ".LCV_W2_%=:\n" \
  "s_mov_b64 exec, 15\n" \
  "buffer_load_dword v58, v2, s[44:47], s68 offen sc1\n" \
  "s_mov_b64 exec, -1\n" \
  "s_sub_u32 s87, s87, 1\n" \
  "s_cbranch_scc1 .LCV_END_%=\n" \
  "s_waitcnt vmcnt(0)\n" \
  "v_cmp_eq_u32 vcc, 0, v58\n" \
  "s_and_b32 vcc_lo, vcc_lo, 15\n" \
  "s_cbranch_scc1 .LCV_W0_%=\n" \
  "s_mov_b32 m0, s99\n" \
  "s_add_u32 s88, s99, 8192\n" \
  "global_load_lds_dwordx4 v10, s[62:63] sc1\n" \
  "global_load_lds_dwordx4 v10, s[62:63] offset:1024 sc1\n" \
  "s_mov_b32 m0, s88\n" \
  "s_nop 0\n" \
  "global_load_lds_dwordx4 v10, s[64:65] sc1\n" \
  "global_load_lds_dwordx4 v10, s[64:65] offset:1024 sc1\n" \
  ".LCV_W1_%=:\n" \
  "s_add_u32 s62, s62, 0x80000\n" \
  "s_addc_u32 s63, s63, 0\n" \
  "s_add_u32 s64, s64, 0x40000\n" \
  "s_addc_u32 s65, s65, 0\n" \
  "s_add_u32 s68, s68, 0x1000\n" \
  "s_cmp_eq_u32 s61, 0\n" \
  "s_cbranch_scc1 .LCV_W4_%=\n" \
  "s_add_u32 s38, s67, 2944\n" \
  "v_mov_b32 v58, s38\n" \
  "buffer_load_dword v58, v58, s[0:3], 0 offen sc1\n" \
  "s_waitcnt vmcnt(0)\n" \
  "v_readfirstlane_b32 vcc_lo, v58\n" \
  "s_movk_i32 s87, 200\n" \
  "s_cmp_eq_u32 vcc_lo, 0\n" \
  "s_cbranch_scc1 .LCV_W4_%=\n" \
  "s_add_u32 vcc_lo, vcc_lo, 590\n" \
  ".LCV_W3_%=:\n" \
  "s_sleep 2\n" \
  "s_memrealtime s[38:39]\n" \
  "s_sub_u32 s87, s87, 1\n" \
  "s_cbranch_scc1 .LCV_W4_%=\n" \
  "s_waitcnt lgkmcnt(0)\n" \
  "s_sub_u32 s38, s38, vcc_lo\n" \
  "s_cmp_lt_i32 s38, 0\n" \
  "s_cbranch_scc1 .LCV_W3_%=\n" \
  ".LCV_W4_%=:\n" \
  "s_waitcnt vmcnt(0)\n" \
  "s_cmp_eq_u32 s72, -1\n" \
  "s_cselect_b32 s90, 23, 1\n" \
  ".p2align 6\n" \
  ".LCV_TOP_%=:\n" \
  "s_waitcnt lgkmcnt(0)\n" \
  "s_barrier\n" \
  "s_setprio 3\n" \
  "ds_read_b128 v[24:27], v1 offset:0\n" \
  "ds_read_b128 v[28:31], v1 offset:1024\n" \
  "ds_read_b128 v[32:35], v1 offset:2048\n" \
  "ds_read_b128 v[36:39], v1 offset:3072\n" \
  "ds_read_b128 v[40:43], v1 offset:4096\n" \
  "ds_read_b128 v[44:47], v1 offset:5120\n" \
  "ds_read_b128 v[48:51], v1 offset:6144\n" \
  "ds_read_b128 v[52:55], v1 offset:7168\n" \
  "s_memrealtime s[38:39]\n" \
  "s_waitcnt lgkmcnt(7)\n" \
  "v_smfmac_f32_16x16x64_bf16 v[12:15], v[24:27], v[64:71], v0\n" \
  "s_waitcnt lgkmcnt(6)\n" \
  "v_smfmac_f32_16x16x64_bf16 v[12:15], v[28:31], v[72:79], v0\n" \
  "s_waitcnt lgkmcnt(5)\n" \
  "v_smfmac_f32_16x16x64_bf16 v[12:15], v[32:35], v[80:87], v0\n" \
  "s_waitcnt lgkmcnt(4)\n" \
  "v_smfmac_f32_16x16x64_bf16 v[12:15], v[36:39], v[88:95], v0\n" \
  "s_waitcnt lgkmcnt(3)\n" \
  "v_smfmac_f32_16x16x64_bf16 v[12:15], v[40:43], v[96:103], v0\n" \
  "s_waitcnt lgkmcnt(2)\n" \
  "v_smfmac_f32_16x16x64_bf16 v[12:15], v[44:47], v[104:111], v0\n" \
  "s_waitcnt lgkmcnt(1)\n" \
  "v_smfmac_f32_16x16x64_bf16 v[12:15], v[48:51], v[112:119], v0\n" \
  "s_waitcnt lgkmcnt(0)\n" \
  "v_smfmac_f32_16x16x64_bf16 v[12:15], v[52:55], v[120:127], v0\n" \
  "ds_read_b32 v11, v1 offset:26688\n" \
  "s_mov_b32 s61, s38\n" \
  "s_add_u32 s38, s38, s79\n" \
  "s_add_u32 s38, s38, s96\n" \
  "s_mov_b32 s96, 0\n" \
  "v_or_b32 v57, 1, s38\n" \
  "v_add_u32 v56, s67, v10\n" \
  "s_add_u32 s39, s67, s76\n" \
  "s_add_u32 s38, s38, s77\n" \
  "s_bitset1_b32 s38, 0\n" \
  "v_writelane_b32 v56, s39, 1\n" \
  "v_writelane_b32 v57, s38, 1\n" \
  "s_sub_u32 s39, s71, s72\n" \
  "s_cmp_lt_u32 s39, 7\n" \
  "s_cselect_b64 exec, 19, 3\n" \
  "buffer_store_dword v57, v56, s[0:3], 0 offen sc1\n" \
  "s_mov_b64 exec, -1\n" \
  "v_permlane32_swap_b32 v12, v14\n" \
  "v_permlane32_swap_b32 v13, v15\n" \
  "v_pk_add_f32 v[12:13], v[12:13], v[14:15]\n" \
  "s_nop 0\n" \
  "v_cvt_pk_bf16_f32 v12, v12, v13\n" \
  "ds_write_b32 v4, v12\n" \
  "s_setprio 2\n" \
  "v_mov_b64 v[12:13], s[92:93]\n" \
  "v_mov_b64 v[14:15], s[92:93]\n" \
  "s_waitcnt lgkmcnt(1)\n" \
  "v_readfirstlane_b32 s86, v11\n" \
  "s_lshr_b32 s86, s86, 23\n" \
  "s_add_i32 s86, s86, s83\n" \
  "s_ashr_i32 s86, s86, 1\n" \
  "s_max_i32 s86, s86, s82\n" \
  "s_min_i32 s86, s86, 100\n" \
  "s_lshl_b32 s86, s86, 23\n" \
  "s_sub_i32 s84, 0xc8800000, s86\n" \
  "s_cmp_eq_u32 s73, 5\n" \
  "v_smfmac_f32_16x16x64_bf16 v[16:19], v[24:27], v[128:135], v0\n" \
  "v_smfmac_f32_16x16x64_bf16 v[16:19], v[28:31], v[136:143], v0\n" \
  "v_smfmac_f32_16x16x64_bf16 v[16:19], v[32:35], v[144:151], v0\n" \
  "v_smfmac_f32_16x16x64_bf16 v[16:19], v[36:39], v[152:159], v0\n" \
  "v_smfmac_f32_16x16x64_bf16 v[16:19], v[40:43], v[160:167], v0\n" \
  "v_smfmac_f32_16x16x64_bf16 v[16:19], v[44:47], v[168:175], v0\n" \
  "s_waitcnt lgkmcnt(0)\n" \
  "s_barrier\n" \
  "ds_read_b64_tr_b16 v[60:61], v6 offset:0\n" \
  "ds_read_b64_tr_b16 v[62:63], v9 offset:0\n" \
  "v_smfmac_f32_16x16x64_bf16 v[16:19], v[48:51], v[176:183], v0\n" \
  "v_smfmac_f32_16x16x64_bf16 v[16:19], v[52:55], v[184:191], v0\n" \
  "v_smfmac_f32_16x16x64_bf16 v[20:23], v[24:27], v[192:199], v0\n" \
  "v_smfmac_f32_16x16x64_bf16 v[20:23], v[28:31], v[200:207], v0\n" \
  "v_smfmac_f32_16x16x64_bf16 v[20:23], v[32:35], v[208:215], v0\n" \
  "v_smfmac_f32_16x16x64_bf16 v[20:23], v[36:39], v[216:223], v0\n" \
  "v_smfmac_f32_16x16x64_bf16 v[20:23], v[40:43], v[224:231], v0\n" \
  "v_smfmac_f32_16x16x64_bf16 v[20:23], v[44:47], v[232:239], v0\n" \
  "v_smfmac_f32_16x16x64_bf16 v[20:23], v[48:51], v[240:247], v0\n" \
  "v_smfmac_f32_16x16x64_bf16 v[20:23], v[52:55], v[248:255], v0\n" \
  "s_cmp_eq_u32 s71, 0\n" \
  "v_permlane32_swap_b32 v16, v18\n" \
  "v_permlane32_swap_b32 v17, v19\n" \
  "v_add_f32 v16, v16, v18\n" \
  "v_add_f32 v17, v17, v19\n" \
  "v_cvt_pk_bf16_f32 v16, v16, v17\n" \
  "s_waitcnt lgkmcnt(0)\n" \
  "v_max_u32 v60, 1, v60\n" \
  "v_max_u32 v61, 1, v61\n" \
  "v_max_u32 v62, 1, v62\n" \
  "v_max_u32 v63, 1, v63\n" \
  "buffer_store_dwordx4 v[60:63], v7, s[48:51], s69 offen sc1\n" \
  "v_permlane32_swap_b32 v20, v22\n" \
  "v_permlane32_swap_b32 v21, v23\n" \
  "v_pk_add_f32 v[20:21], v[20:21], v[22:23]\n" \
  "v_max_u32 v18, 1, v16\n" \
  "v_cvt_pk_bf16_f32 v17, v20, v21\n" \
  "ds_write_b64 v5, v[16:17]\n" \
  "v_max_u32 v19, 1, v17\n" \
  "s_nop 1\n" \
  "v_permlane32_swap_b32 v18, v19\n" \
  "buffer_store_dwordx2 v[18:19], v8, s[52:55], s70 offen sc1\n" \
  "ds_read_b128 v[24:27], v3 offset:0\n" \
  "ds_read_b128 v[28:31], v3 offset:1024\n" \
  "ds_read_b128 v[32:35], v3 offset:2048\n" \
  "ds_read_b128 v[36:39], v3 offset:3072\n" \
  "ds_read_b128 v[40:43], v3 offset:8192\n" \
  "ds_read_b128 v[44:47], v3 offset:9216\n" \
  "ds_read_b128 v[48:51], v3 offset:10240\n" \
  "ds_read_b128 v[52:55], v3 offset:11264\n" \
  "s_cbranch_scc1 .LCV_LAST_%=\n" \
  "s_cmp_eq_u32 s73, 5\n" \
  "s_cbranch_scc0 .LCV_NMK_%=\n" \
  "s_mov_b32 m0, s78\n" \
  "s_nop 0\n" \
  "buffer_load_dword v2, s[0:3], s66 offen sc1 lds\n" \
  ".LCV_NMK_%=:\n" \
  "s_setprio 0\n" \
  "s_waitcnt lgkmcnt(8)\n" \
  "s_barrier\n" \
  "s_add_u32 s67, s67, 1024\n" \
  "v_mov_b32 v11, 0\n" \
  "ds_write_b32 v59, v11\n" \
  "ds_read_b128 v[56:59], v3 offset:4096\n" \
  "ds_read_b128 v[60:63], v3 offset:5120\n" \
  "v_mov_b64 v[20:21], s[92:93]\n" \
  "v_mov_b64 v[22:23], s[92:93]\n" \
  "v_mov_b64 v[16:17], s[92:93]\n" \
  "v_mov_b64 v[18:19], s[92:93]\n" \
  "s_mov_b32 m0, s94\n" \
  "s_cmp_eq_u32 s72, -1\n" \
  "s_cselect_b64 exec, 0, 15\n" \
  "buffer_load_dword v2, s[44:47], s68 offen sc1 lds\n" \
  "s_mov_b64 exec, -1\n" \
  "s_setprio 2\n" \
  "s_waitcnt lgkmcnt(0)\n" \
  "s_barrier\n" \
  "ds_read_b128 v[4:7], v1 offset:8192\n" \
  "ds_read_b128 v[8:11], v1 offset:9216\n" \
  "s_waitcnt lgkmcnt(1)\n" \
  "v_smfmac_f32_16x16x64_bf16 v[12:15], v[4:7], v[24:31], v0\n" \
  "ds_read_b128 v[24:27], v3 offset:6144\n" \
  "ds_read_b128 v[28:31], v3 offset:7168\n" \
  "v_smfmac_f32_16x16x64_bf16 v[16:19], v[4:7], v[32:39], v0\n" \
  "ds_read_b128 v[32:35], v3 offset:12288\n" \
  "ds_read_b128 v[36:39], v3 offset:13312\n" \
  "s_waitcnt lgkmcnt(4)\n" \
  "v_smfmac_f32_16x16x64_bf16 v[12:15], v[8:11], v[40:47], v0\n" \
  "v_smfmac_f32_16x16x64_bf16 v[16:19], v[8:11], v[48:55], v0\n" \
  "v_smfmac_f32_16x16x64_bf16 v[20:23], v[4:7], v[56:63], v0\n" \
  "ds_read_b128 v[56:59], v3 offset:14336\n" \
  "ds_read_b128 v[60:63], v3 offset:15360\n" \
  "v_mov_b64 v[40:41], s[92:93]\n" \
  "v_mov_b64 v[42:43], s[92:93]\n" \
  "s_nop 2\n" \
  "v_permlane32_swap_b32 v12, v17\n" \
  "v_permlane32_swap_b32 v13, v16\n" \
  "v_permlane32_swap_b32 v14, v19\n" \
  "v_permlane32_swap_b32 v15, v18\n" \
  "v_pk_add_f32 v[12:13], v[12:13], v[16:17] op_sel:[0,1] op_sel_hi:[1,0]\n" \
  "v_pk_add_f32 v[14:15], v[14:15], v[18:19] op_sel:[0,1] op_sel_hi:[1,0]\n" \
  "buffer_atomic_add_x2 v[12:13], v2, s[40:43], s56 offen\n" \
  "buffer_atomic_add_x2 v[14:15], v2, s[40:43], s57 offen\n" \
  "s_waitcnt lgkmcnt(2)\n" \
  "v_smfmac_f32_16x16x64_bf16 v[20:23], v[8:11], v[32:39], v0\n" \
  "v_smfmac_f32_16x16x64_bf16 v[40:43], v[4:7], v[24:31], v0\n" \
  "s_waitcnt lgkmcnt(0)\n" \
  "v_smfmac_f32_16x16x64_bf16 v[40:43], v[8:11], v[56:63], v0\n" \
  "s_nop 7\n" \
  "v_permlane32_swap_b32 v20, v41\n" \
  "v_permlane32_swap_b32 v21, v40\n" \
  "v_permlane32_swap_b32 v22, v43\n" \
  "v_permlane32_swap_b32 v23, v42\n" \
  "v_pk_add_f32 v[20:21], v[20:21], v[40:41] op_sel:[0,1] op_sel_hi:[1,0]\n" \
  "v_pk_add_f32 v[22:23], v[22:23], v[42:43] op_sel:[0,1] op_sel_hi:[1,0]\n" \
  "buffer_atomic_add_x2 v[20:21], v2, s[40:43], s58 offen\n" \
  "buffer_atomic_add_x2 v[22:23], v2, s[40:43], s59 offen\n" \
  "s_setprio 0\n" \
  "s_memrealtime s[38:39]\n" \
  "v_mbcnt_lo_u32_b32 v48, -1, 0\n" \
  "v_mbcnt_hi_u32_b32 v48, -1, v48\n" \
  "s_add_u32 s88, s74, 157760\n" \
  "v_mov_b32 v59, s88\n" \
  "v_lshl_add_u32 v52, v48, 2, v59\n" \
  "ds_read_b32 v52, v52 offset:192\n" \
  "v_lshrrev_b32 v49, 3, v48\n" \
  "v_and_b32 v50, 7, v48\n" \
  "v_lshlrev_b32 v50, 4, v50\n" \
  "v_lshl_or_b32 v44, v49, 8, v50\n" \
  "s_mov_b32 s87, 0x40000\n" \
  "v_lshlrev_b32 v10, 4, v48\n" \
  "s_cmp_eq_u32 s72, -1\n" \
  "s_cbranch_scc1 .LCV_GOT_%=\n" \
  "v_lshl_add_u32 v58, v48, 2, s94\n" \
  "s_waitcnt vmcnt(4)\n" \
  "ds_read_b32 v58, v58\n" \
  "s_waitcnt lgkmcnt(0)\n" \
  "v_cmp_eq_u32 vcc, 0, v58\n" \
  "s_and_b32 vcc_lo, vcc_lo, 15\n" \
  "s_cbranch_scc0 .LCV_GOT_%=\n" \
  "s_branch .LCV_ASK_%=\n" \
  ".LCV_LATE_%=:\n" \
  "v_cmp_eq_u32 vcc, 0, v58\n" \
  "s_and_b32 vcc_lo, vcc_lo, 15\n" \
  "s_cbranch_scc0 .LCV_GOT_%=\n" \
  "s_sleep 12\n" \
  ".LCV_ASK_%=:\n" \
  "s_mov_b64 exec, 15\n" \
  "buffer_load_dword v58, v2, s[44:47], s68 offen sc1\n" \
  "s_mov_b64 exec, -1\n" \
  "s_sub_u32 s87, s87, 1\n" \
  "s_cbranch_scc1 .LCV_END_%=\n" \
  "s_waitcnt vmcnt(0)\n" \
  "s_branch .LCV_LATE_%=\n" \
  ".LCV_GOT_%=:\n" \
  "s_mov_b32 m0, s99\n" \
  "s_add_u32 s88, s99, 8192\n" \
  "global_load_lds_dwordx4 v10, s[62:63] sc1\n" \
  "global_load_lds_dwordx4 v10, s[62:63] offset:1024 sc1\n" \
  "s_mov_b32 m0, s88\n" \
  "s_nop 0\n" \
  "global_load_lds_dwordx4 v10, s[64:65] sc1\n" \
  "global_load_lds_dwordx4 v10, s[64:65] offset:1024 sc1\n" \
  ".LCV_HAVE_%=:\n" \
  "s_waitcnt lgkmcnt(0)\n" \
  "s_sub_u32 s61, s38, s61\n" \
  "s_add_u32 s79, s79, 2\n" \
  "s_min_u32 s79, s79, s61\n" \
  "s_sub_u32 s39, s38, 150\n" \
  "v_subrev_u32 v52, s39, v52\n" \
  "v_cmp_lt_u32 vcc, 0x1000, v52\n" \
  "s_cmp_eq_u64 vcc, 0\n" \
  "s_cbranch_scc1 .LCV_OKM_%=\n" \
  ".LCV_AGN_%=:\n" \
  "buffer_load_dword v52, v2, s[0:3], s66 offen sc1\n" \
  "s_sub_u32 s87, s87, 1\n" \
  "s_cbranch_scc1 .LCV_END_%=\n" \
  "s_waitcnt vmcnt(0)\n" \
  "v_cmp_eq_u32 vcc, 0, v52\n" \
  "v_subrev_u32 v52, s39, v52\n" \
  "s_cmp_eq_u64 vcc, 0\n" \
  "s_cbranch_scc1 .LCV_OKM_%=\n" \
  "s_sleep 4\n" \
  "s_branch .LCV_AGN_%=\n" \
  ".LCV_OKM_%=:\n" \
  "s_nop 1\n" \
  "v_max_i32_dpp v52, v52, v52 row_ror:1 row_mask:0xf bank_mask:0xf\n" \
  "s_nop 1\n" \
  "v_max_i32_dpp v52, v52, v52 row_ror:2 row_mask:0xf bank_mask:0xf\n" \
  "s_nop 1\n" \
  "v_max_i32_dpp v52, v52, v52 row_ror:4 row_mask:0xf bank_mask:0xf\n" \
  "s_nop 1\n" \
  "v_max_i32_dpp v52, v52, v52 row_ror:8 row_mask:0xf bank_mask:0xf\n" \
  "s_movk_i32 vcc_lo, 150\n" \
  "v_readlane_b32 s88, v52, 0\n" \
  "s_max_i32 vcc_lo, vcc_lo, s88\n" \
  "v_readlane_b32 s88, v52, 16\n" \
  "s_max_i32 vcc_lo, vcc_lo, s88\n" \
  "v_readlane_b32 s88, v52, 32\n" \
  "s_max_i32 vcc_lo, vcc_lo, s88\n" \
  "v_readlane_b32 s88, v52, 48\n" \
  "s_max_i32 vcc_lo, vcc_lo, s88\n" \
  "s_add_u32 s66, s66, 1024\n" \
  "s_add_u32 vcc_lo, vcc_lo, s39\n" \
  "s_add_u32 vcc_lo, vcc_lo, 23\n" \
  ".LCV_TW_%=:\n" \
  "s_memrealtime s[38:39]\n" \
  "s_waitcnt lgkmcnt(0)\n" \
  "s_sub_u32 s38, s38, vcc_lo\n" \
  "s_cmp_lt_i32 s38, 0\n" \
  "s_cbranch_scc1 .LCV_TW_%=\n" \
  "buffer_load_dwordx4 v[24:27], v44, s[40:43], s60 offen sc1\n" \
  "buffer_load_dwordx4 v[28:31], v44, s[40:43], s60 offen offset:2048 sc1\n" \
  "s_add_u32 s69, s69, 0x10000\n" \
  "s_add_u32 s70, s70, 0x10000\n" \
  "s_sub_u32 s88, s73, 4\n" \
  "v_lshrrev_b32 v49, 2, v48\n" \
  "v_lshrrev_b32 v50, 4, v48\n" \
  "v_bitop3_b32 v50, v49, v50, 3 bitop3:0x28\n" \
  "v_lshl_add_u32 v4, v50, 3, s97\n" \
  "v_and_b32 v50, 3, v48\n" \
  "v_lshl_add_u32 v4, v50, 5, v4\n" \
  "v_and_b32 v49, 1, v49\n" \
  "v_lshl_add_u32 v4, v49, 8, v4\n" \
  "v_and_b32 v49, 8, v48\n" \
  "v_lshl_add_u32 v4, v49, 4, v4\n" \
  "v_and_b32 v51, 15, v48\n" \
  "v_bfe_u32 v49, v48, 4, 1\n" \
  "v_lshrrev_b32 v50, 5, v48\n" \
  "s_lshl_b32 s89, s88, 10\n" \
  "s_add_u32 s89, s89, s74\n" \
  "s_add_u32 s89, s89, 158720\n" \
  "v_lshrrev_b32 v9, 3, v51\n" \
  "v_mul_u32_u24 v6, 41, v9\n" \
  "v_xor_b32 v6, v6, v51\n" \
  "v_lshl_add_u32 v6, v6, 3, s89\n" \
  "v_lshl_add_u32 v6, v49, 6, v6\n" \
  "v_lshl_add_u32 v6, v50, 9, v6\n" \
  "v_xor_b32 v9, 0x90, v6\n" \
  "v_lshlrev_b32 v8, 4, v49\n" \
  "v_lshl_add_u32 v8, v51, 5, v8\n" \
  "v_lshl_add_u32 v8, v50, 3, v8\n" \
  "s_lshl_b32 s89, s88, 8\n" \
  "s_add_u32 s89, s89, s74\n" \
  "s_add_u32 s89, s89, 139264\n" \
  "v_and_b32 v49, 31, v48\n" \
  "v_lshl_add_u32 v5, v49, 3, s89\n" \
  "v_lshl_add_u32 v5, v50, 10, v5\n" \
  "v_lshrrev_b32 v49, 2, v51\n" \
  "v_and_b32 v50, 5, v51\n" \
  "v_and_or_b32 v49, v49, 2, v50\n" \
  "v_and_b32 v50, 48, v48\n" \
  "v_lshlrev_b32 v7, 3, v50\n" \
  "v_lshl_add_u32 v7, v49, 4, v7\n" \
  "v_bitop3_b32 v49, v51, 2, 2 bitop3:0x0c\n" \
  "v_lshl_add_u32 v7, v49, 29, v7\n" \
  "v_mov_b64 v[12:13], 0\n" \
  "v_mov_b64 v[14:15], 0\n" \
  "v_mov_b64 v[16:17], 0\n" \
  "v_mov_b64 v[18:19], 0\n" \
  "v_mov_b64 v[20:21], 0\n" \
  "v_mov_b64 v[22:23], 0\n" \
  "v_and_b32 v50, 3, v48\n" \
  "v_lshl_add_u32 v40, v50, 2, s98\n" \
  "v_lshrrev_b32 v50, 4, v48\n" \
  "v_mad_u32_u24 v40, v50, s95, v40\n" \
  "v_bfe_u32 v49, v48, 3, 1\n" \
  "v_lshl_add_u32 v40, v49, 6, v40\n" \
  "v_bfe_u32 v49, v48, 2, 1\n" \
  "v_lshl_add_u32 v40, v49, 7, v40\n" \
  "v_xor_b32 v41, 16, v40\n" \
  "v_xor_b32 v42, 32, v40\n" \
  "v_xor_b32 v43, 48, v40\n" \
  "s_add_i32 s80, s86, 0x35000000\n" \
  "v_mov_b32 v11, 0\n" \
  "s_mov_b32 s89, s60\n" \
  "s_bitcmp1_b32 s60, 7\n" \
  "s_cselect_b32 s88, 0x10000, 0\n" \
  "s_bitset1_b32 s88, 7\n" \
  "s_xor_b32 s56, s56, s88\n" \
  "s_xor_b32 s57, s57, s88\n" \
  "s_xor_b32 s58, s58, s88\n" \
  "s_xor_b32 s59, s59, s88\n" \
  "s_xor_b32 s60, s60, s88\n" \
  "s_sub_u32 s71, s71, 1\n" \
  "s_waitcnt vmcnt(2)\n" \
  "v_add_u32 v53, s99, v10\n" \
  "ds_read_b32 v54, v53 offset:0\n" \
  "ds_read_b32 v55, v53 offset:1024\n" \
  "ds_read_b32 v56, v53 offset:8192\n" \
  "ds_read_b32 v57, v53 offset:9216\n" \
  "s_lshr_b32 s88, s71, 4\n" \
  "s_waitcnt lgkmcnt(0)\n" \
  "v_bitop3_b32 v54, v54, v55, s88 bitop3:0x7e\n" \
  "v_bitop3_b32 v56, v56, v57, s88 bitop3:0x7e\n" \
  "v_bitop3_b32 v54, v54, v56, 1 bitop3:0xa8\n" \
  "v_cmp_ne_u32 vcc, 0, v54\n" \
  "s_cmp_eq_u64 vcc, 0\n" \
  "s_cbranch_scc0 .LCV_MISS_%=\n" \
  ".LCV_M3_%=:\n" \
  "s_add_u32 s62, s62, 0x80000\n" \
  "s_addc_u32 s63, s63, 0\n" \
  "s_add_u32 s64, s64, 0x40000\n" \
  "s_addc_u32 s65, s65, 0\n" \
  "s_bitset0_b32 s62, s90\n" \
  "s_sub_u32 s88, s90, 1\n" \
  "s_bitset0_b32 s64, s88\n" \
  "s_add_u32 s68, s68, 0x1000\n" \
  ".LCV_SEE_%=:\n" \
  "s_waitcnt vmcnt(1)\n" \
  "v_cvt_f32_i32 v24, v24\n" \
  "v_cvt_f32_i32 v25, v25\n" \
  "v_cvt_f32_i32 v26, v26\n" \
  "v_cvt_f32_i32 v27, v27\n" \
  "v_max_f32 v45, |v24|, |v26|\n" \
  "v_pk_mul_f32 v[24:25], v[24:25], s[80:81] op_sel_hi:[1,0]\n" \
  "v_pk_mul_f32 v[26:27], v[26:27], s[80:81] op_sel_hi:[1,0]\n" \
  "s_nop 0\n" \
  "v_cvt_pk_bf16_f32 v32, v24, v26\n" \
  "v_cvt_pk_bf16_f32 v33, v25, v27\n" \
  "ds_write_b32 v40, v32\n" \
  "ds_write_b32 v41, v33\n" \
  "s_waitcnt vmcnt(0)\n" \
  "v_cvt_f32_i32 v28, v28\n" \
  "v_cvt_f32_i32 v30, v30\n" \
  "v_cvt_f32_i32 v29, v29\n" \
  "v_max3_f32 v45, v45, |v28|, |v30|\n" \
  "v_cvt_f32_i32 v31, v31\n" \
  "v_cmp_lt_f32 vcc, s91, v45\n" \
  "v_pk_mul_f32 v[28:29], v[28:29], s[80:81] op_sel_hi:[1,0]\n" \
  "v_pk_mul_f32 v[30:31], v[30:31], s[80:81] op_sel_hi:[1,0]\n" \
  "s_cmp_eq_u64 vcc, 0\n" \
  "s_cbranch_scc0 .LCV_POLL_%=\n" \
  "v_cvt_pk_bf16_f32 v36, v28, v30\n" \
  "v_cvt_pk_bf16_f32 v37, v29, v31\n" \
  "ds_write_b32 v42, v36\n" \
  "ds_write_b32 v43, v37\n" \
  "v_dot2c_f32_bf16 v11, v32, v32\n" \
  "v_dot2c_f32_bf16 v11, v33, v33\n" \
  "v_dot2c_f32_bf16 v11, v36, v36\n" \
  "v_dot2c_f32_bf16 v11, v37, v37\n" \
  "s_nop 1\n" \
  "s_nop 0\n" \
  "v_cmp_lt_f32 vcc, 4.0, v11\n" \
  "s_cbranch_vccz .LCV_TOP_%=\n" \
  "s_mov_b64 exec, vcc\n" \
  "ds_max_u32 v59, v11\n" \
  "s_mov_b64 exec, -1\n" \
  "s_branch .LCV_TOP_%=\n" \
  ".LCV_POLL_%=:\n" \
  "buffer_load_dwordx4 v[24:27], v44, s[40:43], s89 offen sc1\n" \
  "buffer_load_dwordx4 v[28:31], v44, s[40:43], s89 offen offset:2048 sc1\n" \
  "s_sub_u32 s87, s87, 1\n" \
  "s_cbranch_scc1 .LCV_END_%=\n" \
  "s_branch .LCV_SEE_%=\n" \
  ".LCV_MISS_%=:\n" \
  "s_mov_b32 m0, s99\n" \
  "s_add_u32 s88, s99, 8192\n" \
  "global_load_lds_dwordx4 v10, s[62:63] sc1\n" \
  "global_load_lds_dwordx4 v10, s[62:63] offset:1024 sc1\n" \
  "s_mov_b32 m0, s88\n" \
  "s_nop 0\n" \
  "global_load_lds_dwordx4 v10, s[64:65] sc1\n" \
  "global_load_lds_dwordx4 v10, s[64:65] offset:1024 sc1\n" \
  "s_waitcnt vmcnt(0)\n" \
  "v_add_u32 v53, s99, v10\n" \
  "ds_read_b32 v54, v53 offset:0\n" \
  "ds_read_b32 v55, v53 offset:1024\n" \
  "ds_read_b32 v56, v53 offset:8192\n" \
  "ds_read_b32 v57, v53 offset:9216\n" \
  "s_lshr_b32 s88, s71, 4\n" \
  "s_waitcnt lgkmcnt(0)\n" \
  "v_bitop3_b32 v54, v54, v55, s88 bitop3:0x7e\n" \
  "v_bitop3_b32 v56, v56, v57, s88 bitop3:0x7e\n" \
  "v_bitop3_b32 v54, v54, v56, 1 bitop3:0xa8\n" \
  "v_cmp_ne_u32 vcc, 0, v54\n" \
  "s_cmp_eq_u64 vcc, 0\n" \
  "s_cbranch_scc1 .LCV_M3_%=\n" \
  ".LCV_M1_%=:\n" \
  "s_mov_b64 exec, 15\n" \
  "buffer_load_dword v58, v2, s[44:47], s68 offen sc1\n" \
  "s_mov_b64 exec, -1\n" \
  "s_sub_u32 s87, s87, 1\n" \
  "s_cbranch_scc1 .LCV_END_%=\n" \
  "s_waitcnt vmcnt(0)\n" \
  "v_cmp_eq_u32 vcc, 0, v58\n" \
  "s_and_b32 vcc_lo, vcc_lo, 15\n" \
  "s_cbranch_scc0 .LCV_M2_%=\n" \
  "s_sleep 8\n" \
  "s_branch .LCV_M1_%=\n" \
  ".LCV_M2_%=:\n" \
  "s_mov_b32 m0, s99\n" \
  "s_add_u32 s88, s99, 8192\n" \
  "global_load_lds_dwordx4 v10, s[62:63] sc1\n" \
  "global_load_lds_dwordx4 v10, s[62:63] offset:1024 sc1\n" \
  "s_mov_b32 m0, s88\n" \
  "s_nop 0\n" \
  "global_load_lds_dwordx4 v10, s[64:65] sc1\n" \
  "global_load_lds_dwordx4 v10, s[64:65] offset:1024 sc1\n" \
  "s_waitcnt vmcnt(0)\n" \
  "s_branch .LCV_M3_%=\n" \
  ".LCV_LAST_%=:\n" \
  "s_waitcnt vmcnt(0)\n" \
  ".LCV_END_%=:\n" \
  "s_endpgm\n"
#define TOK_AL(p, o, m) ((((unsigned)(size_t)(p) + (unsigned)(o) + (m)) & ~(m)) - (unsigned)(size_t)(p))
#define TOK_QK(p, L) (GEN_TR * (L) ? TOK_AL(p, OFF_QK + SZ_QK, (0x100000u << GEN_TR) - 1) : (unsigned)(OFF_QK + (L) * SZ_QK))
#define TOK_V(p, L) (GEN_TR * (L) ? TOK_AL(p, OFF_VF + SZ_O, (0x80000u << GEN_TR) - 1) : (unsigned)(OFF_VF + (L) * SZ_O))
__device__ __forceinline__ void start_kgen(const MegaArgs& A, char* smem, const int kg) {
  const int w = A.wv, l = lane_id(), tid = 64 * w + l, c = l & 15, g = l >> 4;
  KGen G;
  unsigned tp = 0u;
#define TEXT_ASK()                                                                                                     \
  {                                                                                                                    \
    const char* pc;                                                                                                    \
    unsigned n;                                                                                                        \
    asm volatile("s_getpc_b64 %0\n.LKG_PC:\n\ts_mov_b32 %1, .LKG_LAST-.LKG_PC" : "=s"(pc), "=s"(n));                    \
    const unsigned o = (unsigned)(tid * 8 + ((blockIdx.x >> 3) & 7)) * IPS;                                            \
    if ((int)o < (int)n) asm volatile("global_load_dword %0, %1, %2" : "=v"(tp) : "v"(o), "s"(pc) : "memory");         \
  }
  if (IPF == 2) TEXT_ASK()
  v4u raw[2] = {};
#pragma unroll
  for (int i = 0; i < TP; ++i)
    if (kg < 32) raw[i] = *reinterpret_cast<const v4u*>(A.x0 + (size_t)(TP * (8 * (kg & 7) + w) + i) * DM + l * 8);
  G.load(A, 8 * kg + w, l);
  {
    const __amdgpu_buffer_rsrc_t rz = MKRS(A.ws + OFF_Z, Z_BYTES);
#pragma unroll
    for (int i = 0; i < 4; ++i) {
      const u64 z = i == 0 && kg < (int)(2 * SZ_ACC / 8192) ? ACC_INIT : 0ull;
      ST128((v4u{(unsigned)z, (unsigned)(z >> 32), (unsigned)z, (unsigned)(z >> 32)}), rz, tid * 16, (i * 64 + kg) * 8192);
    }
  }
  const u64 tag = call_tag(A);
  if (TP && kg < 32) {
    unsigned t = row_stats(raw[0]);
    if (TP > 1) {
      const unsigned t1 = row_stats(raw[1]);
      t = l < 2 ? t : t1;
    }
    if (l < 2 * TP) __hip_atomic_store((gu64*)(A.ws + OFF_GT) + 2 * (L_SEQ + TP * (8 * kg + w)) + l, tagged(tag, (l & 1) ? t : t << 16), RLX_AGENT);
  }
  DRAIN();
  __syncthreads();
  if (tid < 8) __hip_atomic_store((gu64*)(A.ws + OFF_RD) + tid * 64 + kg, tag, RLX_AGENT);
  asm volatile("" : "+v"(G.pa), "+v"(G.pb), "+v"(G.pe));
  G.setup();
  G.st = smem + w * 16640;
  const __amdgpu_buffer_rsrc_t rs = MKRS(A.ws + OFF_KF + (8 * kg + w) * 2048, 386 * 4);
  SST(1);
  unsigned tw = 0u;
#define TLB_ASK()                                                                                                      \
  if (tid < 384) {                                                                                                     \
    const unsigned o = (unsigned)OFF_XA + (unsigned)(tid * 8 + ((blockIdx.x >> 3) & 7)) * 65536u;                      \
    if (o < WS_TOTAL) asm volatile("global_load_dword %0, %1, %2" : "=v"(tw) : "v"(o), "s"(A.ws) : "memory");          \
  }
#define X2_CLEAR() \
  _Pragma("unroll") for (int i = 0; i < 4; ++i) ST128((v4u{0u, 0u, 0u, 0u}), MKRS(A.ws + OFF_X2, SZ_X), tid * 16, (4 * kg + i) * 8192);
  if (XZP == 1) { X2_CLEAR() }
  if (IPF == 1) TEXT_ASK()
  if (TLBW == 1) TLB_ASK()
  unsigned pf[6] = {};
#define PF_ASK(M)                                                                                                                  \
  if (kg < 32) {                                                                                                                   \
    if ((M) & 1) asm volatile("global_load_dword %0, %2, %3\n\tglobal_load_dword %1, %2, %3 offset:128" : "=&v"(pf[0]), "=&v"(pf[1]) : "v"(kg * 131072 + tid * 256), "s"(A.Wo)); \
    if ((M) & 2) asm volatile("global_load_dword %0, %2, %3\n\tglobal_load_dword %1, %2, %3 offset:128" : "=&v"(pf[2]), "=&v"(pf[3]) : "v"(kg * 256 + tid * 8192), "s"(A.Wq)); \
    if ((M) & 4) asm volatile("global_load_dword %0, %2, %3\n\tglobal_load_dword %1, %2, %3 offset:128" : "=&v"(pf[4]), "=&v"(pf[5]) : "v"(kg * 256 + tid * 8192), "s"(A.Wk)); \
  }
  PF_ASK(PFA)
  G.columns(l, 0, 16);
  G.get(G.bh, G.bl, l);
  if (XZP == 2) { X2_CLEAR() }
  PF_ASK(PFB)
  if (TLBW == 2) TLB_ASK()
  (void)G.scale();
  const Cx zb = G.power(256.f);
  Cx b = G.C, z = G.power(2.f);
#pragma unroll 1
  for (int f = 1; f < 9;) {
    char* p = G.st + l * 8;
    const int fe = f == 1 ? 4 : 9;
#pragma unroll 1
    for (; f < fe; ++f) {
      b = cmul(b, zb);
      if (f == 2 || f == 4) z = cmul(z, z);
      Cx y = b;
      p = G.rows(p, y, z, f == 1 ? 8 : f < 4 ? 4 : 2);
    }
    const f32x4 d = G.tile<false>(l);
    const v4u r = {pkh(d.x, d.x), pkh(d.y, d.y), pkh(d.z, d.z), pkh(d.w, d.w)};
    if (f == 4) {
      ST128(r, rs, (g < 2 ? 8 * c + 4 * g : 64 * g + 4 * c) * 4, 0);
    } else {
      ST64((v2u{r.x, r.y}), rs, (256 + 64 * g + 2 * c) * 4, 0);
      ST64((v2u{r.z, r.w}), rs, (288 + 64 * g + 2 * c) * 4, 0);
    }
  }
  SST(2);
  DRAIN();
  asm volatile("" ::"v"(tp), "v"(tw), "v"(pf[0]), "v"(pf[1]), "v"(pf[2]), "v"(pf[3]), "v"(pf[4]), "v"(pf[5]));
  if (l == 0) __hip_atomic_store((gu64*)(A.ws + OFF_KG) + kg * 16 + w, tag, RLX_AGENT);
  if (XZP == 3) { X2_CLEAR() }
  const u64 rv = (XZP & 3) && w == 0 ? ready_look(A) : 0;
  if (TLBW == 3) {
    TLB_ASK()
    DRAIN();
    asm volatile("" ::"v"(tw));
  }
  DRAIN();
  __syncthreads();
  if ((XZP & 3) && w == 0) {
    ready_wait(A, tag, rv);
    if (l < 8) set_flag((unsigned*)(A.ws + OFF_FL) + FL_FZ + l * 64 + kg);
  }
  SST(3);
}

constexpr int C_SM = 131072, C_SO = C_SM + 8192, C_KT = C_SO + 2048, C_X = C_KT + 16384, C_SQ = C_X + 1024;
static_assert(C_SQ + 4096 <= 163840 && C_SQ % 512 == 0, "lds");
__device__ __forceinline__ v4u mk_srd(const void* p, const unsigned bytes) {
  const unsigned long long a = (unsigned long long)p;
  return v4u{(unsigned)a, (unsigned)(a >> 32) & 0xffffu, bytes, 0x00020000u};
}
template <bool ATT>
__device__ __forceinline__ void role_chain_t(const MegaArgs& A, char* smem, const int L, const int h, const int rep) {
  const int w = A.wv;
  char* ws = A.ws;
  unsigned* FL = (unsigned*)(ws + OFF_FL);
  const int l = lane_id(), c = l & 15, g = l >> 4;
  unsigned* FQ = FL + FL_FQ + L * 64 * 32 * 32;
  const int wb = (w + ROT_H) & 7, tb = ROT_T;
  const unsigned sb = lds_base(smem);
  glds16(A.mem0, (unsigned)((c & 7) * DM + 64 * w + 16 * g + 8 * (c >> 3)) * 2, sb + C_SM + w * 1024);
#pragma unroll
  for (int i = 0; i < 16; ++i)
    glds16((const char*)A.Wo + (size_t)(h * DH + 32 * (i >> 2) + 4 * w + (i & 2)) * (DM * 2),
           (unsigned)((i & 1) * 1024 + ((l ^ (((i & 1) | ((w & 3) << 1)) << 1)) << 4)), sb + w * 16384 + i * 1024, WNT & 1);
  const u64 tag = call_tag(A);
  const char* gK = ws + TOK_QK(ws, L) + ((size_t)h * 16 + 2 * w) * 1024;
  const char* gV = ws + TOK_V(ws, L) + ((size_t)h * 8 + 2 * w - 8) * 1024;
  v8u pv, pw;
  {
    const unsigned voff = g * 256 + c * 8, wp = sb + w * 16384 + l * 16;
    pv[0] = (c & 8) ? 0xEEEE : 0x4444;
    pv[1] = sb + C_SM + l * 16;
    const unsigned ra = sb + C_SQ + (g >> 1) * 512 + (c ^ ((g & 1) * 18)) * 8;
    pv[2] = ATT ? ra : voff;
    pv[3] = ATT ? ra ^ 0x108 : wp;
    pv[4] = sb + ((c & 2) ? C_SQ : C_SO) + w * 256 + (((c & 5) | ((c >> 2) & 2)) + 8 * (g >> 1)) * 16 + 8 * ((g ^ (c >> 1)) & 1);
    pv[5] = voff;
    pv[6] = wp;
    pv[7] = sb + C_SO + (l >> 5) * 1024 + w * 256 + (l & 31) * 8;
    const int q = (l >> 2) & 3, p = l & 3;
    const int f = (q & 1) | ((g & 1) << 1) | ((q >> 1) << 2);
#pragma unroll
    for (int i = 0; i < 4; ++i) pw[i] = (4 * i + g) * (HD * 2) + ((c ^ (((g & 1) | (i << 1)) << 1)) << 4) - 1024 * i;
    pw[4] = sb + (g >> 1) * 16384 + (4 * (g & 1) + (q & 1) + 8 * (q >> 1)) * 256 + (w ^ f) * 32 + p * 8;
    pw[5] = sb + (4 * (g >> 1) + 2 * (q >> 1) + (g & 1)) * 16384 + (q & 1) * 1024 + p * 8 + ((4 * wb) ^ f ^ tb) * 32;
    pw[6] = pw[7] = 0u;
  }
  const size_t wo = (size_t)(16 * w) * HD + h * DH;
#define POL 2
#define PG(w) (POL == 2 ? (w) * 4096 : ((w) & 1) * 4096 + ((w) >> 1) * 16384)
  const v4u rsA = mk_srd(ws + OFF_ACC + L * SZ_ACC + (POL == 1 ? 8192 : 0), SZ_ACC - 8192);
  const v4u rsF = mk_srd(FQ, 64 * 32 * 32 * 4);
  const v4u rsMK = mk_srd(ws + OFF_MEMK + L * SZ_MEM, SZ_MEM);
  const v4u rsMV = mk_srd(ws + OFF_MEMV + L * SZ_MEM, SZ_MEM);
  const v4u pAB = (v4u{0u, 2048u, 3072u, 1024u} ^ (1536u * tb)) + PG(wb);
  const v4u rsT = mk_srd(FL + FL_FK + L * 32, 64 * 1024 - 128);
  unsigned sc[8];
  sc[0] = h * 128;
  sc[1] = (h * 4 + w - 4) * 512;
  sc[2] = (h * 4 + w - 4) * 512;
  sc[3] = NSEG - 1;
  sc[5] = w;
  sc[6] = sb;
  const char* dbg = ws;
#define CHAIN_OPS                                                                                                          \
  ::"{v[0:7]}"(pv), "{v[24:31]}"(pw), "{s[4:5]}"(A.Wv + wo), "{s[6:7]}"(A.Wq + wo), "{s[8:9]}"(A.Wk + wo), "{s[10:11]}"(tag),         \
      "{s[12:13]}"(ws + OFF_RD + (blockIdx.x & 7) * 512), "{s[14:15]}"(ws + OFF_G + (blockIdx.x & 7) * 256), "{s[16:17]}"(ws + OFF_G + h * 8), \
      "{s18}"(w == 4 && L == 0 ? 255 : 0), "{s19}"(sb + w * 16384), "{s[20:21]}"(ws + WS_TOTAL + (w == 0 ? 534 * 256 + A.vb : 610 * 256) * 8), \
      "{s[40:43]}"(rsA), "{s[44:47]}"(rsF), "{s[48:51]}"(rsMK), "{s[52:55]}"(rsMV), "{s[56:59]}"(pAB), "{s60}"(PG(w)), "{s61}"(ATT ? PG(h >> 2) + (h & 3) * 1024 : L), \
      "{s[62:63]}"(gK), "{s[64:65]}"(gV), "{s66}"(0), "{s67}"(w == 4 ? (h >> 3) * 256 + (h & 7) * 8 : 0x40000000), "{s68}"(sc[0]), "{s69}"(sc[1]), "{s70}"(sc[2]),    \
      "{s71}"(sc[3]), "{s73}"(sc[5]), "{s74}"(sc[6]), "{s[36:37]}"(dbg),                   \
      "{s[0:3]}"(rsT)                                                                                                      \
      : "memory", "vcc", "scc", "s99", "v255"
  if (ATT) asm volatile("s_mov_b64 exec, -1\ns_nop 4\n" CHAIN_ASM_ATT ".LKG_LAS2:\n" CHAIN_OPS);
  else asm volatile("s_mov_b64 exec, -1\ns_nop 4\n" CHAIN_ASM_VO CHAIN_OPS);
#undef CHAIN_OPS
}

__device__ __forceinline__ void role_chain(const MegaArgs& A, char* smem, const int L, const int h, const int rep) {
  if (A.wv < 4) role_chain_t<true>(A, smem, L, h, rep);
  else role_chain_t<false>(A, smem, L, h, rep);
}

constexpr int AW_SO = 0, AW_TK = 32768, AW_CNT = AW_TK + 98304;
__device__ __forceinline__ unsigned umin3(unsigned a, unsigned b, unsigned c) {
  return __builtin_elementwise_min(__builtin_elementwise_min(a, b), c);
}
#define PW_FREE(L_, s_, i_) \
  if (l == 0) set_flag(FL + FL_FD + ((s_) * 2 + (L_)) * 32 + (i_))
#define AW_ASM_X \
  "s_mov_b32 s61, 0\n" \
  "s_mov_b32 s67, 1\n" \
  "s_mov_b32 s68, 0\n" \
  "s_mov_b32 s70, 0\n" \
  "s_mov_b32 s73, 0\n" \
  "s_mov_b32 s71, 0x3e0293ee\n" \
  "s_mov_b32 s72, 0xbe0293ee\n" \
  "s_mov_b32 s65, 49152\n" \
  "s_bitcmp0_b32 s58, 2\n" \
  "s_cbranch_scc1 .LCX_OWN_%=\n" \
  "v_swap_b32 v192, v208\n" \
  "v_swap_b32 v193, v209\n" \
  "v_swap_b32 v194, v210\n" \
  "v_swap_b32 v195, v211\n" \
  "v_swap_b32 v196, v212\n" \
  "v_swap_b32 v197, v213\n" \
  "v_swap_b32 v198, v214\n" \
  "v_swap_b32 v199, v215\n" \
  "v_swap_b32 v200, v216\n" \
  "v_swap_b32 v201, v217\n" \
  "v_swap_b32 v202, v218\n" \
  "v_swap_b32 v203, v219\n" \
  "v_swap_b32 v204, v220\n" \
  "v_swap_b32 v205, v221\n" \
  "v_swap_b32 v206, v222\n" \
  "v_swap_b32 v207, v223\n" \
  "v_swap_b32 v224, v240\n" \
  "v_swap_b32 v225, v241\n" \
  "v_swap_b32 v226, v242\n" \
  "v_swap_b32 v227, v243\n" \
  "v_swap_b32 v228, v244\n" \
  "v_swap_b32 v229, v245\n" \
  "v_swap_b32 v230, v246\n" \
  "v_swap_b32 v231, v247\n" \
  "v_swap_b32 v232, v248\n" \
  "v_swap_b32 v233, v249\n" \
  "v_swap_b32 v234, v250\n" \
  "v_swap_b32 v235, v251\n" \
  "v_swap_b32 v236, v252\n" \
  "v_swap_b32 v237, v253\n" \
  "v_swap_b32 v238, v254\n" \
  "v_swap_b32 v239, v255\n" \
  ".LCX_OWN_%=:\n" \
  "v_mov_b32 v38, s58\n" \
  "s_memrealtime s[38:39]\n" \
  "s_waitcnt lgkmcnt(0)\n" \
  "s_mov_b32 s69, s38\n" \
  "s_setprio 3\n" \
  ".p2align 6\n" \
  ".LCX_TOP_%=:\n" \
  "s_mov_b32 s62, 0x200000\n" \
  "s_cmp_ge_u32 s73, s67\n" \
  "s_cbranch_scc1 .LCX_TOK_%=\n" \
  "s_cmp_eq_u32 s61, 0\n" \
  "s_cbranch_scc1 .LCX_SPIN_%=\n" \
  ".LCX_RE_%=:\n" \
  "ds_read_b32 v37, v38\n" \
  "s_add_u32 s62, s62, 1\n" \
  "s_waitcnt lgkmcnt(0)\n" \
  "v_readfirstlane_b32 s73, v37\n" \
  "s_bfe_u32 s64, s73, 0x80008\n" \
  "s_and_b32 s73, s73, 0xff\n" \
  "s_min_u32 s73, s73, s64\n" \
  "s_cmp_ge_u32 s73, s67\n" \
  "s_cbranch_scc1 .LCX_TOK_%=\n" \
  "s_sleep 1\n" \
  "s_cmp_lt_u32 s62, 2097158\n" \
  "s_cbranch_scc1 .LCX_RE_%=\n" \
  ".LCX_SPIN_%=:\n" \
  "s_sleep 2\n" \
  "s_memrealtime s[38:39]\n" \
  "ds_read_b32 v37, v38\n" \
  "s_sub_u32 s62, s62, 1\n" \
  "s_cbranch_scc1 .LCX_END_%=\n" \
  "s_waitcnt lgkmcnt(0)\n" \
  "v_readfirstlane_b32 s73, v37\n" \
  "s_add_u32 s38, s38, 80\n" \
  "s_bfe_u32 s64, s73, 0x80008\n" \
  "s_and_b32 s73, s73, 0xff\n" \
  "s_min_u32 s73, s73, s64\n" \
  "s_cmp_ge_u32 s73, s67\n" \
  "s_cbranch_scc0 .LCX_SPIN_%=\n" \
  "s_cmp_eq_u32 s70, 0\n" \
  "s_cbranch_scc1 .LCX_TOK_%=\n" \
  "s_sub_u32 s63, s38, s69\n" \
  "s_max_i32 s63, s63, 0\n" \
  "s_add_u32 s69, s69, s63\n" \
  ".LCX_TOK_%=:\n" \
  "ds_read_b128 v[48:51], v6 offset:0\n" \
  "ds_read_b128 v[64:67], v0 offset:8192\n" \
  "ds_read_b128 v[80:83], v0 offset:12288\n" \
  "ds_read_b128 v[52:55], v6 offset:1024\n" \
  "ds_read_b128 v[68:71], v0 offset:9216\n" \
  "ds_read_b128 v[84:87], v0 offset:13312\n" \
  "ds_read_b128 v[56:59], v6 offset:2048\n" \
  "ds_read_b128 v[72:75], v0 offset:10240\n" \
  "ds_read_b128 v[88:91], v0 offset:14336\n" \
  "ds_read_b128 v[60:63], v6 offset:3072\n" \
  "ds_read_b128 v[76:79], v0 offset:11264\n" \
  "ds_read_b128 v[92:95], v0 offset:15360\n" \
  "s_waitcnt lgkmcnt(9)\n" \
  "v_mfma_f32_16x16x32_bf16 v[20:23], v[64:67], v[48:51], v[8:11]\n" \
  "v_mfma_f32_16x16x32_bf16 v[24:27], v[80:83], v[48:51], v[12:15]\n" \
  "s_memrealtime s[38:39]\n" \
  "s_waitcnt lgkmcnt(6)\n" \
  "v_mfma_f32_16x16x32_bf16 v[20:23], v[68:71], v[52:55], v[20:23]\n" \
  "v_mfma_f32_16x16x32_bf16 v[24:27], v[84:87], v[52:55], v[24:27]\n" \
  "s_waitcnt lgkmcnt(3)\n" \
  "v_mfma_f32_16x16x32_bf16 v[20:23], v[72:75], v[56:59], v[20:23]\n" \
  "v_mfma_f32_16x16x32_bf16 v[24:27], v[88:91], v[56:59], v[24:27]\n" \
  "s_waitcnt lgkmcnt(0)\n" \
  "v_mfma_f32_16x16x32_bf16 v[20:23], v[76:79], v[60:63], v[20:23]\n" \
  "v_mfma_f32_16x16x32_bf16 v[24:27], v[92:95], v[60:63], v[24:27]\n" \
  "s_sub_u32 s63, s38, s69\n" \
  "s_add_u32 s63, s63, 8\n" \
  "s_or_b32 s63, s63, s68\n" \
  "s_cmp_lt_i32 s63, 0\n" \
  "s_cbranch_scc1 .LCX_D2_%=\n" \
  "s_setprio 3\n" \
  "v_mov_b32 v35, 0\n" \
  "global_load_dwordx4 v[128:131], v5, s[48:49] offset:0 sc1\n" \
  "global_load_dwordx4 v[132:135], v5, s[48:49] offset:1024 sc1\n" \
  "global_load_dwordx4 v[136:139], v3, s[48:49] offset:0 sc1\n" \
  "global_load_dwordx4 v[140:143], v3, s[48:49] offset:512 sc1\n" \
  "global_load_dword v35, v35, s[50:51] sc1\n" \
  "global_load_dword v146, v2, s[52:53] sc1\n" \
  "s_setprio 0\n" \
  "s_mov_b32 s68, -2\n" \
  ".LCX_D2_%=:\n" \
  "ds_read_b128 v[96:99], v0 offset:16384\n" \
  "ds_read_b128 v[100:103], v0 offset:17408\n" \
  "ds_read_b128 v[104:107], v0 offset:18432\n" \
  "ds_read_b128 v[108:111], v0 offset:19456\n" \
  "ds_read_b128 v[112:115], v0 offset:20480\n" \
  "ds_read_b128 v[116:119], v0 offset:21504\n" \
  "ds_read_b128 v[120:123], v0 offset:22528\n" \
  "ds_read_b128 v[124:127], v0 offset:23552\n" \
  "s_add_u32 s67, s67, 1\n" \
  "v_add_u32 v0, s65, v0\n" \
  "v_add_u32 v6, s65, v6\n" \
  "s_sub_u32 s65, 0, s65\n" \
  "s_nop 1\n" \
  "v_max3_f32 v32, v20, v21, v22\n" \
  "v_max3_f32 v33, v23, v24, v25\n" \
  "v_max3_f32 v32, v32, v26, v27\n" \
  "v_max_f32 v32, v32, v33\n" \
  "s_memrealtime s[38:39]\n" \
  "v_mov_b32 v33, v32\n" \
  "s_nop 1\n" \
  "v_permlane16_swap_b32 v32, v33\n" \
  "v_max_f32 v32, v32, v33\n" \
  "v_mov_b32 v33, v32\n" \
  "s_nop 1\n" \
  "v_permlane32_swap_b32 v32, v33\n" \
  "v_max_f32 v32, v32, v33\n" \
  "s_waitcnt lgkmcnt(0)\n" \
  "s_sub_u32 s63, s38, s69\n" \
  "s_add_u32 s63, s63, 8\n" \
  "s_or_b32 s63, s63, s68\n" \
  "s_cmp_lt_i32 s63, 0\n" \
  "s_cbranch_scc1 .LCX_D3_%=\n" \
  "s_setprio 3\n" \
  "v_mov_b32 v35, 0\n" \
  "global_load_dwordx4 v[128:131], v5, s[48:49] offset:0 sc1\n" \
  "global_load_dwordx4 v[132:135], v5, s[48:49] offset:1024 sc1\n" \
  "global_load_dwordx4 v[136:139], v3, s[48:49] offset:0 sc1\n" \
  "global_load_dwordx4 v[140:143], v3, s[48:49] offset:512 sc1\n" \
  "global_load_dword v35, v35, s[50:51] sc1\n" \
  "global_load_dword v146, v2, s[52:53] sc1\n" \
  "s_setprio 0\n" \
  "s_mov_b32 s68, -2\n" \
  ".LCX_D3_%=:\n" \
  "v_mul_f32 v33, s72, v32\n" \
  "v_fma_f32 v20, v20, s71, v33\n" \
  "v_fma_f32 v21, v21, s71, v33\n" \
  "v_fma_f32 v22, v22, s71, v33\n" \
  "v_fma_f32 v23, v23, s71, v33\n" \
  "v_fma_f32 v24, v24, s71, v33\n" \
  "v_fma_f32 v25, v25, s71, v33\n" \
  "v_fma_f32 v26, v26, s71, v33\n" \
  "v_fma_f32 v27, v27, s71, v33\n" \
  "v_exp_f32 v20, v20\n" \
  "v_exp_f32 v21, v21\n" \
  "v_exp_f32 v22, v22\n" \
  "v_exp_f32 v23, v23\n" \
  "v_exp_f32 v24, v24\n" \
  "v_exp_f32 v25, v25\n" \
  "v_exp_f32 v26, v26\n" \
  "v_exp_f32 v27, v27\n" \
  "v_add_f32 v34, v20, v21\n" \
  "v_add_f32 v33, v22, v23\n" \
  "v_add_f32 v36, v24, v25\n" \
  "v_add_f32 v34, v34, v33\n" \
  "v_add_f32 v33, v26, v27\n" \
  "v_add_f32 v34, v34, v36\n" \
  "v_add_f32 v34, v34, v33\n" \
  "v_mov_b64 v[28:29], 0\n" \
  "v_mov_b64 v[30:31], 0\n" \
  "s_waitcnt vmcnt(2) lgkmcnt(0)\n" \
  "s_cmp_gt_i32 s68, -2\n" \
  "s_cbranch_scc1 .LCX_NP_%=\n" \
  "s_waitcnt vmcnt(0)\n" \
  ".LCX_NP_%=:\n" \
  "s_cmp_lg_u32 s68, 0\n" \
  "s_cbranch_scc1 .LCX_TEST_%=\n" \
  ".LCX_WF_%=:\n" \
  "s_memrealtime s[38:39]\n" \
  "s_waitcnt lgkmcnt(0)\n" \
  "s_sub_u32 s63, s69, s38\n" \
  "s_cmp_le_i32 s63, 0\n" \
  "s_cbranch_scc1 .LCX_GO_%=\n" \
  "s_cmp_lt_i32 s63, 12\n" \
  "s_cbranch_scc1 .LCX_WF_%=\n" \
  "s_sleep 3\n" \
  "s_branch .LCX_WF_%=\n" \
  ".LCX_GO_%=:\n" \
  "v_mov_b32 v35, 0\n" \
  "global_load_dwordx4 v[128:131], v5, s[48:49] offset:0 sc1\n" \
  "global_load_dwordx4 v[132:135], v5, s[48:49] offset:1024 sc1\n" \
  "global_load_dwordx4 v[136:139], v3, s[48:49] offset:0 sc1\n" \
  "global_load_dwordx4 v[140:143], v3, s[48:49] offset:512 sc1\n" \
  "global_load_dword v35, v35, s[50:51] sc1\n" \
  "global_load_dword v146, v2, s[52:53] sc1\n" \
  "s_sub_u32 s62, s62, 8\n" \
  "s_cbranch_scc1 .LCX_END_%=\n" \
  "s_waitcnt vmcnt(0)\n" \
  ".LCX_TEST_%=:\n" \
  "s_setprio 3\n" \
  "v_smfmac_f32_16x16x64_bf16 v[28:31], v[128:131], v[48:55], v1\n" \
  "v_smfmac_f32_16x16x64_bf16 v[28:31], v[132:135], v[56:63], v1\n" \
  "v_min3_u32 v33, v128, v129, v130\n" \
  "v_min3_u32 v36, v131, v132, v133\n" \
  "v_min3_u32 v33, v33, v134, v135\n" \
  "v_min3_u32 v36, v36, v136, v137\n" \
  "v_min3_u32 v33, v33, v138, v139\n" \
  "v_min3_u32 v36, v36, v140, v141\n" \
  "v_min3_u32 v33, v33, v142, v143\n" \
  "v_min3_u32 v33, v33, v36, v146\n" \
  "v_cmp_eq_u32 vcc, 0, v33\n" \
  "v_readfirstlane_b32 s64, v35\n" \
  "s_mov_b32 s68, 0\n" \
  "s_cmp_eq_u64 vcc, 0\n" \
  "s_cbranch_scc0 .LCX_FAIL_%=\n" \
  "v_permlane32_swap_b32 v28, v30\n" \
  "v_permlane32_swap_b32 v29, v31\n" \
  "v_mov_b32 v33, s67\n" \
  "ds_write_b32 v38, v33 offset:16\n" \
  "v_add_f32 v28, v28, v30\n" \
  "v_add_f32 v29, v29, v31\n" \
  "v_max3_f32 v36, v28, v29, v32\n" \
  "v_mov_b32 v33, v36\n" \
  "s_cmp_eq_u32 s64, 0\n" \
  "s_cselect_b32 s64, s38, s64\n" \
  "v_permlane16_swap_b32 v36, v33\n" \
  "v_max_f32 v36, v36, v33\n" \
  "v_mov_b32 v33, v36\n" \
  "s_sub_u32 s63, s64, s70\n" \
  "s_mov_b32 s70, s64\n" \
  "v_permlane32_swap_b32 v36, v33\n" \
  "v_max_f32 v36, v36, v33\n" \
  "v_mul_f32 v36, s72, v36\n" \
  "v_fma_f32 v32, v32, s71, v36\n" \
  "v_fma_f32 v28, v28, s71, v36\n" \
  "v_fma_f32 v29, v29, s71, v36\n" \
  "v_exp_f32 v32, v32\n" \
  "v_exp_f32 v28, v28\n" \
  "v_exp_f32 v29, v29\n" \
  "v_fma_f32 v34, v34, v32, v28\n" \
  "v_add_f32 v34, v34, v29\n" \
  "v_mov_b32 v33, v34\n" \
  "s_min_u32 s63, s63, 250\n" \
  "s_add_u32 s69, s64, s63\n" \
  "v_permlane16_swap_b32 v34, v33\n" \
  "v_add_f32 v34, v34, v33\n" \
  "v_mov_b32 v33, v34\n" \
  "s_add_u32 s69, s69, -52\n" \
  "s_add_u32 s48, s48, 0x10000\n" \
  "s_addc_u32 s49, s49, 0\n" \
  "v_permlane32_swap_b32 v34, v33\n" \
  "v_add_f32 v34, v34, v33\n" \
  "v_rcp_f32 v34, v34\n" \
  "s_add_u32 s50, s50, 1024\n" \
  "s_addc_u32 s51, s51, 0\n" \
  "s_add_u32 s52, s52, 256\n" \
  "s_addc_u32 s53, s53, 0\n" \
  "v_mul_f32 v32, v32, v34\n" \
  "v_mul_f32 v20, v20, v32\n" \
  "v_mul_f32 v21, v21, v32\n" \
  "v_mul_f32 v22, v22, v32\n" \
  "v_mul_f32 v23, v23, v32\n" \
  "v_mul_f32 v24, v24, v32\n" \
  "v_mul_f32 v25, v25, v32\n" \
  "v_mul_f32 v26, v26, v32\n" \
  "v_mul_f32 v27, v27, v32\n" \
  "v_cvt_pk_bf16_f32 v40, v20, v21\n" \
  "v_cvt_pk_bf16_f32 v41, v22, v23\n" \
  "v_cvt_pk_bf16_f32 v42, v24, v25\n" \
  "v_cvt_pk_bf16_f32 v43, v26, v27\n" \
  "v_mul_f32 v28, v28, v34\n" \
  "v_mul_f32 v29, v29, v34\n" \
  "v_mfma_f32_16x16x32_bf16 v[64:67], v[96:99], v[40:43], 0\n" \
  "v_cvt_pk_bf16_f32 v44, v28, v29\n" \
  "v_mov_b32 v45, 0\n" \
  "v_mfma_f32_16x16x32_bf16 v[68:71], v[100:103], v[40:43], 0\n" \
  "v_cvt_pk_bf16_f32 v47, v28, v29\n" \
  "v_mov_b32 v46, 0\n" \
  "v_mfma_f32_16x16x32_bf16 v[72:75], v[104:107], v[40:43], 0\n" \
  "v_permlane32_swap_b32 v44, v45\n" \
  "v_mfma_f32_16x16x32_bf16 v[76:79], v[108:111], v[40:43], 0\n" \
  "v_permlane32_swap_b32 v46, v47\n" \
  "v_mfma_f32_16x16x32_bf16 v[80:83], v[112:115], v[40:43], 0\n" \
  "v_xor_b32 v16, 4096, v7\n" \
  "v_mfma_f32_16x16x32_bf16 v[84:87], v[116:119], v[40:43], 0\n" \
  "v_xor_b32 v17, 8192, v7\n" \
  "v_mfma_f32_16x16x32_bf16 v[88:91], v[120:123], v[40:43], 0\n" \
  "v_xor_b32 v18, 12288, v7\n" \
  "v_mfma_f32_16x16x32_bf16 v[92:95], v[124:127], v[40:43], 0\n" \
  "s_memrealtime s[38:39]\n" \
  "ds_read_b32 v37, v38\n" \
  "v_mfma_f32_16x16x16_bf16 v[64:67], v[136:137], v[44:45], v[64:67]\n" \
  "v_mfma_f32_16x16x16_bf16 v[68:71], v[140:141], v[44:45], v[68:71]\n" \
  "v_mfma_f32_16x16x16_bf16 v[72:75], v[136:137], v[46:47], v[72:75]\n" \
  "v_mfma_f32_16x16x16_bf16 v[76:79], v[140:141], v[46:47], v[76:79]\n" \
  "v_mfma_f32_16x16x16_bf16 v[80:83], v[138:139], v[44:45], v[80:83]\n" \
  "v_mfma_f32_16x16x16_bf16 v[84:87], v[142:143], v[44:45], v[84:87]\n" \
  "v_mfma_f32_16x16x16_bf16 v[88:91], v[138:139], v[46:47], v[88:91]\n" \
  "v_mfma_f32_16x16x16_bf16 v[92:95], v[142:143], v[46:47], v[92:95]\n" \
  "s_nop 0\n" \
  "v_cvt_pk_bf16_f32 v160, v64, v65\n" \
  "v_cvt_pk_bf16_f32 v161, v66, v67\n" \
  "v_cvt_pk_bf16_f32 v162, v68, v69\n" \
  "v_cvt_pk_bf16_f32 v163, v70, v71\n" \
  "ds_write_b128 v7, v[160:163] offset:0\n" \
  "v_cvt_pk_bf16_f32 v164, v72, v73\n" \
  "v_cvt_pk_bf16_f32 v165, v74, v75\n" \
  "v_cvt_pk_bf16_f32 v166, v76, v77\n" \
  "v_cvt_pk_bf16_f32 v167, v78, v79\n" \
  "ds_write_b128 v7, v[164:167] offset:1024\n" \
  "v_cvt_pk_bf16_f32 v168, v80, v81\n" \
  "v_cvt_pk_bf16_f32 v169, v82, v83\n" \
  "v_cvt_pk_bf16_f32 v170, v84, v85\n" \
  "v_cvt_pk_bf16_f32 v171, v86, v87\n" \
  "ds_write_b128 v7, v[168:171] offset:2048\n" \
  "v_cvt_pk_bf16_f32 v172, v88, v89\n" \
  "v_cvt_pk_bf16_f32 v173, v90, v91\n" \
  "v_cvt_pk_bf16_f32 v174, v92, v93\n" \
  "v_cvt_pk_bf16_f32 v175, v94, v95\n" \
  "ds_write_b128 v7, v[172:175] offset:3072\n" \
  "v_xor_b32 v7, 0x4000, v7\n" \
  "v_mfma_f32_16x16x32_bf16 v[112:115], v[192:195], v[160:163], 0\n" \
  "v_mfma_f32_16x16x32_bf16 v[116:119], v[224:227], v[160:163], 0\n" \
  "s_waitcnt lgkmcnt(0)\n" \
  "s_barrier\n" \
  "v_mfma_f32_16x16x32_bf16 v[112:115], v[196:199], v[164:167], v[112:115]\n" \
  "ds_read_b128 v[64:67], v16 offset:0\n" \
  "ds_read_b128 v[68:71], v16 offset:1024\n" \
  "ds_read_b128 v[72:75], v16 offset:2048\n" \
  "ds_read_b128 v[76:79], v16 offset:3072\n" \
  "s_sub_u32 s59, s60, 1\n" \
  "s_bfe_u32 s59, s59, 0x10003\n" \
  "v_readfirstlane_b32 s73, v37\n" \
  "s_sub_u32 s63, s38, s69\n" \
  "s_bfe_u32 s64, s73, 0x80010\n" \
  "s_sub_u32 s64, s64, s67\n" \
  "s_or_b32 s63, s63, s64\n" \
  "s_bfe_u32 s64, s73, 0x80008\n" \
  "s_and_b32 s73, s73, 0xff\n" \
  "s_min_u32 s73, s73, s64\n" \
  "s_or_b32 s63, s63, s68\n" \
  "s_cmp_lt_i32 s63, 0\n" \
  "s_cbranch_scc1 .LCX_E4_%=\n" \
  "v_mov_b32 v35, 0\n" \
  "global_load_dwordx4 v[128:131], v5, s[48:49] offset:0 sc1\n" \
  "global_load_dwordx4 v[132:135], v5, s[48:49] offset:1024 sc1\n" \
  "global_load_dwordx4 v[136:139], v3, s[48:49] offset:0 sc1\n" \
  "global_load_dwordx4 v[140:143], v3, s[48:49] offset:512 sc1\n" \
  "global_load_dword v35, v35, s[50:51] sc1\n" \
  "global_load_dword v146, v2, s[52:53] sc1\n" \
  "s_mov_b32 s68, -1\n" \
  ".LCX_E4_%=:\n" \
  "v_mfma_f32_16x16x32_bf16 v[116:119], v[228:231], v[164:167], v[116:119]\n" \
  "v_mfma_f32_16x16x32_bf16 v[112:115], v[200:203], v[168:171], v[112:115]\n" \
  "v_mfma_f32_16x16x32_bf16 v[116:119], v[232:235], v[168:171], v[116:119]\n" \
  "v_mfma_f32_16x16x32_bf16 v[112:115], v[204:207], v[172:175], v[112:115]\n" \
  "v_mfma_f32_16x16x32_bf16 v[116:119], v[236:239], v[172:175], v[116:119]\n" \
  "s_waitcnt lgkmcnt(3)\n" \
  "v_mfma_f32_16x16x32_bf16 v[120:123], v[192:195], v[64:67], 0\n" \
  "v_mfma_f32_16x16x32_bf16 v[124:127], v[224:227], v[64:67], 0\n" \
  "ds_read_b128 v[80:83], v17 offset:0\n" \
  "s_waitcnt lgkmcnt(3)\n" \
  "v_mfma_f32_16x16x32_bf16 v[120:123], v[196:199], v[68:71], v[120:123]\n" \
  "v_mfma_f32_16x16x32_bf16 v[124:127], v[228:231], v[68:71], v[124:127]\n" \
  "ds_read_b128 v[84:87], v17 offset:1024\n" \
  "s_waitcnt lgkmcnt(3)\n" \
  "v_mfma_f32_16x16x32_bf16 v[120:123], v[200:203], v[72:75], v[120:123]\n" \
  "v_mfma_f32_16x16x32_bf16 v[124:127], v[232:235], v[72:75], v[124:127]\n" \
  "ds_read_b128 v[88:91], v17 offset:2048\n" \
  "s_waitcnt lgkmcnt(3)\n" \
  "v_mfma_f32_16x16x32_bf16 v[120:123], v[204:207], v[76:79], v[120:123]\n" \
  "v_mfma_f32_16x16x32_bf16 v[124:127], v[236:239], v[76:79], v[124:127]\n" \
  "ds_read_b128 v[92:95], v17 offset:3072\n" \
  "s_waitcnt lgkmcnt(3)\n" \
  "v_mfma_f32_16x16x32_bf16 v[112:115], v[208:211], v[80:83], v[112:115]\n" \
  "v_mfma_f32_16x16x32_bf16 v[116:119], v[240:243], v[80:83], v[116:119]\n" \
  "ds_read_b128 v[96:99], v18 offset:0\n" \
  "s_waitcnt lgkmcnt(3)\n" \
  "v_mfma_f32_16x16x32_bf16 v[112:115], v[212:215], v[84:87], v[112:115]\n" \
  "v_mfma_f32_16x16x32_bf16 v[116:119], v[244:247], v[84:87], v[116:119]\n" \
  "ds_read_b128 v[100:103], v18 offset:1024\n" \
  "s_waitcnt lgkmcnt(3)\n" \
  "v_mfma_f32_16x16x32_bf16 v[112:115], v[216:219], v[88:91], v[112:115]\n" \
  "v_mfma_f32_16x16x32_bf16 v[116:119], v[248:251], v[88:91], v[116:119]\n" \
  "ds_read_b128 v[104:107], v18 offset:2048\n" \
  "s_waitcnt lgkmcnt(3)\n" \
  "v_mfma_f32_16x16x32_bf16 v[112:115], v[220:223], v[92:95], v[112:115]\n" \
  "v_mfma_f32_16x16x32_bf16 v[116:119], v[252:255], v[92:95], v[116:119]\n" \
  "ds_read_b128 v[108:111], v18 offset:3072\n" \
  "s_waitcnt lgkmcnt(3)\n" \
  "v_mfma_f32_16x16x32_bf16 v[120:123], v[208:211], v[96:99], v[120:123]\n" \
  "v_mfma_f32_16x16x32_bf16 v[124:127], v[240:243], v[96:99], v[124:127]\n" \
  "s_memrealtime s[38:39]\n" \
  "ds_read_b32 v37, v38\n" \
  "s_waitcnt lgkmcnt(3)\n" \
  "v_mfma_f32_16x16x32_bf16 v[120:123], v[212:215], v[100:103], v[120:123]\n" \
  "v_mfma_f32_16x16x32_bf16 v[124:127], v[244:247], v[100:103], v[124:127]\n" \
  "v_cvt_pk_bf16_f32 v20, v112, v113\n" \
  "v_cvt_pk_bf16_f32 v21, v114, v115\n" \
  "v_cvt_pk_bf16_f32 v22, v116, v117\n" \
  "v_cvt_pk_bf16_f32 v23, v118, v119\n" \
  "v_and_or_b32 v20, v20, -2, s59\n" \
  "s_waitcnt lgkmcnt(2)\n" \
  "v_mfma_f32_16x16x32_bf16 v[120:123], v[216:219], v[104:107], v[120:123]\n" \
  "v_mfma_f32_16x16x32_bf16 v[124:127], v[248:251], v[104:107], v[124:127]\n" \
  "buffer_store_dwordx4 v[20:23], v4, s[40:43], s54 offen sc1\n" \
  "s_waitcnt lgkmcnt(0)\n" \
  "v_mfma_f32_16x16x32_bf16 v[120:123], v[220:223], v[108:111], v[120:123]\n" \
  "v_mfma_f32_16x16x32_bf16 v[124:127], v[252:255], v[108:111], v[124:127]\n" \
  "s_xor_b32 s64, s54, 16384\n" \
  "s_mov_b32 s61, 1\n" \
  "s_nop 3\n" \
  "v_cvt_pk_bf16_f32 v24, v120, v121\n" \
  "v_cvt_pk_bf16_f32 v25, v122, v123\n" \
  "v_cvt_pk_bf16_f32 v26, v124, v125\n" \
  "v_cvt_pk_bf16_f32 v27, v126, v127\n" \
  "v_and_or_b32 v24, v24, -2, s59\n" \
  "buffer_store_dwordx4 v[24:27], v4, s[40:43], s64 offen sc1\n" \
  "v_readfirstlane_b32 s73, v37\n" \
  "s_sub_u32 s63, s38, s69\n" \
  "s_bfe_u32 s64, s73, 0x80010\n" \
  "s_sub_u32 s64, s64, s67\n" \
  "s_or_b32 s63, s63, s64\n" \
  "s_bfe_u32 s64, s73, 0x80008\n" \
  "s_and_b32 s73, s73, 0xff\n" \
  "s_min_u32 s73, s73, s64\n" \
  "s_or_b32 s63, s63, s68\n" \
  "s_cmp_lt_i32 s63, 0\n" \
  "s_cbranch_scc1 .LCX_E5_%=\n" \
  "v_mov_b32 v35, 0\n" \
  "global_load_dwordx4 v[128:131], v5, s[48:49] offset:0 sc1\n" \
  "global_load_dwordx4 v[132:135], v5, s[48:49] offset:1024 sc1\n" \
  "global_load_dwordx4 v[136:139], v3, s[48:49] offset:0 sc1\n" \
  "global_load_dwordx4 v[140:143], v3, s[48:49] offset:512 sc1\n" \
  "global_load_dword v35, v35, s[50:51] sc1\n" \
  "global_load_dword v146, v2, s[52:53] sc1\n" \
  "s_mov_b32 s68, -2\n" \
  ".LCX_E5_%=:\n" \
  "s_setprio 0\n" \
  "s_add_u32 s54, s54, 0x80000\n" \
  "s_bitset0_b32 s54, 22\n" \
  "s_sub_u32 s60, s60, 1\n" \
  "s_cmp_lg_u32 s60, 0\n" \
  "s_cbranch_scc1 .LCX_TOP_%=\n" \
  "s_waitcnt vmcnt(0)\n" \
  ".LCX_END_%=:\n" \
  "s_endpgm\n" \
  ".LCX_FAIL_%=:\n" \
  "v_mov_b64 v[28:29], 0\n" \
  "v_mov_b64 v[30:31], 0\n" \
  ".LCX_FAIL2_%=:\n" \
  "s_add_u32 s69, s64, -52\n" \
  "s_cmp_lg_u32 s64, 0\n" \
  "s_cbranch_scc1 .LCX_WF_%=\n" \
  "s_sleep 4\n" \
  "global_load_dword v35, v35, s[50:51] sc1\n" \
  "s_sub_u32 s62, s62, 8\n" \
  "s_cbranch_scc1 .LCX_END_%=\n" \
  "s_waitcnt vmcnt(0)\n" \
  "v_readfirstlane_b32 s64, v35\n" \
  "s_branch .LCX_FAIL2_%=\n"
#define AW_ASM_Y \
  "s_sub_u32 s78, s77, 1\n" \
  "s_mov_b32 s72, 0\n" \
  "s_mov_b32 s67, 0\n" \
  "s_mov_b32 s68, 2\n" \
  "s_mov_b32 s69, 0\n" \
  "s_mov_b32 s65, 49152\n" \
  "s_mov_b32 s66, 16384\n" \
  "v_mov_b32 v5, s58\n" \
  "v_mov_b32 v60, v3\n" \
  "v_add_u32 v3, 4096, v2\n" \
  "s_mov_b32 s62, 0x40000\n" \
  "s_setprio 1\n" \
  "s_branch .LCY_ASK_%=\n" \
  ".p2align 6\n" \
  ".LCY_BAR_%=:\n" \
  "s_barrier\n" \
  "s_memrealtime s[38:39]\n" \
  "ds_read_b128 v[8:11], v0 offset:0\n" \
  "ds_read_b128 v[12:15], v0 offset:1024\n" \
  "ds_read_b128 v[16:19], v0 offset:2048\n" \
  "s_sub_u32 s59, s60, 1\n" \
  "s_bfe_u32 s59, s59, 0x10003\n" \
  "s_add_u32 s68, s68, 1\n" \
  "s_add_u32 s64, s54, 16384\n" \
  "s_cmp_eq_u32 s69, 0\n" \
  "s_cbranch_scc1 .LCY_NI_%=\n" \
  "s_waitcnt vmcnt(3)\n" \
  "s_cmp_lg_u32 s72, 0\n" \
  "s_cbranch_scc1 .LCY_NJ_%=\n" \
  "s_waitcnt vmcnt(0)\n" \
  ".LCY_NJ_%=:\n" \
  "v_mov_b32 v6, s55\n" \
  "s_mov_b64 exec, 1\n" \
  "ds_add_u32 v5, v6\n" \
  "s_mov_b64 exec, -1\n" \
  "s_mov_b32 s69, 0\n" \
  ".LCY_NI_%=:\n" \
  "s_waitcnt lgkmcnt(0)\n" \
  "s_add_u32 s63, s38, 205\n" \
  "s_mov_b32 s72, 0\n" \
  "v_or_b32 v7, 1, s63\n" \
  "s_mov_b64 exec, 1\n" \
  "global_store_dword v2, v7, s[52:53] sc1\n" \
  "s_mov_b64 exec, -1\n" \
  "s_add_u32 s52, s52, 2048\n" \
  "s_addc_u32 s53, s53, 0\n" \
  "v_cmp_eq_u32 vcc, 0, v4\n" \
  "s_cmp_eq_u64 vcc, 0\n" \
  "s_cbranch_scc0 .LCY_NS_%=\n" \
  "v_lshlrev_b32 v6, 16, s55\n" \
  "s_mov_b64 exec, 1\n" \
  "ds_add_u32 v5, v6\n" \
  "s_mov_b64 exec, -1\n" \
  "s_mov_b32 m0, s56\n" \
  "s_add_u32 s71, s56, 4096\n" \
  "global_load_lds_dwordx4 v2, s[48:49] offset:0 sc1\n" \
  "global_load_lds_dwordx4 v2, s[48:49] offset:1024 sc1\n" \
  "global_load_lds_dwordx4 v2, s[48:49] offset:2048 sc1\n" \
  "global_load_lds_dwordx4 v2, s[48:49] offset:3072 sc1\n" \
  ".LCY_NS_%=:\n" \
  "ds_read_b128 v[20:23], v0 offset:3072\n" \
  "v_mfma_f32_16x16x32_bf16 v[32:35], v[64:67], v[8:11], 0\n" \
  "v_mfma_f32_16x16x32_bf16 v[36:39], v[96:99], v[8:11], 0\n" \
  "v_mfma_f32_16x16x32_bf16 v[40:43], v[128:131], v[8:11], 0\n" \
  "v_mfma_f32_16x16x32_bf16 v[44:47], v[160:163], v[8:11], 0\n" \
  "v_mfma_f32_16x16x32_bf16 v[48:51], v[192:195], v[8:11], 0\n" \
  "v_mfma_f32_16x16x32_bf16 v[52:55], v[224:227], v[8:11], 0\n" \
  "ds_read_b128 v[8:11], v0 offset:8192\n" \
  "v_mfma_f32_16x16x32_bf16 v[32:35], v[68:71], v[12:15], v[32:35]\n" \
  "v_mfma_f32_16x16x32_bf16 v[36:39], v[100:103], v[12:15], v[36:39]\n" \
  "v_mfma_f32_16x16x32_bf16 v[40:43], v[132:135], v[12:15], v[40:43]\n" \
  "v_mfma_f32_16x16x32_bf16 v[44:47], v[164:167], v[12:15], v[44:47]\n" \
  "v_mfma_f32_16x16x32_bf16 v[48:51], v[196:199], v[12:15], v[48:51]\n" \
  "v_mfma_f32_16x16x32_bf16 v[52:55], v[228:231], v[12:15], v[52:55]\n" \
  "s_cbranch_vccnz .LCY_D1_%=\n" \
  "s_mov_b32 m0, s71\n" \
  "s_add_u32 s56, s56, s65\n" \
  "global_load_lds_dwordx4 v3, s[48:49] offset:0 sc1\n" \
  ".LCY_D1_%=:\n" \
  "ds_read_b128 v[12:15], v0 offset:9216\n" \
  "v_mfma_f32_16x16x32_bf16 v[32:35], v[72:75], v[16:19], v[32:35]\n" \
  "v_mfma_f32_16x16x32_bf16 v[36:39], v[104:107], v[16:19], v[36:39]\n" \
  "v_mfma_f32_16x16x32_bf16 v[40:43], v[136:139], v[16:19], v[40:43]\n" \
  "v_mfma_f32_16x16x32_bf16 v[44:47], v[168:171], v[16:19], v[44:47]\n" \
  "v_mfma_f32_16x16x32_bf16 v[48:51], v[200:203], v[16:19], v[48:51]\n" \
  "v_mfma_f32_16x16x32_bf16 v[52:55], v[232:235], v[16:19], v[52:55]\n" \
  "s_cbranch_vccnz .LCY_D2_%=\n" \
  "global_load_lds_dwordx4 v3, s[48:49] offset:1024 sc1\n" \
  ".LCY_D2_%=:\n" \
  "ds_read_b128 v[16:19], v0 offset:10240\n" \
  "s_waitcnt lgkmcnt(3)\n" \
  "v_mfma_f32_16x16x32_bf16 v[32:35], v[76:79], v[20:23], v[32:35]\n" \
  "v_mfma_f32_16x16x32_bf16 v[36:39], v[108:111], v[20:23], v[36:39]\n" \
  "v_mfma_f32_16x16x32_bf16 v[40:43], v[140:143], v[20:23], v[40:43]\n" \
  "v_mfma_f32_16x16x32_bf16 v[44:47], v[172:175], v[20:23], v[44:47]\n" \
  "v_mfma_f32_16x16x32_bf16 v[48:51], v[204:207], v[20:23], v[48:51]\n" \
  "v_mfma_f32_16x16x32_bf16 v[52:55], v[236:239], v[20:23], v[52:55]\n" \
  "s_cbranch_vccnz .LCY_D3_%=\n" \
  "global_load_lds_dwordx4 v3, s[48:49] offset:2048 sc1\n" \
  ".LCY_D3_%=:\n" \
  "ds_read_b128 v[20:23], v0 offset:11264\n" \
  "s_waitcnt lgkmcnt(3)\n" \
  "v_mfma_f32_16x16x32_bf16 v[32:35], v[80:83], v[8:11], v[32:35]\n" \
  "v_mfma_f32_16x16x32_bf16 v[36:39], v[112:115], v[8:11], v[36:39]\n" \
  "v_mfma_f32_16x16x32_bf16 v[40:43], v[144:147], v[8:11], v[40:43]\n" \
  "v_mfma_f32_16x16x32_bf16 v[44:47], v[176:179], v[8:11], v[44:47]\n" \
  "v_mfma_f32_16x16x32_bf16 v[48:51], v[208:211], v[8:11], v[48:51]\n" \
  "v_mfma_f32_16x16x32_bf16 v[52:55], v[240:243], v[8:11], v[52:55]\n" \
  "s_cbranch_vccnz .LCY_D4_%=\n" \
  "global_load_lds_dwordx4 v3, s[48:49] offset:3072 sc1\n" \
  "s_add_u32 s48, s48, 0x80000\n" \
  "s_addc_u32 s49, s49, 0\n" \
  ".LCY_D4_%=:\n" \
  "ds_read_b128 v[8:11], v0 offset:4096\n" \
  "s_waitcnt lgkmcnt(3)\n" \
  "v_mfma_f32_16x16x32_bf16 v[32:35], v[84:87], v[12:15], v[32:35]\n" \
  "v_mfma_f32_16x16x32_bf16 v[36:39], v[116:119], v[12:15], v[36:39]\n" \
  "v_mfma_f32_16x16x32_bf16 v[40:43], v[148:151], v[12:15], v[40:43]\n" \
  "v_mfma_f32_16x16x32_bf16 v[44:47], v[180:183], v[12:15], v[44:47]\n" \
  "v_mfma_f32_16x16x32_bf16 v[48:51], v[212:215], v[12:15], v[48:51]\n" \
  "v_mfma_f32_16x16x32_bf16 v[52:55], v[244:247], v[12:15], v[52:55]\n" \
  "s_cbranch_vccnz .LCY_AS_%=\n" \
  "s_mov_b32 m0, s57\n" \
  "s_add_u32 s57, s57, s65\n" \
  "global_load_lds_dwordx4 v2, s[50:51] offset:0 sc1\n" \
  ".LCY_D5_%=:\n" \
  "ds_read_b128 v[12:15], v0 offset:5120\n" \
  "s_waitcnt lgkmcnt(3)\n" \
  "v_mfma_f32_16x16x32_bf16 v[32:35], v[88:91], v[16:19], v[32:35]\n" \
  "v_mfma_f32_16x16x32_bf16 v[36:39], v[120:123], v[16:19], v[36:39]\n" \
  "v_mfma_f32_16x16x32_bf16 v[40:43], v[152:155], v[16:19], v[40:43]\n" \
  "v_mfma_f32_16x16x32_bf16 v[44:47], v[184:187], v[16:19], v[44:47]\n" \
  "v_mfma_f32_16x16x32_bf16 v[48:51], v[216:219], v[16:19], v[48:51]\n" \
  "v_mfma_f32_16x16x32_bf16 v[52:55], v[248:251], v[16:19], v[52:55]\n" \
  "s_cbranch_vccnz .LCY_D6_%=\n" \
  "global_load_lds_dwordx4 v2, s[50:51] offset:1024 sc1\n" \
  "s_sub_u32 s65, 0, s65\n" \
  "s_add_u32 s67, s67, 4096\n" \
  "s_bitset0_b32 s48, s77\n" \
  ".LCY_D6_%=:\n" \
  "ds_read_b128 v[16:19], v0 offset:6144\n" \
  "s_waitcnt lgkmcnt(3)\n" \
  "v_mfma_f32_16x16x32_bf16 v[32:35], v[92:95], v[20:23], v[32:35]\n" \
  "v_mfma_f32_16x16x32_bf16 v[36:39], v[124:127], v[20:23], v[36:39]\n" \
  "v_mfma_f32_16x16x32_bf16 v[40:43], v[156:159], v[20:23], v[40:43]\n" \
  "v_mfma_f32_16x16x32_bf16 v[44:47], v[188:191], v[20:23], v[44:47]\n" \
  "v_mfma_f32_16x16x32_bf16 v[48:51], v[220:223], v[20:23], v[48:51]\n" \
  "v_mfma_f32_16x16x32_bf16 v[52:55], v[252:255], v[20:23], v[52:55]\n" \
  "ds_read_b128 v[20:23], v0 offset:7168\n" \
  "s_waitcnt lgkmcnt(3)\n" \
  "s_nop 1\n" \
  "v_cvt_pk_bf16_f32 v24, v32, v33\n" \
  "v_cvt_pk_bf16_f32 v25, v34, v35\n" \
  "v_cvt_pk_bf16_f32 v26, v36, v37\n" \
  "v_cvt_pk_bf16_f32 v27, v38, v39\n" \
  "s_nop 0\n" \
  "v_mfma_f32_16x16x32_bf16 v[32:35], v[64:67], v[8:11], 0\n" \
  "v_mfma_f32_16x16x32_bf16 v[36:39], v[96:99], v[8:11], 0\n" \
  "v_and_or_b32 v24, v24, -2, s59\n" \
  "buffer_store_dwordx4 v[24:27], v1, s[40:43], s54 offen offset:0 sc1\n" \
  "v_cvt_pk_bf16_f32 v28, v40, v41\n" \
  "v_cvt_pk_bf16_f32 v29, v42, v43\n" \
  "v_cvt_pk_bf16_f32 v30, v44, v45\n" \
  "v_cvt_pk_bf16_f32 v31, v46, v47\n" \
  "s_nop 0\n" \
  "v_mfma_f32_16x16x32_bf16 v[40:43], v[128:131], v[8:11], 0\n" \
  "v_mfma_f32_16x16x32_bf16 v[44:47], v[160:163], v[8:11], 0\n" \
  "v_and_or_b32 v28, v28, -2, s59\n" \
  "buffer_store_dwordx4 v[28:31], v1, s[40:43], s54 offen offset:128 sc1\n" \
  "v_cvt_pk_bf16_f32 v56, v48, v49\n" \
  "v_cvt_pk_bf16_f32 v57, v50, v51\n" \
  "v_cvt_pk_bf16_f32 v58, v52, v53\n" \
  "v_cvt_pk_bf16_f32 v59, v54, v55\n" \
  "s_nop 0\n" \
  "v_mfma_f32_16x16x32_bf16 v[48:51], v[192:195], v[8:11], 0\n" \
  "v_mfma_f32_16x16x32_bf16 v[52:55], v[224:227], v[8:11], 0\n" \
  "v_and_or_b32 v56, v56, -2, s59\n" \
  "buffer_store_dwordx4 v[56:59], v1, s[40:43], s54 offen offset:256 sc1\n" \
  "s_cbranch_vccnz .LCY_AL0_%=\n" \
  ".LCY_AL1_%=:\n" \
  "ds_read_b128 v[8:11], v0 offset:12288\n" \
  "s_waitcnt lgkmcnt(3)\n" \
  "v_mfma_f32_16x16x32_bf16 v[32:35], v[68:71], v[12:15], v[32:35]\n" \
  "v_mfma_f32_16x16x32_bf16 v[36:39], v[100:103], v[12:15], v[36:39]\n" \
  "v_mfma_f32_16x16x32_bf16 v[40:43], v[132:135], v[12:15], v[40:43]\n" \
  "v_mfma_f32_16x16x32_bf16 v[44:47], v[164:167], v[12:15], v[44:47]\n" \
  "v_mfma_f32_16x16x32_bf16 v[48:51], v[196:199], v[12:15], v[48:51]\n" \
  "v_mfma_f32_16x16x32_bf16 v[52:55], v[228:231], v[12:15], v[52:55]\n" \
  "s_cbranch_vccnz .LCY_D7_%=\n" \
  "global_load_lds_dwordx4 v2, s[50:51] offset:2048 sc1\n" \
  "s_sub_u32 s68, s68, 1\n" \
  "s_mov_b32 s69, 1\n" \
  ".LCY_D7_%=:\n" \
  "ds_read_b128 v[12:15], v0 offset:13312\n" \
  "s_waitcnt lgkmcnt(3)\n" \
  "v_mfma_f32_16x16x32_bf16 v[32:35], v[72:75], v[16:19], v[32:35]\n" \
  "v_mfma_f32_16x16x32_bf16 v[36:39], v[104:107], v[16:19], v[36:39]\n" \
  "v_mfma_f32_16x16x32_bf16 v[40:43], v[136:139], v[16:19], v[40:43]\n" \
  "v_mfma_f32_16x16x32_bf16 v[44:47], v[168:171], v[16:19], v[44:47]\n" \
  "v_mfma_f32_16x16x32_bf16 v[48:51], v[200:203], v[16:19], v[48:51]\n" \
  "v_mfma_f32_16x16x32_bf16 v[52:55], v[232:235], v[16:19], v[52:55]\n" \
  "s_cbranch_vccnz .LCY_D8_%=\n" \
  "global_load_lds_dwordx4 v2, s[50:51] offset:3072 sc1\n" \
  "s_add_u32 s50, s50, 0x40000\n" \
  "s_addc_u32 s51, s51, 0\n" \
  "s_bitset0_b32 s50, s78\n" \
  ".LCY_D8_%=:\n" \
  "ds_read_b128 v[16:19], v0 offset:14336\n" \
  "s_waitcnt lgkmcnt(3)\n" \
  "v_mfma_f32_16x16x32_bf16 v[32:35], v[76:79], v[20:23], v[32:35]\n" \
  "v_mfma_f32_16x16x32_bf16 v[36:39], v[108:111], v[20:23], v[36:39]\n" \
  "v_mfma_f32_16x16x32_bf16 v[40:43], v[140:143], v[20:23], v[40:43]\n" \
  "v_mfma_f32_16x16x32_bf16 v[44:47], v[172:175], v[20:23], v[44:47]\n" \
  "v_mfma_f32_16x16x32_bf16 v[48:51], v[204:207], v[20:23], v[48:51]\n" \
  "v_mfma_f32_16x16x32_bf16 v[52:55], v[236:239], v[20:23], v[52:55]\n" \
  "ds_read_b128 v[20:23], v0 offset:15360\n" \
  "s_waitcnt lgkmcnt(3)\n" \
  "v_mfma_f32_16x16x32_bf16 v[32:35], v[80:83], v[8:11], v[32:35]\n" \
  "v_mfma_f32_16x16x32_bf16 v[36:39], v[112:115], v[8:11], v[36:39]\n" \
  "v_mfma_f32_16x16x32_bf16 v[40:43], v[144:147], v[8:11], v[40:43]\n" \
  "v_mfma_f32_16x16x32_bf16 v[44:47], v[176:179], v[8:11], v[44:47]\n" \
  "v_mfma_f32_16x16x32_bf16 v[48:51], v[208:211], v[8:11], v[48:51]\n" \
  "v_mfma_f32_16x16x32_bf16 v[52:55], v[240:243], v[8:11], v[52:55]\n" \
  "s_waitcnt lgkmcnt(2)\n" \
  "v_mfma_f32_16x16x32_bf16 v[32:35], v[84:87], v[12:15], v[32:35]\n" \
  "v_mfma_f32_16x16x32_bf16 v[36:39], v[116:119], v[12:15], v[36:39]\n" \
  "v_mfma_f32_16x16x32_bf16 v[40:43], v[148:151], v[12:15], v[40:43]\n" \
  "v_mfma_f32_16x16x32_bf16 v[44:47], v[180:183], v[12:15], v[44:47]\n" \
  "v_mfma_f32_16x16x32_bf16 v[48:51], v[212:215], v[12:15], v[48:51]\n" \
  "v_mfma_f32_16x16x32_bf16 v[52:55], v[244:247], v[12:15], v[52:55]\n" \
  "s_waitcnt lgkmcnt(1)\n" \
  "v_mfma_f32_16x16x32_bf16 v[32:35], v[88:91], v[16:19], v[32:35]\n" \
  "v_mfma_f32_16x16x32_bf16 v[36:39], v[120:123], v[16:19], v[36:39]\n" \
  "v_mfma_f32_16x16x32_bf16 v[40:43], v[152:155], v[16:19], v[40:43]\n" \
  "v_mfma_f32_16x16x32_bf16 v[44:47], v[184:187], v[16:19], v[44:47]\n" \
  "v_mfma_f32_16x16x32_bf16 v[48:51], v[216:219], v[16:19], v[48:51]\n" \
  "v_mfma_f32_16x16x32_bf16 v[52:55], v[248:251], v[16:19], v[52:55]\n" \
  "s_cbranch_vccnz .LCY_NT_%=\n" \
  "v_mov_b32 v4, 0\n" \
  "buffer_load_dword v4, v60, s[44:47], s67 offen sc1\n" \
  ".LCY_NT_%=:\n" \
  "s_waitcnt lgkmcnt(0)\n" \
  "v_mfma_f32_16x16x32_bf16 v[32:35], v[92:95], v[20:23], v[32:35]\n" \
  "v_mfma_f32_16x16x32_bf16 v[36:39], v[124:127], v[20:23], v[36:39]\n" \
  "v_mfma_f32_16x16x32_bf16 v[40:43], v[156:159], v[20:23], v[40:43]\n" \
  "v_mfma_f32_16x16x32_bf16 v[44:47], v[188:191], v[20:23], v[44:47]\n" \
  "v_mfma_f32_16x16x32_bf16 v[48:51], v[220:223], v[20:23], v[48:51]\n" \
  "v_mfma_f32_16x16x32_bf16 v[52:55], v[252:255], v[20:23], v[52:55]\n" \
  ".LCY_JOIN_%=:\n" \
  "v_add_u32 v0, s66, v0\n" \
  "s_sub_u32 s66, 0, s66\n" \
  "s_add_u32 s54, s54, 0x80000\n" \
  "s_bitset0_b32 s54, 22\n" \
  "s_mov_b32 s62, 0x40000\n" \
  "s_nop 0\n" \
  "v_cvt_pk_bf16_f32 v24, v32, v33\n" \
  "v_cvt_pk_bf16_f32 v25, v34, v35\n" \
  "v_cvt_pk_bf16_f32 v26, v36, v37\n" \
  "v_cvt_pk_bf16_f32 v27, v38, v39\n" \
  "v_and_or_b32 v24, v24, -2, s59\n" \
  "buffer_store_dwordx4 v[24:27], v1, s[40:43], s64 offen offset:0 sc1\n" \
  "v_cvt_pk_bf16_f32 v28, v40, v41\n" \
  "v_cvt_pk_bf16_f32 v29, v42, v43\n" \
  "v_cvt_pk_bf16_f32 v30, v44, v45\n" \
  "v_cvt_pk_bf16_f32 v31, v46, v47\n" \
  "v_and_or_b32 v28, v28, -2, s59\n" \
  "buffer_store_dwordx4 v[28:31], v1, s[40:43], s64 offen offset:128 sc1\n" \
  "v_cvt_pk_bf16_f32 v56, v48, v49\n" \
  "v_cvt_pk_bf16_f32 v57, v50, v51\n" \
  "v_cvt_pk_bf16_f32 v58, v52, v53\n" \
  "v_cvt_pk_bf16_f32 v59, v54, v55\n" \
  "v_and_or_b32 v56, v56, -2, s59\n" \
  "buffer_store_dwordx4 v[56:59], v1, s[40:43], s64 offen offset:256 sc1\n" \
  "s_cbranch_vccnz .LCY_ND_%=\n" \
  "s_cmp_eq_u32 s68, 0\n" \
  "s_cselect_b32 s72, 1, 0\n" \
  "s_cbranch_scc1 .LCY_DEF_%=\n" \
  "s_waitcnt vmcnt(0)\n" \
  "v_mov_b32 v6, s55\n" \
  "s_mov_b64 exec, 1\n" \
  "ds_add_u32 v5, v6\n" \
  "s_mov_b64 exec, -1\n" \
  "s_mov_b32 s69, 0\n" \
  "s_branch .LCY_WC_%=\n" \
  ".LCY_ND_%=:\n" \
  "v_mov_b32 v4, 0\n" \
  "buffer_load_dword v4, v60, s[44:47], s67 offen sc1\n" \
  "s_waitcnt vmcnt(0)\n" \
  ".LCY_WC_%=:\n" \
  "s_branch .LCY_DEF_%=\n" \
  ".LCY_AS_%=:\n" \
  "s_sub_u32 s63, s60, 10\n" \
  "s_cmp_lt_u32 s63, 51\n" \
  "s_cbranch_scc1 .LCY_D5_%=\n" \
  "v_mov_b32 v4, 0\n" \
  "buffer_load_dword v4, v60, s[44:47], s67 offen sc1\n" \
  "s_branch .LCY_D5_%=\n" \
  ".LCY_AL0_%=:\n" \
  "s_sub_u32 s63, s60, 10\n" \
  "s_cmp_lt_u32 s63, 51\n" \
  "s_cbranch_scc1 .LCY_AL1_%=\n" \
  "s_bitcmp1_b32 s67, 18\n" \
  "s_cbranch_scc1 .LCY_AL1_%=\n" \
  "s_branch .LCY_ALT_%=\n" \
  ".LCY_DEF_%=:\n" \
  "s_sub_u32 s60, s60, 1\n" \
  "s_cmp_eq_u32 s60, 0\n" \
  "s_cbranch_scc1 .LCY_END_%=\n" \
  ".LCY_NEED_%=:\n" \
  "s_cmp_eq_u32 s68, 0\n" \
  "s_cbranch_scc1 .LCY_BAR_%=\n" \
  "v_cmp_eq_u32 vcc, 0, v4\n" \
  "s_cmp_eq_u64 vcc, 0\n" \
  "s_cbranch_scc1 .LCY_GET_%=\n" \
  "s_cmp_ge_u32 s67, 0x40000\n" \
  "s_cbranch_scc1 .LCY_BAR_%=\n" \
  "s_sleep 4\n" \
  "ds_read_b32 v6, v5 offset:16\n" \
  "s_waitcnt lgkmcnt(0)\n" \
  "v_readfirstlane_b32 s63, v6\n" \
  "s_add_u32 s63, s63, s60\n" \
  "s_cmp_ge_u32 s63, 66\n" \
  "s_cbranch_scc1 .LCY_BAR_%=\n" \
  ".LCY_ASK_%=:\n" \
  "v_mov_b32 v4, 0\n" \
  "buffer_load_dword v4, v60, s[44:47], s67 offen sc1\n" \
  "s_sub_u32 s62, s62, 1\n" \
  "s_cbranch_scc1 .LCY_END_%=\n" \
  "s_waitcnt vmcnt(0)\n" \
  "s_branch .LCY_NEED_%=\n" \
  ".LCY_GET_%=:\n" \
  "v_lshlrev_b32 v6, 16, s55\n" \
  "s_mov_b64 exec, 1\n" \
  "ds_add_u32 v5, v6\n" \
  "s_mov_b64 exec, -1\n" \
  "s_mov_b32 m0, s56\n" \
  "s_add_u32 s71, s56, 4096\n" \
  "global_load_lds_dwordx4 v2, s[48:49] offset:0 sc1\n" \
  "global_load_lds_dwordx4 v2, s[48:49] offset:1024 sc1\n" \
  "global_load_lds_dwordx4 v2, s[48:49] offset:2048 sc1\n" \
  "global_load_lds_dwordx4 v2, s[48:49] offset:3072 sc1\n" \
  "s_mov_b32 m0, s71\n" \
  "s_add_u32 s56, s56, s65\n" \
  "global_load_lds_dwordx4 v3, s[48:49] offset:0 sc1\n" \
  "global_load_lds_dwordx4 v3, s[48:49] offset:1024 sc1\n" \
  "global_load_lds_dwordx4 v3, s[48:49] offset:2048 sc1\n" \
  "global_load_lds_dwordx4 v3, s[48:49] offset:3072 sc1\n" \
  "s_add_u32 s48, s48, 0x80000\n" \
  "s_addc_u32 s49, s49, 0\n" \
  "s_mov_b32 m0, s57\n" \
  "s_add_u32 s57, s57, s65\n" \
  "global_load_lds_dwordx4 v2, s[50:51] offset:0 sc1\n" \
  "global_load_lds_dwordx4 v2, s[50:51] offset:1024 sc1\n" \
  "s_sub_u32 s65, 0, s65\n" \
  "s_add_u32 s67, s67, 4096\n" \
  "s_bitset0_b32 s48, s77\n" \
  "global_load_lds_dwordx4 v2, s[50:51] offset:2048 sc1\n" \
  "s_sub_u32 s68, s68, 1\n" \
  "s_mov_b32 s69, 1\n" \
  "global_load_lds_dwordx4 v2, s[50:51] offset:3072 sc1\n" \
  "s_add_u32 s50, s50, 0x40000\n" \
  "s_addc_u32 s51, s51, 0\n" \
  "s_bitset0_b32 s50, s78\n" \
  "v_mov_b32 v4, 0\n" \
  "buffer_load_dword v4, v60, s[44:47], s67 offen sc1\n" \
  "s_mov_b32 s61, 0\n" \
  "s_cmp_eq_u32 s68, 0\n" \
  "s_cbranch_scc1 .LCY_BAR_%=\n" \
  "s_waitcnt vmcnt(0)\n" \
  "v_mov_b32 v6, s55\n" \
  "s_mov_b64 exec, 1\n" \
  "ds_add_u32 v5, v6\n" \
  "s_mov_b64 exec, -1\n" \
  "s_mov_b32 s69, 0\n" \
  "s_branch .LCY_NEED_%=\n" \
  ".LCY_END_%=:\n" \
  "s_endpgm\n" \
  ".LCY_ALT_%=:\n" \
  "ds_read_b128 v[8:11], v0 offset:12288\n" \
  "s_waitcnt lgkmcnt(3)\n" \
  "v_mfma_f32_16x16x32_bf16 v[32:35], v[68:71], v[12:15], v[32:35]\n" \
  "v_mfma_f32_16x16x32_bf16 v[36:39], v[100:103], v[12:15], v[36:39]\n" \
  "s_waitcnt vmcnt(3)\n" \
  "v_cmp_eq_u32 vcc, 0, v4\n" \
  "v_mfma_f32_16x16x32_bf16 v[40:43], v[132:135], v[12:15], v[40:43]\n" \
  "s_cbranch_vccnz .LCY_D10_%=\n" \
  "v_lshlrev_b32 v6, 16, s55\n" \
  "s_mov_b64 exec, 1\n" \
  "ds_add_u32 v5, v6\n" \
  "s_mov_b64 exec, -1\n" \
  "s_mov_b32 m0, s56\n" \
  "s_add_u32 s71, s56, 4096\n" \
  "global_load_lds_dwordx4 v2, s[48:49] offset:0 sc1\n" \
  ".LCY_D10_%=:\n" \
  "v_mfma_f32_16x16x32_bf16 v[44:47], v[164:167], v[12:15], v[44:47]\n" \
  "v_mfma_f32_16x16x32_bf16 v[48:51], v[196:199], v[12:15], v[48:51]\n" \
  "v_mfma_f32_16x16x32_bf16 v[52:55], v[228:231], v[12:15], v[52:55]\n" \
  "s_cbranch_vccnz .LCY_D11_%=\n" \
  "global_load_lds_dwordx4 v2, s[48:49] offset:1024 sc1\n" \
  ".LCY_D11_%=:\n" \
  "ds_read_b128 v[12:15], v0 offset:13312\n" \
  "s_waitcnt lgkmcnt(3)\n" \
  "v_mfma_f32_16x16x32_bf16 v[32:35], v[72:75], v[16:19], v[32:35]\n" \
  "v_mfma_f32_16x16x32_bf16 v[36:39], v[104:107], v[16:19], v[36:39]\n" \
  "v_mfma_f32_16x16x32_bf16 v[40:43], v[136:139], v[16:19], v[40:43]\n" \
  "s_cbranch_vccnz .LCY_D12_%=\n" \
  "global_load_lds_dwordx4 v2, s[48:49] offset:2048 sc1\n" \
  ".LCY_D12_%=:\n" \
  "v_mfma_f32_16x16x32_bf16 v[44:47], v[168:171], v[16:19], v[44:47]\n" \
  "v_mfma_f32_16x16x32_bf16 v[48:51], v[200:203], v[16:19], v[48:51]\n" \
  "v_mfma_f32_16x16x32_bf16 v[52:55], v[232:235], v[16:19], v[52:55]\n" \
  "s_cbranch_vccnz .LCY_D13_%=\n" \
  "global_load_lds_dwordx4 v2, s[48:49] offset:3072 sc1\n" \
  ".LCY_D13_%=:\n" \
  "ds_read_b128 v[16:19], v0 offset:14336\n" \
  "s_waitcnt lgkmcnt(3)\n" \
  "v_mfma_f32_16x16x32_bf16 v[32:35], v[76:79], v[20:23], v[32:35]\n" \
  "v_mfma_f32_16x16x32_bf16 v[36:39], v[108:111], v[20:23], v[36:39]\n" \
  "v_mfma_f32_16x16x32_bf16 v[40:43], v[140:143], v[20:23], v[40:43]\n" \
  "s_cbranch_vccnz .LCY_D14_%=\n" \
  "s_mov_b32 m0, s71\n" \
  "s_add_u32 s56, s56, s65\n" \
  "global_load_lds_dwordx4 v3, s[48:49] offset:0 sc1\n" \
  ".LCY_D14_%=:\n" \
  "v_mfma_f32_16x16x32_bf16 v[44:47], v[172:175], v[20:23], v[44:47]\n" \
  "v_mfma_f32_16x16x32_bf16 v[48:51], v[204:207], v[20:23], v[48:51]\n" \
  "v_mfma_f32_16x16x32_bf16 v[52:55], v[236:239], v[20:23], v[52:55]\n" \
  "s_cbranch_vccnz .LCY_D15_%=\n" \
  "global_load_lds_dwordx4 v3, s[48:49] offset:1024 sc1\n" \
  ".LCY_D15_%=:\n" \
  "ds_read_b128 v[20:23], v0 offset:15360\n" \
  "s_waitcnt lgkmcnt(3)\n" \
  "v_mfma_f32_16x16x32_bf16 v[32:35], v[80:83], v[8:11], v[32:35]\n" \
  "v_mfma_f32_16x16x32_bf16 v[36:39], v[112:115], v[8:11], v[36:39]\n" \
  "v_mfma_f32_16x16x32_bf16 v[40:43], v[144:147], v[8:11], v[40:43]\n" \
  "s_cbranch_vccnz .LCY_D16_%=\n" \
  "global_load_lds_dwordx4 v3, s[48:49] offset:2048 sc1\n" \
  ".LCY_D16_%=:\n" \
  "v_mfma_f32_16x16x32_bf16 v[44:47], v[176:179], v[8:11], v[44:47]\n" \
  "v_mfma_f32_16x16x32_bf16 v[48:51], v[208:211], v[8:11], v[48:51]\n" \
  "v_mfma_f32_16x16x32_bf16 v[52:55], v[240:243], v[8:11], v[52:55]\n" \
  "s_cbranch_vccnz .LCY_D17_%=\n" \
  "global_load_lds_dwordx4 v3, s[48:49] offset:3072 sc1\n" \
  "s_add_u32 s48, s48, 0x80000\n" \
  "s_addc_u32 s49, s49, 0\n" \
  ".LCY_D17_%=:\n" \
  "s_waitcnt lgkmcnt(2)\n" \
  "v_mfma_f32_16x16x32_bf16 v[32:35], v[84:87], v[12:15], v[32:35]\n" \
  "v_mfma_f32_16x16x32_bf16 v[36:39], v[116:119], v[12:15], v[36:39]\n" \
  "v_mfma_f32_16x16x32_bf16 v[40:43], v[148:151], v[12:15], v[40:43]\n" \
  "s_cbranch_vccnz .LCY_D18_%=\n" \
  "s_mov_b32 m0, s57\n" \
  "s_add_u32 s57, s57, s65\n" \
  "global_load_lds_dwordx4 v2, s[50:51] offset:0 sc1\n" \
  ".LCY_D18_%=:\n" \
  "v_mfma_f32_16x16x32_bf16 v[44:47], v[180:183], v[12:15], v[44:47]\n" \
  "v_mfma_f32_16x16x32_bf16 v[48:51], v[212:215], v[12:15], v[48:51]\n" \
  "v_mfma_f32_16x16x32_bf16 v[52:55], v[244:247], v[12:15], v[52:55]\n" \
  "s_cbranch_vccnz .LCY_D19_%=\n" \
  "global_load_lds_dwordx4 v2, s[50:51] offset:1024 sc1\n" \
  "s_sub_u32 s65, 0, s65\n" \
  "s_add_u32 s67, s67, 4096\n" \
  "s_bitset0_b32 s48, s77\n" \
  ".LCY_D19_%=:\n" \
  "s_waitcnt lgkmcnt(1)\n" \
  "v_mfma_f32_16x16x32_bf16 v[32:35], v[88:91], v[16:19], v[32:35]\n" \
  "v_mfma_f32_16x16x32_bf16 v[36:39], v[120:123], v[16:19], v[36:39]\n" \
  "v_mfma_f32_16x16x32_bf16 v[40:43], v[152:155], v[16:19], v[40:43]\n" \
  "s_cbranch_vccnz .LCY_D20_%=\n" \
  "global_load_lds_dwordx4 v2, s[50:51] offset:2048 sc1\n" \
  "s_sub_u32 s68, s68, 1\n" \
  "s_mov_b32 s69, 1\n" \
  ".LCY_D20_%=:\n" \
  "v_mfma_f32_16x16x32_bf16 v[44:47], v[184:187], v[16:19], v[44:47]\n" \
  "v_mfma_f32_16x16x32_bf16 v[48:51], v[216:219], v[16:19], v[48:51]\n" \
  "v_mfma_f32_16x16x32_bf16 v[52:55], v[248:251], v[16:19], v[52:55]\n" \
  "s_cbranch_vccnz .LCY_D21_%=\n" \
  "global_load_lds_dwordx4 v2, s[50:51] offset:3072 sc1\n" \
  "s_add_u32 s50, s50, 0x40000\n" \
  "s_addc_u32 s51, s51, 0\n" \
  "s_bitset0_b32 s50, s78\n" \
  "v_mov_b32 v4, 0\n" \
  "buffer_load_dword v4, v60, s[44:47], s67 offen sc1\n" \
  ".LCY_D21_%=:\n" \
  "s_waitcnt lgkmcnt(0)\n" \
  "v_mfma_f32_16x16x32_bf16 v[32:35], v[92:95], v[20:23], v[32:35]\n" \
  "v_mfma_f32_16x16x32_bf16 v[36:39], v[124:127], v[20:23], v[36:39]\n" \
  "v_mfma_f32_16x16x32_bf16 v[40:43], v[156:159], v[20:23], v[40:43]\n" \
  "v_mfma_f32_16x16x32_bf16 v[44:47], v[188:191], v[20:23], v[44:47]\n" \
  "v_mfma_f32_16x16x32_bf16 v[48:51], v[220:223], v[20:23], v[48:51]\n" \
  "v_mfma_f32_16x16x32_bf16 v[52:55], v[252:255], v[20:23], v[52:55]\n" \
  "s_branch .LCY_JOIN_%=\n"
template <bool AT>
__device__ __forceinline__ void role_aw_t(const MegaArgs& A, char* smem, const int L, const int a, const u64 tag) {
  const int w = A.wv;
  const int l = lane_id(), c = l & 15, g = l >> 4;
  const int qt = w & 1, hh = (w >> 1) & 1, h = 2 * a + hh;
  char* ws = A.ws;
  unsigned* FL = (unsigned*)(ws + OFF_FL);
  const unsigned sb = lds_base(smem);
  if (w == 0 && l < 8) reinterpret_cast<unsigned*>(smem + AW_CNT)[l] = 0u;
  constexpr int NP = AT ? 1 : 3;
  const int cp0 = AT ? w : 3 * w - 8;
  v8u wo[2 * NP][4];
  {
    const int q = (l >> 2) & 3, p = l & 3, f = q | ((g & 1) << 2);
    const unsigned ra = sb + (4 * g + q) * 256 + (p & 1) * 16;
#define PW_CLEAR(n)                                                                                                         \
  _Pragma("unroll") for (int i = 0; i < 16; ++i)                                                                            \
    ST128((v4u{0u, 0u, 0u, 0u}), MKRS(ws + OFF_PW, SZ_PW), l * 16 + (i & 3) * 1024,                                         \
          ((((n) >> RGB) * 64 + ((n) & ((1 << RGB) - 1))) * 32 + 2 * a + qt) * 16384 + (i >> 2) * 4096);
    if (EF && AT && L && PC0 == 0) { PW_CLEAR(hh) }
#pragma unroll
    for (int r = 0; r < 2; ++r) {
#pragma unroll
      for (int k = 0; k < 2; ++k) {
        const char* base_ = (const char*)A.Wo + ((size_t)((2 * a + r) * DH + 64 * (w & 1) + 32 * k) * DM + 128 * (w >> 1)) * 2;
#pragma unroll
        for (int i = 0; i < 8; ++i) {
          const int fd = g | ((i & 1) << 2);
          glds16(base_, (unsigned)((4 * i + g) * (DM * 2) + ((c ^ (fd << 1)) << 4)), sb + w * 16384 + k * 8192 + i * 1024, WNT & 16);
        }
      }
      SST(4 + 3 * r);
      if (EF && AT && L && PC0 == 1 + r) { PW_CLEAR(hh) }
      if (r == 1) ready_wait(A, tag, ready_look(A));
      if (EF && AT && L && PC0 == 1 && r == 0) WAIT_VM(16);
      else WAIT_VM(0);
      if (EF && AT && r == (PC0 != 0) && L) {
        if (r == 0) ready_wait(A, tag, ready_look(A));
        PW_FREE(0, hh, 2 * a + qt);
      }
      RAW_BAR();
      SST(5 + 3 * r);
#pragma unroll
      for (int k2 = 0; k2 < 2; ++k2) {
#pragma unroll
        for (int ct = 0; ct < 2 * NP; ++ct) {
          const int cp = cp0 + (ct >> 1);
          const unsigned a_ = ra + (cp >> 2) * 32768 + k2 * 16384 + ((2 * (cp & 3) + (p >> 1)) ^ f) * 32 + (ct & 1) * 8;
          const v2u r0 = trd(a_), r1 = trd(a_ + 4096), r2 = trd(a_ + 8192), r3 = trd(a_ + 12288);
          wo[ct][2 * r + k2] = v8u{r0.x, r0.y, r1.x, r1.y, r2.x, r2.y, r3.x, r3.y};
          asm volatile("" : "+v"(wo[ct][2 * r + k2]));
        }
      }
      RAW_BAR();
      SST(6 + 3 * r);
    }
  }
  WSTAMP2();
  if (EF && L) {
    const unsigned t0 = now10();
#pragma unroll 1
    for (int n = 2 + (w >> 1); n < (2 << RGB); n += 4) {
      gate(t0 + PCT + (n - 2) * PCD);
      PW_CLEAR(n)
      DRAIN();
      PW_FREE(n >> RGB, n & ((1 << RGB) - 1), 2 * a + qt);
    }
    if (AWD) gate(t0 + AWD);
  }
  const v4u rsW = mk_srd(ws + OFF_PW, SZ_PW);
  const int dp = (FL_FP - FL_FQ + (L * 16 + a - L * 64 * 32 - h) * 32 + 2 * hh) * 4;
  const v4u rsF = mk_srd(FL + FL_FQ + (L * 64 * 32 + h) * 32, (qt ? 0 : dp) + 64 * 4096 - 128);
  const char* p0 = AT ? ws + OFF_MEMK + L * SZ_MEM + h * 2048 : ws + TOK_QK(ws, L) + ((size_t)h * 16 + 8 * qt) * 1024;
  const char* p1 = AT ? (const char*)(FL + FL_FK + L * 32 + (h >> 3) * 64 + (h & 7) * 2) : ws + TOK_V(ws, L) + ((size_t)h * 8 + 4 * qt) * 1024;
  const unsigned* pFW = EF && AT ? FL + FL_FD + L * 32 : FL + FL_FW + (L * 64 * 16 + (a >> 2)) * 32 + (a & 3) + (EF ? (w & 3) * 128 : 0);
  const unsigned tk = sb + AW_TK + hh * 24 * 1024, voff = (c >> 1) * 2048 + (c & 1) * 64 + g * 16;
  const char* dbg = ws + WS_TOTAL + A.vb * 64 * 8;
#define AW_SOPS                                                                                                                 \
  "{s[40:43]}"(rsW), "{s[44:47]}"(rsF), "{s[48:49]}"(p0), "{s[50:51]}"(p1), "{s[52:53]}"(pFW),                                   \
      "{s54}"((L * 64 * 16 + a) * 32768 + cp0 * 128 + (AT ? qt * 16384 : 0)), "{s55}"(1 << (8 * qt)), "{s56}"(tk + qt * 8192), "{s57}"(tk + 16384 + qt * 4096),                  \
      "{s58}"(sb + AW_CNT + 8 + 4 * hh), "{s59}"(sb + AW_CNT), "{s60}"(NSEG), "{s77}"(L ? 19 + GEN_TR : 1), "{s[36:37]}"(dbg)             \
      : "memory", "vcc", "scc", "s80"
  if constexpr (AT) {
    const v8u pv = {tk + l * 16, (c & 8) ? 0xEEEEu : 0x4444u, (l & 31) * 4u, (unsigned)(OFF_MEMV - OFF_MEMK) + c * 32 + (g & 1) * 16 + (g >> 1) * 1024,
                    voff, (unsigned)l * 16, tk + qt * 4096 + l * 16, sb + AW_SO + w * 4096 + l * 16};
    const unsigned neg = 0xff61b1e6u;
    v8u bt;
#pragma unroll
    for (int i = 0; i < 4; ++i) {
      const unsigned d = 4 * g + i > c ? neg : 0u;
      bt[i] = qt ? 0u : d;
      bt[4 + i] = qt ? d : neg;
    }
    asm volatile("s_mov_b64 exec, -1\ns_waitcnt lgkmcnt(0)\ns_nop 4\n" AW_ASM_X ".LKG_LAST:\n"
                 ::"{v[192:199]}"(wo[0][0]), "{v[200:207]}"(wo[0][1]), "{v[208:215]}"(wo[0][2]), "{v[216:223]}"(wo[0][3]),
                 "{v[224:231]}"(wo[1][0]), "{v[232:239]}"(wo[1][1]), "{v[240:247]}"(wo[1][2]), "{v[248:255]}"(wo[1][3]),
                 "{v[0:7]}"(pv), "{v[8:15]}"(bt), AW_SOPS);
  } else {
    const v4u pv = {sb + AW_SO + l * 16, voff, (unsigned)l * 16, (l & 2) ? 16u + 8 * qt : qt ? 8u * (l & 1) : dp + 4 * (l & 1)};
    asm volatile("s_mov_b64 exec, -1\ns_waitcnt vmcnt(0) lgkmcnt(0)\ns_nop 4\n" AW_ASM_Y
                 ::"{v[64:71]}"(wo[0][0]), "{v[72:79]}"(wo[0][1]), "{v[80:87]}"(wo[0][2]), "{v[88:95]}"(wo[0][3]),
                 "{v[96:103]}"(wo[1][0]), "{v[104:111]}"(wo[1][1]), "{v[112:119]}"(wo[1][2]), "{v[120:127]}"(wo[1][3]),
                 "{v[128:135]}"(wo[2][0]), "{v[136:143]}"(wo[2][1]), "{v[144:151]}"(wo[2][2]), "{v[152:159]}"(wo[2][3]),
                 "{v[160:167]}"(wo[3][0]), "{v[168:175]}"(wo[3][1]), "{v[176:183]}"(wo[3][2]), "{v[184:191]}"(wo[3][3]),
                 "{v[192:199]}"(wo[4][0]), "{v[200:207]}"(wo[4][1]), "{v[208:215]}"(wo[4][2]), "{v[216:223]}"(wo[4][3]),
                 "{v[224:231]}"(wo[5][0]), "{v[232:239]}"(wo[5][1]), "{v[240:247]}"(wo[5][2]), "{v[248:255]}"(wo[5][3]),
                 "{v[0:3]}"(pv), AW_SOPS);
  }
#undef AW_SOPS
}
__device__ __forceinline__ void role_aw(const MegaArgs& A, char* smem, const int L, const int a, const u64 tag) {
  if (A.wv < 4) role_aw_t<true>(A, smem, L, a, tag);
  else role_aw_t<false>(A, smem, L, a, tag);
}

#define P_ASM \
  "s_mov_b32 s68, 0\n" \
  "s_mov_b32 s66, 3\n" \
  "s_mov_b32 s67, 2\n" \
  "s_mov_b32 s72, 0\n" \
  "v_mov_b32 v7, 1\n" \
  "v_mov_b32 v6, s70\n" \
  "v_mov_b32 v5, 0\n" \
  "v_add_u32 v10, 4096, v2\n" \
  ".p2align 6\n" \
  ".LCP_TOP_%=:\n" \
  "ds_read_b128 v[28:31], v1 offset:0\n" \
  "ds_read_b128 v[32:35], v1 offset:1024\n" \
  "s_sub_u32 s73, s64, 1\n" \
  "s_bitset0_b32 s65, s74\n" \
  "s_bfe_u32 s73, s73, 0x10004\n" \
  "s_add_u32 s66, s66, 3\n" \
  "s_add_u32 s67, s67, 2\n" \
  "s_mov_b32 s69, 0x400000\n" \
  "v_xor_b32 v0, 0x8000, v0\n" \
  "v_xor_b32 v3, 0x100, v3\n" \
  "v_readfirstlane_b32 s71, v5\n" \
  "s_cmp_ge_u32 s71, s66\n" \
  "s_cbranch_scc1 .LCP_GO_%=\n" \
  "s_cmp_eq_u32 s68, 1\n" \
  "s_cbranch_scc0 .LCP_X1_%=\n" \
  "s_waitcnt vmcnt(0)\n" \
  "s_mov_b64 exec, 1\n" \
  "buffer_store_dword v7, off, s[56:59], s72 sc1\n" \
  "s_mov_b64 exec, -1\n" \
  "s_add_u32 s72, s72, 0x1000\n" \
  "s_mov_b32 s68, 0\n" \
  ".LCP_X1_%=:\n" \
  ".LCP_S2_%=:\n" \
  "s_sleep 1\n" \
  "ds_read_b32 v5, v6 offset:0\n" \
  "s_sub_u32 s69, s69, 1\n" \
  "s_cbranch_scc1 .LCP_END_%=\n" \
  "s_waitcnt lgkmcnt(0)\n" \
  "v_readfirstlane_b32 s71, v5\n" \
  "s_cmp_ge_u32 s71, s66\n" \
  "s_cbranch_scc1 .LCP_GO_%=\n" \
  "s_branch .LCP_S2_%=\n" \
  ".LCP_GO_%=:\n" \
  "ds_read_b128 v[12:15], v0 offset:0\n" \
  "ds_read_b128 v[16:19], v0 offset:2048\n" \
  "ds_read_b128 v[20:23], v0 offset:4096\n" \
  "ds_read_b128 v[24:27], v0 offset:6144\n" \
  "ds_read_b128 v[36:39], v1 offset:2048\n" \
  "s_waitcnt lgkmcnt(4)\n" \
  "v_mfma_f32_16x16x32_bf16 v[48:51], v[64:67], v[12:15], 0\n" \
  "v_mfma_f32_16x16x32_bf16 v[52:55], v[128:131], v[12:15], 0\n" \
  "v_mfma_f32_16x16x32_bf16 v[56:59], v[192:195], v[12:15], 0\n" \
  "v_mfma_f32_16x16x32_bf16 v[60:63], v[28:31], v[12:15], 0\n" \
  "ds_read_b128 v[12:15], v0 offset:8192\n" \
  "ds_read_b128 v[28:31], v1 offset:3072\n" \
  "s_waitcnt lgkmcnt(5)\n" \
  "v_mfma_f32_16x16x32_bf16 v[48:51], v[68:71], v[16:19], v[48:51]\n" \
  "v_mfma_f32_16x16x32_bf16 v[52:55], v[132:135], v[16:19], v[52:55]\n" \
  "v_mfma_f32_16x16x32_bf16 v[56:59], v[196:199], v[16:19], v[56:59]\n" \
  "v_mfma_f32_16x16x32_bf16 v[60:63], v[32:35], v[16:19], v[60:63]\n" \
  "ds_read_b128 v[16:19], v0 offset:10240\n" \
  "ds_read_b128 v[32:35], v1 offset:4096\n" \
  "s_waitcnt lgkmcnt(6)\n" \
  "v_mfma_f32_16x16x32_bf16 v[48:51], v[72:75], v[20:23], v[48:51]\n" \
  "v_mfma_f32_16x16x32_bf16 v[52:55], v[136:139], v[20:23], v[52:55]\n" \
  "v_mfma_f32_16x16x32_bf16 v[56:59], v[200:203], v[20:23], v[56:59]\n" \
  "s_waitcnt lgkmcnt(4)\n" \
  "v_mfma_f32_16x16x32_bf16 v[60:63], v[36:39], v[20:23], v[60:63]\n" \
  "ds_read_b128 v[20:23], v0 offset:12288\n" \
  "ds_read_b128 v[36:39], v1 offset:5120\n" \
  "v_mfma_f32_16x16x32_bf16 v[48:51], v[76:79], v[24:27], v[48:51]\n" \
  "v_mfma_f32_16x16x32_bf16 v[52:55], v[140:143], v[24:27], v[52:55]\n" \
  "v_mfma_f32_16x16x32_bf16 v[56:59], v[204:207], v[24:27], v[56:59]\n" \
  "s_waitcnt lgkmcnt(4)\n" \
  "v_mfma_f32_16x16x32_bf16 v[60:63], v[28:31], v[24:27], v[60:63]\n" \
  "ds_read_b128 v[24:27], v0 offset:14336\n" \
  "ds_read_b128 v[28:31], v1 offset:6144\n" \
  "v_mfma_f32_16x16x32_bf16 v[48:51], v[80:83], v[12:15], v[48:51]\n" \
  "v_mfma_f32_16x16x32_bf16 v[52:55], v[144:147], v[12:15], v[52:55]\n" \
  "v_mfma_f32_16x16x32_bf16 v[56:59], v[208:211], v[12:15], v[56:59]\n" \
  "s_waitcnt lgkmcnt(4)\n" \
  "v_mfma_f32_16x16x32_bf16 v[60:63], v[32:35], v[12:15], v[60:63]\n" \
  "ds_read_b128 v[12:15], v0 offset:16384\n" \
  "ds_read_b128 v[32:35], v1 offset:7168\n" \
  "v_mfma_f32_16x16x32_bf16 v[48:51], v[84:87], v[16:19], v[48:51]\n" \
  "v_mfma_f32_16x16x32_bf16 v[52:55], v[148:151], v[16:19], v[52:55]\n" \
  "v_mfma_f32_16x16x32_bf16 v[56:59], v[212:215], v[16:19], v[56:59]\n" \
  "s_waitcnt lgkmcnt(4)\n" \
  "v_mfma_f32_16x16x32_bf16 v[60:63], v[36:39], v[16:19], v[60:63]\n" \
  "ds_read_b128 v[16:19], v0 offset:18432\n" \
  "ds_read_b128 v[36:39], v1 offset:8192\n" \
  "v_mfma_f32_16x16x32_bf16 v[48:51], v[88:91], v[20:23], v[48:51]\n" \
  "v_mfma_f32_16x16x32_bf16 v[52:55], v[152:155], v[20:23], v[52:55]\n" \
  "v_mfma_f32_16x16x32_bf16 v[56:59], v[216:219], v[20:23], v[56:59]\n" \
  "s_waitcnt lgkmcnt(4)\n" \
  "v_mfma_f32_16x16x32_bf16 v[60:63], v[28:31], v[20:23], v[60:63]\n" \
  "ds_read_b128 v[20:23], v0 offset:20480\n" \
  "ds_read_b128 v[28:31], v1 offset:9216\n" \
  "v_mfma_f32_16x16x32_bf16 v[48:51], v[92:95], v[24:27], v[48:51]\n" \
  "v_mfma_f32_16x16x32_bf16 v[52:55], v[156:159], v[24:27], v[52:55]\n" \
  "v_mfma_f32_16x16x32_bf16 v[56:59], v[220:223], v[24:27], v[56:59]\n" \
  "s_waitcnt lgkmcnt(4)\n" \
  "v_mfma_f32_16x16x32_bf16 v[60:63], v[32:35], v[24:27], v[60:63]\n" \
  "ds_read_b128 v[24:27], v0 offset:22528\n" \
  "ds_read_b128 v[32:35], v1 offset:10240\n" \
  "v_mfma_f32_16x16x32_bf16 v[48:51], v[96:99], v[12:15], v[48:51]\n" \
  "v_mfma_f32_16x16x32_bf16 v[52:55], v[160:163], v[12:15], v[52:55]\n" \
  "v_mfma_f32_16x16x32_bf16 v[56:59], v[224:227], v[12:15], v[56:59]\n" \
  "s_waitcnt lgkmcnt(4)\n" \
  "v_mfma_f32_16x16x32_bf16 v[60:63], v[36:39], v[12:15], v[60:63]\n" \
  "ds_read_b128 v[12:15], v0 offset:24576\n" \
  "ds_read_b128 v[36:39], v1 offset:11264\n" \
  "v_mfma_f32_16x16x32_bf16 v[48:51], v[100:103], v[16:19], v[48:51]\n" \
  "v_mfma_f32_16x16x32_bf16 v[52:55], v[164:167], v[16:19], v[52:55]\n" \
  "v_mfma_f32_16x16x32_bf16 v[56:59], v[228:231], v[16:19], v[56:59]\n" \
  "s_waitcnt lgkmcnt(4)\n" \
  "v_mfma_f32_16x16x32_bf16 v[60:63], v[28:31], v[16:19], v[60:63]\n" \
  "ds_read_b32 v5, v6 offset:16\n" \
  "ds_read_b128 v[16:19], v0 offset:26624\n" \
  "ds_read_b128 v[28:31], v1 offset:12288\n" \
  "v_mfma_f32_16x16x32_bf16 v[48:51], v[104:107], v[20:23], v[48:51]\n" \
  "v_mfma_f32_16x16x32_bf16 v[52:55], v[168:171], v[20:23], v[52:55]\n" \
  "v_mfma_f32_16x16x32_bf16 v[56:59], v[232:235], v[20:23], v[56:59]\n" \
  "s_waitcnt lgkmcnt(5)\n" \
  "v_mfma_f32_16x16x32_bf16 v[60:63], v[32:35], v[20:23], v[60:63]\n" \
  "ds_read_b128 v[20:23], v0 offset:28672\n" \
  "ds_read_b128 v[32:35], v1 offset:13312\n" \
  "v_mfma_f32_16x16x32_bf16 v[48:51], v[108:111], v[24:27], v[48:51]\n" \
  "v_mfma_f32_16x16x32_bf16 v[52:55], v[172:175], v[24:27], v[52:55]\n" \
  "v_mfma_f32_16x16x32_bf16 v[56:59], v[236:239], v[24:27], v[56:59]\n" \
  "s_waitcnt lgkmcnt(5)\n" \
  "v_mfma_f32_16x16x32_bf16 v[60:63], v[36:39], v[24:27], v[60:63]\n" \
  "s_cmp_eq_u32 s68, 1\n" \
  "s_cbranch_scc0 .LCP_X3_%=\n" \
  "s_waitcnt vmcnt(0)\n" \
  "s_mov_b64 exec, 1\n" \
  "buffer_store_dword v7, off, s[56:59], s72 sc1\n" \
  "s_mov_b64 exec, -1\n" \
  "s_add_u32 s72, s72, 0x1000\n" \
  "s_mov_b32 s68, 0\n" \
  ".LCP_X3_%=:\n" \
  "s_waitcnt lgkmcnt(4)\n" \
  "v_readfirstlane_b32 s71, v5\n" \
  "s_cmp_ge_u32 s71, s67\n" \
  "s_cbranch_scc1 .LCP_ST_%=\n" \
  ".LCP_S4_%=:\n" \
  "s_sleep 1\n" \
  "ds_read_b32 v5, v6 offset:16\n" \
  "s_sub_u32 s69, s69, 1\n" \
  "s_cbranch_scc1 .LCP_END_%=\n" \
  "s_waitcnt lgkmcnt(0)\n" \
  "v_readfirstlane_b32 s71, v5\n" \
  "s_cmp_ge_u32 s71, s67\n" \
  "s_cbranch_scc1 .LCP_ST_%=\n" \
  "s_branch .LCP_S4_%=\n" \
  ".LCP_ST_%=:\n" \
  "ds_read_b128 v[24:27], v0 offset:30720\n" \
  "ds_read_b128 v[36:39], v1 offset:14336\n" \
  "ds_read_b128 v[40:43], v4 offset:0\n" \
  "ds_read_b128 v[44:47], v4 offset:64\n" \
  "ds_read2_b32 v[8:9], v3 offset0:0 offset1:32\n" \
  "v_mfma_f32_16x16x32_bf16 v[48:51], v[112:115], v[12:15], v[48:51]\n" \
  "v_mfma_f32_16x16x32_bf16 v[52:55], v[176:179], v[12:15], v[52:55]\n" \
  "v_mfma_f32_16x16x32_bf16 v[56:59], v[240:243], v[12:15], v[56:59]\n" \
  "s_waitcnt lgkmcnt(7)\n" \
  "v_mfma_f32_16x16x32_bf16 v[60:63], v[28:31], v[12:15], v[60:63]\n" \
  "ds_read_b128 v[12:15], v0 offset:256\n" \
  "ds_read_b128 v[28:31], v1 offset:15360\n" \
  "v_mfma_f32_16x16x32_bf16 v[48:51], v[116:119], v[16:19], v[48:51]\n" \
  "v_mfma_f32_16x16x32_bf16 v[52:55], v[180:183], v[16:19], v[52:55]\n" \
  "v_mfma_f32_16x16x32_bf16 v[56:59], v[244:247], v[16:19], v[56:59]\n" \
  "s_waitcnt lgkmcnt(7)\n" \
  "v_mfma_f32_16x16x32_bf16 v[60:63], v[32:35], v[16:19], v[60:63]\n" \
  "ds_read_b128 v[16:19], v0 offset:2304\n" \
  "ds_read_b128 v[32:35], v4 offset:128\n" \
  "v_mfma_f32_16x16x32_bf16 v[48:51], v[120:123], v[20:23], v[48:51]\n" \
  "s_waitcnt lgkmcnt(4)\n" \
  "v_mul_f32 v40, v40, v8\n" \
  "v_mul_f32 v41, v41, v8\n" \
  "v_mfma_f32_16x16x32_bf16 v[52:55], v[184:187], v[20:23], v[52:55]\n" \
  "v_mul_f32 v42, v42, v8\n" \
  "v_mul_f32 v43, v43, v8\n" \
  "v_mfma_f32_16x16x32_bf16 v[56:59], v[248:251], v[20:23], v[56:59]\n" \
  "v_mul_f32 v44, v44, v8\n" \
  "v_mul_f32 v45, v45, v8\n" \
  "v_mfma_f32_16x16x32_bf16 v[60:63], v[36:39], v[20:23], v[60:63]\n" \
  "v_mul_f32 v46, v46, v8\n" \
  "v_mul_f32 v47, v47, v8\n" \
  "ds_read_b128 v[20:23], v0 offset:4352\n" \
  "ds_read_b128 v[36:39], v4 offset:192\n" \
  "v_mfma_f32_16x16x32_bf16 v[48:51], v[124:127], v[24:27], v[48:51]\n" \
  "v_mfma_f32_16x16x32_bf16 v[52:55], v[188:191], v[24:27], v[52:55]\n" \
  "v_mfma_f32_16x16x32_bf16 v[56:59], v[252:255], v[24:27], v[56:59]\n" \
  "s_waitcnt lgkmcnt(2)\n" \
  "v_mul_f32 v32, v32, v8\n" \
  "v_mul_f32 v33, v33, v8\n" \
  "v_mfma_f32_16x16x32_bf16 v[60:63], v[28:31], v[24:27], v[60:63]\n" \
  "v_mul_f32 v34, v34, v8\n" \
  "v_mul_f32 v35, v35, v8\n" \
  "ds_read_b128 v[28:31], v1 offset:0\n" \
  "ds_read_b128 v[24:27], v0 offset:6400\n" \
  "v_fmac_f32 v40, v48, v9\n" \
  "v_fmac_f32 v41, v49, v9\n" \
  "v_fmac_f32 v42, v50, v9\n" \
  "v_fmac_f32 v43, v51, v9\n" \
  "v_mfma_f32_16x16x32_bf16 v[48:51], v[64:67], v[12:15], 0\n" \
  "v_fmac_f32 v44, v52, v9\n" \
  "v_fmac_f32 v45, v53, v9\n" \
  "v_fmac_f32 v46, v54, v9\n" \
  "v_fmac_f32 v47, v55, v9\n" \
  "v_mfma_f32_16x16x32_bf16 v[52:55], v[128:131], v[12:15], 0\n" \
  "v_cvt_pk_bf16_f32 v40, v40, v41\n" \
  "v_cvt_pk_bf16_f32 v41, v42, v43\n" \
  "v_cvt_pk_bf16_f32 v42, v44, v45\n" \
  "v_cvt_pk_bf16_f32 v43, v46, v47\n" \
  "v_and_or_b32 v40, v40, -2, s73\n" \
  "v_fmac_f32 v32, v56, v9\n" \
  "v_fmac_f32 v33, v57, v9\n" \
  "v_fmac_f32 v34, v58, v9\n" \
  "v_fmac_f32 v35, v59, v9\n" \
  "v_mfma_f32_16x16x32_bf16 v[56:59], v[192:195], v[12:15], 0\n" \
  "s_waitcnt lgkmcnt(2)\n" \
  "v_mul_f32 v36, v36, v8\n" \
  "v_mul_f32 v37, v37, v8\n" \
  "v_mul_f32 v38, v38, v8\n" \
  "v_mul_f32 v39, v39, v8\n" \
  "v_fmac_f32 v36, v60, v9\n" \
  "v_fmac_f32 v37, v61, v9\n" \
  "v_fmac_f32 v38, v62, v9\n" \
  "v_fmac_f32 v39, v63, v9\n" \
  "s_waitcnt lgkmcnt(1)\n" \
  "v_mfma_f32_16x16x32_bf16 v[60:63], v[28:31], v[12:15], 0\n" \
  "v_cvt_pk_bf16_f32 v44, v32, v33\n" \
  "v_cvt_pk_bf16_f32 v45, v34, v35\n" \
  "v_cvt_pk_bf16_f32 v46, v36, v37\n" \
  "v_cvt_pk_bf16_f32 v47, v38, v39\n" \
  "v_and_or_b32 v44, v44, -2, s73\n" \
  "ds_read_b128 v[32:35], v1 offset:1024\n" \
  "ds_read_b128 v[36:39], v1 offset:2048\n" \
  "ds_read_b128 v[12:15], v0 offset:8448\n" \
  "ds_read_b128 v[28:31], v1 offset:3072\n" \
  "v_mfma_f32_16x16x32_bf16 v[48:51], v[68:71], v[16:19], v[48:51]\n" \
  "buffer_store_dwordx4 v[40:43], v2, s[40:43], s65 offen offset:0 sc1\n" \
  "v_mfma_f32_16x16x32_bf16 v[52:55], v[132:135], v[16:19], v[52:55]\n" \
  "buffer_store_dwordx4 v[44:47], v2, s[40:43], s65 offen offset:1024 sc1\n" \
  "v_mfma_f32_16x16x32_bf16 v[56:59], v[196:199], v[16:19], v[56:59]\n" \
  "s_waitcnt lgkmcnt(3)\n" \
  "v_mfma_f32_16x16x32_bf16 v[60:63], v[32:35], v[16:19], v[60:63]\n" \
  "ds_read_b128 v[16:19], v0 offset:10496\n" \
  "ds_read_b128 v[32:35], v1 offset:4096\n" \
  "v_mfma_f32_16x16x32_bf16 v[48:51], v[72:75], v[20:23], v[48:51]\n" \
  "v_mfma_f32_16x16x32_bf16 v[52:55], v[136:139], v[20:23], v[52:55]\n" \
  "v_mfma_f32_16x16x32_bf16 v[56:59], v[200:203], v[20:23], v[56:59]\n" \
  "s_waitcnt lgkmcnt(4)\n" \
  "v_mfma_f32_16x16x32_bf16 v[60:63], v[36:39], v[20:23], v[60:63]\n" \
  "ds_read_b128 v[20:23], v0 offset:12544\n" \
  "ds_read_b128 v[36:39], v1 offset:5120\n" \
  "v_mfma_f32_16x16x32_bf16 v[48:51], v[76:79], v[24:27], v[48:51]\n" \
  "v_mfma_f32_16x16x32_bf16 v[52:55], v[140:143], v[24:27], v[52:55]\n" \
  "v_mfma_f32_16x16x32_bf16 v[56:59], v[204:207], v[24:27], v[56:59]\n" \
  "s_waitcnt lgkmcnt(4)\n" \
  "v_mfma_f32_16x16x32_bf16 v[60:63], v[28:31], v[24:27], v[60:63]\n" \
  "ds_read_b128 v[24:27], v0 offset:14592\n" \
  "ds_read_b128 v[28:31], v1 offset:6144\n" \
  "v_mfma_f32_16x16x32_bf16 v[48:51], v[80:83], v[12:15], v[48:51]\n" \
  "v_mfma_f32_16x16x32_bf16 v[52:55], v[144:147], v[12:15], v[52:55]\n" \
  "v_mfma_f32_16x16x32_bf16 v[56:59], v[208:211], v[12:15], v[56:59]\n" \
  "s_waitcnt lgkmcnt(4)\n" \
  "v_mfma_f32_16x16x32_bf16 v[60:63], v[32:35], v[12:15], v[60:63]\n" \
  "ds_read_b128 v[12:15], v0 offset:16640\n" \
  "ds_read_b128 v[32:35], v1 offset:7168\n" \
  "v_mfma_f32_16x16x32_bf16 v[48:51], v[84:87], v[16:19], v[48:51]\n" \
  "v_mfma_f32_16x16x32_bf16 v[52:55], v[148:151], v[16:19], v[52:55]\n" \
  "v_mfma_f32_16x16x32_bf16 v[56:59], v[212:215], v[16:19], v[56:59]\n" \
  "s_waitcnt lgkmcnt(4)\n" \
  "v_mfma_f32_16x16x32_bf16 v[60:63], v[36:39], v[16:19], v[60:63]\n" \
  "ds_read_b128 v[16:19], v0 offset:18688\n" \
  "ds_read_b128 v[36:39], v1 offset:8192\n" \
  "v_mfma_f32_16x16x32_bf16 v[48:51], v[88:91], v[20:23], v[48:51]\n" \
  "v_mfma_f32_16x16x32_bf16 v[52:55], v[152:155], v[20:23], v[52:55]\n" \
  "v_mfma_f32_16x16x32_bf16 v[56:59], v[216:219], v[20:23], v[56:59]\n" \
  "s_waitcnt lgkmcnt(4)\n" \
  "v_mfma_f32_16x16x32_bf16 v[60:63], v[28:31], v[20:23], v[60:63]\n" \
  "ds_read_b128 v[20:23], v0 offset:20736\n" \
  "ds_read_b128 v[28:31], v1 offset:9216\n" \
  "v_mfma_f32_16x16x32_bf16 v[48:51], v[92:95], v[24:27], v[48:51]\n" \
  "v_mfma_f32_16x16x32_bf16 v[52:55], v[156:159], v[24:27], v[52:55]\n" \
  "v_mfma_f32_16x16x32_bf16 v[56:59], v[220:223], v[24:27], v[56:59]\n" \
  "s_waitcnt lgkmcnt(4)\n" \
  "v_mfma_f32_16x16x32_bf16 v[60:63], v[32:35], v[24:27], v[60:63]\n" \
  "ds_read_b128 v[24:27], v0 offset:22784\n" \
  "ds_read_b128 v[32:35], v1 offset:10240\n" \
  "v_mfma_f32_16x16x32_bf16 v[48:51], v[96:99], v[12:15], v[48:51]\n" \
  "v_mfma_f32_16x16x32_bf16 v[52:55], v[160:163], v[12:15], v[52:55]\n" \
  "v_mfma_f32_16x16x32_bf16 v[56:59], v[224:227], v[12:15], v[56:59]\n" \
  "s_waitcnt lgkmcnt(4)\n" \
  "v_mfma_f32_16x16x32_bf16 v[60:63], v[36:39], v[12:15], v[60:63]\n" \
  "ds_read_b128 v[12:15], v0 offset:24832\n" \
  "ds_read_b128 v[36:39], v1 offset:11264\n" \
  "v_mfma_f32_16x16x32_bf16 v[48:51], v[100:103], v[16:19], v[48:51]\n" \
  "v_mfma_f32_16x16x32_bf16 v[52:55], v[164:167], v[16:19], v[52:55]\n" \
  "v_mfma_f32_16x16x32_bf16 v[56:59], v[228:231], v[16:19], v[56:59]\n" \
  "s_waitcnt lgkmcnt(4)\n" \
  "v_mfma_f32_16x16x32_bf16 v[60:63], v[28:31], v[16:19], v[60:63]\n" \
  "ds_read_b128 v[16:19], v0 offset:26880\n" \
  "ds_read_b128 v[28:31], v1 offset:12288\n" \
  "v_mfma_f32_16x16x32_bf16 v[48:51], v[104:107], v[20:23], v[48:51]\n" \
  "v_mfma_f32_16x16x32_bf16 v[52:55], v[168:171], v[20:23], v[52:55]\n" \
  "v_mfma_f32_16x16x32_bf16 v[56:59], v[232:235], v[20:23], v[56:59]\n" \
  "s_waitcnt lgkmcnt(4)\n" \
  "v_mfma_f32_16x16x32_bf16 v[60:63], v[32:35], v[20:23], v[60:63]\n" \
  "ds_read_b128 v[20:23], v0 offset:28928\n" \
  "ds_read_b128 v[32:35], v1 offset:13312\n" \
  "v_mfma_f32_16x16x32_bf16 v[48:51], v[108:111], v[24:27], v[48:51]\n" \
  "v_mfma_f32_16x16x32_bf16 v[52:55], v[172:175], v[24:27], v[52:55]\n" \
  "v_mfma_f32_16x16x32_bf16 v[56:59], v[236:239], v[24:27], v[56:59]\n" \
  "s_waitcnt lgkmcnt(4)\n" \
  "v_mfma_f32_16x16x32_bf16 v[60:63], v[36:39], v[24:27], v[60:63]\n" \
  "ds_read_b128 v[24:27], v0 offset:30976\n" \
  "ds_read_b128 v[36:39], v1 offset:14336\n" \
  "ds_read_b128 v[40:43], v4 offset:0\n" \
  "ds_read_b128 v[44:47], v4 offset:64\n" \
  "ds_read2_b32 v[8:9], v3 offset0:16 offset1:48\n" \
  "v_mfma_f32_16x16x32_bf16 v[48:51], v[112:115], v[12:15], v[48:51]\n" \
  "v_mfma_f32_16x16x32_bf16 v[52:55], v[176:179], v[12:15], v[52:55]\n" \
  "v_mfma_f32_16x16x32_bf16 v[56:59], v[240:243], v[12:15], v[56:59]\n" \
  "s_waitcnt lgkmcnt(7)\n" \
  "v_mfma_f32_16x16x32_bf16 v[60:63], v[28:31], v[12:15], v[60:63]\n" \
  "ds_read_b128 v[28:31], v1 offset:15360\n" \
  "v_mfma_f32_16x16x32_bf16 v[48:51], v[116:119], v[16:19], v[48:51]\n" \
  "v_mfma_f32_16x16x32_bf16 v[52:55], v[180:183], v[16:19], v[52:55]\n" \
  "v_mfma_f32_16x16x32_bf16 v[56:59], v[244:247], v[16:19], v[56:59]\n" \
  "s_waitcnt lgkmcnt(6)\n" \
  "v_mfma_f32_16x16x32_bf16 v[60:63], v[32:35], v[16:19], v[60:63]\n" \
  "ds_read_b128 v[32:35], v4 offset:128\n" \
  "v_mfma_f32_16x16x32_bf16 v[48:51], v[120:123], v[20:23], v[48:51]\n" \
  "s_waitcnt lgkmcnt(2)\n" \
  "v_mul_f32 v40, v40, v8\n" \
  "v_mul_f32 v41, v41, v8\n" \
  "v_mfma_f32_16x16x32_bf16 v[52:55], v[184:187], v[20:23], v[52:55]\n" \
  "v_mul_f32 v42, v42, v8\n" \
  "v_mul_f32 v43, v43, v8\n" \
  "v_mfma_f32_16x16x32_bf16 v[56:59], v[248:251], v[20:23], v[56:59]\n" \
  "v_mul_f32 v44, v44, v8\n" \
  "v_mul_f32 v45, v45, v8\n" \
  "v_mfma_f32_16x16x32_bf16 v[60:63], v[36:39], v[20:23], v[60:63]\n" \
  "v_mul_f32 v46, v46, v8\n" \
  "v_mul_f32 v47, v47, v8\n" \
  "ds_read_b128 v[36:39], v4 offset:192\n" \
  "s_mov_b64 exec, 1\n" \
  "ds_add_u32 v6, v7 offset:8\n" \
  "s_mov_b64 exec, -1\n" \
  "v_xor_b32 v6, 4, v6\n" \
  "v_mfma_f32_16x16x32_bf16 v[48:51], v[124:127], v[24:27], v[48:51]\n" \
  "v_mfma_f32_16x16x32_bf16 v[52:55], v[188:191], v[24:27], v[52:55]\n" \
  "v_mfma_f32_16x16x32_bf16 v[56:59], v[252:255], v[24:27], v[56:59]\n" \
  "s_waitcnt lgkmcnt(2)\n" \
  "v_mul_f32 v32, v32, v8\n" \
  "v_mul_f32 v33, v33, v8\n" \
  "v_mfma_f32_16x16x32_bf16 v[60:63], v[28:31], v[24:27], v[60:63]\n" \
  "v_mul_f32 v34, v34, v8\n" \
  "v_mul_f32 v35, v35, v8\n" \
  "v_fmac_f32 v40, v48, v9\n" \
  "v_fmac_f32 v41, v49, v9\n" \
  "v_fmac_f32 v42, v50, v9\n" \
  "v_fmac_f32 v43, v51, v9\n" \
  "v_fmac_f32 v44, v52, v9\n" \
  "v_fmac_f32 v45, v53, v9\n" \
  "v_fmac_f32 v46, v54, v9\n" \
  "v_fmac_f32 v47, v55, v9\n" \
  "ds_read_b32 v5, v6 offset:0\n" \
  "v_cvt_pk_bf16_f32 v40, v40, v41\n" \
  "v_cvt_pk_bf16_f32 v41, v42, v43\n" \
  "v_cvt_pk_bf16_f32 v42, v44, v45\n" \
  "v_cvt_pk_bf16_f32 v43, v46, v47\n" \
  "v_and_or_b32 v40, v40, -2, s73\n" \
  "buffer_store_dwordx4 v[40:43], v10, s[40:43], s65 offen offset:0 sc1\n" \
  "v_fmac_f32 v32, v56, v9\n" \
  "v_fmac_f32 v33, v57, v9\n" \
  "v_fmac_f32 v34, v58, v9\n" \
  "v_fmac_f32 v35, v59, v9\n" \
  "s_waitcnt lgkmcnt(2)\n" \
  "v_mul_f32 v36, v36, v8\n" \
  "v_mul_f32 v37, v37, v8\n" \
  "v_mul_f32 v38, v38, v8\n" \
  "v_mul_f32 v39, v39, v8\n" \
  "v_fmac_f32 v36, v60, v9\n" \
  "v_fmac_f32 v37, v61, v9\n" \
  "v_fmac_f32 v38, v62, v9\n" \
  "v_fmac_f32 v39, v63, v9\n" \
  "v_cvt_pk_bf16_f32 v44, v32, v33\n" \
  "v_cvt_pk_bf16_f32 v45, v34, v35\n" \
  "v_cvt_pk_bf16_f32 v46, v36, v37\n" \
  "v_cvt_pk_bf16_f32 v47, v38, v39\n" \
  "v_and_or_b32 v44, v44, -2, s73\n" \
  "buffer_store_dwordx4 v[44:47], v10, s[40:43], s65 offen offset:1024 sc1\n" \
  "s_waitcnt lgkmcnt(0)\n" \
  "s_add_u32 s65, s65, 0x80000\n" \
  "s_sub_u32 s64, s64, 1\n" \
  "s_mov_b32 s68, 1\n" \
  "s_cmp_lg_u32 s64, 0\n" \
  "s_cbranch_scc1 .LCP_TOP_%=\n" \
  "s_waitcnt vmcnt(0)\n" \
  "s_mov_b64 exec, 1\n" \
  "buffer_store_dword v7, off, s[56:59], s72 sc1\n" \
  "s_mov_b64 exec, -1\n" \
  "s_add_u32 s72, s72, 0x1000\n" \
  "s_mov_b32 s68, 0\n" \
  ".LCP_END_%=:\n" \
  "s_endpgm\n"
#define V_ASM \
  "s_mov_b32 s68, 0\n" \
  "s_mov_b32 s66, 3\n" \
  "s_mov_b32 s67, 2\n" \
  "s_mov_b32 s72, 0\n" \
  "v_mov_b32 v7, 1\n" \
  "v_mov_b32 v6, s70\n" \
  "v_mov_b32 v5, 0\n" \
  "ds_read_b32 v8, v4 offset:0\n" \
  "ds_read_b32 v9, v4 offset:64\n" \
  "ds_read_b32 v10, v4 offset:128\n" \
  "ds_read_b32 v4, v4 offset:192\n" \
  "s_waitcnt lgkmcnt(0)\n" \
  ".p2align 6\n" \
  ".LCW_TOP_%=:\n" \
  "ds_read_b128 v[28:31], v1 offset:0\n" \
  "ds_read_b128 v[32:35], v1 offset:1024\n" \
  "s_sub_u32 s73, s64, 1\n" \
  "s_bitset0_b32 s65, s74\n" \
  "s_bfe_u32 s73, s73, 0x10004\n" \
  "s_add_u32 s66, s66, 3\n" \
  "s_add_u32 s67, s67, 2\n" \
  "s_mov_b32 s69, 0x400000\n" \
  "v_xor_b32 v0, 0x8000, v0\n" \
  "v_xor_b32 v3, 0x100, v3\n" \
  "v_readfirstlane_b32 s71, v5\n" \
  "s_cmp_ge_u32 s71, s66\n" \
  "s_cbranch_scc1 .LCW_GO_%=\n" \
  "s_cmp_eq_u32 s68, 1\n" \
  "s_cbranch_scc0 .LCW_X1_%=\n" \
  "s_waitcnt vmcnt(0)\n" \
  "s_mov_b64 exec, 1\n" \
  "buffer_store_dword v7, off, s[56:59], s72 sc1\n" \
  "s_mov_b64 exec, -1\n" \
  "s_add_u32 s72, s72, 0x1000\n" \
  "s_mov_b32 s68, 0\n" \
  ".LCW_X1_%=:\n" \
  ".LCW_S2_%=:\n" \
  "s_sleep 1\n" \
  "ds_read_b32 v5, v6 offset:0\n" \
  "s_sub_u32 s69, s69, 1\n" \
  "s_cbranch_scc1 .LCW_END_%=\n" \
  "s_waitcnt lgkmcnt(0)\n" \
  "v_readfirstlane_b32 s71, v5\n" \
  "s_cmp_ge_u32 s71, s66\n" \
  "s_cbranch_scc1 .LCW_GO_%=\n" \
  "s_branch .LCW_S2_%=\n" \
  ".LCW_GO_%=:\n" \
  "ds_read_b128 v[12:15], v0 offset:0\n" \
  "ds_read_b128 v[16:19], v0 offset:2048\n" \
  "ds_read_b128 v[20:23], v0 offset:4096\n" \
  "ds_read_b128 v[24:27], v0 offset:6144\n" \
  "ds_read_b128 v[36:39], v1 offset:2048\n" \
  "s_waitcnt lgkmcnt(4)\n" \
  "v_mfma_f32_16x16x32_bf16 v[42:45], v[12:15], v[64:67], 0\n" \
  "v_mfma_f32_16x16x32_bf16 v[48:51], v[12:15], v[128:131], 0\n" \
  "v_mfma_f32_16x16x32_bf16 v[54:57], v[12:15], v[192:195], 0\n" \
  "v_mfma_f32_16x16x32_bf16 v[60:63], v[12:15], v[28:31], 0\n" \
  "ds_read_b128 v[12:15], v0 offset:8192\n" \
  "ds_read_b128 v[28:31], v1 offset:3072\n" \
  "s_waitcnt lgkmcnt(5)\n" \
  "v_mfma_f32_16x16x32_bf16 v[42:45], v[16:19], v[68:71], v[42:45]\n" \
  "v_mfma_f32_16x16x32_bf16 v[48:51], v[16:19], v[132:135], v[48:51]\n" \
  "v_mfma_f32_16x16x32_bf16 v[54:57], v[16:19], v[196:199], v[54:57]\n" \
  "v_mfma_f32_16x16x32_bf16 v[60:63], v[16:19], v[32:35], v[60:63]\n" \
  "ds_read_b128 v[16:19], v0 offset:10240\n" \
  "ds_read_b128 v[32:35], v1 offset:4096\n" \
  "s_waitcnt lgkmcnt(6)\n" \
  "v_mfma_f32_16x16x32_bf16 v[42:45], v[20:23], v[72:75], v[42:45]\n" \
  "v_mfma_f32_16x16x32_bf16 v[48:51], v[20:23], v[136:139], v[48:51]\n" \
  "v_mfma_f32_16x16x32_bf16 v[54:57], v[20:23], v[200:203], v[54:57]\n" \
  "s_waitcnt lgkmcnt(4)\n" \
  "v_mfma_f32_16x16x32_bf16 v[60:63], v[20:23], v[36:39], v[60:63]\n" \
  "ds_read_b128 v[20:23], v0 offset:12288\n" \
  "ds_read_b128 v[36:39], v1 offset:5120\n" \
  "v_mfma_f32_16x16x32_bf16 v[42:45], v[24:27], v[76:79], v[42:45]\n" \
  "v_mfma_f32_16x16x32_bf16 v[48:51], v[24:27], v[140:143], v[48:51]\n" \
  "v_mfma_f32_16x16x32_bf16 v[54:57], v[24:27], v[204:207], v[54:57]\n" \
  "s_waitcnt lgkmcnt(4)\n" \
  "v_mfma_f32_16x16x32_bf16 v[60:63], v[24:27], v[28:31], v[60:63]\n" \
  "ds_read_b128 v[24:27], v0 offset:14336\n" \
  "ds_read_b128 v[28:31], v1 offset:6144\n" \
  "v_mfma_f32_16x16x32_bf16 v[42:45], v[12:15], v[80:83], v[42:45]\n" \
  "v_mfma_f32_16x16x32_bf16 v[48:51], v[12:15], v[144:147], v[48:51]\n" \
  "v_mfma_f32_16x16x32_bf16 v[54:57], v[12:15], v[208:211], v[54:57]\n" \
  "s_waitcnt lgkmcnt(4)\n" \
  "v_mfma_f32_16x16x32_bf16 v[60:63], v[12:15], v[32:35], v[60:63]\n" \
  "ds_read_b128 v[12:15], v0 offset:16384\n" \
  "ds_read_b128 v[32:35], v1 offset:7168\n" \
  "v_mfma_f32_16x16x32_bf16 v[42:45], v[16:19], v[84:87], v[42:45]\n" \
  "v_mfma_f32_16x16x32_bf16 v[48:51], v[16:19], v[148:151], v[48:51]\n" \
  "v_mfma_f32_16x16x32_bf16 v[54:57], v[16:19], v[212:215], v[54:57]\n" \
  "s_waitcnt lgkmcnt(4)\n" \
  "v_mfma_f32_16x16x32_bf16 v[60:63], v[16:19], v[36:39], v[60:63]\n" \
  "ds_read_b128 v[16:19], v0 offset:18432\n" \
  "ds_read_b128 v[36:39], v1 offset:8192\n" \
  "v_mfma_f32_16x16x32_bf16 v[42:45], v[20:23], v[88:91], v[42:45]\n" \
  "v_mfma_f32_16x16x32_bf16 v[48:51], v[20:23], v[152:155], v[48:51]\n" \
  "v_mfma_f32_16x16x32_bf16 v[54:57], v[20:23], v[216:219], v[54:57]\n" \
  "s_waitcnt lgkmcnt(4)\n" \
  "v_mfma_f32_16x16x32_bf16 v[60:63], v[20:23], v[28:31], v[60:63]\n" \
  "ds_read_b128 v[20:23], v0 offset:20480\n" \
  "ds_read_b128 v[28:31], v1 offset:9216\n" \
  "v_mfma_f32_16x16x32_bf16 v[42:45], v[24:27], v[92:95], v[42:45]\n" \
  "v_mfma_f32_16x16x32_bf16 v[48:51], v[24:27], v[156:159], v[48:51]\n" \
  "v_mfma_f32_16x16x32_bf16 v[54:57], v[24:27], v[220:223], v[54:57]\n" \
  "s_waitcnt lgkmcnt(4)\n" \
  "v_mfma_f32_16x16x32_bf16 v[60:63], v[24:27], v[32:35], v[60:63]\n" \
  "ds_read_b128 v[24:27], v0 offset:22528\n" \
  "ds_read_b128 v[32:35], v1 offset:10240\n" \
  "v_mfma_f32_16x16x32_bf16 v[42:45], v[12:15], v[96:99], v[42:45]\n" \
  "v_mfma_f32_16x16x32_bf16 v[48:51], v[12:15], v[160:163], v[48:51]\n" \
  "v_mfma_f32_16x16x32_bf16 v[54:57], v[12:15], v[224:227], v[54:57]\n" \
  "s_waitcnt lgkmcnt(4)\n" \
  "v_mfma_f32_16x16x32_bf16 v[60:63], v[12:15], v[36:39], v[60:63]\n" \
  "ds_read_b128 v[12:15], v0 offset:24576\n" \
  "ds_read_b128 v[36:39], v1 offset:11264\n" \
  "v_mfma_f32_16x16x32_bf16 v[42:45], v[16:19], v[100:103], v[42:45]\n" \
  "v_mfma_f32_16x16x32_bf16 v[48:51], v[16:19], v[164:167], v[48:51]\n" \
  "v_mfma_f32_16x16x32_bf16 v[54:57], v[16:19], v[228:231], v[54:57]\n" \
  "s_waitcnt lgkmcnt(4)\n" \
  "v_mfma_f32_16x16x32_bf16 v[60:63], v[16:19], v[28:31], v[60:63]\n" \
  "ds_read_b128 v[16:19], v0 offset:26624\n" \
  "ds_read_b128 v[28:31], v1 offset:12288\n" \
  "v_mfma_f32_16x16x32_bf16 v[42:45], v[20:23], v[104:107], v[42:45]\n" \
  "v_mfma_f32_16x16x32_bf16 v[48:51], v[20:23], v[168:171], v[48:51]\n" \
  "v_mfma_f32_16x16x32_bf16 v[54:57], v[20:23], v[232:235], v[54:57]\n" \
  "s_waitcnt lgkmcnt(4)\n" \
  "v_mfma_f32_16x16x32_bf16 v[60:63], v[20:23], v[32:35], v[60:63]\n" \
  "ds_read_b128 v[20:23], v0 offset:28672\n" \
  "ds_read_b128 v[32:35], v1 offset:13312\n" \
  "v_mfma_f32_16x16x32_bf16 v[42:45], v[24:27], v[108:111], v[42:45]\n" \
  "v_mfma_f32_16x16x32_bf16 v[48:51], v[24:27], v[172:175], v[48:51]\n" \
  "v_mfma_f32_16x16x32_bf16 v[54:57], v[24:27], v[236:239], v[54:57]\n" \
  "s_waitcnt lgkmcnt(4)\n" \
  "v_mfma_f32_16x16x32_bf16 v[60:63], v[24:27], v[36:39], v[60:63]\n" \
  "s_cmp_eq_u32 s68, 1\n" \
  "s_cbranch_scc0 .LCW_X3_%=\n" \
  "s_waitcnt vmcnt(0)\n" \
  "s_mov_b64 exec, 1\n" \
  "buffer_store_dword v7, off, s[56:59], s72 sc1\n" \
  "s_mov_b64 exec, -1\n" \
  "s_add_u32 s72, s72, 0x1000\n" \
  "s_mov_b32 s68, 0\n" \
  ".LCW_X3_%=:\n" \
  "ds_read_b32 v5, v6 offset:16\n" \
  "ds_read_b128 v[24:27], v0 offset:30720\n" \
  "ds_read_b128 v[36:39], v1 offset:14336\n" \
  "v_mfma_f32_16x16x32_bf16 v[42:45], v[12:15], v[112:115], v[42:45]\n" \
  "v_mfma_f32_16x16x32_bf16 v[48:51], v[12:15], v[176:179], v[48:51]\n" \
  "v_mfma_f32_16x16x32_bf16 v[54:57], v[12:15], v[240:243], v[54:57]\n" \
  "s_waitcnt lgkmcnt(5)\n" \
  "v_mfma_f32_16x16x32_bf16 v[60:63], v[12:15], v[28:31], v[60:63]\n" \
  "ds_read_b128 v[12:15], v0 offset:256\n" \
  "ds_read_b128 v[28:31], v1 offset:15360\n" \
  "v_mfma_f32_16x16x32_bf16 v[42:45], v[16:19], v[116:119], v[42:45]\n" \
  "v_mfma_f32_16x16x32_bf16 v[48:51], v[16:19], v[180:183], v[48:51]\n" \
  "v_mfma_f32_16x16x32_bf16 v[54:57], v[16:19], v[244:247], v[54:57]\n" \
  "s_waitcnt lgkmcnt(5)\n" \
  "v_mfma_f32_16x16x32_bf16 v[60:63], v[16:19], v[32:35], v[60:63]\n" \
  "s_waitcnt lgkmcnt(4)\n" \
  "v_readfirstlane_b32 s71, v5\n" \
  "s_cmp_ge_u32 s71, s67\n" \
  "s_cbranch_scc1 .LCW_ST_%=\n" \
  ".LCW_S4_%=:\n" \
  "s_sleep 1\n" \
  "ds_read_b32 v5, v6 offset:16\n" \
  "s_sub_u32 s69, s69, 1\n" \
  "s_cbranch_scc1 .LCW_END_%=\n" \
  "s_waitcnt lgkmcnt(0)\n" \
  "v_readfirstlane_b32 s71, v5\n" \
  "s_cmp_ge_u32 s71, s67\n" \
  "s_cbranch_scc1 .LCW_ST_%=\n" \
  "s_branch .LCW_S4_%=\n" \
  ".LCW_ST_%=:\n" \
  "ds_read_b128 v[16:19], v0 offset:2304\n" \
  "ds_read_b128 v[32:35], v3 offset:128\n" \
  "v_mfma_f32_16x16x32_bf16 v[42:45], v[20:23], v[120:123], v[42:45]\n" \
  "v_mfma_f32_16x16x32_bf16 v[48:51], v[20:23], v[184:187], v[48:51]\n" \
  "v_mfma_f32_16x16x32_bf16 v[54:57], v[20:23], v[248:251], v[54:57]\n" \
  "s_waitcnt lgkmcnt(4)\n" \
  "v_mfma_f32_16x16x32_bf16 v[60:63], v[20:23], v[36:39], v[60:63]\n" \
  "ds_read_b128 v[20:23], v0 offset:4352\n" \
  "ds_read_b128 v[36:39], v3 offset:0\n" \
  "v_mfma_f32_16x16x32_bf16 v[42:45], v[24:27], v[124:127], v[42:45]\n" \
  "v_mfma_f32_16x16x32_bf16 v[48:51], v[24:27], v[188:191], v[48:51]\n" \
  "v_mfma_f32_16x16x32_bf16 v[54:57], v[24:27], v[252:255], v[54:57]\n" \
  "s_waitcnt lgkmcnt(4)\n" \
  "v_mfma_f32_16x16x32_bf16 v[60:63], v[24:27], v[28:31], v[60:63]\n" \
  "ds_read_b128 v[28:31], v1 offset:0\n" \
  "ds_read_b128 v[24:27], v0 offset:6400\n" \
  "s_waitcnt lgkmcnt(4)\n" \
  "v_mul_f32 v42, v42, v32\n" \
  "v_mul_f32 v43, v43, v33\n" \
  "v_mul_f32 v44, v44, v34\n" \
  "v_mul_f32 v45, v45, v35\n" \
  "s_waitcnt lgkmcnt(2)\n" \
  "v_fmac_f32 v42, v36, v8\n" \
  "v_fmac_f32 v43, v37, v8\n" \
  "v_fmac_f32 v44, v38, v8\n" \
  "v_fmac_f32 v45, v39, v8\n" \
  "v_cvt_pk_bf16_f32 v40, v42, v43\n" \
  "v_cvt_pk_bf16_f32 v41, v44, v45\n" \
  "v_and_or_b32 v40, v40, -2, s73\n" \
  "v_mfma_f32_16x16x32_bf16 v[42:45], v[12:15], v[64:67], 0\n" \
  "v_mul_f32 v48, v48, v32\n" \
  "v_mul_f32 v49, v49, v33\n" \
  "v_mul_f32 v50, v50, v34\n" \
  "v_mul_f32 v51, v51, v35\n" \
  "v_fmac_f32 v48, v36, v9\n" \
  "v_fmac_f32 v49, v37, v9\n" \
  "v_fmac_f32 v50, v38, v9\n" \
  "v_fmac_f32 v51, v39, v9\n" \
  "v_cvt_pk_bf16_f32 v46, v48, v49\n" \
  "v_cvt_pk_bf16_f32 v47, v50, v51\n" \
  "v_and_or_b32 v46, v46, -2, s73\n" \
  "v_mfma_f32_16x16x32_bf16 v[48:51], v[12:15], v[128:131], 0\n" \
  "v_mul_f32 v54, v54, v32\n" \
  "v_mul_f32 v55, v55, v33\n" \
  "v_mul_f32 v56, v56, v34\n" \
  "v_mul_f32 v57, v57, v35\n" \
  "v_fmac_f32 v54, v36, v10\n" \
  "v_fmac_f32 v55, v37, v10\n" \
  "v_fmac_f32 v56, v38, v10\n" \
  "v_fmac_f32 v57, v39, v10\n" \
  "v_cvt_pk_bf16_f32 v52, v54, v55\n" \
  "v_cvt_pk_bf16_f32 v53, v56, v57\n" \
  "v_and_or_b32 v52, v52, -2, s73\n" \
  "v_mfma_f32_16x16x32_bf16 v[54:57], v[12:15], v[192:195], 0\n" \
  "v_mul_f32 v60, v60, v32\n" \
  "v_mul_f32 v61, v61, v33\n" \
  "v_mul_f32 v62, v62, v34\n" \
  "v_mul_f32 v63, v63, v35\n" \
  "v_fmac_f32 v60, v36, v4\n" \
  "v_fmac_f32 v61, v37, v4\n" \
  "v_fmac_f32 v62, v38, v4\n" \
  "v_fmac_f32 v63, v39, v4\n" \
  "v_cvt_pk_bf16_f32 v58, v60, v61\n" \
  "v_cvt_pk_bf16_f32 v59, v62, v63\n" \
  "v_and_or_b32 v58, v58, -2, s73\n" \
  "ds_read_b128 v[32:35], v1 offset:1024\n" \
  "ds_read_b128 v[36:39], v1 offset:2048\n" \
  "s_waitcnt lgkmcnt(3)\n" \
  "v_mfma_f32_16x16x32_bf16 v[60:63], v[12:15], v[28:31], 0\n" \
  "ds_read_b128 v[12:15], v0 offset:8448\n" \
  "ds_read_b128 v[28:31], v1 offset:3072\n" \
  "v_mfma_f32_16x16x32_bf16 v[42:45], v[16:19], v[68:71], v[42:45]\n" \
  "v_mfma_f32_16x16x32_bf16 v[48:51], v[16:19], v[132:135], v[48:51]\n" \
  "v_mfma_f32_16x16x32_bf16 v[54:57], v[16:19], v[196:199], v[54:57]\n" \
  "s_waitcnt lgkmcnt(3)\n" \
  "v_mfma_f32_16x16x32_bf16 v[60:63], v[16:19], v[32:35], v[60:63]\n" \
  "ds_read_b128 v[16:19], v0 offset:10496\n" \
  "ds_read_b128 v[32:35], v1 offset:4096\n" \
  "v_mfma_f32_16x16x32_bf16 v[42:45], v[20:23], v[72:75], v[42:45]\n" \
  "v_mfma_f32_16x16x32_bf16 v[48:51], v[20:23], v[136:139], v[48:51]\n" \
  "v_mfma_f32_16x16x32_bf16 v[54:57], v[20:23], v[200:203], v[54:57]\n" \
  "s_waitcnt lgkmcnt(4)\n" \
  "v_mfma_f32_16x16x32_bf16 v[60:63], v[20:23], v[36:39], v[60:63]\n" \
  "ds_read_b128 v[20:23], v0 offset:12544\n" \
  "ds_read_b128 v[36:39], v1 offset:5120\n" \
  "v_mfma_f32_16x16x32_bf16 v[42:45], v[24:27], v[76:79], v[42:45]\n" \
  "v_mfma_f32_16x16x32_bf16 v[48:51], v[24:27], v[140:143], v[48:51]\n" \
  "v_mfma_f32_16x16x32_bf16 v[54:57], v[24:27], v[204:207], v[54:57]\n" \
  "s_waitcnt lgkmcnt(4)\n" \
  "v_mfma_f32_16x16x32_bf16 v[60:63], v[24:27], v[28:31], v[60:63]\n" \
  "ds_read_b128 v[24:27], v0 offset:14592\n" \
  "ds_read_b128 v[28:31], v1 offset:6144\n" \
  "v_mfma_f32_16x16x32_bf16 v[42:45], v[12:15], v[80:83], v[42:45]\n" \
  "v_mfma_f32_16x16x32_bf16 v[48:51], v[12:15], v[144:147], v[48:51]\n" \
  "v_mfma_f32_16x16x32_bf16 v[54:57], v[12:15], v[208:211], v[54:57]\n" \
  "s_waitcnt lgkmcnt(4)\n" \
  "v_mfma_f32_16x16x32_bf16 v[60:63], v[12:15], v[32:35], v[60:63]\n" \
  "ds_read_b128 v[12:15], v0 offset:16640\n" \
  "ds_read_b128 v[32:35], v1 offset:7168\n" \
  "v_mfma_f32_16x16x32_bf16 v[42:45], v[16:19], v[84:87], v[42:45]\n" \
  "v_mfma_f32_16x16x32_bf16 v[48:51], v[16:19], v[148:151], v[48:51]\n" \
  "v_mfma_f32_16x16x32_bf16 v[54:57], v[16:19], v[212:215], v[54:57]\n" \
  "s_waitcnt lgkmcnt(4)\n" \
  "v_mfma_f32_16x16x32_bf16 v[60:63], v[16:19], v[36:39], v[60:63]\n" \
  "ds_read_b128 v[16:19], v0 offset:18688\n" \
  "ds_read_b128 v[36:39], v1 offset:8192\n" \
  "v_mfma_f32_16x16x32_bf16 v[42:45], v[20:23], v[88:91], v[42:45]\n" \
  "v_mfma_f32_16x16x32_bf16 v[48:51], v[20:23], v[152:155], v[48:51]\n" \
  "v_mfma_f32_16x16x32_bf16 v[54:57], v[20:23], v[216:219], v[54:57]\n" \
  "s_waitcnt lgkmcnt(4)\n" \
  "v_mfma_f32_16x16x32_bf16 v[60:63], v[20:23], v[28:31], v[60:63]\n" \
  "ds_read_b128 v[20:23], v0 offset:20736\n" \
  "ds_read_b128 v[28:31], v1 offset:9216\n" \
  "v_mfma_f32_16x16x32_bf16 v[42:45], v[24:27], v[92:95], v[42:45]\n" \
  "v_mfma_f32_16x16x32_bf16 v[48:51], v[24:27], v[156:159], v[48:51]\n" \
  "v_mfma_f32_16x16x32_bf16 v[54:57], v[24:27], v[220:223], v[54:57]\n" \
  "s_waitcnt lgkmcnt(4)\n" \
  "v_mfma_f32_16x16x32_bf16 v[60:63], v[24:27], v[32:35], v[60:63]\n" \
  "ds_read_b128 v[24:27], v0 offset:22784\n" \
  "ds_read_b128 v[32:35], v1 offset:10240\n" \
  "v_mfma_f32_16x16x32_bf16 v[42:45], v[12:15], v[96:99], v[42:45]\n" \
  "v_mfma_f32_16x16x32_bf16 v[48:51], v[12:15], v[160:163], v[48:51]\n" \
  "v_mfma_f32_16x16x32_bf16 v[54:57], v[12:15], v[224:227], v[54:57]\n" \
  "s_waitcnt lgkmcnt(4)\n" \
  "v_mfma_f32_16x16x32_bf16 v[60:63], v[12:15], v[36:39], v[60:63]\n" \
  "ds_read_b128 v[12:15], v0 offset:24832\n" \
  "ds_read_b128 v[36:39], v1 offset:11264\n" \
  "v_mfma_f32_16x16x32_bf16 v[42:45], v[16:19], v[100:103], v[42:45]\n" \
  "v_mfma_f32_16x16x32_bf16 v[48:51], v[16:19], v[164:167], v[48:51]\n" \
  "v_mfma_f32_16x16x32_bf16 v[54:57], v[16:19], v[228:231], v[54:57]\n" \
  "s_waitcnt lgkmcnt(4)\n" \
  "v_mfma_f32_16x16x32_bf16 v[60:63], v[16:19], v[28:31], v[60:63]\n" \
  "ds_read_b128 v[16:19], v0 offset:26880\n" \
  "ds_read_b128 v[28:31], v1 offset:12288\n" \
  "v_mfma_f32_16x16x32_bf16 v[42:45], v[20:23], v[104:107], v[42:45]\n" \
  "v_mfma_f32_16x16x32_bf16 v[48:51], v[20:23], v[168:171], v[48:51]\n" \
  "v_mfma_f32_16x16x32_bf16 v[54:57], v[20:23], v[232:235], v[54:57]\n" \
  "s_waitcnt lgkmcnt(4)\n" \
  "v_mfma_f32_16x16x32_bf16 v[60:63], v[20:23], v[32:35], v[60:63]\n" \
  "ds_read_b128 v[20:23], v0 offset:28928\n" \
  "ds_read_b128 v[32:35], v1 offset:13312\n" \
  "v_mfma_f32_16x16x32_bf16 v[42:45], v[24:27], v[108:111], v[42:45]\n" \
  "v_mfma_f32_16x16x32_bf16 v[48:51], v[24:27], v[172:175], v[48:51]\n" \
  "v_mfma_f32_16x16x32_bf16 v[54:57], v[24:27], v[236:239], v[54:57]\n" \
  "s_waitcnt lgkmcnt(4)\n" \
  "v_mfma_f32_16x16x32_bf16 v[60:63], v[24:27], v[36:39], v[60:63]\n" \
  "ds_read_b128 v[24:27], v0 offset:30976\n" \
  "ds_read_b128 v[36:39], v1 offset:14336\n" \
  "v_mfma_f32_16x16x32_bf16 v[42:45], v[12:15], v[112:115], v[42:45]\n" \
  "v_mfma_f32_16x16x32_bf16 v[48:51], v[12:15], v[176:179], v[48:51]\n" \
  "v_mfma_f32_16x16x32_bf16 v[54:57], v[12:15], v[240:243], v[54:57]\n" \
  "s_waitcnt lgkmcnt(4)\n" \
  "v_mfma_f32_16x16x32_bf16 v[60:63], v[12:15], v[28:31], v[60:63]\n" \
  "ds_read_b128 v[28:31], v1 offset:15360\n" \
  "v_mfma_f32_16x16x32_bf16 v[42:45], v[16:19], v[116:119], v[42:45]\n" \
  "v_mfma_f32_16x16x32_bf16 v[48:51], v[16:19], v[180:183], v[48:51]\n" \
  "v_mfma_f32_16x16x32_bf16 v[54:57], v[16:19], v[244:247], v[54:57]\n" \
  "s_waitcnt lgkmcnt(3)\n" \
  "v_mfma_f32_16x16x32_bf16 v[60:63], v[16:19], v[32:35], v[60:63]\n" \
  "ds_read_b128 v[32:35], v3 offset:192\n" \
  "v_mfma_f32_16x16x32_bf16 v[42:45], v[20:23], v[120:123], v[42:45]\n" \
  "v_mfma_f32_16x16x32_bf16 v[48:51], v[20:23], v[184:187], v[48:51]\n" \
  "v_mfma_f32_16x16x32_bf16 v[54:57], v[20:23], v[248:251], v[54:57]\n" \
  "s_waitcnt lgkmcnt(2)\n" \
  "v_mfma_f32_16x16x32_bf16 v[60:63], v[20:23], v[36:39], v[60:63]\n" \
  "ds_read_b128 v[36:39], v3 offset:64\n" \
  "s_mov_b64 exec, 1\n" \
  "ds_add_u32 v6, v7 offset:8\n" \
  "s_mov_b64 exec, -1\n" \
  "v_xor_b32 v6, 4, v6\n" \
  "v_mfma_f32_16x16x32_bf16 v[42:45], v[24:27], v[124:127], v[42:45]\n" \
  "v_mfma_f32_16x16x32_bf16 v[48:51], v[24:27], v[188:191], v[48:51]\n" \
  "v_mfma_f32_16x16x32_bf16 v[54:57], v[24:27], v[252:255], v[54:57]\n" \
  "s_waitcnt lgkmcnt(3)\n" \
  "v_mfma_f32_16x16x32_bf16 v[60:63], v[24:27], v[28:31], v[60:63]\n" \
  "s_waitcnt lgkmcnt(2)\n" \
  "v_mul_f32 v42, v42, v32\n" \
  "v_mul_f32 v43, v43, v33\n" \
  "v_mul_f32 v44, v44, v34\n" \
  "v_mul_f32 v45, v45, v35\n" \
  "s_waitcnt lgkmcnt(1)\n" \
  "v_fmac_f32 v42, v36, v8\n" \
  "v_fmac_f32 v43, v37, v8\n" \
  "v_fmac_f32 v44, v38, v8\n" \
  "v_fmac_f32 v45, v39, v8\n" \
  "v_cvt_pk_bf16_f32 v42, v42, v43\n" \
  "v_cvt_pk_bf16_f32 v43, v44, v45\n" \
  "buffer_store_dwordx4 v[40:43], v2, s[40:43], s65 offen offset:0 sc1\n" \
  "v_mul_f32 v48, v48, v32\n" \
  "v_mul_f32 v49, v49, v33\n" \
  "v_mul_f32 v50, v50, v34\n" \
  "v_mul_f32 v51, v51, v35\n" \
  "v_fmac_f32 v48, v36, v9\n" \
  "v_fmac_f32 v49, v37, v9\n" \
  "v_fmac_f32 v50, v38, v9\n" \
  "v_fmac_f32 v51, v39, v9\n" \
  "v_cvt_pk_bf16_f32 v48, v48, v49\n" \
  "v_cvt_pk_bf16_f32 v49, v50, v51\n" \
  "ds_read_b32 v5, v6 offset:0\n" \
  "buffer_store_dwordx4 v[46:49], v2, s[40:43], s65 offen offset:1024 sc1\n" \
  "v_mul_f32 v54, v54, v32\n" \
  "v_mul_f32 v55, v55, v33\n" \
  "v_mul_f32 v56, v56, v34\n" \
  "v_mul_f32 v57, v57, v35\n" \
  "v_fmac_f32 v54, v36, v10\n" \
  "v_fmac_f32 v55, v37, v10\n" \
  "v_fmac_f32 v56, v38, v10\n" \
  "v_fmac_f32 v57, v39, v10\n" \
  "v_cvt_pk_bf16_f32 v54, v54, v55\n" \
  "v_cvt_pk_bf16_f32 v55, v56, v57\n" \
  "buffer_store_dwordx4 v[52:55], v2, s[40:43], s65 offen offset:2048 sc1\n" \
  "v_mul_f32 v60, v60, v32\n" \
  "v_mul_f32 v61, v61, v33\n" \
  "v_mul_f32 v62, v62, v34\n" \
  "v_mul_f32 v63, v63, v35\n" \
  "v_fmac_f32 v60, v36, v4\n" \
  "v_fmac_f32 v61, v37, v4\n" \
  "v_fmac_f32 v62, v38, v4\n" \
  "v_fmac_f32 v63, v39, v4\n" \
  "v_cvt_pk_bf16_f32 v60, v60, v61\n" \
  "v_cvt_pk_bf16_f32 v61, v62, v63\n" \
  "buffer_store_dwordx4 v[58:61], v2, s[40:43], s65 offen offset:3072 sc1\n" \
  "s_waitcnt lgkmcnt(0)\n" \
  "s_add_u32 s65, s65, 0x40000\n" \
  "s_sub_u32 s64, s64, 1\n" \
  "s_mov_b32 s68, 1\n" \
  "s_cmp_lg_u32 s64, 0\n" \
  "s_cbranch_scc1 .LCW_TOP_%=\n" \
  "s_waitcnt vmcnt(0)\n" \
  "s_mov_b64 exec, 1\n" \
  "buffer_store_dword v7, off, s[56:59], s72 sc1\n" \
  "s_mov_b64 exec, -1\n" \
  "s_add_u32 s72, s72, 0x1000\n" \
  "s_mov_b32 s68, 0\n" \
  ".LCW_END_%=:\n" \
  "s_endpgm\n"
constexpr int RG = 2;
#define MARKS(s) __builtin_amdgcn_raw_buffer_load_b32(rm, (c >> 2) * 128 + (c & 3) * 4, (s) * 2048, AUX_SC1)
struct Yes { static constexpr bool value = true; };
struct No { static constexpr bool value = false; };
#define RFL(x) __builtin_amdgcn_readfirstlane(x)
#define HALF(x, i) BF8(((i) ? __builtin_shufflevector(x, x, 4, 5, 6, 7) : __builtin_shufflevector(x, x, 0, 1, 2, 3)))
constexpr int P_W = 65536, P_ST = 131072, P_SS = P_ST + 1024, P_CN = P_SS + 512;
template <bool PR>
__device__ __forceinline__ void role_proj_t(const MegaArgs& A, char* smem, const int L, const int idx, const bool kv) {
  const int w = A.wv;
  const int l = lane_id();
  const int c = l & 15, g = l >> 4;
  char* ws = A.ws;
  unsigned* FL = (unsigned*)(ws + OFF_FL);
  unsigned* TMO = FL + FL_TMO;
  const unsigned sb = lds_base(smem);
  auto cn = (volatile __attribute__((address_space(3))) unsigned*)(unsigned long)(sb + P_CN);
  u64 tag;
  const bool h4 = kv && L;
  const __amdgpu_buffer_rsrc_t rsM = MKRS(ws + OFF_MEMK + L * SZ_MEM, kv && w == 2 ? (4 - L) * SZ_MEM : 0);
  const bool mc = kv && w == 2 && (MCE < 2 || !L);
  constexpr int NMC = MCE == 2 ? 8 : 4;
  const int b0 = RFL(h4 ? (H4S ? 2 : 3) * w : w == 2 ? 0 : HX0),
            b1 = RFL(h4 ? (H4S ? 2 * w + 2 : w == 2 ? 8 : 3 * w + 3 * (w < 2)) : w == 2 ? HX0 : 8);
  const __amdgpu_buffer_rsrc_t rsF = MKRS(FL + FL_FC + L * 64 * 32 * 32, 64 * 4096);
  const int fo = ((blockIdx.x & 3) * 8 + ((H4S != 0 && h4) || w == 3 ? l >> 3 : min(b0 + (l >> 3), b1 - 1))) * 128 + (l & 7) * 4;
#define FC_LOOK(s) __builtin_amdgcn_raw_buffer_load_b32(rsF, fo, (s) * 4096, AUX_SC1)
#define MEM_CLEAR(s)                                                                                                          \
  _Pragma("unroll") for (int i = 0; i < NMC; ++i)                                                                             \
    ST128((v4u{0u, 0u, 0u, 0u}), rsM, l * 16 + (i & 1) * 1024, (s) * 65536 + idx * 2048 + (i >> 1) * (int)SZ_MEM * (MCE == 2 ? 1 : 2));
  const char* Xb = ws + (L ? OFF_X3 : OFF_XA);
  const __amdgpu_buffer_rsrc_t rsS = MKRS(ws + OFF_H + L * (SZ_X / 2), SZ_X / 2);
  unsigned nf = 0u;
  bool early = false;
  v4u t0[16];
#define X_ASK(s) \
  for (int i = b0; i < b1; ++i) glds64c(Xb + RFL((s) * 32768 + i * 4096), l * 16, RFL(sb + ((s) & 1) * 32768 + i * 4096));
#define S_ASK(s, t) _Pragma("unroll") for (int i = 0; i < 16; ++i) t[i] = LD128(rsS, l * 16, (s) * 16384 + i * 1024);
  {
    const int j = w & 3;
    const bool vt = kv && j >= 2;
    constexpr int NT = PR ? 3 : 1;
    v8u wq[NT][8];
    const u16* Wm = (kv ? (PR ? A.Wv : A.Wk) : A.Wq) - (PR ? 128 : 0) * HD;
    const int hm = kv ? idx : 2 * idx + PR;
    const int q = (l >> 2) & 3, p = l & 3, f = q | ((g & 1) << 2);
    unsigned ra[NT];
#pragma unroll
    for (int m = 0; m < NT; ++m) {
      const int ct = PR ? m : 3;
      ra[m] = sb + (j >> 1) * 65536 + (8 * g + q) * 256 + ((4 * (j & 1) + (vt ? ct : (ct & 2) + (p >> 1))) ^ f) * 32 + (vt ? p * 8 : (p & 1) * 16 + (ct & 1) * 8);
    }
#define W_ISSUE(n) wqkv_issue<false>(Wm + (128 * (n) - 256 * ((n) & 1)) * HD, (n) & 1, hm, w, l, sb, (WNT >> (kv ? 1 + PR : 3) & 1) && (L == 0 || (WNT & 32)))
#define W_STAGE(n, WAIT, NEXT)                                                                             \
  {                                                                                                        \
    WAIT;                                                                                                  \
    if (n == 3 && EL == 2 && !PR && L == 0 && w >= 2) nf = FC_LOOK(0);                                     \
    if (n == 3 && EL == 1 && !PR && L == 0 && w >= 2) {                                                    \
      early = __all(nf != 0u);                                                                             \
      if (early) {                                                                                         \
        X_ASK(0)                                                                                           \
        if (w == 3) { S_ASK(0, t0) }                                                                       \
      }                                                                                                    \
      nf = FC_LOOK(early);                                                                                 \
    }                                                                                                      \
    RAW_BAR();                                                                                             \
    if (n == 3 && EL == 3 && !PR && L == 0 && w >= 2) nf = FC_LOOK(0);                                     \
    _Pragma("unroll") for (int m = 0; m < NT; ++m) {                                                       \
      _Pragma("unroll") for (int t = 0; t < 2; ++t) {                                                      \
        const unsigned a_ = ra[m] + ((n) & 1) * 32768 + t * 16384;                                         \
        const v2u r0 = trd(a_), r1 = trd(a_ + 1024), r2 = trd(a_ + 8192), r3 = trd(a_ + 8192 + 1024);      \
        wq[m][2 * (n) + t] = v8u{r0.x, r0.y, r1.x, r1.y, r2.x, r2.y, r3.x, r3.y};                          \
      }                                                                                                    \
    }                                                                                                      \
    _Pragma("unroll") for (int m = 0; m < 2 * NT; ++m) asm volatile("" : "+v"(wq[m >> 1][2 * (n) + (m & 1)])); \
    RAW_BAR();                                                                                             \
    NEXT;                                                                                                  \
    _Pragma("unroll") for (int m = 0; m < 4 * NT; ++m)                                                     \
      cs[m % NT] = MFMA16(HALF(wq[m % NT][2 * (n) + m / NT / 2], (m / NT) & 1), BF8(one), cs[m % NT]);     \
    _Pragma("unroll") for (int m = 0; m < NT; ++m) asm volatile("" : "+v"(cs[m]));                         \
  }
    if (EL && !MC3 && mc) { MEM_CLEAR(0) }
    W_ISSUE(0);
    W_ISSUE(1);
    tag = call_tag(A);
    if (w == 0 && l < 8) cn[l] = l == 1 ? 3u : l == 5 ? 2u : 0u;
    const v4u one = {0x3f803f80u, 0x3f803f80u, 0x3f803f80u, 0x3f803f80u};
    f32x4 cs[NT] = {};
    u64 rv = 0;
    W_STAGE(0, WAIT_VM(8), W_ISSUE(2); if (EL) RD_ASK(rv));
    SST(4);
    W_STAGE(1, if (EL) WAIT_VM(9); else WAIT_VM(8), W_ISSUE(3); if (EL != 0 && MC3 != 0 && mc) { MEM_CLEAR(0) if (MC3 == 2) { MEM_CLEAR(1) } });
    SST(5);
    W_STAGE(2, if (EL != 0 && MC3 == 2 && mc) WAIT_VM(16); else if (EL != 0 && MC3 != 0 && mc) WAIT_VM(12); else WAIT_VM(8), (void)0);
    SST(6);
    asm volatile("" : "+v"(rv));
    if (!EL) ready_wait(A, tag, ready_look(A));
    for (unsigned spins = 0; EL && !__all(rv == tag) && spins < SPIN_CAP; ++spins) {
      __builtin_amdgcn_s_sleep(8);
      RD_ASK(rv);
      asm volatile("s_waitcnt vmcnt(0)" : "+v"(rv)::"memory");
    }
    if (EL == 1 && !PR && L == 0 && w >= 2) nf = FC_LOOK(0);
    W_STAGE(3, if (EL >= 2 && MC3 == 2 && mc) WAIT_VM(8); else if (EL >= 2 && MC3 == 1 && mc) WAIT_VM(4); else WAIT_VM(0), if (!PR) {
      _Pragma("unroll") for (int ks = 0; ks < 16; ++ks)
        *reinterpret_cast<bf16x8*>(smem + P_W + j * 16384 + ks * 1024 + l * 16) = HALF(wq[0][ks >> 1], ks & 1);
    });
    SST(7);
#undef W_STAGE
#undef W_ISSUE
#pragma unroll
    for (int m = 0; m < NT; ++m) *reinterpret_cast<f32x4*>(smem + P_ST + j * 256 + (PR ? m : 3) * 64 + g * 16) = cs[m];
    RAW_BAR();
    SST(8);
    if constexpr (PR) {
      const v4u rsO = mk_srd(ws + RFL(vt ? TOK_V(ws, L) : TOK_QK(ws, L)), RFL((unsigned)(vt ? SZ_O : SZ_QK) >> (GEN_TR * L ? 6 - GEN_TR : 0)));
      const v4u rsP = mk_srd(FL + RFL(kv ? FL_FQ + (L * 64 * 32 + idx) * 32 + 2 * j : FL_FP + (L * 16 + idx) * 32 + j), 63 * 4096 + 4);
      const int so = RFL(vt ? idx * 8 + 4 * (j & 1) : (kv ? idx * 16 + 8 : (2 * idx + (j >> 1)) * 16) + 2 * (j & 1));
      const v8u pv = {sb + 32768 + g * 512 + c * 16, sb + P_W + j * 16384 + l * 16, (unsigned)l * 16, sb + P_SS + 256 + (vt ? g * 16 : c * 4),
                      sb + P_ST + j * 256 + (vt ? c * 4 : g * 16), 0u, 0u, 0u};
      const char* dbg = nullptr;
#define P_OPS                                                                                                                               \
  ::"{v[64:71]}"(wq[0][0]), "{v[72:79]}"(wq[0][1]), "{v[80:87]}"(wq[0][2]), "{v[88:95]}"(wq[0][3]), "{v[96:103]}"(wq[0][4]),                \
      "{v[104:111]}"(wq[0][5]), "{v[112:119]}"(wq[0][6]), "{v[120:127]}"(wq[0][7]), "{v[128:135]}"(wq[1][0]), "{v[136:143]}"(wq[1][1]),     \
      "{v[144:151]}"(wq[1][2]), "{v[152:159]}"(wq[1][3]), "{v[160:167]}"(wq[1][4]), "{v[168:175]}"(wq[1][5]), "{v[176:183]}"(wq[1][6]),     \
      "{v[184:191]}"(wq[1][7]), "{v[192:199]}"(wq[2][0]), "{v[200:207]}"(wq[2][1]), "{v[208:215]}"(wq[2][2]), "{v[216:223]}"(wq[2][3]),     \
      "{v[224:231]}"(wq[2][4]), "{v[232:239]}"(wq[2][5]), "{v[240:247]}"(wq[2][6]), "{v[248:255]}"(wq[2][7]), "{v[0:7]}"(pv),               \
      "{s[40:43]}"(rsO), "{s[56:59]}"(rsP), "{s64}"(NSEG), "{s65}"(so * 1024), "{s70}"(sb + P_CN), "{s74}"(RFL(L ? 19 + GEN_TR - vt : 0)), "{s[36:37]}"(dbg) \
      : "memory", "vcc", "scc", "s80", "v63"
      if (vt) asm volatile("s_mov_b64 exec, -1\ns_waitcnt vmcnt(0) lgkmcnt(0)\ns_nop 4\n" V_ASM P_OPS);
      else asm volatile("s_mov_b64 exec, -1\ns_waitcnt vmcnt(0) lgkmcnt(0)\ns_nop 4\n" P_ASM P_OPS);
#undef P_OPS
      return;
    }
  }
  __builtin_amdgcn_s_setprio(RPRIO);
  if (h4 || w >= 2) {
    if (!EL && mc) { MEM_CLEAR(0) }
    auto stats = [&](const v4u (&t)[16], const int s) {
      float a = 0.f, b = 0.f, qa = 0.f, qb = 0.f;
#pragma unroll
      for (int i = 0; i < 16; ++i) {
        a = one(a + __uint_as_float(t[i].x));
        b = one(b + __uint_as_float(t[i].z));
        qa = one(qa + __uint_as_float(t[i].y));
        qb = one(qb + __uint_as_float(t[i].w));
      }
      a = xsum32(xsum16(a));
      b = xsum32(xsum16(b));
      float da = 0.f, db = 0.f;
#pragma unroll
      for (int i = 0; i < 16; ++i) {
        const float u = one(__uint_as_float(t[i].x) - a * (1.0f / 64)), v = one(__uint_as_float(t[i].z) - b * (1.0f / 64));
        da = one(da + u * u);
        db = one(db + v * v);
      }
      const float var = xsum32(swap_add16(qa + da * 0.125f, qb + db * 0.125f)) * (1.0f / DM);
      const float ri = 1.0f / bfr1(sqrtf(bfr1(bfr1(var) + 1e-5f)));
      reinterpret_cast<float*>(smem + P_SS)[(s & 1) * 64 + (g >> 1) * 32 + 2 * c + (g & 1)] = g < 2 ? bfr1(((g & 1) ? b : a) * (1.0f / DM)) * (EP ? -ri : 1.0f) : ri;
      asm volatile("" ::: "memory");
      if (l == 0) cn[4 + (s & 1)] = 2 * s + 4;
    };
    static_assert(EL != 1 || MC3 == 2, "clear");
    if (early) {
      if (w == 3) WAIT_VM(17);
      else WAIT_VM(1);
      if (l == 0) __hip_atomic_fetch_add((__attribute__((address_space(3))) unsigned*)cn, 3u, __ATOMIC_RELAXED, __HIP_MEMORY_SCOPE_WORKGROUP);
      SSX(10, w == 2);
      SSX(9, w == 2);
      if (w == 3) stats(t0, 0);
      SSX(11, w == 3);
    }
    if (L) {
      char* ws = A.ws;
      int w = A.wv, i1 = idx + 32 * kv;
      asm volatile("" : "+s"(ws), "+s"(w), "+s"(i1));
      const int idx = i1 & 31;
      const bool h4 = i1 >= 32, mc = h4 && w == 2;
      const int b0 = h4 ? 3 * w : w == 2 ? 0 : HX0, b1 = h4 ? (w == 2 ? 8 : 3 * w + 3 * (w < 2)) : w == 2 ? HX0 : 8;
      unsigned* FL = (unsigned*)(ws + OFF_FL);
      unsigned* TMO = FL + FL_TMO;
      const char* Xb = ws + OFF_X3;
      const __amdgpu_buffer_rsrc_t rsM = MKRS(ws + OFF_MEMK + SZ_MEM, 3 * SZ_MEM), rsF = MKRS(FL + FL_FC + 64 * 32 * 32, 64 * 4096),
                                   rsS = MKRS(ws + OFF_H + SZ_X / 2, SZ_X / 2);
      unsigned mk;
      int l16 = l * 16;
      asm volatile("" : "+v"(l16));
      const __amdgpu_buffer_rsrc_t rm = MKRS(FL + FL_FW + (blockIdx.x & 3) * 128, 63 * 2048 + 512);
      const __amdgpu_buffer_rsrc_t rsK = MKRS(ws + (w < 2 ? TOK_QK(ws, 1) + idx * 16384 + 8192 : TOK_V(ws, 1) + idx * 8192) + (w & 1) * 4096,
                                              KC && h4 ? (GEN_TR ? (1 << GEN_TR) - 1 : 63) * (w < 2 ? 0x80000 : 0x40000) + 4096 : 0);
#define KV_CLEAR(s) \
  _Pragma("unroll") for (int i = 0; i < 4 * KC; ++i) ST128((v4u{0u, 0u, 0u, 0u}), rsK, l16 + i * 1024, (s) << (w < 2 ? 19 : 18));
      static_assert(KC || !GEN_TL, "clear");
      KV_CLEAR(1)
      if (!wait_n<SLP_V, 32>(FL + FL_FZ + (blockIdx.x & 7) * 64, 64, TMO)) return;
      const __amdgpu_buffer_rsrc_t rf = MKRS(FL + FL_FW + (64 - (1 << GEN_TR)) * 512 + (blockIdx.x & 3) * 128, 65 * 2048);
#define FREE_LOOK(s) __builtin_amdgcn_raw_buffer_load_b32(rf, (c >> 2) * 128 + (c & 3) * 4, (s) * 2048, AUX_SC1)
#pragma unroll 1
      for (unsigned spins = 0;; ++spins) {
        if (spin_timeout(spins, TMO)) return;
        __builtin_amdgcn_s_sleep(32);
        mk = MARKS(0);
        if (__all(mk != 0u)) break;
        asm volatile("" ::: "memory");
      }
      gate(latest(mk, RFL(mk)) + KD + K0 + (h4 ? 0 : P2D));
      auto feed = [&](auto ST_) {
        constexpr bool ST = ST_.value;
        bool due = true;
        int sf = -1;
#pragma unroll 1
        for (int s = 0, n = 0; s < NSEG && n < 4 * NSEG; ++n) {
#pragma unroll 1
          for (unsigned spins = 0; !due; ++spins) {
            if (spin_timeout(spins, TMO)) return;
            if (spins) __builtin_amdgcn_s_sleep(SLP_H);
            due = __all(FC_LOOK(s) != 0u);
            asm volatile("" ::: "memory");
          }
          TS(4, s, 128);
#pragma unroll 1
          for (unsigned spins = 0; cn[2 + (s & 1)] < 4u * (s >> 1); ++spins) {
            if (spins > 64u * SPIN_CAP) return;
            __builtin_amdgcn_s_sleep(2);
          }
          TS(5, s, 128);
          X_ASK(s)
          v4u t[ST ? 16 : 1];
          if (ST) {
            int so = s * 16384;
#pragma unroll
            for (int i = 0; i < 16; ++i) {
              if ((i & 3) == 0) asm volatile("" : "+s"(so));
              t[i] = LD128(rsS, l16 + (i & 3) * 1024, so);
              if ((i & 3) == 3) so += 4096;
              __builtin_amdgcn_sched_barrier(0);
            }
          }
          mk = MARKS(s + 1);
          unsigned z = 1u, fr = 1u;
          if (GEN_TR != 0 && ST) fr = FREE_LOOK(s + 1);
          if (GEN_TR != 0 && ST) WAIT_VM(18);
          else if (ST) WAIT_VM(17);
          else WAIT_VM(1);
#pragma unroll 1
          for (int k = b0; k < b1; k += 3) {
            const unsigned* q = reinterpret_cast<const unsigned*>(smem + (s & 1) * 32768 + k * 4096 + l16 + 4);
            const unsigned* q2 = q + min(b1 - k - 1, 2) * 1024;
#pragma unroll
            for (int i = 0; i < 12; i += 2) z = i < 8 ? umin3(z, q[256 * i], q[256 * i + 256]) : umin3(z, q2[256 * (i - 8)], q2[256 * (i - 7)]);
          }
          if (ST) {
#pragma unroll
            for (int i = 0; i < 16; ++i) z = umin3(z, t[i].y, t[i].w);
          }
          due = __all(z != 0u);
          if (!due) {
            due = KR && sf != s;
            sf = s;
            continue;
          }
          asm volatile("" ::: "memory");
          if (l == 0 && !(ST && h4)) __hip_atomic_fetch_add((__attribute__((address_space(3))) unsigned*)cn + (s & 1), h4 ? 2u : 3u, __ATOMIC_RELAXED, __HIP_MEMORY_SCOPE_WORKGROUP);
          TS(6, s, 128);
          if constexpr (ST) {
            stats(t, s);
            TS(7, s, 192);
#pragma unroll 1
            for (unsigned spins = 0; GEN_TR != 0 && s >= (1 << GEN_TR) - 1 && !__all(fr != 0u); ++spins) {
              if (spin_timeout(spins, TMO)) return;
              __builtin_amdgcn_s_sleep(SLP_H);
              fr = FREE_LOOK(s + 1);
              asm volatile("" ::: "memory");
            }
          }
          due = __all(mk != 0u);
          const unsigned td = latest(mk, RFL(mk)) + (h4 ? KD : PD) + (s < 7 - R0 ? K1 : s >= NSEG - 1 - TS1 ? -TL3 : 0);
          if (!ST && mc && s < NSEG - 1 && s) { MEM_CLEAR(s + 1) }
          if (h4 && (!GEN_TR || s < (1 << GEN_TR) - 2)) {
            if (KCT && due) gate(td - KCT);
            KV_CLEAR(s + 2)
          }
          if (due) gate(td);
          ++s;
        }
      };
      static_assert(HX0 == 6, "blocks");
      if (w == 3) feed(Yes{});
      else feed(No{});
      return;
    }
    unsigned tn = now10();
    for (int s = RFL((int)early); s < NSEG; ++s) {
#pragma unroll 1
      for (unsigned spins = 0; !__all(nf != 0u); ++spins) {
        if (spin_timeout(spins, TMO)) return;
        if (spins) __builtin_amdgcn_s_sleep(SLP_H);
        nf = FC_LOOK(s);
        asm volatile("" ::: "memory");
      }
      if (NFW) asm volatile("" ::"v"(nf));
      TS(4, s, 128);
#pragma unroll 1
      for (unsigned spins = 0; cn[2 + (s & 1)] < 4u * (s >> 1); ++spins) {
        if (spins > 64u * SPIN_CAP) return;
        __builtin_amdgcn_s_sleep(2);
      }
      if (s >= PACE0) gate(tn);
      else tn = now10();
      tn += PACE;
      TS(5, s, 128);
      X_ASK(s)
      if (w != 3) {
        nf = FC_LOOK(s + 1);
        if (mc && s < NSEG - 1 && (EL == 0 || MC3 != 2 || s)) {
          MEM_CLEAR(s + 1)
          if (NMC == 8) WAIT_VM(8);
          else WAIT_VM(4);
        } else {
          WAIT_VM(0);
        }
        if (l == 0) __hip_atomic_fetch_add((__attribute__((address_space(3))) unsigned*)cn + (s & 1), 3u, __ATOMIC_RELAXED, __HIP_MEMORY_SCOPE_WORKGROUP);
        TS(6, s, 128);
      } else {
        v4u t[16];
        S_ASK(s, t)
        WAIT_VM(16);
        nf = FC_LOOK(s + 1);
        if (l == 0) __hip_atomic_fetch_add((__attribute__((address_space(3))) unsigned*)cn + (s & 1), 3u, __ATOMIC_RELAXED, __HIP_MEMORY_SCOPE_WORKGROUP);
        stats(t, s);
        TS(7, s, 192);
      }
    }
    return;
  }
  const int RL = !kv, rp = idx & 15, u = RFL(kv ? idx >> 4 : L);
  const __amdgpu_buffer_rsrc_t rsW = MKRS(ws + OFF_PW, SZ_PW);
  const __amdgpu_buffer_rsrc_t rsX = MKRS(ws + (RL ? OFF_X3 : OFF_XA), SZ_X);
  const __amdgpu_buffer_rsrc_t rsY = RL ? MKRS(A.out, SZ_X) : MKRS(ws + OFF_X2, SZ_X);
  const int r = 2 * rp + (g >> 1), yo = r * (DM * 2) + (l & 31) * 16 + u * 512;
  const __amdgpu_buffer_rsrc_t rsC = MKRS(ws + OFF_H, OFF_QK - OFF_H);
#define X_CLEAR(s)                                                                                              \
  {                                                                                                             \
    ST128((v4u{0u, 0u, 0u, 0u}), rsY, yo, (s) * SEG * DM * 2);                                                  \
    if (KD) {                                                                                                   \
      ST128((v4u{0u, 0u, 0u, 0u}), rsC, l * 16, (int)(OFF_X3 - OFF_H) + ((s) * 32 + idx) * 1024);               \
      if (l < 32) ST128((v4u{0u, 0u, 0u, 0u}), rsC, l * 16, (int)(SZ_X / 2) + ((s) * 32 + idx) * 512);          \
    }                                                                                                           \
  }
  if (kv) {
    if (XZD) gate(now10() + XZD);
    if (XZP & 3) {
      if (!wait_n<SLP_V, 32>(FL + FL_FZ + (blockIdx.x & 7) * 64, 64, TMO)) return;
    } else {
      for (int s = w; s < (XZP ? 2 * RG : NSEG); s += RG) X_CLEAR(s)
      DRAIN();
      if (l < 8) set_flag(FL + FL_FZ + l * 64 + 2 * idx + w);
    }
  }
  v4u sel;
#pragma unroll
  for (int e = 0; e < 4; ++e) sel[e] = (g >> 1) == (c >> 3) && ((c & 7) >> 1) == e ? 0x3f80u << (16 * (c & 1)) : 0u;
  unsigned tl = now10();
  int po = (g & 1) * 32768 + (c >> 2) * 128 + (g >> 1) * 64 + (c & 3) * 16;
  asm volatile("" : "+v"(po));
  for (int s = w; s < NSEG; s += RG) {
    v4u xr;
    if (GF_R) {
      gate(tl);
      const gu32* f = (gu32*)(FL + FL_FW) + ((RL * 64 + s) * 4 + (blockIdx.x & 3)) * 4 * 32 + (c >> 2) * 32 + (c & 3);
      unsigned mk;
      for (unsigned spins = 0;; ++spins) {
        tl = now10() + GF_R;
        mk = __hip_atomic_load(f, RLX_AGENT);
        if (__all(mk != 0u) || spin_timeout(spins, TMO)) break;
        if (s < NSEG - RFA) __builtin_amdgcn_s_sleep(SLP_R);
      }
      if (EF) {
        const unsigned tp = latest(mk, tl);
        asm volatile("" ::: "memory");
        xr = LD128(rsX, (l & 31) * 512 + r * 16, s * 32768 + u * 16384);
        gate(tp - (RL ? (s >= NSEG - RLS ? RLEAD : 0) : s >= NSEG - TS1 ? TL1 : 0));
        if (GR2) tl = tp + ((unsigned)(s - GR0) >= NSEG - 12 - GR0 ? GR3 : GR2);
      }
      asm volatile("" ::: "memory");
    } else
    grp_wait<SLP_R, 4, GATE_R>(true, nullptr, 0, FL + FL_FW + ((RL * 64 + s) * 4 + (blockIdx.x & 3)) * 4 * 32, 16, TMO, tl);
    if (s >= NSEG - 6 && !(EF != 0 && GR2 != 0)) tl -= GF_R - (s >= NSEG - 4 ? GF_RT : GF_RU);
    if (!EF) DRAIN();
    TS(0, s, w * 64);
    v4u p[8][2];
    if (!EF) xr = LD128(rsX, (l & 31) * 512 + r * 16, s * 32768 + u * 16384);
    f32x4 ac[2];
    const int sr = EF ? s & ((1 << RGB) - 1) : s;
    const unsigned old = (s >> RGB) & 1;
    for (unsigned spins = 0;; ++spins) {
      int so = (RL * 64 + sr) * 16 * 32768 + rp * 2048 + u * 1024;
#pragma unroll
      for (int k = 0; k < 8; ++k) {
        asm volatile("" : "+s"(so));
#pragma unroll
        for (int b = 0; b < 2; ++b) p[k][b] = __builtin_amdgcn_raw_buffer_load_b128(rsW, po + b * 512, so, RAUX);
        so += 65536;
      }
      __builtin_amdgcn_sched_barrier(0);
      unsigned z[2] = {1u, 1u};
      ac[0] = ac[1] = f32x4{};
#pragma unroll
      for (int k = 0; k < 8; ++k)
#pragma unroll
        for (int b = 0; b < 2; ++b) {
          ac[b] = MFMA16(BF8(sel), BF8(p[k][b]), ac[b]);
          z[b] = __builtin_amdgcn_bitop3_b32(z[b], p[k][b].x, old, 0x60);
          __builtin_amdgcn_sched_barrier(0);
        }
      if (!EF || !RVAL || __all((z[0] & z[1]) != 0u) || spin_timeout(spins, TMO)) break;
      if (SLP_D) __builtin_amdgcn_s_sleep(SLP_D);
      asm volatile("" ::: "memory");
    }
    v4u xo;
    {
      const auto x = __builtin_amdgcn_permlane16_swap(pk2s(ac[0][0], ac[0][1]), pk2s(ac[1][0], ac[1][1]), false, false);
      const auto y = __builtin_amdgcn_permlane16_swap(pk2s(ac[0][2], ac[0][3]), pk2s(ac[1][2], ac[1][3]), false, false);
      const v4u a = {x[0], y[0], x[1], y[1]};
#pragma unroll
      for (int e = 0; e < 4; ++e) {
        xo[e] = pk2s(lo16(xr[e]) + lo16(a[e]), hi16(xr[e]) + hi16(a[e]));
        if (kv) xo[e] = xo[e] ? xo[e] : 0x8000u;
      }
      ST128(xo, rsY, yo, s * SEG * DM * 2);
    }
    TS(1, s, w * 64);
    if (kv) {
      const unsigned p0 = __builtin_amdgcn_readlane(xo[0], 0), p1 = __builtin_amdgcn_readlane(xo[0], 32);
      const float pv = lo16(g < 2 ? p0 : p1);
      float sm = 0.f, q0 = 0.f, q1 = 0.f;
#pragma unroll
      for (int e = 0; e < 4; ++e) {
        const float a = one(lo16(xo[e]) - pv), b = one(hi16(xo[e]) - pv);
        sm = one(sm + one(a + b));
        q0 = one(q0 + a * a);
        q1 = one(q1 + b * b);
      }
      sm = half_sum(sm);
      const float q = fmaxf(one(half_sum(q0 + q1)) - sm * sm * (1.0f / 256), 0.f);
      const u64 st = (u64)__float_as_uint(sm + pv * 256.f) | (u64)(__float_as_uint(q) | 1u) << 32;
      if ((l & 31) < 4) __hip_atomic_store((gu64*)(FL + FL_FR + ((s * 4 + (l & 3)) * 4 + (r >> 3)) * 32 + (r & 7) * 4 + 2 * u), st, RLX_AGENT);
      if (XZP == 4 && s + 2 * RG < NSEG) X_CLEAR(s + 2 * RG)
    }
    if (EF && s + (1 << RGB) < NSEG) PW_FREE(RL, s + (1 << RGB), 2 * rp + u);
    TS(3, s, w * 64);
  }
  if (L == 1 && idx == 0 && w == 0 && l == 0) __hip_atomic_store((gu64*)(ws + OFF_EP), tag, RLX_AGENT);
}
__device__ __forceinline__ void role_proj(const MegaArgs& A, char* smem, const int L, const int idx, const bool kv) {
  if (A.wv < 4) role_proj_t<false>(A, smem, L, idx, kv);
  else role_proj_t<true>(A, smem, L, idx, kv);
}

constexpr int CUN = 2144;
__device__ __forceinline__ v2u part_stats(const v4u o) {
  float v[8];
#pragma unroll
  for (int e = 0; e < 4; ++e) { v[2 * e] = lo16(o[e]); v[2 * e + 1] = hi16(o[e]); }
  const float s = ((v[0] + v[1]) + (v[2] + v[3])) + ((v[4] + v[5]) + (v[6] + v[7])), m = s * 0.125f;
  float q0 = 0.f, q1 = 0.f;
#pragma unroll
  for (int e = 0; e < 4; ++e) {
    const float a = v[e] - m, b = v[4 + e] - m;
    q0 += a * a;
    q1 += b * b;
  }
  return v2u{__float_as_uint(s), __float_as_uint(q0 + q1) | 1u};
}
#define PS_ST(o, voff, soff)                                                                                              \
  {                                                                                                                       \
    const v2u p_ = part_stats(o);                                                                                         \
    if (PSW) {                                                                                                            \
      const v4u q_ = {p_.x, p_.y, (unsigned)__builtin_amdgcn_update_dpp(0, (int)p_.x, 0xb1, 0xf, 0xf, true),              \
                      (unsigned)__builtin_amdgcn_update_dpp(0, (int)p_.y, 0xb1, 0xf, 0xf, true)};                         \
      if (!(l & 1)) ST128(q_, rps, voff, soff);                                                                           \
    } else {                                                                                                              \
      ST64(p_, rps, voff, soff);                                                                                          \
    }                                                                                                                     \
  }
constexpr int CRN = 176;
constexpr int CTN = 528;
constexpr int CV_LDS = (8 * CKN + 8 * 2 * CUN + 64 + 2 * 32 * 8 + 16 * CRN + 64 + 8 * CTN) * 2 + 8 * 1568;
__device__ __forceinline__ void role_conv(const MegaArgs& A, char* smem, const int cv) {
  u16* Kr = reinterpret_cast<u16*>(smem);
  u16* Uc = Kr + 8 * CKN;
  u16* Us = Uc + 8 * CUN + 63;
  u16* To = Uc + 8 * 2 * CUN + 64;
  u16* Sx = To + 32 * 8;
  u16* Rc = Sx + 32 * 8;
  u16* Rs = Rc + 8 * CRN + 63;
  u16* Ta = Rc + 16 * CRN + 64;
  const int w = A.wv;
  const int l = lane_id(), tid = 64 * w + l, c = l & 15, g = l >> 4;
  const int d = 8 * cv + w;
  char* ws = A.ws;
  unsigned* FL = (unsigned*)(ws + OFF_FL);
  unsigned* TMO = FL + FL_TMO;
  u16* kr = Kr + w * CKN;
  const u16* ka = kr + 2080 - 16 * c + 8 * g;
#define KFRAG(D0) (*reinterpret_cast<const f16x8*>(ka - (D0)))
  typedef __attribute__((address_space(3))) const unsigned* lpu;
  typedef __attribute__((address_space(3))) u16* lph;
#define BFRAG(a) __builtin_bit_cast(f16x8, (v4u{((lpu)(unsigned long)(a))[0], ((lpu)(unsigned long)(a))[1], ((lpu)(unsigned long)(a))[2], ((lpu)(unsigned long)(a))[3]}))
  f32x4 acc[8], al[5];
  v4u tx[4];
  const __amdgpu_buffer_rsrc_t rz1 = MKRS(ws + OFF_XA, SZ_X), rps = MKRS(ws + OFF_H, SZ_X);
  u16* uc = Uc + w * CUN;
  const unsigned ub = lds_base(reinterpret_cast<char*>(((c & 1) ? Us : Uc) + w * CUN + c + 8 * g + 32));
  const int t0 = 16 * c + 4 * g;
  float dsk, ysc;
  unsigned mk = 0u;
  const __amdgpu_buffer_rsrc_t rm = MKRS(FL + FL_FW + (blockIdx.x & 3) * 128, 63 * 2048 + 512);
#define SKIP(T) (*reinterpret_cast<const v2u*>(uc + 32 + 256 * (T) + t0))
#define TILE_OUT(dst, a, uu)                                                                   \
  {                                                                                            \
    v2u v_;                                                                                    \
    _Pragma("unroll") for (int e = 0; e < 2; ++e) {                                            \
      const unsigned yy = pk2(a[2 * e] * ysc, a[2 * e + 1] * ysc), u_ = uu[e];                 \
      const unsigned k_ = pk2(h2f((u16)u_) * dsk, h2f((u16)(u_ >> 16)) * dsk);                 \
      v_[e] = pk2(lo16(yy) + lo16(k_), hi16(yy) + hi16(k_));                                   \
    }                                                                                          \
    *reinterpret_cast<v2u*>((dst) + t0) = v_;                                                  \
  }
#define CHUNK_ST(i, src, N) CHUNK_STX(w + 8 * (i), tx[i], src, N)
#define CHUNK_STX(i, X, src, N)                                                                \
  {                                                                                            \
    const unsigned p_ = lds_base((char*)((src) + (c >> 2) * (N) + (tid & ~15) + 4 * (c & 3))); \
    const v2u a0_ = trd(p_), a1_ = trd(p_ + 8 * (N));                                          \
    const v4u a_ = {a0_.x, a0_.y, a1_.x, a1_.y};                                               \
    v4u o;                                                                                     \
    _Pragma("unroll") for (int e = 0; e < 4; ++e) o[e] = pk2(lo16(X[e]) + lo16(a_[e]), hi16(X[e]) + hi16(a_[e])); \
    ST128(o, rz1, (l >> 5) * 32768 + (l & 31) * 16, (2 * (i) * 64 + cv) * 512);                \
    PS_ST(o, (l >> 5) * 16384 + (l & 31) * 8, (2 * (i) * 64 + cv) * 256)                        \
  }
#define CHUNK_FLAG(i) set_flag(FL + FL_FC + ((8 * (i) + l) * 8 + (cv >> 3)) * 32 + (cv & 7))
  {
#define U_VAL(i)                                                                               \
  {                                                                                            \
    const float mb_ = lo16(st[i]), ri_ = 1024.0f / hi16(st[i]);                                 \
    _Pragma("unroll") for (int e = 0; e < 4; ++e) {                                            \
      const unsigned t_ = pk2(lo16(tx[i][e]) - mb_, hi16(tx[i][e]) - mb_);                     \
      const unsigned q_ = pk2(lo16(t_) * ri_, hi16(t_) * ri_);                                 \
      uv[e] = pkh(lo16(q_), hi16(q_));                                                         \
    }                                                                                          \
  }
    unsigned ta[3], k0p, st[4];
    v4u uv;
    v2u kn;
    u64 tag;
    const __amdgpu_buffer_rsrc_t rst = MKRS(ws + OFF_GT, (L_SEQ + 512) * 16);
    {
      KGen G;
      G.load(A, d, l);
      const u16 pk = A.Dskip[d];
      asm volatile("" ::: "memory");
      if (XL == 1) ask_slow(A, 16 * (w - 4) + (cv & 15), ta);
      if (XL) asm volatile("" : "+v"(G.pa), "+v"(G.pb), "+v"(G.pc), "+v"(G.pd), "+v"(G.pe)::"memory");
      tx[0] = *reinterpret_cast<const v4u*>(A.x0 + (size_t)tid * DM + 8 * cv);
      if (XL != 1) ask_slow(A, 16 * (w - 4) + (cv & 15), ta);
      tag = call_tag(A);
      G.setup();
      dsk = bf2f(pk) * (1.0f / 1024);
      G.st = smem + w * 16640;
      SST(0);
      ysc = G.scale();
      const Cx z = G.power(1.f);
      Cx y = cmul(G.C, z);
      G.rows(G.st + l * 8, y, z, 16);
      G.get(G.bh, G.bl, l);
      const float k0 = 2.0f * wave_sum(G.C.r.x + G.C.r.y);
      k0p = l == 0 ? pkh(k0, 0.f) : 0u;
      if (TP) {
        constexpr int R = 64 * TP, U0 = 2 * R + 32, U1 = 3 * R + 111, KN = 4 * R + 192;
        u16* k1 = reinterpret_cast<u16*>(smem + 8 * 16640) + w * KN;
        u16* t1 = reinterpret_cast<u16*>(smem + 8 * 16640) + 8 * KN;
        v4u s1 = {};
        u64 rv = 0;
        if (w < TP) {
          s1 = LD128(rst, tid * 16, (L_SEQ + R * (cv >> 4)) * 16);
          rv = ready_look(A);
        }
        if (l < R / 8 + 4) reinterpret_cast<v4u*>(k1)[l < R / 8 ? l + R / 8 + 4 : l - R / 8] = v4u{k0p, 0u, 0u, 0u};
        k1[U0 + (l < 32 ? l : R + l)] = k1[U1 + (l < 32 ? l : R + l)] = 0;
        G.columns(l, 0, R / 16);
        const f32x4 kt = G.tile<true>(l);
        if (c < R / 16) *reinterpret_cast<v2u*>(k1 + R + 28 - 16 * c - 4 * g) = v2u{pkh(kt.w, kt.z), pkh(kt.y, kt.x)};
        if (w < TP) {
          for (unsigned spins = 0; !__all(has_tag(tag, s1.x, s1.y) && has_tag(tag, s1.z, s1.w)) && spins < SPIN_CAP; ++spins) {
            __builtin_amdgcn_s_sleep(4);
            s1 = LD128(rst, tid * 16, (L_SEQ + R * (cv >> 4)) * 16);
          }
          st[0] = __builtin_amdgcn_perm(s1.w, s1.y, 0x07060302);
          U_VAL(0);
          u16* up = reinterpret_cast<u16*>(smem + 8 * 16640) + 32 + tid;
#pragma unroll
          for (int e = 0; e < 8; ++e) up[e * KN + U0] = up[e * KN + U1] = (u16)(uv[e >> 1] >> (16 * (e & 1)));
        }
        RAW_BAR();
        const unsigned u1 = lds_base(reinterpret_cast<char*>(k1 + ((c & 1) ? U1 : U0) + c + 8 * g));
        f32x4 a = {};
#pragma unroll
        for (int b = 0; b <= R / 32; ++b)
          a = MFMAH(BFRAG(u1 + 64 * b), *reinterpret_cast<const f16x8*>(k1 + R - 16 * (c & (R / 16 - 1)) + 8 * g + 32 * b), a);
        if (c < R / 16) {
          const v2u sk = *reinterpret_cast<const v2u*>(k1 + U0 + 32 + t0);
          TILE_OUT(t1 + w * (R + 16), a, sk);
        }
        RAW_BAR();
        SST(1);
        if (w < TP) CHUNK_ST(0, t1, R + 16);
        G.columns(l, R / 16, TPS);
        if (w < TP) {
          DRAIN();
          ready_wait(A, tag, rv);
          if (l < 8) CHUNK_FLAG(w);
        }
        SST(6);
        G.columns(l, TPS, 16);
      } else {
        G.columns(l, 0, 16);
      }
      const f32x4 kt = G.tile<true>(l);
      kn = v2u{pkh(kt.w, kt.z), pkh(kt.y, kt.x)};
    }
#define U_PUT(i)                                                                               \
  _Pragma("unroll") for (int e = 0; e < 4; ++e) {                                              \
    ((lph)(unsigned long)up0)[(2 * e) * CUN + 512 * (i)] = (u16)uv[e];                         \
    ((lph)(unsigned long)up0)[(2 * e + 1) * CUN + 512 * (i)] = (u16)(uv[e] >> 16);             \
    ((lph)(unsigned long)up1)[(2 * e) * CUN + 512 * (i)] = (u16)uv[e];                         \
    ((lph)(unsigned long)up1)[(2 * e + 1) * CUN + 512 * (i)] = (u16)(uv[e] >> 16);             \
  }
#define U_ROW(i) { U_VAL(i) U_PUT(i) }
    unsigned up0 = lds_base((char*)(Uc + 32 + tid)), up1 = lds_base((char*)(Us + 32 + tid));
    asm volatile("" : "+v"(up0), "+v"(up1), "+v"(kn), "+v"(k0p));
#define ST_GET(I0, I1)                                                                                                        \
  for (unsigned spins = 0; spins < SPIN_CAP; ++spins) {                                                                       \
    bool ok = true;                                                                                                           \
    v4u t_[4];                                                                                                                \
    _Pragma("unroll") for (int i = I0; i < I1; ++i) t_[i] = LD128(rst, (tid + 512 * i) * 16, 0);                              \
    _Pragma("unroll") for (int i = I0; i < I1; ++i) {                                                                         \
      ok = ok && has_tag(tag, t_[i].x, t_[i].y) && has_tag(tag, t_[i].z, t_[i].w);                                            \
      st[i] = __builtin_amdgcn_perm(t_[i].w, t_[i].y, 0x07060302);                                                            \
    }                                                                                                                         \
    if (__all(ok)) break;                                                                                                     \
    __builtin_amdgcn_s_sleep(4);                                                                                              \
    asm volatile("" ::: "memory");                                                                                            \
  }
    if (w < 4) {
      ST_GET(0, 2);
      U_VAL(0);
      asm volatile("" : "+v"(uv));
    }
#pragma unroll
    for (int i = 1; i < 4; ++i) tx[i] = *reinterpret_cast<const v4u*>(A.x0 + (size_t)(tid + 512 * i) * DM + 8 * cv);
    RAW_BAR();
    SST(2);
    if (w < 4) { U_PUT(0) }
    *reinterpret_cast<v2u*>(kr + 2076 - 16 * c - 4 * g) = kn;
    if (l < 38) reinterpret_cast<v4u*>(kr)[l < 32 ? 260 + l : l < 34 ? 194 + l : l - 34] = v4u{k0p, 0u, 0u, 0u};
    (l < 32 ? uc : Us + w * CUN - 32)[l] = 0;
    uc[32 + L_SEQ + l] = Us[w * CUN + 32 + L_SEQ + l] = 0;
    uc[32 + 256 + l] = Us[w * CUN + 32 + 256 + l] = 0;
    RAW_BAR();
    SST(11);
    {
      f32x4 a0 = {}, a1 = {};
      f16x8 bf[9], af[9];
#pragma unroll
      for (int b = 0; b < 9; ++b) {
        af[b] = KFRAG(32 - 32 * b);
        bf[b] = BFRAG(ub + 64 * (b - 1));
      }
      const v2u k0 = SKIP(0);
      __builtin_amdgcn_sched_barrier(0);
#pragma unroll
      for (int b = 0; b < 9; ++b) (b & 1 ? a1 : a0) = MFMAH(bf[b], af[b], (b & 1 ? a1 : a0));
      a0 += a1;
      TILE_OUT(Ta + w * CTN, a0, k0);
    }
    const u64 rv = ready_look(A);
    RAW_BAR();
    SST(3);
    if (w < 4) {
      if (w >= TP) CHUNK_ST(0, Ta, CTN);
      asm volatile("" : "+v"(tx[1]));
      U_ROW(1);
      DRAIN();
      ready_wait(A, tag, rv);
      if (l < 8 && w >= TP) CHUNK_FLAG(w);
      ST_GET(2, 4);
    } else {
      ST_GET(0, 4);
      ready_wait(A, tag, rv);
      asm volatile("" : "+v"(tx[0]), "+v"(tx[1]));
      U_ROW(0);
      U_ROW(1);
    }
    ASKED(ta);
    if (tid < (16 * CRN + 64) / 8) reinterpret_cast<v4u*>(Rc)[tid] = v4u{0u, 0u, 0u, 0u};
    SST(4);
    const gu64* fk = (gu64*)(ws + OFF_KG) + cv * 16 + w;
    u64 kg = __hip_atomic_load(fk, RLX_AGENT);
    U_ROW(2);
    for (unsigned spins = 0; !__all(kg == tag) && spins < SPIN_CAP; ++spins) {
      __builtin_amdgcn_s_sleep(8);
      kg = __hip_atomic_load(fk, RLX_AGENT);
    }
    asm volatile("" : "+v"(tx[3]), "+v"(st[3])::"memory");
    SST(5);
    const unsigned* kd = reinterpret_cast<const unsigned*>(Ta + 8 * CTN) + w * 392;
    glds16c(ws + OFF_KF + d * 2048, l * 16, lds_base((char*)kd));
    if (l < 34) glds16c(ws + OFF_KF + d * 2048 + 1024, l * 16, lds_base((char*)kd) + 1024);
    U_ROW(3);
    DRAIN();
    {
#define MIX(lo, hi) __builtin_amdgcn_perm(hi, lo, 0x07060100)
      const unsigned a0 = kd[384 - l], b0 = kd[383 - l], a1 = kd[320 - l], b1 = kd[319 - l];
      const unsigned a2 = kd[256 - 2 * l], m2 = kd[255 - 2 * l], b2 = kd[254 - 2 * l];
      const unsigned* k3 = kd + 124 - 4 * (l & 31);
      v4u* ko = reinterpret_cast<v4u*>(kr) + l;
      ko[4] = v4u{a0, a0, (l & 1) ? b0 : MIX(a0, b0), b0};
      ko[68] = v4u{a1, a1, (l & 1) ? b1 : MIX(a1, b1), b1};
      ko[132] = v4u{a2, MIX(a2, m2), m2, b2};
      if (l < 32) ko[196] = v4u{k3[4], MIX(k3[3], k3[2]), k3[2], MIX(k3[1], k3[0])};
#undef MIX
    }
    RAW_BAR();
    {
      const f16x8 bm = BFRAG(ub - 64);
      f32x4 a1 = MFMAH(bm, KFRAG(256 + 32), (f32x4{})), a2 = MFMAH(bm, KFRAG(512 + 32), (f32x4{}));
#pragma unroll 2
      for (int r = 0; r < 8; ++r) {
        f16x8 a[3], bf[3];
#pragma unroll
        for (int e = 0; e < 3; ++e) {
          a[e] = KFRAG(256 * e - 32 * r);
          bf[e] = BFRAG(ub + 512 * e + 64 * r);
        }
#pragma unroll
        for (int e = 0; e < 3; ++e) {
          a2 = MFMAH(bf[2 - e], a[e], a2);
          if (e < 2) a1 = MFMAH(bf[1 - e], a[e], a1);
        }
      }
      SST(8);
      const v2u k1 = SKIP(1), k2 = SKIP(2);
      TILE_OUT(Ta + w * CTN + 256, a1, k1);
      TILE_OUT(Ta + w * CTN, a2, k2);
    }
    RAW_BAR();
    SST(10);
    if (w < 4) {
      tx[0] = tx[1];
      tx[1] = tx[2];
      tx[2] = tx[3];
    }
    CHUNK_STX(w < 4 ? w + 8 : w, tx[0], Ta, CTN);
    {
      const f16x8 bm = BFRAG(ub - 64);
#pragma unroll
      for (int T = 3; T < 8; ++T) al[T - 3] = MFMAH(bm, KFRAG(256 * T + 32), (f32x4{}));
    }
    SST(7);
    if (RSK && w >= 4) __builtin_amdgcn_s_sleep(RSK);
#undef U_ROW
#undef U_VAL
#undef U_PUT
#undef ST_GET
  }
  const unsigned rb = lds_base(reinterpret_cast<char*>(((c & 1) ? Rs : Rc) + w * CRN + c + 8 * g));
#pragma unroll
  for (int j = 0; j < 8; ++j) acc[j] = f32x4{};
  const __amdgpu_buffer_rsrc_t rx = MKRS(ws + OFF_X2, SZ_X), rz = MKRS(ws + OFF_X3, SZ_X);
  const __amdgpu_buffer_rsrc_t rf = MKRS(FL + FL_FR, 64 * 16 * 128);
  v2u st = {0u, 0u}, xx = {0u, 0u};
#define X2_LOAD(ss, far)                                                                                                      \
  {                                                                                                                      \
    xx = LD64(rx, (l >> 1) * (DM * 2) + (l & 1) * 8, (far) ? (int)SZ_X : ((ss) * SEG * DM + 8 * cv) * 2);                 \
    st = LD64(rf, l * 8, ((ss) * 4 + (blockIdx.x & 3)) * 512);                                                           \
  }
  unsigned tl = now10();
  static_assert(R0 >= 2 && PP > 8 - R0, "rounds");
  if (CVP && w == 1) __builtin_amdgcn_s_setprio(CVP);
  for (int s = -R0; s < NSEG; ++s) {
    const int q = s & 3, q8 = s & 7;
    const u16* kq = ka + 32 * q8;
    if (w == 1 && s >= 0) {
      gate(tl);
      X2_LOAD(s, 0);
    }
    asm volatile("" ::: "memory");
    if (s < 8 - R0) {
      const int r = s + R0;
      const u16* kl = ka + 32 * r - 256 * 7;
      unsigned uq[4] = {ub + 64 * r, ub + 64 * r + 1024, ub + 64 * r + 2048, ub + 64 * r + 3072};
      asm volatile("" : "+v"(uq[0]), "+v"(uq[1]), "+v"(uq[2]), "+v"(uq[3]));
      f16x8 bf[8], a[8];
#pragma unroll
      for (int e = 7; e >= 0; --e) {
        a[e] = *reinterpret_cast<const f16x8*>(kl + 256 * (7 - e));
        bf[7 - e] = BFRAG(uq[(7 - e) >> 1] + 512 * ((7 - e) & 1));
      }
      __builtin_amdgcn_sched_barrier(0);
#pragma unroll
      for (int e = 7; e >= 0; --e)
#pragma unroll
        for (int T = (e > 3 ? e : 3); T < 8; ++T) al[T - 3] = MFMAH(bf[T - e], a[e], al[T - 3]);
      if (r == 1) {
        DRAIN();
        if (l < 8) CHUNK_FLAG(w < 4 ? w + 8 : w);
      }
      if (s == -1) SST(9);
      if (s == -1 && w == 1) {
        (void)wait_n<SLP_V, 32>(FL + FL_FZ + (blockIdx.x & 7) * 64, 64, TMO);
#pragma unroll 1
        for (unsigned spins = 0; spins < SPIN_CAP; ++spins) {
          mk = MARKS(0);
          if (__all(mk != 0u)) break;
          __builtin_amdgcn_s_sleep(16);
          asm volatile("" ::: "memory");
        }
        tl = latest(mk, tl) + CE;
      }
      if (s < 0) continue;
    }
    f16x8 a0 = *reinterpret_cast<const f16x8*>(kq - 32), a1 = *reinterpret_cast<const f16x8*>(kq);
    asm volatile("" : "+v"(a0), "+v"(a1));
    if (w == 1) {
#pragma unroll 1
      for (unsigned spins = 0; !__all(umin3(xx.x, xx.y, st.y) != 0u); ++spins) {
        if (spin_timeout(spins, TMO)) break;
        __builtin_amdgcn_s_sleep(SLP_V);
        X2_LOAD(s, __builtin_popcountll(__ballot(st.y != 0u)) < CVT2);
      }
      TS(0, s, 64);
      const float s0 = __uint_as_float(st.x), s1 = dppr<0xb1>(s0), m2 = __uint_as_float(st.y);
      const float var = (m2 + dppr<0xb1>(m2) + (s0 - s1) * (s0 - s1) * (1.0f / DM)) * (1.0f / DM);
      const float mb = bfr1((s0 + s1) * (1.0f / DM)), ri = 1024.0f * __builtin_amdgcn_rcpf(bfr1(__builtin_amdgcn_sqrtf(bfr1(bfr1(var) + 1e-5f))));
      const unsigned t0 = pk2(lo16(xx.x) - mb, hi16(xx.x) - mb), t1 = pk2(lo16(xx.y) - mb, hi16(xx.y) - mb);
      const unsigned b0 = pk2(lo16(t0) * ri, hi16(t0) * ri), b1 = pk2(lo16(t1) * ri, hi16(t1) * ri);
      const unsigned u0 = pkh(lo16(b0), hi16(b0)), u1 = pkh(lo16(b1), hi16(b1));
      u16* rp = Rc + (l & 1) * 4 * CRN + 32 * q + (l >> 1);
#define R_PUT(o) { rp[o] = (u16)u0; rp[(o) + CRN] = (u16)(u0 >> 16); rp[(o) + 2 * CRN] = (u16)u1; rp[(o) + 3 * CRN] = (u16)(u1 >> 16); }
      R_PUT(0) R_PUT(8 * CRN + 63)
      if (q == 0) { R_PUT(128) R_PUT(8 * CRN + 63 + 128) }
#undef R_PUT
      *reinterpret_cast<v2u*>(Sx + l * 4) = xx;
    }
    unsigned ap = rb + 64 * ((q + 3) & 3), ac = rb + 64 * q, au = lds_base((char*)(Rc + w * CRN + 32 * q + c));
    asm volatile("" : "+v"(ap), "+v"(ac), "+v"(au));
    RAW_BAR();
    TS(1, s, 0);
    const f16x8 bp = BFRAG(ap), bc = BFRAG(ac);
    const u16 h0 = ((lph)(unsigned long)au)[0], h1 = ((lph)(unsigned long)au)[16], y0 = Sx[c * 8 + w], y1 = Sx[(16 + c) * 8 + w];
    __builtin_amdgcn_sched_barrier(0);
    const float u0 = h2f(h0), u1 = h2f(h1), x0 = bf2f(y0), x1 = bf2f(y1);
    acc[0] = MFMAH(a0, bp, (q8 == 0 ? acc[1] : acc[0]));
    const f32x4 tmp = MFMAH(a1, bc, acc[0]);
    const unsigned uu = pk2(u0 * dsk, u1 * dsk);
    const unsigned yy = pk2(((q & 1) ? tmp[2] : tmp[0]) * ysc, ((q & 1) ? tmp[3] : tmp[1]) * ysc);
    const unsigned aa = pk2(lo16(yy) + lo16(uu), hi16(yy) + hi16(uu)), oo = pk2(x0 + lo16(aa), x1 + hi16(aa));
    if (g == (q8 >> 1)) {
      To[c * 8 + w] = (u16)oo;
      To[(16 + c) * 8 + w] = (u16)(oo >> 16);
    }
    RAW_BAR();
    if (w == 0 && l < 32) {
      v4u o = *reinterpret_cast<const v4u*>(To + l * 8);
      o.y = o.y ? o.y : 0x8000u;
      ST128A(o, rz, l * 16, (s * 64 + cv) * 512, XAUX);
      PS_ST(o, l * 8, ((64 + s) * 64 + cv) * 256)
    }
    if (w == 1) {
      tl = now10() + GATE_V - 40;
      mk = MARKS(s + 1);
    }
    const int left = 7 - (s >> 3);
#define PUSH(J0, SH)                                                                                              \
  if (left >= (J0)) {                                                                                             \
    _Pragma("unroll") for (int j = (J0); j < (J0) + 2 && j < 8; ++j)                                              \
      acc[j] = MFMAH(*reinterpret_cast<const f16x8*>(kq - 32 - 256 * j), bp, (j + (SH) < 8 ? acc[(j + (SH)) & 7] : f32x4{})); \
  }
    TS(2, s, 0);
    if (w == 0) {
      DRAIN();
      if (l < 4) set_flag(FL + FL_FC + (((64 + s) * 4 + l) * 8 + (cv >> 3)) * 32 + (cv & 7));
    }
    TS(3, s, 0);
    if (q8 == 0) { PUSH(1, 1) PUSH(3, 1) PUSH(5, 1) PUSH(7, 1) }
    else { PUSH(1, 0) PUSH(3, 0) PUSH(5, 0) PUSH(7, 0) }
#undef PUSH
    if (w == 1 && __all(mk != 0u)) tl = latest(mk, tl) + (s >= NSEG - 1 - TS1 ? CE - TL2 : CE);
    if (s == 8 - R0) {
      v2u k[5];
#pragma unroll
      for (int T = 3; T < 8; ++T) k[T - 3] = SKIP(T);
#pragma unroll
      for (int T = 3; T < 8; ++T) TILE_OUT(uc + 32 + 256 * T, al[T - 3], k[T - 3]);
    }
    {
      const int n = 12 + PW * (s - PP), wi = n + ((w - n) & 7), wf = wi - 8 * (PW < 8);
      if (s > PP && wf < 32 && wf >= n - PW && wf < n) {
        DRAIN();
        if (l < 8) CHUNK_FLAG(wf);
      }
      if (s >= PP && wi < 32 && wi < n + PW) {
        CHUNK_STX(wi, tx[1], Uc + 32 + 64 * (wi & 24), CUN);
        tx[1] = tx[2];
        tx[2] = tx[3];
      }
    }
    MSTAMP();
  }
#undef KFRAG
#undef BFRAG
#undef X2_LOAD
#undef TILE_OUT
#undef SKIP
#undef CHUNK_ST
#undef CHUNK_STX
#undef CHUNK_FLAG
}

__device__ __forceinline__ v4u ln_row_packed(const v4u xr) {
  float v[8];
#pragma unroll
  for (int e = 0; e < 4; ++e) { v[2 * e] = lo16(xr[e]); v[2 * e + 1] = hi16(xr[e]); }
  float s = ((v[0] + v[1]) + (v[2] + v[3])) + ((v[4] + v[5]) + (v[6] + v[7]));
  s = wave_sum(s);
  const float mean = s * (1.0f / DM);
  float q = 0.f;
#pragma unroll
  for (int j = 0; j < 8; ++j) { float t = v[j] - mean; q += t * t; }
  q = wave_sum(q);
  const float var = q * (1.0f / DM);
  const float mb = bfr(mean);
  const float sd = bfr(sqrtf(bfr(bfr(var) + 1e-5f)));
  const float ri = 1.0f / sd;
  v4u o;
#pragma unroll
  for (int e = 0; e < 4; ++e) {
    const unsigned t = pk2(v[2 * e] - mb, v[2 * e + 1] - mb);
    const float x0 = lo16(t), x1 = hi16(t);
    float q0 = x0 * ri, q1 = x1 * ri;
    q0 = __builtin_fmaf(__builtin_fmaf(-q0, sd, x0), ri, q0);
    q1 = __builtin_fmaf(__builtin_fmaf(-q1, sd, x1), ri, q1);
    o[e] = pk2(q0, q1);
  }
  return o;
}

__device__ __forceinline__ void ln4_packed(v4u (&x)[4]) {
  f32x2_t v[16];
#pragma unroll
  for (int e = 0; e < 16; ++e) v[e] = f32x2_t{lo16(x[e >> 2][e & 3]), hi16(x[e >> 2][e & 3])};
  const f32x2_t t = (((v[0] + v[1]) + (v[2] + v[3])) + ((v[4] + v[5]) + (v[6] + v[7]))) +
                    (((v[8] + v[9]) + (v[10] + v[11])) + ((v[12] + v[13]) + (v[14] + v[15])));
  const float mean = red_mod4(t.x + t.y) * (1.0f / DM);
  const float mb = bfr1(mean);
  f32x2_t q0 = {0.f, 0.f}, q1 = {0.f, 0.f};
#pragma unroll
  for (int e = 0; e < 16; e += 2) {
    v[e] -= mb;
    v[e + 1] -= mb;
    q0 += v[e] * v[e];
    q1 += v[e + 1] * v[e + 1];
  }
  q0 += q1;
  const float dm = mean - mb;
  const float var = fmaxf(red_mod4(q0.x + q0.y) * (1.0f / DM) - dm * dm, 0.f);
  const float sd = bfr1(sqrtf(bfr1(bfr1(var) + 1e-5f)));
  const float ri = 1.0f / sd;
#pragma unroll
  for (int e = 0; e < 16; ++e) {
    const unsigned t2 = pk2(v[e].x, v[e].y);
    const f32x2_t q = f32x2_t{lo16(t2), hi16(t2)} * ri;
    x[e >> 2][e & 3] = pk2(q.x, q.y);
  }
}

constexpr int MEGA_WGS = 256;
constexpr size_t MEGA_LDS = (size_t)(32 * 4 * 64 * 8 + 8 * SMS + 15 * QS + 128 + 16 * 64 * 8 + 8 * SOS + 512) * 2;
static_assert(MEGA_LDS <= 163840 && MEGA_LDS - 480 >= C_SQ + 4096, "lds");
static_assert(MEGA_LDS >= (size_t)AW_CNT + 128 && MEGA_LDS >= (size_t)P_CN + 512, "lds");
static_assert(MEGA_LDS >= (size_t)CV_LDS, "lds");
static_assert(MEGA_LDS >= (size_t)2 * 32 * SMS * 2 && MEGA_LDS >= 131072, "lds");
static_assert(MEGA_LDS >= (size_t)8 * (16640 + 2 * (704 + 144)), "lds");
__device__ __forceinline__ MegaArgs role_args(const int b, const int wv, const unsigned k0, const unsigned k1) {
  auto p = (const __attribute__((address_space(4))) u64*)__builtin_amdgcn_kernarg_segment_ptr();
  asm volatile("s_waitcnt lgkmcnt(0)" : "+s"(p) : "s"(k0), "s"(k1));
#define KA(i) ((u16*)(__attribute__((address_space(1))) u16*)p[i])
  return MegaArgs{KA(0), KA(1), (char*)KA(2), KA(3), KA(4), KA(5), KA(6), KA(7), KA(8), KA(9), KA(10), KA(11), KA(12), KA(13), b, wv};
#undef KA
}
__global__ __launch_bounds__(512, 2) void mega_kernel(const u16* mem0, const u16* Dskip, char* ws, u16* out, const u16* x0,
                                                      const u16* logAre, const u16* Aim, const u16* Cre, const u16* Cim,
                                                      const u16* logdt, const u16* Wq, const u16* Wk, const u16* Wv,
                                                      const u16* Wo) {
  extern __shared__ __attribute__((aligned(16))) char smem[];
  unsigned k0 = 0u, k1 = 0u;
  if (IPF == 3) {
    unsigned o;
    asm volatile(
        "s_load_dword %0, %3, 0\n\ts_load_dword %1, %3, 64\n\ts_getpc_b64 s[6:7]\n.LKG_PC:\n\ts_bfe_u32 s4, %4, 0x30003\n\t"
        "v_lshl_add_u32 %2, %5, 3, s4\n\tv_lshlrev_b32 %2, 7, %2\n\ts_mov_b32 m0, %6\n\ts_mov_b32 s4, .LKG_LAST-.LKG_PC\n\t"
        "s_max_i32 s4, s4, .LKG_LAS2-.LKG_PC\n\tv_cmp_gt_i32 vcc, s4, %2\n\t"
        "s_and_saveexec_b64 s[4:5], vcc\n\tglobal_load_lds_dword %2, s[6:7]\n\ts_mov_b64 exec, s[4:5]"
        : "=&s"(k0), "=&s"(k1), "=&v"(o)
        : "s"(__builtin_amdgcn_kernarg_segment_ptr()), "s"(blockIdx.x), "v"(threadIdx.x), "s"(lds_base(smem) + (unsigned)MEGA_LDS - 480)
        : "memory", "vcc", "scc", "s4", "s5", "s6", "s7");
  }
  const int b = vbid(), wv = __builtin_amdgcn_readfirstlane(threadIdx.x >> 6);
#define A role_args(b, wv, k0, k1)
  u64 tag = 0;
  if (__builtin_expect((0xde000ff0u >> (b >> 3)) & 1u, 0)) {
    const unsigned t0 = now10();
    if ((b >= 32 && b < 64) || (b >= 200 && b < 232)) start_kgen(A, smem, b < 64 ? b - 32 : b - 168);
    else if (b >= 64 && b < 96) tag = start_stats<4>(A, b - 64);
    else if (b >= 240) (void)start_stats<8>(A, b - 224);
    const unsigned dl = (b >= 32 && b < 64) ? DL_C : (b >= 64 && b < 80) ? DL_A : (b >= 80 && b < 96) ? DL_A2 : b >= 240 ? DL_R2 : ((b >= 96 && b < 104) || b >= 232) ? DL_R : (b >= 200 && b < 232) ? DL_Q : 0u;
    while (now10() - t0 < dl) __builtin_amdgcn_s_sleep(16);
    __builtin_amdgcn_s_waitcnt(0x0f70);
  }
  if ((EXP_OFF & 1) && b >= 64 && b < 80) return;
  if ((EXP_OFF & 8) && b >= 200 && b < 232) return;
  if ((EXP_OFF & 16) && b >= 32 && b < 64) return;
  if ((EXP_OFF & 32) && b >= 80 && b < 96) return;
  if (b < 64) role_chain(A, smem, b >> 5, b & 31, 0);
  else if (b < 96) role_aw(A, smem, (b >> 4) & 1, b & 15, tag);
  else if (b >= 104 && b < 168) role_conv(A, smem, b - 104);
  else {
    const bool kv = b >= 168 && b < 232;
    role_proj(A, smem, b >= (kv ? 200 : 240), kv ? (b - 168) & 31 : b & 15, kv);
  }
#undef A
}

extern "C" void kernel_launch(void* const* d_in, const int* in_sizes, int n_in, void* d_out, int out_size, void* d_ws,
                              size_t ws_size, hipStream_t stream) {
  MegaArgs A;
  A.x0 = (const u16*)d_in[0];
  A.logAre = (const u16*)d_in[1];
  A.Aim = (const u16*)d_in[2];
  A.Cre = (const u16*)d_in[3];
  A.Cim = (const u16*)d_in[4];
  A.logdt = (const u16*)d_in[5];
  A.Dskip = (const u16*)d_in[6];
  A.mem0 = (const u16*)d_in[8];
  A.Wq = (const u16*)d_in[9];
  A.Wk = (const u16*)d_in[10];
  A.Wv = (const u16*)d_in[11];
  A.Wo = (const u16*)d_in[12];
  A.ws = (char*)d_ws;
  A.out = (u16*)d_out;

  (void)hipFuncSetAttribute((const void*)mega_kernel, hipFuncAttributeMaxDynamicSharedMemorySize, (int)MEGA_LDS);
  mega_kernel<<<MEGA_WGS, 512, MEGA_LDS, stream>>>(A.mem0, A.Dskip, A.ws, A.out, A.x0, A.logAre, A.Aim, A.Cre, A.Cim, A.logdt,
                                                   A.Wq, A.Wk, A.Wv, A.Wo);
}
```

```cpp
#include <hip/hip_runtime.h>
#include <hip/hip_bf16.h>
#include <stdint.h>

constexpr int L_SEQ = 2048;
constexpr int DM    = 512;
constexpr int NST   = 128;
constexpr int NH    = 32;
constexpr int DH    = 128;
constexpr int HD    = NH * DH;
constexpr int MM    = 8;
constexpr int SEG   = 32;
constexpr int NSEG  = L_SEQ / SEG;
constexpr int LK    = MM + SEG;

typedef unsigned short u16;
using bf16x8 = __attribute__((ext_vector_type(8))) short;
using f32x4  = __attribute__((ext_vector_type(4))) float;
using f32x16 = __attribute__((ext_vector_type(16))) float;

static __device__ __forceinline__ u16 f2bf(float f) {
  unsigned u = __float_as_uint(f);
  u += 0x7fffu + ((u >> 16) & 1u);
  return (u16)(u >> 16);
}
static __device__ __forceinline__ float bf2f(u16 h) { return __uint_as_float(((unsigned)h) << 16); }
static __device__ __forceinline__ float bfr(float f) { return bf2f(f2bf(f)); }

constexpr size_t SZ_X      = (size_t)L_SEQ * DM * 2;
constexpr size_t OFF_KF    = 0;
constexpr size_t OFF_UT    = OFF_KF + (size_t)DM * L_SEQ * 4;
constexpr size_t OFF_YT    = OFF_UT + SZ_X;
constexpr size_t OFF_XA    = OFF_YT + SZ_X;
constexpr size_t OFF_H     = OFF_XA + SZ_X;
constexpr size_t OFF_X2    = OFF_H + SZ_X;
constexpr size_t OFF_U2    = OFF_X2 + SZ_X;
constexpr size_t OFF_X3    = OFF_U2 + SZ_X;
constexpr size_t SZ_QK     = (size_t)L_SEQ * 2 * HD * 2;
constexpr size_t OFF_QK    = OFF_X3 + SZ_X;
constexpr size_t SZ_O      = (size_t)L_SEQ * HD * 2;
constexpr size_t OFF_VF    = OFF_QK + 2 * SZ_QK;
constexpr size_t SZ_PW     = (size_t)2 * NSEG * 16 * SEG * DM * 2;
constexpr size_t OFF_PW    = OFF_VF + 2 * SZ_O;
constexpr size_t SZ_MEM    = (size_t)NSEG * MM * HD * 2;
constexpr size_t OFF_MEMK  = OFF_PW + SZ_PW;
constexpr size_t OFF_MEMV  = OFF_MEMK + 2 * SZ_MEM;
constexpr size_t OFF_GT    = OFF_MEMV + 2 * SZ_MEM;
constexpr size_t OFF_EP    = OFF_GT + 65536;
constexpr size_t OFF_RD    = OFF_EP + 4096;
constexpr size_t OFF_KG    = OFF_RD + 4096;
constexpr size_t OFF_G     = OFF_KG + 8192;
constexpr size_t SZ_ACC    = (size_t)2 * 65536;
constexpr size_t OFF_Z     = OFF_GT + (size_t)128 * DM * 4;
constexpr size_t OFF_ACC   = OFF_Z;
constexpr size_t OFF_FL    = OFF_ACC + 2 * SZ_ACC;
constexpr int FL_FK  = 0;
constexpr int FL_FW  = FL_FK + 64 * 8 * 32;
constexpr int FL_FR  = FL_FW + 2 * 64 * 16 * 32;
constexpr int FL_FC  = FL_FR + 64 * 16 * 32;
constexpr int FL_FQ  = FL_FC + 2 * 64 * 32 * 32;
constexpr int FL_FP  = FL_FQ + 2 * 64 * 32 * 32;
constexpr int FL_FD  = FL_FP + 64 * 32 * 32;
constexpr int FL_FZ  = FL_FD + 64 * 2 * 32;
constexpr int FL_TMO = FL_FZ + 16 * 32;
constexpr int FL_END = FL_TMO + 64;
constexpr size_t Z_BYTES   = OFF_FL + (size_t)FL_END * 4 - OFF_Z;
constexpr size_t WS_TOTAL  = OFF_Z + Z_BYTES;
static_assert(Z_BYTES % 16 == 0 && OFF_Z % 256 == 0 && Z_BYTES <= 4 * 64 * 8192 && OFF_G + 2048 <= OFF_Z, "zero region");
static_assert(WS_TOTAL + (4 << 20) <= (size_t)256 * 1024 * 1024, "workspace too large");

static_assert(WS_TOTAL - OFF_XA <= (size_t)384 * 8 * 65536, "translation warm-up");
constexpr int CKN = 2336;
static_assert((size_t)DM * 2 * CKN * 2 <= (size_t)DM * L_SEQ * 4 + SZ_X, "K buffer");
__device__ __forceinline__ float pos_of_index(int p) {
  if (p < 256) return (float)p;
  if (p < 384) return (float)(256 + 2 * (p - 256));
  if (p < 512) return (float)(512 + 4 * (p - 384));
  if (p < 640) return (float)(1024 + 8 * (p - 512));
  return 2048.0f;
}
__device__ __forceinline__ int index_of_l(int l) {
  float pf = bfr((float)l);
  int p = (int)pf;
  if (p < 256) return p;
  if (p < 512) return 256 + ((p - 256) >> 1);
  if (p < 1024) return 384 + ((p - 512) >> 2);
  if (p < 2048) return 512 + ((p - 1024) >> 3);
  return 640;
}

template <int CTRL> __device__ __forceinline__ float dppr(float x) {
  return __builtin_bit_cast(float, __builtin_amdgcn_update_dpp(0, __builtin_bit_cast(int, x), CTRL, 0xf, 0xf, false));
}
__device__ __forceinline__ float wave_sum(float v) {
  v += dppr<0x121>(v);
  v += dppr<0x122>(v);
  v += dppr<0x124>(v);
  v += dppr<0x128>(v);
  {
    auto r = __builtin_amdgcn_permlane16_swap(__float_as_uint(v), __float_as_uint(v), false, false);
    v = __uint_as_float(r[0]) + __uint_as_float(r[1]);
  }
  {
    auto r = __builtin_amdgcn_permlane32_swap(__float_as_uint(v), __float_as_uint(v), false, false);
    v = __uint_as_float(r[0]) + __uint_as_float(r[1]);
  }
  return v;
}
__device__ __forceinline__ void ln_row8(float (&v)[8]) {
  float s = 0.f;
#pragma unroll
  for (int j = 0; j < 8; ++j) s += v[j];
  s = wave_sum(s);
  const float mean = s * (1.0f / DM);
  float q = 0.f;
#pragma unroll
  for (int j = 0; j < 8; ++j) { float t = v[j] - mean; q += t * t; }
  q = wave_sum(q);
  const float var = q * (1.0f / DM);
  const float mb = bfr(mean);
  const float sd = bfr(sqrtf(bfr(bfr(var) + 1e-5f)));
#pragma unroll
  for (int j = 0; j < 8; ++j) v[j] = bfr(bfr(v[j] - mb) / sd);
}

#define RLX_AGENT __ATOMIC_RELAXED, __HIP_MEMORY_SCOPE_AGENT
typedef __attribute__((address_space(1))) unsigned gu32;
typedef unsigned v4u __attribute__((ext_vector_type(4)));
constexpr int AUX_SC1 = 16;
constexpr unsigned SPIN_CAP = 1u << 18;

__device__ __forceinline__ bool wait_flags(const unsigned* flags, int lane, int nfl, unsigned want, unsigned* tmo) {
  for (unsigned spins = 0;; ++spins) {
    unsigned v = want;
    if (lane < nfl) v = __hip_atomic_load((gu32*)flags + lane * 32, RLX_AGENT);
    if (__all(v >= want)) break;
    if (spins > SPIN_CAP) {
      if (lane == 0) __hip_atomic_store((gu32*)tmo, 1u, RLX_AGENT);
      return false;
    }
  }
  __builtin_amdgcn_fence(__ATOMIC_ACQUIRE, "wavefront");
  return true;
}

#define STAMP(i)
#define STAMP_DUMP()
#define WSTAMP(i)
template <int CTRL> __device__ __forceinline__ float dppf(float old, float x) {
  return __builtin_bit_cast(float, __builtin_amdgcn_update_dpp(__builtin_bit_cast(int, old), __builtin_bit_cast(int, x), CTRL, 0xf, 0xf, false));
}
__device__ __forceinline__ float wave_max_dpp(float x) {
  const float ninf = -3e38f;
  x = fmaxf(x, dppf<0x111>(ninf, x));
  x = fmaxf(x, dppf<0x112>(ninf, x));
  x = fmaxf(x, dppf<0x114>(ninf, x));
  x = fmaxf(x, dppf<0x118>(ninf, x));
  x = fmaxf(x, dppf<0x142>(ninf, x));
  x = fmaxf(x, dppf<0x143>(ninf, x));
  return __builtin_bit_cast(float, __builtin_amdgcn_readlane(__builtin_bit_cast(int, x), 63));
}
__device__ __forceinline__ float wave_sum_dpp(float x) {
  x += dppf<0x111>(0.f, x);
  x += dppf<0x112>(0.f, x);
  x += dppf<0x114>(0.f, x);
  x += dppf<0x118>(0.f, x);
  x += dppf<0x142>(0.f, x);
  x += dppf<0x143>(0.f, x);
  return __builtin_bit_cast(float, __builtin_amdgcn_readlane(__builtin_bit_cast(int, x), 63));
}
constexpr int SMS = 528;
constexpr int QS = 144;
constexpr int VS = 80;
constexpr int SOS = 144;
#define VAR_NREP 4
constexpr int NREP = VAR_NREP;
__device__ __forceinline__ float xsum16(float x) {
  auto r = __builtin_amdgcn_permlane16_swap(__float_as_uint(x), __float_as_uint(x), false, false);
  return __uint_as_float(r[0]) + __uint_as_float(r[1]);
}
__device__ __forceinline__ float xsum32(float x) {
  auto r = __builtin_amdgcn_permlane32_swap(__float_as_uint(x), __float_as_uint(x), false, false);
  return __uint_as_float(r[0]) + __uint_as_float(r[1]);
}
__device__ __forceinline__ float xmax16(float x) {
  auto r = __builtin_amdgcn_permlane16_swap(__float_as_uint(x), __float_as_uint(x), false, false);
  return fmaxf(__uint_as_float(r[0]), __uint_as_float(r[1]));
}
__device__ __forceinline__ float xmax32(float x) {
  auto r = __builtin_amdgcn_permlane32_swap(__float_as_uint(x), __float_as_uint(x), false, false);
  return fmaxf(__uint_as_float(r[0]), __uint_as_float(r[1]));
}
__device__ __forceinline__ float red_mod4(float x) {
  x += dppf<0x124>(0.f, x);
  x += dppf<0x128>(0.f, x);
  return xsum32(xsum16(x));
}
typedef __bf16 bf16x2_t __attribute__((ext_vector_type(2)));
typedef float f32x2_t __attribute__((ext_vector_type(2)));
__device__ __forceinline__ unsigned pk2(float a, float b) {
  f32x2_t v = {a, b};
  bf16x2_t r = __builtin_convertvector(v, bf16x2_t);
  return __builtin_bit_cast(unsigned, r);
}
typedef _Float16 f16x2_t __attribute__((ext_vector_type(2)));
typedef _Float16 f16x8 __attribute__((ext_vector_type(8)));
__device__ __forceinline__ unsigned pkh(float a, float b) {
  f32x2_t v = {a, b};
  return __builtin_bit_cast(unsigned, __builtin_convertvector(v, f16x2_t));
}
__device__ __forceinline__ float h2f(u16 h) { return (float)__builtin_bit_cast(_Float16, h); }
__device__ __forceinline__ float one(float x) {
  asm("" : "+v"(x));
  return x;
}
__device__ __forceinline__ unsigned pk2s(float a, float b) { return pk2(one(a), one(b)); }
__device__ __forceinline__ float dot2sq(const unsigned x, const float acc) {
  return __builtin_amdgcn_fdot2_f32_bf16(__builtin_bit_cast(bf16x2_t, x), __builtin_bit_cast(bf16x2_t, x), acc, false);
}
__device__ __forceinline__ float lo16(unsigned u) { return __uint_as_float(u << 16); }
__device__ __forceinline__ float hi16(unsigned u) { return __uint_as_float(u & 0xffff0000u); }
__device__ __forceinline__ float bfr1(float x) { return lo16(pk2(x, x)); }
__device__ __forceinline__ float half_sum(float v) {
  v += dppr<0x121>(v);
  v += dppr<0x122>(v);
  v += dppr<0x124>(v);
  v += dppr<0x128>(v);
  return xsum16(v);
}
__device__ __forceinline__ void scale_pair(float a, float b, float scale, float& oa, float& ob) {
  unsigned u = pk2(a, b);
  unsigned v = pk2s(lo16(u) * scale, hi16(u) * scale);
  oa = lo16(v);
  ob = hi16(v);
}

#define XMAP 2
__device__ __forceinline__ int vbid() {
  const int b = blockIdx.x, x = b & 7, t = b >> 3;
  if (XMAP == 2) {
    const int q = (t >> 2) + 2 * (x & 4);
    const unsigned long long m = (q & 8) ? 0xf0d85040cdc9ada9ull : 0x76726e6a25210501ull;
    const int e = (int)(m >> (8 * (q & 7))) & 255;
    return (e & 252) + ((x & 3) << (2 + (e & 3))) + (t & 3) + (q == 14 ? ((x & 2) ? 8 : -120) : 0);
  }
  if (XMAP == 0) {
    if (b < 64) return (b & 32) + 4 * x + (t & 3);
    if (b < 96) return (b & ~15) + 2 * x + (t & 1);
    if (b >= 104 && b < 168) return 104 + 8 * x + t - 13;
    if (b >= 168 && b < 232) return 168 + ((b - 168) & 32) + 4 * x + ((t - 21) & 3);
    return b;
  }
  if (XMAP == 3) {
    const int y = x & 3;
    if (t >= 16 && t < 24) return 104 + 16 * y + 2 * (x & 4) + t - 16;
    if (x < 4) return t < 16 ? 4 * (t & 8) + 8 * x + (t & 7) : 64 + 4 * (t & 4) + 4 * y + (t & 3);
    if (t >= 24) return (t >= 28 ? 240 : y < 2 ? 96 : 224) + 4 * y + (t & 3);
    return 168 + 4 * (t & 8) + 8 * y + (t & 7);
  }
  if (XMAP == 4) {
    const int y = x & 3;
    if (x < 4) return t < 16 ? 4 * (t & 8) + 8 * x + (t & 7) : t < 28 ? 104 + 16 * y + t - 16 : 64 + 4 * y + (t & 3);
    if (t >= 24) return (t >= 28 ? 240 : y < 2 ? 96 : 224) + 4 * y + (t & 3);
    if (t < 16) return 168 + 4 * (t & 8) + 8 * y + (t & 7);
    return t < 20 ? 104 + 16 * y + t - 4 : 80 + 4 * y + (t & 3);
  }
  if (XMAP == 2 || XMAP >= 5) {
    if (XMAP == 5 && x < 4 && t < 16) return 16 * x + t;
    if (XMAP == 6 && x < 4 && t < 16) return 32 * (x & 1) + 8 * (x & 2) + t;
    if (XMAP == 7 && x < 4) return (x & 1) ? 104 + 16 * (x & 2) + t : 16 * x + t;
    if (x < 4) return t < 16 ? 4 * (t & 8) + 8 * x + (t & 7) : 104 + 16 * x + t - 16;
    const int y = x - 4;
    if (t >= 24) return (t >= 28 ? 240 : y < 2 ? 96 : 224) + 4 * y + (t & 3);
    if (t < 16) return 168 + 4 * (t & 8) + 8 * y + (t & 7);
    return 64 + 4 * (t & 4) + 4 * y + (t & 3);
  }
  if (x < 2) return 2 * (t & 16) + 16 * x + (t & 15);
  const int i = 16 * (x - 2) + (t & 15);
  if (t < 16) {
    const int k = (i * 171) >> 9, r = i - 3 * k;
    return r == 0 ? 168 + k : r == 1 ? 200 + k : 64 + 16 * (k & 1) + (k >> 1);
  }
  const int g = i >> 3, e = i & 7;
  if (g == 3) return 96 + e;
  if (g == 7) return 232 + e;
  if (g == 9) return 240 + e;
  if (g == 11) return 248 + e;
  return 104 + 8 * (g - (g > 3) - (g > 7) - (g > 9)) + e;
}
#define MSTAMP()
#define WSTAMP2()
#define SST(k)
#define SSX(k, c)
#define SSW(k, v)
#define TS(k, ss, t)
#define LS(k, ss)
#define LS_DUMP()
struct MegaArgs {
  const u16* mem0;
  const u16* Dskip;
  char* ws;
  u16* out;
  const u16* x0;
  const u16 *logAre, *Aim, *Cre, *Cim, *logdt;
  const u16 *Wq, *Wk, *Wv, *Wo;
  int vb, wv;
};

__device__ __forceinline__ int lane_id() {
  int l;
  asm volatile("v_mbcnt_lo_u32_b32 %0, -1, 0\n\tv_mbcnt_hi_u32_b32 %0, -1, %0" : "=v"(l));
  __builtin_assume(l >= 0 && l < 64);
  return l;
}
__device__ __forceinline__ bool spin_timeout(unsigned spins, unsigned* tmo) {
  if (spins > SPIN_CAP) {
    if (lane_id() == 0) __hip_atomic_store((gu32*)tmo, 1u, RLX_AGENT);
    return true;
  }
  return false;
}
#define ROT_H (4 * (h & 1))
#define ROT_T (h & 2)
#define C2G 420
#define CVSP 40
#define PFA 0
#define PFB 0
#define WO1 1
#define AWH 0
#define PEEK0 1
#define SLP0 2
#define GF_R 400
#define GF_V 0
#define PP (9 - R0)
#define EL 1
#define IPF 3
#define XL 0
#define MC3 2
#define XZD 300
#define XZP 4
#define NFW 1
#define IPS 128u
#define WNT 17
#define OAUX 16
#define XAUX 16
#define GF_RT 0
#define GF_RU 0
#define EF 1
#define RGB 3
#define GR2 345
#define GR3 280
#define RLS 1
#define GT1 120
#define GT2 32
#define GT3 1
#define CE 190
#define KE 0
#define PE KE
#define KD 360
#define PD (KD + 20)
#define PC0 2
#define GR0 6
#define DL_A2 DL_A
#define PCT 0
#define EP 1
#define CQ 1
#define K0 40
#define PW 1
#define R0 3
#define PSW 1
#define RSK 0
#define KC 1
#define AWD 2200
#define P2D 0
#define TS1 3
#define TL1 20
#define TL2 30
#define TL3 50
#define KF 240
#define R0D 0
#define C0D 0
#define YQ 1
#define KCT 20
#define KR 1
#define K1 50
#define CVP 2
#define PCD 100
#define RAUX AUX_SC1
#define RVAL 1
#define RLEAD 60
#define SLP_D 0
#define RFA 1
#define TP 1
#define TPS (4 * TP + 4)
#define MCE 1
#define H4S 0
#define GF_H 150
#define CVT2 48
#define GF_P 0
#define GF0 8
#define SLP_H 12
#define HX0 6
#define CVSTEP 2
#define CVLATE 64
#define PACE 204
#define PACE0 12
#define RPRIO 3
#define DL_R2 1400
#define PNL 8
#define GSL 12
#define AWDIR 1
#define AWAT 0
#define AWRP 0
#define PEPS 4
#define FGATE 0
#define DL_C 0
#define DL_A 300
#define DL_R 0
#define DL_Q 0
#define KC0 0
#define CPRIO 1
#define P1PRIO 3
#define QLEAD 2
#define POLL_SLEEP 12
#define SLP_C POLL_SLEEP
#define SLP_A POLL_SLEEP
#define SLP_W POLL_SLEEP
#define SLP_R POLL_SLEEP
#define SLP_V POLL_SLEEP
#define SLP_Q POLL_SLEEP
#define SLP_M 4
#define AWPRIO 2
#define SLP_S 8
#define SPOS 4
#define CVPRE 12
#define QPD 3
#define QPRI 2
#define TLBW 3
#define QHV 0
#define QCHK 0
#define LEADN 99
#define GATE_V 170
#define GATE_R 380
#define GATE_N 380
#define AWAHEAD 1
#define GATE_M 150
#define PRI_A 3
#define PRI_WA 2
#define PRI_WB 1
#define AWSKIP 3
#define PRI_0 3
#define AWDEF 0
#define SLP_F 2
#define EXP_OFF 0
#define AWSPEC 1
#define CVTHR 48
#define NR_C 8
#define NR_R 8
#define NR_W 8
#define NR_N 2
#define QAHEAD 0
#define NS0 4
#define SLP_N POLL_SLEEP
template <int SLP, int WPL>
__device__ __forceinline__ bool wait_n(const unsigned* f, int n, unsigned* tmo) {
  const int l = lane_id();
  for (unsigned spins = 0;; ++spins) {
    unsigned v = 1u;
    if (l < n) v = __hip_atomic_load((gu32*)f + (l / WPL) * 32 + l % WPL, RLX_AGENT);
    if (__all(v != 0u)) break;
    if (spin_timeout(spins, tmo)) return false;
    __builtin_amdgcn_s_sleep(SLP);
  }
  asm volatile("" ::: "memory");
  return true;
}
__device__ __forceinline__ unsigned peek(const unsigned* base, const unsigned off) {
  unsigned r;
  asm volatile("global_atomic_or %0, %1, %2, %3 sc0\n\ts_waitcnt vmcnt(0)" : "=&v"(r) : "v"(off), "v"(0u), "s"(base) : "memory");
  return r;
}
__device__ __forceinline__ unsigned now10() { return (unsigned)__builtin_amdgcn_s_memrealtime(); }
__device__ __forceinline__ void gate(const unsigned t) {
#pragma unroll 1
  for (int i = 0; i < 64; ++i) {
    const int d = (int)(t - now10());
    if (d <= 0) break;
    if (d > GT1) __builtin_amdgcn_s_sleep(32);
    else if (d > GT2) __builtin_amdgcn_s_sleep(8);
    else __builtin_amdgcn_s_sleep(GT3);
  }
}
__device__ __forceinline__ unsigned latest(const unsigned mk, const unsigned t) {
  int d = (int)(mk - t);
  d = max(d, __builtin_amdgcn_update_dpp(0, d, 0x121, 0xf, 0xf, false));
  d = max(d, __builtin_amdgcn_update_dpp(0, d, 0x122, 0xf, 0xf, false));
  d = max(d, __builtin_amdgcn_update_dpp(0, d, 0x124, 0xf, 0xf, false));
  d = max(d, __builtin_amdgcn_update_dpp(0, d, 0x128, 0xf, 0xf, false));
  return t + __builtin_amdgcn_readfirstlane(d);
}
template <int SLP, int WPL, int GT>
__device__ __forceinline__ void grp_wait(const bool leader, volatile unsigned* sw, const unsigned val, const unsigned* f, int n,
                                         unsigned* tmo, unsigned& tl) {
  if (leader) {
    const int l = lane_id();
    for (unsigned spins = 0;; ++spins) {
      unsigned v = 1u;
      if (l < n) v = __hip_atomic_load((gu32*)f + (l / WPL) * 32 + l % WPL, RLX_AGENT);
      if (__all(v != 0u) || spin_timeout(spins, tmo)) break;
      if (GT && spins == 0) gate(tl);
      else __builtin_amdgcn_s_sleep(SLP);
    }
    if (GT) tl = now10() + GT;
    if (sw && l == 0) *sw = val;
  } else {
    for (unsigned spins = 0; *sw < val; ++spins) {
      if (spins > 64u * SPIN_CAP) break;
      if (FGATE && GT && spins == 0) gate(tl);
      __builtin_amdgcn_s_sleep(SLP_F);
    }
    if (FGATE && GT) tl = now10() + GT;
  }
  asm volatile("" ::: "memory");
}
template <int SLP, int WPL>
__device__ __forceinline__ bool wg_wait(const unsigned* f, int n, unsigned* tmo) {
  if (threadIdx.x < 64) (void)wait_n<SLP, WPL>(f, n, tmo);
  __syncthreads();
  asm volatile("" ::: "memory");
  return true;
}
__device__ __forceinline__ void set_flag(unsigned* f) { __hip_atomic_store((gu32*)f, 1u, RLX_AGENT); }
#define DRAIN() asm volatile("s_waitcnt vmcnt(0)" ::: "memory")
#define MKRS(ptr, bytes) __builtin_amdgcn_make_buffer_rsrc((void*)(ptr), 0, (int)(bytes), 0x00020000)
typedef unsigned v2u __attribute__((ext_vector_type(2)));
#define LD128(rs, voff, soff) __builtin_amdgcn_raw_buffer_load_b128(rs, voff, soff, AUX_SC1)
#define LD64(rs, voff, soff) __builtin_amdgcn_raw_buffer_load_b64(rs, voff, soff, AUX_SC1)
#define ST128(v, rs, voff, soff) ST128A(v, rs, voff, soff, AUX_SC1)
#define ST128A(v, rs, voff, soff, aux)                                        \
  do {                                                                        \
    const v4u st_d_ = (v);                                                    \
    __builtin_amdgcn_raw_buffer_store_b128(st_d_, rs, voff, soff, aux);       \
    asm volatile("s_nop 1" ::"v"(st_d_) : "memory");                          \
  } while (0)
#define ST64(v, rs, voff, soff) __builtin_amdgcn_raw_buffer_store_b64(v, rs, voff, soff, AUX_SC1)
#define BF8(x) __builtin_bit_cast(bf16x8, x)
#define MFMA16(a, b, c) __builtin_amdgcn_mfma_f32_16x16x32_bf16(a, b, c, 0, 0, 0)
typedef short bf16x4 __attribute__((ext_vector_type(4)));
#define MFMA16K(a, b, c) \
  __builtin_amdgcn_mfma_f32_16x16x16bf16_1k(__builtin_bit_cast(bf16x4, a), __builtin_bit_cast(bf16x4, b), c, 0, 0, 0)
__device__ __forceinline__ float fmax_nn(float a, float b) { return __builtin_elementwise_maximum(a, b); }
typedef __bf16 bfv8 __attribute__((ext_vector_type(8)));
typedef __bf16 bfv16 __attribute__((ext_vector_type(16)));
typedef unsigned v8u __attribute__((ext_vector_type(8)));
#define SMF(a, b, c, idx) \
  __builtin_amdgcn_smfmac_f32_16x16x64_bf16(__builtin_bit_cast(bfv8, a), __builtin_bit_cast(bfv16, b), c, idx, 0, 0)
__device__ __forceinline__ float swap_add32(float a, float b) {
  auto r = __builtin_amdgcn_permlane32_swap(__float_as_uint(a), __float_as_uint(b), false, false);
  return __uint_as_float(r[0]) + __uint_as_float(r[1]);
}
__device__ __forceinline__ float swap_add16(float a, float b) {
  auto r = __builtin_amdgcn_permlane16_swap(__float_as_uint(a), __float_as_uint(b), false, false);
  return __uint_as_float(r[0]) + __uint_as_float(r[1]);
}
__device__ __forceinline__ v4u smf_fix(const v4u d) {
  auto x = __builtin_amdgcn_permlane16_swap(d.x, d.z, false, false);
  auto y = __builtin_amdgcn_permlane16_swap(d.y, d.w, false, false);
  return v4u{x[0], x[1], y[0], y[1]};
}
__device__ __forceinline__ v8u smf_bfrag(const u16* p) {
  const v2u a = *reinterpret_cast<const v2u*>(p), b = *reinterpret_cast<const v2u*>(p + 8);
  const v2u c = *reinterpret_cast<const v2u*>(p + 32), d = *reinterpret_cast<const v2u*>(p + 40);
  return v8u{a.x, b.x, a.y, b.y, c.x, d.x, c.y, d.y};
}

#define VAR_PPA 0
#define VAR_PPB 0
#define VAR_THR 0
#define VAR_THR2 0
#define CV_LEAD 100
#define VAR_EARLY 1
#define VAR_DRAIN 1
#define VAR_D1 0
#define VAR_D2 0
#define VAR_DR2 1
#define VAR_S1 0
#define VAR_S2 0
typedef __attribute__((address_space(1))) unsigned long long gu64;
typedef __attribute__((address_space(1))) char gch;
__device__ __forceinline__ int cvt_rpi(float x) {
  int r;
  asm("v_cvt_rpi_i32_f32 %0, %1" : "=v"(r) : "v"(x));
  return r;
}
typedef unsigned long long u64;
__device__ __forceinline__ u64 call_tag(const MegaArgs& A) {
  u64 t = *(const __attribute__((address_space(4))) u64*)(A.ws + OFF_EP) * 6364136223846793005ull + 1442695040888963407ull;
  asm volatile("" : "+s"(t)::"memory");
  return t;
}
__device__ __forceinline__ u64 tagged(const u64 tag, const unsigned v) { return (tag & 0xffffffffffffull) | (u64)(v >> 16) << 48; }
__device__ __forceinline__ bool has_tag(const u64 tag, const unsigned lo, const unsigned hi) {
  return lo == (unsigned)tag && (hi & 0xffffu) == ((unsigned)(tag >> 32) & 0xffffu);
}
__device__ __forceinline__ u64 ready_look(const MegaArgs& A) {
  return __hip_atomic_load((gu64*)(A.ws + OFF_RD) + (blockIdx.x & 7) * 64 + lane_id(), RLX_AGENT);
}
#define RD_ASK(v) \
  asm volatile("global_load_dwordx2 %0, %1, %2 sc1" : "=v"(v) : "v"(lane_id() * 8), "s"(A.ws + OFF_RD + (blockIdx.x & 7) * 512) : "memory")
__device__ __forceinline__ void ready_wait(const MegaArgs& A, const u64 tag, u64 v) {
  for (unsigned spins = 0; !__all(v == tag) && spins < SPIN_CAP; ++spins) {
    __builtin_amdgcn_s_sleep(8);
    v = ready_look(A);
  }
  asm volatile("" ::: "memory");
}
typedef short v4s __attribute__((ext_vector_type(4)));
typedef __attribute__((address_space(3))) v4s* lp4s;
__device__ __forceinline__ void glds16(const void* sbase, const unsigned voff, const unsigned lds, const bool nt = false) {
  unsigned keep;
#define GLDS16(M)                                                                                                                     \
  asm volatile("s_nop 4\n\ts_mov_b32 %0, m0\n\ts_mov_b32 m0, %3\n\ts_nop 0\n\tglobal_load_lds_dwordx4 %1, %2" M "\n\ts_mov_b32 m0, %0" \
               : "=&s"(keep)                                                                                                          \
               : "v"(voff), "s"(sbase), "s"(lds)                                                                                      \
               : "memory")
  if (nt) GLDS16(" nt");
  else GLDS16("");
#undef GLDS16
}
__device__ __forceinline__ void glds16c(const void* sbase, const unsigned voff, const unsigned lds) {
  unsigned keep;
  asm volatile("s_nop 4\n\ts_mov_b32 %0, m0\n\ts_mov_b32 m0, %3\n\ts_nop 0\n\tglobal_load_lds_dwordx4 %1, %2 sc1\n\ts_mov_b32 m0, %0"
               : "=&s"(keep)
               : "v"(voff), "s"(sbase), "s"(lds)
               : "memory");
}
__device__ __forceinline__ void glds64c(const void* sbase, const unsigned voff, const unsigned lds) {
  unsigned keep;
  asm volatile("s_nop 4\n\ts_mov_b32 %0, m0\n\ts_mov_b32 m0, %3\n\ts_nop 0\n\tglobal_load_lds_dwordx4 %1, %2 sc1\n\t"
               "global_load_lds_dwordx4 %1, %2 offset:1024 sc1\n\tglobal_load_lds_dwordx4 %1, %2 offset:2048 sc1\n\t"
               "global_load_lds_dwordx4 %1, %2 offset:3072 sc1\n\ts_mov_b32 m0, %0"
               : "=&s"(keep)
               : "v"(voff), "s"(sbase), "s"(lds)
               : "memory");
}
__device__ __forceinline__ v2u trd(const unsigned a) {
  return __builtin_bit_cast(v2u, __builtin_amdgcn_ds_read_tr16_b64_v4i16((lp4s)(unsigned long)a));
}
__device__ __forceinline__ unsigned lds_base(char* smem) { return (unsigned)(unsigned long)(__attribute__((address_space(3))) char*)smem; }
#define RAW_BAR()                                           \
  do {                                                      \
    asm volatile("s_waitcnt lgkmcnt(0)" ::: "memory");      \
    __builtin_amdgcn_s_barrier();                           \
    asm volatile("" ::: "memory");                          \
  } while (0)
#define WAIT_VM(n) asm volatile("s_waitcnt vmcnt(" #n ")" ::: "memory")
template <bool SP>
__device__ __forceinline__ void wqkv_issue(const u16* Wm, const int hf, const int h, const int w, const int l, const unsigned sb, const bool nt) {
  const char* base = (const char*)Wm + (size_t)(256 * hf + 32 * w) * (HD * 2) + h * (DH * 2);
#define WQKV(NT_)                                                                                                       \
  _Pragma("unroll") for (int i = 0; i < 8; ++i) {                                                                      \
    const int r = l >> 4;                                                                                              \
    const int f = SP ? ((r & 1) | ((i & 1) << 1) | (((i >> 1) & 1) << 2)) : (r | (((i >> 1) & 1) << 2));               \
    glds16(base, (unsigned)((4 * i + r) * (HD * 2) + (((l & 15) ^ (f << 1)) << 4)),                                    \
           sb + (w >> 2) * 65536 + hf * 32768 + (w & 3) * 8192 + i * 1024, NT_);                                       \
  }
  if (nt) { WQKV(true) } else { WQKV(false) }
#undef WQKV
}

__device__ __forceinline__ unsigned row_stats(const v4u raw) {
  float v[8];
#pragma unroll
  for (int e = 0; e < 4; ++e) { v[2 * e] = lo16(raw[e]); v[2 * e + 1] = hi16(raw[e]); }
  float s = 0.f;
#pragma unroll
  for (int e = 0; e < 8; ++e) s += v[e];
  const float mean = wave_sum(s) * (1.0f / DM);
  float q = 0.f;
#pragma unroll
  for (int e = 0; e < 8; ++e) { const float t = v[e] - mean; q += t * t; }
  const float var = wave_sum(q) * (1.0f / DM);
  const float sd = bfr(sqrtf(bfr(bfr(var) + 1e-5f)));
  return (unsigned)f2bf(mean) | ((unsigned)f2bf(sd) << 16);
}
__device__ __forceinline__ void ask_slow(const MegaArgs& A, const int k, unsigned (&t)[3]) {
  const int l = lane_id(), x = blockIdx.x & 7;
  const char *wq = (const char*)A.Wq, *wv = (const char*)A.Wv;
  asm("" : "+s"(wq), "+s"(wv));
#pragma unroll
  for (int m = 0; m < 3; ++m) {
    const char* W = m == 1 ? (const char*)A.Wk : ((m == 0) == (x < 4)) ? wv : wq;
    const int a = (int)((unsigned long)W >> 8) + 8 * (x & 3), h = k < 32 ? ((1 - a) & 3) + 4 * (k & 1) : (31 - a) & 31;
    const int r = k < 32 ? k >> 1 : k - 32;
    t[m] = __builtin_amdgcn_raw_buffer_load_b32(MKRS(W, (unsigned)k < 48u && h < 8 ? DM * HD * 2 : 0), (l >> 1) * (HD * 2) + (l & 1) * 128,
                                                (32 * (r & 15)) * (HD * 2) + (8 * (x & 3) + (h & 7)) * 256, 0);
  }
}
#define ASKED(t) asm volatile("" ::"v"(t[0]), "v"(t[1]), "v"(t[2]))
template <int N>
__device__ __forceinline__ u64 start_stats(const MegaArgs& A, const int idx) {
  const int w = A.wv, l = lane_id();
  const int r0 = N * (8 * idx + w);
  unsigned ta[3], tb[3];
  v4u raw[N];
#pragma unroll
  for (int i = 0; i < N; ++i) raw[i] = *reinterpret_cast<const v4u*>(A.x0 + (size_t)(r0 + i) * DM + l * 8);
  asm volatile("" ::: "memory");
  ask_slow(A, 16 * w + (idx & 3) + (idx >> 4) * N, ta);
  if (N > 4) ask_slow(A, 16 * w + (idx & 3) + 12, tb);
  unsigned tc[2] = {};
  if (N == 4 && AWH > 0) tc[0] = *reinterpret_cast<const unsigned*>(A.Wo + (size_t)idx * 65536 + 32768 + (64 * w + l) * 64);
  if (N == 4 && AWH > 1 && w >= 4) tc[1] = *reinterpret_cast<const unsigned*>(A.Wo + (size_t)idx * 65536 + (64 * w + l) * 64);
  const u64 tag = call_tag(A);
  unsigned stw = 0u;
#pragma unroll
  for (int i = 0; i < N; ++i) {
    const unsigned t = row_stats(raw[i]);
    if ((l >> 1) == i) stw = t;
  }
  if (l < 2 * N) __hip_atomic_store((gu64*)(A.ws + OFF_GT) + 2 * r0 + l, tagged(tag, (l & 1) ? stw : stw << 16), RLX_AGENT);
  SST(0);
  ASKED(ta);
  if (N > 4) ASKED(tb);
  asm volatile("" ::"v"(tc[0]), "v"(tc[1]));
  return tag;
}
#define MFMAH(a, b, c) __builtin_amdgcn_mfma_f32_16x16x32_f16(a, b, c, 0, 0, 0)
struct Cx {
  f32x2_t r, i;
};
__device__ __forceinline__ Cx cmul(const Cx a, const Cx b) { return Cx{a.r * b.r - a.i * b.i, a.r * b.i + a.i * b.r}; }
struct KGen {
  unsigned pa, pb, pc, pd;
  u16 pe;
  f32x2_t ar, ai;
  Cx C;
  char* st;
  v4u bh[8], bl[8];
  __device__ __forceinline__ void load(const MegaArgs& A, const int d, const int l) {
    pa = *reinterpret_cast<const unsigned*>(A.logAre + d * NST + 2 * l);
    pb = *reinterpret_cast<const unsigned*>(A.Aim + d * NST + 2 * l);
    pc = *reinterpret_cast<const unsigned*>(A.Cre + d * NST + 2 * l);
    pd = *reinterpret_cast<const unsigned*>(A.Cim + d * NST + 2 * l);
    pe = A.logdt[d];
  }
  __device__ __forceinline__ void setup() {
    const float dt = bfr(expf(bf2f(pe)));
    ar = f32x2_t{-bfr(expf(lo16(pa))) * dt, -bfr(expf(hi16(pa))) * dt};
    ai = f32x2_t{lo16(pb) * dt, hi16(pb) * dt};
    C = Cx{f32x2_t{lo16(pc) * dt, hi16(pc) * dt}, f32x2_t{lo16(pd) * dt, hi16(pd) * dt}};
  }
  __device__ __forceinline__ float scale() {
    const float b = 2.0f * wave_sum((fabsf(C.r.x) + fabsf(C.r.y)) + (fabsf(C.i.x) + fabsf(C.i.y)));
    const unsigned eb = max(__float_as_uint(b) >> 23, 25u);
    const float ks = __uint_as_float((268u - eb) << 23);
    C.r *= ks;
    C.i *= ks;
    return __uint_as_float((eb - 24u) << 23);
  }
  __device__ __forceinline__ Cx power(const float p) const {
    Cx z;
#pragma unroll
    for (int e = 0; e < 2; ++e) {
      const float x = ai[e] * p;
      const float f = __builtin_fmaf(x, 6.4206382e-9f, __builtin_fmaf(x, 0.15915494f, -__builtin_rintf(x * 0.15915494f)));
      const float m = __expf(ar[e] * p);
      z.r[e] = m * __builtin_amdgcn_cosf(f);
      z.i[e] = m * __builtin_amdgcn_sinf(f);
    }
    return z;
  }
  static __device__ __forceinline__ unsigned rest(const f32x2_t y, const unsigned h) {
    unsigned r;
    asm("v_fma_mixlo_f16 %0, %1, -1.0, %2 op_sel_hi:[1,0,0]" : "=v"(r) : "v"(h), "v"(y.x));
    asm("v_fma_mixhi_f16 %0, %1, -1.0, %2 op_sel:[1,0,0] op_sel_hi:[1,0,0]" : "+v"(r) : "v"(h), "v"(y.y));
    return r;
  }
  __device__ __forceinline__ char* rows(char* p, Cx& y, const Cx s, const int n) const {
#pragma unroll 1
    for (int i = 0; i < n; i += 2) {
#pragma unroll
      for (int k = 0; k < 2; ++k) {
        const unsigned h0 = pkh(y.r.x, y.r.y), h1 = pkh(y.i.x, y.i.y);
        *reinterpret_cast<v2u*>(p + 1040 * k) = v2u{h0, h1};
        *reinterpret_cast<v2u*>(p + 1040 * k + 512) = v2u{rest(y.r, h0), rest(y.i, h1)};
        y = cmul(y, s);
      }
      p += 2080;
    }
    return p;
  }
  __device__ __forceinline__ void get(v4u (&h)[8], v4u (&o)[8], const int l) const {
    const char* p = st + (l & 15) * 1040 + (l >> 4) * 16;
    asm volatile("" ::: "memory");
#pragma unroll
    for (int k = 0; k < 8; ++k) {
      h[k] = *reinterpret_cast<const v4u*>(p + 64 * k);
      o[k] = *reinterpret_cast<const v4u*>(p + 512 + 64 * k);
    }
    asm volatile("" ::: "memory");
  }
  Cx cy, cs;
  __device__ __forceinline__ void columns(const int l, const int j0, const int j1) {
    if (j0 == 0) {
      cs = power(16.f);
      cs.i = -cs.i;
      cy = Cx{f32x2_t{16384.f, 16384.f}, f32x2_t{0.f, 0.f}};
    }
    rows(st + l * 8 + 1040 * j0, cy, cs, j1 - j0);
  }
  template <bool COL>
  __device__ __forceinline__ f32x4 tile(const int l) const {
    v4u h[8], o[8];
    get(h, o, l);
    f32x4 d = {}, e = {}, f = {};
#define H8(x) __builtin_bit_cast(f16x8, x)
#define KMM(a, b, d) (COL ? MFMAH(H8(b), H8(a), d) : MFMAH(H8(a), H8(b), d))
#pragma unroll
    for (int k = 0; k < 8; ++k) {
      d = KMM(h[k], bh[k], d);
      e = KMM(o[k], bh[k], e);
      f = KMM(h[k], bl[k], f);
    }
#undef KMM
#undef H8
    return (d + (e + f)) * (1.0f / 8192);
  }
};
#define ACC_INIT 0xc9b26c86c9b26ca0ull
#define GEN_TL 2
#define GEN_TR 4
#define GEN_F0 2
#define GEN_T1 3
#define GEN_TB 0
#define CHAIN_ASM_ATT \
  "s_mov_b32 s95, 272\n" \
  "s_mov_b32 s91, 0x4c7c2000\n" \
  "s_mov_b32 s85, 0xc9b26c9b\n" \
  "s_mov_b32 s92, 0xc9326c9b\n" \
  "s_mov_b32 s93, 0xc9326c9b\n" \
  "s_mov_b32 s78, 0x3e0293ee\n" \
  "s_mov_b32 s76, 0x33333333\n" \
  "s_mov_b32 s77, 0x33333333\n" \
  "s_lshl_b32 s98, s73, 10\n" \
  "s_add_u32 s98, s98, s74\n" \
  "s_add_u32 s98, s98, 131072\n" \
  "s_lshl_b32 s97, s73, 9\n" \
  "s_add_u32 s97, s97, s74\n" \
  "s_add_u32 s97, s97, 158720\n" \
  "s_lshl_b32 s99, s73, 10\n" \
  "v_mbcnt_lo_u32_b32 v48, -1, 0\n" \
  "v_mbcnt_hi_u32_b32 v48, -1, v48\n" \
  "s_add_u32 s88, s74, 157760\n" \
  "v_mov_b32 v59, s88\n" \
  "v_lshrrev_b32 v49, 2, v48\n" \
  "v_lshrrev_b32 v50, 4, v48\n" \
  "v_bitop3_b32 v50, v49, v50, 3 bitop3:0x28\n" \
  "v_lshl_add_u32 v8, v50, 3, s97\n" \
  "v_and_b32 v50, 3, v48\n" \
  "v_lshl_add_u32 v8, v50, 5, v8\n" \
  "v_and_b32 v49, 1, v49\n" \
  "v_lshl_add_u32 v8, v49, 8, v8\n" \
  "v_and_b32 v49, 8, v48\n" \
  "v_lshl_add_u32 v8, v49, 4, v8\n" \
  "v_lshrrev_b32 v49, 5, v48\n" \
  "v_add_u32 v10, s99, v1\n" \
  "s_lshr_b32 s88, s99, 2\n" \
  "s_add_u32 s88, s88, s74\n" \
  "s_add_u32 s88, s88, 139264\n" \
  "v_lshl_add_u32 v7, v49, 10, s88\n" \
  "v_and_b32 v49, 31, v48\n" \
  "v_lshl_add_u32 v7, v49, 3, v7\n" \
  "v_mov_b64 v[12:13], 0\n" \
  "v_mov_b64 v[14:15], 0\n" \
  "v_mov_b64 v[16:17], 0\n" \
  "v_mov_b64 v[18:19], 0\n" \
  "v_mov_b64 v[20:21], 0\n" \
  "v_mov_b64 v[22:23], 0\n" \
  "v_mov_b32 v34, 0xc9b26ca0\n" \
  "v_mov_b32 v35, 0xc9b26c86\n" \
  "s_mov_b32 s87, 0x40000\n" \
  "v_subrev_u32 v30, 64, v59\n" \
  "v_add_u32 v17, 0x10000, v28\n" \
  "v_xor_b32 v18, 128, v28\n" \
  "v_xor_b32 v19, 128, v17\n" \
  "ds_write_b32 v30, v12 offset:0\n" \
  "ds_write_b32 v30, v12 offset:32\n" \
  "ds_write_b32 v30, v12 offset:64\n" \
  "v_xor_b32 v20, 0, v29\n" \
  "v_xor_b32 v21, 32, v29\n" \
  "v_xor_b32 v22, 64, v29\n" \
  "v_xor_b32 v23, 96, v29\n" \
  "s_waitcnt vmcnt(12) lgkmcnt(0)\n" \
  "s_barrier\n" \
  "ds_read_b64_tr_b16 v[160:161], v20 offset:0\n" \
  "ds_read_b64_tr_b16 v[162:163], v20 offset:2048\n" \
  "ds_read_b64_tr_b16 v[168:169], v21 offset:0\n" \
  "ds_read_b64_tr_b16 v[170:171], v21 offset:2048\n" \
  "ds_read_b64_tr_b16 v[176:177], v22 offset:0\n" \
  "ds_read_b64_tr_b16 v[178:179], v22 offset:2048\n" \
  "ds_read_b64_tr_b16 v[184:185], v23 offset:0\n" \
  "ds_read_b64_tr_b16 v[186:187], v23 offset:2048\n" \
  "s_lshl_b32 s88, s73, 10\n" \
  "v_add_u32 v48, s88, v1\n" \
  "ds_read_b128 v[52:55], v48 offset:0\n" \
  "s_waitcnt lgkmcnt(0)\n" \
  "v_mov_b32 v11, 0\n" \
  "v_dot2c_f32_bf16 v11, v52, v52\n" \
  "v_dot2c_f32_bf16 v11, v53, v53\n" \
  "v_dot2c_f32_bf16 v11, v54, v54\n" \
  "v_dot2c_f32_bf16 v11, v55, v55\n" \
  "s_nop 0\n" \
  "s_nop 1\n" \
  "v_max_u32_dpp v11, v11, v11 row_ror:1 row_mask:0xf bank_mask:0xf\n" \
  "s_nop 1\n" \
  "v_max_u32_dpp v11, v11, v11 row_ror:2 row_mask:0xf bank_mask:0xf\n" \
  "s_nop 1\n" \
  "v_max_u32_dpp v11, v11, v11 row_ror:4 row_mask:0xf bank_mask:0xf\n" \
  "s_nop 1\n" \
  "v_max_u32_dpp v11, v11, v11 row_ror:8 row_mask:0xf bank_mask:0xf\n" \
  "v_mov_b32 v31, v11\n" \
  "s_nop 1\n" \
  "v_permlane16_swap_b32 v11, v31\n" \
  "v_max_u32 v11, v11, v31\n" \
  "v_mov_b32 v31, v11\n" \
  "s_nop 1\n" \
  "v_permlane32_swap_b32 v11, v31\n" \
  "v_max_u32 v11, v11, v31\n" \
  "s_mov_b64 exec, 1\n" \
  "ds_max_u32 v59, v11\n" \
  "s_mov_b64 exec, -1\n" \
  "s_waitcnt vmcnt(8) lgkmcnt(0)\n" \
  "s_barrier\n" \
  "ds_read_b64_tr_b16 v[164:165], v20 offset:4096\n" \
  "ds_read_b64_tr_b16 v[166:167], v20 offset:6144\n" \
  "ds_read_b64_tr_b16 v[172:173], v21 offset:4096\n" \
  "ds_read_b64_tr_b16 v[174:175], v21 offset:6144\n" \
  "ds_read_b64_tr_b16 v[180:181], v22 offset:4096\n" \
  "ds_read_b64_tr_b16 v[182:183], v22 offset:6144\n" \
  "ds_read_b64_tr_b16 v[188:189], v23 offset:4096\n" \
  "ds_read_b64_tr_b16 v[190:191], v23 offset:6144\n" \
  "s_add_u32 m0, s19, 0\n" \
  "s_nop 0\n" \
  "global_load_lds_dwordx4 v24, s[4:5] offset:0\n" \
  "global_load_lds_dwordx4 v25, s[4:5] offset:1024\n" \
  "global_load_lds_dwordx4 v26, s[4:5] offset:2048\n" \
  "global_load_lds_dwordx4 v27, s[4:5] offset:3072\n" \
  "s_add_u32 s4, s4, 0x100000\n" \
  "s_addc_u32 s5, s5, 0\n" \
  "v_mfma_f32_16x16x32_bf16 v[64:67], v[160:163], v[160:163], 0\n" \
  "v_mfma_f32_16x16x32_bf16 v[68:71], v[168:171], v[168:171], 0\n" \
  "v_mfma_f32_16x16x32_bf16 v[72:75], v[176:179], v[176:179], 0\n" \
  "v_mfma_f32_16x16x32_bf16 v[76:79], v[184:187], v[184:187], 0\n" \
  "s_waitcnt vmcnt(8) lgkmcnt(0)\n" \
  "s_barrier\n" \
  "ds_read_b64_tr_b16 v[32:33], v20 offset:8192\n" \
  "ds_read_b64_tr_b16 v[34:35], v20 offset:10240\n" \
  "ds_read_b64_tr_b16 v[40:41], v21 offset:8192\n" \
  "ds_read_b64_tr_b16 v[42:43], v21 offset:10240\n" \
  "ds_read_b64_tr_b16 v[48:49], v22 offset:8192\n" \
  "ds_read_b64_tr_b16 v[50:51], v22 offset:10240\n" \
  "ds_read_b64_tr_b16 v[56:57], v23 offset:8192\n" \
  "ds_read_b64_tr_b16 v[58:59], v23 offset:10240\n" \
  "s_add_u32 m0, s19, 4096\n" \
  "s_nop 0\n" \
  "global_load_lds_dwordx4 v24, s[4:5] offset:0\n" \
  "global_load_lds_dwordx4 v25, s[4:5] offset:1024\n" \
  "global_load_lds_dwordx4 v26, s[4:5] offset:2048\n" \
  "global_load_lds_dwordx4 v27, s[4:5] offset:3072\n" \
  "s_add_u32 s4, s4, 0x100000\n" \
  "s_addc_u32 s5, s5, 0\n" \
  "v_mfma_f32_16x16x32_bf16 v[64:67], v[164:167], v[164:167], v[64:67]\n" \
  "v_mfma_f32_16x16x32_bf16 v[68:71], v[172:175], v[172:175], v[68:71]\n" \
  "v_mfma_f32_16x16x32_bf16 v[72:75], v[180:183], v[180:183], v[72:75]\n" \
  "v_mfma_f32_16x16x32_bf16 v[76:79], v[188:191], v[188:191], v[76:79]\n" \
  "s_waitcnt vmcnt(8) lgkmcnt(0)\n" \
  "s_barrier\n" \
  "ds_read_b64_tr_b16 v[36:37], v20 offset:12288\n" \
  "ds_read_b64_tr_b16 v[38:39], v20 offset:14336\n" \
  "ds_read_b64_tr_b16 v[44:45], v21 offset:12288\n" \
  "ds_read_b64_tr_b16 v[46:47], v21 offset:14336\n" \
  "ds_read_b64_tr_b16 v[52:53], v22 offset:12288\n" \
  "ds_read_b64_tr_b16 v[54:55], v22 offset:14336\n" \
  "ds_read_b64_tr_b16 v[60:61], v23 offset:12288\n" \
  "ds_read_b64_tr_b16 v[62:63], v23 offset:14336\n" \
  "s_add_u32 m0, s19, 8192\n" \
  "s_nop 0\n" \
  "global_load_lds_dwordx4 v24, s[4:5] offset:0\n" \
  "global_load_lds_dwordx4 v25, s[4:5] offset:1024\n" \
  "global_load_lds_dwordx4 v26, s[4:5] offset:2048\n" \
  "global_load_lds_dwordx4 v27, s[4:5] offset:3072\n" \
  "s_add_u32 s4, s4, 0x100000\n" \
  "s_addc_u32 s5, s5, 0\n" \
  "v_mfma_f32_16x16x32_bf16 v[64:67], v[32:35], v[32:35], v[64:67]\n" \
  "v_mfma_f32_16x16x32_bf16 v[68:71], v[40:43], v[40:43], v[68:71]\n" \
  "v_mfma_f32_16x16x32_bf16 v[72:75], v[48:51], v[48:51], v[72:75]\n" \
  "v_mfma_f32_16x16x32_bf16 v[76:79], v[56:59], v[56:59], v[76:79]\n" \
  "s_waitcnt vmcnt(8) lgkmcnt(0)\n" \
  "s_barrier\n" \
  "s_add_u32 m0, s19, 12288\n" \
  "s_nop 0\n" \
  "global_load_lds_dwordx4 v24, s[4:5] offset:0\n" \
  "global_load_lds_dwordx4 v25, s[4:5] offset:1024\n" \
  "global_load_lds_dwordx4 v26, s[4:5] offset:2048\n" \
  "global_load_lds_dwordx4 v27, s[4:5] offset:3072\n" \
  "s_add_u32 s4, s4, 0x100000\n" \
  "s_addc_u32 s5, s5, 0\n" \
  "v_mfma_f32_16x16x32_bf16 v[64:67], v[36:39], v[36:39], v[64:67]\n" \
  "v_mfma_f32_16x16x32_bf16 v[68:71], v[44:47], v[44:47], v[68:71]\n" \
  "v_mfma_f32_16x16x32_bf16 v[72:75], v[52:55], v[52:55], v[72:75]\n" \
  "v_mfma_f32_16x16x32_bf16 v[76:79], v[60:63], v[60:63], v[76:79]\n" \
  "s_nop 7\n" \
  "v_max3_f32 v13, v64, v65, v66\n" \
  "v_max3_f32 v13, v13, v67, v68\n" \
  "v_max3_f32 v13, v13, v69, v70\n" \
  "v_max3_f32 v13, v13, v71, v72\n" \
  "v_max3_f32 v13, v13, v73, v74\n" \
  "v_max3_f32 v13, v13, v75, v76\n" \
  "v_max3_f32 v13, v13, v77, v78\n" \
  "v_max_f32 v13, v13, v79\n" \
  "s_nop 1\n" \
  "v_max_f32_dpp v13, v13, v13 row_ror:1 row_mask:0xf bank_mask:0xf\n" \
  "s_nop 1\n" \
  "v_max_f32_dpp v13, v13, v13 row_ror:2 row_mask:0xf bank_mask:0xf\n" \
  "s_nop 1\n" \
  "v_max_f32_dpp v13, v13, v13 row_ror:4 row_mask:0xf bank_mask:0xf\n" \
  "s_nop 1\n" \
  "v_max_f32_dpp v13, v13, v13 row_ror:8 row_mask:0xf bank_mask:0xf\n" \
  "v_mov_b32 v31, v13\n" \
  "s_nop 1\n" \
  "v_permlane16_swap_b32 v13, v31\n" \
  "v_max_f32 v13, v13, v31\n" \
  "v_mov_b32 v31, v13\n" \
  "s_nop 1\n" \
  "v_permlane32_swap_b32 v13, v31\n" \
  "v_max_f32 v13, v13, v31\n" \
  "s_mov_b64 exec, 1\n" \
  "ds_max_u32 v30, v13 offset:32\n" \
  "s_mov_b64 exec, -1\n" \
  "s_waitcnt vmcnt(8) lgkmcnt(0)\n" \
  "s_barrier\n" \
  "s_add_u32 m0, s19, 0\n" \
  "s_nop 0\n" \
  "global_load_lds_dwordx4 v24, s[6:7] offset:0\n" \
  "global_load_lds_dwordx4 v25, s[6:7] offset:1024\n" \
  "global_load_lds_dwordx4 v26, s[6:7] offset:2048\n" \
  "global_load_lds_dwordx4 v27, s[6:7] offset:3072\n" \
  "s_add_u32 s6, s6, 0x100000\n" \
  "s_addc_u32 s7, s7, 0\n" \
  "s_waitcnt vmcnt(8) lgkmcnt(0)\n" \
  "s_barrier\n" \
  "s_add_u32 m0, s19, 4096\n" \
  "s_nop 0\n" \
  "global_load_lds_dwordx4 v24, s[6:7] offset:0\n" \
  "global_load_lds_dwordx4 v25, s[6:7] offset:1024\n" \
  "global_load_lds_dwordx4 v26, s[6:7] offset:2048\n" \
  "global_load_lds_dwordx4 v27, s[6:7] offset:3072\n" \
  "s_add_u32 s6, s6, 0x100000\n" \
  "s_addc_u32 s7, s7, 0\n" \
  "s_waitcnt vmcnt(8) lgkmcnt(0)\n" \
  "s_barrier\n" \
  "s_add_u32 m0, s19, 8192\n" \
  "s_nop 0\n" \
  "global_load_lds_dwordx4 v24, s[6:7] offset:0\n" \
  "global_load_lds_dwordx4 v25, s[6:7] offset:1024\n" \
  "global_load_lds_dwordx4 v26, s[6:7] offset:2048\n" \
  "global_load_lds_dwordx4 v27, s[6:7] offset:3072\n" \
  "s_add_u32 s6, s6, 0x100000\n" \
  "s_addc_u32 s7, s7, 0\n" \
  "s_waitcnt vmcnt(8) lgkmcnt(0)\n" \
  "s_barrier\n" \
  "ds_read_b64_tr_b16 v[64:65], v28 offset:0\n" \
  "ds_read_b64_tr_b16 v[66:67], v28 offset:512\n" \
  "ds_read_b64_tr_b16 v[68:69], v28 offset:32768\n" \
  "ds_read_b64_tr_b16 v[70:71], v28 offset:33280\n" \
  "ds_read_b64_tr_b16 v[72:73], v17 offset:0\n" \
  "ds_read_b64_tr_b16 v[74:75], v17 offset:512\n" \
  "ds_read_b64_tr_b16 v[76:77], v17 offset:32768\n" \
  "ds_read_b64_tr_b16 v[78:79], v17 offset:33280\n" \
  "ds_read_b64_tr_b16 v[192:193], v18 offset:0\n" \
  "ds_read_b64_tr_b16 v[194:195], v18 offset:512\n" \
  "ds_read_b64_tr_b16 v[196:197], v18 offset:32768\n" \
  "ds_read_b64_tr_b16 v[198:199], v18 offset:33280\n" \
  "ds_read_b64_tr_b16 v[200:201], v19 offset:0\n" \
  "ds_read_b64_tr_b16 v[202:203], v19 offset:512\n" \
  "ds_read_b64_tr_b16 v[204:205], v19 offset:32768\n" \
  "ds_read_b64_tr_b16 v[206:207], v19 offset:33280\n" \
  "s_add_u32 m0, s19, 12288\n" \
  "s_nop 0\n" \
  "global_load_lds_dwordx4 v24, s[6:7] offset:0\n" \
  "global_load_lds_dwordx4 v25, s[6:7] offset:1024\n" \
  "global_load_lds_dwordx4 v26, s[6:7] offset:2048\n" \
  "global_load_lds_dwordx4 v27, s[6:7] offset:3072\n" \
  "s_add_u32 s6, s6, 0x100000\n" \
  "s_addc_u32 s7, s7, 0\n" \
  "s_waitcnt vmcnt(8) lgkmcnt(0)\n" \
  "s_barrier\n" \
  "ds_read_b64_tr_b16 v[80:81], v28 offset:4096\n" \
  "ds_read_b64_tr_b16 v[82:83], v28 offset:4608\n" \
  "ds_read_b64_tr_b16 v[84:85], v28 offset:36864\n" \
  "ds_read_b64_tr_b16 v[86:87], v28 offset:37376\n" \
  "ds_read_b64_tr_b16 v[88:89], v17 offset:4096\n" \
  "ds_read_b64_tr_b16 v[90:91], v17 offset:4608\n" \
  "ds_read_b64_tr_b16 v[92:93], v17 offset:36864\n" \
  "ds_read_b64_tr_b16 v[94:95], v17 offset:37376\n" \
  "ds_read_b64_tr_b16 v[208:209], v18 offset:4096\n" \
  "ds_read_b64_tr_b16 v[210:211], v18 offset:4608\n" \
  "ds_read_b64_tr_b16 v[212:213], v18 offset:36864\n" \
  "ds_read_b64_tr_b16 v[214:215], v18 offset:37376\n" \
  "ds_read_b64_tr_b16 v[216:217], v19 offset:4096\n" \
  "ds_read_b64_tr_b16 v[218:219], v19 offset:4608\n" \
  "ds_read_b64_tr_b16 v[220:221], v19 offset:36864\n" \
  "ds_read_b64_tr_b16 v[222:223], v19 offset:37376\n" \
  "s_add_u32 m0, s19, 0\n" \
  "s_nop 0\n" \
  "global_load_lds_dwordx4 v24, s[8:9] offset:0\n" \
  "global_load_lds_dwordx4 v25, s[8:9] offset:1024\n" \
  "global_load_lds_dwordx4 v26, s[8:9] offset:2048\n" \
  "global_load_lds_dwordx4 v27, s[8:9] offset:3072\n" \
  "s_add_u32 s8, s8, 0x100000\n" \
  "s_addc_u32 s9, s9, 0\n" \
  "s_waitcnt vmcnt(8) lgkmcnt(0)\n" \
  "s_barrier\n" \
  "ds_read_b64_tr_b16 v[96:97], v28 offset:8192\n" \
  "ds_read_b64_tr_b16 v[98:99], v28 offset:8704\n" \
  "ds_read_b64_tr_b16 v[100:101], v28 offset:40960\n" \
  "ds_read_b64_tr_b16 v[102:103], v28 offset:41472\n" \
  "ds_read_b64_tr_b16 v[104:105], v17 offset:8192\n" \
  "ds_read_b64_tr_b16 v[106:107], v17 offset:8704\n" \
  "ds_read_b64_tr_b16 v[108:109], v17 offset:40960\n" \
  "ds_read_b64_tr_b16 v[110:111], v17 offset:41472\n" \
  "ds_read_b64_tr_b16 v[224:225], v18 offset:8192\n" \
  "ds_read_b64_tr_b16 v[226:227], v18 offset:8704\n" \
  "ds_read_b64_tr_b16 v[228:229], v18 offset:40960\n" \
  "ds_read_b64_tr_b16 v[230:231], v18 offset:41472\n" \
  "ds_read_b64_tr_b16 v[232:233], v19 offset:8192\n" \
  "ds_read_b64_tr_b16 v[234:235], v19 offset:8704\n" \
  "ds_read_b64_tr_b16 v[236:237], v19 offset:40960\n" \
  "ds_read_b64_tr_b16 v[238:239], v19 offset:41472\n" \
  "s_add_u32 m0, s19, 4096\n" \
  "s_nop 0\n" \
  "global_load_lds_dwordx4 v24, s[8:9] offset:0\n" \
  "global_load_lds_dwordx4 v25, s[8:9] offset:1024\n" \
  "global_load_lds_dwordx4 v26, s[8:9] offset:2048\n" \
  "global_load_lds_dwordx4 v27, s[8:9] offset:3072\n" \
  "s_add_u32 s8, s8, 0x100000\n" \
  "s_addc_u32 s9, s9, 0\n" \
  "s_waitcnt vmcnt(8) lgkmcnt(0)\n" \
  "s_barrier\n" \
  "ds_read_b64_tr_b16 v[112:113], v28 offset:12288\n" \
  "ds_read_b64_tr_b16 v[114:115], v28 offset:12800\n" \
  "ds_read_b64_tr_b16 v[116:117], v28 offset:45056\n" \
  "ds_read_b64_tr_b16 v[118:119], v28 offset:45568\n" \
  "ds_read_b64_tr_b16 v[120:121], v17 offset:12288\n" \
  "ds_read_b64_tr_b16 v[122:123], v17 offset:12800\n" \
  "ds_read_b64_tr_b16 v[124:125], v17 offset:45056\n" \
  "ds_read_b64_tr_b16 v[126:127], v17 offset:45568\n" \
  "ds_read_b64_tr_b16 v[240:241], v18 offset:12288\n" \
  "ds_read_b64_tr_b16 v[242:243], v18 offset:12800\n" \
  "ds_read_b64_tr_b16 v[244:245], v18 offset:45056\n" \
  "ds_read_b64_tr_b16 v[246:247], v18 offset:45568\n" \
  "ds_read_b64_tr_b16 v[248:249], v19 offset:12288\n" \
  "ds_read_b64_tr_b16 v[250:251], v19 offset:12800\n" \
  "ds_read_b64_tr_b16 v[252:253], v19 offset:45056\n" \
  "ds_read_b64_tr_b16 v[254:255], v19 offset:45568\n" \
  "s_add_u32 m0, s19, 8192\n" \
  "s_nop 0\n" \
  "global_load_lds_dwordx4 v24, s[8:9] offset:0\n" \
  "global_load_lds_dwordx4 v25, s[8:9] offset:1024\n" \
  "global_load_lds_dwordx4 v26, s[8:9] offset:2048\n" \
  "global_load_lds_dwordx4 v27, s[8:9] offset:3072\n" \
  "s_add_u32 s8, s8, 0x100000\n" \
  "s_addc_u32 s9, s9, 0\n" \
  "s_waitcnt vmcnt(8) lgkmcnt(0)\n" \
  "s_barrier\n" \
  "ds_read_b64_tr_b16 v[128:129], v28 offset:0\n" \
  "ds_read_b64_tr_b16 v[130:131], v28 offset:512\n" \
  "ds_read_b64_tr_b16 v[132:133], v28 offset:32768\n" \
  "ds_read_b64_tr_b16 v[134:135], v28 offset:33280\n" \
  "ds_read_b64_tr_b16 v[136:137], v17 offset:0\n" \
  "ds_read_b64_tr_b16 v[138:139], v17 offset:512\n" \
  "ds_read_b64_tr_b16 v[140:141], v17 offset:32768\n" \
  "ds_read_b64_tr_b16 v[142:143], v17 offset:33280\n" \
  "s_add_u32 m0, s19, 12288\n" \
  "s_nop 0\n" \
  "global_load_lds_dwordx4 v24, s[8:9] offset:0\n" \
  "global_load_lds_dwordx4 v25, s[8:9] offset:1024\n" \
  "global_load_lds_dwordx4 v26, s[8:9] offset:2048\n" \
  "global_load_lds_dwordx4 v27, s[8:9] offset:3072\n" \
  "s_add_u32 s8, s8, 0x100000\n" \
  "s_addc_u32 s9, s9, 0\n" \
  "s_waitcnt vmcnt(8) lgkmcnt(0)\n" \
  "s_barrier\n" \
  "ds_write_b128 v6, v[160:163] offset:0\n" \
  "ds_write_b128 v6, v[164:167] offset:1024\n" \
  "ds_write_b128 v6, v[168:171] offset:2048\n" \
  "ds_write_b128 v6, v[172:175] offset:3072\n" \
  "ds_read_b64_tr_b16 v[144:145], v28 offset:4096\n" \
  "ds_read_b64_tr_b16 v[146:147], v28 offset:4608\n" \
  "ds_read_b64_tr_b16 v[148:149], v28 offset:36864\n" \
  "ds_read_b64_tr_b16 v[150:151], v28 offset:37376\n" \
  "ds_read_b64_tr_b16 v[152:153], v17 offset:4096\n" \
  "ds_read_b64_tr_b16 v[154:155], v17 offset:4608\n" \
  "ds_read_b64_tr_b16 v[156:157], v17 offset:36864\n" \
  "ds_read_b64_tr_b16 v[158:159], v17 offset:37376\n" \
  "s_waitcnt vmcnt(4) lgkmcnt(0)\n" \
  "s_barrier\n" \
  "ds_write_b128 v6, v[176:179] offset:4096\n" \
  "ds_write_b128 v6, v[180:183] offset:5120\n" \
  "ds_write_b128 v6, v[184:187] offset:6144\n" \
  "ds_write_b128 v6, v[188:191] offset:7168\n" \
  "ds_read_b64_tr_b16 v[160:161], v28 offset:8192\n" \
  "ds_read_b64_tr_b16 v[162:163], v28 offset:8704\n" \
  "ds_read_b64_tr_b16 v[164:165], v28 offset:40960\n" \
  "ds_read_b64_tr_b16 v[166:167], v28 offset:41472\n" \
  "ds_read_b64_tr_b16 v[168:169], v17 offset:8192\n" \
  "ds_read_b64_tr_b16 v[170:171], v17 offset:8704\n" \
  "ds_read_b64_tr_b16 v[172:173], v17 offset:40960\n" \
  "ds_read_b64_tr_b16 v[174:175], v17 offset:41472\n" \
  "s_waitcnt vmcnt(0) lgkmcnt(0)\n" \
  "s_barrier\n" \
  "ds_write_b128 v6, v[32:35] offset:8192\n" \
  "ds_write_b128 v6, v[36:39] offset:9216\n" \
  "ds_write_b128 v6, v[40:43] offset:10240\n" \
  "ds_write_b128 v6, v[44:47] offset:11264\n" \
  "ds_read_b64_tr_b16 v[176:177], v28 offset:12288\n" \
  "ds_read_b64_tr_b16 v[178:179], v28 offset:12800\n" \
  "ds_read_b64_tr_b16 v[180:181], v28 offset:45056\n" \
  "ds_read_b64_tr_b16 v[182:183], v28 offset:45568\n" \
  "ds_read_b64_tr_b16 v[184:185], v17 offset:12288\n" \
  "ds_read_b64_tr_b16 v[186:187], v17 offset:12800\n" \
  "ds_read_b64_tr_b16 v[188:189], v17 offset:45056\n" \
  "ds_read_b64_tr_b16 v[190:191], v17 offset:45568\n" \
  "ds_read_b32 v31, v30 offset:0\n" \
  "s_waitcnt lgkmcnt(0)\n" \
  "s_barrier\n" \
  "ds_write_b128 v6, v[48:51] offset:12288\n" \
  "ds_write_b128 v6, v[52:55] offset:13312\n" \
  "ds_write_b128 v6, v[56:59] offset:14336\n" \
  "ds_write_b128 v6, v[60:63] offset:15360\n" \
  "v_mul_f32 v31, 0x3f8e353f, v31\n" \
  "v_mul_f32 v30, 0x44870000, v31\n" \
  "v_mov_b64 v[12:13], 0\n" \
  "v_mov_b64 v[14:15], 0\n" \
  "v_mov_b64 v[16:17], 0\n" \
  "v_mov_b64 v[18:19], 0\n" \
  "v_mov_b64 v[20:21], 0\n" \
  "v_mov_b64 v[22:23], 0\n" \
  "v_readfirstlane_b32 s72, v31\n" \
  "v_readfirstlane_b32 s86, v30\n" \
  "s_lshr_b32 s86, s86, 23\n" \
  "s_addk_i32 s86, 0xff83\n" \
  "s_ashr_i32 s86, s86, 1\n" \
  "s_max_i32 s86, s86, -20\n" \
  "s_min_i32 s86, s86, 100\n" \
  "s_mov_b32 s82, s86\n" \
  "s_lshr_b32 s83, s72, 23\n" \
  "s_sub_i32 s83, s83, 243\n" \
  "s_waitcnt lgkmcnt(0)\n" \
  "s_add_u32 s88, s74, 157760\n" \
  "v_mov_b32 v59, s88\n" \
  "v_mov_b32 v34, 0xc9b26ca0\n" \
  "v_mov_b32 v35, 0xc9b26c86\n" \
  ".p2align 6\n" \
  "s_nop 0\n" \
  "s_nop 0\n" \
  "s_nop 0\n" \
  ".LCA_TOP_%=:\n" \
  "s_waitcnt lgkmcnt(0)\n" \
  "s_barrier\n" \
  "ds_read_b128 v[40:43], v1 offset:0\n" \
  "ds_read_b128 v[44:47], v1 offset:1024\n" \
  "ds_read_b128 v[48:51], v1 offset:2048\n" \
  "ds_read_b128 v[52:55], v1 offset:3072\n" \
  "ds_read_b128 v[56:59], v1 offset:4096\n" \
  "ds_read_b128 v[60:63], v1 offset:5120\n" \
  "s_cmp_lg_u32 s73, 0\n" \
  "s_cbranch_scc1 .LCA_NOZ_%=\n" \
  "s_xor_b32 s88, s60, 0x10000\n" \
  "s_add_u32 s88, s88, s61\n" \
  "buffer_store_dwordx2 v[34:35], v5, s[40:43], s88 offen sc1\n" \
  ".LCA_NOZ_%=:\n" \
  "s_waitcnt lgkmcnt(5)\n" \
  "v_smfmac_f32_16x16x64_bf16 v[20:23], v[40:43], v[192:199], v0\n" \
  "v_smfmac_f32_16x16x64_bf16 v[12:15], v[40:43], v[64:71], v0\n" \
  "v_smfmac_f32_16x16x64_bf16 v[16:19], v[40:43], v[128:135], v0\n" \
  "ds_read_b128 v[40:43], v1 offset:6144\n" \
  "s_waitcnt lgkmcnt(5)\n" \
  "v_smfmac_f32_16x16x64_bf16 v[20:23], v[44:47], v[200:207], v0\n" \
  "v_smfmac_f32_16x16x64_bf16 v[12:15], v[44:47], v[72:79], v0\n" \
  "v_smfmac_f32_16x16x64_bf16 v[16:19], v[44:47], v[136:143], v0\n" \
  "ds_read_b128 v[44:47], v1 offset:7168\n" \
  "s_waitcnt lgkmcnt(5)\n" \
  "v_smfmac_f32_16x16x64_bf16 v[20:23], v[48:51], v[208:215], v0\n" \
  "v_smfmac_f32_16x16x64_bf16 v[12:15], v[48:51], v[80:87], v0\n" \
  "v_smfmac_f32_16x16x64_bf16 v[16:19], v[48:51], v[144:151], v0\n" \
  "ds_read_b128 v[24:27], v1 offset:10240\n" \
  "ds_read_b128 v[28:31], v1 offset:14336\n" \
  "s_waitcnt lgkmcnt(6)\n" \
  "v_smfmac_f32_16x16x64_bf16 v[20:23], v[52:55], v[216:223], v0\n" \
  "v_smfmac_f32_16x16x64_bf16 v[12:15], v[52:55], v[88:95], v0\n" \
  "v_smfmac_f32_16x16x64_bf16 v[16:19], v[52:55], v[152:159], v0\n" \
  "ds_read_b128 v[32:35], v1 offset:11264\n" \
  "ds_read_b128 v[36:39], v1 offset:15360\n" \
  "s_waitcnt lgkmcnt(7)\n" \
  "v_smfmac_f32_16x16x64_bf16 v[20:23], v[56:59], v[224:231], v0\n" \
  "v_smfmac_f32_16x16x64_bf16 v[12:15], v[56:59], v[96:103], v0\n" \
  "v_smfmac_f32_16x16x64_bf16 v[16:19], v[56:59], v[160:167], v0\n" \
  "ds_read_b128 v[56:59], v1 offset:12288\n" \
  "s_waitcnt lgkmcnt(7)\n" \
  "v_smfmac_f32_16x16x64_bf16 v[20:23], v[60:63], v[232:239], v0\n" \
  "v_smfmac_f32_16x16x64_bf16 v[12:15], v[60:63], v[104:111], v0\n" \
  "v_smfmac_f32_16x16x64_bf16 v[16:19], v[60:63], v[168:175], v0\n" \
  "ds_read_b128 v[60:63], v1 offset:16384\n" \
  "s_waitcnt lgkmcnt(7)\n" \
  "v_smfmac_f32_16x16x64_bf16 v[20:23], v[40:43], v[240:247], v0\n" \
  "v_smfmac_f32_16x16x64_bf16 v[12:15], v[40:43], v[112:119], v0\n" \
  "v_smfmac_f32_16x16x64_bf16 v[16:19], v[40:43], v[176:183], v0\n" \
  "s_waitcnt lgkmcnt(6)\n" \
  "v_smfmac_f32_16x16x64_bf16 v[20:23], v[44:47], v[248:255], v0\n" \
  "v_smfmac_f32_16x16x64_bf16 v[12:15], v[44:47], v[120:127], v0\n" \
  "v_smfmac_f32_16x16x64_bf16 v[16:19], v[44:47], v[184:191], v0\n" \
  "s_cmp_eq_u32 s71, 0\n" \
  "v_permlane32_swap_b32 v20, v22\n" \
  "v_permlane32_swap_b32 v21, v23\n" \
  "v_pk_add_f32 v[20:21], v[20:21], v[22:23]\n" \
  "s_mov_b64 vcc, s[76:77]\n" \
  "v_permlane32_swap_b32 v12, v14\n" \
  "v_permlane32_swap_b32 v13, v15\n" \
  "v_cvt_pk_bf16_f32 v20, v20, v21\n" \
  "v_pk_add_f32 v[12:13], v[12:13], v[14:15]\n" \
  "v_permlane32_swap_b32 v16, v18\n" \
  "v_permlane32_swap_b32 v17, v19\n" \
  "ds_write_b32 v8, v20 offset:2048\n" \
  "v_cvt_pk_bf16_f32 v12, v12, v13\n" \
  "v_pk_add_f32 v[16:17], v[16:17], v[18:19]\n" \
  "s_nop 0\n" \
  "v_cvt_pk_bf16_f32 v13, v16, v17\n" \
  "ds_write_b64 v8, v[12:13]\n" \
  "s_setprio 1\n" \
  "s_waitcnt lgkmcnt(0)\n" \
  "s_barrier\n" \
  "s_cbranch_scc1 .LCA_LAST_%=\n" \
  "ds_read_b64_tr_b16 v[40:41], v2 offset:0\n" \
  "ds_read_b64_tr_b16 v[42:43], v3 offset:0\n" \
  "ds_read_b64_tr_b16 v[44:45], v2 offset:1024\n" \
  "ds_read_b64_tr_b16 v[46:47], v3 offset:1024\n" \
  "ds_read_b64_tr_b16 v[48:49], v2 offset:2048\n" \
  "ds_read_b64_tr_b16 v[50:51], v3 offset:2048\n" \
  "ds_read_b64_tr_b16 v[52:53], v2 offset:3072\n" \
  "ds_read_b64_tr_b16 v[54:55], v3 offset:3072\n" \
  "ds_read_b32 v11, v1 offset:26688\n" \
  "s_waitcnt lgkmcnt(7)\n" \
  "v_mfma_f32_16x16x32_bf16 v[12:15], v[24:27], v[40:43], 0\n" \
  "v_mfma_f32_16x16x32_bf16 v[16:19], v[28:31], v[40:43], 0\n" \
  "ds_read_b128 v[24:27], v1 offset:13312\n" \
  "ds_read_b128 v[28:31], v1 offset:17408\n" \
  "v_mfma_f32_16x16x32_bf16 v[20:23], v[40:43], v[40:43], 0\n" \
  "s_waitcnt lgkmcnt(7)\n" \
  "v_mfma_f32_16x16x32_bf16 v[12:15], v[32:35], v[44:47], v[12:15]\n" \
  "v_mfma_f32_16x16x32_bf16 v[16:19], v[36:39], v[44:47], v[16:19]\n" \
  "v_mfma_f32_16x16x32_bf16 v[20:23], v[44:47], v[44:47], v[20:23]\n" \
  "s_waitcnt lgkmcnt(2)\n" \
  "v_mfma_f32_16x16x32_bf16 v[12:15], v[56:59], v[48:51], v[12:15]\n" \
  "v_readfirstlane_b32 s86, v11\n" \
  "v_mfma_f32_16x16x32_bf16 v[16:19], v[60:63], v[48:51], v[16:19]\n" \
  "ds_read_b128 v[32:35], v6 offset:8192\n" \
  "ds_read_b128 v[36:39], v6 offset:9216\n" \
  "v_mfma_f32_16x16x32_bf16 v[20:23], v[48:51], v[48:51], v[20:23]\n" \
  "s_lshr_b32 s86, s86, 23\n" \
  "s_add_i32 s86, s86, s83\n" \
  "s_ashr_i32 s86, s86, 1\n" \
  "ds_read_b128 v[48:51], v10 offset:18432\n" \
  "v_mfma_f32_16x16x32_bf16 v[20:23], v[52:55], v[52:55], v[20:23]\n" \
  "s_max_i32 s86, s86, s82\n" \
  "s_min_i32 s86, s86, 100\n" \
  "s_lshl_b32 s86, s86, 23\n" \
  "s_sub_i32 s84, 0xc8800000, s86\n" \
  "s_waitcnt lgkmcnt(3)\n" \
  "v_mfma_f32_16x16x32_bf16 v[12:15], v[24:27], v[52:55], v[12:15]\n" \
  "v_mfma_f32_16x16x32_bf16 v[16:19], v[28:31], v[52:55], v[16:19]\n" \
  "ds_read_b128 v[52:55], v10 offset:22528\n" \
  "ds_read_b128 v[56:59], v6 offset:0\n" \
  "ds_read_b128 v[60:63], v6 offset:1024\n" \
  "ds_read_b128 v[24:27], v6 offset:2048\n" \
  "ds_read_b128 v[28:31], v6 offset:3072\n" \
  "v_cndmask_b32_dpp v20, v23, v22, vcc quad_perm:[2,3,0,1] row_mask:0xf bank_mask:0xf\n" \
  "v_mov_b32 v41, 0\n" \
  "v_mov_b32 v42, 0\n" \
  "v_cndmask_b32_dpp v12, v16, v12, vcc quad_perm:[2,3,0,1] row_mask:0xf bank_mask:0xf\n" \
  "v_cndmask_b32_dpp v13, v17, v13, vcc quad_perm:[2,3,0,1] row_mask:0xf bank_mask:0xf\n" \
  "v_cndmask_b32_dpp v14, v18, v14, vcc quad_perm:[2,3,0,1] row_mask:0xf bank_mask:0xf\n" \
  "v_cndmask_b32_dpp v15, v19, v15, vcc quad_perm:[2,3,0,1] row_mask:0xf bank_mask:0xf\n" \
  "v_maximum3_f32 v16, v20, v12, v13\n" \
  "v_maximum3_f32 v16, v16, v14, v15\n" \
  "s_nop 1\n" \
  "v_max_f32_dpp v16, v16, v16 quad_perm:[2,3,0,1] row_mask:0xf bank_mask:0xf\n" \
  "v_mov_b32 v17, v16\n" \
  "s_nop 1\n" \
  "v_permlane16_swap_b32 v16, v17\n" \
  "v_max_f32 v16, v16, v17\n" \
  "v_mov_b32 v17, v16\n" \
  "s_nop 1\n" \
  "v_permlane32_swap_b32 v16, v17\n" \
  "v_max_f32 v16, v16, v17\n" \
  "v_mul_f32 v16, s78, v16\n" \
  "v_pk_fma_f32 v[12:13], v[12:13], s[78:79], v[16:17] op_sel_hi:[1,0,0] neg_lo:[0,0,1] neg_hi:[0,0,1]\n" \
  "v_pk_fma_f32 v[14:15], v[14:15], s[78:79], v[16:17] op_sel_hi:[1,0,0] neg_lo:[0,0,1] neg_hi:[0,0,1]\n" \
  "v_fma_f32 v20, v20, s78, -v16\n" \
  "v_exp_f32 v12, v12\n" \
  "v_exp_f32 v13, v13\n" \
  "v_exp_f32 v14, v14\n" \
  "v_exp_f32 v15, v15\n" \
  "v_exp_f32 v20, v20\n" \
  "s_waitcnt lgkmcnt(4)\n" \
  "v_cvt_pk_bf16_f32 v44, v12, v13\n" \
  "v_cvt_pk_bf16_f32 v45, v14, v15\n" \
  "v_pk_add_f32 v[8:9], v[12:13], v[14:15]\n" \
  "v_mov_b32_dpp v46, v44 quad_perm:[2,3,0,1] row_mask:0xf bank_mask:0xf\n" \
  "v_mov_b32_dpp v47, v45 quad_perm:[2,3,0,1] row_mask:0xf bank_mask:0xf\n" \
  "v_add_f32 v10, v8, v20\n" \
  "v_add_f32 v10, v10, v9\n" \
  "v_mfma_f32_16x16x32_bf16 v[12:15], v[48:51], v[44:47], 0\n" \
  "v_mov_b32_dpp v21, v20 quad_perm:[2,3,0,1] row_mask:0xf bank_mask:0xf\n" \
  "v_add_f32_dpp v10, v10, v10 quad_perm:[2,3,0,1] row_mask:0xf bank_mask:0xf\n" \
  "v_mov_b32 v11, v10\n" \
  "v_mfma_f32_16x16x32_bf16 v[16:19], v[52:55], v[44:47], 0\n" \
  "s_setprio 3\n" \
  "s_barrier\n" \
  "ds_read_b64 v[22:23], v7 offset:0\n" \
  "s_nop 0\n" \
  "v_permlane16_swap_b32 v10, v11\n" \
  "v_add_f32 v10, v10, v11\n" \
  "v_mov_b32 v11, v10\n" \
  "v_mov_b64 v[8:9], s[92:93]\n" \
  "s_nop 0\n" \
  "v_permlane32_swap_b32 v10, v11\n" \
  "v_add_f32 v10, v10, v11\n" \
  "v_rcp_f32 v10, v10\n" \
  "s_nop 0\n" \
  "v_mul_f32 v10, s84, v10\n" \
  "v_pk_mul_f32 v[20:21], v[20:21], v[10:11] op_sel_hi:[1,0]\n" \
  "v_pk_mul_f32 v[12:13], v[12:13], v[10:11] op_sel_hi:[1,0]\n" \
  "v_pk_mul_f32 v[14:15], v[14:15], v[10:11] op_sel_hi:[1,0]\n" \
  "v_cvt_pk_bf16_f32 v40, v20, v21\n" \
  "v_pk_mul_f32 v[16:17], v[16:17], v[10:11] op_sel_hi:[1,0]\n" \
  "v_cvt_pk_bf16_f32 v43, v20, v21\n" \
  "v_pk_mul_f32 v[18:19], v[18:19], v[10:11] op_sel_hi:[1,0]\n" \
  "s_waitcnt lgkmcnt(0)\n" \
  "v_mfma_f32_16x16x16_bf16 v[12:15], v[22:23], v[40:41], v[12:15]\n" \
  "v_mfma_f32_16x16x16_bf16 v[16:19], v[22:23], v[42:43], v[16:19]\n" \
  "v_mov_b64 v[10:11], s[92:93]\n" \
  "ds_read_b128 v[48:51], v6 offset:10240\n" \
  "ds_read_b128 v[52:55], v6 offset:11264\n" \
  "v_mov_b64 v[20:21], s[92:93]\n" \
  "v_mov_b64 v[22:23], s[92:93]\n" \
  "s_nop 1\n" \
  "v_cvt_pk_bf16_f32 v40, v12, v13\n" \
  "v_cvt_pk_bf16_f32 v41, v14, v15\n" \
  "v_cvt_pk_bf16_f32 v42, v16, v17\n" \
  "ds_write_b64 v4, v[40:41]\n" \
  "v_cvt_pk_bf16_f32 v43, v18, v19\n" \
  "ds_write_b64 v4, v[42:43] offset:1024\n" \
  "v_mov_b64 v[12:13], s[92:93]\n" \
  "v_mov_b64 v[14:15], s[92:93]\n" \
  "v_mov_b64 v[16:17], s[92:93]\n" \
  "v_mov_b64 v[18:19], s[92:93]\n" \
  "s_setprio 0\n" \
  "s_waitcnt lgkmcnt(0)\n" \
  "s_barrier\n" \
  "ds_read_b128 v[40:43], v1 offset:8192\n" \
  "ds_read_b128 v[44:47], v1 offset:9216\n" \
  "s_waitcnt lgkmcnt(1)\n" \
  "v_smfmac_f32_16x16x64_bf16 v[12:15], v[40:43], v[56:63], v0\n" \
  "ds_read_b128 v[56:59], v6 offset:4096\n" \
  "ds_read_b128 v[60:63], v6 offset:5120\n" \
  "v_smfmac_f32_16x16x64_bf16 v[16:19], v[40:43], v[24:31], v0\n" \
  "ds_read_b128 v[24:27], v6 offset:6144\n" \
  "ds_read_b128 v[28:31], v6 offset:7168\n" \
  "s_waitcnt lgkmcnt(4)\n" \
  "v_smfmac_f32_16x16x64_bf16 v[12:15], v[44:47], v[32:39], v0\n" \
  "ds_read_b128 v[32:35], v6 offset:12288\n" \
  "ds_read_b128 v[36:39], v6 offset:13312\n" \
  "v_smfmac_f32_16x16x64_bf16 v[16:19], v[44:47], v[48:55], v0\n" \
  "ds_read_b128 v[48:51], v6 offset:14336\n" \
  "ds_read_b128 v[52:55], v6 offset:15360\n" \
  "s_nop 5\n" \
  "v_permlane32_swap_b32 v12, v17\n" \
  "v_permlane32_swap_b32 v13, v16\n" \
  "v_permlane32_swap_b32 v14, v19\n" \
  "v_permlane32_swap_b32 v15, v18\n" \
  "v_pk_add_f32 v[12:13], v[12:13], v[16:17] op_sel:[0,1] op_sel_hi:[1,0]\n" \
  "v_pk_add_f32 v[14:15], v[14:15], v[18:19] op_sel:[0,1] op_sel_hi:[1,0]\n" \
  "buffer_atomic_add_x2 v[12:13], v5, s[40:43], s56 offen\n" \
  "buffer_atomic_add_x2 v[14:15], v5, s[40:43], s57 offen\n" \
  "s_waitcnt lgkmcnt(6)\n" \
  "v_smfmac_f32_16x16x64_bf16 v[20:23], v[40:43], v[56:63], v0\n" \
  "s_waitcnt lgkmcnt(4)\n" \
  "v_smfmac_f32_16x16x64_bf16 v[8:11], v[40:43], v[24:31], v0\n" \
  "s_waitcnt lgkmcnt(2)\n" \
  "v_smfmac_f32_16x16x64_bf16 v[20:23], v[44:47], v[32:39], v0\n" \
  "s_waitcnt lgkmcnt(0)\n" \
  "v_smfmac_f32_16x16x64_bf16 v[8:11], v[44:47], v[48:55], v0\n" \
  "s_nop 7\n" \
  "v_permlane32_swap_b32 v20, v9\n" \
  "v_permlane32_swap_b32 v21, v8\n" \
  "v_permlane32_swap_b32 v22, v11\n" \
  "v_permlane32_swap_b32 v23, v10\n" \
  "v_pk_add_f32 v[20:21], v[20:21], v[8:9] op_sel:[0,1] op_sel_hi:[1,0]\n" \
  "v_pk_add_f32 v[22:23], v[22:23], v[10:11] op_sel:[0,1] op_sel_hi:[1,0]\n" \
  "buffer_atomic_add_x2 v[20:21], v5, s[40:43], s58 offen\n" \
  "buffer_atomic_add_x2 v[22:23], v5, s[40:43], s59 offen\n" \
  "s_memrealtime s[38:39]\n" \
  "v_mbcnt_lo_u32_b32 v48, -1, 0\n" \
  "v_mbcnt_hi_u32_b32 v48, -1, v48\n" \
  "s_add_u32 s88, s74, 157760\n" \
  "v_mov_b32 v59, s88\n" \
  "v_lshl_add_u32 v52, v48, 2, v59\n" \
  "ds_read_b32 v52, v52 offset:192\n" \
  "v_lshrrev_b32 v49, 3, v48\n" \
  "v_and_b32 v50, 7, v48\n" \
  "v_lshlrev_b32 v50, 4, v50\n" \
  "v_lshl_or_b32 v44, v49, 8, v50\n" \
  "s_mov_b32 s87, 0x40000\n" \
  "v_lshrrev_b32 v49, 2, v48\n" \
  "v_lshrrev_b32 v50, 4, v48\n" \
  "v_bitop3_b32 v50, v49, v50, 3 bitop3:0x28\n" \
  "v_lshl_add_u32 v8, v50, 3, s97\n" \
  "v_and_b32 v50, 3, v48\n" \
  "v_lshl_add_u32 v8, v50, 5, v8\n" \
  "v_and_b32 v49, 1, v49\n" \
  "v_lshl_add_u32 v8, v49, 8, v8\n" \
  "v_and_b32 v49, 8, v48\n" \
  "v_lshl_add_u32 v8, v49, 4, v8\n" \
  "v_lshrrev_b32 v49, 5, v48\n" \
  "v_add_u32 v10, s99, v1\n" \
  "s_lshr_b32 s88, s99, 2\n" \
  "s_add_u32 s88, s88, s74\n" \
  "s_add_u32 s88, s88, 139264\n" \
  "v_lshl_add_u32 v7, v49, 10, s88\n" \
  "v_and_b32 v49, 31, v48\n" \
  "v_lshl_add_u32 v7, v49, 3, v7\n" \
  "v_mov_b64 v[12:13], 0\n" \
  "v_mov_b64 v[14:15], 0\n" \
  "v_mov_b64 v[16:17], 0\n" \
  "v_mov_b64 v[18:19], 0\n" \
  "v_mov_b64 v[20:21], 0\n" \
  "v_mov_b64 v[22:23], 0\n" \
  "v_mov_b32 v34, 0xc9b26ca0\n" \
  "v_mov_b32 v35, 0xc9b26c86\n" \
  "v_and_b32 v50, 3, v48\n" \
  "v_lshl_add_u32 v40, v50, 2, s98\n" \
  "v_lshrrev_b32 v50, 4, v48\n" \
  "v_mad_u32_u24 v40, v50, s95, v40\n" \
  "v_bfe_u32 v49, v48, 3, 1\n" \
  "v_lshl_add_u32 v40, v49, 6, v40\n" \
  "v_bfe_u32 v49, v48, 2, 1\n" \
  "v_lshl_add_u32 v40, v49, 7, v40\n" \
  "v_xor_b32 v41, 16, v40\n" \
  "v_xor_b32 v42, 32, v40\n" \
  "v_xor_b32 v43, 48, v40\n" \
  "s_add_i32 s80, s86, 0x35000000\n" \
  "v_mov_b32 v11, 0\n" \
  "s_mov_b32 s89, s60\n" \
  "s_bitcmp1_b32 s60, 7\n" \
  "s_cselect_b32 s88, 0x10000, 0\n" \
  "s_bitset1_b32 s88, 7\n" \
  "s_xor_b32 s56, s56, s88\n" \
  "s_xor_b32 s57, s57, s88\n" \
  "s_xor_b32 s58, s58, s88\n" \
  "s_xor_b32 s59, s59, s88\n" \
  "s_xor_b32 s60, s60, s88\n" \
  "s_sub_u32 s71, s71, 1\n" \
  "s_waitcnt lgkmcnt(0)\n" \
  "s_sub_u32 s39, s38, 150\n" \
  "v_subrev_u32 v52, s39, v52\n" \
  "v_cmp_lt_u32 vcc, 0x1000, v52\n" \
  "s_cmp_eq_u64 vcc, 0\n" \
  "s_cbranch_scc1 .LCA_OKM_%=\n" \
  ".LCA_AGN_%=:\n" \
  "buffer_load_dword v52, v5, s[0:3], s66 offen sc1\n" \
  "s_sub_u32 s87, s87, 1\n" \
  "s_cbranch_scc1 .LCA_END_%=\n" \
  "s_waitcnt vmcnt(0)\n" \
  "v_cmp_eq_u32 vcc, 0, v52\n" \
  "v_subrev_u32 v52, s39, v52\n" \
  "s_cmp_eq_u64 vcc, 0\n" \
  "s_cbranch_scc1 .LCA_OKM_%=\n" \
  "s_sleep 4\n" \
  "s_branch .LCA_AGN_%=\n" \
  ".LCA_OKM_%=:\n" \
  "s_nop 1\n" \
  "v_max_i32_dpp v52, v52, v52 row_ror:1 row_mask:0xf bank_mask:0xf\n" \
  "s_nop 1\n" \
  "v_max_i32_dpp v52, v52, v52 row_ror:2 row_mask:0xf bank_mask:0xf\n" \
  "s_nop 1\n" \
  "v_max_i32_dpp v52, v52, v52 row_ror:4 row_mask:0xf bank_mask:0xf\n" \
  "s_nop 1\n" \
  "v_max_i32_dpp v52, v52, v52 row_ror:8 row_mask:0xf bank_mask:0xf\n" \
  "s_movk_i32 vcc_lo, 150\n" \
  "v_readlane_b32 s88, v52, 0\n" \
  "s_max_i32 vcc_lo, vcc_lo, s88\n" \
  "v_readlane_b32 s88, v52, 16\n" \
  "s_max_i32 vcc_lo, vcc_lo, s88\n" \
  "v_readlane_b32 s88, v52, 32\n" \
  "s_max_i32 vcc_lo, vcc_lo, s88\n" \
  "v_readlane_b32 s88, v52, 48\n" \
  "s_max_i32 vcc_lo, vcc_lo, s88\n" \
  "s_add_u32 s66, s66, 1024\n" \
  "s_add_u32 vcc_lo, vcc_lo, s39\n" \
  "s_add_u32 vcc_lo, vcc_lo, 23\n" \
  ".LCA_TW_%=:\n" \
  "s_memrealtime s[38:39]\n" \
  "s_waitcnt lgkmcnt(0)\n" \
  "s_sub_u32 s38, s38, vcc_lo\n" \
  "s_cmp_lt_i32 s38, 0\n" \
  "s_cbranch_scc1 .LCA_TW_%=\n" \
  "buffer_load_dwordx4 v[24:27], v44, s[40:43], s89 offen sc1\n" \
  "buffer_load_dwordx4 v[28:31], v44, s[40:43], s89 offen offset:2048 sc1\n" \
  ".LCA_SEE_%=:\n" \
  "s_waitcnt vmcnt(1)\n" \
  "v_cvt_f32_i32 v24, v24\n" \
  "v_cvt_f32_i32 v25, v25\n" \
  "v_cvt_f32_i32 v26, v26\n" \
  "v_cvt_f32_i32 v27, v27\n" \
  "v_max_f32 v45, |v24|, |v26|\n" \
  "v_pk_mul_f32 v[24:25], v[24:25], s[80:81] op_sel_hi:[1,0]\n" \
  "v_pk_mul_f32 v[26:27], v[26:27], s[80:81] op_sel_hi:[1,0]\n" \
  "s_nop 0\n" \
  "v_cvt_pk_bf16_f32 v32, v24, v26\n" \
  "v_cvt_pk_bf16_f32 v33, v25, v27\n" \
  "ds_write_b32 v40, v32\n" \
  "ds_write_b32 v41, v33\n" \
  "s_waitcnt vmcnt(0)\n" \
  "v_cvt_f32_i32 v28, v28\n" \
  "v_cvt_f32_i32 v30, v30\n" \
  "v_cvt_f32_i32 v29, v29\n" \
  "v_max3_f32 v45, v45, |v28|, |v30|\n" \
  "v_cvt_f32_i32 v31, v31\n" \
  "v_cmp_lt_f32 vcc, s91, v45\n" \
  "v_pk_mul_f32 v[28:29], v[28:29], s[80:81] op_sel_hi:[1,0]\n" \
  "v_pk_mul_f32 v[30:31], v[30:31], s[80:81] op_sel_hi:[1,0]\n" \
  "s_cmp_eq_u64 vcc, 0\n" \
  "s_cbranch_scc0 .LCA_POLL_%=\n" \
  "v_cvt_pk_bf16_f32 v36, v28, v30\n" \
  "v_cvt_pk_bf16_f32 v37, v29, v31\n" \
  "ds_write_b32 v42, v36\n" \
  "ds_write_b32 v43, v37\n" \
  "v_dot2c_f32_bf16 v11, v32, v32\n" \
  "v_dot2c_f32_bf16 v11, v33, v33\n" \
  "v_dot2c_f32_bf16 v11, v36, v36\n" \
  "v_dot2c_f32_bf16 v11, v37, v37\n" \
  "s_nop 1\n" \
  "s_nop 0\n" \
  "v_cmp_lt_f32 vcc, 4.0, v11\n" \
  "s_cbranch_vccz .LCA_TOP_%=\n" \
  "s_mov_b64 exec, vcc\n" \
  "ds_max_u32 v59, v11\n" \
  "s_mov_b64 exec, -1\n" \
  "s_branch .LCA_TOP_%=\n" \
  ".LCA_POLL_%=:\n" \
  "buffer_load_dwordx4 v[24:27], v44, s[40:43], s89 offen sc1\n" \
  "buffer_load_dwordx4 v[28:31], v44, s[40:43], s89 offen offset:2048 sc1\n" \
  "s_sub_u32 s87, s87, 1\n" \
  "s_cbranch_scc1 .LCA_END_%=\n" \
  "s_branch .LCA_SEE_%=\n" \
  ".LCA_LAST_%=:\n" \
  ".LCA_END_%=:\n" \
  "s_endpgm\n"
#define CHAIN_ASM_VO \
  "s_mov_b32 s95, 272\n" \
  "s_mov_b32 s91, 0x4c7c2000\n" \
  "s_mov_b32 s85, 0xc9b26c9b\n" \
  "s_mov_b32 s92, 0xc9326c9b\n" \
  "s_mov_b32 s93, 0xc9326c9b\n" \
  "s_mov_b32 s78, 0x3e0293ee\n" \
  "s_mov_b32 s76, 0x33333333\n" \
  "s_mov_b32 s77, 0x33333333\n" \
  "s_lshl_b32 s98, s73, 10\n" \
  "s_add_u32 s98, s98, s74\n" \
  "s_add_u32 s98, s98, 131072\n" \
  "s_lshl_b32 s97, s73, 9\n" \
  "s_add_u32 s97, s97, s74\n" \
  "s_add_u32 s97, s97, 158724\n" \
  "s_lshl_b32 s99, s73, 11\n" \
  "s_add_u32 s99, s99, s74\n" \
  "s_add_u32 s99, s99, 133120\n" \
  "s_lshl_b32 s94, s73, 4\n" \
  "s_add_u32 s94, s94, s74\n" \
  "s_add_u32 s94, s94, 157760\n" \
  "v_mbcnt_lo_u32_b32 v48, -1, 0\n" \
  "v_mbcnt_hi_u32_b32 v48, -1, v48\n" \
  "s_add_u32 s88, s74, 157760\n" \
  "v_mov_b32 v59, s88\n" \
  "s_sub_u32 s88, s73, 4\n" \
  "v_lshrrev_b32 v49, 2, v48\n" \
  "v_lshrrev_b32 v50, 4, v48\n" \
  "v_bitop3_b32 v50, v49, v50, 3 bitop3:0x28\n" \
  "v_lshl_add_u32 v4, v50, 3, s97\n" \
  "v_and_b32 v50, 3, v48\n" \
  "v_lshl_add_u32 v4, v50, 5, v4\n" \
  "v_and_b32 v49, 1, v49\n" \
  "v_lshl_add_u32 v4, v49, 8, v4\n" \
  "v_and_b32 v49, 8, v48\n" \
  "v_lshl_add_u32 v4, v49, 4, v4\n" \
  "v_and_b32 v51, 15, v48\n" \
  "v_bfe_u32 v49, v48, 4, 1\n" \
  "v_lshrrev_b32 v50, 5, v48\n" \
  "s_lshl_b32 s89, s88, 10\n" \
  "s_add_u32 s89, s89, s74\n" \
  "s_add_u32 s89, s89, 158720\n" \
  "v_lshrrev_b32 v9, 3, v51\n" \
  "v_mul_u32_u24 v6, 41, v9\n" \
  "v_xor_b32 v6, v6, v51\n" \
  "v_lshl_add_u32 v6, v6, 3, s89\n" \
  "v_lshl_add_u32 v6, v49, 6, v6\n" \
  "v_lshl_add_u32 v6, v50, 9, v6\n" \
  "v_xor_b32 v9, 0x90, v6\n" \
  "v_lshlrev_b32 v8, 4, v49\n" \
  "v_lshl_add_u32 v8, v51, 5, v8\n" \
  "v_lshl_add_u32 v8, v50, 3, v8\n" \
  "s_lshl_b32 s89, s88, 8\n" \
  "s_add_u32 s89, s89, s74\n" \
  "s_add_u32 s89, s89, 139264\n" \
  "v_and_b32 v49, 31, v48\n" \
  "v_lshl_add_u32 v5, v49, 3, s89\n" \
  "v_lshl_add_u32 v5, v50, 10, v5\n" \
  "v_lshrrev_b32 v49, 2, v51\n" \
  "v_and_b32 v50, 5, v51\n" \
  "v_and_or_b32 v49, v49, 2, v50\n" \
  "v_and_b32 v50, 48, v48\n" \
  "v_lshlrev_b32 v7, 3, v50\n" \
  "v_lshl_add_u32 v7, v49, 4, v7\n" \
  "v_bitop3_b32 v49, v51, 2, 2 bitop3:0x0c\n" \
  "v_lshl_add_u32 v7, v49, 29, v7\n" \
  "v_mov_b64 v[12:13], 0\n" \
  "v_mov_b64 v[14:15], 0\n" \
  "v_mov_b64 v[16:17], 0\n" \
  "v_mov_b64 v[18:19], 0\n" \
  "v_mov_b64 v[20:21], 0\n" \
  "v_mov_b64 v[22:23], 0\n" \
  "v_lshlrev_b32 v10, 4, v48\n" \
  "s_add_u32 s78, s74, 157952\n" \
  "v_lshl_add_u32 v52, v48, 2, v59\n" \
  "ds_write_b32 v52, v12 offset:192\n" \
  "s_movk_i32 s79, 105\n" \
  "s_movk_i32 s96, 25\n" \
  "s_mov_b32 s87, 0x40000\n" \
  "v_subrev_u32 v30, 64, v59\n" \
  "v_add_u32 v17, 0x10000, v28\n" \
  "v_xor_b32 v18, 128, v28\n" \
  "v_xor_b32 v19, 128, v17\n" \
  "ds_write_b32 v30, v12 offset:0\n" \
  "ds_write_b32 v30, v12 offset:32\n" \
  "ds_write_b32 v30, v12 offset:64\n" \
  "v_xor_b32 v20, 0, v29\n" \
  "v_xor_b32 v21, 32, v29\n" \
  "v_xor_b32 v22, 64, v29\n" \
  "v_xor_b32 v23, 96, v29\n" \
  "s_waitcnt vmcnt(12) lgkmcnt(0)\n" \
  "s_barrier\n" \
  "ds_read_b64_tr_b16 v[96:97], v20 offset:0\n" \
  "ds_read_b64_tr_b16 v[98:99], v20 offset:2048\n" \
  "ds_read_b64_tr_b16 v[104:105], v21 offset:0\n" \
  "ds_read_b64_tr_b16 v[106:107], v21 offset:2048\n" \
  "ds_read_b64_tr_b16 v[112:113], v22 offset:0\n" \
  "ds_read_b64_tr_b16 v[114:115], v22 offset:2048\n" \
  "ds_read_b64_tr_b16 v[120:121], v23 offset:0\n" \
  "ds_read_b64_tr_b16 v[122:123], v23 offset:2048\n" \
  "s_lshl_b32 s88, s73, 10\n" \
  "v_add_u32 v48, s88, v1\n" \
  "ds_read_b128 v[52:55], v48 offset:0\n" \
  "s_waitcnt lgkmcnt(0)\n" \
  "v_mov_b32 v11, 0\n" \
  "v_dot2c_f32_bf16 v11, v52, v52\n" \
  "v_dot2c_f32_bf16 v11, v53, v53\n" \
  "v_dot2c_f32_bf16 v11, v54, v54\n" \
  "v_dot2c_f32_bf16 v11, v55, v55\n" \
  "s_nop 0\n" \
  "s_nop 1\n" \
  "v_max_u32_dpp v11, v11, v11 row_ror:1 row_mask:0xf bank_mask:0xf\n" \
  "s_nop 1\n" \
  "v_max_u32_dpp v11, v11, v11 row_ror:2 row_mask:0xf bank_mask:0xf\n" \
  "s_nop 1\n" \
  "v_max_u32_dpp v11, v11, v11 row_ror:4 row_mask:0xf bank_mask:0xf\n" \
  "s_nop 1\n" \
  "v_max_u32_dpp v11, v11, v11 row_ror:8 row_mask:0xf bank_mask:0xf\n" \
  "v_mov_b32 v31, v11\n" \
  "s_nop 1\n" \
  "v_permlane16_swap_b32 v11, v31\n" \
  "v_max_u32 v11, v11, v31\n" \
  "v_mov_b32 v31, v11\n" \
  "s_nop 1\n" \
  "v_permlane32_swap_b32 v11, v31\n" \
  "v_max_u32 v11, v11, v31\n" \
  "s_mov_b64 exec, 1\n" \
  "ds_max_u32 v59, v11\n" \
  "s_mov_b64 exec, -1\n" \
  "s_waitcnt vmcnt(8) lgkmcnt(0)\n" \
  "s_barrier\n" \
  "ds_read_b64_tr_b16 v[100:101], v20 offset:4096\n" \
  "ds_read_b64_tr_b16 v[102:103], v20 offset:6144\n" \
  "ds_read_b64_tr_b16 v[108:109], v21 offset:4096\n" \
  "ds_read_b64_tr_b16 v[110:111], v21 offset:6144\n" \
  "ds_read_b64_tr_b16 v[116:117], v22 offset:4096\n" \
  "ds_read_b64_tr_b16 v[118:119], v22 offset:6144\n" \
  "ds_read_b64_tr_b16 v[124:125], v23 offset:4096\n" \
  "ds_read_b64_tr_b16 v[126:127], v23 offset:6144\n" \
  "s_add_u32 m0, s19, 0\n" \
  "s_nop 0\n" \
  "global_load_lds_dwordx4 v24, s[4:5] offset:0\n" \
  "global_load_lds_dwordx4 v25, s[4:5] offset:1024\n" \
  "global_load_lds_dwordx4 v26, s[4:5] offset:2048\n" \
  "global_load_lds_dwordx4 v27, s[4:5] offset:3072\n" \
  "s_add_u32 s4, s4, 0x100000\n" \
  "s_addc_u32 s5, s5, 0\n" \
  "v_mfma_f32_16x16x32_bf16 v[64:67], v[96:99], v[96:99], 0\n" \
  "v_mfma_f32_16x16x32_bf16 v[68:71], v[104:107], v[104:107], 0\n" \
  "v_mfma_f32_16x16x32_bf16 v[72:75], v[112:115], v[112:115], 0\n" \
  "v_mfma_f32_16x16x32_bf16 v[76:79], v[120:123], v[120:123], 0\n" \
  "s_waitcnt vmcnt(8) lgkmcnt(0)\n" \
  "s_barrier\n" \
  "ds_read_b64_tr_b16 v[32:33], v20 offset:8192\n" \
  "ds_read_b64_tr_b16 v[34:35], v20 offset:10240\n" \
  "ds_read_b64_tr_b16 v[40:41], v21 offset:8192\n" \
  "ds_read_b64_tr_b16 v[42:43], v21 offset:10240\n" \
  "ds_read_b64_tr_b16 v[48:49], v22 offset:8192\n" \
  "ds_read_b64_tr_b16 v[50:51], v22 offset:10240\n" \
  "ds_read_b64_tr_b16 v[56:57], v23 offset:8192\n" \
  "ds_read_b64_tr_b16 v[58:59], v23 offset:10240\n" \
  "s_add_u32 m0, s19, 4096\n" \
  "s_nop 0\n" \
  "global_load_lds_dwordx4 v24, s[4:5] offset:0\n" \
  "global_load_lds_dwordx4 v25, s[4:5] offset:1024\n" \
  "global_load_lds_dwordx4 v26, s[4:5] offset:2048\n" \
  "global_load_lds_dwordx4 v27, s[4:5] offset:3072\n" \
  "s_add_u32 s4, s4, 0x100000\n" \
  "s_addc_u32 s5, s5, 0\n" \
  "v_mfma_f32_16x16x32_bf16 v[64:67], v[100:103], v[100:103], v[64:67]\n" \
  "v_mfma_f32_16x16x32_bf16 v[68:71], v[108:111], v[108:111], v[68:71]\n" \
  "v_mfma_f32_16x16x32_bf16 v[72:75], v[116:119], v[116:119], v[72:75]\n" \
  "v_mfma_f32_16x16x32_bf16 v[76:79], v[124:127], v[124:127], v[76:79]\n" \
  "s_waitcnt vmcnt(8) lgkmcnt(0)\n" \
  "s_barrier\n" \
  "ds_read_b64_tr_b16 v[36:37], v20 offset:12288\n" \
  "ds_read_b64_tr_b16 v[38:39], v20 offset:14336\n" \
  "ds_read_b64_tr_b16 v[44:45], v21 offset:12288\n" \
  "ds_read_b64_tr_b16 v[46:47], v21 offset:14336\n" \
  "ds_read_b64_tr_b16 v[52:53], v22 offset:12288\n" \
  "ds_read_b64_tr_b16 v[54:55], v22 offset:14336\n" \
  "ds_read_b64_tr_b16 v[60:61], v23 offset:12288\n" \
  "ds_read_b64_tr_b16 v[62:63], v23 offset:14336\n" \
  "s_add_u32 m0, s19, 8192\n" \
  "s_nop 0\n" \
  "global_load_lds_dwordx4 v24, s[4:5] offset:0\n" \
  "global_load_lds_dwordx4 v25, s[4:5] offset:1024\n" \
  "global_load_lds_dwordx4 v26, s[4:5] offset:2048\n" \
  "global_load_lds_dwordx4 v27, s[4:5] offset:3072\n" \
  "s_add_u32 s4, s4, 0x100000\n" \
  "s_addc_u32 s5, s5, 0\n" \
  "v_mfma_f32_16x16x32_bf16 v[64:67], v[32:35], v[32:35], v[64:67]\n" \
  "v_mfma_f32_16x16x32_bf16 v[68:71], v[40:43], v[40:43], v[68:71]\n" \
  "v_mfma_f32_16x16x32_bf16 v[72:75], v[48:51], v[48:51], v[72:75]\n" \
  "v_mfma_f32_16x16x32_bf16 v[76:79], v[56:59], v[56:59], v[76:79]\n" \
  "s_waitcnt vmcnt(8) lgkmcnt(0)\n" \
  "s_barrier\n" \
  "ds_read_b64_tr_b16 v[128:129], v18 offset:0\n" \
  "ds_read_b64_tr_b16 v[130:131], v18 offset:512\n" \
  "ds_read_b64_tr_b16 v[132:133], v18 offset:32768\n" \
  "ds_read_b64_tr_b16 v[134:135], v18 offset:33280\n" \
  "ds_read_b64_tr_b16 v[136:137], v19 offset:0\n" \
  "ds_read_b64_tr_b16 v[138:139], v19 offset:512\n" \
  "ds_read_b64_tr_b16 v[140:141], v19 offset:32768\n" \
  "ds_read_b64_tr_b16 v[142:143], v19 offset:33280\n" \
  "ds_read_b64_tr_b16 v[192:193], v28 offset:0\n" \
  "ds_read_b64_tr_b16 v[194:195], v28 offset:512\n" \
  "ds_read_b64_tr_b16 v[196:197], v28 offset:32768\n" \
  "ds_read_b64_tr_b16 v[198:199], v28 offset:33280\n" \
  "ds_read_b64_tr_b16 v[200:201], v17 offset:0\n" \
  "ds_read_b64_tr_b16 v[202:203], v17 offset:512\n" \
  "ds_read_b64_tr_b16 v[204:205], v17 offset:32768\n" \
  "ds_read_b64_tr_b16 v[206:207], v17 offset:33280\n" \
  "s_add_u32 m0, s19, 12288\n" \
  "s_nop 0\n" \
  "global_load_lds_dwordx4 v24, s[4:5] offset:0\n" \
  "global_load_lds_dwordx4 v25, s[4:5] offset:1024\n" \
  "global_load_lds_dwordx4 v26, s[4:5] offset:2048\n" \
  "global_load_lds_dwordx4 v27, s[4:5] offset:3072\n" \
  "s_add_u32 s4, s4, 0x100000\n" \
  "s_addc_u32 s5, s5, 0\n" \
  "v_mfma_f32_16x16x32_bf16 v[64:67], v[36:39], v[36:39], v[64:67]\n" \
  "v_mfma_f32_16x16x32_bf16 v[68:71], v[44:47], v[44:47], v[68:71]\n" \
  "v_mfma_f32_16x16x32_bf16 v[72:75], v[52:55], v[52:55], v[72:75]\n" \
  "v_mfma_f32_16x16x32_bf16 v[76:79], v[60:63], v[60:63], v[76:79]\n" \
  "s_nop 7\n" \
  "v_max3_f32 v13, v64, v65, v66\n" \
  "v_max3_f32 v13, v13, v67, v68\n" \
  "v_max3_f32 v13, v13, v69, v70\n" \
  "v_max3_f32 v13, v13, v71, v72\n" \
  "v_max3_f32 v13, v13, v73, v74\n" \
  "v_max3_f32 v13, v13, v75, v76\n" \
  "v_max3_f32 v13, v13, v77, v78\n" \
  "v_max_f32 v13, v13, v79\n" \
  "s_nop 1\n" \
  "v_max_f32_dpp v13, v13, v13 row_ror:1 row_mask:0xf bank_mask:0xf\n" \
  "s_nop 1\n" \
  "v_max_f32_dpp v13, v13, v13 row_ror:2 row_mask:0xf bank_mask:0xf\n" \
  "s_nop 1\n" \
  "v_max_f32_dpp v13, v13, v13 row_ror:4 row_mask:0xf bank_mask:0xf\n" \
  "s_nop 1\n" \
  "v_max_f32_dpp v13, v13, v13 row_ror:8 row_mask:0xf bank_mask:0xf\n" \
  "v_mov_b32 v31, v13\n" \
  "s_nop 1\n" \
  "v_permlane16_swap_b32 v13, v31\n" \
  "v_max_f32 v13, v13, v31\n" \
  "v_mov_b32 v31, v13\n" \
  "s_nop 1\n" \
  "v_permlane32_swap_b32 v13, v31\n" \
  "v_max_f32 v13, v13, v31\n" \
  "s_mov_b64 exec, 1\n" \
  "ds_max_u32 v30, v13 offset:32\n" \
  "s_mov_b64 exec, -1\n" \
  "s_waitcnt vmcnt(8) lgkmcnt(0)\n" \
  "s_barrier\n" \
  "ds_read_b64_tr_b16 v[144:145], v18 offset:4096\n" \
  "ds_read_b64_tr_b16 v[146:147], v18 offset:4608\n" \
  "ds_read_b64_tr_b16 v[148:149], v18 offset:36864\n" \
  "ds_read_b64_tr_b16 v[150:151], v18 offset:37376\n" \
  "ds_read_b64_tr_b16 v[152:153], v19 offset:4096\n" \
  "ds_read_b64_tr_b16 v[154:155], v19 offset:4608\n" \
  "ds_read_b64_tr_b16 v[156:157], v19 offset:36864\n" \
  "ds_read_b64_tr_b16 v[158:159], v19 offset:37376\n" \
  "ds_read_b64_tr_b16 v[208:209], v28 offset:4096\n" \
  "ds_read_b64_tr_b16 v[210:211], v28 offset:4608\n" \
  "ds_read_b64_tr_b16 v[212:213], v28 offset:36864\n" \
  "ds_read_b64_tr_b16 v[214:215], v28 offset:37376\n" \
  "ds_read_b64_tr_b16 v[216:217], v17 offset:4096\n" \
  "ds_read_b64_tr_b16 v[218:219], v17 offset:4608\n" \
  "ds_read_b64_tr_b16 v[220:221], v17 offset:36864\n" \
  "ds_read_b64_tr_b16 v[222:223], v17 offset:37376\n" \
  "s_add_u32 m0, s19, 0\n" \
  "s_nop 0\n" \
  "global_load_lds_dwordx4 v24, s[6:7] offset:0\n" \
  "global_load_lds_dwordx4 v25, s[6:7] offset:1024\n" \
  "global_load_lds_dwordx4 v26, s[6:7] offset:2048\n" \
  "global_load_lds_dwordx4 v27, s[6:7] offset:3072\n" \
  "s_add_u32 s6, s6, 0x100000\n" \
  "s_addc_u32 s7, s7, 0\n" \
  "v_mfma_f32_16x16x32_bf16 v[64:67], v[128:131], v[128:131], 0\n" \
  "v_mfma_f32_16x16x32_bf16 v[68:71], v[192:195], v[192:195], 0\n" \
  "v_mfma_f32_16x16x32_bf16 v[64:67], v[132:135], v[132:135], v[64:67]\n" \
  "v_mfma_f32_16x16x32_bf16 v[68:71], v[196:199], v[196:199], v[68:71]\n" \
  "v_mfma_f32_16x16x32_bf16 v[64:67], v[136:139], v[136:139], v[64:67]\n" \
  "v_mfma_f32_16x16x32_bf16 v[68:71], v[200:203], v[200:203], v[68:71]\n" \
  "v_mfma_f32_16x16x32_bf16 v[64:67], v[140:143], v[140:143], v[64:67]\n" \
  "v_mfma_f32_16x16x32_bf16 v[68:71], v[204:207], v[204:207], v[68:71]\n" \
  "s_waitcnt vmcnt(8) lgkmcnt(0)\n" \
  "s_barrier\n" \
  "ds_read_b64_tr_b16 v[160:161], v18 offset:8192\n" \
  "ds_read_b64_tr_b16 v[162:163], v18 offset:8704\n" \
  "ds_read_b64_tr_b16 v[164:165], v18 offset:40960\n" \
  "ds_read_b64_tr_b16 v[166:167], v18 offset:41472\n" \
  "ds_read_b64_tr_b16 v[168:169], v19 offset:8192\n" \
  "ds_read_b64_tr_b16 v[170:171], v19 offset:8704\n" \
  "ds_read_b64_tr_b16 v[172:173], v19 offset:40960\n" \
  "ds_read_b64_tr_b16 v[174:175], v19 offset:41472\n" \
  "ds_read_b64_tr_b16 v[224:225], v28 offset:8192\n" \
  "ds_read_b64_tr_b16 v[226:227], v28 offset:8704\n" \
  "ds_read_b64_tr_b16 v[228:229], v28 offset:40960\n" \
  "ds_read_b64_tr_b16 v[230:231], v28 offset:41472\n" \
  "ds_read_b64_tr_b16 v[232:233], v17 offset:8192\n" \
  "ds_read_b64_tr_b16 v[234:235], v17 offset:8704\n" \
  "ds_read_b64_tr_b16 v[236:237], v17 offset:40960\n" \
  "ds_read_b64_tr_b16 v[238:239], v17 offset:41472\n" \
  "s_add_u32 m0, s19, 4096\n" \
  "s_nop 0\n" \
  "global_load_lds_dwordx4 v24, s[6:7] offset:0\n" \
  "global_load_lds_dwordx4 v25, s[6:7] offset:1024\n" \
  "global_load_lds_dwordx4 v26, s[6:7] offset:2048\n" \
  "global_load_lds_dwordx4 v27, s[6:7] offset:3072\n" \
  "s_add_u32 s6, s6, 0x100000\n" \
  "s_addc_u32 s7, s7, 0\n" \
  "v_mfma_f32_16x16x32_bf16 v[64:67], v[144:147], v[144:147], v[64:67]\n" \
  "v_mfma_f32_16x16x32_bf16 v[68:71], v[208:211], v[208:211], v[68:71]\n" \
  "v_mfma_f32_16x16x32_bf16 v[64:67], v[148:151], v[148:151], v[64:67]\n" \
  "v_mfma_f32_16x16x32_bf16 v[68:71], v[212:215], v[212:215], v[68:71]\n" \
  "v_mfma_f32_16x16x32_bf16 v[64:67], v[152:155], v[152:155], v[64:67]\n" \
  "v_mfma_f32_16x16x32_bf16 v[68:71], v[216:219], v[216:219], v[68:71]\n" \
  "v_mfma_f32_16x16x32_bf16 v[64:67], v[156:159], v[156:159], v[64:67]\n" \
  "v_mfma_f32_16x16x32_bf16 v[68:71], v[220:223], v[220:223], v[68:71]\n" \
  "s_waitcnt vmcnt(8) lgkmcnt(0)\n" \
  "s_barrier\n" \
  "ds_read_b64_tr_b16 v[176:177], v18 offset:12288\n" \
  "ds_read_b64_tr_b16 v[178:179], v18 offset:12800\n" \
  "ds_read_b64_tr_b16 v[180:181], v18 offset:45056\n" \
  "ds_read_b64_tr_b16 v[182:183], v18 offset:45568\n" \
  "ds_read_b64_tr_b16 v[184:185], v19 offset:12288\n" \
  "ds_read_b64_tr_b16 v[186:187], v19 offset:12800\n" \
  "ds_read_b64_tr_b16 v[188:189], v19 offset:45056\n" \
  "ds_read_b64_tr_b16 v[190:191], v19 offset:45568\n" \
  "ds_read_b64_tr_b16 v[240:241], v28 offset:12288\n" \
  "ds_read_b64_tr_b16 v[242:243], v28 offset:12800\n" \
  "ds_read_b64_tr_b16 v[244:245], v28 offset:45056\n" \
  "ds_read_b64_tr_b16 v[246:247], v28 offset:45568\n" \
  "ds_read_b64_tr_b16 v[248:249], v17 offset:12288\n" \
  "ds_read_b64_tr_b16 v[250:251], v17 offset:12800\n" \
  "ds_read_b64_tr_b16 v[252:253], v17 offset:45056\n" \
  "ds_read_b64_tr_b16 v[254:255], v17 offset:45568\n" \
  "s_add_u32 m0, s19, 8192\n" \
  "s_nop 0\n" \
  "global_load_lds_dwordx4 v24, s[6:7] offset:0\n" \
  "global_load_lds_dwordx4 v25, s[6:7] offset:1024\n" \
  "global_load_lds_dwordx4 v26, s[6:7] offset:2048\n" \
  "global_load_lds_dwordx4 v27, s[6:7] offset:3072\n" \
  "s_add_u32 s6, s6, 0x100000\n" \
  "s_addc_u32 s7, s7, 0\n" \
  "v_mfma_f32_16x16x32_bf16 v[64:67], v[160:163], v[160:163], v[64:67]\n" \
  "v_mfma_f32_16x16x32_bf16 v[68:71], v[224:227], v[224:227], v[68:71]\n" \
  "v_mfma_f32_16x16x32_bf16 v[64:67], v[164:167], v[164:167], v[64:67]\n" \
  "v_mfma_f32_16x16x32_bf16 v[68:71], v[228:231], v[228:231], v[68:71]\n" \
  "v_mfma_f32_16x16x32_bf16 v[64:67], v[168:171], v[168:171], v[64:67]\n" \
  "v_mfma_f32_16x16x32_bf16 v[68:71], v[232:235], v[232:235], v[68:71]\n" \
  "v_mfma_f32_16x16x32_bf16 v[64:67], v[172:175], v[172:175], v[64:67]\n" \
  "v_mfma_f32_16x16x32_bf16 v[68:71], v[236:239], v[236:239], v[68:71]\n" \
  "s_waitcnt vmcnt(8) lgkmcnt(0)\n" \
  "s_barrier\n" \
  "s_add_u32 m0, s19, 12288\n" \
  "s_nop 0\n" \
  "global_load_lds_dwordx4 v24, s[6:7] offset:0\n" \
  "global_load_lds_dwordx4 v25, s[6:7] offset:1024\n" \
  "global_load_lds_dwordx4 v26, s[6:7] offset:2048\n" \
  "global_load_lds_dwordx4 v27, s[6:7] offset:3072\n" \
  "s_add_u32 s6, s6, 0x100000\n" \
  "s_addc_u32 s7, s7, 0\n" \
  "v_mfma_f32_16x16x32_bf16 v[64:67], v[176:179], v[176:179], v[64:67]\n" \
  "v_mfma_f32_16x16x32_bf16 v[68:71], v[240:243], v[240:243], v[68:71]\n" \
  "v_mfma_f32_16x16x32_bf16 v[64:67], v[180:183], v[180:183], v[64:67]\n" \
  "v_mfma_f32_16x16x32_bf16 v[68:71], v[244:247], v[244:247], v[68:71]\n" \
  "v_mfma_f32_16x16x32_bf16 v[64:67], v[184:187], v[184:187], v[64:67]\n" \
  "v_mfma_f32_16x16x32_bf16 v[68:71], v[248:251], v[248:251], v[68:71]\n" \
  "v_mfma_f32_16x16x32_bf16 v[64:67], v[188:191], v[188:191], v[64:67]\n" \
  "v_mfma_f32_16x16x32_bf16 v[68:71], v[252:255], v[252:255], v[68:71]\n" \
  "s_nop 7\n" \
  "s_mov_b32 exec_lo, 0x100001\n" \
  "s_mov_b32 exec_hi, 0x10000100\n" \
  "v_add_f32 v12, v64, v68\n" \
  "s_mov_b32 exec_lo, 0x200002\n" \
  "s_mov_b32 exec_hi, 0x20000200\n" \
  "v_add_f32 v12, v65, v69\n" \
  "s_mov_b32 exec_lo, 0x400004\n" \
  "s_mov_b32 exec_hi, 0x40000400\n" \
  "v_add_f32 v12, v66, v70\n" \
  "s_mov_b32 exec_lo, 0x800008\n" \
  "s_mov_b32 exec_hi, 0x80000800\n" \
  "v_add_f32 v12, v67, v71\n" \
  "s_mov_b64 exec, -1\n" \
  "s_nop 1\n" \
  "v_add_f32_dpp v12, v12, v12 row_ror:1 row_mask:0xf bank_mask:0xf\n" \
  "s_nop 1\n" \
  "v_add_f32_dpp v12, v12, v12 row_ror:2 row_mask:0xf bank_mask:0xf\n" \
  "s_nop 1\n" \
  "v_add_f32_dpp v12, v12, v12 row_ror:4 row_mask:0xf bank_mask:0xf\n" \
  "s_nop 1\n" \
  "v_add_f32_dpp v12, v12, v12 row_ror:8 row_mask:0xf bank_mask:0xf\n" \
  "v_mov_b32 v31, v12\n" \
  "s_nop 1\n" \
  "v_permlane16_swap_b32 v12, v31\n" \
  "v_add_f32 v12, v12, v31\n" \
  "v_mov_b32 v31, v12\n" \
  "s_nop 1\n" \
  "v_permlane32_swap_b32 v12, v31\n" \
  "v_add_f32 v12, v12, v31\n" \
  "v_lshl_add_u32 v31, s73, 2, v30\n" \
  "s_mov_b64 exec, 1\n" \
  "ds_write_b32 v31, v12\n" \
  "s_mov_b64 exec, -1\n" \
  "s_waitcnt vmcnt(8) lgkmcnt(0)\n" \
  "s_barrier\n" \
  "s_add_u32 m0, s19, 0\n" \
  "s_nop 0\n" \
  "global_load_lds_dwordx4 v24, s[8:9] offset:0\n" \
  "global_load_lds_dwordx4 v25, s[8:9] offset:1024\n" \
  "global_load_lds_dwordx4 v26, s[8:9] offset:2048\n" \
  "global_load_lds_dwordx4 v27, s[8:9] offset:3072\n" \
  "s_add_u32 s8, s8, 0x100000\n" \
  "s_addc_u32 s9, s9, 0\n" \
  "ds_read_b128 v[20:23], v30 offset:16\n" \
  "ds_read_b32 v31, v30 offset:32\n" \
  "v_mbcnt_lo_u32_b32 v16, -1, 0\n" \
  "v_mbcnt_hi_u32_b32 v16, -1, v16\n" \
  "v_lshlrev_b32 v15, 8, v16\n" \
  "s_waitcnt lgkmcnt(0)\n" \
  "v_add_f32 v20, v20, v21\n" \
  "v_add_f32 v22, v22, v23\n" \
  "v_add_f32 v20, v20, v22\n" \
  "v_mul_f32 v31, v20, v31\n" \
  "v_mul_f32 v31, 0x3f8147ae, v31\n" \
  "v_mov_b32 v12, s10\n" \
  "v_mov_b32 v13, 0xffff\n" \
  "v_bfi_b32 v13, v13, s11, v31\n" \
  "s_mov_b32 exec_hi, 0\n" \
  "s_mov_b32 exec_lo, s18\n" \
  "global_store_dwordx2 v15, v[12:13], s[16:17] sc1\n" \
  "s_mov_b64 exec, -1\n" \
  "v_lshlrev_b32 v16, 3, v16\n" \
  "s_waitcnt vmcnt(8) lgkmcnt(0)\n" \
  "s_barrier\n" \
  "s_add_u32 m0, s19, 4096\n" \
  "s_nop 0\n" \
  "global_load_lds_dwordx4 v24, s[8:9] offset:0\n" \
  "global_load_lds_dwordx4 v25, s[8:9] offset:1024\n" \
  "global_load_lds_dwordx4 v26, s[8:9] offset:2048\n" \
  "global_load_lds_dwordx4 v27, s[8:9] offset:3072\n" \
  "s_add_u32 s8, s8, 0x100000\n" \
  "s_addc_u32 s9, s9, 0\n" \
  "global_load_dwordx2 v[20:21], v16, s[12:13] sc1\n" \
  "s_waitcnt vmcnt(9) lgkmcnt(0)\n" \
  "s_barrier\n" \
  "s_add_u32 m0, s19, 8192\n" \
  "s_nop 0\n" \
  "global_load_lds_dwordx4 v24, s[8:9] offset:0\n" \
  "global_load_lds_dwordx4 v25, s[8:9] offset:1024\n" \
  "global_load_lds_dwordx4 v26, s[8:9] offset:2048\n" \
  "global_load_lds_dwordx4 v27, s[8:9] offset:3072\n" \
  "s_add_u32 s8, s8, 0x100000\n" \
  "s_addc_u32 s9, s9, 0\n" \
  "s_waitcnt vmcnt(9) lgkmcnt(0)\n" \
  "s_barrier\n" \
  "ds_read_b64_tr_b16 v[64:65], v28 offset:0\n" \
  "ds_read_b64_tr_b16 v[66:67], v28 offset:512\n" \
  "ds_read_b64_tr_b16 v[68:69], v28 offset:32768\n" \
  "ds_read_b64_tr_b16 v[70:71], v28 offset:33280\n" \
  "ds_read_b64_tr_b16 v[72:73], v17 offset:0\n" \
  "ds_read_b64_tr_b16 v[74:75], v17 offset:512\n" \
  "ds_read_b64_tr_b16 v[76:77], v17 offset:32768\n" \
  "ds_read_b64_tr_b16 v[78:79], v17 offset:33280\n" \
  "s_add_u32 m0, s19, 12288\n" \
  "s_nop 0\n" \
  "global_load_lds_dwordx4 v24, s[8:9] offset:0\n" \
  "global_load_lds_dwordx4 v25, s[8:9] offset:1024\n" \
  "global_load_lds_dwordx4 v26, s[8:9] offset:2048\n" \
  "global_load_lds_dwordx4 v27, s[8:9] offset:3072\n" \
  "s_add_u32 s8, s8, 0x100000\n" \
  "s_addc_u32 s9, s9, 0\n" \
  "s_waitcnt vmcnt(8)\n" \
  ".LCV_RD0_%=:\n" \
  "v_cmp_ne_u32 vcc, s10, v20\n" \
  "v_xor_b32 v31, s11, v21\n" \
  "v_cmp_ne_u32 s[22:23], 0, v31\n" \
  "s_or_b64 vcc, vcc, s[22:23]\n" \
  "s_cbranch_scc0 .LCV_RD1_%=\n" \
  "s_sleep 8\n" \
  "global_load_dwordx2 v[20:21], v16, s[12:13] sc1\n" \
  "s_sub_u32 s87, s87, 1\n" \
  "s_cbranch_scc1 .LCV_END_%=\n" \
  "s_waitcnt vmcnt(0)\n" \
  "s_branch .LCV_RD0_%=\n" \
  ".LCV_RD1_%=:\n" \
  "s_waitcnt vmcnt(8) lgkmcnt(0)\n" \
  "s_barrier\n" \
  "ds_write_b128 v3, v[96:99] offset:0\n" \
  "ds_write_b128 v3, v[100:103] offset:1024\n" \
  "ds_write_b128 v3, v[104:107] offset:2048\n" \
  "ds_write_b128 v3, v[108:111] offset:3072\n" \
  "ds_read_b64_tr_b16 v[80:81], v28 offset:4096\n" \
  "ds_read_b64_tr_b16 v[82:83], v28 offset:4608\n" \
  "ds_read_b64_tr_b16 v[84:85], v28 offset:36864\n" \
  "ds_read_b64_tr_b16 v[86:87], v28 offset:37376\n" \
  "ds_read_b64_tr_b16 v[88:89], v17 offset:4096\n" \
  "ds_read_b64_tr_b16 v[90:91], v17 offset:4608\n" \
  "ds_read_b64_tr_b16 v[92:93], v17 offset:36864\n" \
  "ds_read_b64_tr_b16 v[94:95], v17 offset:37376\n" \
  "v_and_b32 v16, 0xff, v16\n" \
  "global_load_dwordx2 v[22:23], v16, s[14:15] sc1\n" \
  "s_mov_b64 exec, 15\n" \
  "buffer_load_dword v13, v2, s[44:47], s68 offen sc1\n" \
  "s_mov_b64 exec, -1\n" \
  "s_waitcnt vmcnt(6) lgkmcnt(0)\n" \
  "s_barrier\n" \
  "ds_write_b128 v3, v[112:115] offset:4096\n" \
  "ds_write_b128 v3, v[116:119] offset:5120\n" \
  "ds_write_b128 v3, v[120:123] offset:6144\n" \
  "ds_write_b128 v3, v[124:127] offset:7168\n" \
  "ds_read_b64_tr_b16 v[96:97], v28 offset:8192\n" \
  "ds_read_b64_tr_b16 v[98:99], v28 offset:8704\n" \
  "ds_read_b64_tr_b16 v[100:101], v28 offset:40960\n" \
  "ds_read_b64_tr_b16 v[102:103], v28 offset:41472\n" \
  "ds_read_b64_tr_b16 v[104:105], v17 offset:8192\n" \
  "ds_read_b64_tr_b16 v[106:107], v17 offset:8704\n" \
  "ds_read_b64_tr_b16 v[108:109], v17 offset:40960\n" \
  "ds_read_b64_tr_b16 v[110:111], v17 offset:41472\n" \
  "s_waitcnt vmcnt(0)\n" \
  "v_cmp_eq_u32 vcc, 0, v13\n" \
  "s_and_b32 s90, vcc_lo, 15\n" \
  "s_cbranch_scc1 .LCV_GL0_%=\n" \
  "s_mov_b32 m0, s99\n" \
  "s_add_u32 s88, s99, 8192\n" \
  "global_load_lds_dwordx4 v10, s[62:63] sc1\n" \
  "global_load_lds_dwordx4 v10, s[62:63] offset:1024 sc1\n" \
  "s_mov_b32 m0, s88\n" \
  "s_nop 0\n" \
  "global_load_lds_dwordx4 v10, s[64:65] sc1\n" \
  "global_load_lds_dwordx4 v10, s[64:65] offset:1024 sc1\n" \
  ".LCV_GL0_%=:\n" \
  "v_cmp_ne_u32 vcc, s10, v22\n" \
  "v_xor_b32 v31, s11, v23\n" \
  "v_and_b32 v31, 0xffff, v31\n" \
  "v_cmp_ne_u32 s[22:23], 0, v31\n" \
  "s_or_b64 vcc, vcc, s[22:23]\n" \
  "s_cbranch_scc0 .LCV_GL1_%=\n" \
  "s_sleep 12\n" \
  "global_load_dwordx2 v[22:23], v16, s[14:15] sc1\n" \
  "s_sub_u32 s87, s87, 1\n" \
  "s_cbranch_scc1 .LCV_END_%=\n" \
  "s_waitcnt vmcnt(0)\n" \
  "s_branch .LCV_GL0_%=\n" \
  ".LCV_GL1_%=:\n" \
  "v_and_b32 v31, 0xffff0000, v23\n" \
  "s_nop 1\n" \
  "v_max_u32_dpp v31, v31, v31 row_ror:1 row_mask:0xf bank_mask:0xf\n" \
  "s_nop 1\n" \
  "v_max_u32_dpp v31, v31, v31 row_ror:2 row_mask:0xf bank_mask:0xf\n" \
  "s_nop 1\n" \
  "v_max_u32_dpp v31, v31, v31 row_ror:4 row_mask:0xf bank_mask:0xf\n" \
  "s_nop 1\n" \
  "v_max_u32_dpp v31, v31, v31 row_ror:8 row_mask:0xf bank_mask:0xf\n" \
  "v_mov_b32 v12, v31\n" \
  "s_nop 1\n" \
  "v_permlane16_swap_b32 v31, v12\n" \
  "v_max_u32 v31, v31, v12\n" \
  "v_mov_b32 v12, v31\n" \
  "s_nop 1\n" \
  "v_permlane32_swap_b32 v31, v12\n" \
  "v_max_u32 v31, v31, v12\n" \
  "s_mov_b64 exec, 1\n" \
  "ds_write_b32 v30, v31\n" \
  "s_mov_b64 exec, -1\n" \
  "s_waitcnt vmcnt(4) lgkmcnt(0)\n" \
  "s_barrier\n" \
  "ds_write_b128 v3, v[32:35] offset:8192\n" \
  "ds_write_b128 v3, v[36:39] offset:9216\n" \
  "ds_write_b128 v3, v[40:43] offset:10240\n" \
  "ds_write_b128 v3, v[44:47] offset:11264\n" \
  "ds_read_b64_tr_b16 v[112:113], v28 offset:12288\n" \
  "ds_read_b64_tr_b16 v[114:115], v28 offset:12800\n" \
  "ds_read_b64_tr_b16 v[116:117], v28 offset:45056\n" \
  "ds_read_b64_tr_b16 v[118:119], v28 offset:45568\n" \
  "ds_read_b64_tr_b16 v[120:121], v17 offset:12288\n" \
  "ds_read_b64_tr_b16 v[122:123], v17 offset:12800\n" \
  "ds_read_b64_tr_b16 v[124:125], v17 offset:45056\n" \
  "ds_read_b64_tr_b16 v[126:127], v17 offset:45568\n" \
  "ds_read_b32 v31, v30 offset:0\n" \
  "s_waitcnt lgkmcnt(0)\n" \
  "s_barrier\n" \
  "ds_write_b128 v3, v[48:51] offset:12288\n" \
  "ds_write_b128 v3, v[52:55] offset:13312\n" \
  "ds_write_b128 v3, v[56:59] offset:14336\n" \
  "ds_write_b128 v3, v[60:63] offset:15360\n" \
  "v_mul_f32 v31, 0x3f8e353f, v31\n" \
  "v_mul_f32 v30, 0x44870000, v31\n" \
  "v_mov_b64 v[12:13], 0\n" \
  "v_mov_b64 v[14:15], 0\n" \
  "v_mov_b64 v[16:17], 0\n" \
  "v_mov_b64 v[18:19], 0\n" \
  "v_mov_b64 v[20:21], 0\n" \
  "v_mov_b64 v[22:23], 0\n" \
  "v_readfirstlane_b32 s72, v31\n" \
  "v_readfirstlane_b32 s86, v30\n" \
  "s_lshr_b32 s86, s86, 23\n" \
  "s_addk_i32 s86, 0xff83\n" \
  "s_ashr_i32 s86, s86, 1\n" \
  "s_max_i32 s86, s86, -20\n" \
  "s_min_i32 s86, s86, 100\n" \
  "s_mov_b32 s82, s86\n" \
  "s_lshr_b32 s83, s72, 23\n" \
  "s_sub_i32 s83, s83, 243\n" \
  "s_waitcnt lgkmcnt(0)\n" \
  "s_add_u32 s88, s74, 157760\n" \
  "v_mov_b32 v59, s88\n" \
  "s_cmp_lg_u32 s61, 0\n" \
  "s_movk_i32 s76, -5952\n" \
  "s_cmovk_i32 s76, 7104\n" \
  "s_movk_i32 s77, 90\n" \
  "s_cmovk_i32 s77, 90\n" \
  "s_cselect_b32 s72, -1, 57\n" \
  "s_cselect_b32 s75, 0, 63\n" \
  "s_cmp_eq_u32 s61, 0\n" \
  "s_cbranch_scc1 .LCV_W7_%=\n" \
  "s_movk_i32 s87, 100\n" \
  ".LCV_W5_%=:\n" \
  "buffer_load_dword v58, off, s[0:3], 0 offset:896 sc1\n" \
  "s_waitcnt vmcnt(0)\n" \
  "v_readfirstlane_b32 vcc_lo, v58\n" \
  ".LCV_W6_%=:\n" \
  "s_sleep 16\n" \
  "s_sub_u32 s87, s87, 1\n" \
  "s_cbranch_scc1 .LCV_W7_%=\n" \
  "s_cmp_eq_u32 vcc_lo, 0\n" \
  "s_cbranch_scc1 .LCV_W5_%=\n" \
  "s_memrealtime s[38:39]\n" \
  "s_waitcnt lgkmcnt(0)\n" \
  "s_sub_u32 s38, s38, vcc_lo\n" \
  "s_cmp_lt_i32 s38, 800\n" \
  "s_cbranch_scc1 .LCV_W6_%=\n" \
  "s_branch .LCV_FS_%=\n" \
  ".LCV_W7_%=:\n" \
  "s_mov_b32 s87, 0x40000\n" \
  "s_cmp_eq_u32 s90, 0\n" \
  "s_cbranch_scc1 .LCV_W1_%=\n" \
  "s_branch .LCV_W2_%=\n" \
  ".LCV_W0_%=:\n" \
  "s_sleep 2\n" \
  "s_cmp_eq_u32 s61, 0\n" \
  "s_cbranch_scc1 .LCV_W2_%=\n" \
  "s_sleep 8\n" \
  ".LCV_W2_%=:\n" \
  "s_mov_b64 exec, 15\n" \
  "buffer_load_dword v58, v2, s[44:47], s68 offen sc1\n" \
  "s_mov_b64 exec, -1\n" \
  "s_sub_u32 s87, s87, 1\n" \
  "s_cbranch_scc1 .LCV_END_%=\n" \
  "s_waitcnt vmcnt(0)\n" \
  "v_cmp_eq_u32 vcc, 0, v58\n" \
  "s_and_b32 vcc_lo, vcc_lo, 15\n" \
  "s_cbranch_scc1 .LCV_W0_%=\n" \
  "s_mov_b32 m0, s99\n" \
  "s_add_u32 s88, s99, 8192\n" \
  "global_load_lds_dwordx4 v10, s[62:63] sc1\n" \
  "global_load_lds_dwordx4 v10, s[62:63] offset:1024 sc1\n" \
  "s_mov_b32 m0, s88\n" \
  "s_nop 0\n" \
  "global_load_lds_dwordx4 v10, s[64:65] sc1\n" \
  "global_load_lds_dwordx4 v10, s[64:65] offset:1024 sc1\n" \
  ".LCV_W1_%=:\n" \
  "s_add_u32 s62, s62, 0x80000\n" \
  "s_addc_u32 s63, s63, 0\n" \
  "s_add_u32 s64, s64, 0x40000\n" \
  "s_addc_u32 s65, s65, 0\n" \
  "s_add_u32 s68, s68, 0x1000\n" \
  "s_cmp_eq_u32 s61, 0\n" \
  "s_cbranch_scc1 .LCV_W4_%=\n" \
  "s_add_u32 s38, s67, 2944\n" \
  "v_mov_b32 v58, s38\n" \
  "buffer_load_dword v58, v58, s[0:3], 0 offen sc1\n" \
  "s_waitcnt vmcnt(0)\n" \
  "v_readfirstlane_b32 vcc_lo, v58\n" \
  "s_movk_i32 s87, 200\n" \
  "s_cmp_eq_u32 vcc_lo, 0\n" \
  "s_cbranch_scc1 .LCV_W4_%=\n" \
  "s_add_u32 vcc_lo, vcc_lo, 590\n" \
  ".LCV_W3_%=:\n" \
  "s_sleep 2\n" \
  "s_memrealtime s[38:39]\n" \
  "s_sub_u32 s87, s87, 1\n" \
  "s_cbranch_scc1 .LCV_W4_%=\n" \
  "s_waitcnt lgkmcnt(0)\n" \
  "s_sub_u32 s38, s38, vcc_lo\n" \
  "s_cmp_lt_i32 s38, 0\n" \
  "s_cbranch_scc1 .LCV_W3_%=\n" \
  ".LCV_W4_%=:\n" \
  "s_waitcnt vmcnt(0)\n" \
  "s_cmp_eq_u32 s72, -1\n" \
  "s_cselect_b32 s90, 23, 1\n" \
  ".p2align 6\n" \
  ".LCV_TOP_%=:\n" \
  "s_waitcnt lgkmcnt(0)\n" \
  "s_barrier\n" \
  "s_setprio 3\n" \
  "ds_read_b128 v[24:27], v1 offset:0\n" \
  "ds_read_b128 v[28:31], v1 offset:1024\n" \
  "ds_read_b128 v[32:35], v1 offset:2048\n" \
  "ds_read_b128 v[36:39], v1 offset:3072\n" \
  "ds_read_b128 v[40:43], v1 offset:4096\n" \
  "ds_read_b128 v[44:47], v1 offset:5120\n" \
  "ds_read_b128 v[48:51], v1 offset:6144\n" \
  "ds_read_b128 v[52:55], v1 offset:7168\n" \
  "s_memrealtime s[38:39]\n" \
  "s_waitcnt lgkmcnt(7)\n" \
  "v_smfmac_f32_16x16x64_bf16 v[12:15], v[24:27], v[64:71], v0\n" \
  "s_waitcnt lgkmcnt(6)\n" \
  "v_smfmac_f32_16x16x64_bf16 v[12:15], v[28:31], v[72:79], v0\n" \
  "s_waitcnt lgkmcnt(5)\n" \
  "v_smfmac_f32_16x16x64_bf16 v[12:15], v[32:35], v[80:87], v0\n" \
  "s_waitcnt lgkmcnt(4)\n" \
  "v_smfmac_f32_16x16x64_bf16 v[12:15], v[36:39], v[88:95], v0\n" \
  "s_waitcnt lgkmcnt(3)\n" \
  "v_smfmac_f32_16x16x64_bf16 v[12:15], v[40:43], v[96:103], v0\n" \
  "s_waitcnt lgkmcnt(2)\n" \
  "v_smfmac_f32_16x16x64_bf16 v[12:15], v[44:47], v[104:111], v0\n" \
  "s_waitcnt lgkmcnt(1)\n" \
  "v_smfmac_f32_16x16x64_bf16 v[12:15], v[48:51], v[112:119], v0\n" \
  "s_waitcnt lgkmcnt(0)\n" \
  "v_smfmac_f32_16x16x64_bf16 v[12:15], v[52:55], v[120:127], v0\n" \
  "ds_read_b32 v11, v1 offset:26688\n" \
  "s_mov_b32 s61, s38\n" \
  "s_add_u32 s38, s38, s79\n" \
  "s_add_u32 s38, s38, s96\n" \
  "s_mov_b32 s96, 0\n" \
  "v_or_b32 v57, 1, s38\n" \
  "v_add_u32 v56, s67, v10\n" \
  "s_add_u32 s39, s67, s76\n" \
  "s_add_u32 s38, s38, s77\n" \
  "s_bitset1_b32 s38, 0\n" \
  "v_writelane_b32 v56, s39, 1\n" \
  "v_writelane_b32 v57, s38, 1\n" \
  "s_sub_u32 s39, s71, s72\n" \
  "s_cmp_lt_u32 s39, 7\n" \
  "s_cselect_b64 exec, 19, 3\n" \
  "buffer_store_dword v57, v56, s[0:3], 0 offen sc1\n" \
  "s_mov_b64 exec, -1\n" \
  "v_permlane32_swap_b32 v12, v14\n" \
  "v_permlane32_swap_b32 v13, v15\n" \
  "v_pk_add_f32 v[12:13], v[12:13], v[14:15]\n" \
  "s_nop 0\n" \
  "v_cvt_pk_bf16_f32 v12, v12, v13\n" \
  "ds_write_b32 v4, v12\n" \
  "s_setprio 2\n" \
  "v_mov_b64 v[12:13], s[92:93]\n" \
  "v_mov_b64 v[14:15], s[92:93]\n" \
  "s_waitcnt lgkmcnt(1)\n" \
  "v_readfirstlane_b32 s86, v11\n" \
  "s_lshr_b32 s86, s86, 23\n" \
  "s_add_i32 s86, s86, s83\n" \
  "s_ashr_i32 s86, s86, 1\n" \
  "s_max_i32 s86, s86, s82\n" \
  "s_min_i32 s86, s86, 100\n" \
  "s_lshl_b32 s86, s86, 23\n" \
  "s_sub_i32 s84, 0xc8800000, s86\n" \
  "s_cmp_eq_u32 s73, 5\n" \
  "v_smfmac_f32_16x16x64_bf16 v[16:19], v[24:27], v[128:135], v0\n" \
  "v_smfmac_f32_16x16x64_bf16 v[16:19], v[28:31], v[136:143], v0\n" \
  "v_smfmac_f32_16x16x64_bf16 v[16:19], v[32:35], v[144:151], v0\n" \
  "v_smfmac_f32_16x16x64_bf16 v[16:19], v[36:39], v[152:159], v0\n" \
  "v_smfmac_f32_16x16x64_bf16 v[16:19], v[40:43], v[160:167], v0\n" \
  "v_smfmac_f32_16x16x64_bf16 v[16:19], v[44:47], v[168:175], v0\n" \
  "s_waitcnt lgkmcnt(0)\n" \
  "s_barrier\n" \
  "ds_read_b64_tr_b16 v[60:61], v6 offset:0\n" \
  "ds_read_b64_tr_b16 v[62:63], v9 offset:0\n" \
  "v_smfmac_f32_16x16x64_bf16 v[16:19], v[48:51], v[176:183], v0\n" \
  "v_smfmac_f32_16x16x64_bf16 v[16:19], v[52:55], v[184:191], v0\n" \
  "v_smfmac_f32_16x16x64_bf16 v[20:23], v[24:27], v[192:199], v0\n" \
  "v_smfmac_f32_16x16x64_bf16 v[20:23], v[28:31], v[200:207], v0\n" \
  "v_smfmac_f32_16x16x64_bf16 v[20:23], v[32:35], v[208:215], v0\n" \
  "v_smfmac_f32_16x16x64_bf16 v[20:23], v[36:39], v[216:223], v0\n" \
  "v_smfmac_f32_16x16x64_bf16 v[20:23], v[40:43], v[224:231], v0\n" \
  "v_smfmac_f32_16x16x64_bf16 v[20:23], v[44:47], v[232:239], v0\n" \
  "v_smfmac_f32_16x16x64_bf16 v[20:23], v[48:51], v[240:247], v0\n" \
  "v_smfmac_f32_16x16x64_bf16 v[20:23], v[52:55], v[248:255], v0\n" \
  "s_cmp_eq_u32 s71, 0\n" \
  "v_permlane32_swap_b32 v16, v18\n" \
  "v_permlane32_swap_b32 v17, v19\n" \
  "v_add_f32 v16, v16, v18\n" \
  "v_add_f32 v17, v17, v19\n" \
  "v_cvt_pk_bf16_f32 v16, v16, v17\n" \
  "s_waitcnt lgkmcnt(0)\n" \
  "v_max_u32 v60, 1, v60\n" \
  "v_max_u32 v61, 1, v61\n" \
  "v_max_u32 v62, 1, v62\n" \
  "v_max_u32 v63, 1, v63\n" \
  "buffer_store_dwordx4 v[60:63], v7, s[48:51], s69 offen sc1\n" \
  "v_permlane32_swap_b32 v20, v22\n" \
  "v_permlane32_swap_b32 v21, v23\n" \
  "v_pk_add_f32 v[20:21], v[20:21], v[22:23]\n" \
  "v_max_u32 v18, 1, v16\n" \
  "v_cvt_pk_bf16_f32 v17, v20, v21\n" \
  "ds_write_b64 v5, v[16:17]\n" \
  "v_max_u32 v19, 1, v17\n" \
  "s_nop 1\n" \
  "v_permlane32_swap_b32 v18, v19\n" \
  "buffer_store_dwordx2 v[18:19], v8, s[52:55], s70 offen sc1\n" \
  "ds_read_b128 v[24:27], v3 offset:0\n" \
  "ds_read_b128 v[28:31], v3 offset:1024\n" \
  "ds_read_b128 v[32:35], v3 offset:2048\n" \
  "ds_read_b128 v[36:39], v3 offset:3072\n" \
  "ds_read_b128 v[40:43], v3 offset:8192\n" \
  "ds_read_b128 v[44:47], v3 offset:9216\n" \
  "ds_read_b128 v[48:51], v3 offset:10240\n" \
  "ds_read_b128 v[52:55], v3 offset:11264\n" \
  "s_cbranch_scc1 .LCV_LAST_%=\n" \
  "s_cmp_eq_u32 s73, 5\n" \
  "s_cbranch_scc0 .LCV_NMK_%=\n" \
  "s_mov_b32 m0, s78\n" \
  "s_nop 0\n" \
  "buffer_load_dword v2, s[0:3], s66 offen sc1 lds\n" \
  ".LCV_NMK_%=:\n" \
  "s_setprio 0\n" \
  "s_waitcnt lgkmcnt(8)\n" \
  "s_barrier\n" \
  "s_add_u32 s67, s67, 1024\n" \
  "v_mov_b32 v11, 0\n" \
  "ds_write_b32 v59, v11\n" \
  "ds_read_b128 v[56:59], v3 offset:4096\n" \
  "ds_read_b128 v[60:63], v3 offset:5120\n" \
  "v_mov_b64 v[20:21], s[92:93]\n" \
  "v_mov_b64 v[22:23], s[92:93]\n" \
  "v_mov_b64 v[16:17], s[92:93]\n" \
  "v_mov_b64 v[18:19], s[92:93]\n" \
  "s_mov_b32 m0, s94\n" \
  "s_cmp_gt_i32 s71, s75\n" \
  "s_cselect_b64 exec, 0, 15\n" \
  "buffer_load_dword v2, s[44:47], s68 offen sc1 lds\n" \
  "s_mov_b64 exec, -1\n" \
  "s_setprio 2\n" \
  "s_waitcnt lgkmcnt(0)\n" \
  "s_barrier\n" \
  "ds_read_b128 v[4:7], v1 offset:8192\n" \
  "ds_read_b128 v[8:11], v1 offset:9216\n" \
  "s_waitcnt lgkmcnt(1)\n" \
  "v_smfmac_f32_16x16x64_bf16 v[12:15], v[4:7], v[24:31], v0\n" \
  "ds_read_b128 v[24:27], v3 offset:6144\n" \
  "ds_read_b128 v[28:31], v3 offset:7168\n" \
  "v_smfmac_f32_16x16x64_bf16 v[16:19], v[4:7], v[32:39], v0\n" \
  "ds_read_b128 v[32:35], v3 offset:12288\n" \
  "ds_read_b128 v[36:39], v3 offset:13312\n" \
  "s_waitcnt lgkmcnt(4)\n" \
  "v_smfmac_f32_16x16x64_bf16 v[12:15], v[8:11], v[40:47], v0\n" \
  "v_smfmac_f32_16x16x64_bf16 v[16:19], v[8:11], v[48:55], v0\n" \
  "v_smfmac_f32_16x16x64_bf16 v[20:23], v[4:7], v[56:63], v0\n" \
  "ds_read_b128 v[56:59], v3 offset:14336\n" \
  "ds_read_b128 v[60:63], v3 offset:15360\n" \
  "v_mov_b64 v[40:41], s[92:93]\n" \
  "v_mov_b64 v[42:43], s[92:93]\n" \
  "s_nop 2\n" \
  "v_permlane32_swap_b32 v12, v17\n" \
  "v_permlane32_swap_b32 v13, v16\n" \
  "v_permlane32_swap_b32 v14, v19\n" \
  "v_permlane32_swap_b32 v15, v18\n" \
  "v_pk_add_f32 v[12:13], v[12:13], v[16:17] op_sel:[0,1] op_sel_hi:[1,0]\n" \
  "v_pk_add_f32 v[14:15], v[14:15], v[18:19] op_sel:[0,1] op_sel_hi:[1,0]\n" \
  "buffer_atomic_add_x2 v[12:13], v2, s[40:43], s56 offen\n" \
  "buffer_atomic_add_x2 v[14:15], v2, s[40:43], s57 offen\n" \
  "s_waitcnt lgkmcnt(2)\n" \
  "v_smfmac_f32_16x16x64_bf16 v[20:23], v[8:11], v[32:39], v0\n" \
  "v_smfmac_f32_16x16x64_bf16 v[40:43], v[4:7], v[24:31], v0\n" \
  "s_waitcnt lgkmcnt(0)\n" \
  "v_smfmac_f32_16x16x64_bf16 v[40:43], v[8:11], v[56:63], v0\n" \
  "s_nop 7\n" \
  "v_permlane32_swap_b32 v20, v41\n" \
  "v_permlane32_swap_b32 v21, v40\n" \
  "v_permlane32_swap_b32 v22, v43\n" \
  "v_permlane32_swap_b32 v23, v42\n" \
  "v_pk_add_f32 v[20:21], v[20:21], v[40:41] op_sel:[0,1] op_sel_hi:[1,0]\n" \
  "v_pk_add_f32 v[22:23], v[22:23], v[42:43] op_sel:[0,1] op_sel_hi:[1,0]\n" \
  "buffer_atomic_add_x2 v[20:21], v2, s[40:43], s58 offen\n" \
  "buffer_atomic_add_x2 v[22:23], v2, s[40:43], s59 offen\n" \
  "s_setprio 0\n" \
  "s_memrealtime s[38:39]\n" \
  "v_mbcnt_lo_u32_b32 v48, -1, 0\n" \
  "v_mbcnt_hi_u32_b32 v48, -1, v48\n" \
  "s_add_u32 s88, s74, 157760\n" \
  "v_mov_b32 v59, s88\n" \
  "v_lshl_add_u32 v52, v48, 2, v59\n" \
  "ds_read_b32 v52, v52 offset:192\n" \
  "v_lshrrev_b32 v49, 3, v48\n" \
  "v_and_b32 v50, 7, v48\n" \
  "v_lshlrev_b32 v50, 4, v50\n" \
  "v_lshl_or_b32 v44, v49, 8, v50\n" \
  "s_mov_b32 s87, 0x40000\n" \
  "v_lshlrev_b32 v10, 4, v48\n" \
  "s_cmp_gt_i32 s71, s75\n" \
  "s_cbranch_scc1 .LCV_GOT_%=\n" \
  "v_lshl_add_u32 v58, v48, 2, s94\n" \
  "s_waitcnt vmcnt(4)\n" \
  "ds_read_b32 v58, v58\n" \
  "s_waitcnt lgkmcnt(0)\n" \
  "v_cmp_eq_u32 vcc, 0, v58\n" \
  "s_and_b32 vcc_lo, vcc_lo, 15\n" \
  "s_cbranch_scc0 .LCV_GOT_%=\n" \
  "s_branch .LCV_ASK_%=\n" \
  ".LCV_LATE_%=:\n" \
  "v_cmp_eq_u32 vcc, 0, v58\n" \
  "s_and_b32 vcc_lo, vcc_lo, 15\n" \
  "s_cbranch_scc0 .LCV_GOT_%=\n" \
  "s_sleep 12\n" \
  ".LCV_ASK_%=:\n" \
  "s_mov_b64 exec, 15\n" \
  "buffer_load_dword v58, v2, s[44:47], s68 offen sc1\n" \
  "s_mov_b64 exec, -1\n" \
  "s_sub_u32 s87, s87, 1\n" \
  "s_cbranch_scc1 .LCV_END_%=\n" \
  "s_waitcnt vmcnt(0)\n" \
  "s_branch .LCV_LATE_%=\n" \
  ".LCV_GOT_%=:\n" \
  "s_mov_b32 m0, s99\n" \
  "s_add_u32 s88, s99, 8192\n" \
  "global_load_lds_dwordx4 v10, s[62:63] sc1\n" \
  "global_load_lds_dwordx4 v10, s[62:63] offset:1024 sc1\n" \
  "s_mov_b32 m0, s88\n" \
  "s_nop 0\n" \
  "global_load_lds_dwordx4 v10, s[64:65] sc1\n" \
  "global_load_lds_dwordx4 v10, s[64:65] offset:1024 sc1\n" \
  ".LCV_HAVE_%=:\n" \
  "s_waitcnt lgkmcnt(0)\n" \
  "s_sub_u32 s61, s38, s61\n" \
  "s_add_u32 s79, s79, 2\n" \
  "s_min_u32 s79, s79, s61\n" \
  "s_sub_u32 s39, s38, 150\n" \
  "v_subrev_u32 v52, s39, v52\n" \
  "v_cmp_lt_u32 vcc, 0x1000, v52\n" \
  "s_cmp_eq_u64 vcc, 0\n" \
  "s_cbranch_scc1 .LCV_OKM_%=\n" \
  ".LCV_AGN_%=:\n" \
  "buffer_load_dword v52, v2, s[0:3], s66 offen sc1\n" \
  "s_sub_u32 s87, s87, 1\n" \
  "s_cbranch_scc1 .LCV_END_%=\n" \
  "s_waitcnt vmcnt(0)\n" \
  "v_cmp_eq_u32 vcc, 0, v52\n" \
  "v_subrev_u32 v52, s39, v52\n" \
  "s_cmp_eq_u64 vcc, 0\n" \
  "s_cbranch_scc1 .LCV_OKM_%=\n" \
  "s_sleep 4\n" \
  "s_branch .LCV_AGN_%=\n" \
  ".LCV_OKM_%=:\n" \
  "s_nop 1\n" \
  "v_max_i32_dpp v52, v52, v52 row_ror:1 row_mask:0xf bank_mask:0xf\n" \
  "s_nop 1\n" \
  "v_max_i32_dpp v52, v52, v52 row_ror:2 row_mask:0xf bank_mask:0xf\n" \
  "s_nop 1\n" \
  "v_max_i32_dpp v52, v52, v52 row_ror:4 row_mask:0xf bank_mask:0xf\n" \
  "s_nop 1\n" \
  "v_max_i32_dpp v52, v52, v52 row_ror:8 row_mask:0xf bank_mask:0xf\n" \
  "s_movk_i32 vcc_lo, 150\n" \
  "v_readlane_b32 s88, v52, 0\n" \
  "s_max_i32 vcc_lo, vcc_lo, s88\n" \
  "v_readlane_b32 s88, v52, 16\n" \
  "s_max_i32 vcc_lo, vcc_lo, s88\n" \
  "v_readlane_b32 s88, v52, 32\n" \
  "s_max_i32 vcc_lo, vcc_lo, s88\n" \
  "v_readlane_b32 s88, v52, 48\n" \
  "s_max_i32 vcc_lo, vcc_lo, s88\n" \
  "s_add_u32 s66, s66, 1024\n" \
  "s_add_u32 vcc_lo, vcc_lo, s39\n" \
  "s_add_u32 vcc_lo, vcc_lo, 23\n" \
  ".LCV_TW_%=:\n" \
  "s_memrealtime s[38:39]\n" \
  "s_waitcnt lgkmcnt(0)\n" \
  "s_sub_u32 s38, s38, vcc_lo\n" \
  "s_cmp_lt_i32 s38, 0\n" \
  "s_cbranch_scc1 .LCV_TW_%=\n" \
  "buffer_load_dwordx4 v[24:27], v44, s[40:43], s60 offen sc1\n" \
  "buffer_load_dwordx4 v[28:31], v44, s[40:43], s60 offen offset:2048 sc1\n" \
  "s_add_u32 s69, s69, 0x10000\n" \
  "s_add_u32 s70, s70, 0x10000\n" \
  "s_sub_u32 s88, s73, 4\n" \
  "v_lshrrev_b32 v49, 2, v48\n" \
  "v_lshrrev_b32 v50, 4, v48\n" \
  "v_bitop3_b32 v50, v49, v50, 3 bitop3:0x28\n" \
  "v_lshl_add_u32 v4, v50, 3, s97\n" \
  "v_and_b32 v50, 3, v48\n" \
  "v_lshl_add_u32 v4, v50, 5, v4\n" \
  "v_and_b32 v49, 1, v49\n" \
  "v_lshl_add_u32 v4, v49, 8, v4\n" \
  "v_and_b32 v49, 8, v48\n" \
  "v_lshl_add_u32 v4, v49, 4, v4\n" \
  "v_and_b32 v51, 15, v48\n" \
  "v_bfe_u32 v49, v48, 4, 1\n" \
  "v_lshrrev_b32 v50, 5, v48\n" \
  "s_lshl_b32 s89, s88, 10\n" \
  "s_add_u32 s89, s89, s74\n" \
  "s_add_u32 s89, s89, 158720\n" \
  "v_lshrrev_b32 v9, 3, v51\n" \
  "v_mul_u32_u24 v6, 41, v9\n" \
  "v_xor_b32 v6, v6, v51\n" \
  "v_lshl_add_u32 v6, v6, 3, s89\n" \
  "v_lshl_add_u32 v6, v49, 6, v6\n" \
  "v_lshl_add_u32 v6, v50, 9, v6\n" \
  "v_xor_b32 v9, 0x90, v6\n" \
  "v_lshlrev_b32 v8, 4, v49\n" \
  "v_lshl_add_u32 v8, v51, 5, v8\n" \
  "v_lshl_add_u32 v8, v50, 3, v8\n" \
  "s_lshl_b32 s89, s88, 8\n" \
  "s_add_u32 s89, s89, s74\n" \
  "s_add_u32 s89, s89, 139264\n" \
  "v_and_b32 v49, 31, v48\n" \
  "v_lshl_add_u32 v5, v49, 3, s89\n" \
  "v_lshl_add_u32 v5, v50, 10, v5\n" \
  "v_lshrrev_b32 v49, 2, v51\n" \
  "v_and_b32 v50, 5, v51\n" \
  "v_and_or_b32 v49, v49, 2, v50\n" \
  "v_and_b32 v50, 48, v48\n" \
  "v_lshlrev_b32 v7, 3, v50\n" \
  "v_lshl_add_u32 v7, v49, 4, v7\n" \
  "v_bitop3_b32 v49, v51, 2, 2 bitop3:0x0c\n" \
  "v_lshl_add_u32 v7, v49, 29, v7\n" \
  "v_mov_b64 v[12:13], 0\n" \
  "v_mov_b64 v[14:15], 0\n" \
  "v_mov_b64 v[16:17], 0\n" \
  "v_mov_b64 v[18:19], 0\n" \
  "v_mov_b64 v[20:21], 0\n" \
  "v_mov_b64 v[22:23], 0\n" \
  "v_and_b32 v50, 3, v48\n" \
  "v_lshl_add_u32 v40, v50, 2, s98\n" \
  "v_lshrrev_b32 v50, 4, v48\n" \
  "v_mad_u32_u24 v40, v50, s95, v40\n" \
  "v_bfe_u32 v49, v48, 3, 1\n" \
  "v_lshl_add_u32 v40, v49, 6, v40\n" \
  "v_bfe_u32 v49, v48, 2, 1\n" \
  "v_lshl_add_u32 v40, v49, 7, v40\n" \
  "v_xor_b32 v41, 16, v40\n" \
  "v_xor_b32 v42, 32, v40\n" \
  "v_xor_b32 v43, 48, v40\n" \
  "s_add_i32 s80, s86, 0x35000000\n" \
  "v_mov_b32 v11, 0\n" \
  "s_mov_b32 s89, s60\n" \
  "s_bitcmp1_b32 s60, 7\n" \
  "s_cselect_b32 s88, 0x10000, 0\n" \
  "s_bitset1_b32 s88, 7\n" \
  "s_xor_b32 s56, s56, s88\n" \
  "s_xor_b32 s57, s57, s88\n" \
  "s_xor_b32 s58, s58, s88\n" \
  "s_xor_b32 s59, s59, s88\n" \
  "s_xor_b32 s60, s60, s88\n" \
  "s_sub_u32 s71, s71, 1\n" \
  "s_waitcnt vmcnt(2)\n" \
  "v_add_u32 v53, s99, v10\n" \
  "ds_read_b32 v54, v53 offset:0\n" \
  "ds_read_b32 v55, v53 offset:1024\n" \
  "ds_read_b32 v56, v53 offset:8192\n" \
  "ds_read_b32 v57, v53 offset:9216\n" \
  "s_lshr_b32 s88, s71, 4\n" \
  "s_cmp_ge_i32 s71, s75\n" \
  "s_cselect_b32 vcc_lo, 1, 0\n" \
  "s_waitcnt lgkmcnt(0)\n" \
  "v_bitop3_b32 v54, v54, v55, s88 bitop3:0x7e\n" \
  "v_bitop3_b32 v56, v56, v57, s88 bitop3:0x7e\n" \
  "v_bitop3_b32 v54, v54, v56, vcc_lo bitop3:0xa8\n" \
  "v_cmp_ne_u32 vcc, 0, v54\n" \
  "s_cmp_eq_u64 vcc, 0\n" \
  "s_cbranch_scc0 .LCV_MISS_%=\n" \
  ".LCV_M3_%=:\n" \
  "s_add_u32 s62, s62, 0x80000\n" \
  "s_addc_u32 s63, s63, 0\n" \
  "s_add_u32 s64, s64, 0x40000\n" \
  "s_addc_u32 s65, s65, 0\n" \
  "s_bitset0_b32 s62, s90\n" \
  "s_sub_u32 s88, s90, 1\n" \
  "s_bitset0_b32 s64, s88\n" \
  "s_add_u32 s68, s68, 0x1000\n" \
  "s_min_u32 s75, s75, 60\n" \
  ".LCV_SEE_%=:\n" \
  "s_waitcnt vmcnt(1)\n" \
  "v_cvt_f32_i32 v24, v24\n" \
  "v_cvt_f32_i32 v25, v25\n" \
  "v_cvt_f32_i32 v26, v26\n" \
  "v_cvt_f32_i32 v27, v27\n" \
  "v_max_f32 v45, |v24|, |v26|\n" \
  "v_pk_mul_f32 v[24:25], v[24:25], s[80:81] op_sel_hi:[1,0]\n" \
  "v_pk_mul_f32 v[26:27], v[26:27], s[80:81] op_sel_hi:[1,0]\n" \
  "s_nop 0\n" \
  "v_cvt_pk_bf16_f32 v32, v24, v26\n" \
  "v_cvt_pk_bf16_f32 v33, v25, v27\n" \
  "ds_write_b32 v40, v32\n" \
  "ds_write_b32 v41, v33\n" \
  "s_waitcnt vmcnt(0)\n" \
  "v_cvt_f32_i32 v28, v28\n" \
  "v_cvt_f32_i32 v30, v30\n" \
  "v_cvt_f32_i32 v29, v29\n" \
  "v_max3_f32 v45, v45, |v28|, |v30|\n" \
  "v_cvt_f32_i32 v31, v31\n" \
  "v_cmp_lt_f32 vcc, s91, v45\n" \
  "v_pk_mul_f32 v[28:29], v[28:29], s[80:81] op_sel_hi:[1,0]\n" \
  "v_pk_mul_f32 v[30:31], v[30:31], s[80:81] op_sel_hi:[1,0]\n" \
  "s_cmp_eq_u64 vcc, 0\n" \
  "s_cbranch_scc0 .LCV_POLL_%=\n" \
  "v_cvt_pk_bf16_f32 v36, v28, v30\n" \
  "v_cvt_pk_bf16_f32 v37, v29, v31\n" \
  "ds_write_b32 v42, v36\n" \
  "ds_write_b32 v43, v37\n" \
  "v_dot2c_f32_bf16 v11, v32, v32\n" \
  "v_dot2c_f32_bf16 v11, v33, v33\n" \
  "v_dot2c_f32_bf16 v11, v36, v36\n" \
  "v_dot2c_f32_bf16 v11, v37, v37\n" \
  "s_nop 1\n" \
  "s_nop 0\n" \
  "v_cmp_lt_f32 vcc, 4.0, v11\n" \
  "s_cbranch_vccz .LCV_TOP_%=\n" \
  "s_mov_b64 exec, vcc\n" \
  "ds_max_u32 v59, v11\n" \
  "s_mov_b64 exec, -1\n" \
  "s_branch .LCV_TOP_%=\n" \
  ".LCV_POLL_%=:\n" \
  "buffer_load_dwordx4 v[24:27], v44, s[40:43], s89 offen sc1\n" \
  "buffer_load_dwordx4 v[28:31], v44, s[40:43], s89 offen offset:2048 sc1\n" \
  "s_sub_u32 s87, s87, 1\n" \
  "s_cbranch_scc1 .LCV_END_%=\n" \
  "s_branch .LCV_SEE_%=\n" \
  ".LCV_MISS_%=:\n" \
  "s_mov_b32 m0, s99\n" \
  "s_add_u32 s88, s99, 8192\n" \
  "global_load_lds_dwordx4 v10, s[62:63] sc1\n" \
  "global_load_lds_dwordx4 v10, s[62:63] offset:1024 sc1\n" \
  "s_mov_b32 m0, s88\n" \
  "s_nop 0\n" \
  "global_load_lds_dwordx4 v10, s[64:65] sc1\n" \
  "global_load_lds_dwordx4 v10, s[64:65] offset:1024 sc1\n" \
  "s_waitcnt vmcnt(0)\n" \
  "v_add_u32 v53, s99, v10\n" \
  "ds_read_b32 v54, v53 offset:0\n" \
  "ds_read_b32 v55, v53 offset:1024\n" \
  "ds_read_b32 v56, v53 offset:8192\n" \
  "ds_read_b32 v57, v53 offset:9216\n" \
  "s_lshr_b32 s88, s71, 4\n" \
  "s_cmp_ge_i32 s71, s75\n" \
  "s_cselect_b32 vcc_lo, 1, 0\n" \
  "s_waitcnt lgkmcnt(0)\n" \
  "v_bitop3_b32 v54, v54, v55, s88 bitop3:0x7e\n" \
  "v_bitop3_b32 v56, v56, v57, s88 bitop3:0x7e\n" \
  "v_bitop3_b32 v54, v54, v56, vcc_lo bitop3:0xa8\n" \
  "v_cmp_ne_u32 vcc, 0, v54\n" \
  "s_cmp_eq_u64 vcc, 0\n" \
  "s_cbranch_scc1 .LCV_M3_%=\n" \
  ".LCV_M1_%=:\n" \
  "s_mov_b64 exec, 15\n" \
  "buffer_load_dword v58, v2, s[44:47], s68 offen sc1\n" \
  "s_mov_b64 exec, -1\n" \
  "s_sub_u32 s87, s87, 1\n" \
  "s_cbranch_scc1 .LCV_END_%=\n" \
  "s_waitcnt vmcnt(0)\n" \
  "v_cmp_eq_u32 vcc, 0, v58\n" \
  "s_and_b32 vcc_lo, vcc_lo, 15\n" \
  "s_cbranch_scc0 .LCV_M2_%=\n" \
  "s_sleep 8\n" \
  "s_branch .LCV_M1_%=\n" \
  ".LCV_M2_%=:\n" \
  "s_mov_b32 m0, s99\n" \
  "s_add_u32 s88, s99, 8192\n" \
  "global_load_lds_dwordx4 v10, s[62:63] sc1\n" \
  "global_load_lds_dwordx4 v10, s[62:63] offset:1024 sc1\n" \
  "s_mov_b32 m0, s88\n" \
  "s_nop 0\n" \
  "global_load_lds_dwordx4 v10, s[64:65] sc1\n" \
  "global_load_lds_dwordx4 v10, s[64:65] offset:1024 sc1\n" \
  "s_waitcnt vmcnt(0)\n" \
  "s_branch .LCV_M3_%=\n" \
  ".LCV_LAST_%=:\n" \
  "s_waitcnt vmcnt(0)\n" \
  ".LCV_END_%=:\n" \
  "s_endpgm\n" \
  ".LCV_FS_%=:\n" \
  "s_cmp_eq_u32 s90, 0\n" \
  "s_cbranch_scc1 .LCV_F9_%=\n" \
  "s_movk_i32 s87, 400\n" \
  ".LCV_F0_%=:\n" \
  "s_sleep 8\n" \
  "s_mov_b64 exec, 15\n" \
  "buffer_load_dword v58, v2, s[44:47], s68 offen offset:4 sc1\n" \
  "s_mov_b64 exec, -1\n" \
  "s_sub_u32 s87, s87, 1\n" \
  "s_cbranch_scc1 .LCV_F9_%=\n" \
  "s_waitcnt vmcnt(0)\n" \
  "v_cmp_eq_u32 vcc, 0, v58\n" \
  "s_and_b32 vcc_lo, vcc_lo, 15\n" \
  "s_cbranch_scc1 .LCV_F0_%=\n" \
  "v_readlane_b32 vcc_lo, v58, 0\n" \
  "v_readlane_b32 s38, v58, 1\n" \
  "s_max_u32 vcc_lo, vcc_lo, s38\n" \
  "v_readlane_b32 s38, v58, 2\n" \
  "s_max_u32 vcc_lo, vcc_lo, s38\n" \
  "v_readlane_b32 s38, v58, 3\n" \
  "s_max_u32 vcc_lo, vcc_lo, s38\n" \
  ".LCV_F1_%=:\n" \
  "s_sleep 1\n" \
  "s_memrealtime s[38:39]\n" \
  "s_sub_u32 s87, s87, 1\n" \
  "s_cbranch_scc1 .LCV_F9_%=\n" \
  "s_waitcnt lgkmcnt(0)\n" \
  "s_sub_u32 s38, s38, vcc_lo\n" \
  "s_cmp_lt_i32 s38, 0\n" \
  "s_cbranch_scc1 .LCV_F1_%=\n" \
  "s_mov_b32 m0, s99\n" \
  "s_add_u32 s88, s99, 8192\n" \
  "global_load_lds_dwordx4 v10, s[62:63] sc1\n" \
  "global_load_lds_dwordx4 v10, s[62:63] offset:1024 sc1\n" \
  "s_mov_b32 m0, s88\n" \
  "s_nop 0\n" \
  "global_load_lds_dwordx4 v10, s[64:65] sc1\n" \
  "global_load_lds_dwordx4 v10, s[64:65] offset:1024 sc1\n" \
  "s_waitcnt vmcnt(0)\n" \
  "v_add_u32 v53, s99, v10\n" \
  "ds_read_b32 v54, v53 offset:0\n" \
  "ds_read_b32 v55, v53 offset:1024\n" \
  "ds_read_b32 v56, v53 offset:8192\n" \
  "ds_read_b32 v57, v53 offset:9216\n" \
  "s_lshr_b32 s88, s71, 4\n" \
  "s_mov_b32 vcc_lo, 1\n" \
  "s_waitcnt lgkmcnt(0)\n" \
  "v_bitop3_b32 v54, v54, v55, s88 bitop3:0x7e\n" \
  "v_bitop3_b32 v56, v56, v57, s88 bitop3:0x7e\n" \
  "v_bitop3_b32 v54, v54, v56, vcc_lo bitop3:0xa8\n" \
  "v_cmp_ne_u32 vcc, 0, v54\n" \
  "s_cmp_eq_u64 vcc, 0\n" \
  "s_cbranch_scc1 .LCV_W1_%=\n" \
  ".LCV_F9_%=:\n" \
  "s_waitcnt vmcnt(0) lgkmcnt(0)\n" \
  "s_branch .LCV_W7_%=\n"
#define TOUCH(n)
#define TOK_AL(p, o, m) ((((unsigned)(size_t)(p) + (unsigned)(o) + (m)) & ~(m)) - (unsigned)(size_t)(p))
#define TOK_QK(p, L) (GEN_TR * (L) ? TOK_AL(p, OFF_QK + SZ_QK, (0x100000u << GEN_TR) - 1) : (unsigned)(OFF_QK + (L) * SZ_QK))
#define TOK_V(p, L) (GEN_TR * (L) ? TOK_AL(p, OFF_VF + SZ_O, (0x80000u << GEN_TR) - 1) : (unsigned)(OFF_VF + (L) * SZ_O))
__device__ __forceinline__ void start_kgen(const MegaArgs& A, char* smem, const int kg) {
  const int w = A.wv, l = lane_id(), tid = 64 * w + l, c = l & 15, g = l >> 4;
  KGen G;
  unsigned tp = 0u;
#define TEXT_ASK()                                                                                                     \
  {                                                                                                                    \
    const char* pc;                                                                                                    \
    unsigned n;                                                                                                        \
    asm volatile("s_getpc_b64 %0\n.LKG_PC:\n\ts_mov_b32 %1, .LKG_LAST-.LKG_PC" : "=s"(pc), "=s"(n));                    \
    const unsigned o = (unsigned)(tid * 8 + ((blockIdx.x >> 3) & 7)) * IPS;                                            \
    if ((int)o < (int)n) asm volatile("global_load_dword %0, %1, %2" : "=v"(tp) : "v"(o), "s"(pc) : "memory");         \
  }
  if (IPF == 2) TEXT_ASK()
  v4u raw[2] = {};
#pragma unroll
  for (int i = 0; i < TP; ++i)
    if (kg < 32) raw[i] = *reinterpret_cast<const v4u*>(A.x0 + (size_t)(TP * (8 * (kg & 7) + w) + i) * DM + l * 8);
  G.load(A, 8 * kg + w, l);
  {
    const __amdgpu_buffer_rsrc_t rz = MKRS(A.ws + OFF_Z, Z_BYTES);
#pragma unroll
    for (int i = 0; i < 4; ++i) {
      const u64 z = i == 0 && kg < (int)(2 * SZ_ACC / 8192) ? ACC_INIT : 0ull;
      ST128((v4u{(unsigned)z, (unsigned)(z >> 32), (unsigned)z, (unsigned)(z >> 32)}), rz, tid * 16, (i * 64 + kg) * 8192);
    }
  }
  const u64 tag = call_tag(A);
  if (TP && kg < 32) {
    unsigned t = row_stats(raw[0]);
    if (TP > 1) {
      const unsigned t1 = row_stats(raw[1]);
      t = l < 2 ? t : t1;
    }
    if (l < 2 * TP) __hip_atomic_store((gu64*)(A.ws + OFF_GT) + 2 * (L_SEQ + TP * (8 * kg + w)) + l, tagged(tag, (l & 1) ? t : t << 16), RLX_AGENT);
  }
  DRAIN();
  __syncthreads();
  if (tid < 8) __hip_atomic_store((gu64*)(A.ws + OFF_RD) + tid * 64 + kg, tag, RLX_AGENT);
  asm volatile("" : "+v"(G.pa), "+v"(G.pb), "+v"(G.pe));
  G.setup();
  G.st = smem + w * 16640;
  const __amdgpu_buffer_rsrc_t rs = MKRS(A.ws + OFF_KF + (8 * kg + w) * 2048, 386 * 4);
  SST(1);
  unsigned tw = 0u;
#define TLB_ASK()                                                                                                      \
  if (tid < 384) {                                                                                                     \
    const unsigned o = (unsigned)OFF_XA + (unsigned)(tid * 8 + ((blockIdx.x >> 3) & 7)) * 65536u;                      \
    if (o < WS_TOTAL) asm volatile("global_load_dword %0, %1, %2" : "=v"(tw) : "v"(o), "s"(A.ws) : "memory");          \
  }
#define X2_CLEAR() \
  _Pragma("unroll") for (int i = 0; i < 4; ++i) ST128((v4u{0u, 0u, 0u, 0u}), MKRS(A.ws + OFF_X2, SZ_X), tid * 16, (4 * kg + i) * 8192);
  if (XZP == 1) { X2_CLEAR() }
  if (IPF == 1) TEXT_ASK()
  if (TLBW == 1) TLB_ASK()
  unsigned pf[6] = {};
#define PF_ASK(M)                                                                                                                  \
  if (kg < 32) {                                                                                                                   \
    if ((M) & 1) asm volatile("global_load_dword %0, %2, %3\n\tglobal_load_dword %1, %2, %3 offset:128" : "=&v"(pf[0]), "=&v"(pf[1]) : "v"(kg * 131072 + tid * 256), "s"(A.Wo)); \
    if ((M) & 2) asm volatile("global_load_dword %0, %2, %3\n\tglobal_load_dword %1, %2, %3 offset:128" : "=&v"(pf[2]), "=&v"(pf[3]) : "v"(kg * 256 + tid * 8192), "s"(A.Wq)); \
    if ((M) & 4) asm volatile("global_load_dword %0, %2, %3\n\tglobal_load_dword %1, %2, %3 offset:128" : "=&v"(pf[4]), "=&v"(pf[5]) : "v"(kg * 256 + tid * 8192), "s"(A.Wk)); \
  }
  PF_ASK(PFA)
  G.columns(l, 0, 16);
  G.get(G.bh, G.bl, l);
  if (XZP == 2) { X2_CLEAR() }
  PF_ASK(PFB)
  if (TLBW == 2) TLB_ASK()
  (void)G.scale();
  const Cx zb = G.power(256.f);
  Cx b = G.C, z = G.power(2.f);
#pragma unroll 1
  for (int f = 1; f < 9;) {
    char* p = G.st + l * 8;
    const int fe = f == 1 ? 4 : 9;
#pragma unroll 1
    for (; f < fe; ++f) {
      b = cmul(b, zb);
      if (f == 2 || f == 4) z = cmul(z, z);
      Cx y = b;
      p = G.rows(p, y, z, f == 1 ? 8 : f < 4 ? 4 : 2);
    }
    const f32x4 d = G.tile<false>(l);
    const v4u r = {pkh(d.x, d.x), pkh(d.y, d.y), pkh(d.z, d.z), pkh(d.w, d.w)};
    if (f == 4) {
      ST128(r, rs, (g < 2 ? 8 * c + 4 * g : 64 * g + 4 * c) * 4, 0);
    } else {
      ST64((v2u{r.x, r.y}), rs, (256 + 64 * g + 2 * c) * 4, 0);
      ST64((v2u{r.z, r.w}), rs, (288 + 64 * g + 2 * c) * 4, 0);
    }
  }
  SST(2);
  DRAIN();
  asm volatile("" ::"v"(tp), "v"(tw), "v"(pf[0]), "v"(pf[1]), "v"(pf[2]), "v"(pf[3]), "v"(pf[4]), "v"(pf[5]));
  if (l == 0) __hip_atomic_store((gu64*)(A.ws + OFF_KG) + kg * 16 + w, tag, RLX_AGENT);
  if (XZP == 3) { X2_CLEAR() }
  const u64 rv = (XZP & 3) && w == 0 ? ready_look(A) : 0;
  if (TLBW == 3) {
    TLB_ASK()
    DRAIN();
    asm volatile("" ::"v"(tw));
  }
  DRAIN();
  __syncthreads();
  if ((XZP & 3) && w == 0) {
    ready_wait(A, tag, rv);
    if (l < 8) set_flag((unsigned*)(A.ws + OFF_FL) + FL_FZ + l * 64 + kg);
  }
  SST(3);
}

constexpr int C_SM = 131072, C_SO = C_SM + 8192, C_KT = C_SO + 2048, C_X = C_KT + 16384, C_SQ = C_X + 1024;
static_assert(C_SQ + 4096 <= 163840 && C_SQ % 512 == 0, "lds");
__device__ __forceinline__ v4u mk_srd(const void* p, const unsigned bytes) {
  const unsigned long long a = (unsigned long long)p;
  return v4u{(unsigned)a, (unsigned)(a >> 32) & 0xffffu, bytes, 0x00020000u};
}
template <bool ATT>
__device__ __forceinline__ void role_chain_t(const MegaArgs& A, char* smem, const int L, const int h, const int rep) {
  const int w = A.wv;
  char* ws = A.ws;
  unsigned* FL = (unsigned*)(ws + OFF_FL);
  const int l = lane_id(), c = l & 15, g = l >> 4;
  unsigned* FQ = FL + FL_FQ + L * 64 * 32 * 32;
  const int wb = (w + ROT_H) & 7, tb = ROT_T;
  const unsigned sb = lds_base(smem);
  glds16(A.mem0, (unsigned)((c & 7) * DM + 64 * w + 16 * g + 8 * (c >> 3)) * 2, sb + C_SM + w * 1024);
#pragma unroll
  for (int i = 0; i < 16; ++i)
    glds16((const char*)A.Wo + (size_t)(h * DH + 32 * (i >> 2) + 4 * w + (i & 2)) * (DM * 2),
           (unsigned)((i & 1) * 1024 + ((l ^ (((i & 1) | ((w & 3) << 1)) << 1)) << 4)), sb + w * 16384 + i * 1024, WNT & 1);
  const u64 tag = call_tag(A);
  const char* gK = ws + TOK_QK(ws, L) + ((size_t)h * 16 + 2 * w) * 1024;
  const char* gV = ws + TOK_V(ws, L) + ((size_t)h * 8 + 2 * w - 8) * 1024;
  v8u pv, pw;
  {
    const unsigned voff = g * 256 + c * 8, wp = sb + w * 16384 + l * 16;
    pv[0] = (c & 8) ? 0xEEEE : 0x4444;
    pv[1] = sb + C_SM + l * 16;
    const unsigned ra = sb + C_SQ + (g >> 1) * 512 + (c ^ ((g & 1) * 18)) * 8;
    pv[2] = ATT ? ra : voff;
    pv[3] = ATT ? ra ^ 0x108 : wp;
    pv[4] = sb + ((c & 2) ? C_SQ : C_SO) + w * 256 + (((c & 5) | ((c >> 2) & 2)) + 8 * (g >> 1)) * 16 + 8 * ((g ^ (c >> 1)) & 1);
    pv[5] = voff;
    pv[6] = wp;
    pv[7] = sb + C_SO + (l >> 5) * 1024 + w * 256 + (l & 31) * 8;
    const int q = (l >> 2) & 3, p = l & 3;
    const int f = (q & 1) | ((g & 1) << 1) | ((q >> 1) << 2);
#pragma unroll
    for (int i = 0; i < 4; ++i) pw[i] = (4 * i + g) * (HD * 2) + ((c ^ (((g & 1) | (i << 1)) << 1)) << 4) - 1024 * i;
    pw[4] = sb + (g >> 1) * 16384 + (4 * (g & 1) + (q & 1) + 8 * (q >> 1)) * 256 + (w ^ f) * 32 + p * 8;
    pw[5] = sb + (4 * (g >> 1) + 2 * (q >> 1) + (g & 1)) * 16384 + (q & 1) * 1024 + p * 8 + ((4 * wb) ^ f ^ tb) * 32;
    pw[6] = pw[7] = 0u;
  }
  const size_t wo = (size_t)(16 * w) * HD + h * DH;
#define POL 2
#define PG(w) (POL == 2 ? (w) * 4096 : ((w) & 1) * 4096 + ((w) >> 1) * 16384)
  const v4u rsA = mk_srd(ws + OFF_ACC + L * SZ_ACC + (POL == 1 ? 8192 : 0), SZ_ACC - 8192);
  const v4u rsF = mk_srd(FQ, 64 * 32 * 32 * 4);
  const v4u rsMK = mk_srd(ws + OFF_MEMK + L * SZ_MEM, SZ_MEM);
  const v4u rsMV = mk_srd(ws + OFF_MEMV + L * SZ_MEM, SZ_MEM);
  const v4u pAB = (v4u{0u, 2048u, 3072u, 1024u} ^ (1536u * tb)) + PG(wb);
  const v4u rsT = mk_srd(FL + FL_FK + L * 32, 64 * 1024 - 128);
  unsigned sc[8];
  sc[0] = h * 128;
  sc[1] = (h * 4 + w - 4) * 512;
  sc[2] = (h * 4 + w - 4) * 512;
  sc[3] = NSEG - 1;
  sc[5] = w;
  sc[6] = sb;
  const char* dbg = ws;
#define CHAIN_OPS                                                                                                          \
  ::"{v[0:7]}"(pv), "{v[24:31]}"(pw), "{s[4:5]}"(A.Wv + wo), "{s[6:7]}"(A.Wq + wo), "{s[8:9]}"(A.Wk + wo), "{s[10:11]}"(tag),         \
      "{s[12:13]}"(ws + OFF_RD + (blockIdx.x & 7) * 512), "{s[14:15]}"(ws + OFF_G + (blockIdx.x & 7) * 256), "{s[16:17]}"(ws + OFF_G + h * 8), \
      "{s18}"(w == 4 && L == 0 ? 255 : 0), "{s19}"(sb + w * 16384), "{s[20:21]}"(ws + WS_TOTAL + (w == 0 ? 534 * 256 + A.vb : 610 * 256) * 8), \
      "{s[40:43]}"(rsA), "{s[44:47]}"(rsF), "{s[48:51]}"(rsMK), "{s[52:55]}"(rsMV), "{s[56:59]}"(pAB), "{s60}"(PG(w)), "{s61}"(ATT ? PG(h >> 2) + (h & 3) * 1024 : L), \
      "{s[62:63]}"(gK), "{s[64:65]}"(gV), "{s66}"(0), "{s67}"(w == 4 ? (h >> 3) * 256 + (h & 7) * 8 : 0x40000000), "{s68}"(sc[0]), "{s69}"(sc[1]), "{s70}"(sc[2]),    \
      "{s71}"(sc[3]), "{s73}"(sc[5]), "{s74}"(sc[6]), "{s[36:37]}"(dbg),                   \
      "{s[0:3]}"(rsT)                                                                                                      \
      : "memory", "vcc", "scc", "s99", "v255"
  if (ATT) asm volatile("s_mov_b64 exec, -1\ns_nop 4\n" CHAIN_ASM_ATT ".LKG_LAS2:\n" CHAIN_OPS);
  else asm volatile("s_mov_b64 exec, -1\ns_nop 4\n" CHAIN_ASM_VO CHAIN_OPS);
#undef CHAIN_OPS
}

__device__ __forceinline__ void role_chain(const MegaArgs& A, char* smem, const int L, const int h, const int rep) {
  if (A.wv < 4) role_chain_t<true>(A, smem, L, h, rep);
  else role_chain_t<false>(A, smem, L, h, rep);
}

constexpr int AW_SO = 0, AW_TK = 32768, AW_CNT = AW_TK + 98304;
__device__ __forceinline__ unsigned umin3(unsigned a, unsigned b, unsigned c) {
  return __builtin_elementwise_min(__builtin_elementwise_min(a, b), c);
}
#define PW_FREE(L_, s_, i_) \
  if (l == 0) set_flag(FL + FL_FD + ((s_) * 2 + (L_)) * 32 + (i_))
#define AW_ASM_X \
  "s_mov_b32 s61, 0\n" \
  "s_mov_b32 s67, 1\n" \
  "s_mov_b32 s68, 0\n" \
  "s_mov_b32 s70, 0\n" \
  "s_mov_b32 s73, 0\n" \
  "s_mov_b32 s71, 0x3e0293ee\n" \
  "s_mov_b32 s72, 0xbe0293ee\n" \
  "s_mov_b32 s65, 49152\n" \
  "s_bitcmp0_b32 s58, 2\n" \
  "s_cbranch_scc1 .LCX_OWN_%=\n" \
  "v_swap_b32 v192, v208\n" \
  "v_swap_b32 v193, v209\n" \
  "v_swap_b32 v194, v210\n" \
  "v_swap_b32 v195, v211\n" \
  "v_swap_b32 v196, v212\n" \
  "v_swap_b32 v197, v213\n" \
  "v_swap_b32 v198, v214\n" \
  "v_swap_b32 v199, v215\n" \
  "v_swap_b32 v200, v216\n" \
  "v_swap_b32 v201, v217\n" \
  "v_swap_b32 v202, v218\n" \
  "v_swap_b32 v203, v219\n" \
  "v_swap_b32 v204, v220\n" \
  "v_swap_b32 v205, v221\n" \
  "v_swap_b32 v206, v222\n" \
  "v_swap_b32 v207, v223\n" \
  "v_swap_b32 v224, v240\n" \
  "v_swap_b32 v225, v241\n" \
  "v_swap_b32 v226, v242\n" \
  "v_swap_b32 v227, v243\n" \
  "v_swap_b32 v228, v244\n" \
  "v_swap_b32 v229, v245\n" \
  "v_swap_b32 v230, v246\n" \
  "v_swap_b32 v231, v247\n" \
  "v_swap_b32 v232, v248\n" \
  "v_swap_b32 v233, v249\n" \
  "v_swap_b32 v234, v250\n" \
  "v_swap_b32 v235, v251\n" \
  "v_swap_b32 v236, v252\n" \
  "v_swap_b32 v237, v253\n" \
  "v_swap_b32 v238, v254\n" \
  "v_swap_b32 v239, v255\n" \
  ".LCX_OWN_%=:\n" \
  "v_mov_b32 v38, s58\n" \
  "s_memrealtime s[38:39]\n" \
  "s_waitcnt lgkmcnt(0)\n" \
  "s_mov_b32 s69, s38\n" \
  "s_setprio 3\n" \
  ".p2align 6\n" \
  ".LCX_TOP_%=:\n" \
  "s_mov_b32 s62, 0x200000\n" \
  "s_cmp_ge_u32 s73, s67\n" \
  "s_cbranch_scc1 .LCX_TOK_%=\n" \
  "s_cmp_eq_u32 s61, 0\n" \
  "s_cbranch_scc1 .LCX_SPIN_%=\n" \
  ".LCX_RE_%=:\n" \
  "ds_read_b32 v37, v38\n" \
  "s_add_u32 s62, s62, 1\n" \
  "s_waitcnt lgkmcnt(0)\n" \
  "v_readfirstlane_b32 s73, v37\n" \
  "s_bfe_u32 s64, s73, 0x80008\n" \
  "s_and_b32 s73, s73, 0xff\n" \
  "s_min_u32 s73, s73, s64\n" \
  "s_cmp_ge_u32 s73, s67\n" \
  "s_cbranch_scc1 .LCX_TOK_%=\n" \
  "s_sleep 1\n" \
  "s_cmp_lt_u32 s62, 2097158\n" \
  "s_cbranch_scc1 .LCX_RE_%=\n" \
  ".LCX_SPIN_%=:\n" \
  "s_sleep 2\n" \
  "s_memrealtime s[38:39]\n" \
  "ds_read_b32 v37, v38\n" \
  "s_sub_u32 s62, s62, 1\n" \
  "s_cbranch_scc1 .LCX_END_%=\n" \
  "s_waitcnt lgkmcnt(0)\n" \
  "v_readfirstlane_b32 s73, v37\n" \
  "s_add_u32 s38, s38, 80\n" \
  "s_bfe_u32 s64, s73, 0x80008\n" \
  "s_and_b32 s73, s73, 0xff\n" \
  "s_min_u32 s73, s73, s64\n" \
  "s_cmp_ge_u32 s73, s67\n" \
  "s_cbranch_scc0 .LCX_SPIN_%=\n" \
  "s_cmp_eq_u32 s70, 0\n" \
  "s_cbranch_scc1 .LCX_TOK_%=\n" \
  "s_sub_u32 s63, s38, s69\n" \
  "s_max_i32 s63, s63, 0\n" \
  "s_add_u32 s69, s69, s63\n" \
  ".LCX_TOK_%=:\n" \
  "ds_read_b128 v[48:51], v6 offset:0\n" \
  "ds_read_b128 v[64:67], v0 offset:8192\n" \
  "ds_read_b128 v[80:83], v0 offset:12288\n" \
  "ds_read_b128 v[52:55], v6 offset:1024\n" \
  "ds_read_b128 v[68:71], v0 offset:9216\n" \
  "ds_read_b128 v[84:87], v0 offset:13312\n" \
  "ds_read_b128 v[56:59], v6 offset:2048\n" \
  "ds_read_b128 v[72:75], v0 offset:10240\n" \
  "ds_read_b128 v[88:91], v0 offset:14336\n" \
  "ds_read_b128 v[60:63], v6 offset:3072\n" \
  "ds_read_b128 v[76:79], v0 offset:11264\n" \
  "ds_read_b128 v[92:95], v0 offset:15360\n" \
  "s_waitcnt lgkmcnt(9)\n" \
  "v_mfma_f32_16x16x32_bf16 v[20:23], v[64:67], v[48:51], v[8:11]\n" \
  "v_mfma_f32_16x16x32_bf16 v[24:27], v[80:83], v[48:51], v[12:15]\n" \
  "s_memrealtime s[38:39]\n" \
  "s_waitcnt lgkmcnt(6)\n" \
  "v_mfma_f32_16x16x32_bf16 v[20:23], v[68:71], v[52:55], v[20:23]\n" \
  "v_mfma_f32_16x16x32_bf16 v[24:27], v[84:87], v[52:55], v[24:27]\n" \
  "s_waitcnt lgkmcnt(3)\n" \
  "v_mfma_f32_16x16x32_bf16 v[20:23], v[72:75], v[56:59], v[20:23]\n" \
  "v_mfma_f32_16x16x32_bf16 v[24:27], v[88:91], v[56:59], v[24:27]\n" \
  "s_waitcnt lgkmcnt(0)\n" \
  "v_mfma_f32_16x16x32_bf16 v[20:23], v[76:79], v[60:63], v[20:23]\n" \
  "v_mfma_f32_16x16x32_bf16 v[24:27], v[92:95], v[60:63], v[24:27]\n" \
  "s_sub_u32 s63, s38, s69\n" \
  "s_add_u32 s63, s63, 8\n" \
  "s_or_b32 s63, s63, s68\n" \
  "s_cmp_lt_i32 s63, 0\n" \
  "s_cbranch_scc1 .LCX_D2_%=\n" \
  "s_setprio 3\n" \
  "v_mov_b32 v35, 0\n" \
  "global_load_dwordx4 v[128:131], v5, s[48:49] offset:0 sc1\n" \
  "global_load_dwordx4 v[132:135], v5, s[48:49] offset:1024 sc1\n" \
  "global_load_dwordx4 v[136:139], v3, s[48:49] offset:0 sc1\n" \
  "global_load_dwordx4 v[140:143], v3, s[48:49] offset:512 sc1\n" \
  "global_load_dword v35, v35, s[50:51] sc1\n" \
  "global_load_dword v146, v2, s[52:53] sc1\n" \
  "s_setprio 0\n" \
  "s_mov_b32 s68, -2\n" \
  ".LCX_D2_%=:\n" \
  "ds_read_b128 v[96:99], v0 offset:16384\n" \
  "ds_read_b128 v[100:103], v0 offset:17408\n" \
  "ds_read_b128 v[104:107], v0 offset:18432\n" \
  "ds_read_b128 v[108:111], v0 offset:19456\n" \
  "ds_read_b128 v[112:115], v0 offset:20480\n" \
  "ds_read_b128 v[116:119], v0 offset:21504\n" \
  "ds_read_b128 v[120:123], v0 offset:22528\n" \
  "ds_read_b128 v[124:127], v0 offset:23552\n" \
  "s_add_u32 s67, s67, 1\n" \
  "v_add_u32 v0, s65, v0\n" \
  "v_add_u32 v6, s65, v6\n" \
  "s_sub_u32 s65, 0, s65\n" \
  "s_nop 1\n" \
  "v_max3_f32 v32, v20, v21, v22\n" \
  "v_max3_f32 v33, v23, v24, v25\n" \
  "v_max3_f32 v32, v32, v26, v27\n" \
  "v_max_f32 v32, v32, v33\n" \
  "s_memrealtime s[38:39]\n" \
  "v_mov_b32 v33, v32\n" \
  "s_nop 1\n" \
  "v_permlane16_swap_b32 v32, v33\n" \
  "v_max_f32 v32, v32, v33\n" \
  "v_mov_b32 v33, v32\n" \
  "s_nop 1\n" \
  "v_permlane32_swap_b32 v32, v33\n" \
  "v_max_f32 v32, v32, v33\n" \
  "s_waitcnt lgkmcnt(0)\n" \
  "s_sub_u32 s63, s38, s69\n" \
  "s_add_u32 s63, s63, 8\n" \
  "s_or_b32 s63, s63, s68\n" \
  "s_cmp_lt_i32 s63, 0\n" \
  "s_cbranch_scc1 .LCX_D3_%=\n" \
  "s_setprio 3\n" \
  "v_mov_b32 v35, 0\n" \
  "global_load_dwordx4 v[128:131], v5, s[48:49] offset:0 sc1\n" \
  "global_load_dwordx4 v[132:135], v5, s[48:49] offset:1024 sc1\n" \
  "global_load_dwordx4 v[136:139], v3, s[48:49] offset:0 sc1\n" \
  "global_load_dwordx4 v[140:143], v3, s[48:49] offset:512 sc1\n" \
  "global_load_dword v35, v35, s[50:51] sc1\n" \
  "global_load_dword v146, v2, s[52:53] sc1\n" \
  "s_setprio 0\n" \
  "s_mov_b32 s68, -2\n" \
  ".LCX_D3_%=:\n" \
  "v_mul_f32 v33, s72, v32\n" \
  "v_fma_f32 v20, v20, s71, v33\n" \
  "v_fma_f32 v21, v21, s71, v33\n" \
  "v_fma_f32 v22, v22, s71, v33\n" \
  "v_fma_f32 v23, v23, s71, v33\n" \
  "v_fma_f32 v24, v24, s71, v33\n" \
  "v_fma_f32 v25, v25, s71, v33\n" \
  "v_fma_f32 v26, v26, s71, v33\n" \
  "v_fma_f32 v27, v27, s71, v33\n" \
  "v_exp_f32 v20, v20\n" \
  "v_exp_f32 v21, v21\n" \
  "v_exp_f32 v22, v22\n" \
  "v_exp_f32 v23, v23\n" \
  "v_exp_f32 v24, v24\n" \
  "v_exp_f32 v25, v25\n" \
  "v_exp_f32 v26, v26\n" \
  "v_exp_f32 v27, v27\n" \
  "v_add_f32 v34, v20, v21\n" \
  "v_add_f32 v33, v22, v23\n" \
  "v_add_f32 v36, v24, v25\n" \
  "v_add_f32 v34, v34, v33\n" \
  "v_add_f32 v33, v26, v27\n" \
  "v_add_f32 v34, v34, v36\n" \
  "v_add_f32 v34, v34, v33\n" \
  "v_mov_b64 v[28:29], 0\n" \
  "v_mov_b64 v[30:31], 0\n" \
  "s_waitcnt vmcnt(2) lgkmcnt(0)\n" \
  "s_cmp_gt_i32 s68, -2\n" \
  "s_cbranch_scc1 .LCX_NP_%=\n" \
  "s_waitcnt vmcnt(0)\n" \
  ".LCX_NP_%=:\n" \
  "s_cmp_lg_u32 s68, 0\n" \
  "s_cbranch_scc1 .LCX_TEST_%=\n" \
  ".LCX_WF_%=:\n" \
  "s_memrealtime s[38:39]\n" \
  "s_waitcnt lgkmcnt(0)\n" \
  "s_sub_u32 s63, s69, s38\n" \
  "s_cmp_le_i32 s63, 0\n" \
  "s_cbranch_scc1 .LCX_GO_%=\n" \
  "s_cmp_lt_i32 s63, 12\n" \
  "s_cbranch_scc1 .LCX_WF_%=\n" \
  "s_sleep 3\n" \
  "s_branch .LCX_WF_%=\n" \
  ".LCX_GO_%=:\n" \
  "v_mov_b32 v35, 0\n" \
  "global_load_dwordx4 v[128:131], v5, s[48:49] offset:0 sc1\n" \
  "global_load_dwordx4 v[132:135], v5, s[48:49] offset:1024 sc1\n" \
  "global_load_dwordx4 v[136:139], v3, s[48:49] offset:0 sc1\n" \
  "global_load_dwordx4 v[140:143], v3, s[48:49] offset:512 sc1\n" \
  "global_load_dword v35, v35, s[50:51] sc1\n" \
  "global_load_dword v146, v2, s[52:53] sc1\n" \
  "s_sub_u32 s62, s62, 8\n" \
  "s_cbranch_scc1 .LCX_END_%=\n" \
  "s_waitcnt vmcnt(0)\n" \
  ".LCX_TEST_%=:\n" \
  "s_setprio 3\n" \
  "v_smfmac_f32_16x16x64_bf16 v[28:31], v[128:131], v[48:55], v1\n" \
  "v_smfmac_f32_16x16x64_bf16 v[28:31], v[132:135], v[56:63], v1\n" \
  "v_min3_u32 v33, v128, v129, v130\n" \
  "v_min3_u32 v36, v131, v132, v133\n" \
  "v_min3_u32 v33, v33, v134, v135\n" \
  "v_min3_u32 v36, v36, v136, v137\n" \
  "v_min3_u32 v33, v33, v138, v139\n" \
  "v_min3_u32 v36, v36, v140, v141\n" \
  "v_min3_u32 v33, v33, v142, v143\n" \
  "v_min3_u32 v33, v33, v36, v146\n" \
  "v_cmp_eq_u32 vcc, 0, v33\n" \
  "v_readfirstlane_b32 s64, v35\n" \
  "s_mov_b32 s68, 0\n" \
  "s_cmp_eq_u64 vcc, 0\n" \
  "s_cbranch_scc0 .LCX_FAIL_%=\n" \
  "v_permlane32_swap_b32 v28, v30\n" \
  "v_permlane32_swap_b32 v29, v31\n" \
  "v_mov_b32 v33, s67\n" \
  "ds_write_b32 v38, v33 offset:16\n" \
  "v_add_f32 v28, v28, v30\n" \
  "v_add_f32 v29, v29, v31\n" \
  "v_max3_f32 v36, v28, v29, v32\n" \
  "v_mov_b32 v33, v36\n" \
  "s_cmp_eq_u32 s64, 0\n" \
  "s_cselect_b32 s64, s38, s64\n" \
  "v_permlane16_swap_b32 v36, v33\n" \
  "v_max_f32 v36, v36, v33\n" \
  "v_mov_b32 v33, v36\n" \
  "s_sub_u32 s63, s64, s70\n" \
  "s_mov_b32 s70, s64\n" \
  "v_permlane32_swap_b32 v36, v33\n" \
  "v_max_f32 v36, v36, v33\n" \
  "v_mul_f32 v36, s72, v36\n" \
  "v_fma_f32 v32, v32, s71, v36\n" \
  "v_fma_f32 v28, v28, s71, v36\n" \
  "v_fma_f32 v29, v29, s71, v36\n" \
  "v_exp_f32 v32, v32\n" \
  "v_exp_f32 v28, v28\n" \
  "v_exp_f32 v29, v29\n" \
  "v_fma_f32 v34, v34, v32, v28\n" \
  "v_add_f32 v34, v34, v29\n" \
  "v_mov_b32 v33, v34\n" \
  "s_min_u32 s63, s63, 250\n" \
  "s_add_u32 s69, s64, s63\n" \
  "v_permlane16_swap_b32 v34, v33\n" \
  "v_add_f32 v34, v34, v33\n" \
  "v_mov_b32 v33, v34\n" \
  "s_add_u32 s69, s69, -52\n" \
  "s_add_u32 s48, s48, 0x10000\n" \
  "s_addc_u32 s49, s49, 0\n" \
  "v_permlane32_swap_b32 v34, v33\n" \
  "v_add_f32 v34, v34, v33\n" \
  "v_rcp_f32 v34, v34\n" \
  "s_add_u32 s50, s50, 1024\n" \
  "s_addc_u32 s51, s51, 0\n" \
  "s_add_u32 s52, s52, 256\n" \
  "s_addc_u32 s53, s53, 0\n" \
  "v_mul_f32 v32, v32, v34\n" \
  "v_mul_f32 v20, v20, v32\n" \
  "v_mul_f32 v21, v21, v32\n" \
  "v_mul_f32 v22, v22, v32\n" \
  "v_mul_f32 v23, v23, v32\n" \
  "v_mul_f32 v24, v24, v32\n" \
  "v_mul_f32 v25, v25, v32\n" \
  "v_mul_f32 v26, v26, v32\n" \
  "v_mul_f32 v27, v27, v32\n" \
  "v_cvt_pk_bf16_f32 v40, v20, v21\n" \
  "v_cvt_pk_bf16_f32 v41, v22, v23\n" \
  "v_cvt_pk_bf16_f32 v42, v24, v25\n" \
  "v_cvt_pk_bf16_f32 v43, v26, v27\n" \
  "v_mul_f32 v28, v28, v34\n" \
  "v_mul_f32 v29, v29, v34\n" \
  "v_mfma_f32_16x16x32_bf16 v[64:67], v[96:99], v[40:43], 0\n" \
  "v_cvt_pk_bf16_f32 v44, v28, v29\n" \
  "v_mov_b32 v45, 0\n" \
  "v_mfma_f32_16x16x32_bf16 v[68:71], v[100:103], v[40:43], 0\n" \
  "v_cvt_pk_bf16_f32 v47, v28, v29\n" \
  "v_mov_b32 v46, 0\n" \
  "v_mfma_f32_16x16x32_bf16 v[72:75], v[104:107], v[40:43], 0\n" \
  "v_permlane32_swap_b32 v44, v45\n" \
  "v_mfma_f32_16x16x32_bf16 v[76:79], v[108:111], v[40:43], 0\n" \
  "v_permlane32_swap_b32 v46, v47\n" \
  "v_mfma_f32_16x16x32_bf16 v[80:83], v[112:115], v[40:43], 0\n" \
  "v_xor_b32 v16, 4096, v7\n" \
  "v_mfma_f32_16x16x32_bf16 v[84:87], v[116:119], v[40:43], 0\n" \
  "v_xor_b32 v17, 8192, v7\n" \
  "v_mfma_f32_16x16x32_bf16 v[88:91], v[120:123], v[40:43], 0\n" \
  "v_xor_b32 v18, 12288, v7\n" \
  "v_mfma_f32_16x16x32_bf16 v[92:95], v[124:127], v[40:43], 0\n" \
  "s_memrealtime s[38:39]\n" \
  "ds_read_b32 v37, v38\n" \
  "v_mfma_f32_16x16x16_bf16 v[64:67], v[136:137], v[44:45], v[64:67]\n" \
  "v_mfma_f32_16x16x16_bf16 v[68:71], v[140:141], v[44:45], v[68:71]\n" \
  "v_mfma_f32_16x16x16_bf16 v[72:75], v[136:137], v[46:47], v[72:75]\n" \
  "v_mfma_f32_16x16x16_bf16 v[76:79], v[140:141], v[46:47], v[76:79]\n" \
  "v_mfma_f32_16x16x16_bf16 v[80:83], v[138:139], v[44:45], v[80:83]\n" \
  "v_mfma_f32_16x16x16_bf16 v[84:87], v[142:143], v[44:45], v[84:87]\n" \
  "v_mfma_f32_16x16x16_bf16 v[88:91], v[138:139], v[46:47], v[88:91]\n" \
  "v_mfma_f32_16x16x16_bf16 v[92:95], v[142:143], v[46:47], v[92:95]\n" \
  "s_nop 0\n" \
  "v_cvt_pk_bf16_f32 v160, v64, v65\n" \
  "v_cvt_pk_bf16_f32 v161, v66, v67\n" \
  "v_cvt_pk_bf16_f32 v162, v68, v69\n" \
  "v_cvt_pk_bf16_f32 v163, v70, v71\n" \
  "ds_write_b128 v7, v[160:163] offset:0\n" \
  "v_cvt_pk_bf16_f32 v164, v72, v73\n" \
  "v_cvt_pk_bf16_f32 v165, v74, v75\n" \
  "v_cvt_pk_bf16_f32 v166, v76, v77\n" \
  "v_cvt_pk_bf16_f32 v167, v78, v79\n" \
  "ds_write_b128 v7, v[164:167] offset:1024\n" \
  "v_cvt_pk_bf16_f32 v168, v80, v81\n" \
  "v_cvt_pk_bf16_f32 v169, v82, v83\n" \
  "v_cvt_pk_bf16_f32 v170, v84, v85\n" \
  "v_cvt_pk_bf16_f32 v171, v86, v87\n" \
  "ds_write_b128 v7, v[168:171] offset:2048\n" \
  "v_cvt_pk_bf16_f32 v172, v88, v89\n" \
  "v_cvt_pk_bf16_f32 v173, v90, v91\n" \
  "v_cvt_pk_bf16_f32 v174, v92, v93\n" \
  "v_cvt_pk_bf16_f32 v175, v94, v95\n" \
  "ds_write_b128 v7, v[172:175] offset:3072\n" \
  "v_xor_b32 v7, 0x4000, v7\n" \
  "v_mfma_f32_16x16x32_bf16 v[112:115], v[192:195], v[160:163], 0\n" \
  "v_mfma_f32_16x16x32_bf16 v[116:119], v[224:227], v[160:163], 0\n" \
  "s_waitcnt lgkmcnt(0)\n" \
  "s_barrier\n" \
  "v_mfma_f32_16x16x32_bf16 v[112:115], v[196:199], v[164:167], v[112:115]\n" \
  "ds_read_b128 v[64:67], v16 offset:0\n" \
  "ds_read_b128 v[68:71], v16 offset:1024\n" \
  "ds_read_b128 v[72:75], v16 offset:2048\n" \
  "ds_read_b128 v[76:79], v16 offset:3072\n" \
  "s_sub_u32 s59, s60, 1\n" \
  "s_bfe_u32 s59, s59, 0x10003\n" \
  "v_readfirstlane_b32 s73, v37\n" \
  "s_sub_u32 s63, s38, s69\n" \
  "s_bfe_u32 s64, s73, 0x80010\n" \
  "s_sub_u32 s64, s64, s67\n" \
  "s_or_b32 s63, s63, s64\n" \
  "s_bfe_u32 s64, s73, 0x80008\n" \
  "s_and_b32 s73, s73, 0xff\n" \
  "s_min_u32 s73, s73, s64\n" \
  "s_or_b32 s63, s63, s68\n" \
  "s_cmp_lt_i32 s63, 0\n" \
  "s_cbranch_scc1 .LCX_E4_%=\n" \
  "v_mov_b32 v35, 0\n" \
  "global_load_dwordx4 v[128:131], v5, s[48:49] offset:0 sc1\n" \
  "global_load_dwordx4 v[132:135], v5, s[48:49] offset:1024 sc1\n" \
  "global_load_dwordx4 v[136:139], v3, s[48:49] offset:0 sc1\n" \
  "global_load_dwordx4 v[140:143], v3, s[48:49] offset:512 sc1\n" \
  "global_load_dword v35, v35, s[50:51] sc1\n" \
  "global_load_dword v146, v2, s[52:53] sc1\n" \
  "s_mov_b32 s68, -1\n" \
  ".LCX_E4_%=:\n" \
  "v_mfma_f32_16x16x32_bf16 v[116:119], v[228:231], v[164:167], v[116:119]\n" \
  "v_mfma_f32_16x16x32_bf16 v[112:115], v[200:203], v[168:171], v[112:115]\n" \
  "v_mfma_f32_16x16x32_bf16 v[116:119], v[232:235], v[168:171], v[116:119]\n" \
  "v_mfma_f32_16x16x32_bf16 v[112:115], v[204:207], v[172:175], v[112:115]\n" \
  "v_mfma_f32_16x16x32_bf16 v[116:119], v[236:239], v[172:175], v[116:119]\n" \
  "s_waitcnt lgkmcnt(3)\n" \
  "v_mfma_f32_16x16x32_bf16 v[120:123], v[192:195], v[64:67], 0\n" \
  "v_mfma_f32_16x16x32_bf16 v[124:127], v[224:227], v[64:67], 0\n" \
  "ds_read_b128 v[80:83], v17 offset:0\n" \
  "s_waitcnt lgkmcnt(3)\n" \
  "v_mfma_f32_16x16x32_bf16 v[120:123], v[196:199], v[68:71], v[120:123]\n" \
  "v_mfma_f32_16x16x32_bf16 v[124:127], v[228:231], v[68:71], v[124:127]\n" \
  "ds_read_b128 v[84:87], v17 offset:1024\n" \
  "s_waitcnt lgkmcnt(3)\n" \
  "v_mfma_f32_16x16x32_bf16 v[120:123], v[200:203], v[72:75], v[120:123]\n" \
  "v_mfma_f32_16x16x32_bf16 v[124:127], v[232:235], v[72:75], v[124:127]\n" \
  "ds_read_b128 v[88:91], v17 offset:2048\n" \
  "s_waitcnt lgkmcnt(3)\n" \
  "v_mfma_f32_16x16x32_bf16 v[120:123], v[204:207], v[76:79], v[120:123]\n" \
  "v_mfma_f32_16x16x32_bf16 v[124:127], v[236:239], v[76:79], v[124:127]\n" \
  "ds_read_b128 v[92:95], v17 offset:3072\n" \
  "s_waitcnt lgkmcnt(3)\n" \
  "v_mfma_f32_16x16x32_bf16 v[112:115], v[208:211], v[80:83], v[112:115]\n" \
  "v_mfma_f32_16x16x32_bf16 v[116:119], v[240:243], v[80:83], v[116:119]\n" \
  "ds_read_b128 v[96:99], v18 offset:0\n" \
  "s_waitcnt lgkmcnt(3)\n" \
  "v_mfma_f32_16x16x32_bf16 v[112:115], v[212:215], v[84:87], v[112:115]\n" \
  "v_mfma_f32_16x16x32_bf16 v[116:119], v[244:247], v[84:87], v[116:119]\n" \
  "ds_read_b128 v[100:103], v18 offset:1024\n" \
  "s_waitcnt lgkmcnt(3)\n" \
  "v_mfma_f32_16x16x32_bf16 v[112:115], v[216:219], v[88:91], v[112:115]\n" \
  "v_mfma_f32_16x16x32_bf16 v[116:119], v[248:251], v[88:91], v[116:119]\n" \
  "ds_read_b128 v[104:107], v18 offset:2048\n" \
  "s_waitcnt lgkmcnt(3)\n" \
  "v_mfma_f32_16x16x32_bf16 v[112:115], v[220:223], v[92:95], v[112:115]\n" \
  "v_mfma_f32_16x16x32_bf16 v[116:119], v[252:255], v[92:95], v[116:119]\n" \
  "ds_read_b128 v[108:111], v18 offset:3072\n" \
  "s_waitcnt lgkmcnt(3)\n" \
  "v_mfma_f32_16x16x32_bf16 v[120:123], v[208:211], v[96:99], v[120:123]\n" \
  "v_mfma_f32_16x16x32_bf16 v[124:127], v[240:243], v[96:99], v[124:127]\n" \
  "s_memrealtime s[38:39]\n" \
  "ds_read_b32 v37, v38\n" \
  "s_waitcnt lgkmcnt(3)\n" \
  "v_mfma_f32_16x16x32_bf16 v[120:123], v[212:215], v[100:103], v[120:123]\n" \
  "v_mfma_f32_16x16x32_bf16 v[124:127], v[244:247], v[100:103], v[124:127]\n" \
  "v_cvt_pk_bf16_f32 v20, v112, v113\n" \
  "v_cvt_pk_bf16_f32 v21, v114, v115\n" \
  "v_cvt_pk_bf16_f32 v22, v116, v117\n" \
  "v_cvt_pk_bf16_f32 v23, v118, v119\n" \
  "v_and_or_b32 v20, v20, -2, s59\n" \
  "s_waitcnt lgkmcnt(2)\n" \
  "v_mfma_f32_16x16x32_bf16 v[120:123], v[216:219], v[104:107], v[120:123]\n" \
  "v_mfma_f32_16x16x32_bf16 v[124:127], v[248:251], v[104:107], v[124:127]\n" \
  "buffer_store_dwordx4 v[20:23], v4, s[40:43], s54 offen sc1\n" \
  "s_waitcnt lgkmcnt(0)\n" \
  "v_mfma_f32_16x16x32_bf16 v[120:123], v[220:223], v[108:111], v[120:123]\n" \
  "v_mfma_f32_16x16x32_bf16 v[124:127], v[252:255], v[108:111], v[124:127]\n" \
  "s_xor_b32 s64, s54, 16384\n" \
  "s_mov_b32 s61, 1\n" \
  "s_nop 3\n" \
  "v_cvt_pk_bf16_f32 v24, v120, v121\n" \
  "v_cvt_pk_bf16_f32 v25, v122, v123\n" \
  "v_cvt_pk_bf16_f32 v26, v124, v125\n" \
  "v_cvt_pk_bf16_f32 v27, v126, v127\n" \
  "v_and_or_b32 v24, v24, -2, s59\n" \
  "buffer_store_dwordx4 v[24:27], v4, s[40:43], s64 offen sc1\n" \
  "v_readfirstlane_b32 s73, v37\n" \
  "s_sub_u32 s63, s38, s69\n" \
  "s_bfe_u32 s64, s73, 0x80010\n" \
  "s_sub_u32 s64, s64, s67\n" \
  "s_or_b32 s63, s63, s64\n" \
  "s_bfe_u32 s64, s73, 0x80008\n" \
  "s_and_b32 s73, s73, 0xff\n" \
  "s_min_u32 s73, s73, s64\n" \
  "s_or_b32 s63, s63, s68\n" \
  "s_cmp_lt_i32 s63, 0\n" \
  "s_cbranch_scc1 .LCX_E5_%=\n" \
  "v_mov_b32 v35, 0\n" \
  "global_load_dwordx4 v[128:131], v5, s[48:49] offset:0 sc1\n" \
  "global_load_dwordx4 v[132:135], v5, s[48:49] offset:1024 sc1\n" \
  "global_load_dwordx4 v[136:139], v3, s[48:49] offset:0 sc1\n" \
  "global_load_dwordx4 v[140:143], v3, s[48:49] offset:512 sc1\n" \
  "global_load_dword v35, v35, s[50:51] sc1\n" \
  "global_load_dword v146, v2, s[52:53] sc1\n" \
  "s_mov_b32 s68, -2\n" \
  ".LCX_E5_%=:\n" \
  "s_setprio 0\n" \
  "s_add_u32 s54, s54, 0x80000\n" \
  "s_bitset0_b32 s54, 22\n" \
  "s_sub_u32 s60, s60, 1\n" \
  "s_cmp_lg_u32 s60, 0\n" \
  "s_cbranch_scc1 .LCX_TOP_%=\n" \
  "s_waitcnt vmcnt(0)\n" \
  ".LCX_END_%=:\n" \
  "s_endpgm\n" \
  ".LCX_FAIL_%=:\n" \
  "v_mov_b64 v[28:29], 0\n" \
  "v_mov_b64 v[30:31], 0\n" \
  ".LCX_FAIL2_%=:\n" \
  "s_add_u32 s69, s64, -52\n" \
  "s_cmp_lg_u32 s64, 0\n" \
  "s_cbranch_scc1 .LCX_WF_%=\n" \
  "s_sleep 4\n" \
  "global_load_dword v35, v35, s[50:51] sc1\n" \
  "s_sub_u32 s62, s62, 8\n" \
  "s_cbranch_scc1 .LCX_END_%=\n" \
  "s_waitcnt vmcnt(0)\n" \
  "v_readfirstlane_b32 s64, v35\n" \
  "s_branch .LCX_FAIL2_%=\n"
#define AW_ASM_Y \
  "s_sub_u32 s78, s77, 1\n" \
  "s_mov_b32 s72, 0\n" \
  "s_mov_b32 s67, 0\n" \
  "s_mov_b32 s68, 2\n" \
  "s_mov_b32 s69, 0\n" \
  "s_mov_b32 s65, 49152\n" \
  "s_mov_b32 s66, 16384\n" \
  "v_mov_b32 v5, s58\n" \
  "v_mov_b32 v60, v3\n" \
  "v_add_u32 v3, 4096, v2\n" \
  "s_mov_b32 s62, 0x40000\n" \
  "s_setprio 1\n" \
  "v_cmp_eq_u32 vcc, 0, v4\n" \
  "s_cmp_eq_u64 vcc, 0\n" \
  "s_cbranch_scc1 .LCY_GET_%=\n" \
  "s_branch .LCY_ASK_%=\n" \
  ".p2align 6\n" \
  ".LCY_BAR_%=:\n" \
  "s_barrier\n" \
  "s_memrealtime s[38:39]\n" \
  "ds_read_b128 v[8:11], v0 offset:0\n" \
  "ds_read_b128 v[12:15], v0 offset:1024\n" \
  "ds_read_b128 v[16:19], v0 offset:2048\n" \
  "s_sub_u32 s59, s60, 1\n" \
  "s_bfe_u32 s59, s59, 0x10003\n" \
  "s_add_u32 s68, s68, 1\n" \
  "s_add_u32 s64, s54, 16384\n" \
  "s_cmp_eq_u32 s69, 0\n" \
  "s_cbranch_scc1 .LCY_NI_%=\n" \
  "s_waitcnt vmcnt(3)\n" \
  "s_cmp_lg_u32 s72, 0\n" \
  "s_cbranch_scc1 .LCY_NJ_%=\n" \
  "s_waitcnt vmcnt(0)\n" \
  ".LCY_NJ_%=:\n" \
  "v_mov_b32 v6, s55\n" \
  "s_mov_b64 exec, 1\n" \
  "ds_add_u32 v5, v6\n" \
  "s_mov_b64 exec, -1\n" \
  "s_mov_b32 s69, 0\n" \
  ".LCY_NI_%=:\n" \
  "s_waitcnt lgkmcnt(0)\n" \
  "s_add_u32 s63, s38, 205\n" \
  "s_mov_b32 s72, 0\n" \
  "v_or_b32 v7, 1, s63\n" \
  "s_mov_b64 exec, 1\n" \
  "global_store_dword v2, v7, s[52:53] sc1\n" \
  "s_mov_b64 exec, -1\n" \
  "s_add_u32 s52, s52, 2048\n" \
  "s_addc_u32 s53, s53, 0\n" \
  "v_cmp_eq_u32 vcc, 0, v4\n" \
  "s_cmp_eq_u64 vcc, 0\n" \
  "s_cbranch_scc0 .LCY_NS_%=\n" \
  "v_lshlrev_b32 v6, 16, s55\n" \
  "s_mov_b64 exec, 1\n" \
  "ds_add_u32 v5, v6\n" \
  "s_mov_b64 exec, -1\n" \
  "s_mov_b32 m0, s56\n" \
  "s_add_u32 s71, s56, 4096\n" \
  "global_load_lds_dwordx4 v2, s[48:49] offset:0 sc1\n" \
  "global_load_lds_dwordx4 v2, s[48:49] offset:1024 sc1\n" \
  "global_load_lds_dwordx4 v2, s[48:49] offset:2048 sc1\n" \
  "global_load_lds_dwordx4 v2, s[48:49] offset:3072 sc1\n" \
  ".LCY_NS_%=:\n" \
  "ds_read_b128 v[20:23], v0 offset:3072\n" \
  "v_mfma_f32_16x16x32_bf16 v[32:35], v[64:67], v[8:11], 0\n" \
  "v_mfma_f32_16x16x32_bf16 v[36:39], v[96:99], v[8:11], 0\n" \
  "v_mfma_f32_16x16x32_bf16 v[40:43], v[128:131], v[8:11], 0\n" \
  "v_mfma_f32_16x16x32_bf16 v[44:47], v[160:163], v[8:11], 0\n" \
  "v_mfma_f32_16x16x32_bf16 v[48:51], v[192:195], v[8:11], 0\n" \
  "v_mfma_f32_16x16x32_bf16 v[52:55], v[224:227], v[8:11], 0\n" \
  "ds_read_b128 v[8:11], v0 offset:8192\n" \
  "v_mfma_f32_16x16x32_bf16 v[32:35], v[68:71], v[12:15], v[32:35]\n" \
  "v_mfma_f32_16x16x32_bf16 v[36:39], v[100:103], v[12:15], v[36:39]\n" \
  "v_mfma_f32_16x16x32_bf16 v[40:43], v[132:135], v[12:15], v[40:43]\n" \
  "v_mfma_f32_16x16x32_bf16 v[44:47], v[164:167], v[12:15], v[44:47]\n" \
  "v_mfma_f32_16x16x32_bf16 v[48:51], v[196:199], v[12:15], v[48:51]\n" \
  "v_mfma_f32_16x16x32_bf16 v[52:55], v[228:231], v[12:15], v[52:55]\n" \
  "s_cbranch_vccnz .LCY_D1_%=\n" \
  "s_mov_b32 m0, s71\n" \
  "s_add_u32 s56, s56, s65\n" \
  "global_load_lds_dwordx4 v3, s[48:49] offset:0 sc1\n" \
  ".LCY_D1_%=:\n" \
  "ds_read_b128 v[12:15], v0 offset:9216\n" \
  "v_mfma_f32_16x16x32_bf16 v[32:35], v[72:75], v[16:19], v[32:35]\n" \
  "v_mfma_f32_16x16x32_bf16 v[36:39], v[104:107], v[16:19], v[36:39]\n" \
  "v_mfma_f32_16x16x32_bf16 v[40:43], v[136:139], v[16:19], v[40:43]\n" \
  "v_mfma_f32_16x16x32_bf16 v[44:47], v[168:171], v[16:19], v[44:47]\n" \
  "v_mfma_f32_16x16x32_bf16 v[48:51], v[200:203], v[16:19], v[48:51]\n" \
  "v_mfma_f32_16x16x32_bf16 v[52:55], v[232:235], v[16:19], v[52:55]\n" \
  "s_cbranch_vccnz .LCY_D2_%=\n" \
  "global_load_lds_dwordx4 v3, s[48:49] offset:1024 sc1\n" \
  ".LCY_D2_%=:\n" \
  "ds_read_b128 v[16:19], v0 offset:10240\n" \
  "s_waitcnt lgkmcnt(3)\n" \
  "v_mfma_f32_16x16x32_bf16 v[32:35], v[76:79], v[20:23], v[32:35]\n" \
  "v_mfma_f32_16x16x32_bf16 v[36:39], v[108:111], v[20:23], v[36:39]\n" \
  "v_mfma_f32_16x16x32_bf16 v[40:43], v[140:143], v[20:23], v[40:43]\n" \
  "v_mfma_f32_16x16x32_bf16 v[44:47], v[172:175], v[20:23], v[44:47]\n" \
  "v_mfma_f32_16x16x32_bf16 v[48:51], v[204:207], v[20:23], v[48:51]\n" \
  "v_mfma_f32_16x16x32_bf16 v[52:55], v[236:239], v[20:23], v[52:55]\n" \
  "s_cbranch_vccnz .LCY_D3_%=\n" \
  "global_load_lds_dwordx4 v3, s[48:49] offset:2048 sc1\n" \
  ".LCY_D3_%=:\n" \
  "ds_read_b128 v[20:23], v0 offset:11264\n" \
  "s_waitcnt lgkmcnt(3)\n" \
  "v_mfma_f32_16x16x32_bf16 v[32:35], v[80:83], v[8:11], v[32:35]\n" \
  "v_mfma_f32_16x16x32_bf16 v[36:39], v[112:115], v[8:11], v[36:39]\n" \
  "v_mfma_f32_16x16x32_bf16 v[40:43], v[144:147], v[8:11], v[40:43]\n" \
  "v_mfma_f32_16x16x32_bf16 v[44:47], v[176:179], v[8:11], v[44:47]\n" \
  "v_mfma_f32_16x16x32_bf16 v[48:51], v[208:211], v[8:11], v[48:51]\n" \
  "v_mfma_f32_16x16x32_bf16 v[52:55], v[240:243], v[8:11], v[52:55]\n" \
  "s_cbranch_vccnz .LCY_D4_%=\n" \
  "global_load_lds_dwordx4 v3, s[48:49] offset:3072 sc1\n" \
  "s_add_u32 s48, s48, 0x80000\n" \
  "s_addc_u32 s49, s49, 0\n" \
  ".LCY_D4_%=:\n" \
  "ds_read_b128 v[8:11], v0 offset:4096\n" \
  "s_waitcnt lgkmcnt(3)\n" \
  "v_mfma_f32_16x16x32_bf16 v[32:35], v[84:87], v[12:15], v[32:35]\n" \
  "v_mfma_f32_16x16x32_bf16 v[36:39], v[116:119], v[12:15], v[36:39]\n" \
  "v_mfma_f32_16x16x32_bf16 v[40:43], v[148:151], v[12:15], v[40:43]\n" \
  "v_mfma_f32_16x16x32_bf16 v[44:47], v[180:183], v[12:15], v[44:47]\n" \
  "v_mfma_f32_16x16x32_bf16 v[48:51], v[212:215], v[12:15], v[48:51]\n" \
  "v_mfma_f32_16x16x32_bf16 v[52:55], v[244:247], v[12:15], v[52:55]\n" \
  "s_cbranch_vccnz .LCY_AS_%=\n" \
  "s_mov_b32 m0, s57\n" \
  "s_add_u32 s57, s57, s65\n" \
  "global_load_lds_dwordx4 v2, s[50:51] offset:0 sc1\n" \
  ".LCY_D5_%=:\n" \
  "ds_read_b128 v[12:15], v0 offset:5120\n" \
  "s_waitcnt lgkmcnt(3)\n" \
  "v_mfma_f32_16x16x32_bf16 v[32:35], v[88:91], v[16:19], v[32:35]\n" \
  "v_mfma_f32_16x16x32_bf16 v[36:39], v[120:123], v[16:19], v[36:39]\n" \
  "v_mfma_f32_16x16x32_bf16 v[40:43], v[152:155], v[16:19], v[40:43]\n" \
  "v_mfma_f32_16x16x32_bf16 v[44:47], v[184:187], v[16:19], v[44:47]\n" \
  "v_mfma_f32_16x16x32_bf16 v[48:51], v[216:219], v[16:19], v[48:51]\n" \
  "v_mfma_f32_16x16x32_bf16 v[52:55], v[248:251], v[16:19], v[52:55]\n" \
  "s_cbranch_vccnz .LCY_D6_%=\n" \
  "global_load_lds_dwordx4 v2, s[50:51] offset:1024 sc1\n" \
  "s_sub_u32 s65, 0, s65\n" \
  "s_add_u32 s67, s67, 4096\n" \
  "s_bitset0_b32 s48, s77\n" \
  ".LCY_D6_%=:\n" \
  "ds_read_b128 v[16:19], v0 offset:6144\n" \
  "s_waitcnt lgkmcnt(3)\n" \
  "v_mfma_f32_16x16x32_bf16 v[32:35], v[92:95], v[20:23], v[32:35]\n" \
  "v_mfma_f32_16x16x32_bf16 v[36:39], v[124:127], v[20:23], v[36:39]\n" \
  "v_mfma_f32_16x16x32_bf16 v[40:43], v[156:159], v[20:23], v[40:43]\n" \
  "v_mfma_f32_16x16x32_bf16 v[44:47], v[188:191], v[20:23], v[44:47]\n" \
  "v_mfma_f32_16x16x32_bf16 v[48:51], v[220:223], v[20:23], v[48:51]\n" \
  "v_mfma_f32_16x16x32_bf16 v[52:55], v[252:255], v[20:23], v[52:55]\n" \
  "ds_read_b128 v[20:23], v0 offset:7168\n" \
  "s_waitcnt lgkmcnt(3)\n" \
  "s_nop 1\n" \
  "v_cvt_pk_bf16_f32 v24, v32, v33\n" \
  "v_cvt_pk_bf16_f32 v25, v34, v35\n" \
  "v_cvt_pk_bf16_f32 v26, v36, v37\n" \
  "v_cvt_pk_bf16_f32 v27, v38, v39\n" \
  "s_nop 0\n" \
  "v_mfma_f32_16x16x32_bf16 v[32:35], v[64:67], v[8:11], 0\n" \
  "v_mfma_f32_16x16x32_bf16 v[36:39], v[96:99], v[8:11], 0\n" \
  "v_and_or_b32 v24, v24, -2, s59\n" \
  "buffer_store_dwordx4 v[24:27], v1, s[40:43], s54 offen offset:0 sc1\n" \
  "v_cvt_pk_bf16_f32 v28, v40, v41\n" \
  "v_cvt_pk_bf16_f32 v29, v42, v43\n" \
  "v_cvt_pk_bf16_f32 v30, v44, v45\n" \
  "v_cvt_pk_bf16_f32 v31, v46, v47\n" \
  "s_nop 0\n" \
  "v_mfma_f32_16x16x32_bf16 v[40:43], v[128:131], v[8:11], 0\n" \
  "v_mfma_f32_16x16x32_bf16 v[44:47], v[160:163], v[8:11], 0\n" \
  "v_and_or_b32 v28, v28, -2, s59\n" \
  "buffer_store_dwordx4 v[28:31], v1, s[40:43], s54 offen offset:128 sc1\n" \
  "v_cvt_pk_bf16_f32 v56, v48, v49\n" \
  "v_cvt_pk_bf16_f32 v57, v50, v51\n" \
  "v_cvt_pk_bf16_f32 v58, v52, v53\n" \
  "v_cvt_pk_bf16_f32 v59, v54, v55\n" \
  "s_nop 0\n" \
  "v_mfma_f32_16x16x32_bf16 v[48:51], v[192:195], v[8:11], 0\n" \
  "v_mfma_f32_16x16x32_bf16 v[52:55], v[224:227], v[8:11], 0\n" \
  "v_and_or_b32 v56, v56, -2, s59\n" \
  "buffer_store_dwordx4 v[56:59], v1, s[40:43], s54 offen offset:256 sc1\n" \
  "s_cbranch_vccnz .LCY_AL0_%=\n" \
  ".LCY_AL1_%=:\n" \
  "ds_read_b128 v[8:11], v0 offset:12288\n" \
  "s_waitcnt lgkmcnt(3)\n" \
  "v_mfma_f32_16x16x32_bf16 v[32:35], v[68:71], v[12:15], v[32:35]\n" \
  "v_mfma_f32_16x16x32_bf16 v[36:39], v[100:103], v[12:15], v[36:39]\n" \
  "v_mfma_f32_16x16x32_bf16 v[40:43], v[132:135], v[12:15], v[40:43]\n" \
  "v_mfma_f32_16x16x32_bf16 v[44:47], v[164:167], v[12:15], v[44:47]\n" \
  "v_mfma_f32_16x16x32_bf16 v[48:51], v[196:199], v[12:15], v[48:51]\n" \
  "v_mfma_f32_16x16x32_bf16 v[52:55], v[228:231], v[12:15], v[52:55]\n" \
  "s_cbranch_vccnz .LCY_D7_%=\n" \
  "global_load_lds_dwordx4 v2, s[50:51] offset:2048 sc1\n" \
  "s_sub_u32 s68, s68, 1\n" \
  "s_mov_b32 s69, 1\n" \
  ".LCY_D7_%=:\n" \
  "ds_read_b128 v[12:15], v0 offset:13312\n" \
  "s_waitcnt lgkmcnt(3)\n" \
  "v_mfma_f32_16x16x32_bf16 v[32:35], v[72:75], v[16:19], v[32:35]\n" \
  "v_mfma_f32_16x16x32_bf16 v[36:39], v[104:107], v[16:19], v[36:39]\n" \
  "v_mfma_f32_16x16x32_bf16 v[40:43], v[136:139], v[16:19], v[40:43]\n" \
  "v_mfma_f32_16x16x32_bf16 v[44:47], v[168:171], v[16:19], v[44:47]\n" \
  "v_mfma_f32_16x16x32_bf16 v[48:51], v[200:203], v[16:19], v[48:51]\n" \
  "v_mfma_f32_16x16x32_bf16 v[52:55], v[232:235], v[16:19], v[52:55]\n" \
  "s_cbranch_vccnz .LCY_D8_%=\n" \
  "global_load_lds_dwordx4 v2, s[50:51] offset:3072 sc1\n" \
  "s_add_u32 s50, s50, 0x40000\n" \
  "s_addc_u32 s51, s51, 0\n" \
  "s_bitset0_b32 s50, s78\n" \
  ".LCY_D8_%=:\n" \
  "ds_read_b128 v[16:19], v0 offset:14336\n" \
  "s_waitcnt lgkmcnt(3)\n" \
  "v_mfma_f32_16x16x32_bf16 v[32:35], v[76:79], v[20:23], v[32:35]\n" \
  "v_mfma_f32_16x16x32_bf16 v[36:39], v[108:111], v[20:23], v[36:39]\n" \
  "v_mfma_f32_16x16x32_bf16 v[40:43], v[140:143], v[20:23], v[40:43]\n" \
  "v_mfma_f32_16x16x32_bf16 v[44:47], v[172:175], v[20:23], v[44:47]\n" \
  "v_mfma_f32_16x16x32_bf16 v[48:51], v[204:207], v[20:23], v[48:51]\n" \
  "v_mfma_f32_16x16x32_bf16 v[52:55], v[236:239], v[20:23], v[52:55]\n" \
  "ds_read_b128 v[20:23], v0 offset:15360\n" \
  "s_waitcnt lgkmcnt(3)\n" \
  "v_mfma_f32_16x16x32_bf16 v[32:35], v[80:83], v[8:11], v[32:35]\n" \
  "v_mfma_f32_16x16x32_bf16 v[36:39], v[112:115], v[8:11], v[36:39]\n" \
  "v_mfma_f32_16x16x32_bf16 v[40:43], v[144:147], v[8:11], v[40:43]\n" \
  "v_mfma_f32_16x16x32_bf16 v[44:47], v[176:179], v[8:11], v[44:47]\n" \
  "v_mfma_f32_16x16x32_bf16 v[48:51], v[208:211], v[8:11], v[48:51]\n" \
  "v_mfma_f32_16x16x32_bf16 v[52:55], v[240:243], v[8:11], v[52:55]\n" \
  "s_waitcnt lgkmcnt(2)\n" \
  "v_mfma_f32_16x16x32_bf16 v[32:35], v[84:87], v[12:15], v[32:35]\n" \
  "v_mfma_f32_16x16x32_bf16 v[36:39], v[116:119], v[12:15], v[36:39]\n" \
  "v_mfma_f32_16x16x32_bf16 v[40:43], v[148:151], v[12:15], v[40:43]\n" \
  "v_mfma_f32_16x16x32_bf16 v[44:47], v[180:183], v[12:15], v[44:47]\n" \
  "v_mfma_f32_16x16x32_bf16 v[48:51], v[212:215], v[12:15], v[48:51]\n" \
  "v_mfma_f32_16x16x32_bf16 v[52:55], v[244:247], v[12:15], v[52:55]\n" \
  "s_waitcnt lgkmcnt(1)\n" \
  "v_mfma_f32_16x16x32_bf16 v[32:35], v[88:91], v[16:19], v[32:35]\n" \
  "v_mfma_f32_16x16x32_bf16 v[36:39], v[120:123], v[16:19], v[36:39]\n" \
  "v_mfma_f32_16x16x32_bf16 v[40:43], v[152:155], v[16:19], v[40:43]\n" \
  "v_mfma_f32_16x16x32_bf16 v[44:47], v[184:187], v[16:19], v[44:47]\n" \
  "v_mfma_f32_16x16x32_bf16 v[48:51], v[216:219], v[16:19], v[48:51]\n" \
  "v_mfma_f32_16x16x32_bf16 v[52:55], v[248:251], v[16:19], v[52:55]\n" \
  "s_cbranch_vccnz .LCY_NT_%=\n" \
  "v_mov_b32 v4, 0\n" \
  "buffer_load_dword v4, v60, s[44:47], s67 offen sc1\n" \
  ".LCY_NT_%=:\n" \
  "s_waitcnt lgkmcnt(0)\n" \
  "v_mfma_f32_16x16x32_bf16 v[32:35], v[92:95], v[20:23], v[32:35]\n" \
  "v_mfma_f32_16x16x32_bf16 v[36:39], v[124:127], v[20:23], v[36:39]\n" \
  "v_mfma_f32_16x16x32_bf16 v[40:43], v[156:159], v[20:23], v[40:43]\n" \
  "v_mfma_f32_16x16x32_bf16 v[44:47], v[188:191], v[20:23], v[44:47]\n" \
  "v_mfma_f32_16x16x32_bf16 v[48:51], v[220:223], v[20:23], v[48:51]\n" \
  "v_mfma_f32_16x16x32_bf16 v[52:55], v[252:255], v[20:23], v[52:55]\n" \
  ".LCY_JOIN_%=:\n" \
  "v_add_u32 v0, s66, v0\n" \
  "s_sub_u32 s66, 0, s66\n" \
  "s_add_u32 s54, s54, 0x80000\n" \
  "s_bitset0_b32 s54, 22\n" \
  "s_mov_b32 s62, 0x40000\n" \
  "s_nop 0\n" \
  "v_cvt_pk_bf16_f32 v24, v32, v33\n" \
  "v_cvt_pk_bf16_f32 v25, v34, v35\n" \
  "v_cvt_pk_bf16_f32 v26, v36, v37\n" \
  "v_cvt_pk_bf16_f32 v27, v38, v39\n" \
  "v_and_or_b32 v24, v24, -2, s59\n" \
  "buffer_store_dwordx4 v[24:27], v1, s[40:43], s64 offen offset:0 sc1\n" \
  "v_cvt_pk_bf16_f32 v28, v40, v41\n" \
  "v_cvt_pk_bf16_f32 v29, v42, v43\n" \
  "v_cvt_pk_bf16_f32 v30, v44, v45\n" \
  "v_cvt_pk_bf16_f32 v31, v46, v47\n" \
  "v_and_or_b32 v28, v28, -2, s59\n" \
  "buffer_store_dwordx4 v[28:31], v1, s[40:43], s64 offen offset:128 sc1\n" \
  "v_cvt_pk_bf16_f32 v56, v48, v49\n" \
  "v_cvt_pk_bf16_f32 v57, v50, v51\n" \
  "v_cvt_pk_bf16_f32 v58, v52, v53\n" \
  "v_cvt_pk_bf16_f32 v59, v54, v55\n" \
  "v_and_or_b32 v56, v56, -2, s59\n" \
  "buffer_store_dwordx4 v[56:59], v1, s[40:43], s64 offen offset:256 sc1\n" \
  "s_cbranch_vccnz .LCY_ND_%=\n" \
  "s_cmp_eq_u32 s68, 0\n" \
  "s_cselect_b32 s72, 1, 0\n" \
  "s_cbranch_scc1 .LCY_DEF_%=\n" \
  "s_waitcnt vmcnt(0)\n" \
  "v_mov_b32 v6, s55\n" \
  "s_mov_b64 exec, 1\n" \
  "ds_add_u32 v5, v6\n" \
  "s_mov_b64 exec, -1\n" \
  "s_mov_b32 s69, 0\n" \
  "s_branch .LCY_WC_%=\n" \
  ".LCY_ND_%=:\n" \
  "v_mov_b32 v4, 0\n" \
  "buffer_load_dword v4, v60, s[44:47], s67 offen sc1\n" \
  "s_waitcnt vmcnt(0)\n" \
  ".LCY_WC_%=:\n" \
  "s_branch .LCY_DEF_%=\n" \
  ".LCY_AS_%=:\n" \
  "s_sub_u32 s63, s60, 10\n" \
  "s_cmp_lt_u32 s63, 51\n" \
  "s_cbranch_scc1 .LCY_D5_%=\n" \
  "v_mov_b32 v4, 0\n" \
  "buffer_load_dword v4, v60, s[44:47], s67 offen sc1\n" \
  "s_branch .LCY_D5_%=\n" \
  ".LCY_AL0_%=:\n" \
  "s_sub_u32 s63, s60, 10\n" \
  "s_cmp_lt_u32 s63, 51\n" \
  "s_cbranch_scc1 .LCY_AL1_%=\n" \
  "s_bitcmp1_b32 s67, 18\n" \
  "s_cbranch_scc1 .LCY_AL1_%=\n" \
  "s_branch .LCY_ALT_%=\n" \
  ".LCY_DEF_%=:\n" \
  "s_sub_u32 s60, s60, 1\n" \
  "s_cmp_eq_u32 s60, 0\n" \
  "s_cbranch_scc1 .LCY_END_%=\n" \
  ".LCY_NEED_%=:\n" \
  "s_cmp_eq_u32 s68, 0\n" \
  "s_cbranch_scc1 .LCY_BAR_%=\n" \
  "v_cmp_eq_u32 vcc, 0, v4\n" \
  "s_cmp_eq_u64 vcc, 0\n" \
  "s_cbranch_scc1 .LCY_GET_%=\n" \
  "s_cmp_ge_u32 s67, 0x40000\n" \
  "s_cbranch_scc1 .LCY_BAR_%=\n" \
  "s_sleep 4\n" \
  "ds_read_b32 v6, v5 offset:16\n" \
  "s_waitcnt lgkmcnt(0)\n" \
  "v_readfirstlane_b32 s63, v6\n" \
  "s_add_u32 s63, s63, s60\n" \
  "s_cmp_ge_u32 s63, 66\n" \
  "s_cbranch_scc1 .LCY_BAR_%=\n" \
  ".LCY_ASK_%=:\n" \
  "v_mov_b32 v4, 0\n" \
  "buffer_load_dword v4, v60, s[44:47], s67 offen sc1\n" \
  "s_sub_u32 s62, s62, 1\n" \
  "s_cbranch_scc1 .LCY_END_%=\n" \
  "s_waitcnt vmcnt(0)\n" \
  "s_branch .LCY_NEED_%=\n" \
  ".LCY_GET_%=:\n" \
  "v_lshlrev_b32 v6, 16, s55\n" \
  "s_mov_b64 exec, 1\n" \
  "ds_add_u32 v5, v6\n" \
  "s_mov_b64 exec, -1\n" \
  "s_mov_b32 m0, s56\n" \
  "s_add_u32 s71, s56, 4096\n" \
  "global_load_lds_dwordx4 v2, s[48:49] offset:0 sc1\n" \
  "global_load_lds_dwordx4 v2, s[48:49] offset:1024 sc1\n" \
  "global_load_lds_dwordx4 v2, s[48:49] offset:2048 sc1\n" \
  "global_load_lds_dwordx4 v2, s[48:49] offset:3072 sc1\n" \
  "s_mov_b32 m0, s71\n" \
  "s_add_u32 s56, s56, s65\n" \
  "global_load_lds_dwordx4 v3, s[48:49] offset:0 sc1\n" \
  "global_load_lds_dwordx4 v3, s[48:49] offset:1024 sc1\n" \
  "global_load_lds_dwordx4 v3, s[48:49] offset:2048 sc1\n" \
  "global_load_lds_dwordx4 v3, s[48:49] offset:3072 sc1\n" \
  "s_add_u32 s48, s48, 0x80000\n" \
  "s_addc_u32 s49, s49, 0\n" \
  "s_mov_b32 m0, s57\n" \
  "s_add_u32 s57, s57, s65\n" \
  "global_load_lds_dwordx4 v2, s[50:51] offset:0 sc1\n" \
  "global_load_lds_dwordx4 v2, s[50:51] offset:1024 sc1\n" \
  "s_sub_u32 s65, 0, s65\n" \
  "s_add_u32 s67, s67, 4096\n" \
  "s_bitset0_b32 s48, s77\n" \
  "global_load_lds_dwordx4 v2, s[50:51] offset:2048 sc1\n" \
  "s_sub_u32 s68, s68, 1\n" \
  "s_mov_b32 s69, 1\n" \
  "global_load_lds_dwordx4 v2, s[50:51] offset:3072 sc1\n" \
  "s_add_u32 s50, s50, 0x40000\n" \
  "s_addc_u32 s51, s51, 0\n" \
  "s_bitset0_b32 s50, s78\n" \
  "v_mov_b32 v4, 0\n" \
  "buffer_load_dword v4, v60, s[44:47], s67 offen sc1\n" \
  "s_mov_b32 s61, 0\n" \
  "s_cmp_eq_u32 s68, 0\n" \
  "s_cbranch_scc1 .LCY_BAR_%=\n" \
  "s_waitcnt vmcnt(0)\n" \
  "v_mov_b32 v6, s55\n" \
  "s_mov_b64 exec, 1\n" \
  "ds_add_u32 v5, v6\n" \
  "s_mov_b64 exec, -1\n" \
  "s_mov_b32 s69, 0\n" \
  "s_branch .LCY_NEED_%=\n" \
  ".LCY_END_%=:\n" \
  "s_endpgm\n" \
  ".LCY_ALT_%=:\n" \
  "ds_read_b128 v[8:11], v0 offset:12288\n" \
  "s_waitcnt lgkmcnt(3)\n" \
  "v_mfma_f32_16x16x32_bf16 v[32:35], v[68:71], v[12:15], v[32:35]\n" \
  "v_mfma_f32_16x16x32_bf16 v[36:39], v[100:103], v[12:15], v[36:39]\n" \
  "s_waitcnt vmcnt(3)\n" \
  "v_cmp_eq_u32 vcc, 0, v4\n" \
  "v_mfma_f32_16x16x32_bf16 v[40:43], v[132:135], v[12:15], v[40:43]\n" \
  "s_cbranch_vccnz .LCY_D10_%=\n" \
  "v_lshlrev_b32 v6, 16, s55\n" \
  "s_mov_b64 exec, 1\n" \
  "ds_add_u32 v5, v6\n" \
  "s_mov_b64 exec, -1\n" \
  "s_mov_b32 m0, s56\n" \
  "s_add_u32 s71, s56, 4096\n" \
  "global_load_lds_dwordx4 v2, s[48:49] offset:0 sc1\n" \
  ".LCY_D10_%=:\n" \
  "v_mfma_f32_16x16x32_bf16 v[44:47], v[164:167], v[12:15], v[44:47]\n" \
  "v_mfma_f32_16x16x32_bf16 v[48:51], v[196:199], v[12:15], v[48:51]\n" \
  "v_mfma_f32_16x16x32_bf16 v[52:55], v[228:231], v[12:15], v[52:55]\n" \
  "s_cbranch_vccnz .LCY_D11_%=\n" \
  "global_load_lds_dwordx4 v2, s[48:49] offset:1024 sc1\n" \
  ".LCY_D11_%=:\n" \
  "ds_read_b128 v[12:15], v0 offset:13312\n" \
  "s_waitcnt lgkmcnt(3)\n" \
  "v_mfma_f32_16x16x32_bf16 v[32:35], v[72:75], v[16:19], v[32:35]\n" \
  "v_mfma_f32_16x16x32_bf16 v[36:39], v[104:107], v[16:19], v[36:39]\n" \
  "v_mfma_f32_16x16x32_bf16 v[40:43], v[136:139], v[16:19], v[40:43]\n" \
  "s_cbranch_vccnz .LCY_D12_%=\n" \
  "global_load_lds_dwordx4 v2, s[48:49] offset:2048 sc1\n" \
  ".LCY_D12_%=:\n" \
  "v_mfma_f32_16x16x32_bf16 v[44:47], v[168:171], v[16:19], v[44:47]\n" \
  "v_mfma_f32_16x16x32_bf16 v[48:51], v[200:203], v[16:19], v[48:51]\n" \
  "v_mfma_f32_16x16x32_bf16 v[52:55], v[232:235], v[16:19], v[52:55]\n" \
  "s_cbranch_vccnz .LCY_D13_%=\n" \
  "global_load_lds_dwordx4 v2, s[48:49] offset:3072 sc1\n" \
  ".LCY_D13_%=:\n" \
  "ds_read_b128 v[16:19], v0 offset:14336\n" \
  "s_waitcnt lgkmcnt(3)\n" \
  "v_mfma_f32_16x16x32_bf16 v[32:35], v[76:79], v[20:23], v[32:35]\n" \
  "v_mfma_f32_16x16x32_bf16 v[36:39], v[108:111], v[20:23], v[36:39]\n" \
  "v_mfma_f32_16x16x32_bf16 v[40:43], v[140:143], v[20:23], v[40:43]\n" \
  "s_cbranch_vccnz .LCY_D14_%=\n" \
  "s_mov_b32 m0, s71\n" \
  "s_add_u32 s56, s56, s65\n" \
  "global_load_lds_dwordx4 v3, s[48:49] offset:0 sc1\n" \
  ".LCY_D14_%=:\n" \
  "v_mfma_f32_16x16x32_bf16 v[44:47], v[172:175], v[20:23], v[44:47]\n" \
  "v_mfma_f32_16x16x32_bf16 v[48:51], v[204:207], v[20:23], v[48:51]\n" \
  "v_mfma_f32_16x16x32_bf16 v[52:55], v[236:239], v[20:23], v[52:55]\n" \
  "s_cbranch_vccnz .LCY_D15_%=\n" \
  "global_load_lds_dwordx4 v3, s[48:49] offset:1024 sc1\n" \
  ".LCY_D15_%=:\n" \
  "ds_read_b128 v[20:23], v0 offset:15360\n" \
  "s_waitcnt lgkmcnt(3)\n" \
  "v_mfma_f32_16x16x32_bf16 v[32:35], v[80:83], v[8:11], v[32:35]\n" \
  "v_mfma_f32_16x16x32_bf16 v[36:39], v[112:115], v[8:11], v[36:39]\n" \
  "v_mfma_f32_16x16x32_bf16 v[40:43], v[144:147], v[8:11], v[40:43]\n" \
  "s_cbranch_vccnz .LCY_D16_%=\n" \
  "global_load_lds_dwordx4 v3, s[48:49] offset:2048 sc1\n" \
  ".LCY_D16_%=:\n" \
  "v_mfma_f32_16x16x32_bf16 v[44:47], v[176:179], v[8:11], v[44:47]\n" \
  "v_mfma_f32_16x16x32_bf16 v[48:51], v[208:211], v[8:11], v[48:51]\n" \
  "v_mfma_f32_16x16x32_bf16 v[52:55], v[240:243], v[8:11], v[52:55]\n" \
  "s_cbranch_vccnz .LCY_D17_%=\n" \
  "global_load_lds_dwordx4 v3, s[48:49] offset:3072 sc1\n" \
  "s_add_u32 s48, s48, 0x80000\n" \
  "s_addc_u32 s49, s49, 0\n" \
  ".LCY_D17_%=:\n" \
  "s_waitcnt lgkmcnt(2)\n" \
  "v_mfma_f32_16x16x32_bf16 v[32:35], v[84:87], v[12:15], v[32:35]\n" \
  "v_mfma_f32_16x16x32_bf16 v[36:39], v[116:119], v[12:15], v[36:39]\n" \
  "v_mfma_f32_16x16x32_bf16 v[40:43], v[148:151], v[12:15], v[40:43]\n" \
  "s_cbranch_vccnz .LCY_D18_%=\n" \
  "s_mov_b32 m0, s57\n" \
  "s_add_u32 s57, s57, s65\n" \
  "global_load_lds_dwordx4 v2, s[50:51] offset:0 sc1\n" \
  ".LCY_D18_%=:\n" \
  "v_mfma_f32_16x16x32_bf16 v[44:47], v[180:183], v[12:15], v[44:47]\n" \
  "v_mfma_f32_16x16x32_bf16 v[48:51], v[212:215], v[12:15], v[48:51]\n" \
  "v_mfma_f32_16x16x32_bf16 v[52:55], v[244:247], v[12:15], v[52:55]\n" \
  "s_cbranch_vccnz .LCY_D19_%=\n" \
  "global_load_lds_dwordx4 v2, s[50:51] offset:1024 sc1\n" \
  "s_sub_u32 s65, 0, s65\n" \
  "s_add_u32 s67, s67, 4096\n" \
  "s_bitset0_b32 s48, s77\n" \
  ".LCY_D19_%=:\n" \
  "s_waitcnt lgkmcnt(1)\n" \
  "v_mfma_f32_16x16x32_bf16 v[32:35], v[88:91], v[16:19], v[32:35]\n" \
  "v_mfma_f32_16x16x32_bf16 v[36:39], v[120:123], v[16:19], v[36:39]\n" \
  "v_mfma_f32_16x16x32_bf16 v[40:43], v[152:155], v[16:19], v[40:43]\n" \
  "s_cbranch_vccnz .LCY_D20_%=\n" \
  "global_load_lds_dwordx4 v2, s[50:51] offset:2048 sc1\n" \
  "s_sub_u32 s68, s68, 1\n" \
  "s_mov_b32 s69, 1\n" \
  ".LCY_D20_%=:\n" \
  "v_mfma_f32_16x16x32_bf16 v[44:47], v[184:187], v[16:19], v[44:47]\n" \
  "v_mfma_f32_16x16x32_bf16 v[48:51], v[216:219], v[16:19], v[48:51]\n" \
  "v_mfma_f32_16x16x32_bf16 v[52:55], v[248:251], v[16:19], v[52:55]\n" \
  "s_cbranch_vccnz .LCY_D21_%=\n" \
  "global_load_lds_dwordx4 v2, s[50:51] offset:3072 sc1\n" \
  "s_add_u32 s50, s50, 0x40000\n" \
  "s_addc_u32 s51, s51, 0\n" \
  "s_bitset0_b32 s50, s78\n" \
  "v_mov_b32 v4, 0\n" \
  "buffer_load_dword v4, v60, s[44:47], s67 offen sc1\n" \
  ".LCY_D21_%=:\n" \
  "s_waitcnt lgkmcnt(0)\n" \
  "v_mfma_f32_16x16x32_bf16 v[32:35], v[92:95], v[20:23], v[32:35]\n" \
  "v_mfma_f32_16x16x32_bf16 v[36:39], v[124:127], v[20:23], v[36:39]\n" \
  "v_mfma_f32_16x16x32_bf16 v[40:43], v[156:159], v[20:23], v[40:43]\n" \
  "v_mfma_f32_16x16x32_bf16 v[44:47], v[188:191], v[20:23], v[44:47]\n" \
  "v_mfma_f32_16x16x32_bf16 v[48:51], v[220:223], v[20:23], v[48:51]\n" \
  "v_mfma_f32_16x16x32_bf16 v[52:55], v[252:255], v[20:23], v[52:55]\n" \
  "s_branch .LCY_JOIN_%=\n"
template <bool AT>
__device__ __forceinline__ void role_aw_t(const MegaArgs& A, char* smem, const int L, const int a, const u64 tag) {
  const int w = A.wv;
  const int l = lane_id(), c = l & 15, g = l >> 4;
  const int qt = w & 1, hh = (w >> 1) & 1, h = 2 * a + hh;
  char* ws = A.ws;
  unsigned* FL = (unsigned*)(ws + OFF_FL);
  const unsigned sb = lds_base(smem);
  if (w == 0 && l < 8) reinterpret_cast<unsigned*>(smem + AW_CNT)[l] = 0u;
  constexpr int NP = AT ? 1 : 3;
  const int cp0 = AT ? w : 3 * w - 8;
  v8u wo[2 * NP][4];
  const int dp = (FL_FP - FL_FQ + (L * 16 + a - L * 64 * 32 - h) * 32 + 2 * hh) * 4;
  const unsigned fo = (l & 2) ? 16u + 8 * qt : qt ? 8u * (l & 1) : dp + 4 * (l & 1);
  unsigned nf = 0u;
  {
    const int q = (l >> 2) & 3, p = l & 3, f = q | ((g & 1) << 2);
    const unsigned ra = sb + (4 * g + q) * 256 + (p & 1) * 16;
#define PW_CLEAR(n)                                                                                                         \
  _Pragma("unroll") for (int i = 0; i < 16; ++i)                                                                            \
    ST128((v4u{0u, 0u, 0u, 0u}), MKRS(ws + OFF_PW, SZ_PW), l * 16 + (i & 3) * 1024,                                         \
          ((((n) >> RGB) * 64 + ((n) & ((1 << RGB) - 1))) * 32 + 2 * a + qt) * 16384 + (i >> 2) * 4096);
    if (EF && AT && L && PC0 == 0) { PW_CLEAR(hh) }
#pragma unroll
    for (int r = 0; r < 2; ++r) {
#pragma unroll
      for (int k = 0; k < 2; ++k) {
        const char* base_ = (const char*)A.Wo + ((size_t)((2 * a + r) * DH + 64 * (w & 1) + 32 * k) * DM + 128 * (w >> 1)) * 2;
#pragma unroll
        for (int i = 0; i < 8; ++i) {
          const int fd = g | ((i & 1) << 2);
          glds16(base_, (unsigned)((4 * i + g) * (DM * 2) + ((c ^ (fd << 1)) << 4)), sb + w * 16384 + k * 8192 + i * 1024, WNT & 16);
        }
      }
      if (r == 1 && AT) TOUCH("CX");
      if (r == 1 && !AT) TOUCH("CY");
      SST(4 + 3 * r);
      if (EF && AT && L && PC0 == 1 + r) { PW_CLEAR(hh) }
      if (r == 1) ready_wait(A, tag, ready_look(A));
      if (EF && AT && L && PC0 == 1 && r == 0) WAIT_VM(16);
      else WAIT_VM(0);
      if (EF && AT && r == (PC0 != 0) && L) {
        if (r == 0) ready_wait(A, tag, ready_look(A));
        PW_FREE(0, hh, 2 * a + qt);
      }
      RAW_BAR();
      SST(5 + 3 * r);
#pragma unroll
      for (int k2 = 0; k2 < 2; ++k2) {
        if (!AT && r == 1 && k2 == YQ - 1 && !L) nf = __hip_atomic_load((gu32*)((char*)(FL + FL_FQ + h * 32) + fo), RLX_AGENT);
#pragma unroll
        for (int ct = 0; ct < 2 * NP; ++ct) {
          const int cp = cp0 + (ct >> 1);
          const unsigned a_ = ra + (cp >> 2) * 32768 + k2 * 16384 + ((2 * (cp & 3) + (p >> 1)) ^ f) * 32 + (ct & 1) * 8;
          const v2u r0 = trd(a_), r1 = trd(a_ + 4096), r2 = trd(a_ + 8192), r3 = trd(a_ + 12288);
          wo[ct][2 * r + k2] = v8u{r0.x, r0.y, r1.x, r1.y, r2.x, r2.y, r3.x, r3.y};
          asm volatile("" : "+v"(wo[ct][2 * r + k2]));
        }
      }
      RAW_BAR();
      SST(6 + 3 * r);
    }
  }
  WSTAMP2();
  if (EF && L) {
    const unsigned t0 = now10();
#pragma unroll 1
    for (int n = 2 + (w >> 1); n < (2 << RGB); n += 4) {
      gate(t0 + PCT + (n - 2) * PCD);
      PW_CLEAR(n)
      DRAIN();
      PW_FREE(n >> RGB, n & ((1 << RGB) - 1), 2 * a + qt);
    }
    if (AWD) gate(t0 + AWD);
  }
  const v4u rsW = mk_srd(ws + OFF_PW, SZ_PW);
  const v4u rsF = mk_srd(FL + FL_FQ + (L * 64 * 32 + h) * 32, (qt ? 0 : dp) + 64 * 4096 - 128);
  const char* p0 = AT ? ws + OFF_MEMK + L * SZ_MEM + h * 2048 : ws + TOK_QK(ws, L) + ((size_t)h * 16 + 8 * qt) * 1024;
  const char* p1 = AT ? (const char*)(FL + FL_FK + L * 32 + (h >> 3) * 64 + (h & 7) * 2) : ws + TOK_V(ws, L) + ((size_t)h * 8 + 4 * qt) * 1024;
  const unsigned* pFW = EF && AT ? FL + FL_FD + L * 32 : FL + FL_FW + (L * 64 * 16 + (a >> 2)) * 32 + (a & 3) + (EF ? (w & 3) * 128 : 0);
  const unsigned tk = sb + AW_TK + hh * 24 * 1024, voff = (c >> 1) * 2048 + (c & 1) * 64 + g * 16;
  const char* dbg = ws + WS_TOTAL + A.vb * 64 * 8;
#define AW_SOPS                                                                                                                 \
  "{s[40:43]}"(rsW), "{s[44:47]}"(rsF), "{s[48:49]}"(p0), "{s[50:51]}"(p1), "{s[52:53]}"(pFW),                                   \
      "{s54}"((L * 64 * 16 + a) * 32768 + cp0 * 128 + (AT ? qt * 16384 : 0)), "{s55}"(1 << (8 * qt)), "{s56}"(tk + qt * 8192), "{s57}"(tk + 16384 + qt * 4096),                  \
      "{s58}"(sb + AW_CNT + 8 + 4 * hh), "{s59}"(sb + AW_CNT), "{s60}"(NSEG), "{s77}"(L ? 19 + GEN_TR : 1), "{s[36:37]}"(dbg)             \
      : "memory", "vcc", "scc", "s80"
  if constexpr (AT) {
    const v8u pv = {tk + l * 16, (c & 8) ? 0xEEEEu : 0x4444u, (l & 31) * 4u, (unsigned)(OFF_MEMV - OFF_MEMK) + c * 32 + (g & 1) * 16 + (g >> 1) * 1024,
                    voff, (unsigned)l * 16, tk + qt * 4096 + l * 16, sb + AW_SO + w * 4096 + l * 16};
    const unsigned neg = 0xff61b1e6u;
    v8u bt;
#pragma unroll
    for (int i = 0; i < 4; ++i) {
      const unsigned d = 4 * g + i > c ? neg : 0u;
      bt[i] = qt ? 0u : d;
      bt[4 + i] = qt ? d : neg;
    }
    asm volatile("s_mov_b64 exec, -1\ns_waitcnt lgkmcnt(0)\ns_nop 4\n" AW_ASM_X ".LKG_LAST:\n"
                 ::"{v[192:199]}"(wo[0][0]), "{v[200:207]}"(wo[0][1]), "{v[208:215]}"(wo[0][2]), "{v[216:223]}"(wo[0][3]),
                 "{v[224:231]}"(wo[1][0]), "{v[232:239]}"(wo[1][1]), "{v[240:247]}"(wo[1][2]), "{v[248:255]}"(wo[1][3]),
                 "{v[0:7]}"(pv), "{v[8:15]}"(bt), AW_SOPS);
  } else {
    const v4u pv = {sb + AW_SO + l * 16, voff, (unsigned)l * 16, fo};
    asm volatile("s_mov_b64 exec, -1\ns_waitcnt vmcnt(0) lgkmcnt(0)\ns_nop 4\n" AW_ASM_Y
                 ::"{v[64:71]}"(wo[0][0]), "{v[72:79]}"(wo[0][1]), "{v[80:87]}"(wo[0][2]), "{v[88:95]}"(wo[0][3]),
                 "{v[96:103]}"(wo[1][0]), "{v[104:111]}"(wo[1][1]), "{v[112:119]}"(wo[1][2]), "{v[120:127]}"(wo[1][3]),
                 "{v[128:135]}"(wo[2][0]), "{v[136:143]}"(wo[2][1]), "{v[144:151]}"(wo[2][2]), "{v[152:159]}"(wo[2][3]),
                 "{v[160:167]}"(wo[3][0]), "{v[168:175]}"(wo[3][1]), "{v[176:183]}"(wo[3][2]), "{v[184:191]}"(wo[3][3]),
                 "{v[192:199]}"(wo[4][0]), "{v[200:207]}"(wo[4][1]), "{v[208:215]}"(wo[4][2]), "{v[216:223]}"(wo[4][3]),
                 "{v[224:231]}"(wo[5][0]), "{v[232:239]}"(wo[5][1]), "{v[240:247]}"(wo[5][2]), "{v[248:255]}"(wo[5][3]),
                 "{v4}"(nf), "{v[0:3]}"(pv), AW_SOPS);
  }
#undef AW_SOPS
}
__device__ __forceinline__ void role_aw(const MegaArgs& A, char* smem, const int L, const int a, const u64 tag) {
  if (A.wv < 4) role_aw_t<true>(A, smem, L, a, tag);
  else role_aw_t<false>(A, smem, L, a, tag);
}

#define P_ASM \
  "s_mov_b32 s68, 0\n" \
  "s_mov_b32 s66, 3\n" \
  "s_mov_b32 s67, 2\n" \
  "s_mov_b32 s72, 0\n" \
  "v_mov_b32 v7, 1\n" \
  "v_mov_b32 v6, s70\n" \
  "v_mov_b32 v5, 0\n" \
  "v_add_u32 v10, 4096, v2\n" \
  ".p2align 6\n" \
  ".LCP_TOP_%=:\n" \
  "ds_read_b128 v[28:31], v1 offset:0\n" \
  "ds_read_b128 v[32:35], v1 offset:1024\n" \
  "s_sub_u32 s73, s64, 1\n" \
  "s_bitset0_b32 s65, s74\n" \
  "s_bfe_u32 s73, s73, 0x10004\n" \
  "s_cmp_gt_u32 s64, s75\n" \
  "s_cselect_b32 s76, 1, 0\n" \
  "v_mov_b32 v11, s76\n" \
  "s_add_u32 s66, s66, 3\n" \
  "s_add_u32 s67, s67, 2\n" \
  "s_mov_b32 s69, 0x400000\n" \
  "v_xor_b32 v0, 0x8000, v0\n" \
  "v_xor_b32 v3, 0x100, v3\n" \
  "v_readfirstlane_b32 s71, v5\n" \
  "s_cmp_ge_u32 s71, s66\n" \
  "s_cbranch_scc1 .LCP_GO_%=\n" \
  "s_cmp_eq_u32 s68, 1\n" \
  "s_cbranch_scc0 .LCP_X1_%=\n" \
  "s_waitcnt vmcnt(0)\n" \
  "s_mov_b64 exec, 1\n" \
  "buffer_store_dword v7, off, s[56:59], s72 sc1\n" \
  "s_mov_b64 exec, -1\n" \
  "s_add_u32 s72, s72, 0x1000\n" \
  "s_mov_b32 s68, 0\n" \
  ".LCP_X1_%=:\n" \
  ".LCP_S2_%=:\n" \
  "s_sleep 1\n" \
  "ds_read_b32 v5, v6 offset:0\n" \
  "s_sub_u32 s69, s69, 1\n" \
  "s_cbranch_scc1 .LCP_END_%=\n" \
  "s_waitcnt lgkmcnt(0)\n" \
  "v_readfirstlane_b32 s71, v5\n" \
  "s_cmp_ge_u32 s71, s66\n" \
  "s_cbranch_scc1 .LCP_GO_%=\n" \
  "s_branch .LCP_S2_%=\n" \
  ".LCP_GO_%=:\n" \
  "ds_read_b128 v[12:15], v0 offset:0\n" \
  "ds_read_b128 v[16:19], v0 offset:2048\n" \
  "ds_read_b128 v[20:23], v0 offset:4096\n" \
  "ds_read_b128 v[24:27], v0 offset:6144\n" \
  "ds_read_b128 v[36:39], v1 offset:2048\n" \
  "s_waitcnt lgkmcnt(4)\n" \
  "v_mfma_f32_16x16x32_bf16 v[48:51], v[64:67], v[12:15], 0\n" \
  "v_mfma_f32_16x16x32_bf16 v[52:55], v[128:131], v[12:15], 0\n" \
  "v_mfma_f32_16x16x32_bf16 v[56:59], v[192:195], v[12:15], 0\n" \
  "v_mfma_f32_16x16x32_bf16 v[60:63], v[28:31], v[12:15], 0\n" \
  "ds_read_b128 v[12:15], v0 offset:8192\n" \
  "ds_read_b128 v[28:31], v1 offset:3072\n" \
  "s_waitcnt lgkmcnt(5)\n" \
  "v_mfma_f32_16x16x32_bf16 v[48:51], v[68:71], v[16:19], v[48:51]\n" \
  "v_mfma_f32_16x16x32_bf16 v[52:55], v[132:135], v[16:19], v[52:55]\n" \
  "v_mfma_f32_16x16x32_bf16 v[56:59], v[196:199], v[16:19], v[56:59]\n" \
  "v_mfma_f32_16x16x32_bf16 v[60:63], v[32:35], v[16:19], v[60:63]\n" \
  "ds_read_b128 v[16:19], v0 offset:10240\n" \
  "ds_read_b128 v[32:35], v1 offset:4096\n" \
  "s_waitcnt lgkmcnt(6)\n" \
  "v_mfma_f32_16x16x32_bf16 v[48:51], v[72:75], v[20:23], v[48:51]\n" \
  "v_mfma_f32_16x16x32_bf16 v[52:55], v[136:139], v[20:23], v[52:55]\n" \
  "v_mfma_f32_16x16x32_bf16 v[56:59], v[200:203], v[20:23], v[56:59]\n" \
  "s_waitcnt lgkmcnt(4)\n" \
  "v_mfma_f32_16x16x32_bf16 v[60:63], v[36:39], v[20:23], v[60:63]\n" \
  "ds_read_b128 v[20:23], v0 offset:12288\n" \
  "ds_read_b128 v[36:39], v1 offset:5120\n" \
  "v_mfma_f32_16x16x32_bf16 v[48:51], v[76:79], v[24:27], v[48:51]\n" \
  "v_mfma_f32_16x16x32_bf16 v[52:55], v[140:143], v[24:27], v[52:55]\n" \
  "v_mfma_f32_16x16x32_bf16 v[56:59], v[204:207], v[24:27], v[56:59]\n" \
  "s_waitcnt lgkmcnt(4)\n" \
  "v_mfma_f32_16x16x32_bf16 v[60:63], v[28:31], v[24:27], v[60:63]\n" \
  "ds_read_b128 v[24:27], v0 offset:14336\n" \
  "ds_read_b128 v[28:31], v1 offset:6144\n" \
  "v_mfma_f32_16x16x32_bf16 v[48:51], v[80:83], v[12:15], v[48:51]\n" \
  "v_mfma_f32_16x16x32_bf16 v[52:55], v[144:147], v[12:15], v[52:55]\n" \
  "v_mfma_f32_16x16x32_bf16 v[56:59], v[208:211], v[12:15], v[56:59]\n" \
  "s_waitcnt lgkmcnt(4)\n" \
  "v_mfma_f32_16x16x32_bf16 v[60:63], v[32:35], v[12:15], v[60:63]\n" \
  "ds_read_b128 v[12:15], v0 offset:16384\n" \
  "ds_read_b128 v[32:35], v1 offset:7168\n" \
  "v_mfma_f32_16x16x32_bf16 v[48:51], v[84:87], v[16:19], v[48:51]\n" \
  "v_mfma_f32_16x16x32_bf16 v[52:55], v[148:151], v[16:19], v[52:55]\n" \
  "v_mfma_f32_16x16x32_bf16 v[56:59], v[212:215], v[16:19], v[56:59]\n" \
  "s_waitcnt lgkmcnt(4)\n" \
  "v_mfma_f32_16x16x32_bf16 v[60:63], v[36:39], v[16:19], v[60:63]\n" \
  "ds_read_b128 v[16:19], v0 offset:18432\n" \
  "ds_read_b128 v[36:39], v1 offset:8192\n" \
  "v_mfma_f32_16x16x32_bf16 v[48:51], v[88:91], v[20:23], v[48:51]\n" \
  "v_mfma_f32_16x16x32_bf16 v[52:55], v[152:155], v[20:23], v[52:55]\n" \
  "v_mfma_f32_16x16x32_bf16 v[56:59], v[216:219], v[20:23], v[56:59]\n" \
  "s_waitcnt lgkmcnt(4)\n" \
  "v_mfma_f32_16x16x32_bf16 v[60:63], v[28:31], v[20:23], v[60:63]\n" \
  "ds_read_b128 v[20:23], v0 offset:20480\n" \
  "ds_read_b128 v[28:31], v1 offset:9216\n" \
  "v_mfma_f32_16x16x32_bf16 v[48:51], v[92:95], v[24:27], v[48:51]\n" \
  "v_mfma_f32_16x16x32_bf16 v[52:55], v[156:159], v[24:27], v[52:55]\n" \
  "v_mfma_f32_16x16x32_bf16 v[56:59], v[220:223], v[24:27], v[56:59]\n" \
  "s_waitcnt lgkmcnt(4)\n" \
  "v_mfma_f32_16x16x32_bf16 v[60:63], v[32:35], v[24:27], v[60:63]\n" \
  "ds_read_b128 v[24:27], v0 offset:22528\n" \
  "ds_read_b128 v[32:35], v1 offset:10240\n" \
  "v_mfma_f32_16x16x32_bf16 v[48:51], v[96:99], v[12:15], v[48:51]\n" \
  "v_mfma_f32_16x16x32_bf16 v[52:55], v[160:163], v[12:15], v[52:55]\n" \
  "v_mfma_f32_16x16x32_bf16 v[56:59], v[224:227], v[12:15], v[56:59]\n" \
  "s_waitcnt lgkmcnt(4)\n" \
  "v_mfma_f32_16x16x32_bf16 v[60:63], v[36:39], v[12:15], v[60:63]\n" \
  "ds_read_b128 v[12:15], v0 offset:24576\n" \
  "ds_read_b128 v[36:39], v1 offset:11264\n" \
  "v_mfma_f32_16x16x32_bf16 v[48:51], v[100:103], v[16:19], v[48:51]\n" \
  "v_mfma_f32_16x16x32_bf16 v[52:55], v[164:167], v[16:19], v[52:55]\n" \
  "v_mfma_f32_16x16x32_bf16 v[56:59], v[228:231], v[16:19], v[56:59]\n" \
  "s_waitcnt lgkmcnt(4)\n" \
  "v_mfma_f32_16x16x32_bf16 v[60:63], v[28:31], v[16:19], v[60:63]\n" \
  "ds_read_b32 v5, v6 offset:16\n" \
  "ds_read_b128 v[16:19], v0 offset:26624\n" \
  "ds_read_b128 v[28:31], v1 offset:12288\n" \
  "v_mfma_f32_16x16x32_bf16 v[48:51], v[104:107], v[20:23], v[48:51]\n" \
  "v_mfma_f32_16x16x32_bf16 v[52:55], v[168:171], v[20:23], v[52:55]\n" \
  "v_mfma_f32_16x16x32_bf16 v[56:59], v[232:235], v[20:23], v[56:59]\n" \
  "s_waitcnt lgkmcnt(5)\n" \
  "v_mfma_f32_16x16x32_bf16 v[60:63], v[32:35], v[20:23], v[60:63]\n" \
  "ds_read_b128 v[20:23], v0 offset:28672\n" \
  "ds_read_b128 v[32:35], v1 offset:13312\n" \
  "v_mfma_f32_16x16x32_bf16 v[48:51], v[108:111], v[24:27], v[48:51]\n" \
  "v_mfma_f32_16x16x32_bf16 v[52:55], v[172:175], v[24:27], v[52:55]\n" \
  "v_mfma_f32_16x16x32_bf16 v[56:59], v[236:239], v[24:27], v[56:59]\n" \
  "s_waitcnt lgkmcnt(5)\n" \
  "v_mfma_f32_16x16x32_bf16 v[60:63], v[36:39], v[24:27], v[60:63]\n" \
  "s_cmp_eq_u32 s68, 1\n" \
  "s_cbranch_scc0 .LCP_X3_%=\n" \
  "s_waitcnt vmcnt(0)\n" \
  "s_mov_b64 exec, 1\n" \
  "buffer_store_dword v7, off, s[56:59], s72 sc1\n" \
  "s_mov_b64 exec, -1\n" \
  "s_add_u32 s72, s72, 0x1000\n" \
  "s_mov_b32 s68, 0\n" \
  ".LCP_X3_%=:\n" \
  "s_waitcnt lgkmcnt(4)\n" \
  "v_readfirstlane_b32 s71, v5\n" \
  "s_cmp_ge_u32 s71, s67\n" \
  "s_cbranch_scc1 .LCP_ST_%=\n" \
  ".LCP_S4_%=:\n" \
  "s_sleep 1\n" \
  "ds_read_b32 v5, v6 offset:16\n" \
  "s_sub_u32 s69, s69, 1\n" \
  "s_cbranch_scc1 .LCP_END_%=\n" \
  "s_waitcnt lgkmcnt(0)\n" \
  "v_readfirstlane_b32 s71, v5\n" \
  "s_cmp_ge_u32 s71, s67\n" \
  "s_cbranch_scc1 .LCP_ST_%=\n" \
  "s_branch .LCP_S4_%=\n" \
  ".LCP_ST_%=:\n" \
  "ds_read_b128 v[24:27], v0 offset:30720\n" \
  "ds_read_b128 v[36:39], v1 offset:14336\n" \
  "ds_read_b128 v[40:43], v4 offset:0\n" \
  "ds_read_b128 v[44:47], v4 offset:64\n" \
  "ds_read2_b32 v[8:9], v3 offset0:0 offset1:32\n" \
  "v_mfma_f32_16x16x32_bf16 v[48:51], v[112:115], v[12:15], v[48:51]\n" \
  "v_mfma_f32_16x16x32_bf16 v[52:55], v[176:179], v[12:15], v[52:55]\n" \
  "v_mfma_f32_16x16x32_bf16 v[56:59], v[240:243], v[12:15], v[56:59]\n" \
  "s_waitcnt lgkmcnt(7)\n" \
  "v_mfma_f32_16x16x32_bf16 v[60:63], v[28:31], v[12:15], v[60:63]\n" \
  "ds_read_b128 v[12:15], v0 offset:256\n" \
  "ds_read_b128 v[28:31], v1 offset:15360\n" \
  "v_mfma_f32_16x16x32_bf16 v[48:51], v[116:119], v[16:19], v[48:51]\n" \
  "v_mfma_f32_16x16x32_bf16 v[52:55], v[180:183], v[16:19], v[52:55]\n" \
  "v_mfma_f32_16x16x32_bf16 v[56:59], v[244:247], v[16:19], v[56:59]\n" \
  "s_waitcnt lgkmcnt(7)\n" \
  "v_mfma_f32_16x16x32_bf16 v[60:63], v[32:35], v[16:19], v[60:63]\n" \
  "ds_read_b128 v[16:19], v0 offset:2304\n" \
  "ds_read_b128 v[32:35], v4 offset:128\n" \
  "v_mfma_f32_16x16x32_bf16 v[48:51], v[120:123], v[20:23], v[48:51]\n" \
  "s_waitcnt lgkmcnt(4)\n" \
  "v_mul_f32 v40, v40, v8\n" \
  "v_mul_f32 v41, v41, v8\n" \
  "v_mfma_f32_16x16x32_bf16 v[52:55], v[184:187], v[20:23], v[52:55]\n" \
  "v_mul_f32 v42, v42, v8\n" \
  "v_mul_f32 v43, v43, v8\n" \
  "v_mfma_f32_16x16x32_bf16 v[56:59], v[248:251], v[20:23], v[56:59]\n" \
  "v_mul_f32 v44, v44, v8\n" \
  "v_mul_f32 v45, v45, v8\n" \
  "v_mfma_f32_16x16x32_bf16 v[60:63], v[36:39], v[20:23], v[60:63]\n" \
  "v_mul_f32 v46, v46, v8\n" \
  "v_mul_f32 v47, v47, v8\n" \
  "ds_read_b128 v[20:23], v0 offset:4352\n" \
  "ds_read_b128 v[36:39], v4 offset:192\n" \
  "v_mfma_f32_16x16x32_bf16 v[48:51], v[124:127], v[24:27], v[48:51]\n" \
  "v_mfma_f32_16x16x32_bf16 v[52:55], v[188:191], v[24:27], v[52:55]\n" \
  "v_mfma_f32_16x16x32_bf16 v[56:59], v[252:255], v[24:27], v[56:59]\n" \
  "s_waitcnt lgkmcnt(2)\n" \
  "v_mul_f32 v32, v32, v8\n" \
  "v_mul_f32 v33, v33, v8\n" \
  "v_mfma_f32_16x16x32_bf16 v[60:63], v[28:31], v[24:27], v[60:63]\n" \
  "v_mul_f32 v34, v34, v8\n" \
  "v_mul_f32 v35, v35, v8\n" \
  "ds_read_b128 v[28:31], v1 offset:0\n" \
  "ds_read_b128 v[24:27], v0 offset:6400\n" \
  "v_fmac_f32 v40, v48, v9\n" \
  "v_fmac_f32 v41, v49, v9\n" \
  "v_fmac_f32 v42, v50, v9\n" \
  "v_fmac_f32 v43, v51, v9\n" \
  "v_mfma_f32_16x16x32_bf16 v[48:51], v[64:67], v[12:15], 0\n" \
  "v_fmac_f32 v44, v52, v9\n" \
  "v_fmac_f32 v45, v53, v9\n" \
  "v_fmac_f32 v46, v54, v9\n" \
  "v_fmac_f32 v47, v55, v9\n" \
  "v_mfma_f32_16x16x32_bf16 v[52:55], v[128:131], v[12:15], 0\n" \
  "v_cvt_pk_bf16_f32 v40, v40, v41\n" \
  "v_cvt_pk_bf16_f32 v41, v42, v43\n" \
  "v_cvt_pk_bf16_f32 v42, v44, v45\n" \
  "v_cvt_pk_bf16_f32 v43, v46, v47\n" \
  "v_bfi_b32 v40, v11, s73, v40\n" \
  "v_fmac_f32 v32, v56, v9\n" \
  "v_fmac_f32 v33, v57, v9\n" \
  "v_fmac_f32 v34, v58, v9\n" \
  "v_fmac_f32 v35, v59, v9\n" \
  "v_mfma_f32_16x16x32_bf16 v[56:59], v[192:195], v[12:15], 0\n" \
  "s_waitcnt lgkmcnt(2)\n" \
  "v_mul_f32 v36, v36, v8\n" \
  "v_mul_f32 v37, v37, v8\n" \
  "v_mul_f32 v38, v38, v8\n" \
  "v_mul_f32 v39, v39, v8\n" \
  "v_fmac_f32 v36, v60, v9\n" \
  "v_fmac_f32 v37, v61, v9\n" \
  "v_fmac_f32 v38, v62, v9\n" \
  "v_fmac_f32 v39, v63, v9\n" \
  "s_waitcnt lgkmcnt(1)\n" \
  "v_mfma_f32_16x16x32_bf16 v[60:63], v[28:31], v[12:15], 0\n" \
  "v_cvt_pk_bf16_f32 v44, v32, v33\n" \
  "v_cvt_pk_bf16_f32 v45, v34, v35\n" \
  "v_cvt_pk_bf16_f32 v46, v36, v37\n" \
  "v_cvt_pk_bf16_f32 v47, v38, v39\n" \
  "v_bfi_b32 v44, v11, s73, v44\n" \
  "ds_read_b128 v[32:35], v1 offset:1024\n" \
  "ds_read_b128 v[36:39], v1 offset:2048\n" \
  "ds_read_b128 v[12:15], v0 offset:8448\n" \
  "ds_read_b128 v[28:31], v1 offset:3072\n" \
  "v_mfma_f32_16x16x32_bf16 v[48:51], v[68:71], v[16:19], v[48:51]\n" \
  "buffer_store_dwordx4 v[40:43], v2, s[40:43], s65 offen offset:0 sc1\n" \
  "v_mfma_f32_16x16x32_bf16 v[52:55], v[132:135], v[16:19], v[52:55]\n" \
  "buffer_store_dwordx4 v[44:47], v2, s[40:43], s65 offen offset:1024 sc1\n" \
  "v_mfma_f32_16x16x32_bf16 v[56:59], v[196:199], v[16:19], v[56:59]\n" \
  "s_waitcnt lgkmcnt(3)\n" \
  "v_mfma_f32_16x16x32_bf16 v[60:63], v[32:35], v[16:19], v[60:63]\n" \
  "ds_read_b128 v[16:19], v0 offset:10496\n" \
  "ds_read_b128 v[32:35], v1 offset:4096\n" \
  "v_mfma_f32_16x16x32_bf16 v[48:51], v[72:75], v[20:23], v[48:51]\n" \
  "v_mfma_f32_16x16x32_bf16 v[52:55], v[136:139], v[20:23], v[52:55]\n" \
  "v_mfma_f32_16x16x32_bf16 v[56:59], v[200:203], v[20:23], v[56:59]\n" \
  "s_waitcnt lgkmcnt(4)\n" \
  "v_mfma_f32_16x16x32_bf16 v[60:63], v[36:39], v[20:23], v[60:63]\n" \
  "ds_read_b128 v[20:23], v0 offset:12544\n" \
  "ds_read_b128 v[36:39], v1 offset:5120\n" \
  "v_mfma_f32_16x16x32_bf16 v[48:51], v[76:79], v[24:27], v[48:51]\n" \
  "v_mfma_f32_16x16x32_bf16 v[52:55], v[140:143], v[24:27], v[52:55]\n" \
  "v_mfma_f32_16x16x32_bf16 v[56:59], v[204:207], v[24:27], v[56:59]\n" \
  "s_waitcnt lgkmcnt(4)\n" \
  "v_mfma_f32_16x16x32_bf16 v[60:63], v[28:31], v[24:27], v[60:63]\n" \
  "ds_read_b128 v[24:27], v0 offset:14592\n" \
  "ds_read_b128 v[28:31], v1 offset:6144\n" \
  "v_mfma_f32_16x16x32_bf16 v[48:51], v[80:83], v[12:15], v[48:51]\n" \
  "v_mfma_f32_16x16x32_bf16 v[52:55], v[144:147], v[12:15], v[52:55]\n" \
  "v_mfma_f32_16x16x32_bf16 v[56:59], v[208:211], v[12:15], v[56:59]\n" \
  "s_waitcnt lgkmcnt(4)\n" \
  "v_mfma_f32_16x16x32_bf16 v[60:63], v[32:35], v[12:15], v[60:63]\n" \
  "ds_read_b128 v[12:15], v0 offset:16640\n" \
  "ds_read_b128 v[32:35], v1 offset:7168\n" \
  "v_mfma_f32_16x16x32_bf16 v[48:51], v[84:87], v[16:19], v[48:51]\n" \
  "v_mfma_f32_16x16x32_bf16 v[52:55], v[148:151], v[16:19], v[52:55]\n" \
  "v_mfma_f32_16x16x32_bf16 v[56:59], v[212:215], v[16:19], v[56:59]\n" \
  "s_waitcnt lgkmcnt(4)\n" \
  "v_mfma_f32_16x16x32_bf16 v[60:63], v[36:39], v[16:19], v[60:63]\n" \
  "ds_read_b128 v[16:19], v0 offset:18688\n" \
  "ds_read_b128 v[36:39], v1 offset:8192\n" \
  "v_mfma_f32_16x16x32_bf16 v[48:51], v[88:91], v[20:23], v[48:51]\n" \
  "v_mfma_f32_16x16x32_bf16 v[52:55], v[152:155], v[20:23], v[52:55]\n" \
  "v_mfma_f32_16x16x32_bf16 v[56:59], v[216:219], v[20:23], v[56:59]\n" \
  "s_waitcnt lgkmcnt(4)\n" \
  "v_mfma_f32_16x16x32_bf16 v[60:63], v[28:31], v[20:23], v[60:63]\n" \
  "ds_read_b128 v[20:23], v0 offset:20736\n" \
  "ds_read_b128 v[28:31], v1 offset:9216\n" \
  "v_mfma_f32_16x16x32_bf16 v[48:51], v[92:95], v[24:27], v[48:51]\n" \
  "v_mfma_f32_16x16x32_bf16 v[52:55], v[156:159], v[24:27], v[52:55]\n" \
  "v_mfma_f32_16x16x32_bf16 v[56:59], v[220:223], v[24:27], v[56:59]\n" \
  "s_waitcnt lgkmcnt(4)\n" \
  "v_mfma_f32_16x16x32_bf16 v[60:63], v[32:35], v[24:27], v[60:63]\n" \
  "ds_read_b128 v[24:27], v0 offset:22784\n" \
  "ds_read_b128 v[32:35], v1 offset:10240\n" \
  "v_mfma_f32_16x16x32_bf16 v[48:51], v[96:99], v[12:15], v[48:51]\n" \
  "v_mfma_f32_16x16x32_bf16 v[52:55], v[160:163], v[12:15], v[52:55]\n" \
  "v_mfma_f32_16x16x32_bf16 v[56:59], v[224:227], v[12:15], v[56:59]\n" \
  "s_waitcnt lgkmcnt(4)\n" \
  "v_mfma_f32_16x16x32_bf16 v[60:63], v[36:39], v[12:15], v[60:63]\n" \
  "ds_read_b128 v[12:15], v0 offset:24832\n" \
  "ds_read_b128 v[36:39], v1 offset:11264\n" \
  "v_mfma_f32_16x16x32_bf16 v[48:51], v[100:103], v[16:19], v[48:51]\n" \
  "v_mfma_f32_16x16x32_bf16 v[52:55], v[164:167], v[16:19], v[52:55]\n" \
  "v_mfma_f32_16x16x32_bf16 v[56:59], v[228:231], v[16:19], v[56:59]\n" \
  "s_waitcnt lgkmcnt(4)\n" \
  "v_mfma_f32_16x16x32_bf16 v[60:63], v[28:31], v[16:19], v[60:63]\n" \
  "ds_read_b128 v[16:19], v0 offset:26880\n" \
  "ds_read_b128 v[28:31], v1 offset:12288\n" \
  "v_mfma_f32_16x16x32_bf16 v[48:51], v[104:107], v[20:23], v[48:51]\n" \
  "v_mfma_f32_16x16x32_bf16 v[52:55], v[168:171], v[20:23], v[52:55]\n" \
  "v_mfma_f32_16x16x32_bf16 v[56:59], v[232:235], v[20:23], v[56:59]\n" \
  "s_waitcnt lgkmcnt(4)\n" \
  "v_mfma_f32_16x16x32_bf16 v[60:63], v[32:35], v[20:23], v[60:63]\n" \
  "ds_read_b128 v[20:23], v0 offset:28928\n" \
  "ds_read_b128 v[32:35], v1 offset:13312\n" \
  "v_mfma_f32_16x16x32_bf16 v[48:51], v[108:111], v[24:27], v[48:51]\n" \
  "v_mfma_f32_16x16x32_bf16 v[52:55], v[172:175], v[24:27], v[52:55]\n" \
  "v_mfma_f32_16x16x32_bf16 v[56:59], v[236:239], v[24:27], v[56:59]\n" \
  "s_waitcnt lgkmcnt(4)\n" \
  "v_mfma_f32_16x16x32_bf16 v[60:63], v[36:39], v[24:27], v[60:63]\n" \
  "ds_read_b128 v[24:27], v0 offset:30976\n" \
  "ds_read_b128 v[36:39], v1 offset:14336\n" \
  "ds_read_b128 v[40:43], v4 offset:0\n" \
  "ds_read_b128 v[44:47], v4 offset:64\n" \
  "ds_read2_b32 v[8:9], v3 offset0:16 offset1:48\n" \
  "v_mfma_f32_16x16x32_bf16 v[48:51], v[112:115], v[12:15], v[48:51]\n" \
  "v_mfma_f32_16x16x32_bf16 v[52:55], v[176:179], v[12:15], v[52:55]\n" \
  "v_mfma_f32_16x16x32_bf16 v[56:59], v[240:243], v[12:15], v[56:59]\n" \
  "s_waitcnt lgkmcnt(7)\n" \
  "v_mfma_f32_16x16x32_bf16 v[60:63], v[28:31], v[12:15], v[60:63]\n" \
  "ds_read_b128 v[28:31], v1 offset:15360\n" \
  "v_mfma_f32_16x16x32_bf16 v[48:51], v[116:119], v[16:19], v[48:51]\n" \
  "v_mfma_f32_16x16x32_bf16 v[52:55], v[180:183], v[16:19], v[52:55]\n" \
  "v_mfma_f32_16x16x32_bf16 v[56:59], v[244:247], v[16:19], v[56:59]\n" \
  "s_waitcnt lgkmcnt(6)\n" \
  "v_mfma_f32_16x16x32_bf16 v[60:63], v[32:35], v[16:19], v[60:63]\n" \
  "ds_read_b128 v[32:35], v4 offset:128\n" \
  "v_mfma_f32_16x16x32_bf16 v[48:51], v[120:123], v[20:23], v[48:51]\n" \
  "s_waitcnt lgkmcnt(2)\n" \
  "v_mul_f32 v40, v40, v8\n" \
  "v_mul_f32 v41, v41, v8\n" \
  "v_mfma_f32_16x16x32_bf16 v[52:55], v[184:187], v[20:23], v[52:55]\n" \
  "v_mul_f32 v42, v42, v8\n" \
  "v_mul_f32 v43, v43, v8\n" \
  "v_mfma_f32_16x16x32_bf16 v[56:59], v[248:251], v[20:23], v[56:59]\n" \
  "v_mul_f32 v44, v44, v8\n" \
  "v_mul_f32 v45, v45, v8\n" \
  "v_mfma_f32_16x16x32_bf16 v[60:63], v[36:39], v[20:23], v[60:63]\n" \
  "v_mul_f32 v46, v46, v8\n" \
  "v_mul_f32 v47, v47, v8\n" \
  "ds_read_b128 v[36:39], v4 offset:192\n" \
  "s_mov_b64 exec, 1\n" \
  "ds_add_u32 v6, v7 offset:8\n" \
  "s_mov_b64 exec, -1\n" \
  "v_xor_b32 v6, 4, v6\n" \
  "v_mfma_f32_16x16x32_bf16 v[48:51], v[124:127], v[24:27], v[48:51]\n" \
  "v_mfma_f32_16x16x32_bf16 v[52:55], v[188:191], v[24:27], v[52:55]\n" \
  "v_mfma_f32_16x16x32_bf16 v[56:59], v[252:255], v[24:27], v[56:59]\n" \
  "s_waitcnt lgkmcnt(2)\n" \
  "v_mul_f32 v32, v32, v8\n" \
  "v_mul_f32 v33, v33, v8\n" \
  "v_mfma_f32_16x16x32_bf16 v[60:63], v[28:31], v[24:27], v[60:63]\n" \
  "v_mul_f32 v34, v34, v8\n" \
  "v_mul_f32 v35, v35, v8\n" \
  "v_fmac_f32 v40, v48, v9\n" \
  "v_fmac_f32 v41, v49, v9\n" \
  "v_fmac_f32 v42, v50, v9\n" \
  "v_fmac_f32 v43, v51, v9\n" \
  "v_fmac_f32 v44, v52, v9\n" \
  "v_fmac_f32 v45, v53, v9\n" \
  "v_fmac_f32 v46, v54, v9\n" \
  "v_fmac_f32 v47, v55, v9\n" \
  "ds_read_b32 v5, v6 offset:0\n" \
  "v_cvt_pk_bf16_f32 v40, v40, v41\n" \
  "v_cvt_pk_bf16_f32 v41, v42, v43\n" \
  "v_cvt_pk_bf16_f32 v42, v44, v45\n" \
  "v_cvt_pk_bf16_f32 v43, v46, v47\n" \
  "v_bfi_b32 v40, v11, s73, v40\n" \
  "buffer_store_dwordx4 v[40:43], v10, s[40:43], s65 offen offset:0 sc1\n" \
  "v_fmac_f32 v32, v56, v9\n" \
  "v_fmac_f32 v33, v57, v9\n" \
  "v_fmac_f32 v34, v58, v9\n" \
  "v_fmac_f32 v35, v59, v9\n" \
  "s_waitcnt lgkmcnt(2)\n" \
  "v_mul_f32 v36, v36, v8\n" \
  "v_mul_f32 v37, v37, v8\n" \
  "v_mul_f32 v38, v38, v8\n" \
  "v_mul_f32 v39, v39, v8\n" \
  "v_fmac_f32 v36, v60, v9\n" \
  "v_fmac_f32 v37, v61, v9\n" \
  "v_fmac_f32 v38, v62, v9\n" \
  "v_fmac_f32 v39, v63, v9\n" \
  "v_cvt_pk_bf16_f32 v44, v32, v33\n" \
  "v_cvt_pk_bf16_f32 v45, v34, v35\n" \
  "v_cvt_pk_bf16_f32 v46, v36, v37\n" \
  "v_cvt_pk_bf16_f32 v47, v38, v39\n" \
  "v_bfi_b32 v44, v11, s73, v44\n" \
  "buffer_store_dwordx4 v[44:47], v10, s[40:43], s65 offen offset:1024 sc1\n" \
  "s_waitcnt lgkmcnt(0)\n" \
  "s_add_u32 s65, s65, 0x80000\n" \
  "s_sub_u32 s64, s64, 1\n" \
  "s_mov_b32 s68, 1\n" \
  "s_cmp_lg_u32 s64, 0\n" \
  "s_cbranch_scc1 .LCP_TOP_%=\n" \
  "s_waitcnt vmcnt(0)\n" \
  "s_mov_b64 exec, 1\n" \
  "buffer_store_dword v7, off, s[56:59], s72 sc1\n" \
  "s_mov_b64 exec, -1\n" \
  "s_add_u32 s72, s72, 0x1000\n" \
  "s_mov_b32 s68, 0\n" \
  ".LCP_END_%=:\n" \
  "s_endpgm\n"
#define V_ASM \
  "s_mov_b32 s68, 0\n" \
  "s_mov_b32 s66, 3\n" \
  "s_mov_b32 s67, 2\n" \
  "s_mov_b32 s72, 0\n" \
  "v_mov_b32 v7, 1\n" \
  "v_mov_b32 v6, s70\n" \
  "v_mov_b32 v5, 0\n" \
  "ds_read_b32 v8, v4 offset:0\n" \
  "ds_read_b32 v9, v4 offset:64\n" \
  "ds_read_b32 v10, v4 offset:128\n" \
  "ds_read_b32 v4, v4 offset:192\n" \
  "s_waitcnt lgkmcnt(0)\n" \
  ".p2align 6\n" \
  ".LCW_TOP_%=:\n" \
  "ds_read_b128 v[28:31], v1 offset:0\n" \
  "ds_read_b128 v[32:35], v1 offset:1024\n" \
  "s_sub_u32 s73, s64, 1\n" \
  "s_bitset0_b32 s65, s74\n" \
  "s_bfe_u32 s73, s73, 0x10004\n" \
  "s_cmp_gt_u32 s64, s75\n" \
  "s_cselect_b32 s76, 1, 0\n" \
  "v_mov_b32 v11, s76\n" \
  "s_add_u32 s66, s66, 3\n" \
  "s_add_u32 s67, s67, 2\n" \
  "s_mov_b32 s69, 0x400000\n" \
  "v_xor_b32 v0, 0x8000, v0\n" \
  "v_xor_b32 v3, 0x100, v3\n" \
  "v_readfirstlane_b32 s71, v5\n" \
  "s_cmp_ge_u32 s71, s66\n" \
  "s_cbranch_scc1 .LCW_GO_%=\n" \
  "s_cmp_eq_u32 s68, 1\n" \
  "s_cbranch_scc0 .LCW_X1_%=\n" \
  "s_waitcnt vmcnt(0)\n" \
  "s_mov_b64 exec, 1\n" \
  "buffer_store_dword v7, off, s[56:59], s72 sc1\n" \
  "s_mov_b64 exec, -1\n" \
  "s_add_u32 s72, s72, 0x1000\n" \
  "s_mov_b32 s68, 0\n" \
  ".LCW_X1_%=:\n" \
  ".LCW_S2_%=:\n" \
  "s_sleep 1\n" \
  "ds_read_b32 v5, v6 offset:0\n" \
  "s_sub_u32 s69, s69, 1\n" \
  "s_cbranch_scc1 .LCW_END_%=\n" \
  "s_waitcnt lgkmcnt(0)\n" \
  "v_readfirstlane_b32 s71, v5\n" \
  "s_cmp_ge_u32 s71, s66\n" \
  "s_cbranch_scc1 .LCW_GO_%=\n" \
  "s_branch .LCW_S2_%=\n" \
  ".LCW_GO_%=:\n" \
  "ds_read_b128 v[12:15], v0 offset:0\n" \
  "ds_read_b128 v[16:19], v0 offset:2048\n" \
  "ds_read_b128 v[20:23], v0 offset:4096\n" \
  "ds_read_b128 v[24:27], v0 offset:6144\n" \
  "ds_read_b128 v[36:39], v1 offset:2048\n" \
  "s_waitcnt lgkmcnt(4)\n" \
  "v_mfma_f32_16x16x32_bf16 v[42:45], v[12:15], v[64:67], 0\n" \
  "v_mfma_f32_16x16x32_bf16 v[48:51], v[12:15], v[128:131], 0\n" \
  "v_mfma_f32_16x16x32_bf16 v[54:57], v[12:15], v[192:195], 0\n" \
  "v_mfma_f32_16x16x32_bf16 v[60:63], v[12:15], v[28:31], 0\n" \
  "ds_read_b128 v[12:15], v0 offset:8192\n" \
  "ds_read_b128 v[28:31], v1 offset:3072\n" \
  "s_waitcnt lgkmcnt(5)\n" \
  "v_mfma_f32_16x16x32_bf16 v[42:45], v[16:19], v[68:71], v[42:45]\n" \
  "v_mfma_f32_16x16x32_bf16 v[48:51], v[16:19], v[132:135], v[48:51]\n" \
  "v_mfma_f32_16x16x32_bf16 v[54:57], v[16:19], v[196:199], v[54:57]\n" \
  "v_mfma_f32_16x16x32_bf16 v[60:63], v[16:19], v[32:35], v[60:63]\n" \
  "ds_read_b128 v[16:19], v0 offset:10240\n" \
  "ds_read_b128 v[32:35], v1 offset:4096\n" \
  "s_waitcnt lgkmcnt(6)\n" \
  "v_mfma_f32_16x16x32_bf16 v[42:45], v[20:23], v[72:75], v[42:45]\n" \
  "v_mfma_f32_16x16x32_bf16 v[48:51], v[20:23], v[136:139], v[48:51]\n" \
  "v_mfma_f32_16x16x32_bf16 v[54:57], v[20:23], v[200:203], v[54:57]\n" \
  "s_waitcnt lgkmcnt(4)\n" \
  "v_mfma_f32_16x16x32_bf16 v[60:63], v[20:23], v[36:39], v[60:63]\n" \
  "ds_read_b128 v[20:23], v0 offset:12288\n" \
  "ds_read_b128 v[36:39], v1 offset:5120\n" \
  "v_mfma_f32_16x16x32_bf16 v[42:45], v[24:27], v[76:79], v[42:45]\n" \
  "v_mfma_f32_16x16x32_bf16 v[48:51], v[24:27], v[140:143], v[48:51]\n" \
  "v_mfma_f32_16x16x32_bf16 v[54:57], v[24:27], v[204:207], v[54:57]\n" \
  "s_waitcnt lgkmcnt(4)\n" \
  "v_mfma_f32_16x16x32_bf16 v[60:63], v[24:27], v[28:31], v[60:63]\n" \
  "ds_read_b128 v[24:27], v0 offset:14336\n" \
  "ds_read_b128 v[28:31], v1 offset:6144\n" \
  "v_mfma_f32_16x16x32_bf16 v[42:45], v[12:15], v[80:83], v[42:45]\n" \
  "v_mfma_f32_16x16x32_bf16 v[48:51], v[12:15], v[144:147], v[48:51]\n" \
  "v_mfma_f32_16x16x32_bf16 v[54:57], v[12:15], v[208:211], v[54:57]\n" \
  "s_waitcnt lgkmcnt(4)\n" \
  "v_mfma_f32_16x16x32_bf16 v[60:63], v[12:15], v[32:35], v[60:63]\n" \
  "ds_read_b128 v[12:15], v0 offset:16384\n" \
  "ds_read_b128 v[32:35], v1 offset:7168\n" \
  "v_mfma_f32_16x16x32_bf16 v[42:45], v[16:19], v[84:87], v[42:45]\n" \
  "v_mfma_f32_16x16x32_bf16 v[48:51], v[16:19], v[148:151], v[48:51]\n" \
  "v_mfma_f32_16x16x32_bf16 v[54:57], v[16:19], v[212:215], v[54:57]\n" \
  "s_waitcnt lgkmcnt(4)\n" \
  "v_mfma_f32_16x16x32_bf16 v[60:63], v[16:19], v[36:39], v[60:63]\n" \
  "ds_read_b128 v[16:19], v0 offset:18432\n" \
  "ds_read_b128 v[36:39], v1 offset:8192\n" \
  "v_mfma_f32_16x16x32_bf16 v[42:45], v[20:23], v[88:91], v[42:45]\n" \
  "v_mfma_f32_16x16x32_bf16 v[48:51], v[20:23], v[152:155], v[48:51]\n" \
  "v_mfma_f32_16x16x32_bf16 v[54:57], v[20:23], v[216:219], v[54:57]\n" \
  "s_waitcnt lgkmcnt(4)\n" \
  "v_mfma_f32_16x16x32_bf16 v[60:63], v[20:23], v[28:31], v[60:63]\n" \
  "ds_read_b128 v[20:23], v0 offset:20480\n" \
  "ds_read_b128 v[28:31], v1 offset:9216\n" \
  "v_mfma_f32_16x16x32_bf16 v[42:45], v[24:27], v[92:95], v[42:45]\n" \
  "v_mfma_f32_16x16x32_bf16 v[48:51], v[24:27], v[156:159], v[48:51]\n" \
  "v_mfma_f32_16x16x32_bf16 v[54:57], v[24:27], v[220:223], v[54:57]\n" \
  "s_waitcnt lgkmcnt(4)\n" \
  "v_mfma_f32_16x16x32_bf16 v[60:63], v[24:27], v[32:35], v[60:63]\n" \
  "ds_read_b128 v[24:27], v0 offset:22528\n" \
  "ds_read_b128 v[32:35], v1 offset:10240\n" \
  "v_mfma_f32_16x16x32_bf16 v[42:45], v[12:15], v[96:99], v[42:45]\n" \
  "v_mfma_f32_16x16x32_bf16 v[48:51], v[12:15], v[160:163], v[48:51]\n" \
  "v_mfma_f32_16x16x32_bf16 v[54:57], v[12:15], v[224:227], v[54:57]\n" \
  "s_waitcnt lgkmcnt(4)\n" \
  "v_mfma_f32_16x16x32_bf16 v[60:63], v[12:15], v[36:39], v[60:63]\n" \
  "ds_read_b128 v[12:15], v0 offset:24576\n" \
  "ds_read_b128 v[36:39], v1 offset:11264\n" \
  "v_mfma_f32_16x16x32_bf16 v[42:45], v[16:19], v[100:103], v[42:45]\n" \
  "v_mfma_f32_16x16x32_bf16 v[48:51], v[16:19], v[164:167], v[48:51]\n" \
  "v_mfma_f32_16x16x32_bf16 v[54:57], v[16:19], v[228:231], v[54:57]\n" \
  "s_waitcnt lgkmcnt(4)\n" \
  "v_mfma_f32_16x16x32_bf16 v[60:63], v[16:19], v[28:31], v[60:63]\n" \
  "ds_read_b128 v[16:19], v0 offset:26624\n" \
  "ds_read_b128 v[28:31], v1 offset:12288\n" \
  "v_mfma_f32_16x16x32_bf16 v[42:45], v[20:23], v[104:107], v[42:45]\n" \
  "v_mfma_f32_16x16x32_bf16 v[48:51], v[20:23], v[168:171], v[48:51]\n" \
  "v_mfma_f32_16x16x32_bf16 v[54:57], v[20:23], v[232:235], v[54:57]\n" \
  "s_waitcnt lgkmcnt(4)\n" \
  "v_mfma_f32_16x16x32_bf16 v[60:63], v[20:23], v[32:35], v[60:63]\n" \
  "ds_read_b128 v[20:23], v0 offset:28672\n" \
  "ds_read_b128 v[32:35], v1 offset:13312\n" \
  "v_mfma_f32_16x16x32_bf16 v[42:45], v[24:27], v[108:111], v[42:45]\n" \
  "v_mfma_f32_16x16x32_bf16 v[48:51], v[24:27], v[172:175], v[48:51]\n" \
  "v_mfma_f32_16x16x32_bf16 v[54:57], v[24:27], v[236:239], v[54:57]\n" \
  "s_waitcnt lgkmcnt(4)\n" \
  "v_mfma_f32_16x16x32_bf16 v[60:63], v[24:27], v[36:39], v[60:63]\n" \
  "s_cmp_eq_u32 s68, 1\n" \
  "s_cbranch_scc0 .LCW_X3_%=\n" \
  "s_waitcnt vmcnt(0)\n" \
  "s_mov_b64 exec, 1\n" \
  "buffer_store_dword v7, off, s[56:59], s72 sc1\n" \
  "s_mov_b64 exec, -1\n" \
  "s_add_u32 s72, s72, 0x1000\n" \
  "s_mov_b32 s68, 0\n" \
  ".LCW_X3_%=:\n" \
  "ds_read_b32 v5, v6 offset:16\n" \
  "ds_read_b128 v[24:27], v0 offset:30720\n" \
  "ds_read_b128 v[36:39], v1 offset:14336\n" \
  "v_mfma_f32_16x16x32_bf16 v[42:45], v[12:15], v[112:115], v[42:45]\n" \
  "v_mfma_f32_16x16x32_bf16 v[48:51], v[12:15], v[176:179], v[48:51]\n" \
  "v_mfma_f32_16x16x32_bf16 v[54:57], v[12:15], v[240:243], v[54:57]\n" \
  "s_waitcnt lgkmcnt(5)\n" \
  "v_mfma_f32_16x16x32_bf16 v[60:63], v[12:15], v[28:31], v[60:63]\n" \
  "ds_read_b128 v[12:15], v0 offset:256\n" \
  "ds_read_b128 v[28:31], v1 offset:15360\n" \
  "v_mfma_f32_16x16x32_bf16 v[42:45], v[16:19], v[116:119], v[42:45]\n" \
  "v_mfma_f32_16x16x32_bf16 v[48:51], v[16:19], v[180:183], v[48:51]\n" \
  "v_mfma_f32_16x16x32_bf16 v[54:57], v[16:19], v[244:247], v[54:57]\n" \
  "s_waitcnt lgkmcnt(5)\n" \
  "v_mfma_f32_16x16x32_bf16 v[60:63], v[16:19], v[32:35], v[60:63]\n" \
  "s_waitcnt lgkmcnt(4)\n" \
  "v_readfirstlane_b32 s71, v5\n" \
  "s_cmp_ge_u32 s71, s67\n" \
  "s_cbranch_scc1 .LCW_ST_%=\n" \
  ".LCW_S4_%=:\n" \
  "s_sleep 1\n" \
  "ds_read_b32 v5, v6 offset:16\n" \
  "s_sub_u32 s69, s69, 1\n" \
  "s_cbranch_scc1 .LCW_END_%=\n" \
  "s_waitcnt lgkmcnt(0)\n" \
  "v_readfirstlane_b32 s71, v5\n" \
  "s_cmp_ge_u32 s71, s67\n" \
  "s_cbranch_scc1 .LCW_ST_%=\n" \
  "s_branch .LCW_S4_%=\n" \
  ".LCW_ST_%=:\n" \
  "ds_read_b128 v[16:19], v0 offset:2304\n" \
  "ds_read_b128 v[32:35], v3 offset:128\n" \
  "v_mfma_f32_16x16x32_bf16 v[42:45], v[20:23], v[120:123], v[42:45]\n" \
  "v_mfma_f32_16x16x32_bf16 v[48:51], v[20:23], v[184:187], v[48:51]\n" \
  "v_mfma_f32_16x16x32_bf16 v[54:57], v[20:23], v[248:251], v[54:57]\n" \
  "s_waitcnt lgkmcnt(4)\n" \
  "v_mfma_f32_16x16x32_bf16 v[60:63], v[20:23], v[36:39], v[60:63]\n" \
  "ds_read_b128 v[20:23], v0 offset:4352\n" \
  "ds_read_b128 v[36:39], v3 offset:0\n" \
  "v_mfma_f32_16x16x32_bf16 v[42:45], v[24:27], v[124:127], v[42:45]\n" \
  "v_mfma_f32_16x16x32_bf16 v[48:51], v[24:27], v[188:191], v[48:51]\n" \
  "v_mfma_f32_16x16x32_bf16 v[54:57], v[24:27], v[252:255], v[54:57]\n" \
  "s_waitcnt lgkmcnt(4)\n" \
  "v_mfma_f32_16x16x32_bf16 v[60:63], v[24:27], v[28:31], v[60:63]\n" \
  "ds_read_b128 v[28:31], v1 offset:0\n" \
  "ds_read_b128 v[24:27], v0 offset:6400\n" \
  "s_waitcnt lgkmcnt(4)\n" \
  "v_mul_f32 v42, v42, v32\n" \
  "v_mul_f32 v43, v43, v33\n" \
  "v_mul_f32 v44, v44, v34\n" \
  "v_mul_f32 v45, v45, v35\n" \
  "s_waitcnt lgkmcnt(2)\n" \
  "v_fmac_f32 v42, v36, v8\n" \
  "v_fmac_f32 v43, v37, v8\n" \
  "v_fmac_f32 v44, v38, v8\n" \
  "v_fmac_f32 v45, v39, v8\n" \
  "v_cvt_pk_bf16_f32 v40, v42, v43\n" \
  "v_cvt_pk_bf16_f32 v41, v44, v45\n" \
  "v_bfi_b32 v40, v11, s73, v40\n" \
  "v_mfma_f32_16x16x32_bf16 v[42:45], v[12:15], v[64:67], 0\n" \
  "v_mul_f32 v48, v48, v32\n" \
  "v_mul_f32 v49, v49, v33\n" \
  "v_mul_f32 v50, v50, v34\n" \
  "v_mul_f32 v51, v51, v35\n" \
  "v_fmac_f32 v48, v36, v9\n" \
  "v_fmac_f32 v49, v37, v9\n" \
  "v_fmac_f32 v50, v38, v9\n" \
  "v_fmac_f32 v51, v39, v9\n" \
  "v_cvt_pk_bf16_f32 v46, v48, v49\n" \
  "v_cvt_pk_bf16_f32 v47, v50, v51\n" \
  "v_bfi_b32 v46, v11, s73, v46\n" \
  "v_mfma_f32_16x16x32_bf16 v[48:51], v[12:15], v[128:131], 0\n" \
  "v_mul_f32 v54, v54, v32\n" \
  "v_mul_f32 v55, v55, v33\n" \
  "v_mul_f32 v56, v56, v34\n" \
  "v_mul_f32 v57, v57, v35\n" \
  "v_fmac_f32 v54, v36, v10\n" \
  "v_fmac_f32 v55, v37, v10\n" \
  "v_fmac_f32 v56, v38, v10\n" \
  "v_fmac_f32 v57, v39, v10\n" \
  "v_cvt_pk_bf16_f32 v52, v54, v55\n" \
  "v_cvt_pk_bf16_f32 v53, v56, v57\n" \
  "v_bfi_b32 v52, v11, s73, v52\n" \
  "v_mfma_f32_16x16x32_bf16 v[54:57], v[12:15], v[192:195], 0\n" \
  "v_mul_f32 v60, v60, v32\n" \
  "v_mul_f32 v61, v61, v33\n" \
  "v_mul_f32 v62, v62, v34\n" \
  "v_mul_f32 v63, v63, v35\n" \
  "v_fmac_f32 v60, v36, v4\n" \
  "v_fmac_f32 v61, v37, v4\n" \
  "v_fmac_f32 v62, v38, v4\n" \
  "v_fmac_f32 v63, v39, v4\n" \
  "v_cvt_pk_bf16_f32 v58, v60, v61\n" \
  "v_cvt_pk_bf16_f32 v59, v62, v63\n" \
  "v_bfi_b32 v58, v11, s73, v58\n" \
  "ds_read_b128 v[32:35], v1 offset:1024\n" \
  "ds_read_b128 v[36:39], v1 offset:2048\n" \
  "s_waitcnt lgkmcnt(3)\n" \
  "v_mfma_f32_16x16x32_bf16 v[60:63], v[12:15], v[28:31], 0\n" \
  "ds_read_b128 v[12:15], v0 offset:8448\n" \
  "ds_read_b128 v[28:31], v1 offset:3072\n" \
  "v_mfma_f32_16x16x32_bf16 v[42:45], v[16:19], v[68:71], v[42:45]\n" \
  "v_mfma_f32_16x16x32_bf16 v[48:51], v[16:19], v[132:135], v[48:51]\n" \
  "v_mfma_f32_16x16x32_bf16 v[54:57], v[16:19], v[196:199], v[54:57]\n" \
  "s_waitcnt lgkmcnt(3)\n" \
  "v_mfma_f32_16x16x32_bf16 v[60:63], v[16:19], v[32:35], v[60:63]\n" \
  "ds_read_b128 v[16:19], v0 offset:10496\n" \
  "ds_read_b128 v[32:35], v1 offset:4096\n" \
  "v_mfma_f32_16x16x32_bf16 v[42:45], v[20:23], v[72:75], v[42:45]\n" \
  "v_mfma_f32_16x16x32_bf16 v[48:51], v[20:23], v[136:139], v[48:51]\n" \
  "v_mfma_f32_16x16x32_bf16 v[54:57], v[20:23], v[200:203], v[54:57]\n" \
  "s_waitcnt lgkmcnt(4)\n" \
  "v_mfma_f32_16x16x32_bf16 v[60:63], v[20:23], v[36:39], v[60:63]\n" \
  "ds_read_b128 v[20:23], v0 offset:12544\n" \
  "ds_read_b128 v[36:39], v1 offset:5120\n" \
  "v_mfma_f32_16x16x32_bf16 v[42:45], v[24:27], v[76:79], v[42:45]\n" \
  "v_mfma_f32_16x16x32_bf16 v[48:51], v[24:27], v[140:143], v[48:51]\n" \
  "v_mfma_f32_16x16x32_bf16 v[54:57], v[24:27], v[204:207], v[54:57]\n" \
  "s_waitcnt lgkmcnt(4)\n" \
  "v_mfma_f32_16x16x32_bf16 v[60:63], v[24:27], v[28:31], v[60:63]\n" \
  "ds_read_b128 v[24:27], v0 offset:14592\n" \
  "ds_read_b128 v[28:31], v1 offset:6144\n" \
  "v_mfma_f32_16x16x32_bf16 v[42:45], v[12:15], v[80:83], v[42:45]\n" \
  "v_mfma_f32_16x16x32_bf16 v[48:51], v[12:15], v[144:147], v[48:51]\n" \
  "v_mfma_f32_16x16x32_bf16 v[54:57], v[12:15], v[208:211], v[54:57]\n" \
  "s_waitcnt lgkmcnt(4)\n" \
  "v_mfma_f32_16x16x32_bf16 v[60:63], v[12:15], v[32:35], v[60:63]\n" \
  "ds_read_b128 v[12:15], v0 offset:16640\n" \
  "ds_read_b128 v[32:35], v1 offset:7168\n" \
  "v_mfma_f32_16x16x32_bf16 v[42:45], v[16:19], v[84:87], v[42:45]\n" \
  "v_mfma_f32_16x16x32_bf16 v[48:51], v[16:19], v[148:151], v[48:51]\n" \
  "v_mfma_f32_16x16x32_bf16 v[54:57], v[16:19], v[212:215], v[54:57]\n" \
  "s_waitcnt lgkmcnt(4)\n" \
  "v_mfma_f32_16x16x32_bf16 v[60:63], v[16:19], v[36:39], v[60:63]\n" \
  "ds_read_b128 v[16:19], v0 offset:18688\n" \
  "ds_read_b128 v[36:39], v1 offset:8192\n" \
  "v_mfma_f32_16x16x32_bf16 v[42:45], v[20:23], v[88:91], v[42:45]\n" \
  "v_mfma_f32_16x16x32_bf16 v[48:51], v[20:23], v[152:155], v[48:51]\n" \
  "v_mfma_f32_16x16x32_bf16 v[54:57], v[20:23], v[216:219], v[54:57]\n" \
  "s_waitcnt lgkmcnt(4)\n" \
  "v_mfma_f32_16x16x32_bf16 v[60:63], v[20:23], v[28:31], v[60:63]\n" \
  "ds_read_b128 v[20:23], v0 offset:20736\n" \
  "ds_read_b128 v[28:31], v1 offset:9216\n" \
  "v_mfma_f32_16x16x32_bf16 v[42:45], v[24:27], v[92:95], v[42:45]\n" \
  "v_mfma_f32_16x16x32_bf16 v[48:51], v[24:27], v[156:159], v[48:51]\n" \
  "v_mfma_f32_16x16x32_bf16 v[54:57], v[24:27], v[220:223], v[54:57]\n" \
  "s_waitcnt lgkmcnt(4)\n" \
  "v_mfma_f32_16x16x32_bf16 v[60:63], v[24:27], v[32:35], v[60:63]\n" \
  "ds_read_b128 v[24:27], v0 offset:22784\n" \
  "ds_read_b128 v[32:35], v1 offset:10240\n" \
  "v_mfma_f32_16x16x32_bf16 v[42:45], v[12:15], v[96:99], v[42:45]\n" \
  "v_mfma_f32_16x16x32_bf16 v[48:51], v[12:15], v[160:163], v[48:51]\n" \
  "v_mfma_f32_16x16x32_bf16 v[54:57], v[12:15], v[224:227], v[54:57]\n" \
  "s_waitcnt lgkmcnt(4)\n" \
  "v_mfma_f32_16x16x32_bf16 v[60:63], v[12:15], v[36:39], v[60:63]\n" \
  "ds_read_b128 v[12:15], v0 offset:24832\n" \
  "ds_read_b128 v[36:39], v1 offset:11264\n" \
  "v_mfma_f32_16x16x32_bf16 v[42:45], v[16:19], v[100:103], v[42:45]\n" \
  "v_mfma_f32_16x16x32_bf16 v[48:51], v[16:19], v[164:167], v[48:51]\n" \
  "v_mfma_f32_16x16x32_bf16 v[54:57], v[16:19], v[228:231], v[54:57]\n" \
  "s_waitcnt lgkmcnt(4)\n" \
  "v_mfma_f32_16x16x32_bf16 v[60:63], v[16:19], v[28:31], v[60:63]\n" \
  "ds_read_b128 v[16:19], v0 offset:26880\n" \
  "ds_read_b128 v[28:31], v1 offset:12288\n" \
  "v_mfma_f32_16x16x32_bf16 v[42:45], v[20:23], v[104:107], v[42:45]\n" \
  "v_mfma_f32_16x16x32_bf16 v[48:51], v[20:23], v[168:171], v[48:51]\n" \
  "v_mfma_f32_16x16x32_bf16 v[54:57], v[20:23], v[232:235], v[54:57]\n" \
  "s_waitcnt lgkmcnt(4)\n" \
  "v_mfma_f32_16x16x32_bf16 v[60:63], v[20:23], v[32:35], v[60:63]\n" \
  "ds_read_b128 v[20:23], v0 offset:28928\n" \
  "ds_read_b128 v[32:35], v1 offset:13312\n" \
  "v_mfma_f32_16x16x32_bf16 v[42:45], v[24:27], v[108:111], v[42:45]\n" \
  "v_mfma_f32_16x16x32_bf16 v[48:51], v[24:27], v[172:175], v[48:51]\n" \
  "v_mfma_f32_16x16x32_bf16 v[54:57], v[24:27], v[236:239], v[54:57]\n" \
  "s_waitcnt lgkmcnt(4)\n" \
  "v_mfma_f32_16x16x32_bf16 v[60:63], v[24:27], v[36:39], v[60:63]\n" \
  "ds_read_b128 v[24:27], v0 offset:30976\n" \
  "ds_read_b128 v[36:39], v1 offset:14336\n" \
  "v_mfma_f32_16x16x32_bf16 v[42:45], v[12:15], v[112:115], v[42:45]\n" \
  "v_mfma_f32_16x16x32_bf16 v[48:51], v[12:15], v[176:179], v[48:51]\n" \
  "v_mfma_f32_16x16x32_bf16 v[54:57], v[12:15], v[240:243], v[54:57]\n" \
  "s_waitcnt lgkmcnt(4)\n" \
  "v_mfma_f32_16x16x32_bf16 v[60:63], v[12:15], v[28:31], v[60:63]\n" \
  "ds_read_b128 v[28:31], v1 offset:15360\n" \
  "v_mfma_f32_16x16x32_bf16 v[42:45], v[16:19], v[116:119], v[42:45]\n" \
  "v_mfma_f32_16x16x32_bf16 v[48:51], v[16:19], v[180:183], v[48:51]\n" \
  "v_mfma_f32_16x16x32_bf16 v[54:57], v[16:19], v[244:247], v[54:57]\n" \
  "s_waitcnt lgkmcnt(3)\n" \
  "v_mfma_f32_16x16x32_bf16 v[60:63], v[16:19], v[32:35], v[60:63]\n" \
  "ds_read_b128 v[32:35], v3 offset:192\n" \
  "v_mfma_f32_16x16x32_bf16 v[42:45], v[20:23], v[120:123], v[42:45]\n" \
  "v_mfma_f32_16x16x32_bf16 v[48:51], v[20:23], v[184:187], v[48:51]\n" \
  "v_mfma_f32_16x16x32_bf16 v[54:57], v[20:23], v[248:251], v[54:57]\n" \
  "s_waitcnt lgkmcnt(2)\n" \
  "v_mfma_f32_16x16x32_bf16 v[60:63], v[20:23], v[36:39], v[60:63]\n" \
  "ds_read_b128 v[36:39], v3 offset:64\n" \
  "s_mov_b64 exec, 1\n" \
  "ds_add_u32 v6, v7 offset:8\n" \
  "s_mov_b64 exec, -1\n" \
  "v_xor_b32 v6, 4, v6\n" \
  "v_mfma_f32_16x16x32_bf16 v[42:45], v[24:27], v[124:127], v[42:45]\n" \
  "v_mfma_f32_16x16x32_bf16 v[48:51], v[24:27], v[188:191], v[48:51]\n" \
  "v_mfma_f32_16x16x32_bf16 v[54:57], v[24:27], v[252:255], v[54:57]\n" \
  "s_waitcnt lgkmcnt(3)\n" \
  "v_mfma_f32_16x16x32_bf16 v[60:63], v[24:27], v[28:31], v[60:63]\n" \
  "s_waitcnt lgkmcnt(2)\n" \
  "v_mul_f32 v42, v42, v32\n" \
  "v_mul_f32 v43, v43, v33\n" \
  "v_mul_f32 v44, v44, v34\n" \
  "v_mul_f32 v45, v45, v35\n" \
  "s_waitcnt lgkmcnt(1)\n" \
  "v_fmac_f32 v42, v36, v8\n" \
  "v_fmac_f32 v43, v37, v8\n" \
  "v_fmac_f32 v44, v38, v8\n" \
  "v_fmac_f32 v45, v39, v8\n" \
  "v_cvt_pk_bf16_f32 v42, v42, v43\n" \
  "v_cvt_pk_bf16_f32 v43, v44, v45\n" \
  "buffer_store_dwordx4 v[40:43], v2, s[40:43], s65 offen offset:0 sc1\n" \
  "v_mul_f32 v48, v48, v32\n" \
  "v_mul_f32 v49, v49, v33\n" \
  "v_mul_f32 v50, v50, v34\n" \
  "v_mul_f32 v51, v51, v35\n" \
  "v_fmac_f32 v48, v36, v9\n" \
  "v_fmac_f32 v49, v37, v9\n" \
  "v_fmac_f32 v50, v38, v9\n" \
  "v_fmac_f32 v51, v39, v9\n" \
  "v_cvt_pk_bf16_f32 v48, v48, v49\n" \
  "v_cvt_pk_bf16_f32 v49, v50, v51\n" \
  "ds_read_b32 v5, v6 offset:0\n" \
  "buffer_store_dwordx4 v[46:49], v2, s[40:43], s65 offen offset:1024 sc1\n" \
  "v_mul_f32 v54, v54, v32\n" \
  "v_mul_f32 v55, v55, v33\n" \
  "v_mul_f32 v56, v56, v34\n" \
  "v_mul_f32 v57, v57, v35\n" \
  "v_fmac_f32 v54, v36, v10\n" \
  "v_fmac_f32 v55, v37, v10\n" \
  "v_fmac_f32 v56, v38, v10\n" \
  "v_fmac_f32 v57, v39, v10\n" \
  "v_cvt_pk_bf16_f32 v54, v54, v55\n" \
  "v_cvt_pk_bf16_f32 v55, v56, v57\n" \
  "buffer_store_dwordx4 v[52:55], v2, s[40:43], s65 offen offset:2048 sc1\n" \
  "v_mul_f32 v60, v60, v32\n" \
  "v_mul_f32 v61, v61, v33\n" \
  "v_mul_f32 v62, v62, v34\n" \
  "v_mul_f32 v63, v63, v35\n" \
  "v_fmac_f32 v60, v36, v4\n" \
  "v_fmac_f32 v61, v37, v4\n" \
  "v_fmac_f32 v62, v38, v4\n" \
  "v_fmac_f32 v63, v39, v4\n" \
  "v_cvt_pk_bf16_f32 v60, v60, v61\n" \
  "v_cvt_pk_bf16_f32 v61, v62, v63\n" \
  "buffer_store_dwordx4 v[58:61], v2, s[40:43], s65 offen offset:3072 sc1\n" \
  "s_waitcnt lgkmcnt(0)\n" \
  "s_add_u32 s65, s65, 0x40000\n" \
  "s_sub_u32 s64, s64, 1\n" \
  "s_mov_b32 s68, 1\n" \
  "s_cmp_lg_u32 s64, 0\n" \
  "s_cbranch_scc1 .LCW_TOP_%=\n" \
  "s_waitcnt vmcnt(0)\n" \
  "s_mov_b64 exec, 1\n" \
  "buffer_store_dword v7, off, s[56:59], s72 sc1\n" \
  "s_mov_b64 exec, -1\n" \
  "s_add_u32 s72, s72, 0x1000\n" \
  "s_mov_b32 s68, 0\n" \
  ".LCW_END_%=:\n" \
  "s_endpgm\n"
constexpr int RG = 2;
#define MARKS(s) __builtin_amdgcn_raw_buffer_load_b32(rm, (c >> 2) * 128 + (c & 3) * 4, (s) * 2048, AUX_SC1)
struct Yes { static constexpr bool value = true; };
struct No { static constexpr bool value = false; };
#define RFL(x) __builtin_amdgcn_readfirstlane(x)
#define HALF(x, i) BF8(((i) ? __builtin_shufflevector(x, x, 4, 5, 6, 7) : __builtin_shufflevector(x, x, 0, 1, 2, 3)))
constexpr int P_W = 65536, P_ST = 131072, P_SS = P_ST + 1024, P_CN = P_SS + 512;
template <bool PR>
__device__ __forceinline__ void role_proj_t(const MegaArgs& A, char* smem, const int L, const int idx, const bool kv) {
  const int w = A.wv;
  const int l = lane_id();
  const int c = l & 15, g = l >> 4;
  char* ws = A.ws;
  unsigned* FL = (unsigned*)(ws + OFF_FL);
  unsigned* TMO = FL + FL_TMO;
  const unsigned sb = lds_base(smem);
  auto cn = (volatile __attribute__((address_space(3))) unsigned*)(unsigned long)(sb + P_CN);
  u64 tag;
  const bool h4 = kv && L;
  const __amdgpu_buffer_rsrc_t rsM = MKRS(ws + OFF_MEMK + L * SZ_MEM, kv && w == 2 ? (4 - L) * SZ_MEM : 0);
  const bool mc = kv && w == 2 && (MCE < 2 || !L);
  constexpr int NMC = MCE == 2 ? 8 : 4;
  const int b0 = RFL(h4 ? (H4S ? 2 : 3) * w : w == 2 ? 0 : HX0),
            b1 = RFL(h4 ? (H4S ? 2 * w + 2 : w == 2 ? 8 : 3 * w + 3 * (w < 2)) : w == 2 ? HX0 : 8);
  const __amdgpu_buffer_rsrc_t rsF = MKRS(FL + FL_FC + L * 64 * 32 * 32, 64 * 4096);
  const int fo = ((blockIdx.x & 3) * 8 + ((H4S != 0 && h4) || w == 3 ? l >> 3 : min(b0 + (l >> 3), b1 - 1))) * 128 + (l & 7) * 4;
#define FC_LOOK(s) __builtin_amdgcn_raw_buffer_load_b32(rsF, fo, (s) * 4096, AUX_SC1)
#define MEM_CLEAR(s)                                                                                                          \
  _Pragma("unroll") for (int i = 0; i < NMC; ++i)                                                                             \
    ST128((v4u{0u, 0u, 0u, 0u}), rsM, l * 16 + (i & 1) * 1024, (s) * 65536 + idx * 2048 + (i >> 1) * (int)SZ_MEM * (MCE == 2 ? 1 : 2));
  const char* Xb = ws + (L ? OFF_X3 : OFF_XA);
  const __amdgpu_buffer_rsrc_t rsS = MKRS(ws + OFF_H + L * (SZ_X / 2), SZ_X / 2);
  unsigned nf = 0u;
  bool early = false;
  v4u t0[16];
#define X_ASK(s) \
  for (int i = b0; i < b1; ++i) glds64c(Xb + RFL((s) * 32768 + i * 4096), l * 16, RFL(sb + ((s) & 1) * 32768 + i * 4096));
#define S_ASK(s, t) _Pragma("unroll") for (int i = 0; i < 16; ++i) t[i] = LD128(rsS, l * 16, (s) * 16384 + i * 1024);
  {
    const int j = w & 3;
    const bool vt = kv && j >= 2;
    constexpr int NT = PR ? 3 : 1;
    v8u wq[NT][8];
    const u16* Wm = (kv ? (PR ? A.Wv : A.Wk) : A.Wq) - (PR ? 128 : 0) * HD;
    const int hm = kv ? idx : 2 * idx + PR;
    const int q = (l >> 2) & 3, p = l & 3, f = q | ((g & 1) << 2);
    unsigned ra[NT];
#pragma unroll
    for (int m = 0; m < NT; ++m) {
      const int ct = PR ? m : 3;
      ra[m] = sb + (j >> 1) * 65536 + (8 * g + q) * 256 + ((4 * (j & 1) + (vt ? ct : (ct & 2) + (p >> 1))) ^ f) * 32 + (vt ? p * 8 : (p & 1) * 16 + (ct & 1) * 8);
    }
#define W_ISSUE(n) wqkv_issue<false>(Wm + (128 * (n) - 256 * ((n) & 1)) * HD, (n) & 1, hm, w, l, sb, (WNT >> (kv ? 1 + PR : 3) & 1) && (L == 0 || (WNT & 32)))
#define W_STAGE(n, WAIT, NEXT)                                                                             \
  {                                                                                                        \
    WAIT;                                                                                                  \
    if (n == 3 && EL == 2 && !PR && L == 0 && w >= 2) nf = FC_LOOK(0);                                     \
    if (n == 3 && EL == 1 && !PR && L == 0 && w >= 2) {                                                    \
      early = __all(nf != 0u);                                                                             \
      if (early) {                                                                                         \
        X_ASK(0)                                                                                           \
        if (w == 3) { S_ASK(0, t0) }                                                                       \
      }                                                                                                    \
      nf = FC_LOOK(early);                                                                                 \
    }                                                                                                      \
    RAW_BAR();                                                                                             \
    if (n == 3 && EL == 3 && !PR && L == 0 && w >= 2) nf = FC_LOOK(0);                                     \
    _Pragma("unroll") for (int m = 0; m < NT; ++m) {                                                       \
      _Pragma("unroll") for (int t = 0; t < 2; ++t) {                                                      \
        const unsigned a_ = ra[m] + ((n) & 1) * 32768 + t * 16384;                                         \
        const v2u r0 = trd(a_), r1 = trd(a_ + 1024), r2 = trd(a_ + 8192), r3 = trd(a_ + 8192 + 1024);      \
        wq[m][2 * (n) + t] = v8u{r0.x, r0.y, r1.x, r1.y, r2.x, r2.y, r3.x, r3.y};                          \
      }                                                                                                    \
    }                                                                                                      \
    _Pragma("unroll") for (int m = 0; m < 2 * NT; ++m) asm volatile("" : "+v"(wq[m >> 1][2 * (n) + (m & 1)])); \
    RAW_BAR();                                                                                             \
    NEXT;                                                                                                  \
    _Pragma("unroll") for (int m = 0; m < 4 * NT; ++m)                                                     \
      cs[m % NT] = MFMA16(HALF(wq[m % NT][2 * (n) + m / NT / 2], (m / NT) & 1), BF8(one), cs[m % NT]);     \
    _Pragma("unroll") for (int m = 0; m < NT; ++m) asm volatile("" : "+v"(cs[m]));                         \
  }
    if (EL && !MC3 && mc) { MEM_CLEAR(0) }
    W_ISSUE(0);
    W_ISSUE(1);
    const bool c1 = GEN_T1 != 0 && kv && !L;
    if (c1) {
      const __amdgpu_buffer_rsrc_t rz = MKRS(ws + RFL((w & 2) ? (int)OFF_VF + idx * 8192 + ((2 + PR) << 18) : (int)OFF_QK + idx * 16384 + 8192 + ((2 + PR) << 19)) + (w & 1) * 4096, 4096);
#pragma unroll
      for (int i = 0; i < 4; ++i) ST128((v4u{0u, 0u, 0u, 0u}), rz, l * 16 + i * 1024, 0);
    }
    if (PR && vt) TOUCH("CW");
    if (PR && !vt) TOUCH("CP");
    tag = call_tag(A);
    if (w == 0 && l < 8) cn[l] = l == 1 ? 3u : l == 5 ? 2u : 0u;
    const v4u one = {0x3f803f80u, 0x3f803f80u, 0x3f803f80u, 0x3f803f80u};
    f32x4 cs[NT] = {};
    u64 rv = 0;
    static_assert(GEN_T1 == 0 || GEN_T1 == 3, "clear");
    W_STAGE(0, if (c1) WAIT_VM(12); else WAIT_VM(8), W_ISSUE(2); if (EL) RD_ASK(rv));
    SST(4);
    W_STAGE(1, if (EL) WAIT_VM(9); else WAIT_VM(8), W_ISSUE(3); if (EL != 0 && MC3 != 0 && mc) { MEM_CLEAR(0) if (MC3 == 2) { MEM_CLEAR(1) } });
    SST(5);
    W_STAGE(2, if (EL != 0 && MC3 == 2 && mc) WAIT_VM(16); else if (EL != 0 && MC3 != 0 && mc) WAIT_VM(12); else WAIT_VM(8), (void)0);
    SST(6);
    asm volatile("" : "+v"(rv));
    if (!EL) ready_wait(A, tag, ready_look(A));
    for (unsigned spins = 0; EL && !__all(rv == tag) && spins < SPIN_CAP; ++spins) {
      __builtin_amdgcn_s_sleep(8);
      RD_ASK(rv);
      asm volatile("s_waitcnt vmcnt(0)" : "+v"(rv)::"memory");
    }
    if (EL == 1 && !PR && L == 0 && w >= 2) nf = FC_LOOK(0);
    W_STAGE(3, if (EL >= 2 && MC3 == 2 && mc) WAIT_VM(8); else if (EL >= 2 && MC3 == 1 && mc) WAIT_VM(4); else WAIT_VM(0), if (!PR) {
      _Pragma("unroll") for (int ks = 0; ks < 16; ++ks)
        *reinterpret_cast<bf16x8*>(smem + P_W + j * 16384 + ks * 1024 + l * 16) = HALF(wq[0][ks >> 1], ks & 1);
    });
    SST(7);
#undef W_STAGE
#undef W_ISSUE
#pragma unroll
    for (int m = 0; m < NT; ++m) *reinterpret_cast<f32x4*>(smem + P_ST + j * 256 + (PR ? m : 3) * 64 + g * 16) = cs[m];
    RAW_BAR();
    SST(8);
    if constexpr (PR) {
      const v4u rsO = mk_srd(ws + RFL(vt ? TOK_V(ws, L) : TOK_QK(ws, L)), RFL((unsigned)(vt ? SZ_O : SZ_QK) >> (GEN_TR * L ? 6 - GEN_TR : 0)));
      const v4u rsP = mk_srd(FL + RFL(kv ? FL_FQ + (L * 64 * 32 + idx) * 32 + 2 * j : FL_FP + (L * 16 + idx) * 32 + j), 63 * 4096 + 4);
      const int so = RFL(vt ? idx * 8 + 4 * (j & 1) : (kv ? idx * 16 + 8 : (2 * idx + (j >> 1)) * 16) + 2 * (j & 1));
      const v8u pv = {sb + 32768 + g * 512 + c * 16, sb + P_W + j * 16384 + l * 16, (unsigned)l * 16, sb + P_SS + 256 + (vt ? g * 16 : c * 4),
                      sb + P_ST + j * 256 + (vt ? c * 4 : g * 16), 0u, 0u, 0u};
      const char* dbg = nullptr;
#define P_OPS                                                                                                                               \
  ::"{v[64:71]}"(wq[0][0]), "{v[72:79]}"(wq[0][1]), "{v[80:87]}"(wq[0][2]), "{v[88:95]}"(wq[0][3]), "{v[96:103]}"(wq[0][4]),                \
      "{v[104:111]}"(wq[0][5]), "{v[112:119]}"(wq[0][6]), "{v[120:127]}"(wq[0][7]), "{v[128:135]}"(wq[1][0]), "{v[136:143]}"(wq[1][1]),     \
      "{v[144:151]}"(wq[1][2]), "{v[152:159]}"(wq[1][3]), "{v[160:167]}"(wq[1][4]), "{v[168:175]}"(wq[1][5]), "{v[176:183]}"(wq[1][6]),     \
      "{v[184:191]}"(wq[1][7]), "{v[192:199]}"(wq[2][0]), "{v[200:207]}"(wq[2][1]), "{v[208:215]}"(wq[2][2]), "{v[216:223]}"(wq[2][3]),     \
      "{v[224:231]}"(wq[2][4]), "{v[232:239]}"(wq[2][5]), "{v[240:247]}"(wq[2][6]), "{v[248:255]}"(wq[2][7]), "{v[0:7]}"(pv),               \
      "{s[40:43]}"(rsO), "{s[56:59]}"(rsP), "{s64}"(NSEG), "{s65}"(so * 1024), "{s70}"(sb + P_CN), "{s74}"(RFL(L ? 19 + GEN_TR - vt : 0)), "{s75}"(RFL(!kv ? 64 : L ? 0 : 63 - GEN_T1)), "{s[36:37]}"(dbg) \
      : "memory", "vcc", "scc", "s80", "v63"
      if (vt) asm volatile("s_mov_b64 exec, -1\ns_waitcnt vmcnt(0) lgkmcnt(0)\ns_nop 4\n" V_ASM P_OPS);
      else asm volatile("s_mov_b64 exec, -1\ns_waitcnt vmcnt(0) lgkmcnt(0)\ns_nop 4\n" P_ASM P_OPS);
#undef P_OPS
      return;
    }
  }
  __builtin_amdgcn_s_setprio(RPRIO);
  if (h4 || w >= 2) {
    if (!EL && mc) { MEM_CLEAR(0) }
    auto stats = [&](const v4u (&t)[16], const int s) {
      float a = 0.f, b = 0.f, qa = 0.f, qb = 0.f;
#pragma unroll
      for (int i = 0; i < 16; ++i) {
        a = one(a + __uint_as_float(t[i].x));
        b = one(b + __uint_as_float(t[i].z));
        qa = one(qa + __uint_as_float(t[i].y));
        qb = one(qb + __uint_as_float(t[i].w));
      }
      a = xsum32(xsum16(a));
      b = xsum32(xsum16(b));
      float da = 0.f, db = 0.f;
#pragma unroll
      for (int i = 0; i < 16; ++i) {
        const float u = one(__uint_as_float(t[i].x) - a * (1.0f / 64)), v = one(__uint_as_float(t[i].z) - b * (1.0f / 64));
        da = one(da + u * u);
        db = one(db + v * v);
      }
      const float var = xsum32(swap_add16(qa + da * 0.125f, qb + db * 0.125f)) * (1.0f / DM);
      const float ri = 1.0f / bfr1(sqrtf(bfr1(bfr1(var) + 1e-5f)));
      reinterpret_cast<float*>(smem + P_SS)[(s & 1) * 64 + (g >> 1) * 32 + 2 * c + (g & 1)] = g < 2 ? bfr1(((g & 1) ? b : a) * (1.0f / DM)) * (EP ? -ri : 1.0f) : ri;
      asm volatile("" ::: "memory");
      if (l == 0) cn[4 + (s & 1)] = 2 * s + 4;
    };
    static_assert(EL != 1 || MC3 == 2, "clear");
    if (early) {
      if (w == 3) WAIT_VM(17);
      else WAIT_VM(1);
      if (l == 0) __hip_atomic_fetch_add((__attribute__((address_space(3))) unsigned*)cn, 3u, __ATOMIC_RELAXED, __HIP_MEMORY_SCOPE_WORKGROUP);
      SSX(10, w == 2);
      SSX(9, w == 2);
      if (w == 3) stats(t0, 0);
      SSX(11, w == 3);
    }
    if (L) {
      char* ws = A.ws;
      int w = A.wv, i1 = idx + 32 * kv;
      asm volatile("" : "+s"(ws), "+s"(w), "+s"(i1));
      const int idx = i1 & 31;
      const bool h4 = i1 >= 32, mc = h4 && w == 2;
      const int b0 = h4 ? 3 * w : w == 2 ? 0 : HX0, b1 = h4 ? (w == 2 ? 8 : 3 * w + 3 * (w < 2)) : w == 2 ? HX0 : 8;
      unsigned* FL = (unsigned*)(ws + OFF_FL);
      unsigned* TMO = FL + FL_TMO;
      const char* Xb = ws + OFF_X3;
      const __amdgpu_buffer_rsrc_t rsM = MKRS(ws + OFF_MEMK + SZ_MEM, 3 * SZ_MEM), rsF = MKRS(FL + FL_FC + 64 * 32 * 32, 64 * 4096),
                                   rsS = MKRS(ws + OFF_H + SZ_X / 2, SZ_X / 2);
      unsigned mk;
      int l16 = l * 16;
      asm volatile("" : "+v"(l16));
      const __amdgpu_buffer_rsrc_t rm = MKRS(FL + FL_FW + (blockIdx.x & 3) * 128, 63 * 2048 + 512);
      const __amdgpu_buffer_rsrc_t rsK = MKRS(ws + (w < 2 ? TOK_QK(ws, 1) + idx * 16384 + 8192 : TOK_V(ws, 1) + idx * 8192) + (w & 1) * 4096,
                                              KC && h4 ? (GEN_TR ? (1 << GEN_TR) - 1 : 63) * (w < 2 ? 0x80000 : 0x40000) + 4096 : 0);
#define KV_CLEAR(s) \
  _Pragma("unroll") for (int i = 0; i < 4 * KC; ++i) ST128((v4u{0u, 0u, 0u, 0u}), rsK, l16 + i * 1024, (s) << (w < 2 ? 19 : 18));
      static_assert(KC || !GEN_TL, "clear");
      if (GEN_F0) { KV_CLEAR(0) }
      KV_CLEAR(1)
      if (!wait_n<SLP_V, 32>(FL + FL_FZ + (blockIdx.x & 7) * 64, 64, TMO)) return;
      const __amdgpu_buffer_rsrc_t rf = MKRS(FL + FL_FW + (64 - (1 << GEN_TR)) * 512 + (blockIdx.x & 3) * 128, 65 * 2048);
#define FREE_LOOK(s) __builtin_amdgcn_raw_buffer_load_b32(rf, (c >> 2) * 128 + (c & 3) * 4, (s) * 2048, AUX_SC1)
#pragma unroll 1
      for (unsigned spins = 0;; ++spins) {
        if (spin_timeout(spins, TMO)) return;
        __builtin_amdgcn_s_sleep(32);
        mk = MARKS(0);
        if (__all(mk != 0u)) break;
        asm volatile("" ::: "memory");
      }
      gate(latest(mk, RFL(mk)) + KD + K0 + (h4 ? 0 : P2D));
      auto feed = [&](auto ST_) {
        constexpr bool ST = ST_.value;
        bool due = true;
        int sf = -1;
#pragma unroll 1
        for (int s = 0, n = 0; s < NSEG && n < 4 * NSEG; ++n) {
#pragma unroll 1
          for (unsigned spins = 0; !due; ++spins) {
            if (spin_timeout(spins, TMO)) return;
            if (spins) __builtin_amdgcn_s_sleep(SLP_H);
            due = __all(FC_LOOK(s) != 0u);
            asm volatile("" ::: "memory");
          }
          TS(4, s, 128);
#pragma unroll 1
          for (unsigned spins = 0; cn[2 + (s & 1)] < 4u * (s >> 1); ++spins) {
            if (spins > 64u * SPIN_CAP) return;
            __builtin_amdgcn_s_sleep(2);
          }
          TS(5, s, 128);
          X_ASK(s)
          v4u t[ST ? 16 : 1];
          if (ST) {
            int so = s * 16384;
#pragma unroll
            for (int i = 0; i < 16; ++i) {
              if ((i & 3) == 0) asm volatile("" : "+s"(so));
              t[i] = LD128(rsS, l16 + (i & 3) * 1024, so);
              if ((i & 3) == 3) so += 4096;
              __builtin_amdgcn_sched_barrier(0);
            }
          }
          mk = MARKS(s + 1);
          unsigned z = 1u, fr = 1u;
          if (GEN_TR != 0 && ST) fr = FREE_LOOK(s + 1);
          if (GEN_TR != 0 && ST) WAIT_VM(18);
          else if (ST) WAIT_VM(17);
          else WAIT_VM(1);
#pragma unroll 1
          for (int k = b0; k < b1; k += 3) {
            const unsigned* q = reinterpret_cast<const unsigned*>(smem + (s & 1) * 32768 + k * 4096 + l16 + 4);
            const unsigned* q2 = q + min(b1 - k - 1, 2) * 1024;
#pragma unroll
            for (int i = 0; i < 12; i += 2) z = i < 8 ? umin3(z, q[256 * i], q[256 * i + 256]) : umin3(z, q2[256 * (i - 8)], q2[256 * (i - 7)]);
          }
          if (ST) {
#pragma unroll
            for (int i = 0; i < 16; ++i) z = umin3(z, t[i].y, t[i].w);
          }
          due = __all(z != 0u);
          if (!due) {
            due = KR && sf != s;
            sf = s;
            continue;
          }
          asm volatile("" ::: "memory");
          if (l == 0 && !(ST && h4)) __hip_atomic_fetch_add((__attribute__((address_space(3))) unsigned*)cn + (s & 1), h4 ? 2u : 3u, __ATOMIC_RELAXED, __HIP_MEMORY_SCOPE_WORKGROUP);
          TS(6, s, 128);
          if (GEN_F0 != 0 && h4 && s == 0 && l == 0) __hip_atomic_store((gu32*)(FL + FL_FQ + (64 * 32 + idx) * 32 + 2 * w + 1), (now10() + KF) | 1u, RLX_AGENT);
          if constexpr (ST) {
            stats(t, s);
            TS(7, s, 192);
#pragma unroll 1
            for (unsigned spins = 0; GEN_TR != 0 && s >= (1 << GEN_TR) - 1 && !__all(fr != 0u); ++spins) {
              if (spin_timeout(spins, TMO)) return;
              __builtin_amdgcn_s_sleep(SLP_H);
              fr = FREE_LOOK(s + 1);
              asm volatile("" ::: "memory");
            }
          }
          due = __all(mk != 0u);
          const unsigned td = latest(mk, RFL(mk)) + (h4 ? KD : PD) + (s < 7 - R0 ? K1 : s >= NSEG - 1 - TS1 ? -TL3 : 0);
          if (!ST && mc && s < NSEG - 1 && s) { MEM_CLEAR(s + 1) }
          if (h4 && (!GEN_TR || s < (1 << GEN_TR) - 2)) {
            if (KCT && due) gate(td - KCT);
            KV_CLEAR(s + 2)
          }
          if (due) gate(td);
          ++s;
        }
      };
      static_assert(HX0 == 6, "blocks");
      if (w == 3) feed(Yes{});
      else feed(No{});
      return;
    }
    unsigned tn = now10();
    for (int s = RFL((int)early); s < NSEG; ++s) {
#pragma unroll 1
      for (unsigned spins = 0; !__all(nf != 0u); ++spins) {
        if (spin_timeout(spins, TMO)) return;
        if (spins) __builtin_amdgcn_s_sleep(SLP_H);
        nf = FC_LOOK(s);
        asm volatile("" ::: "memory");
      }
      if (NFW) asm volatile("" ::"v"(nf));
      TS(4, s, 128);
#pragma unroll 1
      for (unsigned spins = 0; cn[2 + (s & 1)] < 4u * (s >> 1); ++spins) {
        if (spins > 64u * SPIN_CAP) return;
        __builtin_amdgcn_s_sleep(2);
      }
      if (s >= PACE0) gate(tn);
      else tn = now10();
      tn += PACE;
      TS(5, s, 128);
      X_ASK(s)
      if (w != 3) {
        nf = FC_LOOK(s + 1);
        if (mc && s < NSEG - 1 && (EL == 0 || MC3 != 2 || s)) {
          MEM_CLEAR(s + 1)
          if (NMC == 8) WAIT_VM(8);
          else WAIT_VM(4);
        } else {
          WAIT_VM(0);
        }
        if (l == 0) __hip_atomic_fetch_add((__attribute__((address_space(3))) unsigned*)cn + (s & 1), 3u, __ATOMIC_RELAXED, __HIP_MEMORY_SCOPE_WORKGROUP);
        TS(6, s, 128);
      } else {
        v4u t[16];
        S_ASK(s, t)
        WAIT_VM(16);
        nf = FC_LOOK(s + 1);
        if (l == 0) __hip_atomic_fetch_add((__attribute__((address_space(3))) unsigned*)cn + (s & 1), 3u, __ATOMIC_RELAXED, __HIP_MEMORY_SCOPE_WORKGROUP);
        stats(t, s);
        TS(7, s, 192);
      }
    }
    return;
  }
  const int RL = !kv, rp = idx & 15, u = RFL(kv ? idx >> 4 : L);
  const __amdgpu_buffer_rsrc_t rsW = MKRS(ws + OFF_PW, SZ_PW);
  const __amdgpu_buffer_rsrc_t rsX = MKRS(ws + (RL ? OFF_X3 : OFF_XA), SZ_X);
  const __amdgpu_buffer_rsrc_t rsY = RL ? MKRS(A.out, SZ_X) : MKRS(ws + OFF_X2, SZ_X);
  const int r = 2 * rp + (g >> 1), yo = r * (DM * 2) + (l & 31) * 16 + u * 512;
  const __amdgpu_buffer_rsrc_t rsC = MKRS(ws + OFF_H, OFF_QK - OFF_H);
#define X_CLEAR(s)                                                                                              \
  {                                                                                                             \
    ST128((v4u{0u, 0u, 0u, 0u}), rsY, yo, (s) * SEG * DM * 2);                                                  \
    if (KD) {                                                                                                   \
      ST128((v4u{0u, 0u, 0u, 0u}), rsC, l * 16, (int)(OFF_X3 - OFF_H) + ((s) * 32 + idx) * 1024);               \
      if (l < 32) ST128((v4u{0u, 0u, 0u, 0u}), rsC, l * 16, (int)(SZ_X / 2) + ((s) * 32 + idx) * 512);          \
    }                                                                                                           \
  }
  if (kv) {
    if (XZD) gate(now10() + XZD);
    if (XZP & 3) {
      if (!wait_n<SLP_V, 32>(FL + FL_FZ + (blockIdx.x & 7) * 64, 64, TMO)) return;
    } else {
      for (int s = w; s < (XZP ? 2 * RG : NSEG); s += RG) X_CLEAR(s)
      DRAIN();
      if (l < 8) set_flag(FL + FL_FZ + l * 64 + 2 * idx + w);
    }
  }
  v4u sel;
#pragma unroll
  for (int e = 0; e < 4; ++e) sel[e] = (g >> 1) == (c >> 3) && ((c & 7) >> 1) == e ? 0x3f80u << (16 * (c & 1)) : 0u;
  unsigned tl = now10();
  int po = (g & 1) * 32768 + (c >> 2) * 128 + (g >> 1) * 64 + (c & 3) * 16;
  asm volatile("" : "+v"(po));
  for (int s = w; s < NSEG; s += RG) {
    v4u xr;
    if (GF_R) {
      gate(tl);
      const gu32* f = (gu32*)(FL + FL_FW) + ((RL * 64 + s) * 4 + (blockIdx.x & 3)) * 4 * 32 + (c >> 2) * 32 + (c & 3);
      unsigned mk;
      for (unsigned spins = 0;; ++spins) {
        tl = now10() + GF_R;
        mk = __hip_atomic_load(f, RLX_AGENT);
        if (__all(mk != 0u) || spin_timeout(spins, TMO)) break;
        if (s < NSEG - RFA) __builtin_amdgcn_s_sleep(SLP_R);
      }
      if (EF) {
        const unsigned tp = latest(mk, tl);
        asm volatile("" ::: "memory");
        xr = LD128(rsX, (l & 31) * 512 + r * 16, s * 32768 + u * 16384);
        gate(tp - (RL ? (s >= NSEG - RLS ? RLEAD : 0) : s >= NSEG - TS1 ? TL1 : s ? 0 : -R0D));
        if (GR2) tl = tp + ((unsigned)(s - GR0) >= NSEG - 12 - GR0 ? GR3 : GR2);
      }
      asm volatile("" ::: "memory");
    } else
    grp_wait<SLP_R, 4, GATE_R>(true, nullptr, 0, FL + FL_FW + ((RL * 64 + s) * 4 + (blockIdx.x & 3)) * 4 * 32, 16, TMO, tl);
    if (s >= NSEG - 6 && !(EF != 0 && GR2 != 0)) tl -= GF_R - (s >= NSEG - 4 ? GF_RT : GF_RU);
    if (!EF) DRAIN();
    TS(0, s, w * 64);
    v4u p[8][2];
    if (!EF) xr = LD128(rsX, (l & 31) * 512 + r * 16, s * 32768 + u * 16384);
    f32x4 ac[2];
    const int sr = EF ? s & ((1 << RGB) - 1) : s;
    const unsigned old = (s >> RGB) & 1;
    for (unsigned spins = 0;; ++spins) {
      int so = (RL * 64 + sr) * 16 * 32768 + rp * 2048 + u * 1024;
#pragma unroll
      for (int k = 0; k < 8; ++k) {
        asm volatile("" : "+s"(so));
#pragma unroll
        for (int b = 0; b < 2; ++b) p[k][b] = __builtin_amdgcn_raw_buffer_load_b128(rsW, po + b * 512, so, RAUX);
        so += 65536;
      }
      __builtin_amdgcn_sched_barrier(0);
      unsigned z[2] = {1u, 1u};
      ac[0] = ac[1] = f32x4{};
#pragma unroll
      for (int k = 0; k < 8; ++k)
#pragma unroll
        for (int b = 0; b < 2; ++b) {
          ac[b] = MFMA16(BF8(sel), BF8(p[k][b]), ac[b]);
          z[b] = __builtin_amdgcn_bitop3_b32(z[b], p[k][b].x, old, 0x60);
          __builtin_amdgcn_sched_barrier(0);
        }
      if (!EF || !RVAL || __all((z[0] & z[1]) != 0u) || spin_timeout(spins, TMO)) break;
      if (SLP_D) __builtin_amdgcn_s_sleep(SLP_D);
      asm volatile("" ::: "memory");
    }
    v4u xo;
    {
      const auto x = __builtin_amdgcn_permlane16_swap(pk2s(ac[0][0], ac[0][1]), pk2s(ac[1][0], ac[1][1]), false, false);
      const auto y = __builtin_amdgcn_permlane16_swap(pk2s(ac[0][2], ac[0][3]), pk2s(ac[1][2], ac[1][3]), false, false);
      const v4u a = {x[0], y[0], x[1], y[1]};
#pragma unroll
      for (int e = 0; e < 4; ++e) {
        xo[e] = pk2s(lo16(xr[e]) + lo16(a[e]), hi16(xr[e]) + hi16(a[e]));
        if (kv) xo[e] = xo[e] ? xo[e] : 0x8000u;
      }
      ST128(xo, rsY, yo, s * SEG * DM * 2);
    }
    TS(1, s, w * 64);
    if (kv) {
      const unsigned p0 = __builtin_amdgcn_readlane(xo[0], 0), p1 = __builtin_amdgcn_readlane(xo[0], 32);
      const float pv = lo16(g < 2 ? p0 : p1);
      float sm = 0.f, q0 = 0.f, q1 = 0.f;
#pragma unroll
      for (int e = 0; e < 4; ++e) {
        const float a = one(lo16(xo[e]) - pv), b = one(hi16(xo[e]) - pv);
        sm = one(sm + one(a + b));
        q0 = one(q0 + a * a);
        q1 = one(q1 + b * b);
      }
      sm = half_sum(sm);
      const float q = fmaxf(one(half_sum(q0 + q1)) - sm * sm * (1.0f / 256), 0.f);
      const u64 st = (u64)__float_as_uint(sm + pv * 256.f) | (u64)(__float_as_uint(q) | 1u) << 32;
      if ((l & 31) < 4) __hip_atomic_store((gu64*)(FL + FL_FR + ((s * 4 + (l & 3)) * 4 + (r >> 3)) * 32 + (r & 7) * 4 + 2 * u), st, RLX_AGENT);
      if (XZP == 4 && s + 2 * RG < NSEG) X_CLEAR(s + 2 * RG)
    }
    if (EF && s + (1 << RGB) < NSEG) PW_FREE(RL, s + (1 << RGB), 2 * rp + u);
    TS(3, s, w * 64);
  }
  if (L == 1 && idx == 0 && w == 0 && l == 0) __hip_atomic_store((gu64*)(ws + OFF_EP), tag, RLX_AGENT);
}
__device__ __forceinline__ void role_proj(const MegaArgs& A, char* smem, const int L, const int idx, const bool kv) {
  if (A.wv < 4) role_proj_t<false>(A, smem, L, idx, kv);
  else role_proj_t<true>(A, smem, L, idx, kv);
}

constexpr int CUN = 2144;
__device__ __forceinline__ v2u part_stats(const v4u o) {
  float v[8];
#pragma unroll
  for (int e = 0; e < 4; ++e) { v[2 * e] = lo16(o[e]); v[2 * e + 1] = hi16(o[e]); }
  const float s = ((v[0] + v[1]) + (v[2] + v[3])) + ((v[4] + v[5]) + (v[6] + v[7])), m = s * 0.125f;
  float q0 = 0.f, q1 = 0.f;
#pragma unroll
  for (int e = 0; e < 4; ++e) {
    const float a = v[e] - m, b = v[4 + e] - m;
    q0 += a * a;
    q1 += b * b;
  }
  return v2u{__float_as_uint(s), __float_as_uint(q0 + q1) | 1u};
}
#define PS_ST(o, voff, soff)                                                                                              \
  {                                                                                                                       \
    const v2u p_ = part_stats(o);                                                                                         \
    if (PSW) {                                                                                                            \
      const v4u q_ = {p_.x, p_.y, (unsigned)__builtin_amdgcn_update_dpp(0, (int)p_.x, 0xb1, 0xf, 0xf, true),              \
                      (unsigned)__builtin_amdgcn_update_dpp(0, (int)p_.y, 0xb1, 0xf, 0xf, true)};                         \
      if (!(l & 1)) ST128(q_, rps, voff, soff);                                                                           \
    } else {                                                                                                              \
      ST64(p_, rps, voff, soff);                                                                                          \
    }                                                                                                                     \
  }
constexpr int CRN = 176;
constexpr int CTN = 528;
constexpr int CV_LDS = (8 * CKN + 8 * 2 * CUN + 64 + 2 * 32 * 8 + 16 * CRN + 64 + 8 * CTN) * 2 + 8 * 1568;
__device__ __forceinline__ void role_conv(const MegaArgs& A, char* smem, const int cv) {
  u16* Kr = reinterpret_cast<u16*>(smem);
  u16* Uc = Kr + 8 * CKN;
  u16* Us = Uc + 8 * CUN + 63;
  u16* To = Uc + 8 * 2 * CUN + 64;
  u16* Sx = To + 32 * 8;
  u16* Rc = Sx + 32 * 8;
  u16* Rs = Rc + 8 * CRN + 63;
  u16* Ta = Rc + 16 * CRN + 64;
  const int w = A.wv;
  const int l = lane_id(), tid = 64 * w + l, c = l & 15, g = l >> 4;
  const int d = 8 * cv + w;
  char* ws = A.ws;
  unsigned* FL = (unsigned*)(ws + OFF_FL);
  unsigned* TMO = FL + FL_TMO;
  u16* kr = Kr + w * CKN;
  const u16* ka = kr + 2080 - 16 * c + 8 * g;
#define KFRAG(D0) (*reinterpret_cast<const f16x8*>(ka - (D0)))
  typedef __attribute__((address_space(3))) const unsigned* lpu;
  typedef __attribute__((address_space(3))) u16* lph;
#define BFRAG(a) __builtin_bit_cast(f16x8, (v4u{((lpu)(unsigned long)(a))[0], ((lpu)(unsigned long)(a))[1], ((lpu)(unsigned long)(a))[2], ((lpu)(unsigned long)(a))[3]}))
  f32x4 acc[8], al[5];
  v4u tx[4];
  const __amdgpu_buffer_rsrc_t rz1 = MKRS(ws + OFF_XA, SZ_X), rps = MKRS(ws + OFF_H, SZ_X);
  u16* uc = Uc + w * CUN;
  const unsigned ub = lds_base(reinterpret_cast<char*>(((c & 1) ? Us : Uc) + w * CUN + c + 8 * g + 32));
  const int t0 = 16 * c + 4 * g;
  float dsk, ysc;
  unsigned mk = 0u;
  const __amdgpu_buffer_rsrc_t rm = MKRS(FL + FL_FW + (blockIdx.x & 3) * 128, 63 * 2048 + 512);
#define SKIP(T) (*reinterpret_cast<const v2u*>(uc + 32 + 256 * (T) + t0))
#define TILE_OUT(dst, a, uu)                                                                   \
  {                                                                                            \
    v2u v_;                                                                                    \
    _Pragma("unroll") for (int e = 0; e < 2; ++e) {                                            \
      const unsigned yy = pk2(a[2 * e] * ysc, a[2 * e + 1] * ysc), u_ = uu[e];                 \
      const unsigned k_ = pk2(h2f((u16)u_) * dsk, h2f((u16)(u_ >> 16)) * dsk);                 \
      v_[e] = pk2(lo16(yy) + lo16(k_), hi16(yy) + hi16(k_));                                   \
    }                                                                                          \
    *reinterpret_cast<v2u*>((dst) + t0) = v_;                                                  \
  }
#define CHUNK_ST(i, src, N) CHUNK_STX(w + 8 * (i), tx[i], src, N)
#define CHUNK_STX(i, X, src, N)                                                                \
  {                                                                                            \
    const unsigned p_ = lds_base((char*)((src) + (c >> 2) * (N) + (tid & ~15) + 4 * (c & 3))); \
    const v2u a0_ = trd(p_), a1_ = trd(p_ + 8 * (N));                                          \
    const v4u a_ = {a0_.x, a0_.y, a1_.x, a1_.y};                                               \
    v4u o;                                                                                     \
    _Pragma("unroll") for (int e = 0; e < 4; ++e) o[e] = pk2(lo16(X[e]) + lo16(a_[e]), hi16(X[e]) + hi16(a_[e])); \
    ST128(o, rz1, (l >> 5) * 32768 + (l & 31) * 16, (2 * (i) * 64 + cv) * 512);                \
    PS_ST(o, (l >> 5) * 16384 + (l & 31) * 8, (2 * (i) * 64 + cv) * 256)                        \
  }
#define CHUNK_FLAG(i) set_flag(FL + FL_FC + ((8 * (i) + l) * 8 + (cv >> 3)) * 32 + (cv & 7))
  {
#define U_VAL(i)                                                                               \
  {                                                                                            \
    const float mb_ = lo16(st[i]), ri_ = 1024.0f / hi16(st[i]);                                 \
    _Pragma("unroll") for (int e = 0; e < 4; ++e) {                                            \
      const unsigned t_ = pk2(lo16(tx[i][e]) - mb_, hi16(tx[i][e]) - mb_);                     \
      const unsigned q_ = pk2(lo16(t_) * ri_, hi16(t_) * ri_);                                 \
      uv[e] = pkh(lo16(q_), hi16(q_));                                                         \
    }                                                                                          \
  }
    unsigned ta[3], k0p, st[4];
    v4u uv;
    v2u kn;
    u64 tag;
    const __amdgpu_buffer_rsrc_t rst = MKRS(ws + OFF_GT, (L_SEQ + 512) * 16);
    {
      KGen G;
      G.load(A, d, l);
      const u16 pk = A.Dskip[d];
      asm volatile("" ::: "memory");
      if (XL == 1) ask_slow(A, 16 * (w - 4) + (cv & 15), ta);
      if (XL) asm volatile("" : "+v"(G.pa), "+v"(G.pb), "+v"(G.pc), "+v"(G.pd), "+v"(G.pe)::"memory");
      tx[0] = *reinterpret_cast<const v4u*>(A.x0 + (size_t)tid * DM + 8 * cv);
      if (XL != 1) ask_slow(A, 16 * (w - 4) + (cv & 15), ta);
      tag = call_tag(A);
      G.setup();
      dsk = bf2f(pk) * (1.0f / 1024);
      G.st = smem + w * 16640;
      SST(0);
      ysc = G.scale();
      const Cx z = G.power(1.f);
      Cx y = cmul(G.C, z);
      G.rows(G.st + l * 8, y, z, 16);
      G.get(G.bh, G.bl, l);
      const float k0 = 2.0f * wave_sum(G.C.r.x + G.C.r.y);
      k0p = l == 0 ? pkh(k0, 0.f) : 0u;
      if (TP) {
        constexpr int R = 64 * TP, U0 = 2 * R + 32, U1 = 3 * R + 111, KN = 4 * R + 192;
        u16* k1 = reinterpret_cast<u16*>(smem + 8 * 16640) + w * KN;
        u16* t1 = reinterpret_cast<u16*>(smem + 8 * 16640) + 8 * KN;
        v4u s1 = {};
        u64 rv = 0;
        if (w < TP) {
          s1 = LD128(rst, tid * 16, (L_SEQ + R * (cv >> 4)) * 16);
          rv = ready_look(A);
        }
        if (l < R / 8 + 4) reinterpret_cast<v4u*>(k1)[l < R / 8 ? l + R / 8 + 4 : l - R / 8] = v4u{k0p, 0u, 0u, 0u};
        k1[U0 + (l < 32 ? l : R + l)] = k1[U1 + (l < 32 ? l : R + l)] = 0;
        G.columns(l, 0, R / 16);
        const f32x4 kt = G.tile<true>(l);
        if (c < R / 16) *reinterpret_cast<v2u*>(k1 + R + 28 - 16 * c - 4 * g) = v2u{pkh(kt.w, kt.z), pkh(kt.y, kt.x)};
        if (w < TP) {
          for (unsigned spins = 0; !__all(has_tag(tag, s1.x, s1.y) && has_tag(tag, s1.z, s1.w)) && spins < SPIN_CAP; ++spins) {
            __builtin_amdgcn_s_sleep(4);
            s1 = LD128(rst, tid * 16, (L_SEQ + R * (cv >> 4)) * 16);
          }
          st[0] = __builtin_amdgcn_perm(s1.w, s1.y, 0x07060302);
          U_VAL(0);
          u16* up = reinterpret_cast<u16*>(smem + 8 * 16640) + 32 + tid;
#pragma unroll
          for (int e = 0; e < 8; ++e) up[e * KN + U0] = up[e * KN + U1] = (u16)(uv[e >> 1] >> (16 * (e & 1)));
        }
        RAW_BAR();
        const unsigned u1 = lds_base(reinterpret_cast<char*>(k1 + ((c & 1) ? U1 : U0) + c + 8 * g));
        f32x4 a = {};
#pragma unroll
        for (int b = 0; b <= R / 32; ++b)
          a = MFMAH(BFRAG(u1 + 64 * b), *reinterpret_cast<const f16x8*>(k1 + R - 16 * (c & (R / 16 - 1)) + 8 * g + 32 * b), a);
        if (c < R / 16) {
          const v2u sk = *reinterpret_cast<const v2u*>(k1 + U0 + 32 + t0);
          TILE_OUT(t1 + w * (R + 16), a, sk);
        }
        RAW_BAR();
        SST(1);
        if (w < TP) CHUNK_ST(0, t1, R + 16);
        G.columns(l, R / 16, TPS);
        if (w < TP) {
          DRAIN();
          ready_wait(A, tag, rv);
          if (l < 8) CHUNK_FLAG(w);
        }
        SST(6);
        G.columns(l, TPS, 16);
      } else {
        G.columns(l, 0, 16);
      }
      const f32x4 kt = G.tile<true>(l);
      kn = v2u{pkh(kt.w, kt.z), pkh(kt.y, kt.x)};
    }
#define U_PUT(i)                                                                               \
  _Pragma("unroll") for (int e = 0; e < 4; ++e) {                                              \
    ((lph)(unsigned long)up0)[(2 * e) * CUN + 512 * (i)] = (u16)uv[e];                         \
    ((lph)(unsigned long)up0)[(2 * e + 1) * CUN + 512 * (i)] = (u16)(uv[e] >> 16);             \
    ((lph)(unsigned long)up1)[(2 * e) * CUN + 512 * (i)] = (u16)uv[e];                         \
    ((lph)(unsigned long)up1)[(2 * e + 1) * CUN + 512 * (i)] = (u16)(uv[e] >> 16);             \
  }
#define U_ROW(i) { U_VAL(i) U_PUT(i) }
    unsigned up0 = lds_base((char*)(Uc + 32 + tid)), up1 = lds_base((char*)(Us + 32 + tid));
    asm volatile("" : "+v"(up0), "+v"(up1), "+v"(kn), "+v"(k0p));
#define ST_GET(I0, I1)                                                                                                        \
  for (unsigned spins = 0; spins < SPIN_CAP; ++spins) {                                                                       \
    bool ok = true;                                                                                                           \
    v4u t_[4];                                                                                                                \
    _Pragma("unroll") for (int i = I0; i < I1; ++i) t_[i] = LD128(rst, (tid + 512 * i) * 16, 0);                              \
    _Pragma("unroll") for (int i = I0; i < I1; ++i) {                                                                         \
      ok = ok && has_tag(tag, t_[i].x, t_[i].y) && has_tag(tag, t_[i].z, t_[i].w);                                            \
      st[i] = __builtin_amdgcn_perm(t_[i].w, t_[i].y, 0x07060302);                                                            \
    }                                                                                                                         \
    if (__all(ok)) break;                                                                                                     \
    __builtin_amdgcn_s_sleep(4);                                                                                              \
    asm volatile("" ::: "memory");                                                                                            \
  }
    if (w < 4) {
      ST_GET(0, 2);
      U_VAL(0);
      asm volatile("" : "+v"(uv));
    }
#pragma unroll
    for (int i = 1; i < 4; ++i) tx[i] = *reinterpret_cast<const v4u*>(A.x0 + (size_t)(tid + 512 * i) * DM + 8 * cv);
    RAW_BAR();
    SST(2);
    if (w < 4) { U_PUT(0) }
    *reinterpret_cast<v2u*>(kr + 2076 - 16 * c - 4 * g) = kn;
    if (l < 38) reinterpret_cast<v4u*>(kr)[l < 32 ? 260 + l : l < 34 ? 194 + l : l - 34] = v4u{k0p, 0u, 0u, 0u};
    (l < 32 ? uc : Us + w * CUN - 32)[l] = 0;
    uc[32 + L_SEQ + l] = Us[w * CUN + 32 + L_SEQ + l] = 0;
    uc[32 + 256 + l] = Us[w * CUN + 32 + 256 + l] = 0;
    RAW_BAR();
    SST(11);
    {
      f32x4 a0 = {}, a1 = {};
      f16x8 bf[9], af[9];
#pragma unroll
      for (int b = 0; b < 9; ++b) {
        af[b] = KFRAG(32 - 32 * b);
        bf[b] = BFRAG(ub + 64 * (b - 1));
      }
      const v2u k0 = SKIP(0);
      __builtin_amdgcn_sched_barrier(0);
#pragma unroll
      for (int b = 0; b < 9; ++b) (b & 1 ? a1 : a0) = MFMAH(bf[b], af[b], (b & 1 ? a1 : a0));
      a0 += a1;
      TILE_OUT(Ta + w * CTN, a0, k0);
    }
    const u64 rv = ready_look(A);
    RAW_BAR();
    SST(3);
    if (w < 4) {
      if (w >= TP) CHUNK_ST(0, Ta, CTN);
      asm volatile("" : "+v"(tx[1]));
      U_ROW(1);
      DRAIN();
      ready_wait(A, tag, rv);
      if (l < 8 && w >= TP) CHUNK_FLAG(w);
      ST_GET(2, 4);
    } else {
      ST_GET(0, 4);
      ready_wait(A, tag, rv);
      asm volatile("" : "+v"(tx[0]), "+v"(tx[1]));
      U_ROW(0);
      U_ROW(1);
    }
    ASKED(ta);
    if (tid < (16 * CRN + 64) / 8) reinterpret_cast<v4u*>(Rc)[tid] = v4u{0u, 0u, 0u, 0u};
    SST(4);
    const gu64* fk = (gu64*)(ws + OFF_KG) + cv * 16 + w;
    u64 kg = __hip_atomic_load(fk, RLX_AGENT);
    U_ROW(2);
    for (unsigned spins = 0; !__all(kg == tag) && spins < SPIN_CAP; ++spins) {
      __builtin_amdgcn_s_sleep(8);
      kg = __hip_atomic_load(fk, RLX_AGENT);
    }
    asm volatile("" : "+v"(tx[3]), "+v"(st[3])::"memory");
    SST(5);
    const unsigned* kd = reinterpret_cast<const unsigned*>(Ta + 8 * CTN) + w * 392;
    glds16c(ws + OFF_KF + d * 2048, l * 16, lds_base((char*)kd));
    if (l < 34) glds16c(ws + OFF_KF + d * 2048 + 1024, l * 16, lds_base((char*)kd) + 1024);
    U_ROW(3);
    DRAIN();
    {
#define MIX(lo, hi) __builtin_amdgcn_perm(hi, lo, 0x07060100)
      const unsigned a0 = kd[384 - l], b0 = kd[383 - l], a1 = kd[320 - l], b1 = kd[319 - l];
      const unsigned a2 = kd[256 - 2 * l], m2 = kd[255 - 2 * l], b2 = kd[254 - 2 * l];
      const unsigned* k3 = kd + 124 - 4 * (l & 31);
      v4u* ko = reinterpret_cast<v4u*>(kr) + l;
      ko[4] = v4u{a0, a0, (l & 1) ? b0 : MIX(a0, b0), b0};
      ko[68] = v4u{a1, a1, (l & 1) ? b1 : MIX(a1, b1), b1};
      ko[132] = v4u{a2, MIX(a2, m2), m2, b2};
      if (l < 32) ko[196] = v4u{k3[4], MIX(k3[3], k3[2]), k3[2], MIX(k3[1], k3[0])};
#undef MIX
    }
    RAW_BAR();
    {
      const f16x8 bm = BFRAG(ub - 64);
      f32x4 a1 = MFMAH(bm, KFRAG(256 + 32), (f32x4{})), a2 = MFMAH(bm, KFRAG(512 + 32), (f32x4{}));
#pragma unroll 2
      for (int r = 0; r < 8; ++r) {
        f16x8 a[3], bf[3];
#pragma unroll
        for (int e = 0; e < 3; ++e) {
          a[e] = KFRAG(256 * e - 32 * r);
          bf[e] = BFRAG(ub + 512 * e + 64 * r);
        }
#pragma unroll
        for (int e = 0; e < 3; ++e) {
          a2 = MFMAH(bf[2 - e], a[e], a2);
          if (e < 2) a1 = MFMAH(bf[1 - e], a[e], a1);
        }
      }
      SST(8);
      const v2u k1 = SKIP(1), k2 = SKIP(2);
      TILE_OUT(Ta + w * CTN + 256, a1, k1);
      TILE_OUT(Ta + w * CTN, a2, k2);
    }
    RAW_BAR();
    SST(10);
    if (w < 4) {
      tx[0] = tx[1];
      tx[1] = tx[2];
      tx[2] = tx[3];
    }
    CHUNK_STX(w < 4 ? w + 8 : w, tx[0], Ta, CTN);
    {
      const f16x8 bm = BFRAG(ub - 64);
#pragma unroll
      for (int T = 3; T < 8; ++T) al[T - 3] = MFMAH(bm, KFRAG(256 * T + 32), (f32x4{}));
    }
    SST(7);
    if (RSK && w >= 4) __builtin_amdgcn_s_sleep(RSK);
#undef U_ROW
#undef U_VAL
#undef U_PUT
#undef ST_GET
  }
  const unsigned rb = lds_base(reinterpret_cast<char*>(((c & 1) ? Rs : Rc) + w * CRN + c + 8 * g));
#pragma unroll
  for (int j = 0; j < 8; ++j) acc[j] = f32x4{};
  const __amdgpu_buffer_rsrc_t rx = MKRS(ws + OFF_X2, SZ_X), rz = MKRS(ws + OFF_X3, SZ_X);
  const __amdgpu_buffer_rsrc_t rf = MKRS(FL + FL_FR, 64 * 16 * 128);
  v2u st = {0u, 0u}, xx = {0u, 0u};
#define X2_LOAD(ss, far)                                                                                                      \
  {                                                                                                                      \
    xx = LD64(rx, (l >> 1) * (DM * 2) + (l & 1) * 8, (far) ? (int)SZ_X : ((ss) * SEG * DM + 8 * cv) * 2);                 \
    st = LD64(rf, l * 8, ((ss) * 4 + (blockIdx.x & 3)) * 512);                                                           \
  }
  unsigned tl = now10();
  static_assert(R0 >= 2 && PP > 8 - R0, "rounds");
  if (CVP && w == 1) __builtin_amdgcn_s_setprio(CVP);
  for (int s = -R0; s < NSEG; ++s) {
    const int q = s & 3, q8 = s & 7;
    const u16* kq = ka + 32 * q8;
    if (w == 1 && s >= 0) {
      gate(tl);
      X2_LOAD(s, 0);
    }
    asm volatile("" ::: "memory");
    if (s < 8 - R0) {
      const int r = s + R0;
      const u16* kl = ka + 32 * r - 256 * 7;
      unsigned uq[4] = {ub + 64 * r, ub + 64 * r + 1024, ub + 64 * r + 2048, ub + 64 * r + 3072};
      asm volatile("" : "+v"(uq[0]), "+v"(uq[1]), "+v"(uq[2]), "+v"(uq[3]));
      f16x8 bf[8], a[8];
#pragma unroll
      for (int e = 7; e >= 0; --e) {
        a[e] = *reinterpret_cast<const f16x8*>(kl + 256 * (7 - e));
        bf[7 - e] = BFRAG(uq[(7 - e) >> 1] + 512 * ((7 - e) & 1));
      }
      __builtin_amdgcn_sched_barrier(0);
#pragma unroll
      for (int e = 7; e >= 0; --e)
#pragma unroll
        for (int T = (e > 3 ? e : 3); T < 8; ++T) al[T - 3] = MFMAH(bf[T - e], a[e], al[T - 3]);
      if (r == 1) {
        DRAIN();
        if (l < 8) CHUNK_FLAG(w < 4 ? w + 8 : w);
      }
      if (s == -1) SST(9);
      if (s == -1 && w == 1) {
        (void)wait_n<SLP_V, 32>(FL + FL_FZ + (blockIdx.x & 7) * 64, 64, TMO);
#pragma unroll 1
        for (unsigned spins = 0; spins < SPIN_CAP; ++spins) {
          mk = MARKS(0);
          if (__all(mk != 0u)) break;
          __builtin_amdgcn_s_sleep(16);
          asm volatile("" ::: "memory");
        }
        tl = latest(mk, tl) + CE + C0D;
      }
      if (s < 0) continue;
    }
    f16x8 a0 = *reinterpret_cast<const f16x8*>(kq - 32), a1 = *reinterpret_cast<const f16x8*>(kq);
    asm volatile("" : "+v"(a0), "+v"(a1));
    if (w == 1) {
#pragma unroll 1
      for (unsigned spins = 0; !__all(umin3(xx.x, xx.y, st.y) != 0u); ++spins) {
        if (spin_timeout(spins, TMO)) break;
        __builtin_amdgcn_s_sleep(SLP_V);
        X2_LOAD(s, __builtin_popcountll(__ballot(st.y != 0u)) < CVT2);
      }
      TS(0, s, 64);
      const float s0 = __uint_as_float(st.x), s1 = dppr<0xb1>(s0), m2 = __uint_as_float(st.y);
      const float var = (m2 + dppr<0xb1>(m2) + (s0 - s1) * (s0 - s1) * (1.0f / DM)) * (1.0f / DM);
      const float mb = bfr1((s0 + s1) * (1.0f / DM)), ri = 1024.0f * __builtin_amdgcn_rcpf(bfr1(__builtin_amdgcn_sqrtf(bfr1(bfr1(var) + 1e-5f))));
      const unsigned t0 = pk2(lo16(xx.x) - mb, hi16(xx.x) - mb), t1 = pk2(lo16(xx.y) - mb, hi16(xx.y) - mb);
      const unsigned b0 = pk2(lo16(t0) * ri, hi16(t0) * ri), b1 = pk2(lo16(t1) * ri, hi16(t1) * ri);
      const unsigned u0 = pkh(lo16(b0), hi16(b0)), u1 = pkh(lo16(b1), hi16(b1));
      u16* rp = Rc + (l & 1) * 4 * CRN + 32 * q + (l >> 1);
#define R_PUT(o) { rp[o] = (u16)u0; rp[(o) + CRN] = (u16)(u0 >> 16); rp[(o) + 2 * CRN] = (u16)u1; rp[(o) + 3 * CRN] = (u16)(u1 >> 16); }
      R_PUT(0) R_PUT(8 * CRN + 63)
      if (q == 0) { R_PUT(128) R_PUT(8 * CRN + 63 + 128) }
#undef R_PUT
      *reinterpret_cast<v2u*>(Sx + l * 4) = xx;
    }
    unsigned ap = rb + 64 * ((q + 3) & 3), ac = rb + 64 * q, au = lds_base((char*)(Rc + w * CRN + 32 * q + c));
    asm volatile("" : "+v"(ap), "+v"(ac), "+v"(au));
    RAW_BAR();
    TS(1, s, 0);
    const f16x8 bp = BFRAG(ap), bc = BFRAG(ac);
    const u16 h0 = ((lph)(unsigned long)au)[0], h1 = ((lph)(unsigned long)au)[16], y0 = Sx[c * 8 + w], y1 = Sx[(16 + c) * 8 + w];
    __builtin_amdgcn_sched_barrier(0);
    const float u0 = h2f(h0), u1 = h2f(h1), x0 = bf2f(y0), x1 = bf2f(y1);
    acc[0] = MFMAH(a0, bp, (q8 == 0 ? acc[1] : acc[0]));
    const f32x4 tmp = MFMAH(a1, bc, acc[0]);
    const unsigned uu = pk2(u0 * dsk, u1 * dsk);
    const unsigned yy = pk2(((q & 1) ? tmp[2] : tmp[0]) * ysc, ((q & 1) ? tmp[3] : tmp[1]) * ysc);
    const unsigned aa = pk2(lo16(yy) + lo16(uu), hi16(yy) + hi16(uu)), oo = pk2(x0 + lo16(aa), x1 + hi16(aa));
    if (g == (q8 >> 1)) {
      To[c * 8 + w] = (u16)oo;
      To[(16 + c) * 8 + w] = (u16)(oo >> 16);
    }
    RAW_BAR();
    if (w == 0 && l < 32) {
      v4u o = *reinterpret_cast<const v4u*>(To + l * 8);
      o.y = o.y ? o.y : 0x8000u;
      ST128A(o, rz, l * 16, (s * 64 + cv) * 512, XAUX);
      PS_ST(o, l * 8, ((64 + s) * 64 + cv) * 256)
    }
    if (w == 1) {
      tl = now10() + GATE_V - 40;
      mk = MARKS(s + 1);
    }
    const int left = 7 - (s >> 3);
#define PUSH(J0, SH)                                                                                              \
  if (left >= (J0)) {                                                                                             \
    _Pragma("unroll") for (int j = (J0); j < (J0) + 2 && j < 8; ++j)                                              \
      acc[j] = MFMAH(*reinterpret_cast<const f16x8*>(kq - 32 - 256 * j), bp, (j + (SH) < 8 ? acc[(j + (SH)) & 7] : f32x4{})); \
  }
    TS(2, s, 0);
    if (w == 0) {
      DRAIN();
      if (l < 4) set_flag(FL + FL_FC + (((64 + s) * 4 + l) * 8 + (cv >> 3)) * 32 + (cv & 7));
    }
    TS(3, s, 0);
    if (q8 == 0) { PUSH(1, 1) PUSH(3, 1) PUSH(5, 1) PUSH(7, 1) }
    else { PUSH(1, 0) PUSH(3, 0) PUSH(5, 0) PUSH(7, 0) }
#undef PUSH
    if (w == 1 && __all(mk != 0u)) tl = latest(mk, tl) + (s >= NSEG - 1 - TS1 ? CE - TL2 : CE);
    if (s == 8 - R0) {
      v2u k[5];
#pragma unroll
      for (int T = 3; T < 8; ++T) k[T - 3] = SKIP(T);
#pragma unroll
      for (int T = 3; T < 8; ++T) TILE_OUT(uc + 32 + 256 * T, al[T - 3], k[T - 3]);
    }
    {
      const int n = 12 + PW * (s - PP), wi = n + ((w - n) & 7), wf = wi - 8 * (PW < 8);
      if (s > PP && wf < 32 && wf >= n - PW && wf < n) {
        DRAIN();
        if (l < 8) CHUNK_FLAG(wf);
      }
      if (s >= PP && wi < 32 && wi < n + PW) {
        CHUNK_STX(wi, tx[1], Uc + 32 + 64 * (wi & 24), CUN);
        tx[1] = tx[2];
        tx[2] = tx[3];
      }
    }
    MSTAMP();
  }
#undef KFRAG
#undef BFRAG
#undef X2_LOAD
#undef TILE_OUT
#undef SKIP
#undef CHUNK_ST
#undef CHUNK_STX
#undef CHUNK_FLAG
}

__device__ __forceinline__ v4u ln_row_packed(const v4u xr) {
  float v[8];
#pragma unroll
  for (int e = 0; e < 4; ++e) { v[2 * e] = lo16(xr[e]); v[2 * e + 1] = hi16(xr[e]); }
  float s = ((v[0] + v[1]) + (v[2] + v[3])) + ((v[4] + v[5]) + (v[6] + v[7]));
  s = wave_sum(s);
  const float mean = s * (1.0f / DM);
  float q = 0.f;
#pragma unroll
  for (int j = 0; j < 8; ++j) { float t = v[j] - mean; q += t * t; }
  q = wave_sum(q);
  const float var = q * (1.0f / DM);
  const float mb = bfr(mean);
  const float sd = bfr(sqrtf(bfr(bfr(var) + 1e-5f)));
  const float ri = 1.0f / sd;
  v4u o;
#pragma unroll
  for (int e = 0; e < 4; ++e) {
    const unsigned t = pk2(v[2 * e] - mb, v[2 * e + 1] - mb);
    const float x0 = lo16(t), x1 = hi16(t);
    float q0 = x0 * ri, q1 = x1 * ri;
    q0 = __builtin_fmaf(__builtin_fmaf(-q0, sd, x0), ri, q0);
    q1 = __builtin_fmaf(__builtin_fmaf(-q1, sd, x1), ri, q1);
    o[e] = pk2(q0, q1);
  }
  return o;
}

__device__ __forceinline__ void ln4_packed(v4u (&x)[4]) {
  f32x2_t v[16];
#pragma unroll
  for (int e = 0; e < 16; ++e) v[e] = f32x2_t{lo16(x[e >> 2][e & 3]), hi16(x[e >> 2][e & 3])};
  const f32x2_t t = (((v[0] + v[1]) + (v[2] + v[3])) + ((v[4] + v[5]) + (v[6] + v[7]))) +
                    (((v[8] + v[9]) + (v[10] + v[11])) + ((v[12] + v[13]) + (v[14] + v[15])));
  const float mean = red_mod4(t.x + t.y) * (1.0f / DM);
  const float mb = bfr1(mean);
  f32x2_t q0 = {0.f, 0.f}, q1 = {0.f, 0.f};
#pragma unroll
  for (int e = 0; e < 16; e += 2) {
    v[e] -= mb;
    v[e + 1] -= mb;
    q0 += v[e] * v[e];
    q1 += v[e + 1] * v[e + 1];
  }
  q0 += q1;
  const float dm = mean - mb;
  const float var = fmaxf(red_mod4(q0.x + q0.y) * (1.0f / DM) - dm * dm, 0.f);
  const float sd = bfr1(sqrtf(bfr1(bfr1(var) + 1e-5f)));
  const float ri = 1.0f / sd;
#pragma unroll
  for (int e = 0; e < 16; ++e) {
    const unsigned t2 = pk2(v[e].x, v[e].y);
    const f32x2_t q = f32x2_t{lo16(t2), hi16(t2)} * ri;
    x[e >> 2][e & 3] = pk2(q.x, q.y);
  }
}

constexpr int MEGA_WGS = 256;
constexpr size_t MEGA_LDS = (size_t)(32 * 4 * 64 * 8 + 8 * SMS + 15 * QS + 128 + 16 * 64 * 8 + 8 * SOS + 512) * 2;
static_assert(MEGA_LDS <= 163840 && MEGA_LDS - 480 >= C_SQ + 4096, "lds");
static_assert(MEGA_LDS >= (size_t)AW_CNT + 128 && MEGA_LDS >= (size_t)P_CN + 512, "lds");
static_assert(MEGA_LDS >= (size_t)CV_LDS, "lds");
static_assert(MEGA_LDS >= (size_t)2 * 32 * SMS * 2 && MEGA_LDS >= 131072, "lds");
static_assert(MEGA_LDS >= (size_t)8 * (16640 + 2 * (704 + 144)), "lds");
__device__ __forceinline__ MegaArgs role_args(const int b, const int wv, const unsigned k0, const unsigned k1) {
  auto p = (const __attribute__((address_space(4))) u64*)__builtin_amdgcn_kernarg_segment_ptr();
  asm volatile("s_waitcnt lgkmcnt(0)" : "+s"(p) : "s"(k0), "s"(k1));
#define KA(i) ((u16*)(__attribute__((address_space(1))) u16*)p[i])
  return MegaArgs{KA(0), KA(1), (char*)KA(2), KA(3), KA(4), KA(5), KA(6), KA(7), KA(8), KA(9), KA(10), KA(11), KA(12), KA(13), b, wv};
#undef KA
}
__global__ __launch_bounds__(512, 2) void mega_kernel(const u16* mem0, const u16* Dskip, char* ws, u16* out, const u16* x0,
                                                      const u16* logAre, const u16* Aim, const u16* Cre, const u16* Cim,
                                                      const u16* logdt, const u16* Wq, const u16* Wk, const u16* Wv,
                                                      const u16* Wo) {
  extern __shared__ __attribute__((aligned(16))) char smem[];
  unsigned k0 = 0u, k1 = 0u;
  if (IPF == 3) {
    unsigned o;
    asm volatile(
        "s_load_dword %0, %3, 0\n\ts_load_dword %1, %3, 64\n\ts_getpc_b64 s[6:7]\n.LKG_PC:\n\ts_bfe_u32 s4, %4, 0x30003\n\t"
        "v_lshl_add_u32 %2, %5, 3, s4\n\tv_lshlrev_b32 %2, 7, %2\n\ts_mov_b32 m0, %6\n\ts_mov_b32 s4, .LKG_LAST-.LKG_PC\n\t"
        "s_max_i32 s4, s4, .LKG_LAS2-.LKG_PC\n\tv_cmp_gt_i32 vcc, s4, %2\n\t"
        "s_and_saveexec_b64 s[4:5], vcc\n\tglobal_load_lds_dword %2, s[6:7]\n\ts_mov_b64 exec, s[4:5]"
        : "=&s"(k0), "=&s"(k1), "=&v"(o)
        : "s"(__builtin_amdgcn_kernarg_segment_ptr()), "s"(blockIdx.x), "v"(threadIdx.x), "s"(lds_base(smem) + (unsigned)MEGA_LDS - 480)
        : "memory", "vcc", "scc", "s4", "s5", "s6", "s7");
  }
  const int b = vbid(), wv = __builtin_amdgcn_readfirstlane(threadIdx.x >> 6);
#define A role_args(b, wv, k0, k1)
  u64 tag = 0;
  if (__builtin_expect((0xde000ff0u >> (b >> 3)) & 1u, 0)) {
    const unsigned t0 = now10();
    if ((b >= 32 && b < 64) || (b >= 200 && b < 232)) start_kgen(A, smem, b < 64 ? b - 32 : b - 168);
    else if (b >= 64 && b < 96) tag = start_stats<4>(A, b - 64);
    else if (b >= 240) (void)start_stats<8>(A, b - 224);
    const unsigned dl = (b >= 32 && b < 64) ? DL_C : (b >= 64 && b < 80) ? DL_A : (b >= 80 && b < 96) ? DL_A2 : b >= 240 ? DL_R2 : ((b >= 96 && b < 104) || b >= 232) ? DL_R : (b >= 200 && b < 232) ? DL_Q : 0u;
    while (now10() - t0 < dl) __builtin_amdgcn_s_sleep(16);
    __builtin_amdgcn_s_waitcnt(0x0f70);
  }
  if ((EXP_OFF & 1) && b >= 64 && b < 80) return;
  if ((EXP_OFF & 8) && b >= 200 && b < 232) return;
  if ((EXP_OFF & 16) && b >= 32 && b < 64) return;
  if ((EXP_OFF & 32) && b >= 80 && b < 96) return;
  if (b < 64) role_chain(A, smem, b >> 5, b & 31, 0);
  else if (b < 96) role_aw(A, smem, (b >> 4) & 1, b & 15, tag);
  else if (b >= 104 && b < 168) role_conv(A, smem, b - 104);
  else {
    const bool kv = b >= 168 && b < 232;
    role_proj(A, smem, b >= (kv ? 200 : 240), kv ? (b - 168) & 31 : b & 15, kv);
  }
#undef A
}

extern "C" void kernel_launch(void* const* d_in, const int* in_sizes, int n_in, void* d_out, int out_size, void* d_ws,
                              size_t ws_size, hipStream_t stream) {
  MegaArgs A;
  A.x0 = (const u16*)d_in[0];
  A.logAre = (const u16*)d_in[1];
  A.Aim = (const u16*)d_in[2];
  A.Cre = (const u16*)d_in[3];
  A.Cim = (const u16*)d_in[4];
  A.logdt = (const u16*)d_in[5];
  A.Dskip = (const u16*)d_in[6];
  A.mem0 = (const u16*)d_in[8];
  A.Wq = (const u16*)d_in[9];
  A.Wk = (const u16*)d_in[10];
  A.Wv = (const u16*)d_in[11];
  A.Wo = (const u16*)d_in[12];
  A.ws = (char*)d_ws;
  A.out = (u16*)d_out;

  (void)hipFuncSetAttribute((const void*)mega_kernel, hipFuncAttributeMaxDynamicSharedMemorySize, (int)MEGA_LDS);
  mega_kernel<<<MEGA_WGS, 512, MEGA_LDS, stream>>>(A.mem0, A.Dskip, A.ws, A.out, A.x0, A.logAre, A.Aim, A.Cre, A.Cim, A.logdt,
                                                   A.Wq, A.Wk, A.Wv, A.Wo);
}
```

```cpp
#include <hip/hip_runtime.h>
#include <hip/hip_bf16.h>
#include <stdint.h>

constexpr int L_SEQ = 2048;
constexpr int DM    = 512;
constexpr int NST   = 128;
constexpr int NH    = 32;
constexpr int DH    = 128;
constexpr int HD    = NH * DH;
constexpr int MM    = 8;
constexpr int SEG   = 32;
constexpr int NSEG  = L_SEQ / SEG;
constexpr int LK    = MM + SEG;

typedef unsigned short u16;
using bf16x8 = __attribute__((ext_vector_type(8))) short;
using f32x4  = __attribute__((ext_vector_type(4))) float;
using f32x16 = __attribute__((ext_vector_type(16))) float;

static __device__ __forceinline__ u16 f2bf(float f) {
  unsigned u = __float_as_uint(f);
  u += 0x7fffu + ((u >> 16) & 1u);
  return (u16)(u >> 16);
}
static __device__ __forceinline__ float bf2f(u16 h) { return __uint_as_float(((unsigned)h) << 16); }
static __device__ __forceinline__ float bfr(float f) { return bf2f(f2bf(f)); }

constexpr size_t SZ_X      = (size_t)L_SEQ * DM * 2;
constexpr size_t OFF_KF    = 0;
constexpr size_t OFF_UT    = OFF_KF + (size_t)DM * L_SEQ * 4;
constexpr size_t OFF_YT    = OFF_UT + SZ_X;
constexpr size_t OFF_XA    = OFF_YT + SZ_X;
constexpr size_t OFF_H     = OFF_XA + SZ_X;
constexpr size_t OFF_X2    = OFF_H + SZ_X;
constexpr size_t OFF_U2    = OFF_X2 + SZ_X;
constexpr size_t OFF_X3    = OFF_U2 + SZ_X;
constexpr size_t SZ_QK     = (size_t)L_SEQ * 2 * HD * 2;
constexpr size_t OFF_QK    = OFF_X3 + SZ_X;
constexpr size_t SZ_O      = (size_t)L_SEQ * HD * 2;
constexpr size_t OFF_VF    = OFF_QK + 2 * SZ_QK;
constexpr size_t SZ_PW     = (size_t)2 * NSEG * 16 * SEG * DM * 2;
constexpr size_t OFF_PW    = OFF_VF + 2 * SZ_O;
constexpr size_t SZ_MEM    = (size_t)NSEG * MM * HD * 2;
constexpr size_t OFF_MEMK  = OFF_PW + SZ_PW;
constexpr size_t OFF_MEMV  = OFF_MEMK + 2 * SZ_MEM;
constexpr size_t OFF_GT    = OFF_MEMV + 2 * SZ_MEM;
constexpr size_t OFF_EP    = OFF_GT + 65536;
constexpr size_t OFF_RD    = OFF_EP + 4096;
constexpr size_t OFF_KG    = OFF_RD + 4096;
constexpr size_t OFF_G     = OFF_KG + 8192;
constexpr size_t SZ_ACC    = (size_t)2 * 65536;
constexpr size_t OFF_Z     = OFF_GT + (size_t)128 * DM * 4;
constexpr size_t OFF_ACC   = OFF_Z;
constexpr size_t OFF_FL    = OFF_ACC + 2 * SZ_ACC;
constexpr int FL_FK  = 0;
constexpr int FL_FW  = FL_FK + 64 * 8 * 32;
constexpr int FL_FR  = FL_FW + 2 * 64 * 16 * 32;
constexpr int FL_FC  = FL_FR + 64 * 16 * 32;
constexpr int FL_FQ  = FL_FC + 2 * 64 * 32 * 32;
constexpr int FL_FP  = FL_FQ + 2 * 64 * 32 * 32;
constexpr int FL_FD  = FL_FP + 64 * 32 * 32;
constexpr int FL_FZ  = FL_FD + 64 * 2 * 32;
constexpr int FL_TMO = FL_FZ + 16 * 32;
constexpr int FL_END = FL_TMO + 64;
constexpr size_t Z_BYTES   = OFF_FL + (size_t)FL_END * 4 - OFF_Z;
constexpr size_t WS_TOTAL  = OFF_Z + Z_BYTES;
static_assert(Z_BYTES % 16 == 0 && OFF_Z % 256 == 0 && Z_BYTES <= 4 * 64 * 8192 && OFF_G + 2048 <= OFF_Z, "zero region");
static_assert(WS_TOTAL + (4 << 20) <= (size_t)256 * 1024 * 1024, "workspace too large");

static_assert(WS_TOTAL - OFF_XA <= (size_t)384 * 8 * 65536, "translation warm-up");
constexpr int CKN = 2336;
static_assert((size_t)DM * 2 * CKN * 2 <= (size_t)DM * L_SEQ * 4 + SZ_X, "K buffer");
__device__ __forceinline__ float pos_of_index(int p) {
  if (p < 256) return (float)p;
  if (p < 384) return (float)(256 + 2 * (p - 256));
  if (p < 512) return (float)(512 + 4 * (p - 384));
  if (p < 640) return (float)(1024 + 8 * (p - 512));
  return 2048.0f;
}
__device__ __forceinline__ int index_of_l(int l) {
  float pf = bfr((float)l);
  int p = (int)pf;
  if (p < 256) return p;
  if (p < 512) return 256 + ((p - 256) >> 1);
  if (p < 1024) return 384 + ((p - 512) >> 2);
  if (p < 2048) return 512 + ((p - 1024) >> 3);
  return 640;
}

template <int CTRL> __device__ __forceinline__ float dppr(float x) {
  return __builtin_bit_cast(float, __builtin_amdgcn_update_dpp(0, __builtin_bit_cast(int, x), CTRL, 0xf, 0xf, false));
}
__device__ __forceinline__ float wave_sum(float v) {
  v += dppr<0x121>(v);
  v += dppr<0x122>(v);
  v += dppr<0x124>(v);
  v += dppr<0x128>(v);
  {
    auto r = __builtin_amdgcn_permlane16_swap(__float_as_uint(v), __float_as_uint(v), false, false);
    v = __uint_as_float(r[0]) + __uint_as_float(r[1]);
  }
  {
    auto r = __builtin_amdgcn_permlane32_swap(__float_as_uint(v), __float_as_uint(v), false, false);
    v = __uint_as_float(r[0]) + __uint_as_float(r[1]);
  }
  return v;
}
__device__ __forceinline__ void ln_row8(float (&v)[8]) {
  float s = 0.f;
#pragma unroll
  for (int j = 0; j < 8; ++j) s += v[j];
  s = wave_sum(s);
  const float mean = s * (1.0f / DM);
  float q = 0.f;
#pragma unroll
  for (int j = 0; j < 8; ++j) { float t = v[j] - mean; q += t * t; }
  q = wave_sum(q);
  const float var = q * (1.0f / DM);
  const float mb = bfr(mean);
  const float sd = bfr(sqrtf(bfr(bfr(var) + 1e-5f)));
#pragma unroll
  for (int j = 0; j < 8; ++j) v[j] = bfr(bfr(v[j] - mb) / sd);
}

#define RLX_AGENT __ATOMIC_RELAXED, __HIP_MEMORY_SCOPE_AGENT
typedef __attribute__((address_space(1))) unsigned gu32;
typedef unsigned v4u __attribute__((ext_vector_type(4)));
constexpr int AUX_SC1 = 16;
constexpr unsigned SPIN_CAP = 1u << 18;

__device__ __forceinline__ bool wait_flags(const unsigned* flags, int lane, int nfl, unsigned want, unsigned* tmo) {
  for (unsigned spins = 0;; ++spins) {
    unsigned v = want;
    if (lane < nfl) v = __hip_atomic_load((gu32*)flags + lane * 32, RLX_AGENT);
    if (__all(v >= want)) break;
    if (spins > SPIN_CAP) {
      if (lane == 0) __hip_atomic_store((gu32*)tmo, 1u, RLX_AGENT);
      return false;
    }
  }
  __builtin_amdgcn_fence(__ATOMIC_ACQUIRE, "wavefront");
  return true;
}

#define STAMP(i)
#define STAMP_DUMP()
#define WSTAMP(i)
template <int CTRL> __device__ __forceinline__ float dppf(float old, float x) {
  return __builtin_bit_cast(float, __builtin_amdgcn_update_dpp(__builtin_bit_cast(int, old), __builtin_bit_cast(int, x), CTRL, 0xf, 0xf, false));
}
__device__ __forceinline__ float wave_max_dpp(float x) {
  const float ninf = -3e38f;
  x = fmaxf(x, dppf<0x111>(ninf, x));
  x = fmaxf(x, dppf<0x112>(ninf, x));
  x = fmaxf(x, dppf<0x114>(ninf, x));
  x = fmaxf(x, dppf<0x118>(ninf, x));
  x = fmaxf(x, dppf<0x142>(ninf, x));
  x = fmaxf(x, dppf<0x143>(ninf, x));
  return __builtin_bit_cast(float, __builtin_amdgcn_readlane(__builtin_bit_cast(int, x), 63));
}
__device__ __forceinline__ float wave_sum_dpp(float x) {
  x += dppf<0x111>(0.f, x);
  x += dppf<0x112>(0.f, x);
  x += dppf<0x114>(0.f, x);
  x += dppf<0x118>(0.f, x);
  x += dppf<0x142>(0.f, x);
  x += dppf<0x143>(0.f, x);
  return __builtin_bit_cast(float, __builtin_amdgcn_readlane(__builtin_bit_cast(int, x), 63));
}
constexpr int SMS = 528;
constexpr int QS = 144;
constexpr int VS = 80;
constexpr int SOS = 144;
#define VAR_NREP 4
constexpr int NREP = VAR_NREP;
__device__ __forceinline__ float xsum16(float x) {
  auto r = __builtin_amdgcn_permlane16_swap(__float_as_uint(x), __float_as_uint(x), false, false);
  return __uint_as_float(r[0]) + __uint_as_float(r[1]);
}
__device__ __forceinline__ float xsum32(float x) {
  auto r = __builtin_amdgcn_permlane32_swap(__float_as_uint(x), __float_as_uint(x), false, false);
  return __uint_as_float(r[0]) + __uint_as_float(r[1]);
}
__device__ __forceinline__ float xmax16(float x) {
  auto r = __builtin_amdgcn_permlane16_swap(__float_as_uint(x), __float_as_uint(x), false, false);
  return fmaxf(__uint_as_float(r[0]), __uint_as_float(r[1]));
}
__device__ __forceinline__ float xmax32(float x) {
  auto r = __builtin_amdgcn_permlane32_swap(__float_as_uint(x), __float_as_uint(x), false, false);
  return fmaxf(__uint_as_float(r[0]), __uint_as_float(r[1]));
}
__device__ __forceinline__ float red_mod4(float x) {
  x += dppf<0x124>(0.f, x);
  x += dppf<0x128>(0.f, x);
  return xsum32(xsum16(x));
}
typedef __bf16 bf16x2_t __attribute__((ext_vector_type(2)));
typedef float f32x2_t __attribute__((ext_vector_type(2)));
__device__ __forceinline__ unsigned pk2(float a, float b) {
  f32x2_t v = {a, b};
  bf16x2_t r = __builtin_convertvector(v, bf16x2_t);
  return __builtin_bit_cast(unsigned, r);
}
typedef _Float16 f16x2_t __attribute__((ext_vector_type(2)));
typedef _Float16 f16x8 __attribute__((ext_vector_type(8)));
__device__ __forceinline__ unsigned pkh(float a, float b) {
  f32x2_t v = {a, b};
  return __builtin_bit_cast(unsigned, __builtin_convertvector(v, f16x2_t));
}
__device__ __forceinline__ float h2f(u16 h) { return (float)__builtin_bit_cast(_Float16, h); }
__device__ __forceinline__ float one(float x) {
  asm("" : "+v"(x));
  return x;
}
__device__ __forceinline__ unsigned pk2s(float a, float b) { return pk2(one(a), one(b)); }
__device__ __forceinline__ float dot2sq(const unsigned x, const float acc) {
  return __builtin_amdgcn_fdot2_f32_bf16(__builtin_bit_cast(bf16x2_t, x), __builtin_bit_cast(bf16x2_t, x), acc, false);
}
__device__ __forceinline__ float lo16(unsigned u) { return __uint_as_float(u << 16); }
__device__ __forceinline__ float hi16(unsigned u) { return __uint_as_float(u & 0xffff0000u); }
__device__ __forceinline__ float bfr1(float x) { return lo16(pk2(x, x)); }
__device__ __forceinline__ float half_sum(float v) {
  v += dppr<0x121>(v);
  v += dppr<0x122>(v);
  v += dppr<0x124>(v);
  v += dppr<0x128>(v);
  return xsum16(v);
}
__device__ __forceinline__ void scale_pair(float a, float b, float scale, float& oa, float& ob) {
  unsigned u = pk2(a, b);
  unsigned v = pk2s(lo16(u) * scale, hi16(u) * scale);
  oa = lo16(v);
  ob = hi16(v);
}

#define XMAP 2
__device__ __forceinline__ int vbid() {
  const int b = blockIdx.x, x = b & 7, t = b >> 3;
  if (XMAP == 2) {
    const int q = (t >> 2) + 2 * (x & 4);
    const unsigned long long m = (q & 8) ? 0xf0d85040cdc9ada9ull : 0x76726e6a25210501ull;
    const int e = (int)(m >> (8 * (q & 7))) & 255;
    return (e & 252) + ((x & 3) << (2 + (e & 3))) + (t & 3) + (q == 14 ? ((x & 2) ? 8 : -120) : 0);
  }
  if (XMAP == 0) {
    if (b < 64) return (b & 32) + 4 * x + (t & 3);
    if (b < 96) return (b & ~15) + 2 * x + (t & 1);
    if (b >= 104 && b < 168) return 104 + 8 * x + t - 13;
    if (b >= 168 && b < 232) return 168 + ((b - 168) & 32) + 4 * x + ((t - 21) & 3);
    return b;
  }
  if (XMAP == 3) {
    const int y = x & 3;
    if (t >= 16 && t < 24) return 104 + 16 * y + 2 * (x & 4) + t - 16;
    if (x < 4) return t < 16 ? 4 * (t & 8) + 8 * x + (t & 7) : 64 + 4 * (t & 4) + 4 * y + (t & 3);
    if (t >= 24) return (t >= 28 ? 240 : y < 2 ? 96 : 224) + 4 * y + (t & 3);
    return 168 + 4 * (t & 8) + 8 * y + (t & 7);
  }
  if (XMAP == 4) {
    const int y = x & 3;
    if (x < 4) return t < 16 ? 4 * (t & 8) + 8 * x + (t & 7) : t < 28 ? 104 + 16 * y + t - 16 : 64 + 4 * y + (t & 3);
    if (t >= 24) return (t >= 28 ? 240 : y < 2 ? 96 : 224) + 4 * y + (t & 3);
    if (t < 16) return 168 + 4 * (t & 8) + 8 * y + (t & 7);
    return t < 20 ? 104 + 16 * y + t - 4 : 80 + 4 * y + (t & 3);
  }
  if (XMAP == 2 || XMAP >= 5) {
    if (XMAP == 5 && x < 4 && t < 16) return 16 * x + t;
    if (XMAP == 6 && x < 4 && t < 16) return 32 * (x & 1) + 8 * (x & 2) + t;
    if (XMAP == 7 && x < 4) return (x & 1) ? 104 + 16 * (x & 2) + t : 16 * x + t;
    if (x < 4) return t < 16 ? 4 * (t & 8) + 8 * x + (t & 7) : 104 + 16 * x + t - 16;
    const int y = x - 4;
    if (t >= 24) return (t >= 28 ? 240 : y < 2 ? 96 : 224) + 4 * y + (t & 3);
    if (t < 16) return 168 + 4 * (t & 8) + 8 * y + (t & 7);
    return 64 + 4 * (t & 4) + 4 * y + (t & 3);
  }
  if (x < 2) return 2 * (t & 16) + 16 * x + (t & 15);
  const int i = 16 * (x - 2) + (t & 15);
  if (t < 16) {
    const int k = (i * 171) >> 9, r = i - 3 * k;
    return r == 0 ? 168 + k : r == 1 ? 200 + k : 64 + 16 * (k & 1) + (k >> 1);
  }
  const int g = i >> 3, e = i & 7;
  if (g == 3) return 96 + e;
  if (g == 7) return 232 + e;
  if (g == 9) return 240 + e;
  if (g == 11) return 248 + e;
  return 104 + 8 * (g - (g > 3) - (g > 7) - (g > 9)) + e;
}
#define MSTAMP()
#define WSTAMP2()
#define SST(k)
#define SSX(k, c)
#define SSW(k, v)
#define TS(k, ss, t)
#define LS(k, ss)
#define LS_DUMP()
struct MegaArgs {
  const u16* mem0;
  const u16* Dskip;
  char* ws;
  u16* out;
  const u16* x0;
  const u16 *logAre, *Aim, *Cre, *Cim, *logdt;
  const u16 *Wq, *Wk, *Wv, *Wo;
  int vb, wv;
};

__device__ __forceinline__ int lane_id() {
  int l;
  asm volatile("v_mbcnt_lo_u32_b32 %0, -1, 0\n\tv_mbcnt_hi_u32_b32 %0, -1, %0" : "=v"(l));
  __builtin_assume(l >= 0 && l < 64);
  return l;
}
__device__ __forceinline__ bool spin_timeout(unsigned spins, unsigned* tmo) {
  if (spins > SPIN_CAP) {
    if (lane_id() == 0) __hip_atomic_store((gu32*)tmo, 1u, RLX_AGENT);
    return true;
  }
  return false;
}
#define ROT_H (4 * (h & 1))
#define ROT_T (h & 2)
#define C2G 420
#define CVSP 40
#define PFA 0
#define PFB 0
#define WO1 1
#define AWH 0
#define PEEK0 1
#define SLP0 2
#define GF_R 400
#define GF_V 0
#define PP (9 - R0)
#define EL 1
#define IPF 3
#define XL 0
#define MC3 2
#define XZD 300
#define XZP 4
#define NFW 1
#define IPS 128u
#define WNT 17
#define OAUX 16
#define XAUX 16
#define GF_RT 0
#define GF_RU 0
#define EF 1
#define RGB 3
#define GR2 345
#define GR3 280
#define RLS 1
#define GT1 120
#define GT2 32
#define GT3 1
#define CE 190
#define KE 0
#define PE KE
#define KD 360
#define PD (KD + 20)
#define PC0 2
#define GR0 6
#define DL_A2 DL_A
#define PCT 0
#define EP 1
#define CQ 1
#define K0 40
#define PW 1
#define R0 3
#define PSW 1
#define RSK 0
#define KC 1
#define AWD 2200
#define P2D 0
#define TS1 3
#define TL1 20
#define TL2 30
#define TL3 50
#define KF 260
#define R0D 0
#define C0D 0
#define YQ 1
#define KF1 200
#define KW1 0
#define KCT 20
#define KR 1
#define K1 50
#define CVP 2
#define PCD 100
#define RAUX AUX_SC1
#define RVAL 1
#define RLEAD 60
#define SLP_D 0
#define RFA 1
#define TP 1
#define TPS (4 * TP + 4)
#define MCE 1
#define H4S 0
#define GF_H 150
#define CVT2 48
#define GF_P 0
#define GF0 8
#define SLP_H 12
#define HX0 6
#define CVSTEP 2
#define CVLATE 64
#define PACE 204
#define PACE0 12
#define RPRIO 3
#define DL_R2 1400
#define PNL 8
#define GSL 12
#define AWDIR 1
#define AWAT 0
#define AWRP 0
#define PEPS 4
#define FGATE 0
#define DL_C 0
#define DL_A 250
#define DL_R 0
#define DL_Q 0
#define KC0 0
#define CPRIO 1
#define P1PRIO 3
#define QLEAD 2
#define POLL_SLEEP 12
#define SLP_C POLL_SLEEP
#define SLP_A POLL_SLEEP
#define SLP_W POLL_SLEEP
#define SLP_R POLL_SLEEP
#define SLP_V POLL_SLEEP
#define SLP_Q POLL_SLEEP
#define SLP_M 4
#define AWPRIO 2
#define SLP_S 8
#define SPOS 4
#define CVPRE 12
#define QPD 3
#define QPRI 2
#define TLBW 3
#define QHV 0
#define QCHK 0
#define LEADN 99
#define GATE_V 170
#define GATE_R 380
#define GATE_N 380
#define AWAHEAD 1
#define GATE_M 150
#define PRI_A 3
#define PRI_WA 2
#define PRI_WB 1
#define AWSKIP 3
#define PRI_0 3
#define AWDEF 0
#define SLP_F 2
#define EXP_OFF 0
#define AWSPEC 1
#define CVTHR 48
#define NR_C 8
#define NR_R 8
#define NR_W 8
#define NR_N 2
#define QAHEAD 0
#define NS0 4
#define SLP_N POLL_SLEEP
template <int SLP, int WPL>
__device__ __forceinline__ bool wait_n(const unsigned* f, int n, unsigned* tmo) {
  const int l = lane_id();
  for (unsigned spins = 0;; ++spins) {
    unsigned v = 1u;
    if (l < n) v = __hip_atomic_load((gu32*)f + (l / WPL) * 32 + l % WPL, RLX_AGENT);
    if (__all(v != 0u)) break;
    if (spin_timeout(spins, tmo)) return false;
    __builtin_amdgcn_s_sleep(SLP);
  }
  asm volatile("" ::: "memory");
  return true;
}
__device__ __forceinline__ unsigned peek(const unsigned* base, const unsigned off) {
  unsigned r;
  asm volatile("global_atomic_or %0, %1, %2, %3 sc0\n\ts_waitcnt vmcnt(0)" : "=&v"(r) : "v"(off), "v"(0u), "s"(base) : "memory");
  return r;
}
__device__ __forceinline__ unsigned now10() { return (unsigned)__builtin_amdgcn_s_memrealtime(); }
__device__ __forceinline__ void gate(const unsigned t) {
#pragma unroll 1
  for (int i = 0; i < 64; ++i) {
    const int d = (int)(t - now10());
    if (d <= 0) break;
    if (d > GT1) __builtin_amdgcn_s_sleep(32);
    else if (d > GT2) __builtin_amdgcn_s_sleep(8);
    else __builtin_amdgcn_s_sleep(GT3);
  }
}
__device__ __forceinline__ unsigned latest(const unsigned mk, const unsigned t) {
  int d = (int)(mk - t);
  d = max(d, __builtin_amdgcn_update_dpp(0, d, 0x121, 0xf, 0xf, false));
  d = max(d, __builtin_amdgcn_update_dpp(0, d, 0x122, 0xf, 0xf, false));
  d = max(d, __builtin_amdgcn_update_dpp(0, d, 0x124, 0xf, 0xf, false));
  d = max(d, __builtin_amdgcn_update_dpp(0, d, 0x128, 0xf, 0xf, false));
  return t + __builtin_amdgcn_readfirstlane(d);
}
template <int SLP, int WPL, int GT>
__device__ __forceinline__ void grp_wait(const bool leader, volatile unsigned* sw, const unsigned val, const unsigned* f, int n,
                                         unsigned* tmo, unsigned& tl) {
  if (leader) {
    const int l = lane_id();
    for (unsigned spins = 0;; ++spins) {
      unsigned v = 1u;
      if (l < n) v = __hip_atomic_load((gu32*)f + (l / WPL) * 32 + l % WPL, RLX_AGENT);
      if (__all(v != 0u) || spin_timeout(spins, tmo)) break;
      if (GT && spins == 0) gate(tl);
      else __builtin_amdgcn_s_sleep(SLP);
    }
    if (GT) tl = now10() + GT;
    if (sw && l == 0) *sw = val;
  } else {
    for (unsigned spins = 0; *sw < val; ++spins) {
      if (spins > 64u * SPIN_CAP) break;
      if (FGATE && GT && spins == 0) gate(tl);
      __builtin_amdgcn_s_sleep(SLP_F);
    }
    if (FGATE && GT) tl = now10() + GT;
  }
  asm volatile("" ::: "memory");
}
template <int SLP, int WPL>
__device__ __forceinline__ bool wg_wait(const unsigned* f, int n, unsigned* tmo) {
  if (threadIdx.x < 64) (void)wait_n<SLP, WPL>(f, n, tmo);
  __syncthreads();
  asm volatile("" ::: "memory");
  return true;
}
__device__ __forceinline__ void set_flag(unsigned* f) { __hip_atomic_store((gu32*)f, 1u, RLX_AGENT); }
#define DRAIN() asm volatile("s_waitcnt vmcnt(0)" ::: "memory")
#define MKRS(ptr, bytes) __builtin_amdgcn_make_buffer_rsrc((void*)(ptr), 0, (int)(bytes), 0x00020000)
typedef unsigned v2u __attribute__((ext_vector_type(2)));
#define LD128(rs, voff, soff) __builtin_amdgcn_raw_buffer_load_b128(rs, voff, soff, AUX_SC1)
#define LD64(rs, voff, soff) __builtin_amdgcn_raw_buffer_load_b64(rs, voff, soff, AUX_SC1)
#define ST128(v, rs, voff, soff) ST128A(v, rs, voff, soff, AUX_SC1)
#define ST128A(v, rs, voff, soff, aux)                                        \
  do {                                                                        \
    const v4u st_d_ = (v);                                                    \
    __builtin_amdgcn_raw_buffer_store_b128(st_d_, rs, voff, soff, aux);       \
    asm volatile("s_nop 1" ::"v"(st_d_) : "memory");                          \
  } while (0)
#define ST64(v, rs, voff, soff) __builtin_amdgcn_raw_buffer_store_b64(v, rs, voff, soff, AUX_SC1)
#define BF8(x) __builtin_bit_cast(bf16x8, x)
#define MFMA16(a, b, c) __builtin_amdgcn_mfma_f32_16x16x32_bf16(a, b, c, 0, 0, 0)
typedef short bf16x4 __attribute__((ext_vector_type(4)));
#define MFMA16K(a, b, c) \
  __builtin_amdgcn_mfma_f32_16x16x16bf16_1k(__builtin_bit_cast(bf16x4, a), __builtin_bit_cast(bf16x4, b), c, 0, 0, 0)
__device__ __forceinline__ float fmax_nn(float a, float b) { return __builtin_elementwise_maximum(a, b); }
typedef __bf16 bfv8 __attribute__((ext_vector_type(8)));
typedef __bf16 bfv16 __attribute__((ext_vector_type(16)));
typedef unsigned v8u __attribute__((ext_vector_type(8)));
#define SMF(a, b, c, idx) \
  __builtin_amdgcn_smfmac_f32_16x16x64_bf16(__builtin_bit_cast(bfv8, a), __builtin_bit_cast(bfv16, b), c, idx, 0, 0)
__device__ __forceinline__ float swap_add32(float a, float b) {
  auto r = __builtin_amdgcn_permlane32_swap(__float_as_uint(a), __float_as_uint(b), false, false);
  return __uint_as_float(r[0]) + __uint_as_float(r[1]);
}
__device__ __forceinline__ float swap_add16(float a, float b) {
  auto r = __builtin_amdgcn_permlane16_swap(__float_as_uint(a), __float_as_uint(b), false, false);
  return __uint_as_float(r[0]) + __uint_as_float(r[1]);
}
__device__ __forceinline__ v4u smf_fix(const v4u d) {
  auto x = __builtin_amdgcn_permlane16_swap(d.x, d.z, false, false);
  auto y = __builtin_amdgcn_permlane16_swap(d.y, d.w, false, false);
  return v4u{x[0], x[1], y[0], y[1]};
}
__device__ __forceinline__ v8u smf_bfrag(const u16* p) {
  const v2u a = *reinterpret_cast<const v2u*>(p), b = *reinterpret_cast<const v2u*>(p + 8);
  const v2u c = *reinterpret_cast<const v2u*>(p + 32), d = *reinterpret_cast<const v2u*>(p + 40);
  return v8u{a.x, b.x, a.y, b.y, c.x, d.x, c.y, d.y};
}

#define VAR_PPA 0
#define VAR_PPB 0
#define VAR_THR 0
#define VAR_THR2 0
#define CV_LEAD 100
#define VAR_EARLY 1
#define VAR_DRAIN 1
#define VAR_D1 0
#define VAR_D2 0
#define VAR_DR2 1
#define VAR_S1 0
#define VAR_S2 0
typedef __attribute__((address_space(1))) unsigned long long gu64;
typedef __attribute__((address_space(1))) char gch;
__device__ __forceinline__ int cvt_rpi(float x) {
  int r;
  asm("v_cvt_rpi_i32_f32 %0, %1" : "=v"(r) : "v"(x));
  return r;
}
typedef unsigned long long u64;
__device__ __forceinline__ u64 call_tag(const MegaArgs& A) {
  u64 t = *(const __attribute__((address_space(4))) u64*)(A.ws + OFF_EP) * 6364136223846793005ull + 1442695040888963407ull;
  asm volatile("" : "+s"(t)::"memory");
  return t;
}
__device__ __forceinline__ u64 tagged(const u64 tag, const unsigned v) { return (tag & 0xffffffffffffull) | (u64)(v >> 16) << 48; }
__device__ __forceinline__ bool has_tag(const u64 tag, const unsigned lo, const unsigned hi) {
  return lo == (unsigned)tag && (hi & 0xffffu) == ((unsigned)(tag >> 32) & 0xffffu);
}
__device__ __forceinline__ u64 ready_look(const MegaArgs& A) {
  return __hip_atomic_load((gu64*)(A.ws + OFF_RD) + (blockIdx.x & 7) * 64 + lane_id(), RLX_AGENT);
}
#define RD_ASK(v) \
  asm volatile("global_load_dwordx2 %0, %1, %2 sc1" : "=v"(v) : "v"(lane_id() * 8), "s"(A.ws + OFF_RD + (blockIdx.x & 7) * 512) : "memory")
__device__ __forceinline__ void ready_wait(const MegaArgs& A, const u64 tag, u64 v) {
  for (unsigned spins = 0; !__all(v == tag) && spins < SPIN_CAP; ++spins) {
    __builtin_amdgcn_s_sleep(8);
    v = ready_look(A);
  }
  asm volatile("" ::: "memory");
}
typedef short v4s __attribute__((ext_vector_type(4)));
typedef __attribute__((address_space(3))) v4s* lp4s;
__device__ __forceinline__ void glds16(const void* sbase, const unsigned voff, const unsigned lds, const bool nt = false) {
  unsigned keep;
#define GLDS16(M)                                                                                                                     \
  asm volatile("s_nop 4\n\ts_mov_b32 %0, m0\n\ts_mov_b32 m0, %3\n\ts_nop 0\n\tglobal_load_lds_dwordx4 %1, %2" M "\n\ts_mov_b32 m0, %0" \
               : "=&s"(keep)                                                                                                          \
               : "v"(voff), "s"(sbase), "s"(lds)                                                                                      \
               : "memory")
  if (nt) GLDS16(" nt");
  else GLDS16("");
#undef GLDS16
}
__device__ __forceinline__ void glds16c(const void* sbase, const unsigned voff, const unsigned lds) {
  unsigned keep;
  asm volatile("s_nop 4\n\ts_mov_b32 %0, m0\n\ts_mov_b32 m0, %3\n\ts_nop 0\n\tglobal_load_lds_dwordx4 %1, %2 sc1\n\ts_mov_b32 m0, %0"
               : "=&s"(keep)
               : "v"(voff), "s"(sbase), "s"(lds)
               : "memory");
}
__device__ __forceinline__ void glds64c(const void* sbase, const unsigned voff, const unsigned lds) {
  unsigned keep;
  asm volatile("s_nop 4\n\ts_mov_b32 %0, m0\n\ts_mov_b32 m0, %3\n\ts_nop 0\n\tglobal_load_lds_dwordx4 %1, %2 sc1\n\t"
               "global_load_lds_dwordx4 %1, %2 offset:1024 sc1\n\tglobal_load_lds_dwordx4 %1, %2 offset:2048 sc1\n\t"
               "global_load_lds_dwordx4 %1, %2 offset:3072 sc1\n\ts_mov_b32 m0, %0"
               : "=&s"(keep)
               : "v"(voff), "s"(sbase), "s"(lds)
               : "memory");
}
__device__ __forceinline__ v2u trd(const unsigned a) {
  return __builtin_bit_cast(v2u, __builtin_amdgcn_ds_read_tr16_b64_v4i16((lp4s)(unsigned long)a));
}
__device__ __forceinline__ unsigned lds_base(char* smem) { return (unsigned)(unsigned long)(__attribute__((address_space(3))) char*)smem; }
#define RAW_BAR()                                           \
  do {                                                      \
    asm volatile("s_waitcnt lgkmcnt(0)" ::: "memory");      \
    __builtin_amdgcn_s_barrier();                           \
    asm volatile("" ::: "memory");                          \
  } while (0)
#define WAIT_VM(n) asm volatile("s_waitcnt vmcnt(" #n ")" ::: "memory")
template <bool SP>
__device__ __forceinline__ void wqkv_issue(const u16* Wm, const int hf, const int h, const int w, const int l, const unsigned sb, const bool nt) {
  const char* base = (const char*)Wm + (size_t)(256 * hf + 32 * w) * (HD * 2) + h * (DH * 2);
#define WQKV(NT_)                                                                                                       \
  _Pragma("unroll") for (int i = 0; i < 8; ++i) {                                                                      \
    const int r = l >> 4;                                                                                              \
    const int f = SP ? ((r & 1) | ((i & 1) << 1) | (((i >> 1) & 1) << 2)) : (r | (((i >> 1) & 1) << 2));               \
    glds16(base, (unsigned)((4 * i + r) * (HD * 2) + (((l & 15) ^ (f << 1)) << 4)),                                    \
           sb + (w >> 2) * 65536 + hf * 32768 + (w & 3) * 8192 + i * 1024, NT_);                                       \
  }
  if (nt) { WQKV(true) } else { WQKV(false) }
#undef WQKV
}

__device__ __forceinline__ unsigned row_stats(const v4u raw) {
  float v[8];
#pragma unroll
  for (int e = 0; e < 4; ++e) { v[2 * e] = lo16(raw[e]); v[2 * e + 1] = hi16(raw[e]); }
  float s = 0.f;
#pragma unroll
  for (int e = 0; e < 8; ++e) s += v[e];
  const float mean = wave_sum(s) * (1.0f / DM);
  float q = 0.f;
#pragma unroll
  for (int e = 0; e < 8; ++e) { const float t = v[e] - mean; q += t * t; }
  const float var = wave_sum(q) * (1.0f / DM);
  const float sd = bfr(sqrtf(bfr(bfr(var) + 1e-5f)));
  return (unsigned)f2bf(mean) | ((unsigned)f2bf(sd) << 16);
}
__device__ __forceinline__ void ask_slow(const MegaArgs& A, const int k, unsigned (&t)[3]) {
  const int l = lane_id(), x = blockIdx.x & 7;
  const char *wq = (const char*)A.Wq, *wv = (const char*)A.Wv;
  asm("" : "+s"(wq), "+s"(wv));
#pragma unroll
  for (int m = 0; m < 3; ++m) {
    const char* W = m == 1 ? (const char*)A.Wk : ((m == 0) == (x < 4)) ? wv : wq;
    const int a = (int)((unsigned long)W >> 8) + 8 * (x & 3), h = k < 32 ? ((1 - a) & 3) + 4 * (k & 1) : (31 - a) & 31;
    const int r = k < 32 ? k >> 1 : k - 32;
    t[m] = __builtin_amdgcn_raw_buffer_load_b32(MKRS(W, (unsigned)k < 48u && h < 8 ? DM * HD * 2 : 0), (l >> 1) * (HD * 2) + (l & 1) * 128,
                                                (32 * (r & 15)) * (HD * 2) + (8 * (x & 3) + (h & 7)) * 256, 0);
  }
}
#define ASKED(t) asm volatile("" ::"v"(t[0]), "v"(t[1]), "v"(t[2]))
template <int N>
__device__ __forceinline__ u64 start_stats(const MegaArgs& A, const int idx) {
  const int w = A.wv, l = lane_id();
  const int r0 = N * (8 * idx + w);
  unsigned ta[3], tb[3];
  v4u raw[N];
#pragma unroll
  for (int i = 0; i < N; ++i) raw[i] = *reinterpret_cast<const v4u*>(A.x0 + (size_t)(r0 + i) * DM + l * 8);
  asm volatile("" ::: "memory");
  ask_slow(A, 16 * w + (idx & 3) + (idx >> 4) * N, ta);
  if (N > 4) ask_slow(A, 16 * w + (idx & 3) + 12, tb);
  unsigned tc[2] = {};
  if (N == 4 && AWH > 0) tc[0] = *reinterpret_cast<const unsigned*>(A.Wo + (size_t)idx * 65536 + 32768 + (64 * w + l) * 64);
  if (N == 4 && AWH > 1 && w >= 4) tc[1] = *reinterpret_cast<const unsigned*>(A.Wo + (size_t)idx * 65536 + (64 * w + l) * 64);
  const u64 tag = call_tag(A);
  unsigned stw = 0u;
#pragma unroll
  for (int i = 0; i < N; ++i) {
    const unsigned t = row_stats(raw[i]);
    if ((l >> 1) == i) stw = t;
  }
  if (l < 2 * N) __hip_atomic_store((gu64*)(A.ws + OFF_GT) + 2 * r0 + l, tagged(tag, (l & 1) ? stw : stw << 16), RLX_AGENT);
  SST(0);
  ASKED(ta);
  if (N > 4) ASKED(tb);
  asm volatile("" ::"v"(tc[0]), "v"(tc[1]));
  return tag;
}
#define MFMAH(a, b, c) __builtin_amdgcn_mfma_f32_16x16x32_f16(a, b, c, 0, 0, 0)
struct Cx {
  f32x2_t r, i;
};
__device__ __forceinline__ Cx cmul(const Cx a, const Cx b) { return Cx{a.r * b.r - a.i * b.i, a.r * b.i + a.i * b.r}; }
struct KGen {
  unsigned pa, pb, pc, pd;
  u16 pe;
  f32x2_t ar, ai;
  Cx C;
  char* st;
  v4u bh[8], bl[8];
  __device__ __forceinline__ void load(const MegaArgs& A, const int d, const int l) {
    pa = *reinterpret_cast<const unsigned*>(A.logAre + d * NST + 2 * l);
    pb = *reinterpret_cast<const unsigned*>(A.Aim + d * NST + 2 * l);
    pc = *reinterpret_cast<const unsigned*>(A.Cre + d * NST + 2 * l);
    pd = *reinterpret_cast<const unsigned*>(A.Cim + d * NST + 2 * l);
    pe = A.logdt[d];
  }
  __device__ __forceinline__ void setup() {
    const float dt = bfr(expf(bf2f(pe)));
    ar = f32x2_t{-bfr(expf(lo16(pa))) * dt, -bfr(expf(hi16(pa))) * dt};
    ai = f32x2_t{lo16(pb) * dt, hi16(pb) * dt};
    C = Cx{f32x2_t{lo16(pc) * dt, hi16(pc) * dt}, f32x2_t{lo16(pd) * dt, hi16(pd) * dt}};
  }
  __device__ __forceinline__ float scale() {
    const float b = 2.0f * wave_sum((fabsf(C.r.x) + fabsf(C.r.y)) + (fabsf(C.i.x) + fabsf(C.i.y)));
    const unsigned eb = max(__float_as_uint(b) >> 23, 25u);
    const float ks = __uint_as_float((268u - eb) << 23);
    C.r *= ks;
    C.i *= ks;
    return __uint_as_float((eb - 24u) << 23);
  }
  __device__ __forceinline__ Cx power(const float p) const {
    Cx z;
#pragma unroll
    for (int e = 0; e < 2; ++e) {
      const float x = ai[e] * p;
      const float f = __builtin_fmaf(x, 6.4206382e-9f, __builtin_fmaf(x, 0.15915494f, -__builtin_rintf(x * 0.15915494f)));
      const float m = __expf(ar[e] * p);
      z.r[e] = m * __builtin_amdgcn_cosf(f);
      z.i[e] = m * __builtin_amdgcn_sinf(f);
    }
    return z;
  }
  static __device__ __forceinline__ unsigned rest(const f32x2_t y, const unsigned h) {
    unsigned r;
    asm("v_fma_mixlo_f16 %0, %1, -1.0, %2 op_sel_hi:[1,0,0]" : "=v"(r) : "v"(h), "v"(y.x));
    asm("v_fma_mixhi_f16 %0, %1, -1.0, %2 op_sel:[1,0,0] op_sel_hi:[1,0,0]" : "+v"(r) : "v"(h), "v"(y.y));
    return r;
  }
  __device__ __forceinline__ char* rows(char* p, Cx& y, const Cx s, const int n) const {
#pragma unroll 1
    for (int i = 0; i < n; i += 2) {
#pragma unroll
      for (int k = 0; k < 2; ++k) {
        const unsigned h0 = pkh(y.r.x, y.r.y), h1 = pkh(y.i.x, y.i.y);
        *reinterpret_cast<v2u*>(p + 1040 * k) = v2u{h0, h1};
        *reinterpret_cast<v2u*>(p + 1040 * k + 512) = v2u{rest(y.r, h0), rest(y.i, h1)};
        y = cmul(y, s);
      }
      p += 2080;
    }
    return p;
  }
  __device__ __forceinline__ void get(v4u (&h)[8], v4u (&o)[8], const int l) const {
    const char* p = st + (l & 15) * 1040 + (l >> 4) * 16;
    asm volatile("" ::: "memory");
#pragma unroll
    for (int k = 0; k < 8; ++k) {
      h[k] = *reinterpret_cast<const v4u*>(p + 64 * k);
      o[k] = *reinterpret_cast<const v4u*>(p + 512 + 64 * k);
    }
    asm volatile("" ::: "memory");
  }
  Cx cy, cs;
  __device__ __forceinline__ void columns(const int l, const int j0, const int j1) {
    if (j0 == 0) {
      cs = power(16.f);
      cs.i = -cs.i;
      cy = Cx{f32x2_t{16384.f, 16384.f}, f32x2_t{0.f, 0.f}};
    }
    rows(st + l * 8 + 1040 * j0, cy, cs, j1 - j0);
  }
  template <bool COL>
  __device__ __forceinline__ f32x4 tile(const int l) const {
    v4u h[8], o[8];
    get(h, o, l);
    f32x4 d = {}, e = {}, f = {};
#define H8(x) __builtin_bit_cast(f16x8, x)
#define KMM(a, b, d) (COL ? MFMAH(H8(b), H8(a), d) : MFMAH(H8(a), H8(b), d))
#pragma unroll
    for (int k = 0; k < 8; ++k) {
      d = KMM(h[k], bh[k], d);
      e = KMM(o[k], bh[k], e);
      f = KMM(h[k], bl[k], f);
    }
#undef KMM
#undef H8
    return (d + (e + f)) * (1.0f / 8192);
  }
};
#define ACC_INIT 0xc9b26c86c9b26ca0ull
#define GEN_TL 2
#define GEN_TR 4
#define GEN_F0 2
#define GEN_T1 3
#define GEN_TB 0
#define GEN_F1 0
#define CHAIN_ASM_ATT \
  "s_mov_b32 s95, 272\n" \
  "s_mov_b32 s91, 0x4c7c2000\n" \
  "s_mov_b32 s85, 0xc9b26c9b\n" \
  "s_mov_b32 s92, 0xc9326c9b\n" \
  "s_mov_b32 s93, 0xc9326c9b\n" \
  "s_mov_b32 s78, 0x3e0293ee\n" \
  "s_mov_b32 s76, 0x33333333\n" \
  "s_mov_b32 s77, 0x33333333\n" \
  "s_lshl_b32 s98, s73, 10\n" \
  "s_add_u32 s98, s98, s74\n" \
  "s_add_u32 s98, s98, 131072\n" \
  "s_lshl_b32 s97, s73, 9\n" \
  "s_add_u32 s97, s97, s74\n" \
  "s_add_u32 s97, s97, 158720\n" \
  "s_lshl_b32 s99, s73, 10\n" \
  "v_mbcnt_lo_u32_b32 v48, -1, 0\n" \
  "v_mbcnt_hi_u32_b32 v48, -1, v48\n" \
  "s_add_u32 s88, s74, 157760\n" \
  "v_mov_b32 v59, s88\n" \
  "v_lshrrev_b32 v49, 2, v48\n" \
  "v_lshrrev_b32 v50, 4, v48\n" \
  "v_bitop3_b32 v50, v49, v50, 3 bitop3:0x28\n" \
  "v_lshl_add_u32 v8, v50, 3, s97\n" \
  "v_and_b32 v50, 3, v48\n" \
  "v_lshl_add_u32 v8, v50, 5, v8\n" \
  "v_and_b32 v49, 1, v49\n" \
  "v_lshl_add_u32 v8, v49, 8, v8\n" \
  "v_and_b32 v49, 8, v48\n" \
  "v_lshl_add_u32 v8, v49, 4, v8\n" \
  "v_lshrrev_b32 v49, 5, v48\n" \
  "v_add_u32 v10, s99, v1\n" \
  "s_lshr_b32 s88, s99, 2\n" \
  "s_add_u32 s88, s88, s74\n" \
  "s_add_u32 s88, s88, 139264\n" \
  "v_lshl_add_u32 v7, v49, 10, s88\n" \
  "v_and_b32 v49, 31, v48\n" \
  "v_lshl_add_u32 v7, v49, 3, v7\n" \
  "v_mov_b64 v[12:13], 0\n" \
  "v_mov_b64 v[14:15], 0\n" \
  "v_mov_b64 v[16:17], 0\n" \
  "v_mov_b64 v[18:19], 0\n" \
  "v_mov_b64 v[20:21], 0\n" \
  "v_mov_b64 v[22:23], 0\n" \
  "v_mov_b32 v34, 0xc9b26ca0\n" \
  "v_mov_b32 v35, 0xc9b26c86\n" \
  "s_mov_b32 s87, 0x40000\n" \
  "v_subrev_u32 v30, 64, v59\n" \
  "v_add_u32 v17, 0x10000, v28\n" \
  "v_xor_b32 v18, 128, v28\n" \
  "v_xor_b32 v19, 128, v17\n" \
  "ds_write_b32 v30, v12 offset:0\n" \
  "ds_write_b32 v30, v12 offset:32\n" \
  "ds_write_b32 v30, v12 offset:64\n" \
  "v_xor_b32 v20, 0, v29\n" \
  "v_xor_b32 v21, 32, v29\n" \
  "v_xor_b32 v22, 64, v29\n" \
  "v_xor_b32 v23, 96, v29\n" \
  "s_waitcnt vmcnt(12) lgkmcnt(0)\n" \
  "s_barrier\n" \
  "ds_read_b64_tr_b16 v[160:161], v20 offset:0\n" \
  "ds_read_b64_tr_b16 v[162:163], v20 offset:2048\n" \
  "ds_read_b64_tr_b16 v[168:169], v21 offset:0\n" \
  "ds_read_b64_tr_b16 v[170:171], v21 offset:2048\n" \
  "ds_read_b64_tr_b16 v[176:177], v22 offset:0\n" \
  "ds_read_b64_tr_b16 v[178:179], v22 offset:2048\n" \
  "ds_read_b64_tr_b16 v[184:185], v23 offset:0\n" \
  "ds_read_b64_tr_b16 v[186:187], v23 offset:2048\n" \
  "s_lshl_b32 s88, s73, 10\n" \
  "v_add_u32 v48, s88, v1\n" \
  "ds_read_b128 v[52:55], v48 offset:0\n" \
  "s_waitcnt lgkmcnt(0)\n" \
  "v_mov_b32 v11, 0\n" \
  "v_dot2c_f32_bf16 v11, v52, v52\n" \
  "v_dot2c_f32_bf16 v11, v53, v53\n" \
  "v_dot2c_f32_bf16 v11, v54, v54\n" \
  "v_dot2c_f32_bf16 v11, v55, v55\n" \
  "s_nop 0\n" \
  "s_nop 1\n" \
  "v_max_u32_dpp v11, v11, v11 row_ror:1 row_mask:0xf bank_mask:0xf\n" \
  "s_nop 1\n" \
  "v_max_u32_dpp v11, v11, v11 row_ror:2 row_mask:0xf bank_mask:0xf\n" \
  "s_nop 1\n" \
  "v_max_u32_dpp v11, v11, v11 row_ror:4 row_mask:0xf bank_mask:0xf\n" \
  "s_nop 1\n" \
  "v_max_u32_dpp v11, v11, v11 row_ror:8 row_mask:0xf bank_mask:0xf\n" \
  "v_mov_b32 v31, v11\n" \
  "s_nop 1\n" \
  "v_permlane16_swap_b32 v11, v31\n" \
  "v_max_u32 v11, v11, v31\n" \
  "v_mov_b32 v31, v11\n" \
  "s_nop 1\n" \
  "v_permlane32_swap_b32 v11, v31\n" \
  "v_max_u32 v11, v11, v31\n" \
  "s_mov_b64 exec, 1\n" \
  "ds_max_u32 v59, v11\n" \
  "s_mov_b64 exec, -1\n" \
  "s_waitcnt vmcnt(8) lgkmcnt(0)\n" \
  "s_barrier\n" \
  "ds_read_b64_tr_b16 v[164:165], v20 offset:4096\n" \
  "ds_read_b64_tr_b16 v[166:167], v20 offset:6144\n" \
  "ds_read_b64_tr_b16 v[172:173], v21 offset:4096\n" \
  "ds_read_b64_tr_b16 v[174:175], v21 offset:6144\n" \
  "ds_read_b64_tr_b16 v[180:181], v22 offset:4096\n" \
  "ds_read_b64_tr_b16 v[182:183], v22 offset:6144\n" \
  "ds_read_b64_tr_b16 v[188:189], v23 offset:4096\n" \
  "ds_read_b64_tr_b16 v[190:191], v23 offset:6144\n" \
  "s_add_u32 m0, s19, 0\n" \
  "s_nop 0\n" \
  "global_load_lds_dwordx4 v24, s[4:5] offset:0\n" \
  "global_load_lds_dwordx4 v25, s[4:5] offset:1024\n" \
  "global_load_lds_dwordx4 v26, s[4:5] offset:2048\n" \
  "global_load_lds_dwordx4 v27, s[4:5] offset:3072\n" \
  "s_add_u32 s4, s4, 0x100000\n" \
  "s_addc_u32 s5, s5, 0\n" \
  "v_mfma_f32_16x16x32_bf16 v[64:67], v[160:163], v[160:163], 0\n" \
  "v_mfma_f32_16x16x32_bf16 v[68:71], v[168:171], v[168:171], 0\n" \
  "v_mfma_f32_16x16x32_bf16 v[72:75], v[176:179], v[176:179], 0\n" \
  "v_mfma_f32_16x16x32_bf16 v[76:79], v[184:187], v[184:187], 0\n" \
  "s_waitcnt vmcnt(8) lgkmcnt(0)\n" \
  "s_barrier\n" \
  "ds_read_b64_tr_b16 v[32:33], v20 offset:8192\n" \
  "ds_read_b64_tr_b16 v[34:35], v20 offset:10240\n" \
  "ds_read_b64_tr_b16 v[40:41], v21 offset:8192\n" \
  "ds_read_b64_tr_b16 v[42:43], v21 offset:10240\n" \
  "ds_read_b64_tr_b16 v[48:49], v22 offset:8192\n" \
  "ds_read_b64_tr_b16 v[50:51], v22 offset:10240\n" \
  "ds_read_b64_tr_b16 v[56:57], v23 offset:8192\n" \
  "ds_read_b64_tr_b16 v[58:59], v23 offset:10240\n" \
  "s_add_u32 m0, s19, 4096\n" \
  "s_nop 0\n" \
  "global_load_lds_dwordx4 v24, s[4:5] offset:0\n" \
  "global_load_lds_dwordx4 v25, s[4:5] offset:1024\n" \
  "global_load_lds_dwordx4 v26, s[4:5] offset:2048\n" \
  "global_load_lds_dwordx4 v27, s[4:5] offset:3072\n" \
  "s_add_u32 s4, s4, 0x100000\n" \
  "s_addc_u32 s5, s5, 0\n" \
  "v_mfma_f32_16x16x32_bf16 v[64:67], v[164:167], v[164:167], v[64:67]\n" \
  "v_mfma_f32_16x16x32_bf16 v[68:71], v[172:175], v[172:175], v[68:71]\n" \
  "v_mfma_f32_16x16x32_bf16 v[72:75], v[180:183], v[180:183], v[72:75]\n" \
  "v_mfma_f32_16x16x32_bf16 v[76:79], v[188:191], v[188:191], v[76:79]\n" \
  "s_waitcnt vmcnt(8) lgkmcnt(0)\n" \
  "s_barrier\n" \
  "ds_read_b64_tr_b16 v[36:37], v20 offset:12288\n" \
  "ds_read_b64_tr_b16 v[38:39], v20 offset:14336\n" \
  "ds_read_b64_tr_b16 v[44:45], v21 offset:12288\n" \
  "ds_read_b64_tr_b16 v[46:47], v21 offset:14336\n" \
  "ds_read_b64_tr_b16 v[52:53], v22 offset:12288\n" \
  "ds_read_b64_tr_b16 v[54:55], v22 offset:14336\n" \
  "ds_read_b64_tr_b16 v[60:61], v23 offset:12288\n" \
  "ds_read_b64_tr_b16 v[62:63], v23 offset:14336\n" \
  "s_add_u32 m0, s19, 8192\n" \
  "s_nop 0\n" \
  "global_load_lds_dwordx4 v24, s[4:5] offset:0\n" \
  "global_load_lds_dwordx4 v25, s[4:5] offset:1024\n" \
  "global_load_lds_dwordx4 v26, s[4:5] offset:2048\n" \
  "global_load_lds_dwordx4 v27, s[4:5] offset:3072\n" \
  "s_add_u32 s4, s4, 0x100000\n" \
  "s_addc_u32 s5, s5, 0\n" \
  "v_mfma_f32_16x16x32_bf16 v[64:67], v[32:35], v[32:35], v[64:67]\n" \
  "v_mfma_f32_16x16x32_bf16 v[68:71], v[40:43], v[40:43], v[68:71]\n" \
  "v_mfma_f32_16x16x32_bf16 v[72:75], v[48:51], v[48:51], v[72:75]\n" \
  "v_mfma_f32_16x16x32_bf16 v[76:79], v[56:59], v[56:59], v[76:79]\n" \
  "s_waitcnt vmcnt(8) lgkmcnt(0)\n" \
  "s_barrier\n" \
  "s_add_u32 m0, s19, 12288\n" \
  "s_nop 0\n" \
  "global_load_lds_dwordx4 v24, s[4:5] offset:0\n" \
  "global_load_lds_dwordx4 v25, s[4:5] offset:1024\n" \
  "global_load_lds_dwordx4 v26, s[4:5] offset:2048\n" \
  "global_load_lds_dwordx4 v27, s[4:5] offset:3072\n" \
  "s_add_u32 s4, s4, 0x100000\n" \
  "s_addc_u32 s5, s5, 0\n" \
  "v_mfma_f32_16x16x32_bf16 v[64:67], v[36:39], v[36:39], v[64:67]\n" \
  "v_mfma_f32_16x16x32_bf16 v[68:71], v[44:47], v[44:47], v[68:71]\n" \
  "v_mfma_f32_16x16x32_bf16 v[72:75], v[52:55], v[52:55], v[72:75]\n" \
  "v_mfma_f32_16x16x32_bf16 v[76:79], v[60:63], v[60:63], v[76:79]\n" \
  "s_nop 7\n" \
  "v_max3_f32 v13, v64, v65, v66\n" \
  "v_max3_f32 v13, v13, v67, v68\n" \
  "v_max3_f32 v13, v13, v69, v70\n" \
  "v_max3_f32 v13, v13, v71, v72\n" \
  "v_max3_f32 v13, v13, v73, v74\n" \
  "v_max3_f32 v13, v13, v75, v76\n" \
  "v_max3_f32 v13, v13, v77, v78\n" \
  "v_max_f32 v13, v13, v79\n" \
  "s_nop 1\n" \
  "v_max_f32_dpp v13, v13, v13 row_ror:1 row_mask:0xf bank_mask:0xf\n" \
  "s_nop 1\n" \
  "v_max_f32_dpp v13, v13, v13 row_ror:2 row_mask:0xf bank_mask:0xf\n" \
  "s_nop 1\n" \
  "v_max_f32_dpp v13, v13, v13 row_ror:4 row_mask:0xf bank_mask:0xf\n" \
  "s_nop 1\n" \
  "v_max_f32_dpp v13, v13, v13 row_ror:8 row_mask:0xf bank_mask:0xf\n" \
  "v_mov_b32 v31, v13\n" \
  "s_nop 1\n" \
  "v_permlane16_swap_b32 v13, v31\n" \
  "v_max_f32 v13, v13, v31\n" \
  "v_mov_b32 v31, v13\n" \
  "s_nop 1\n" \
  "v_permlane32_swap_b32 v13, v31\n" \
  "v_max_f32 v13, v13, v31\n" \
  "s_mov_b64 exec, 1\n" \
  "ds_max_u32 v30, v13 offset:32\n" \
  "s_mov_b64 exec, -1\n" \
  "s_waitcnt vmcnt(8) lgkmcnt(0)\n" \
  "s_barrier\n" \
  "s_add_u32 m0, s19, 0\n" \
  "s_nop 0\n" \
  "global_load_lds_dwordx4 v24, s[6:7] offset:0\n" \
  "global_load_lds_dwordx4 v25, s[6:7] offset:1024\n" \
  "global_load_lds_dwordx4 v26, s[6:7] offset:2048\n" \
  "global_load_lds_dwordx4 v27, s[6:7] offset:3072\n" \
  "s_add_u32 s6, s6, 0x100000\n" \
  "s_addc_u32 s7, s7, 0\n" \
  "s_waitcnt vmcnt(8) lgkmcnt(0)\n" \
  "s_barrier\n" \
  "s_add_u32 m0, s19, 4096\n" \
  "s_nop 0\n" \
  "global_load_lds_dwordx4 v24, s[6:7] offset:0\n" \
  "global_load_lds_dwordx4 v25, s[6:7] offset:1024\n" \
  "global_load_lds_dwordx4 v26, s[6:7] offset:2048\n" \
  "global_load_lds_dwordx4 v27, s[6:7] offset:3072\n" \
  "s_add_u32 s6, s6, 0x100000\n" \
  "s_addc_u32 s7, s7, 0\n" \
  "s_waitcnt vmcnt(8) lgkmcnt(0)\n" \
  "s_barrier\n" \
  "s_add_u32 m0, s19, 8192\n" \
  "s_nop 0\n" \
  "global_load_lds_dwordx4 v24, s[6:7] offset:0\n" \
  "global_load_lds_dwordx4 v25, s[6:7] offset:1024\n" \
  "global_load_lds_dwordx4 v26, s[6:7] offset:2048\n" \
  "global_load_lds_dwordx4 v27, s[6:7] offset:3072\n" \
  "s_add_u32 s6, s6, 0x100000\n" \
  "s_addc_u32 s7, s7, 0\n" \
  "s_waitcnt vmcnt(8) lgkmcnt(0)\n" \
  "s_barrier\n" \
  "ds_read_b64_tr_b16 v[64:65], v28 offset:0\n" \
  "ds_read_b64_tr_b16 v[66:67], v28 offset:512\n" \
  "ds_read_b64_tr_b16 v[68:69], v28 offset:32768\n" \
  "ds_read_b64_tr_b16 v[70:71], v28 offset:33280\n" \
  "ds_read_b64_tr_b16 v[72:73], v17 offset:0\n" \
  "ds_read_b64_tr_b16 v[74:75], v17 offset:512\n" \
  "ds_read_b64_tr_b16 v[76:77], v17 offset:32768\n" \
  "ds_read_b64_tr_b16 v[78:79], v17 offset:33280\n" \
  "ds_read_b64_tr_b16 v[192:193], v18 offset:0\n" \
  "ds_read_b64_tr_b16 v[194:195], v18 offset:512\n" \
  "ds_read_b64_tr_b16 v[196:197], v18 offset:32768\n" \
  "ds_read_b64_tr_b16 v[198:199], v18 offset:33280\n" \
  "ds_read_b64_tr_b16 v[200:201], v19 offset:0\n" \
  "ds_read_b64_tr_b16 v[202:203], v19 offset:512\n" \
  "ds_read_b64_tr_b16 v[204:205], v19 offset:32768\n" \
  "ds_read_b64_tr_b16 v[206:207], v19 offset:33280\n" \
  "s_add_u32 m0, s19, 12288\n" \
  "s_nop 0\n" \
  "global_load_lds_dwordx4 v24, s[6:7] offset:0\n" \
  "global_load_lds_dwordx4 v25, s[6:7] offset:1024\n" \
  "global_load_lds_dwordx4 v26, s[6:7] offset:2048\n" \
  "global_load_lds_dwordx4 v27, s[6:7] offset:3072\n" \
  "s_add_u32 s6, s6, 0x100000\n" \
  "s_addc_u32 s7, s7, 0\n" \
  "s_waitcnt vmcnt(8) lgkmcnt(0)\n" \
  "s_barrier\n" \
  "ds_read_b64_tr_b16 v[80:81], v28 offset:4096\n" \
  "ds_read_b64_tr_b16 v[82:83], v28 offset:4608\n" \
  "ds_read_b64_tr_b16 v[84:85], v28 offset:36864\n" \
  "ds_read_b64_tr_b16 v[86:87], v28 offset:37376\n" \
  "ds_read_b64_tr_b16 v[88:89], v17 offset:4096\n" \
  "ds_read_b64_tr_b16 v[90:91], v17 offset:4608\n" \
  "ds_read_b64_tr_b16 v[92:93], v17 offset:36864\n" \
  "ds_read_b64_tr_b16 v[94:95], v17 offset:37376\n" \
  "ds_read_b64_tr_b16 v[208:209], v18 offset:4096\n" \
  "ds_read_b64_tr_b16 v[210:211], v18 offset:4608\n" \
  "ds_read_b64_tr_b16 v[212:213], v18 offset:36864\n" \
  "ds_read_b64_tr_b16 v[214:215], v18 offset:37376\n" \
  "ds_read_b64_tr_b16 v[216:217], v19 offset:4096\n" \
  "ds_read_b64_tr_b16 v[218:219], v19 offset:4608\n" \
  "ds_read_b64_tr_b16 v[220:221], v19 offset:36864\n" \
  "ds_read_b64_tr_b16 v[222:223], v19 offset:37376\n" \
  "s_add_u32 m0, s19, 0\n" \
  "s_nop 0\n" \
  "global_load_lds_dwordx4 v24, s[8:9] offset:0\n" \
  "global_load_lds_dwordx4 v25, s[8:9] offset:1024\n" \
  "global_load_lds_dwordx4 v26, s[8:9] offset:2048\n" \
  "global_load_lds_dwordx4 v27, s[8:9] offset:3072\n" \
  "s_add_u32 s8, s8, 0x100000\n" \
  "s_addc_u32 s9, s9, 0\n" \
  "s_waitcnt vmcnt(8) lgkmcnt(0)\n" \
  "s_barrier\n" \
  "ds_read_b64_tr_b16 v[96:97], v28 offset:8192\n" \
  "ds_read_b64_tr_b16 v[98:99], v28 offset:8704\n" \
  "ds_read_b64_tr_b16 v[100:101], v28 offset:40960\n" \
  "ds_read_b64_tr_b16 v[102:103], v28 offset:41472\n" \
  "ds_read_b64_tr_b16 v[104:105], v17 offset:8192\n" \
  "ds_read_b64_tr_b16 v[106:107], v17 offset:8704\n" \
  "ds_read_b64_tr_b16 v[108:109], v17 offset:40960\n" \
  "ds_read_b64_tr_b16 v[110:111], v17 offset:41472\n" \
  "ds_read_b64_tr_b16 v[224:225], v18 offset:8192\n" \
  "ds_read_b64_tr_b16 v[226:227], v18 offset:8704\n" \
  "ds_read_b64_tr_b16 v[228:229], v18 offset:40960\n" \
  "ds_read_b64_tr_b16 v[230:231], v18 offset:41472\n" \
  "ds_read_b64_tr_b16 v[232:233], v19 offset:8192\n" \
  "ds_read_b64_tr_b16 v[234:235], v19 offset:8704\n" \
  "ds_read_b64_tr_b16 v[236:237], v19 offset:40960\n" \
  "ds_read_b64_tr_b16 v[238:239], v19 offset:41472\n" \
  "s_add_u32 m0, s19, 4096\n" \
  "s_nop 0\n" \
  "global_load_lds_dwordx4 v24, s[8:9] offset:0\n" \
  "global_load_lds_dwordx4 v25, s[8:9] offset:1024\n" \
  "global_load_lds_dwordx4 v26, s[8:9] offset:2048\n" \
  "global_load_lds_dwordx4 v27, s[8:9] offset:3072\n" \
  "s_add_u32 s8, s8, 0x100000\n" \
  "s_addc_u32 s9, s9, 0\n" \
  "s_waitcnt vmcnt(8) lgkmcnt(0)\n" \
  "s_barrier\n" \
  "ds_read_b64_tr_b16 v[112:113], v28 offset:12288\n" \
  "ds_read_b64_tr_b16 v[114:115], v28 offset:12800\n" \
  "ds_read_b64_tr_b16 v[116:117], v28 offset:45056\n" \
  "ds_read_b64_tr_b16 v[118:119], v28 offset:45568\n" \
  "ds_read_b64_tr_b16 v[120:121], v17 offset:12288\n" \
  "ds_read_b64_tr_b16 v[122:123], v17 offset:12800\n" \
  "ds_read_b64_tr_b16 v[124:125], v17 offset:45056\n" \
  "ds_read_b64_tr_b16 v[126:127], v17 offset:45568\n" \
  "ds_read_b64_tr_b16 v[240:241], v18 offset:12288\n" \
  "ds_read_b64_tr_b16 v[242:243], v18 offset:12800\n" \
  "ds_read_b64_tr_b16 v[244:245], v18 offset:45056\n" \
  "ds_read_b64_tr_b16 v[246:247], v18 offset:45568\n" \
  "ds_read_b64_tr_b16 v[248:249], v19 offset:12288\n" \
  "ds_read_b64_tr_b16 v[250:251], v19 offset:12800\n" \
  "ds_read_b64_tr_b16 v[252:253], v19 offset:45056\n" \
  "ds_read_b64_tr_b16 v[254:255], v19 offset:45568\n" \
  "s_add_u32 m0, s19, 8192\n" \
  "s_nop 0\n" \
  "global_load_lds_dwordx4 v24, s[8:9] offset:0\n" \
  "global_load_lds_dwordx4 v25, s[8:9] offset:1024\n" \
  "global_load_lds_dwordx4 v26, s[8:9] offset:2048\n" \
  "global_load_lds_dwordx4 v27, s[8:9] offset:3072\n" \
  "s_add_u32 s8, s8, 0x100000\n" \
  "s_addc_u32 s9, s9, 0\n" \
  "s_waitcnt vmcnt(8) lgkmcnt(0)\n" \
  "s_barrier\n" \
  "ds_read_b64_tr_b16 v[128:129], v28 offset:0\n" \
  "ds_read_b64_tr_b16 v[130:131], v28 offset:512\n" \
  "ds_read_b64_tr_b16 v[132:133], v28 offset:32768\n" \
  "ds_read_b64_tr_b16 v[134:135], v28 offset:33280\n" \
  "ds_read_b64_tr_b16 v[136:137], v17 offset:0\n" \
  "ds_read_b64_tr_b16 v[138:139], v17 offset:512\n" \
  "ds_read_b64_tr_b16 v[140:141], v17 offset:32768\n" \
  "ds_read_b64_tr_b16 v[142:143], v17 offset:33280\n" \
  "s_add_u32 m0, s19, 12288\n" \
  "s_nop 0\n" \
  "global_load_lds_dwordx4 v24, s[8:9] offset:0\n" \
  "global_load_lds_dwordx4 v25, s[8:9] offset:1024\n" \
  "global_load_lds_dwordx4 v26, s[8:9] offset:2048\n" \
  "global_load_lds_dwordx4 v27, s[8:9] offset:3072\n" \
  "s_add_u32 s8, s8, 0x100000\n" \
  "s_addc_u32 s9, s9, 0\n" \
  "s_waitcnt vmcnt(8) lgkmcnt(0)\n" \
  "s_barrier\n" \
  "ds_write_b128 v6, v[160:163] offset:0\n" \
  "ds_write_b128 v6, v[164:167] offset:1024\n" \
  "ds_write_b128 v6, v[168:171] offset:2048\n" \
  "ds_write_b128 v6, v[172:175] offset:3072\n" \
  "ds_read_b64_tr_b16 v[144:145], v28 offset:4096\n" \
  "ds_read_b64_tr_b16 v[146:147], v28 offset:4608\n" \
  "ds_read_b64_tr_b16 v[148:149], v28 offset:36864\n" \
  "ds_read_b64_tr_b16 v[150:151], v28 offset:37376\n" \
  "ds_read_b64_tr_b16 v[152:153], v17 offset:4096\n" \
  "ds_read_b64_tr_b16 v[154:155], v17 offset:4608\n" \
  "ds_read_b64_tr_b16 v[156:157], v17 offset:36864\n" \
  "ds_read_b64_tr_b16 v[158:159], v17 offset:37376\n" \
  "s_waitcnt vmcnt(4) lgkmcnt(0)\n" \
  "s_barrier\n" \
  "ds_write_b128 v6, v[176:179] offset:4096\n" \
  "ds_write_b128 v6, v[180:183] offset:5120\n" \
  "ds_write_b128 v6, v[184:187] offset:6144\n" \
  "ds_write_b128 v6, v[188:191] offset:7168\n" \
  "ds_read_b64_tr_b16 v[160:161], v28 offset:8192\n" \
  "ds_read_b64_tr_b16 v[162:163], v28 offset:8704\n" \
  "ds_read_b64_tr_b16 v[164:165], v28 offset:40960\n" \
  "ds_read_b64_tr_b16 v[166:167], v28 offset:41472\n" \
  "ds_read_b64_tr_b16 v[168:169], v17 offset:8192\n" \
  "ds_read_b64_tr_b16 v[170:171], v17 offset:8704\n" \
  "ds_read_b64_tr_b16 v[172:173], v17 offset:40960\n" \
  "ds_read_b64_tr_b16 v[174:175], v17 offset:41472\n" \
  "s_waitcnt vmcnt(0) lgkmcnt(0)\n" \
  "s_barrier\n" \
  "ds_write_b128 v6, v[32:35] offset:8192\n" \
  "ds_write_b128 v6, v[36:39] offset:9216\n" \
  "ds_write_b128 v6, v[40:43] offset:10240\n" \
  "ds_write_b128 v6, v[44:47] offset:11264\n" \
  "ds_read_b64_tr_b16 v[176:177], v28 offset:12288\n" \
  "ds_read_b64_tr_b16 v[178:179], v28 offset:12800\n" \
  "ds_read_b64_tr_b16 v[180:181], v28 offset:45056\n" \
  "ds_read_b64_tr_b16 v[182:183], v28 offset:45568\n" \
  "ds_read_b64_tr_b16 v[184:185], v17 offset:12288\n" \
  "ds_read_b64_tr_b16 v[186:187], v17 offset:12800\n" \
  "ds_read_b64_tr_b16 v[188:189], v17 offset:45056\n" \
  "ds_read_b64_tr_b16 v[190:191], v17 offset:45568\n" \
  "ds_read_b32 v31, v30 offset:0\n" \
  "s_waitcnt lgkmcnt(0)\n" \
  "s_barrier\n" \
  "ds_write_b128 v6, v[48:51] offset:12288\n" \
  "ds_write_b128 v6, v[52:55] offset:13312\n" \
  "ds_write_b128 v6, v[56:59] offset:14336\n" \
  "ds_write_b128 v6, v[60:63] offset:15360\n" \
  "v_mul_f32 v31, 0x3f8e353f, v31\n" \
  "v_mul_f32 v30, 0x44870000, v31\n" \
  "v_mov_b64 v[12:13], 0\n" \
  "v_mov_b64 v[14:15], 0\n" \
  "v_mov_b64 v[16:17], 0\n" \
  "v_mov_b64 v[18:19], 0\n" \
  "v_mov_b64 v[20:21], 0\n" \
  "v_mov_b64 v[22:23], 0\n" \
  "v_readfirstlane_b32 s72, v31\n" \
  "v_readfirstlane_b32 s86, v30\n" \
  "s_lshr_b32 s86, s86, 23\n" \
  "s_addk_i32 s86, 0xff83\n" \
  "s_ashr_i32 s86, s86, 1\n" \
  "s_max_i32 s86, s86, -20\n" \
  "s_min_i32 s86, s86, 100\n" \
  "s_mov_b32 s82, s86\n" \
  "s_lshr_b32 s83, s72, 23\n" \
  "s_sub_i32 s83, s83, 243\n" \
  "s_waitcnt lgkmcnt(0)\n" \
  "s_add_u32 s88, s74, 157760\n" \
  "v_mov_b32 v59, s88\n" \
  "v_mov_b32 v34, 0xc9b26ca0\n" \
  "v_mov_b32 v35, 0xc9b26c86\n" \
  ".p2align 6\n" \
  "s_nop 0\n" \
  "s_nop 0\n" \
  "s_nop 0\n" \
  "s_nop 0\n" \
  ".LCA_TOP_%=:\n" \
  "s_waitcnt lgkmcnt(0)\n" \
  "s_barrier\n" \
  "ds_read_b128 v[40:43], v1 offset:0\n" \
  "ds_read_b128 v[44:47], v1 offset:1024\n" \
  "ds_read_b128 v[48:51], v1 offset:2048\n" \
  "ds_read_b128 v[52:55], v1 offset:3072\n" \
  "ds_read_b128 v[56:59], v1 offset:4096\n" \
  "ds_read_b128 v[60:63], v1 offset:5120\n" \
  "s_cmp_lg_u32 s73, 0\n" \
  "s_cbranch_scc1 .LCA_NOZ_%=\n" \
  "s_xor_b32 s88, s60, 0x10000\n" \
  "s_add_u32 s88, s88, s61\n" \
  "buffer_store_dwordx2 v[34:35], v5, s[40:43], s88 offen sc1\n" \
  ".LCA_NOZ_%=:\n" \
  "s_waitcnt lgkmcnt(5)\n" \
  "v_smfmac_f32_16x16x64_bf16 v[20:23], v[40:43], v[192:199], v0\n" \
  "v_smfmac_f32_16x16x64_bf16 v[12:15], v[40:43], v[64:71], v0\n" \
  "v_smfmac_f32_16x16x64_bf16 v[16:19], v[40:43], v[128:135], v0\n" \
  "ds_read_b128 v[40:43], v1 offset:6144\n" \
  "s_waitcnt lgkmcnt(5)\n" \
  "v_smfmac_f32_16x16x64_bf16 v[20:23], v[44:47], v[200:207], v0\n" \
  "v_smfmac_f32_16x16x64_bf16 v[12:15], v[44:47], v[72:79], v0\n" \
  "v_smfmac_f32_16x16x64_bf16 v[16:19], v[44:47], v[136:143], v0\n" \
  "ds_read_b128 v[44:47], v1 offset:7168\n" \
  "s_waitcnt lgkmcnt(5)\n" \
  "v_smfmac_f32_16x16x64_bf16 v[20:23], v[48:51], v[208:215], v0\n" \
  "v_smfmac_f32_16x16x64_bf16 v[12:15], v[48:51], v[80:87], v0\n" \
  "v_smfmac_f32_16x16x64_bf16 v[16:19], v[48:51], v[144:151], v0\n" \
  "ds_read_b128 v[24:27], v1 offset:10240\n" \
  "ds_read_b128 v[28:31], v1 offset:14336\n" \
  "s_waitcnt lgkmcnt(6)\n" \
  "v_smfmac_f32_16x16x64_bf16 v[20:23], v[52:55], v[216:223], v0\n" \
  "v_smfmac_f32_16x16x64_bf16 v[12:15], v[52:55], v[88:95], v0\n" \
  "v_smfmac_f32_16x16x64_bf16 v[16:19], v[52:55], v[152:159], v0\n" \
  "ds_read_b128 v[32:35], v1 offset:11264\n" \
  "ds_read_b128 v[36:39], v1 offset:15360\n" \
  "s_waitcnt lgkmcnt(7)\n" \
  "v_smfmac_f32_16x16x64_bf16 v[20:23], v[56:59], v[224:231], v0\n" \
  "v_smfmac_f32_16x16x64_bf16 v[12:15], v[56:59], v[96:103], v0\n" \
  "v_smfmac_f32_16x16x64_bf16 v[16:19], v[56:59], v[160:167], v0\n" \
  "ds_read_b128 v[56:59], v1 offset:12288\n" \
  "s_waitcnt lgkmcnt(7)\n" \
  "v_smfmac_f32_16x16x64_bf16 v[20:23], v[60:63], v[232:239], v0\n" \
  "v_smfmac_f32_16x16x64_bf16 v[12:15], v[60:63], v[104:111], v0\n" \
  "v_smfmac_f32_16x16x64_bf16 v[16:19], v[60:63], v[168:175], v0\n" \
  "ds_read_b128 v[60:63], v1 offset:16384\n" \
  "s_waitcnt lgkmcnt(7)\n" \
  "v_smfmac_f32_16x16x64_bf16 v[20:23], v[40:43], v[240:247], v0\n" \
  "v_smfmac_f32_16x16x64_bf16 v[12:15], v[40:43], v[112:119], v0\n" \
  "v_smfmac_f32_16x16x64_bf16 v[16:19], v[40:43], v[176:183], v0\n" \
  "s_waitcnt lgkmcnt(6)\n" \
  "v_smfmac_f32_16x16x64_bf16 v[20:23], v[44:47], v[248:255], v0\n" \
  "v_smfmac_f32_16x16x64_bf16 v[12:15], v[44:47], v[120:127], v0\n" \
  "v_smfmac_f32_16x16x64_bf16 v[16:19], v[44:47], v[184:191], v0\n" \
  "s_cmp_eq_u32 s71, 0\n" \
  "v_permlane32_swap_b32 v20, v22\n" \
  "v_permlane32_swap_b32 v21, v23\n" \
  "v_pk_add_f32 v[20:21], v[20:21], v[22:23]\n" \
  "s_mov_b64 vcc, s[76:77]\n" \
  "v_permlane32_swap_b32 v12, v14\n" \
  "v_permlane32_swap_b32 v13, v15\n" \
  "v_cvt_pk_bf16_f32 v20, v20, v21\n" \
  "v_pk_add_f32 v[12:13], v[12:13], v[14:15]\n" \
  "v_permlane32_swap_b32 v16, v18\n" \
  "v_permlane32_swap_b32 v17, v19\n" \
  "ds_write_b32 v8, v20 offset:2048\n" \
  "v_cvt_pk_bf16_f32 v12, v12, v13\n" \
  "v_pk_add_f32 v[16:17], v[16:17], v[18:19]\n" \
  "s_nop 0\n" \
  "v_cvt_pk_bf16_f32 v13, v16, v17\n" \
  "ds_write_b64 v8, v[12:13]\n" \
  "s_setprio 1\n" \
  "s_waitcnt lgkmcnt(0)\n" \
  "s_barrier\n" \
  "s_cbranch_scc1 .LCA_LAST_%=\n" \
  "ds_read_b64_tr_b16 v[40:41], v2 offset:0\n" \
  "ds_read_b64_tr_b16 v[42:43], v3 offset:0\n" \
  "ds_read_b64_tr_b16 v[44:45], v2 offset:1024\n" \
  "ds_read_b64_tr_b16 v[46:47], v3 offset:1024\n" \
  "ds_read_b64_tr_b16 v[48:49], v2 offset:2048\n" \
  "ds_read_b64_tr_b16 v[50:51], v3 offset:2048\n" \
  "ds_read_b64_tr_b16 v[52:53], v2 offset:3072\n" \
  "ds_read_b64_tr_b16 v[54:55], v3 offset:3072\n" \
  "ds_read_b32 v11, v1 offset:26688\n" \
  "s_waitcnt lgkmcnt(7)\n" \
  "v_mfma_f32_16x16x32_bf16 v[12:15], v[24:27], v[40:43], 0\n" \
  "v_mfma_f32_16x16x32_bf16 v[16:19], v[28:31], v[40:43], 0\n" \
  "ds_read_b128 v[24:27], v1 offset:13312\n" \
  "ds_read_b128 v[28:31], v1 offset:17408\n" \
  "v_mfma_f32_16x16x32_bf16 v[20:23], v[40:43], v[40:43], 0\n" \
  "s_waitcnt lgkmcnt(7)\n" \
  "v_mfma_f32_16x16x32_bf16 v[12:15], v[32:35], v[44:47], v[12:15]\n" \
  "v_mfma_f32_16x16x32_bf16 v[16:19], v[36:39], v[44:47], v[16:19]\n" \
  "v_mfma_f32_16x16x32_bf16 v[20:23], v[44:47], v[44:47], v[20:23]\n" \
  "s_waitcnt lgkmcnt(2)\n" \
  "v_mfma_f32_16x16x32_bf16 v[12:15], v[56:59], v[48:51], v[12:15]\n" \
  "v_readfirstlane_b32 s86, v11\n" \
  "v_mfma_f32_16x16x32_bf16 v[16:19], v[60:63], v[48:51], v[16:19]\n" \
  "ds_read_b128 v[32:35], v6 offset:8192\n" \
  "ds_read_b128 v[36:39], v6 offset:9216\n" \
  "v_mfma_f32_16x16x32_bf16 v[20:23], v[48:51], v[48:51], v[20:23]\n" \
  "s_lshr_b32 s86, s86, 23\n" \
  "s_add_i32 s86, s86, s83\n" \
  "s_ashr_i32 s86, s86, 1\n" \
  "ds_read_b128 v[48:51], v10 offset:18432\n" \
  "v_mfma_f32_16x16x32_bf16 v[20:23], v[52:55], v[52:55], v[20:23]\n" \
  "s_max_i32 s86, s86, s82\n" \
  "s_min_i32 s86, s86, 100\n" \
  "s_lshl_b32 s86, s86, 23\n" \
  "s_sub_i32 s84, 0xc8800000, s86\n" \
  "s_waitcnt lgkmcnt(3)\n" \
  "v_mfma_f32_16x16x32_bf16 v[12:15], v[24:27], v[52:55], v[12:15]\n" \
  "v_mfma_f32_16x16x32_bf16 v[16:19], v[28:31], v[52:55], v[16:19]\n" \
  "ds_read_b128 v[52:55], v10 offset:22528\n" \
  "ds_read_b128 v[56:59], v6 offset:0\n" \
  "ds_read_b128 v[60:63], v6 offset:1024\n" \
  "ds_read_b128 v[24:27], v6 offset:2048\n" \
  "ds_read_b128 v[28:31], v6 offset:3072\n" \
  "v_cndmask_b32_dpp v20, v23, v22, vcc quad_perm:[2,3,0,1] row_mask:0xf bank_mask:0xf\n" \
  "v_mov_b32 v41, 0\n" \
  "v_mov_b32 v42, 0\n" \
  "v_cndmask_b32_dpp v12, v16, v12, vcc quad_perm:[2,3,0,1] row_mask:0xf bank_mask:0xf\n" \
  "v_cndmask_b32_dpp v13, v17, v13, vcc quad_perm:[2,3,0,1] row_mask:0xf bank_mask:0xf\n" \
  "v_cndmask_b32_dpp v14, v18, v14, vcc quad_perm:[2,3,0,1] row_mask:0xf bank_mask:0xf\n" \
  "v_cndmask_b32_dpp v15, v19, v15, vcc quad_perm:[2,3,0,1] row_mask:0xf bank_mask:0xf\n" \
  "v_maximum3_f32 v16, v20, v12, v13\n" \
  "v_maximum3_f32 v16, v16, v14, v15\n" \
  "s_nop 1\n" \
  "v_max_f32_dpp v16, v16, v16 quad_perm:[2,3,0,1] row_mask:0xf bank_mask:0xf\n" \
  "v_mov_b32 v17, v16\n" \
  "s_nop 1\n" \
  "v_permlane16_swap_b32 v16, v17\n" \
  "v_max_f32 v16, v16, v17\n" \
  "v_mov_b32 v17, v16\n" \
  "s_nop 1\n" \
  "v_permlane32_swap_b32 v16, v17\n" \
  "v_max_f32 v16, v16, v17\n" \
  "v_mul_f32 v16, s78, v16\n" \
  "v_pk_fma_f32 v[12:13], v[12:13], s[78:79], v[16:17] op_sel_hi:[1,0,0] neg_lo:[0,0,1] neg_hi:[0,0,1]\n" \
  "v_pk_fma_f32 v[14:15], v[14:15], s[78:79], v[16:17] op_sel_hi:[1,0,0] neg_lo:[0,0,1] neg_hi:[0,0,1]\n" \
  "v_fma_f32 v20, v20, s78, -v16\n" \
  "v_exp_f32 v12, v12\n" \
  "v_exp_f32 v13, v13\n" \
  "v_exp_f32 v14, v14\n" \
  "v_exp_f32 v15, v15\n" \
  "v_exp_f32 v20, v20\n" \
  "s_waitcnt lgkmcnt(4)\n" \
  "v_cvt_pk_bf16_f32 v44, v12, v13\n" \
  "v_cvt_pk_bf16_f32 v45, v14, v15\n" \
  "v_pk_add_f32 v[8:9], v[12:13], v[14:15]\n" \
  "v_mov_b32_dpp v46, v44 quad_perm:[2,3,0,1] row_mask:0xf bank_mask:0xf\n" \
  "v_mov_b32_dpp v47, v45 quad_perm:[2,3,0,1] row_mask:0xf bank_mask:0xf\n" \
  "v_add_f32 v10, v8, v20\n" \
  "v_add_f32 v10, v10, v9\n" \
  "v_mfma_f32_16x16x32_bf16 v[12:15], v[48:51], v[44:47], 0\n" \
  "v_mov_b32_dpp v21, v20 quad_perm:[2,3,0,1] row_mask:0xf bank_mask:0xf\n" \
  "v_add_f32_dpp v10, v10, v10 quad_perm:[2,3,0,1] row_mask:0xf bank_mask:0xf\n" \
  "v_mov_b32 v11, v10\n" \
  "v_mfma_f32_16x16x32_bf16 v[16:19], v[52:55], v[44:47], 0\n" \
  "s_setprio 3\n" \
  "s_barrier\n" \
  "ds_read_b64 v[22:23], v7 offset:0\n" \
  "s_nop 0\n" \
  "v_permlane16_swap_b32 v10, v11\n" \
  "v_add_f32 v10, v10, v11\n" \
  "v_mov_b32 v11, v10\n" \
  "v_mov_b64 v[8:9], s[92:93]\n" \
  "s_nop 0\n" \
  "v_permlane32_swap_b32 v10, v11\n" \
  "v_add_f32 v10, v10, v11\n" \
  "v_rcp_f32 v10, v10\n" \
  "s_nop 0\n" \
  "v_mul_f32 v10, s84, v10\n" \
  "v_pk_mul_f32 v[20:21], v[20:21], v[10:11] op_sel_hi:[1,0]\n" \
  "v_pk_mul_f32 v[12:13], v[12:13], v[10:11] op_sel_hi:[1,0]\n" \
  "v_pk_mul_f32 v[14:15], v[14:15], v[10:11] op_sel_hi:[1,0]\n" \
  "v_cvt_pk_bf16_f32 v40, v20, v21\n" \
  "v_pk_mul_f32 v[16:17], v[16:17], v[10:11] op_sel_hi:[1,0]\n" \
  "v_cvt_pk_bf16_f32 v43, v20, v21\n" \
  "v_pk_mul_f32 v[18:19], v[18:19], v[10:11] op_sel_hi:[1,0]\n" \
  "s_waitcnt lgkmcnt(0)\n" \
  "v_mfma_f32_16x16x16_bf16 v[12:15], v[22:23], v[40:41], v[12:15]\n" \
  "v_mfma_f32_16x16x16_bf16 v[16:19], v[22:23], v[42:43], v[16:19]\n" \
  "v_mov_b64 v[10:11], s[92:93]\n" \
  "ds_read_b128 v[48:51], v6 offset:10240\n" \
  "ds_read_b128 v[52:55], v6 offset:11264\n" \
  "v_mov_b64 v[20:21], s[92:93]\n" \
  "v_mov_b64 v[22:23], s[92:93]\n" \
  "s_nop 1\n" \
  "v_cvt_pk_bf16_f32 v40, v12, v13\n" \
  "v_cvt_pk_bf16_f32 v41, v14, v15\n" \
  "v_cvt_pk_bf16_f32 v42, v16, v17\n" \
  "ds_write_b64 v4, v[40:41]\n" \
  "v_cvt_pk_bf16_f32 v43, v18, v19\n" \
  "ds_write_b64 v4, v[42:43] offset:1024\n" \
  "v_mov_b64 v[12:13], s[92:93]\n" \
  "v_mov_b64 v[14:15], s[92:93]\n" \
  "v_mov_b64 v[16:17], s[92:93]\n" \
  "v_mov_b64 v[18:19], s[92:93]\n" \
  "s_setprio 0\n" \
  "s_waitcnt lgkmcnt(0)\n" \
  "s_barrier\n" \
  "ds_read_b128 v[40:43], v1 offset:8192\n" \
  "ds_read_b128 v[44:47], v1 offset:9216\n" \
  "s_waitcnt lgkmcnt(1)\n" \
  "v_smfmac_f32_16x16x64_bf16 v[12:15], v[40:43], v[56:63], v0\n" \
  "ds_read_b128 v[56:59], v6 offset:4096\n" \
  "ds_read_b128 v[60:63], v6 offset:5120\n" \
  "v_smfmac_f32_16x16x64_bf16 v[16:19], v[40:43], v[24:31], v0\n" \
  "ds_read_b128 v[24:27], v6 offset:6144\n" \
  "ds_read_b128 v[28:31], v6 offset:7168\n" \
  "s_waitcnt lgkmcnt(4)\n" \
  "v_smfmac_f32_16x16x64_bf16 v[12:15], v[44:47], v[32:39], v0\n" \
  "ds_read_b128 v[32:35], v6 offset:12288\n" \
  "ds_read_b128 v[36:39], v6 offset:13312\n" \
  "v_smfmac_f32_16x16x64_bf16 v[16:19], v[44:47], v[48:55], v0\n" \
  "ds_read_b128 v[48:51], v6 offset:14336\n" \
  "ds_read_b128 v[52:55], v6 offset:15360\n" \
  "s_nop 5\n" \
  "v_permlane32_swap_b32 v12, v17\n" \
  "v_permlane32_swap_b32 v13, v16\n" \
  "v_permlane32_swap_b32 v14, v19\n" \
  "v_permlane32_swap_b32 v15, v18\n" \
  "v_pk_add_f32 v[12:13], v[12:13], v[16:17] op_sel:[0,1] op_sel_hi:[1,0]\n" \
  "v_pk_add_f32 v[14:15], v[14:15], v[18:19] op_sel:[0,1] op_sel_hi:[1,0]\n" \
  "buffer_atomic_add_x2 v[12:13], v5, s[40:43], s56 offen\n" \
  "buffer_atomic_add_x2 v[14:15], v5, s[40:43], s57 offen\n" \
  "s_waitcnt lgkmcnt(6)\n" \
  "v_smfmac_f32_16x16x64_bf16 v[20:23], v[40:43], v[56:63], v0\n" \
  "s_waitcnt lgkmcnt(4)\n" \
  "v_smfmac_f32_16x16x64_bf16 v[8:11], v[40:43], v[24:31], v0\n" \
  "s_waitcnt lgkmcnt(2)\n" \
  "v_smfmac_f32_16x16x64_bf16 v[20:23], v[44:47], v[32:39], v0\n" \
  "s_waitcnt lgkmcnt(0)\n" \
  "v_smfmac_f32_16x16x64_bf16 v[8:11], v[44:47], v[48:55], v0\n" \
  "s_nop 7\n" \
  "v_permlane32_swap_b32 v20, v9\n" \
  "v_permlane32_swap_b32 v21, v8\n" \
  "v_permlane32_swap_b32 v22, v11\n" \
  "v_permlane32_swap_b32 v23, v10\n" \
  "v_pk_add_f32 v[20:21], v[20:21], v[8:9] op_sel:[0,1] op_sel_hi:[1,0]\n" \
  "v_pk_add_f32 v[22:23], v[22:23], v[10:11] op_sel:[0,1] op_sel_hi:[1,0]\n" \
  "buffer_atomic_add_x2 v[20:21], v5, s[40:43], s58 offen\n" \
  "buffer_atomic_add_x2 v[22:23], v5, s[40:43], s59 offen\n" \
  "s_memrealtime s[38:39]\n" \
  "v_mbcnt_lo_u32_b32 v48, -1, 0\n" \
  "v_mbcnt_hi_u32_b32 v48, -1, v48\n" \
  "s_add_u32 s88, s74, 157760\n" \
  "v_mov_b32 v59, s88\n" \
  "v_lshl_add_u32 v52, v48, 2, v59\n" \
  "ds_read_b32 v52, v52 offset:192\n" \
  "v_lshrrev_b32 v49, 3, v48\n" \
  "v_and_b32 v50, 7, v48\n" \
  "v_lshlrev_b32 v50, 4, v50\n" \
  "v_lshl_or_b32 v44, v49, 8, v50\n" \
  "s_mov_b32 s87, 0x40000\n" \
  "v_lshrrev_b32 v49, 2, v48\n" \
  "v_lshrrev_b32 v50, 4, v48\n" \
  "v_bitop3_b32 v50, v49, v50, 3 bitop3:0x28\n" \
  "v_lshl_add_u32 v8, v50, 3, s97\n" \
  "v_and_b32 v50, 3, v48\n" \
  "v_lshl_add_u32 v8, v50, 5, v8\n" \
  "v_and_b32 v49, 1, v49\n" \
  "v_lshl_add_u32 v8, v49, 8, v8\n" \
  "v_and_b32 v49, 8, v48\n" \
  "v_lshl_add_u32 v8, v49, 4, v8\n" \
  "v_lshrrev_b32 v49, 5, v48\n" \
  "v_add_u32 v10, s99, v1\n" \
  "s_lshr_b32 s88, s99, 2\n" \
  "s_add_u32 s88, s88, s74\n" \
  "s_add_u32 s88, s88, 139264\n" \
  "v_lshl_add_u32 v7, v49, 10, s88\n" \
  "v_and_b32 v49, 31, v48\n" \
  "v_lshl_add_u32 v7, v49, 3, v7\n" \
  "v_mov_b64 v[12:13], 0\n" \
  "v_mov_b64 v[14:15], 0\n" \
  "v_mov_b64 v[16:17], 0\n" \
  "v_mov_b64 v[18:19], 0\n" \
  "v_mov_b64 v[20:21], 0\n" \
  "v_mov_b64 v[22:23], 0\n" \
  "v_mov_b32 v34, 0xc9b26ca0\n" \
  "v_mov_b32 v35, 0xc9b26c86\n" \
  "v_and_b32 v50, 3, v48\n" \
  "v_lshl_add_u32 v40, v50, 2, s98\n" \
  "v_lshrrev_b32 v50, 4, v48\n" \
  "v_mad_u32_u24 v40, v50, s95, v40\n" \
  "v_bfe_u32 v49, v48, 3, 1\n" \
  "v_lshl_add_u32 v40, v49, 6, v40\n" \
  "v_bfe_u32 v49, v48, 2, 1\n" \
  "v_lshl_add_u32 v40, v49, 7, v40\n" \
  "v_xor_b32 v41, 16, v40\n" \
  "v_xor_b32 v42, 32, v40\n" \
  "v_xor_b32 v43, 48, v40\n" \
  "s_add_i32 s80, s86, 0x35000000\n" \
  "v_mov_b32 v11, 0\n" \
  "s_mov_b32 s89, s60\n" \
  "s_bitcmp1_b32 s60, 7\n" \
  "s_cselect_b32 s88, 0x10000, 0\n" \
  "s_bitset1_b32 s88, 7\n" \
  "s_xor_b32 s56, s56, s88\n" \
  "s_xor_b32 s57, s57, s88\n" \
  "s_xor_b32 s58, s58, s88\n" \
  "s_xor_b32 s59, s59, s88\n" \
  "s_xor_b32 s60, s60, s88\n" \
  "s_sub_u32 s71, s71, 1\n" \
  "s_waitcnt lgkmcnt(0)\n" \
  "s_sub_u32 s39, s38, 150\n" \
  "v_subrev_u32 v52, s39, v52\n" \
  "v_cmp_lt_u32 vcc, 0x1000, v52\n" \
  "s_cmp_eq_u64 vcc, 0\n" \
  "s_cbranch_scc1 .LCA_OKM_%=\n" \
  ".LCA_AGN_%=:\n" \
  "buffer_load_dword v52, v5, s[0:3], s66 offen sc1\n" \
  "s_sub_u32 s87, s87, 1\n" \
  "s_cbranch_scc1 .LCA_END_%=\n" \
  "s_waitcnt vmcnt(0)\n" \
  "v_cmp_eq_u32 vcc, 0, v52\n" \
  "v_subrev_u32 v52, s39, v52\n" \
  "s_cmp_eq_u64 vcc, 0\n" \
  "s_cbranch_scc1 .LCA_OKM_%=\n" \
  "s_sleep 4\n" \
  "s_branch .LCA_AGN_%=\n" \
  ".LCA_OKM_%=:\n" \
  "s_nop 1\n" \
  "v_max_i32_dpp v52, v52, v52 row_ror:1 row_mask:0xf bank_mask:0xf\n" \
  "s_nop 1\n" \
  "v_max_i32_dpp v52, v52, v52 row_ror:2 row_mask:0xf bank_mask:0xf\n" \
  "s_nop 1\n" \
  "v_max_i32_dpp v52, v52, v52 row_ror:4 row_mask:0xf bank_mask:0xf\n" \
  "s_nop 1\n" \
  "v_max_i32_dpp v52, v52, v52 row_ror:8 row_mask:0xf bank_mask:0xf\n" \
  "s_movk_i32 vcc_lo, 150\n" \
  "v_readlane_b32 s88, v52, 0\n" \
  "s_max_i32 vcc_lo, vcc_lo, s88\n" \
  "v_readlane_b32 s88, v52, 16\n" \
  "s_max_i32 vcc_lo, vcc_lo, s88\n" \
  "v_readlane_b32 s88, v52, 32\n" \
  "s_max_i32 vcc_lo, vcc_lo, s88\n" \
  "v_readlane_b32 s88, v52, 48\n" \
  "s_max_i32 vcc_lo, vcc_lo, s88\n" \
  "s_add_u32 s66, s66, 1024\n" \
  "s_add_u32 vcc_lo, vcc_lo, s39\n" \
  "s_add_u32 vcc_lo, vcc_lo, 21\n" \
  ".LCA_TW_%=:\n" \
  "s_memrealtime s[38:39]\n" \
  "s_waitcnt lgkmcnt(0)\n" \
  "s_sub_u32 s38, s38, vcc_lo\n" \
  "s_cmp_lt_i32 s38, 0\n" \
  "s_cbranch_scc1 .LCA_TW_%=\n" \
  "buffer_load_dwordx4 v[24:27], v44, s[40:43], s89 offen sc1\n" \
  "buffer_load_dwordx4 v[28:31], v44, s[40:43], s89 offen offset:2048 sc1\n" \
  ".LCA_SEE_%=:\n" \
  "s_waitcnt vmcnt(1)\n" \
  "v_cvt_f32_i32 v24, v24\n" \
  "v_cvt_f32_i32 v25, v25\n" \
  "v_cvt_f32_i32 v26, v26\n" \
  "v_cvt_f32_i32 v27, v27\n" \
  "v_max_f32 v45, |v24|, |v26|\n" \
  "v_pk_mul_f32 v[24:25], v[24:25], s[80:81] op_sel_hi:[1,0]\n" \
  "v_pk_mul_f32 v[26:27], v[26:27], s[80:81] op_sel_hi:[1,0]\n" \
  "s_nop 0\n" \
  "v_cvt_pk_bf16_f32 v32, v24, v26\n" \
  "v_cvt_pk_bf16_f32 v33, v25, v27\n" \
  "ds_write_b32 v40, v32\n" \
  "ds_write_b32 v41, v33\n" \
  "s_waitcnt vmcnt(0)\n" \
  "v_cvt_f32_i32 v28, v28\n" \
  "v_cvt_f32_i32 v30, v30\n" \
  "v_cvt_f32_i32 v29, v29\n" \
  "v_max3_f32 v45, v45, |v28|, |v30|\n" \
  "v_cvt_f32_i32 v31, v31\n" \
  "v_cmp_lt_f32 vcc, s91, v45\n" \
  "v_pk_mul_f32 v[28:29], v[28:29], s[80:81] op_sel_hi:[1,0]\n" \
  "v_pk_mul_f32 v[30:31], v[30:31], s[80:81] op_sel_hi:[1,0]\n" \
  "s_cmp_eq_u64 vcc, 0\n" \
  "s_cbranch_scc0 .LCA_POLL_%=\n" \
  "v_cvt_pk_bf16_f32 v36, v28, v30\n" \
  "v_cvt_pk_bf16_f32 v37, v29, v31\n" \
  "ds_write_b32 v42, v36\n" \
  "ds_write_b32 v43, v37\n" \
  "v_dot2c_f32_bf16 v11, v32, v32\n" \
  "v_dot2c_f32_bf16 v11, v33, v33\n" \
  "v_dot2c_f32_bf16 v11, v36, v36\n" \
  "v_dot2c_f32_bf16 v11, v37, v37\n" \
  "s_nop 1\n" \
  "s_nop 0\n" \
  "v_cmp_lt_f32 vcc, 4.0, v11\n" \
  "s_cbranch_vccz .LCA_TOP_%=\n" \
  "s_mov_b64 exec, vcc\n" \
  "ds_max_u32 v59, v11\n" \
  "s_mov_b64 exec, -1\n" \
  "s_branch .LCA_TOP_%=\n" \
  ".LCA_POLL_%=:\n" \
  "buffer_load_dwordx4 v[24:27], v44, s[40:43], s89 offen sc1\n" \
  "buffer_load_dwordx4 v[28:31], v44, s[40:43], s89 offen offset:2048 sc1\n" \
  "s_sub_u32 s87, s87, 1\n" \
  "s_cbranch_scc1 .LCA_END_%=\n" \
  "s_branch .LCA_SEE_%=\n" \
  ".LCA_LAST_%=:\n" \
  ".LCA_END_%=:\n" \
  "s_endpgm\n"
#define CHAIN_ASM_VO \
  "s_mov_b32 s95, 272\n" \
  "s_mov_b32 s91, 0x4c7c2000\n" \
  "s_mov_b32 s85, 0xc9b26c9b\n" \
  "s_mov_b32 s92, 0xc9326c9b\n" \
  "s_mov_b32 s93, 0xc9326c9b\n" \
  "s_mov_b32 s78, 0x3e0293ee\n" \
  "s_mov_b32 s76, 0x33333333\n" \
  "s_mov_b32 s77, 0x33333333\n" \
  "s_lshl_b32 s98, s73, 10\n" \
  "s_add_u32 s98, s98, s74\n" \
  "s_add_u32 s98, s98, 131072\n" \
  "s_lshl_b32 s97, s73, 9\n" \
  "s_add_u32 s97, s97, s74\n" \
  "s_add_u32 s97, s97, 158724\n" \
  "s_lshl_b32 s99, s73, 11\n" \
  "s_add_u32 s99, s99, s74\n" \
  "s_add_u32 s99, s99, 133120\n" \
  "s_lshl_b32 s94, s73, 4\n" \
  "s_add_u32 s94, s94, s74\n" \
  "s_add_u32 s94, s94, 157760\n" \
  "v_mbcnt_lo_u32_b32 v48, -1, 0\n" \
  "v_mbcnt_hi_u32_b32 v48, -1, v48\n" \
  "s_add_u32 s88, s74, 157760\n" \
  "v_mov_b32 v59, s88\n" \
  "s_sub_u32 s88, s73, 4\n" \
  "v_lshrrev_b32 v49, 2, v48\n" \
  "v_lshrrev_b32 v50, 4, v48\n" \
  "v_bitop3_b32 v50, v49, v50, 3 bitop3:0x28\n" \
  "v_lshl_add_u32 v4, v50, 3, s97\n" \
  "v_and_b32 v50, 3, v48\n" \
  "v_lshl_add_u32 v4, v50, 5, v4\n" \
  "v_and_b32 v49, 1, v49\n" \
  "v_lshl_add_u32 v4, v49, 8, v4\n" \
  "v_and_b32 v49, 8, v48\n" \
  "v_lshl_add_u32 v4, v49, 4, v4\n" \
  "v_and_b32 v51, 15, v48\n" \
  "v_bfe_u32 v49, v48, 4, 1\n" \
  "v_lshrrev_b32 v50, 5, v48\n" \
  "s_lshl_b32 s89, s88, 10\n" \
  "s_add_u32 s89, s89, s74\n" \
  "s_add_u32 s89, s89, 158720\n" \
  "v_lshrrev_b32 v9, 3, v51\n" \
  "v_mul_u32_u24 v6, 41, v9\n" \
  "v_xor_b32 v6, v6, v51\n" \
  "v_lshl_add_u32 v6, v6, 3, s89\n" \
  "v_lshl_add_u32 v6, v49, 6, v6\n" \
  "v_lshl_add_u32 v6, v50, 9, v6\n" \
  "v_xor_b32 v9, 0x90, v6\n" \
  "v_lshlrev_b32 v8, 4, v49\n" \
  "v_lshl_add_u32 v8, v51, 5, v8\n" \
  "v_lshl_add_u32 v8, v50, 3, v8\n" \
  "s_lshl_b32 s89, s88, 8\n" \
  "s_add_u32 s89, s89, s74\n" \
  "s_add_u32 s89, s89, 139264\n" \
  "v_and_b32 v49, 31, v48\n" \
  "v_lshl_add_u32 v5, v49, 3, s89\n" \
  "v_lshl_add_u32 v5, v50, 10, v5\n" \
  "v_lshrrev_b32 v49, 2, v51\n" \
  "v_and_b32 v50, 5, v51\n" \
  "v_and_or_b32 v49, v49, 2, v50\n" \
  "v_and_b32 v50, 48, v48\n" \
  "v_lshlrev_b32 v7, 3, v50\n" \
  "v_lshl_add_u32 v7, v49, 4, v7\n" \
  "v_bitop3_b32 v49, v51, 2, 2 bitop3:0x0c\n" \
  "v_lshl_add_u32 v7, v49, 29, v7\n" \
  "v_mov_b64 v[12:13], 0\n" \
  "v_mov_b64 v[14:15], 0\n" \
  "v_mov_b64 v[16:17], 0\n" \
  "v_mov_b64 v[18:19], 0\n" \
  "v_mov_b64 v[20:21], 0\n" \
  "v_mov_b64 v[22:23], 0\n" \
  "v_lshlrev_b32 v10, 4, v48\n" \
  "s_add_u32 s78, s74, 157952\n" \
  "v_lshl_add_u32 v52, v48, 2, v59\n" \
  "ds_write_b32 v52, v12 offset:192\n" \
  "s_movk_i32 s79, 105\n" \
  "s_movk_i32 s96, 25\n" \
  "s_mov_b32 s87, 0x40000\n" \
  "v_subrev_u32 v30, 64, v59\n" \
  "v_add_u32 v17, 0x10000, v28\n" \
  "v_xor_b32 v18, 128, v28\n" \
  "v_xor_b32 v19, 128, v17\n" \
  "ds_write_b32 v30, v12 offset:0\n" \
  "ds_write_b32 v30, v12 offset:32\n" \
  "ds_write_b32 v30, v12 offset:64\n" \
  "v_xor_b32 v20, 0, v29\n" \
  "v_xor_b32 v21, 32, v29\n" \
  "v_xor_b32 v22, 64, v29\n" \
  "v_xor_b32 v23, 96, v29\n" \
  "s_waitcnt vmcnt(12) lgkmcnt(0)\n" \
  "s_barrier\n" \
  "ds_read_b64_tr_b16 v[96:97], v20 offset:0\n" \
  "ds_read_b64_tr_b16 v[98:99], v20 offset:2048\n" \
  "ds_read_b64_tr_b16 v[104:105], v21 offset:0\n" \
  "ds_read_b64_tr_b16 v[106:107], v21 offset:2048\n" \
  "ds_read_b64_tr_b16 v[112:113], v22 offset:0\n" \
  "ds_read_b64_tr_b16 v[114:115], v22 offset:2048\n" \
  "ds_read_b64_tr_b16 v[120:121], v23 offset:0\n" \
  "ds_read_b64_tr_b16 v[122:123], v23 offset:2048\n" \
  "s_lshl_b32 s88, s73, 10\n" \
  "v_add_u32 v48, s88, v1\n" \
  "ds_read_b128 v[52:55], v48 offset:0\n" \
  "s_waitcnt lgkmcnt(0)\n" \
  "v_mov_b32 v11, 0\n" \
  "v_dot2c_f32_bf16 v11, v52, v52\n" \
  "v_dot2c_f32_bf16 v11, v53, v53\n" \
  "v_dot2c_f32_bf16 v11, v54, v54\n" \
  "v_dot2c_f32_bf16 v11, v55, v55\n" \
  "s_nop 0\n" \
  "s_nop 1\n" \
  "v_max_u32_dpp v11, v11, v11 row_ror:1 row_mask:0xf bank_mask:0xf\n" \
  "s_nop 1\n" \
  "v_max_u32_dpp v11, v11, v11 row_ror:2 row_mask:0xf bank_mask:0xf\n" \
  "s_nop 1\n" \
  "v_max_u32_dpp v11, v11, v11 row_ror:4 row_mask:0xf bank_mask:0xf\n" \
  "s_nop 1\n" \
  "v_max_u32_dpp v11, v11, v11 row_ror:8 row_mask:0xf bank_mask:0xf\n" \
  "v_mov_b32 v31, v11\n" \
  "s_nop 1\n" \
  "v_permlane16_swap_b32 v11, v31\n" \
  "v_max_u32 v11, v11, v31\n" \
  "v_mov_b32 v31, v11\n" \
  "s_nop 1\n" \
  "v_permlane32_swap_b32 v11, v31\n" \
  "v_max_u32 v11, v11, v31\n" \
  "s_mov_b64 exec, 1\n" \
  "ds_max_u32 v59, v11\n" \
  "s_mov_b64 exec, -1\n" \
  "s_waitcnt vmcnt(8) lgkmcnt(0)\n" \
  "s_barrier\n" \
  "ds_read_b64_tr_b16 v[100:101], v20 offset:4096\n" \
  "ds_read_b64_tr_b16 v[102:103], v20 offset:6144\n" \
  "ds_read_b64_tr_b16 v[108:109], v21 offset:4096\n" \
  "ds_read_b64_tr_b16 v[110:111], v21 offset:6144\n" \
  "ds_read_b64_tr_b16 v[116:117], v22 offset:4096\n" \
  "ds_read_b64_tr_b16 v[118:119], v22 offset:6144\n" \
  "ds_read_b64_tr_b16 v[124:125], v23 offset:4096\n" \
  "ds_read_b64_tr_b16 v[126:127], v23 offset:6144\n" \
  "s_add_u32 m0, s19, 0\n" \
  "s_nop 0\n" \
  "global_load_lds_dwordx4 v24, s[4:5] offset:0\n" \
  "global_load_lds_dwordx4 v25, s[4:5] offset:1024\n" \
  "global_load_lds_dwordx4 v26, s[4:5] offset:2048\n" \
  "global_load_lds_dwordx4 v27, s[4:5] offset:3072\n" \
  "s_add_u32 s4, s4, 0x100000\n" \
  "s_addc_u32 s5, s5, 0\n" \
  "v_mfma_f32_16x16x32_bf16 v[64:67], v[96:99], v[96:99], 0\n" \
  "v_mfma_f32_16x16x32_bf16 v[68:71], v[104:107], v[104:107], 0\n" \
  "v_mfma_f32_16x16x32_bf16 v[72:75], v[112:115], v[112:115], 0\n" \
  "v_mfma_f32_16x16x32_bf16 v[76:79], v[120:123], v[120:123], 0\n" \
  "s_waitcnt vmcnt(8) lgkmcnt(0)\n" \
  "s_barrier\n" \
  "ds_read_b64_tr_b16 v[32:33], v20 offset:8192\n" \
  "ds_read_b64_tr_b16 v[34:35], v20 offset:10240\n" \
  "ds_read_b64_tr_b16 v[40:41], v21 offset:8192\n" \
  "ds_read_b64_tr_b16 v[42:43], v21 offset:10240\n" \
  "ds_read_b64_tr_b16 v[48:49], v22 offset:8192\n" \
  "ds_read_b64_tr_b16 v[50:51], v22 offset:10240\n" \
  "ds_read_b64_tr_b16 v[56:57], v23 offset:8192\n" \
  "ds_read_b64_tr_b16 v[58:59], v23 offset:10240\n" \
  "s_add_u32 m0, s19, 4096\n" \
  "s_nop 0\n" \
  "global_load_lds_dwordx4 v24, s[4:5] offset:0\n" \
  "global_load_lds_dwordx4 v25, s[4:5] offset:1024\n" \
  "global_load_lds_dwordx4 v26, s[4:5] offset:2048\n" \
  "global_load_lds_dwordx4 v27, s[4:5] offset:3072\n" \
  "s_add_u32 s4, s4, 0x100000\n" \
  "s_addc_u32 s5, s5, 0\n" \
  "v_mfma_f32_16x16x32_bf16 v[64:67], v[100:103], v[100:103], v[64:67]\n" \
  "v_mfma_f32_16x16x32_bf16 v[68:71], v[108:111], v[108:111], v[68:71]\n" \
  "v_mfma_f32_16x16x32_bf16 v[72:75], v[116:119], v[116:119], v[72:75]\n" \
  "v_mfma_f32_16x16x32_bf16 v[76:79], v[124:127], v[124:127], v[76:79]\n" \
  "s_waitcnt vmcnt(8) lgkmcnt(0)\n" \
  "s_barrier\n" \
  "ds_read_b64_tr_b16 v[36:37], v20 offset:12288\n" \
  "ds_read_b64_tr_b16 v[38:39], v20 offset:14336\n" \
  "ds_read_b64_tr_b16 v[44:45], v21 offset:12288\n" \
  "ds_read_b64_tr_b16 v[46:47], v21 offset:14336\n" \
  "ds_read_b64_tr_b16 v[52:53], v22 offset:12288\n" \
  "ds_read_b64_tr_b16 v[54:55], v22 offset:14336\n" \
  "ds_read_b64_tr_b16 v[60:61], v23 offset:12288\n" \
  "ds_read_b64_tr_b16 v[62:63], v23 offset:14336\n" \
  "s_add_u32 m0, s19, 8192\n" \
  "s_nop 0\n" \
  "global_load_lds_dwordx4 v24, s[4:5] offset:0\n" \
  "global_load_lds_dwordx4 v25, s[4:5] offset:1024\n" \
  "global_load_lds_dwordx4 v26, s[4:5] offset:2048\n" \
  "global_load_lds_dwordx4 v27, s[4:5] offset:3072\n" \
  "s_add_u32 s4, s4, 0x100000\n" \
  "s_addc_u32 s5, s5, 0\n" \
  "v_mfma_f32_16x16x32_bf16 v[64:67], v[32:35], v[32:35], v[64:67]\n" \
  "v_mfma_f32_16x16x32_bf16 v[68:71], v[40:43], v[40:43], v[68:71]\n" \
  "v_mfma_f32_16x16x32_bf16 v[72:75], v[48:51], v[48:51], v[72:75]\n" \
  "v_mfma_f32_16x16x32_bf16 v[76:79], v[56:59], v[56:59], v[76:79]\n" \
  "s_waitcnt vmcnt(8) lgkmcnt(0)\n" \
  "s_barrier\n" \
  "ds_read_b64_tr_b16 v[128:129], v18 offset:0\n" \
  "ds_read_b64_tr_b16 v[130:131], v18 offset:512\n" \
  "ds_read_b64_tr_b16 v[132:133], v18 offset:32768\n" \
  "ds_read_b64_tr_b16 v[134:135], v18 offset:33280\n" \
  "ds_read_b64_tr_b16 v[136:137], v19 offset:0\n" \
  "ds_read_b64_tr_b16 v[138:139], v19 offset:512\n" \
  "ds_read_b64_tr_b16 v[140:141], v19 offset:32768\n" \
  "ds_read_b64_tr_b16 v[142:143], v19 offset:33280\n" \
  "ds_read_b64_tr_b16 v[192:193], v28 offset:0\n" \
  "ds_read_b64_tr_b16 v[194:195], v28 offset:512\n" \
  "ds_read_b64_tr_b16 v[196:197], v28 offset:32768\n" \
  "ds_read_b64_tr_b16 v[198:199], v28 offset:33280\n" \
  "ds_read_b64_tr_b16 v[200:201], v17 offset:0\n" \
  "ds_read_b64_tr_b16 v[202:203], v17 offset:512\n" \
  "ds_read_b64_tr_b16 v[204:205], v17 offset:32768\n" \
  "ds_read_b64_tr_b16 v[206:207], v17 offset:33280\n" \
  "s_add_u32 m0, s19, 12288\n" \
  "s_nop 0\n" \
  "global_load_lds_dwordx4 v24, s[4:5] offset:0\n" \
  "global_load_lds_dwordx4 v25, s[4:5] offset:1024\n" \
  "global_load_lds_dwordx4 v26, s[4:5] offset:2048\n" \
  "global_load_lds_dwordx4 v27, s[4:5] offset:3072\n" \
  "s_add_u32 s4, s4, 0x100000\n" \
  "s_addc_u32 s5, s5, 0\n" \
  "v_mfma_f32_16x16x32_bf16 v[64:67], v[36:39], v[36:39], v[64:67]\n" \
  "v_mfma_f32_16x16x32_bf16 v[68:71], v[44:47], v[44:47], v[68:71]\n" \
  "v_mfma_f32_16x16x32_bf16 v[72:75], v[52:55], v[52:55], v[72:75]\n" \
  "v_mfma_f32_16x16x32_bf16 v[76:79], v[60:63], v[60:63], v[76:79]\n" \
  "s_nop 7\n" \
  "v_max3_f32 v13, v64, v65, v66\n" \
  "v_max3_f32 v13, v13, v67, v68\n" \
  "v_max3_f32 v13, v13, v69, v70\n" \
  "v_max3_f32 v13, v13, v71, v72\n" \
  "v_max3_f32 v13, v13, v73, v74\n" \
  "v_max3_f32 v13, v13, v75, v76\n" \
  "v_max3_f32 v13, v13, v77, v78\n" \
  "v_max_f32 v13, v13, v79\n" \
  "s_nop 1\n" \
  "v_max_f32_dpp v13, v13, v13 row_ror:1 row_mask:0xf bank_mask:0xf\n" \
  "s_nop 1\n" \
  "v_max_f32_dpp v13, v13, v13 row_ror:2 row_mask:0xf bank_mask:0xf\n" \
  "s_nop 1\n" \
  "v_max_f32_dpp v13, v13, v13 row_ror:4 row_mask:0xf bank_mask:0xf\n" \
  "s_nop 1\n" \
  "v_max_f32_dpp v13, v13, v13 row_ror:8 row_mask:0xf bank_mask:0xf\n" \
  "v_mov_b32 v31, v13\n" \
  "s_nop 1\n" \
  "v_permlane16_swap_b32 v13, v31\n" \
  "v_max_f32 v13, v13, v31\n" \
  "v_mov_b32 v31, v13\n" \
  "s_nop 1\n" \
  "v_permlane32_swap_b32 v13, v31\n" \
  "v_max_f32 v13, v13, v31\n" \
  "s_mov_b64 exec, 1\n" \
  "ds_max_u32 v30, v13 offset:32\n" \
  "s_mov_b64 exec, -1\n" \
  "s_waitcnt vmcnt(8) lgkmcnt(0)\n" \
  "s_barrier\n" \
  "ds_read_b64_tr_b16 v[144:145], v18 offset:4096\n" \
  "ds_read_b64_tr_b16 v[146:147], v18 offset:4608\n" \
  "ds_read_b64_tr_b16 v[148:149], v18 offset:36864\n" \
  "ds_read_b64_tr_b16 v[150:151], v18 offset:37376\n" \
  "ds_read_b64_tr_b16 v[152:153], v19 offset:4096\n" \
  "ds_read_b64_tr_b16 v[154:155], v19 offset:4608\n" \
  "ds_read_b64_tr_b16 v[156:157], v19 offset:36864\n" \
  "ds_read_b64_tr_b16 v[158:159], v19 offset:37376\n" \
  "ds_read_b64_tr_b16 v[208:209], v28 offset:4096\n" \
  "ds_read_b64_tr_b16 v[210:211], v28 offset:4608\n" \
  "ds_read_b64_tr_b16 v[212:213], v28 offset:36864\n" \
  "ds_read_b64_tr_b16 v[214:215], v28 offset:37376\n" \
  "ds_read_b64_tr_b16 v[216:217], v17 offset:4096\n" \
  "ds_read_b64_tr_b16 v[218:219], v17 offset:4608\n" \
  "ds_read_b64_tr_b16 v[220:221], v17 offset:36864\n" \
  "ds_read_b64_tr_b16 v[222:223], v17 offset:37376\n" \
  "s_add_u32 m0, s19, 0\n" \
  "s_nop 0\n" \
  "global_load_lds_dwordx4 v24, s[6:7] offset:0\n" \
  "global_load_lds_dwordx4 v25, s[6:7] offset:1024\n" \
  "global_load_lds_dwordx4 v26, s[6:7] offset:2048\n" \
  "global_load_lds_dwordx4 v27, s[6:7] offset:3072\n" \
  "s_add_u32 s6, s6, 0x100000\n" \
  "s_addc_u32 s7, s7, 0\n" \
  "v_mfma_f32_16x16x32_bf16 v[64:67], v[128:131], v[128:131], 0\n" \
  "v_mfma_f32_16x16x32_bf16 v[68:71], v[192:195], v[192:195], 0\n" \
  "v_mfma_f32_16x16x32_bf16 v[64:67], v[132:135], v[132:135], v[64:67]\n" \
  "v_mfma_f32_16x16x32_bf16 v[68:71], v[196:199], v[196:199], v[68:71]\n" \
  "v_mfma_f32_16x16x32_bf16 v[64:67], v[136:139], v[136:139], v[64:67]\n" \
  "v_mfma_f32_16x16x32_bf16 v[68:71], v[200:203], v[200:203], v[68:71]\n" \
  "v_mfma_f32_16x16x32_bf16 v[64:67], v[140:143], v[140:143], v[64:67]\n" \
  "v_mfma_f32_16x16x32_bf16 v[68:71], v[204:207], v[204:207], v[68:71]\n" \
  "s_waitcnt vmcnt(8) lgkmcnt(0)\n" \
  "s_barrier\n" \
  "ds_read_b64_tr_b16 v[160:161], v18 offset:8192\n" \
  "ds_read_b64_tr_b16 v[162:163], v18 offset:8704\n" \
  "ds_read_b64_tr_b16 v[164:165], v18 offset:40960\n" \
  "ds_read_b64_tr_b16 v[166:167], v18 offset:41472\n" \
  "ds_read_b64_tr_b16 v[168:169], v19 offset:8192\n" \
  "ds_read_b64_tr_b16 v[170:171], v19 offset:8704\n" \
  "ds_read_b64_tr_b16 v[172:173], v19 offset:40960\n" \
  "ds_read_b64_tr_b16 v[174:175], v19 offset:41472\n" \
  "ds_read_b64_tr_b16 v[224:225], v28 offset:8192\n" \
  "ds_read_b64_tr_b16 v[226:227], v28 offset:8704\n" \
  "ds_read_b64_tr_b16 v[228:229], v28 offset:40960\n" \
  "ds_read_b64_tr_b16 v[230:231], v28 offset:41472\n" \
  "ds_read_b64_tr_b16 v[232:233], v17 offset:8192\n" \
  "ds_read_b64_tr_b16 v[234:235], v17 offset:8704\n" \
  "ds_read_b64_tr_b16 v[236:237], v17 offset:40960\n" \
  "ds_read_b64_tr_b16 v[238:239], v17 offset:41472\n" \
  "s_add_u32 m0, s19, 4096\n" \
  "s_nop 0\n" \
  "global_load_lds_dwordx4 v24, s[6:7] offset:0\n" \
  "global_load_lds_dwordx4 v25, s[6:7] offset:1024\n" \
  "global_load_lds_dwordx4 v26, s[6:7] offset:2048\n" \
  "global_load_lds_dwordx4 v27, s[6:7] offset:3072\n" \
  "s_add_u32 s6, s6, 0x100000\n" \
  "s_addc_u32 s7, s7, 0\n" \
  "v_mfma_f32_16x16x32_bf16 v[64:67], v[144:147], v[144:147], v[64:67]\n" \
  "v_mfma_f32_16x16x32_bf16 v[68:71], v[208:211], v[208:211], v[68:71]\n" \
  "v_mfma_f32_16x16x32_bf16 v[64:67], v[148:151], v[148:151], v[64:67]\n" \
  "v_mfma_f32_16x16x32_bf16 v[68:71], v[212:215], v[212:215], v[68:71]\n" \
  "v_mfma_f32_16x16x32_bf16 v[64:67], v[152:155], v[152:155], v[64:67]\n" \
  "v_mfma_f32_16x16x32_bf16 v[68:71], v[216:219], v[216:219], v[68:71]\n" \
  "v_mfma_f32_16x16x32_bf16 v[64:67], v[156:159], v[156:159], v[64:67]\n" \
  "v_mfma_f32_16x16x32_bf16 v[68:71], v[220:223], v[220:223], v[68:71]\n" \
  "s_waitcnt vmcnt(8) lgkmcnt(0)\n" \
  "s_barrier\n" \
  "ds_read_b64_tr_b16 v[176:177], v18 offset:12288\n" \
  "ds_read_b64_tr_b16 v[178:179], v18 offset:12800\n" \
  "ds_read_b64_tr_b16 v[180:181], v18 offset:45056\n" \
  "ds_read_b64_tr_b16 v[182:183], v18 offset:45568\n" \
  "ds_read_b64_tr_b16 v[184:185], v19 offset:12288\n" \
  "ds_read_b64_tr_b16 v[186:187], v19 offset:12800\n" \
  "ds_read_b64_tr_b16 v[188:189], v19 offset:45056\n" \
  "ds_read_b64_tr_b16 v[190:191], v19 offset:45568\n" \
  "ds_read_b64_tr_b16 v[240:241], v28 offset:12288\n" \
  "ds_read_b64_tr_b16 v[242:243], v28 offset:12800\n" \
  "ds_read_b64_tr_b16 v[244:245], v28 offset:45056\n" \
  "ds_read_b64_tr_b16 v[246:247], v28 offset:45568\n" \
  "ds_read_b64_tr_b16 v[248:249], v17 offset:12288\n" \
  "ds_read_b64_tr_b16 v[250:251], v17 offset:12800\n" \
  "ds_read_b64_tr_b16 v[252:253], v17 offset:45056\n" \
  "ds_read_b64_tr_b16 v[254:255], v17 offset:45568\n" \
  "s_add_u32 m0, s19, 8192\n" \
  "s_nop 0\n" \
  "global_load_lds_dwordx4 v24, s[6:7] offset:0\n" \
  "global_load_lds_dwordx4 v25, s[6:7] offset:1024\n" \
  "global_load_lds_dwordx4 v26, s[6:7] offset:2048\n" \
  "global_load_lds_dwordx4 v27, s[6:7] offset:3072\n" \
  "s_add_u32 s6, s6, 0x100000\n" \
  "s_addc_u32 s7, s7, 0\n" \
  "v_mfma_f32_16x16x32_bf16 v[64:67], v[160:163], v[160:163], v[64:67]\n" \
  "v_mfma_f32_16x16x32_bf16 v[68:71], v[224:227], v[224:227], v[68:71]\n" \
  "v_mfma_f32_16x16x32_bf16 v[64:67], v[164:167], v[164:167], v[64:67]\n" \
  "v_mfma_f32_16x16x32_bf16 v[68:71], v[228:231], v[228:231], v[68:71]\n" \
  "v_mfma_f32_16x16x32_bf16 v[64:67], v[168:171], v[168:171], v[64:67]\n" \
  "v_mfma_f32_16x16x32_bf16 v[68:71], v[232:235], v[232:235], v[68:71]\n" \
  "v_mfma_f32_16x16x32_bf16 v[64:67], v[172:175], v[172:175], v[64:67]\n" \
  "v_mfma_f32_16x16x32_bf16 v[68:71], v[236:239], v[236:239], v[68:71]\n" \
  "s_waitcnt vmcnt(8) lgkmcnt(0)\n" \
  "s_barrier\n" \
  "s_add_u32 m0, s19, 12288\n" \
  "s_nop 0\n" \
  "global_load_lds_dwordx4 v24, s[6:7] offset:0\n" \
  "global_load_lds_dwordx4 v25, s[6:7] offset:1024\n" \
  "global_load_lds_dwordx4 v26, s[6:7] offset:2048\n" \
  "global_load_lds_dwordx4 v27, s[6:7] offset:3072\n" \
  "s_add_u32 s6, s6, 0x100000\n" \
  "s_addc_u32 s7, s7, 0\n" \
  "v_mfma_f32_16x16x32_bf16 v[64:67], v[176:179], v[176:179], v[64:67]\n" \
  "v_mfma_f32_16x16x32_bf16 v[68:71], v[240:243], v[240:243], v[68:71]\n" \
  "v_mfma_f32_16x16x32_bf16 v[64:67], v[180:183], v[180:183], v[64:67]\n" \
  "v_mfma_f32_16x16x32_bf16 v[68:71], v[244:247], v[244:247], v[68:71]\n" \
  "v_mfma_f32_16x16x32_bf16 v[64:67], v[184:187], v[184:187], v[64:67]\n" \
  "v_mfma_f32_16x16x32_bf16 v[68:71], v[248:251], v[248:251], v[68:71]\n" \
  "v_mfma_f32_16x16x32_bf16 v[64:67], v[188:191], v[188:191], v[64:67]\n" \
  "v_mfma_f32_16x16x32_bf16 v[68:71], v[252:255], v[252:255], v[68:71]\n" \
  "s_nop 7\n" \
  "s_mov_b32 exec_lo, 0x100001\n" \
  "s_mov_b32 exec_hi, 0x10000100\n" \
  "v_add_f32 v12, v64, v68\n" \
  "s_mov_b32 exec_lo, 0x200002\n" \
  "s_mov_b32 exec_hi, 0x20000200\n" \
  "v_add_f32 v12, v65, v69\n" \
  "s_mov_b32 exec_lo, 0x400004\n" \
  "s_mov_b32 exec_hi, 0x40000400\n" \
  "v_add_f32 v12, v66, v70\n" \
  "s_mov_b32 exec_lo, 0x800008\n" \
  "s_mov_b32 exec_hi, 0x80000800\n" \
  "v_add_f32 v12, v67, v71\n" \
  "s_mov_b64 exec, -1\n" \
  "s_nop 1\n" \
  "v_add_f32_dpp v12, v12, v12 row_ror:1 row_mask:0xf bank_mask:0xf\n" \
  "s_nop 1\n" \
  "v_add_f32_dpp v12, v12, v12 row_ror:2 row_mask:0xf bank_mask:0xf\n" \
  "s_nop 1\n" \
  "v_add_f32_dpp v12, v12, v12 row_ror:4 row_mask:0xf bank_mask:0xf\n" \
  "s_nop 1\n" \
  "v_add_f32_dpp v12, v12, v12 row_ror:8 row_mask:0xf bank_mask:0xf\n" \
  "v_mov_b32 v31, v12\n" \
  "s_nop 1\n" \
  "v_permlane16_swap_b32 v12, v31\n" \
  "v_add_f32 v12, v12, v31\n" \
  "v_mov_b32 v31, v12\n" \
  "s_nop 1\n" \
  "v_permlane32_swap_b32 v12, v31\n" \
  "v_add_f32 v12, v12, v31\n" \
  "v_lshl_add_u32 v31, s73, 2, v30\n" \
  "s_mov_b64 exec, 1\n" \
  "ds_write_b32 v31, v12\n" \
  "s_mov_b64 exec, -1\n" \
  "s_waitcnt vmcnt(8) lgkmcnt(0)\n" \
  "s_barrier\n" \
  "s_add_u32 m0, s19, 0\n" \
  "s_nop 0\n" \
  "global_load_lds_dwordx4 v24, s[8:9] offset:0\n" \
  "global_load_lds_dwordx4 v25, s[8:9] offset:1024\n" \
  "global_load_lds_dwordx4 v26, s[8:9] offset:2048\n" \
  "global_load_lds_dwordx4 v27, s[8:9] offset:3072\n" \
  "s_add_u32 s8, s8, 0x100000\n" \
  "s_addc_u32 s9, s9, 0\n" \
  "ds_read_b128 v[20:23], v30 offset:16\n" \
  "ds_read_b32 v31, v30 offset:32\n" \
  "v_mbcnt_lo_u32_b32 v16, -1, 0\n" \
  "v_mbcnt_hi_u32_b32 v16, -1, v16\n" \
  "v_lshlrev_b32 v15, 8, v16\n" \
  "s_waitcnt lgkmcnt(0)\n" \
  "v_add_f32 v20, v20, v21\n" \
  "v_add_f32 v22, v22, v23\n" \
  "v_add_f32 v20, v20, v22\n" \
  "v_mul_f32 v31, v20, v31\n" \
  "v_mul_f32 v31, 0x3f8147ae, v31\n" \
  "v_mov_b32 v12, s10\n" \
  "v_mov_b32 v13, 0xffff\n" \
  "v_bfi_b32 v13, v13, s11, v31\n" \
  "s_mov_b32 exec_hi, 0\n" \
  "s_mov_b32 exec_lo, s18\n" \
  "global_store_dwordx2 v15, v[12:13], s[16:17] sc1\n" \
  "s_mov_b64 exec, -1\n" \
  "v_lshlrev_b32 v16, 3, v16\n" \
  "s_waitcnt vmcnt(8) lgkmcnt(0)\n" \
  "s_barrier\n" \
  "s_add_u32 m0, s19, 4096\n" \
  "s_nop 0\n" \
  "global_load_lds_dwordx4 v24, s[8:9] offset:0\n" \
  "global_load_lds_dwordx4 v25, s[8:9] offset:1024\n" \
  "global_load_lds_dwordx4 v26, s[8:9] offset:2048\n" \
  "global_load_lds_dwordx4 v27, s[8:9] offset:3072\n" \
  "s_add_u32 s8, s8, 0x100000\n" \
  "s_addc_u32 s9, s9, 0\n" \
  "global_load_dwordx2 v[20:21], v16, s[12:13] sc1\n" \
  "s_waitcnt vmcnt(9) lgkmcnt(0)\n" \
  "s_barrier\n" \
  "s_add_u32 m0, s19, 8192\n" \
  "s_nop 0\n" \
  "global_load_lds_dwordx4 v24, s[8:9] offset:0\n" \
  "global_load_lds_dwordx4 v25, s[8:9] offset:1024\n" \
  "global_load_lds_dwordx4 v26, s[8:9] offset:2048\n" \
  "global_load_lds_dwordx4 v27, s[8:9] offset:3072\n" \
  "s_add_u32 s8, s8, 0x100000\n" \
  "s_addc_u32 s9, s9, 0\n" \
  "s_waitcnt vmcnt(9) lgkmcnt(0)\n" \
  "s_barrier\n" \
  "ds_read_b64_tr_b16 v[64:65], v28 offset:0\n" \
  "ds_read_b64_tr_b16 v[66:67], v28 offset:512\n" \
  "ds_read_b64_tr_b16 v[68:69], v28 offset:32768\n" \
  "ds_read_b64_tr_b16 v[70:71], v28 offset:33280\n" \
  "ds_read_b64_tr_b16 v[72:73], v17 offset:0\n" \
  "ds_read_b64_tr_b16 v[74:75], v17 offset:512\n" \
  "ds_read_b64_tr_b16 v[76:77], v17 offset:32768\n" \
  "ds_read_b64_tr_b16 v[78:79], v17 offset:33280\n" \
  "s_add_u32 m0, s19, 12288\n" \
  "s_nop 0\n" \
  "global_load_lds_dwordx4 v24, s[8:9] offset:0\n" \
  "global_load_lds_dwordx4 v25, s[8:9] offset:1024\n" \
  "global_load_lds_dwordx4 v26, s[8:9] offset:2048\n" \
  "global_load_lds_dwordx4 v27, s[8:9] offset:3072\n" \
  "s_add_u32 s8, s8, 0x100000\n" \
  "s_addc_u32 s9, s9, 0\n" \
  "s_waitcnt vmcnt(8)\n" \
  ".LCV_RD0_%=:\n" \
  "v_cmp_ne_u32 vcc, s10, v20\n" \
  "v_xor_b32 v31, s11, v21\n" \
  "v_cmp_ne_u32 s[22:23], 0, v31\n" \
  "s_or_b64 vcc, vcc, s[22:23]\n" \
  "s_cbranch_scc0 .LCV_RD1_%=\n" \
  "s_sleep 8\n" \
  "global_load_dwordx2 v[20:21], v16, s[12:13] sc1\n" \
  "s_sub_u32 s87, s87, 1\n" \
  "s_cbranch_scc1 .LCV_END_%=\n" \
  "s_waitcnt vmcnt(0)\n" \
  "s_branch .LCV_RD0_%=\n" \
  ".LCV_RD1_%=:\n" \
  "s_waitcnt vmcnt(8) lgkmcnt(0)\n" \
  "s_barrier\n" \
  "ds_write_b128 v3, v[96:99] offset:0\n" \
  "ds_write_b128 v3, v[100:103] offset:1024\n" \
  "ds_write_b128 v3, v[104:107] offset:2048\n" \
  "ds_write_b128 v3, v[108:111] offset:3072\n" \
  "ds_read_b64_tr_b16 v[80:81], v28 offset:4096\n" \
  "ds_read_b64_tr_b16 v[82:83], v28 offset:4608\n" \
  "ds_read_b64_tr_b16 v[84:85], v28 offset:36864\n" \
  "ds_read_b64_tr_b16 v[86:87], v28 offset:37376\n" \
  "ds_read_b64_tr_b16 v[88:89], v17 offset:4096\n" \
  "ds_read_b64_tr_b16 v[90:91], v17 offset:4608\n" \
  "ds_read_b64_tr_b16 v[92:93], v17 offset:36864\n" \
  "ds_read_b64_tr_b16 v[94:95], v17 offset:37376\n" \
  "v_and_b32 v16, 0xff, v16\n" \
  "global_load_dwordx2 v[22:23], v16, s[14:15] sc1\n" \
  "s_mov_b64 exec, 15\n" \
  "buffer_load_dword v13, v2, s[44:47], s68 offen sc1\n" \
  "s_mov_b64 exec, -1\n" \
  "s_waitcnt vmcnt(6) lgkmcnt(0)\n" \
  "s_barrier\n" \
  "ds_write_b128 v3, v[112:115] offset:4096\n" \
  "ds_write_b128 v3, v[116:119] offset:5120\n" \
  "ds_write_b128 v3, v[120:123] offset:6144\n" \
  "ds_write_b128 v3, v[124:127] offset:7168\n" \
  "ds_read_b64_tr_b16 v[96:97], v28 offset:8192\n" \
  "ds_read_b64_tr_b16 v[98:99], v28 offset:8704\n" \
  "ds_read_b64_tr_b16 v[100:101], v28 offset:40960\n" \
  "ds_read_b64_tr_b16 v[102:103], v28 offset:41472\n" \
  "ds_read_b64_tr_b16 v[104:105], v17 offset:8192\n" \
  "ds_read_b64_tr_b16 v[106:107], v17 offset:8704\n" \
  "ds_read_b64_tr_b16 v[108:109], v17 offset:40960\n" \
  "ds_read_b64_tr_b16 v[110:111], v17 offset:41472\n" \
  "s_waitcnt vmcnt(0)\n" \
  "v_cmp_eq_u32 vcc, 0, v13\n" \
  "s_and_b32 s90, vcc_lo, 15\n" \
  "s_cbranch_scc1 .LCV_GL0_%=\n" \
  "s_mov_b32 m0, s99\n" \
  "s_add_u32 s88, s99, 8192\n" \
  "global_load_lds_dwordx4 v10, s[62:63] sc1\n" \
  "global_load_lds_dwordx4 v10, s[62:63] offset:1024 sc1\n" \
  "s_mov_b32 m0, s88\n" \
  "s_nop 0\n" \
  "global_load_lds_dwordx4 v10, s[64:65] sc1\n" \
  "global_load_lds_dwordx4 v10, s[64:65] offset:1024 sc1\n" \
  ".LCV_GL0_%=:\n" \
  "v_cmp_ne_u32 vcc, s10, v22\n" \
  "v_xor_b32 v31, s11, v23\n" \
  "v_and_b32 v31, 0xffff, v31\n" \
  "v_cmp_ne_u32 s[22:23], 0, v31\n" \
  "s_or_b64 vcc, vcc, s[22:23]\n" \
  "s_cbranch_scc0 .LCV_GL1_%=\n" \
  "s_sleep 12\n" \
  "global_load_dwordx2 v[22:23], v16, s[14:15] sc1\n" \
  "s_sub_u32 s87, s87, 1\n" \
  "s_cbranch_scc1 .LCV_END_%=\n" \
  "s_waitcnt vmcnt(0)\n" \
  "s_branch .LCV_GL0_%=\n" \
  ".LCV_GL1_%=:\n" \
  "v_and_b32 v31, 0xffff0000, v23\n" \
  "s_nop 1\n" \
  "v_max_u32_dpp v31, v31, v31 row_ror:1 row_mask:0xf bank_mask:0xf\n" \
  "s_nop 1\n" \
  "v_max_u32_dpp v31, v31, v31 row_ror:2 row_mask:0xf bank_mask:0xf\n" \
  "s_nop 1\n" \
  "v_max_u32_dpp v31, v31, v31 row_ror:4 row_mask:0xf bank_mask:0xf\n" \
  "s_nop 1\n" \
  "v_max_u32_dpp v31, v31, v31 row_ror:8 row_mask:0xf bank_mask:0xf\n" \
  "v_mov_b32 v12, v31\n" \
  "s_nop 1\n" \
  "v_permlane16_swap_b32 v31, v12\n" \
  "v_max_u32 v31, v31, v12\n" \
  "v_mov_b32 v12, v31\n" \
  "s_nop 1\n" \
  "v_permlane32_swap_b32 v31, v12\n" \
  "v_max_u32 v31, v31, v12\n" \
  "s_mov_b64 exec, 1\n" \
  "ds_write_b32 v30, v31\n" \
  "s_mov_b64 exec, -1\n" \
  "s_waitcnt vmcnt(4) lgkmcnt(0)\n" \
  "s_barrier\n" \
  "ds_write_b128 v3, v[32:35] offset:8192\n" \
  "ds_write_b128 v3, v[36:39] offset:9216\n" \
  "ds_write_b128 v3, v[40:43] offset:10240\n" \
  "ds_write_b128 v3, v[44:47] offset:11264\n" \
  "ds_read_b64_tr_b16 v[112:113], v28 offset:12288\n" \
  "ds_read_b64_tr_b16 v[114:115], v28 offset:12800\n" \
  "ds_read_b64_tr_b16 v[116:117], v28 offset:45056\n" \
  "ds_read_b64_tr_b16 v[118:119], v28 offset:45568\n" \
  "ds_read_b64_tr_b16 v[120:121], v17 offset:12288\n" \
  "ds_read_b64_tr_b16 v[122:123], v17 offset:12800\n" \
  "ds_read_b64_tr_b16 v[124:125], v17 offset:45056\n" \
  "ds_read_b64_tr_b16 v[126:127], v17 offset:45568\n" \
  "ds_read_b32 v31, v30 offset:0\n" \
  "s_waitcnt lgkmcnt(0)\n" \
  "s_barrier\n" \
  "ds_write_b128 v3, v[48:51] offset:12288\n" \
  "ds_write_b128 v3, v[52:55] offset:13312\n" \
  "ds_write_b128 v3, v[56:59] offset:14336\n" \
  "ds_write_b128 v3, v[60:63] offset:15360\n" \
  "v_mul_f32 v31, 0x3f8e353f, v31\n" \
  "v_mul_f32 v30, 0x44870000, v31\n" \
  "v_mov_b64 v[12:13], 0\n" \
  "v_mov_b64 v[14:15], 0\n" \
  "v_mov_b64 v[16:17], 0\n" \
  "v_mov_b64 v[18:19], 0\n" \
  "v_mov_b64 v[20:21], 0\n" \
  "v_mov_b64 v[22:23], 0\n" \
  "v_readfirstlane_b32 s72, v31\n" \
  "v_readfirstlane_b32 s86, v30\n" \
  "s_lshr_b32 s86, s86, 23\n" \
  "s_addk_i32 s86, 0xff83\n" \
  "s_ashr_i32 s86, s86, 1\n" \
  "s_max_i32 s86, s86, -20\n" \
  "s_min_i32 s86, s86, 100\n" \
  "s_mov_b32 s82, s86\n" \
  "s_lshr_b32 s83, s72, 23\n" \
  "s_sub_i32 s83, s83, 243\n" \
  "s_waitcnt lgkmcnt(0)\n" \
  "s_add_u32 s88, s74, 157760\n" \
  "v_mov_b32 v59, s88\n" \
  "s_cmp_lg_u32 s61, 0\n" \
  "s_movk_i32 s76, -5952\n" \
  "s_cmovk_i32 s76, 7104\n" \
  "s_movk_i32 s77, 75\n" \
  "s_cmovk_i32 s77, 106\n" \
  "s_cselect_b32 s72, -1, 57\n" \
  "s_cselect_b32 s75, 0, 63\n" \
  "s_cmp_eq_u32 s61, 0\n" \
  "s_cbranch_scc1 .LCV_W7_%=\n" \
  "s_movk_i32 s87, 100\n" \
  ".LCV_W5_%=:\n" \
  "buffer_load_dword v58, off, s[0:3], 0 offset:896 sc1\n" \
  "s_waitcnt vmcnt(0)\n" \
  "v_readfirstlane_b32 vcc_lo, v58\n" \
  ".LCV_W6_%=:\n" \
  "s_sleep 16\n" \
  "s_sub_u32 s87, s87, 1\n" \
  "s_cbranch_scc1 .LCV_W7_%=\n" \
  "s_cmp_eq_u32 vcc_lo, 0\n" \
  "s_cbranch_scc1 .LCV_W5_%=\n" \
  "s_memrealtime s[38:39]\n" \
  "s_waitcnt lgkmcnt(0)\n" \
  "s_sub_u32 s38, s38, vcc_lo\n" \
  "s_cmp_lt_i32 s38, 800\n" \
  "s_cbranch_scc1 .LCV_W6_%=\n" \
  "s_branch .LCV_FS_%=\n" \
  ".LCV_W7_%=:\n" \
  "s_mov_b32 s87, 0x40000\n" \
  "s_cmp_eq_u32 s90, 0\n" \
  "s_cbranch_scc1 .LCV_W1_%=\n" \
  "s_branch .LCV_W2_%=\n" \
  ".LCV_W0_%=:\n" \
  "s_sleep 2\n" \
  "s_cmp_eq_u32 s61, 0\n" \
  "s_cbranch_scc1 .LCV_W2_%=\n" \
  "s_sleep 8\n" \
  ".LCV_W2_%=:\n" \
  "s_mov_b64 exec, 15\n" \
  "buffer_load_dword v58, v2, s[44:47], s68 offen sc1\n" \
  "s_mov_b64 exec, -1\n" \
  "s_sub_u32 s87, s87, 1\n" \
  "s_cbranch_scc1 .LCV_END_%=\n" \
  "s_waitcnt vmcnt(0)\n" \
  "v_cmp_eq_u32 vcc, 0, v58\n" \
  "s_and_b32 vcc_lo, vcc_lo, 15\n" \
  "s_cbranch_scc1 .LCV_W0_%=\n" \
  "s_mov_b32 m0, s99\n" \
  "s_add_u32 s88, s99, 8192\n" \
  "global_load_lds_dwordx4 v10, s[62:63] sc1\n" \
  "global_load_lds_dwordx4 v10, s[62:63] offset:1024 sc1\n" \
  "s_mov_b32 m0, s88\n" \
  "s_nop 0\n" \
  "global_load_lds_dwordx4 v10, s[64:65] sc1\n" \
  "global_load_lds_dwordx4 v10, s[64:65] offset:1024 sc1\n" \
  ".LCV_W1_%=:\n" \
  "s_add_u32 s62, s62, 0x80000\n" \
  "s_addc_u32 s63, s63, 0\n" \
  "s_add_u32 s64, s64, 0x40000\n" \
  "s_addc_u32 s65, s65, 0\n" \
  "s_add_u32 s68, s68, 0x1000\n" \
  "s_cmp_eq_u32 s61, 0\n" \
  "s_cbranch_scc1 .LCV_W4_%=\n" \
  "s_add_u32 s38, s67, 2944\n" \
  "v_mov_b32 v58, s38\n" \
  "buffer_load_dword v58, v58, s[0:3], 0 offen sc1\n" \
  "s_waitcnt vmcnt(0)\n" \
  "v_readfirstlane_b32 vcc_lo, v58\n" \
  "s_movk_i32 s87, 200\n" \
  "s_cmp_eq_u32 vcc_lo, 0\n" \
  "s_cbranch_scc1 .LCV_W4_%=\n" \
  "s_add_u32 vcc_lo, vcc_lo, 565\n" \
  ".LCV_W3_%=:\n" \
  "s_sleep 2\n" \
  "s_memrealtime s[38:39]\n" \
  "s_sub_u32 s87, s87, 1\n" \
  "s_cbranch_scc1 .LCV_W4_%=\n" \
  "s_waitcnt lgkmcnt(0)\n" \
  "s_sub_u32 s38, s38, vcc_lo\n" \
  "s_cmp_lt_i32 s38, 0\n" \
  "s_cbranch_scc1 .LCV_W3_%=\n" \
  ".LCV_W4_%=:\n" \
  "s_waitcnt vmcnt(0)\n" \
  "s_cmp_eq_u32 s72, -1\n" \
  "s_cselect_b32 s90, 23, 1\n" \
  ".p2align 6\n" \
  "s_nop 0\n" \
  "s_nop 0\n" \
  ".LCV_TOP_%=:\n" \
  "s_waitcnt lgkmcnt(0)\n" \
  "s_barrier\n" \
  "s_setprio 3\n" \
  "ds_read_b128 v[24:27], v1 offset:0\n" \
  "ds_read_b128 v[28:31], v1 offset:1024\n" \
  "ds_read_b128 v[32:35], v1 offset:2048\n" \
  "ds_read_b128 v[36:39], v1 offset:3072\n" \
  "ds_read_b128 v[40:43], v1 offset:4096\n" \
  "ds_read_b128 v[44:47], v1 offset:5120\n" \
  "ds_read_b128 v[48:51], v1 offset:6144\n" \
  "ds_read_b128 v[52:55], v1 offset:7168\n" \
  "s_memrealtime s[38:39]\n" \
  "s_waitcnt lgkmcnt(7)\n" \
  "v_smfmac_f32_16x16x64_bf16 v[12:15], v[24:27], v[64:71], v0\n" \
  "s_waitcnt lgkmcnt(6)\n" \
  "v_smfmac_f32_16x16x64_bf16 v[12:15], v[28:31], v[72:79], v0\n" \
  "s_waitcnt lgkmcnt(5)\n" \
  "v_smfmac_f32_16x16x64_bf16 v[12:15], v[32:35], v[80:87], v0\n" \
  "s_waitcnt lgkmcnt(4)\n" \
  "v_smfmac_f32_16x16x64_bf16 v[12:15], v[36:39], v[88:95], v0\n" \
  "s_waitcnt lgkmcnt(3)\n" \
  "v_smfmac_f32_16x16x64_bf16 v[12:15], v[40:43], v[96:103], v0\n" \
  "s_waitcnt lgkmcnt(2)\n" \
  "v_smfmac_f32_16x16x64_bf16 v[12:15], v[44:47], v[104:111], v0\n" \
  "s_waitcnt lgkmcnt(1)\n" \
  "v_smfmac_f32_16x16x64_bf16 v[12:15], v[48:51], v[112:119], v0\n" \
  "s_waitcnt lgkmcnt(0)\n" \
  "v_smfmac_f32_16x16x64_bf16 v[12:15], v[52:55], v[120:127], v0\n" \
  "ds_read_b32 v11, v1 offset:26688\n" \
  "s_mov_b32 s61, s38\n" \
  "s_add_u32 s38, s38, s79\n" \
  "s_add_u32 s38, s38, s96\n" \
  "s_mov_b32 s96, 0\n" \
  "v_or_b32 v57, 1, s38\n" \
  "v_add_u32 v56, s67, v10\n" \
  "s_add_u32 s39, s67, s76\n" \
  "s_add_u32 s38, s38, s77\n" \
  "s_bitset1_b32 s38, 0\n" \
  "v_writelane_b32 v56, s39, 1\n" \
  "v_writelane_b32 v57, s38, 1\n" \
  "s_sub_u32 s39, s71, s72\n" \
  "s_cmp_lt_u32 s39, 7\n" \
  "s_cselect_b64 exec, 19, 3\n" \
  "buffer_store_dword v57, v56, s[0:3], 0 offen sc1\n" \
  "s_mov_b64 exec, -1\n" \
  "v_permlane32_swap_b32 v12, v14\n" \
  "v_permlane32_swap_b32 v13, v15\n" \
  "v_pk_add_f32 v[12:13], v[12:13], v[14:15]\n" \
  "s_nop 0\n" \
  "v_cvt_pk_bf16_f32 v12, v12, v13\n" \
  "ds_write_b32 v4, v12\n" \
  "s_setprio 2\n" \
  "v_mov_b64 v[12:13], s[92:93]\n" \
  "v_mov_b64 v[14:15], s[92:93]\n" \
  "s_waitcnt lgkmcnt(1)\n" \
  "v_readfirstlane_b32 s86, v11\n" \
  "s_lshr_b32 s86, s86, 23\n" \
  "s_add_i32 s86, s86, s83\n" \
  "s_ashr_i32 s86, s86, 1\n" \
  "s_max_i32 s86, s86, s82\n" \
  "s_min_i32 s86, s86, 100\n" \
  "s_lshl_b32 s86, s86, 23\n" \
  "s_sub_i32 s84, 0xc8800000, s86\n" \
  "s_cmp_eq_u32 s73, 5\n" \
  "v_smfmac_f32_16x16x64_bf16 v[16:19], v[24:27], v[128:135], v0\n" \
  "v_smfmac_f32_16x16x64_bf16 v[16:19], v[28:31], v[136:143], v0\n" \
  "v_smfmac_f32_16x16x64_bf16 v[16:19], v[32:35], v[144:151], v0\n" \
  "v_smfmac_f32_16x16x64_bf16 v[16:19], v[36:39], v[152:159], v0\n" \
  "v_smfmac_f32_16x16x64_bf16 v[16:19], v[40:43], v[160:167], v0\n" \
  "v_smfmac_f32_16x16x64_bf16 v[16:19], v[44:47], v[168:175], v0\n" \
  "s_waitcnt lgkmcnt(0)\n" \
  "s_barrier\n" \
  "ds_read_b64_tr_b16 v[60:61], v6 offset:0\n" \
  "ds_read_b64_tr_b16 v[62:63], v9 offset:0\n" \
  "v_smfmac_f32_16x16x64_bf16 v[16:19], v[48:51], v[176:183], v0\n" \
  "v_smfmac_f32_16x16x64_bf16 v[16:19], v[52:55], v[184:191], v0\n" \
  "v_smfmac_f32_16x16x64_bf16 v[20:23], v[24:27], v[192:199], v0\n" \
  "v_smfmac_f32_16x16x64_bf16 v[20:23], v[28:31], v[200:207], v0\n" \
  "v_smfmac_f32_16x16x64_bf16 v[20:23], v[32:35], v[208:215], v0\n" \
  "v_smfmac_f32_16x16x64_bf16 v[20:23], v[36:39], v[216:223], v0\n" \
  "v_smfmac_f32_16x16x64_bf16 v[20:23], v[40:43], v[224:231], v0\n" \
  "v_smfmac_f32_16x16x64_bf16 v[20:23], v[44:47], v[232:239], v0\n" \
  "v_smfmac_f32_16x16x64_bf16 v[20:23], v[48:51], v[240:247], v0\n" \
  "v_smfmac_f32_16x16x64_bf16 v[20:23], v[52:55], v[248:255], v0\n" \
  "s_cmp_eq_u32 s71, 0\n" \
  "v_permlane32_swap_b32 v16, v18\n" \
  "v_permlane32_swap_b32 v17, v19\n" \
  "v_add_f32 v16, v16, v18\n" \
  "v_add_f32 v17, v17, v19\n" \
  "v_cvt_pk_bf16_f32 v16, v16, v17\n" \
  "s_waitcnt lgkmcnt(0)\n" \
  "v_max_u32 v60, 1, v60\n" \
  "v_max_u32 v61, 1, v61\n" \
  "v_max_u32 v62, 1, v62\n" \
  "v_max_u32 v63, 1, v63\n" \
  "buffer_store_dwordx4 v[60:63], v7, s[48:51], s69 offen sc1\n" \
  "v_permlane32_swap_b32 v20, v22\n" \
  "v_permlane32_swap_b32 v21, v23\n" \
  "v_pk_add_f32 v[20:21], v[20:21], v[22:23]\n" \
  "v_max_u32 v18, 1, v16\n" \
  "v_cvt_pk_bf16_f32 v17, v20, v21\n" \
  "ds_write_b64 v5, v[16:17]\n" \
  "v_max_u32 v19, 1, v17\n" \
  "s_nop 1\n" \
  "v_permlane32_swap_b32 v18, v19\n" \
  "buffer_store_dwordx2 v[18:19], v8, s[52:55], s70 offen sc1\n" \
  "ds_read_b128 v[24:27], v3 offset:0\n" \
  "ds_read_b128 v[28:31], v3 offset:1024\n" \
  "ds_read_b128 v[32:35], v3 offset:2048\n" \
  "ds_read_b128 v[36:39], v3 offset:3072\n" \
  "ds_read_b128 v[40:43], v3 offset:8192\n" \
  "ds_read_b128 v[44:47], v3 offset:9216\n" \
  "ds_read_b128 v[48:51], v3 offset:10240\n" \
  "ds_read_b128 v[52:55], v3 offset:11264\n" \
  "s_cbranch_scc1 .LCV_LAST_%=\n" \
  "s_cmp_eq_u32 s73, 5\n" \
  "s_cbranch_scc0 .LCV_NMK_%=\n" \
  "s_mov_b32 m0, s78\n" \
  "s_nop 0\n" \
  "buffer_load_dword v2, s[0:3], s66 offen sc1 lds\n" \
  ".LCV_NMK_%=:\n" \
  "s_setprio 0\n" \
  "s_waitcnt lgkmcnt(8)\n" \
  "s_barrier\n" \
  "s_add_u32 s67, s67, 1024\n" \
  "v_mov_b32 v11, 0\n" \
  "ds_write_b32 v59, v11\n" \
  "ds_read_b128 v[56:59], v3 offset:4096\n" \
  "ds_read_b128 v[60:63], v3 offset:5120\n" \
  "v_mov_b64 v[20:21], s[92:93]\n" \
  "v_mov_b64 v[22:23], s[92:93]\n" \
  "v_mov_b64 v[16:17], s[92:93]\n" \
  "v_mov_b64 v[18:19], s[92:93]\n" \
  "s_mov_b32 m0, s94\n" \
  "s_cmp_gt_i32 s71, s75\n" \
  "s_cselect_b64 exec, 0, 15\n" \
  "buffer_load_dword v2, s[44:47], s68 offen sc1 lds\n" \
  "s_mov_b64 exec, -1\n" \
  "s_setprio 2\n" \
  "s_waitcnt lgkmcnt(0)\n" \
  "s_barrier\n" \
  "ds_read_b128 v[4:7], v1 offset:8192\n" \
  "ds_read_b128 v[8:11], v1 offset:9216\n" \
  "s_waitcnt lgkmcnt(1)\n" \
  "v_smfmac_f32_16x16x64_bf16 v[12:15], v[4:7], v[24:31], v0\n" \
  "ds_read_b128 v[24:27], v3 offset:6144\n" \
  "ds_read_b128 v[28:31], v3 offset:7168\n" \
  "v_smfmac_f32_16x16x64_bf16 v[16:19], v[4:7], v[32:39], v0\n" \
  "ds_read_b128 v[32:35], v3 offset:12288\n" \
  "ds_read_b128 v[36:39], v3 offset:13312\n" \
  "s_waitcnt lgkmcnt(4)\n" \
  "v_smfmac_f32_16x16x64_bf16 v[12:15], v[8:11], v[40:47], v0\n" \
  "v_smfmac_f32_16x16x64_bf16 v[16:19], v[8:11], v[48:55], v0\n" \
  "v_smfmac_f32_16x16x64_bf16 v[20:23], v[4:7], v[56:63], v0\n" \
  "ds_read_b128 v[56:59], v3 offset:14336\n" \
  "ds_read_b128 v[60:63], v3 offset:15360\n" \
  "v_mov_b64 v[40:41], s[92:93]\n" \
  "v_mov_b64 v[42:43], s[92:93]\n" \
  "s_nop 2\n" \
  "v_permlane32_swap_b32 v12, v17\n" \
  "v_permlane32_swap_b32 v13, v16\n" \
  "v_permlane32_swap_b32 v14, v19\n" \
  "v_permlane32_swap_b32 v15, v18\n" \
  "v_pk_add_f32 v[12:13], v[12:13], v[16:17] op_sel:[0,1] op_sel_hi:[1,0]\n" \
  "v_pk_add_f32 v[14:15], v[14:15], v[18:19] op_sel:[0,1] op_sel_hi:[1,0]\n" \
  "buffer_atomic_add_x2 v[12:13], v2, s[40:43], s56 offen\n" \
  "buffer_atomic_add_x2 v[14:15], v2, s[40:43], s57 offen\n" \
  "s_waitcnt lgkmcnt(2)\n" \
  "v_smfmac_f32_16x16x64_bf16 v[20:23], v[8:11], v[32:39], v0\n" \
  "v_smfmac_f32_16x16x64_bf16 v[40:43], v[4:7], v[24:31], v0\n" \
  "s_waitcnt lgkmcnt(0)\n" \
  "v_smfmac_f32_16x16x64_bf16 v[40:43], v[8:11], v[56:63], v0\n" \
  "s_nop 7\n" \
  "v_permlane32_swap_b32 v20, v41\n" \
  "v_permlane32_swap_b32 v21, v40\n" \
  "v_permlane32_swap_b32 v22, v43\n" \
  "v_permlane32_swap_b32 v23, v42\n" \
  "v_pk_add_f32 v[20:21], v[20:21], v[40:41] op_sel:[0,1] op_sel_hi:[1,0]\n" \
  "v_pk_add_f32 v[22:23], v[22:23], v[42:43] op_sel:[0,1] op_sel_hi:[1,0]\n" \
  "buffer_atomic_add_x2 v[20:21], v2, s[40:43], s58 offen\n" \
  "buffer_atomic_add_x2 v[22:23], v2, s[40:43], s59 offen\n" \
  "s_setprio 0\n" \
  "s_memrealtime s[38:39]\n" \
  "v_mbcnt_lo_u32_b32 v48, -1, 0\n" \
  "v_mbcnt_hi_u32_b32 v48, -1, v48\n" \
  "s_add_u32 s88, s74, 157760\n" \
  "v_mov_b32 v59, s88\n" \
  "v_lshl_add_u32 v52, v48, 2, v59\n" \
  "ds_read_b32 v52, v52 offset:192\n" \
  "v_lshrrev_b32 v49, 3, v48\n" \
  "v_and_b32 v50, 7, v48\n" \
  "v_lshlrev_b32 v50, 4, v50\n" \
  "v_lshl_or_b32 v44, v49, 8, v50\n" \
  "s_mov_b32 s87, 0x40000\n" \
  "v_lshlrev_b32 v10, 4, v48\n" \
  "s_cmp_gt_i32 s71, s75\n" \
  "s_cbranch_scc1 .LCV_GO2_%=\n" \
  "v_lshl_add_u32 v58, v48, 2, s94\n" \
  "s_waitcnt vmcnt(4)\n" \
  "ds_read_b32 v58, v58\n" \
  "s_waitcnt lgkmcnt(0)\n" \
  "v_cmp_eq_u32 vcc, 0, v58\n" \
  "s_and_b32 vcc_lo, vcc_lo, 15\n" \
  "s_cbranch_scc0 .LCV_GOT_%=\n" \
  "s_branch .LCV_ASK_%=\n" \
  ".LCV_LATE_%=:\n" \
  "v_cmp_eq_u32 vcc, 0, v58\n" \
  "s_and_b32 vcc_lo, vcc_lo, 15\n" \
  "s_cbranch_scc0 .LCV_GOT_%=\n" \
  "s_sleep 12\n" \
  ".LCV_ASK_%=:\n" \
  "s_mov_b64 exec, 15\n" \
  "buffer_load_dword v58, v2, s[44:47], s68 offen sc1\n" \
  "s_mov_b64 exec, -1\n" \
  "s_sub_u32 s87, s87, 1\n" \
  "s_cbranch_scc1 .LCV_END_%=\n" \
  "s_waitcnt vmcnt(0)\n" \
  "s_branch .LCV_LATE_%=\n" \
  ".LCV_GO2_%=:\n" \
  ".LCV_GOT_%=:\n" \
  "s_nop 3\n" \
  "s_mov_b32 m0, s99\n" \
  "s_add_u32 s88, s99, 8192\n" \
  "global_load_lds_dwordx4 v10, s[62:63] sc1\n" \
  "global_load_lds_dwordx4 v10, s[62:63] offset:1024 sc1\n" \
  "s_mov_b32 m0, s88\n" \
  "s_nop 0\n" \
  "global_load_lds_dwordx4 v10, s[64:65] sc1\n" \
  "global_load_lds_dwordx4 v10, s[64:65] offset:1024 sc1\n" \
  ".LCV_HAVE_%=:\n" \
  "s_waitcnt lgkmcnt(0)\n" \
  "s_sub_u32 s61, s38, s61\n" \
  "s_add_u32 s79, s79, 2\n" \
  "s_min_u32 s79, s79, s61\n" \
  "s_sub_u32 s39, s38, 150\n" \
  "v_subrev_u32 v52, s39, v52\n" \
  "v_cmp_lt_u32 vcc, 0x1000, v52\n" \
  "s_cmp_eq_u64 vcc, 0\n" \
  "s_cbranch_scc1 .LCV_OKM_%=\n" \
  ".LCV_AGN_%=:\n" \
  "buffer_load_dword v52, v2, s[0:3], s66 offen sc1\n" \
  "s_sub_u32 s87, s87, 1\n" \
  "s_cbranch_scc1 .LCV_END_%=\n" \
  "s_waitcnt vmcnt(0)\n" \
  "v_cmp_eq_u32 vcc, 0, v52\n" \
  "v_subrev_u32 v52, s39, v52\n" \
  "s_cmp_eq_u64 vcc, 0\n" \
  "s_cbranch_scc1 .LCV_OKM_%=\n" \
  "s_sleep 4\n" \
  "s_branch .LCV_AGN_%=\n" \
  ".LCV_OKM_%=:\n" \
  "s_nop 1\n" \
  "v_max_i32_dpp v52, v52, v52 row_ror:1 row_mask:0xf bank_mask:0xf\n" \
  "s_nop 1\n" \
  "v_max_i32_dpp v52, v52, v52 row_ror:2 row_mask:0xf bank_mask:0xf\n" \
  "s_nop 1\n" \
  "v_max_i32_dpp v52, v52, v52 row_ror:4 row_mask:0xf bank_mask:0xf\n" \
  "s_nop 1\n" \
  "v_max_i32_dpp v52, v52, v52 row_ror:8 row_mask:0xf bank_mask:0xf\n" \
  "s_movk_i32 vcc_lo, 150\n" \
  "v_readlane_b32 s88, v52, 0\n" \
  "s_max_i32 vcc_lo, vcc_lo, s88\n" \
  "v_readlane_b32 s88, v52, 16\n" \
  "s_max_i32 vcc_lo, vcc_lo, s88\n" \
  "v_readlane_b32 s88, v52, 32\n" \
  "s_max_i32 vcc_lo, vcc_lo, s88\n" \
  "v_readlane_b32 s88, v52, 48\n" \
  "s_max_i32 vcc_lo, vcc_lo, s88\n" \
  "s_add_u32 s66, s66, 1024\n" \
  "s_add_u32 vcc_lo, vcc_lo, s39\n" \
  "s_add_u32 vcc_lo, vcc_lo, 24\n" \
  ".LCV_TW_%=:\n" \
  "s_memrealtime s[38:39]\n" \
  "s_waitcnt lgkmcnt(0)\n" \
  "s_sub_u32 s38, s38, vcc_lo\n" \
  "s_cmp_lt_i32 s38, 0\n" \
  "s_cbranch_scc1 .LCV_TW_%=\n" \
  "buffer_load_dwordx4 v[24:27], v44, s[40:43], s60 offen sc1\n" \
  "buffer_load_dwordx4 v[28:31], v44, s[40:43], s60 offen offset:2048 sc1\n" \
  "s_add_u32 s69, s69, 0x10000\n" \
  "s_add_u32 s70, s70, 0x10000\n" \
  "s_sub_u32 s88, s73, 4\n" \
  "v_lshrrev_b32 v49, 2, v48\n" \
  "v_lshrrev_b32 v50, 4, v48\n" \
  "v_bitop3_b32 v50, v49, v50, 3 bitop3:0x28\n" \
  "v_lshl_add_u32 v4, v50, 3, s97\n" \
  "v_and_b32 v50, 3, v48\n" \
  "v_lshl_add_u32 v4, v50, 5, v4\n" \
  "v_and_b32 v49, 1, v49\n" \
  "v_lshl_add_u32 v4, v49, 8, v4\n" \
  "v_and_b32 v49, 8, v48\n" \
  "v_lshl_add_u32 v4, v49, 4, v4\n" \
  "v_and_b32 v51, 15, v48\n" \
  "v_bfe_u32 v49, v48, 4, 1\n" \
  "v_lshrrev_b32 v50, 5, v48\n" \
  "s_lshl_b32 s89, s88, 10\n" \
  "s_add_u32 s89, s89, s74\n" \
  "s_add_u32 s89, s89, 158720\n" \
  "v_lshrrev_b32 v9, 3, v51\n" \
  "v_mul_u32_u24 v6, 41, v9\n" \
  "v_xor_b32 v6, v6, v51\n" \
  "v_lshl_add_u32 v6, v6, 3, s89\n" \
  "v_lshl_add_u32 v6, v49, 6, v6\n" \
  "v_lshl_add_u32 v6, v50, 9, v6\n" \
  "v_xor_b32 v9, 0x90, v6\n" \
  "v_lshlrev_b32 v8, 4, v49\n" \
  "v_lshl_add_u32 v8, v51, 5, v8\n" \
  "v_lshl_add_u32 v8, v50, 3, v8\n" \
  "s_lshl_b32 s89, s88, 8\n" \
  "s_add_u32 s89, s89, s74\n" \
  "s_add_u32 s89, s89, 139264\n" \
  "v_and_b32 v49, 31, v48\n" \
  "v_lshl_add_u32 v5, v49, 3, s89\n" \
  "v_lshl_add_u32 v5, v50, 10, v5\n" \
  "v_lshrrev_b32 v49, 2, v51\n" \
  "v_and_b32 v50, 5, v51\n" \
  "v_and_or_b32 v49, v49, 2, v50\n" \
  "v_and_b32 v50, 48, v48\n" \
  "v_lshlrev_b32 v7, 3, v50\n" \
  "v_lshl_add_u32 v7, v49, 4, v7\n" \
  "v_bitop3_b32 v49, v51, 2, 2 bitop3:0x0c\n" \
  "v_lshl_add_u32 v7, v49, 29, v7\n" \
  "v_mov_b64 v[12:13], 0\n" \
  "v_mov_b64 v[14:15], 0\n" \
  "v_mov_b64 v[16:17], 0\n" \
  "v_mov_b64 v[18:19], 0\n" \
  "v_mov_b64 v[20:21], 0\n" \
  "v_mov_b64 v[22:23], 0\n" \
  "v_and_b32 v50, 3, v48\n" \
  "v_lshl_add_u32 v40, v50, 2, s98\n" \
  "v_lshrrev_b32 v50, 4, v48\n" \
  "v_mad_u32_u24 v40, v50, s95, v40\n" \
  "v_bfe_u32 v49, v48, 3, 1\n" \
  "v_lshl_add_u32 v40, v49, 6, v40\n" \
  "v_bfe_u32 v49, v48, 2, 1\n" \
  "v_lshl_add_u32 v40, v49, 7, v40\n" \
  "v_xor_b32 v41, 16, v40\n" \
  "v_xor_b32 v42, 32, v40\n" \
  "v_xor_b32 v43, 48, v40\n" \
  "s_add_i32 s80, s86, 0x35000000\n" \
  "v_mov_b32 v11, 0\n" \
  "s_mov_b32 s89, s60\n" \
  "s_bitcmp1_b32 s60, 7\n" \
  "s_cselect_b32 s88, 0x10000, 0\n" \
  "s_bitset1_b32 s88, 7\n" \
  "s_xor_b32 s56, s56, s88\n" \
  "s_xor_b32 s57, s57, s88\n" \
  "s_xor_b32 s58, s58, s88\n" \
  "s_xor_b32 s59, s59, s88\n" \
  "s_xor_b32 s60, s60, s88\n" \
  "s_sub_u32 s71, s71, 1\n" \
  "s_waitcnt vmcnt(2)\n" \
  "v_add_u32 v53, s99, v10\n" \
  "ds_read_b32 v54, v53 offset:0\n" \
  "ds_read_b32 v55, v53 offset:1024\n" \
  "ds_read_b32 v56, v53 offset:8192\n" \
  "ds_read_b32 v57, v53 offset:9216\n" \
  "s_lshr_b32 s88, s71, 4\n" \
  "s_cmp_ge_i32 s71, s75\n" \
  "s_cselect_b32 vcc_lo, 1, 0\n" \
  "s_waitcnt lgkmcnt(0)\n" \
  "v_bitop3_b32 v54, v54, v55, s88 bitop3:0x7e\n" \
  "v_bitop3_b32 v56, v56, v57, s88 bitop3:0x7e\n" \
  "v_bitop3_b32 v54, v54, v56, vcc_lo bitop3:0xa8\n" \
  "v_cmp_ne_u32 vcc, 0, v54\n" \
  "s_cmp_eq_u64 vcc, 0\n" \
  "s_cbranch_scc0 .LCV_MISS_%=\n" \
  ".LCV_M3_%=:\n" \
  "s_add_u32 s62, s62, 0x80000\n" \
  "s_addc_u32 s63, s63, 0\n" \
  "s_add_u32 s64, s64, 0x40000\n" \
  "s_addc_u32 s65, s65, 0\n" \
  "s_bitset0_b32 s62, s90\n" \
  "s_sub_u32 s88, s90, 1\n" \
  "s_bitset0_b32 s64, s88\n" \
  "s_add_u32 s68, s68, 0x1000\n" \
  "s_min_u32 s75, s75, 60\n" \
  ".LCV_SEE_%=:\n" \
  "s_waitcnt vmcnt(1)\n" \
  "v_cvt_f32_i32 v24, v24\n" \
  "v_cvt_f32_i32 v25, v25\n" \
  "v_cvt_f32_i32 v26, v26\n" \
  "v_cvt_f32_i32 v27, v27\n" \
  "v_max_f32 v45, |v24|, |v26|\n" \
  "v_pk_mul_f32 v[24:25], v[24:25], s[80:81] op_sel_hi:[1,0]\n" \
  "v_pk_mul_f32 v[26:27], v[26:27], s[80:81] op_sel_hi:[1,0]\n" \
  "s_nop 0\n" \
  "v_cvt_pk_bf16_f32 v32, v24, v26\n" \
  "v_cvt_pk_bf16_f32 v33, v25, v27\n" \
  "ds_write_b32 v40, v32\n" \
  "ds_write_b32 v41, v33\n" \
  "s_waitcnt vmcnt(0)\n" \
  "v_cvt_f32_i32 v28, v28\n" \
  "v_cvt_f32_i32 v30, v30\n" \
  "v_cvt_f32_i32 v29, v29\n" \
  "v_max3_f32 v45, v45, |v28|, |v30|\n" \
  "v_cvt_f32_i32 v31, v31\n" \
  "v_cmp_lt_f32 vcc, s91, v45\n" \
  "v_pk_mul_f32 v[28:29], v[28:29], s[80:81] op_sel_hi:[1,0]\n" \
  "v_pk_mul_f32 v[30:31], v[30:31], s[80:81] op_sel_hi:[1,0]\n" \
  "s_cmp_eq_u64 vcc, 0\n" \
  "s_cbranch_scc0 .LCV_POLL_%=\n" \
  "v_cvt_pk_bf16_f32 v36, v28, v30\n" \
  "v_cvt_pk_bf16_f32 v37, v29, v31\n" \
  "ds_write_b32 v42, v36\n" \
  "ds_write_b32 v43, v37\n" \
  "v_dot2c_f32_bf16 v11, v32, v32\n" \
  "v_dot2c_f32_bf16 v11, v33, v33\n" \
  "v_dot2c_f32_bf16 v11, v36, v36\n" \
  "v_dot2c_f32_bf16 v11, v37, v37\n" \
  "s_nop 1\n" \
  "s_nop 0\n" \
  "v_cmp_lt_f32 vcc, 4.0, v11\n" \
  "s_cbranch_vccz .LCV_TOP_%=\n" \
  "s_mov_b64 exec, vcc\n" \
  "ds_max_u32 v59, v11\n" \
  "s_mov_b64 exec, -1\n" \
  "s_branch .LCV_TOP_%=\n" \
  ".LCV_POLL_%=:\n" \
  "buffer_load_dwordx4 v[24:27], v44, s[40:43], s89 offen sc1\n" \
  "buffer_load_dwordx4 v[28:31], v44, s[40:43], s89 offen offset:2048 sc1\n" \
  "s_sub_u32 s87, s87, 1\n" \
  "s_cbranch_scc1 .LCV_END_%=\n" \
  "s_branch .LCV_SEE_%=\n" \
  ".LCV_MISS_%=:\n" \
  "s_mov_b32 m0, s99\n" \
  "s_add_u32 s88, s99, 8192\n" \
  "global_load_lds_dwordx4 v10, s[62:63] sc1\n" \
  "global_load_lds_dwordx4 v10, s[62:63] offset:1024 sc1\n" \
  "s_mov_b32 m0, s88\n" \
  "s_nop 0\n" \
  "global_load_lds_dwordx4 v10, s[64:65] sc1\n" \
  "global_load_lds_dwordx4 v10, s[64:65] offset:1024 sc1\n" \
  "s_waitcnt vmcnt(0)\n" \
  "v_add_u32 v53, s99, v10\n" \
  "ds_read_b32 v54, v53 offset:0\n" \
  "ds_read_b32 v55, v53 offset:1024\n" \
  "ds_read_b32 v56, v53 offset:8192\n" \
  "ds_read_b32 v57, v53 offset:9216\n" \
  "s_lshr_b32 s88, s71, 4\n" \
  "s_cmp_ge_i32 s71, s75\n" \
  "s_cselect_b32 vcc_lo, 1, 0\n" \
  "s_waitcnt lgkmcnt(0)\n" \
  "v_bitop3_b32 v54, v54, v55, s88 bitop3:0x7e\n" \
  "v_bitop3_b32 v56, v56, v57, s88 bitop3:0x7e\n" \
  "v_bitop3_b32 v54, v54, v56, vcc_lo bitop3:0xa8\n" \
  "v_cmp_ne_u32 vcc, 0, v54\n" \
  "s_cmp_eq_u64 vcc, 0\n" \
  "s_cbranch_scc1 .LCV_M3_%=\n" \
  ".LCV_M1_%=:\n" \
  "s_mov_b64 exec, 15\n" \
  "buffer_load_dword v58, v2, s[44:47], s68 offen sc1\n" \
  "s_mov_b64 exec, -1\n" \
  "s_sub_u32 s87, s87, 1\n" \
  "s_cbranch_scc1 .LCV_END_%=\n" \
  "s_waitcnt vmcnt(0)\n" \
  "v_cmp_eq_u32 vcc, 0, v58\n" \
  "s_and_b32 vcc_lo, vcc_lo, 15\n" \
  "s_cbranch_scc0 .LCV_M2_%=\n" \
  "s_sleep 8\n" \
  "s_branch .LCV_M1_%=\n" \
  ".LCV_M2_%=:\n" \
  "s_mov_b32 m0, s99\n" \
  "s_add_u32 s88, s99, 8192\n" \
  "global_load_lds_dwordx4 v10, s[62:63] sc1\n" \
  "global_load_lds_dwordx4 v10, s[62:63] offset:1024 sc1\n" \
  "s_mov_b32 m0, s88\n" \
  "s_nop 0\n" \
  "global_load_lds_dwordx4 v10, s[64:65] sc1\n" \
  "global_load_lds_dwordx4 v10, s[64:65] offset:1024 sc1\n" \
  "s_waitcnt vmcnt(0)\n" \
  "s_branch .LCV_M3_%=\n" \
  ".LCV_LAST_%=:\n" \
  "s_waitcnt vmcnt(0)\n" \
  ".LCV_END_%=:\n" \
  "s_endpgm\n" \
  ".LCV_FS_%=:\n" \
  "s_cmp_eq_u32 s90, 0\n" \
  "s_cbranch_scc1 .LCV_F9_%=\n" \
  "s_movk_i32 s87, 400\n" \
  ".LCV_F0_%=:\n" \
  "s_sleep 8\n" \
  "s_mov_b64 exec, 15\n" \
  "buffer_load_dword v58, v2, s[44:47], s68 offen offset:4 sc1\n" \
  "s_mov_b64 exec, -1\n" \
  "s_sub_u32 s87, s87, 1\n" \
  "s_cbranch_scc1 .LCV_F9_%=\n" \
  "s_waitcnt vmcnt(0)\n" \
  "v_cmp_eq_u32 vcc, 0, v58\n" \
  "s_and_b32 vcc_lo, vcc_lo, 15\n" \
  "s_cbranch_scc1 .LCV_F0_%=\n" \
  "v_readlane_b32 vcc_lo, v58, 0\n" \
  "v_readlane_b32 s38, v58, 1\n" \
  "s_max_u32 vcc_lo, vcc_lo, s38\n" \
  "v_readlane_b32 s38, v58, 2\n" \
  "s_max_u32 vcc_lo, vcc_lo, s38\n" \
  "v_readlane_b32 s38, v58, 3\n" \
  "s_max_u32 vcc_lo, vcc_lo, s38\n" \
  ".LCV_F1_%=:\n" \
  "s_sleep 1\n" \
  "s_memrealtime s[38:39]\n" \
  "s_sub_u32 s87, s87, 1\n" \
  "s_cbranch_scc1 .LCV_F9_%=\n" \
  "s_waitcnt lgkmcnt(0)\n" \
  "s_sub_u32 s38, s38, vcc_lo\n" \
  "s_cmp_lt_i32 s38, 0\n" \
  "s_cbranch_scc1 .LCV_F1_%=\n" \
  "s_mov_b32 m0, s99\n" \
  "s_add_u32 s88, s99, 8192\n" \
  "global_load_lds_dwordx4 v10, s[62:63] sc1\n" \
  "global_load_lds_dwordx4 v10, s[62:63] offset:1024 sc1\n" \
  "s_mov_b32 m0, s88\n" \
  "s_nop 0\n" \
  "global_load_lds_dwordx4 v10, s[64:65] sc1\n" \
  "global_load_lds_dwordx4 v10, s[64:65] offset:1024 sc1\n" \
  "s_waitcnt vmcnt(0)\n" \
  "v_add_u32 v53, s99, v10\n" \
  "ds_read_b32 v54, v53 offset:0\n" \
  "ds_read_b32 v55, v53 offset:1024\n" \
  "ds_read_b32 v56, v53 offset:8192\n" \
  "ds_read_b32 v57, v53 offset:9216\n" \
  "s_lshr_b32 s88, s71, 4\n" \
  "s_mov_b32 vcc_lo, 1\n" \
  "s_waitcnt lgkmcnt(0)\n" \
  "v_bitop3_b32 v54, v54, v55, s88 bitop3:0x7e\n" \
  "v_bitop3_b32 v56, v56, v57, s88 bitop3:0x7e\n" \
  "v_bitop3_b32 v54, v54, v56, vcc_lo bitop3:0xa8\n" \
  "v_cmp_ne_u32 vcc, 0, v54\n" \
  "s_cmp_eq_u64 vcc, 0\n" \
  "s_cbranch_scc1 .LCV_W1_%=\n" \
  ".LCV_F9_%=:\n" \
  "s_waitcnt vmcnt(0) lgkmcnt(0)\n" \
  "s_branch .LCV_W7_%=\n"
#define TOUCH(n)
#define PAGE() asm volatile("s_branch .LPG%=\n.p2align 12\n.LPG%=:" ::: "memory")
#define PG 0
#define TOK_AL(p, o, m) ((((unsigned)(size_t)(p) + (unsigned)(o) + (m)) & ~(m)) - (unsigned)(size_t)(p))
#define TOK_QK(p, L) (GEN_TR * (L) ? TOK_AL(p, OFF_QK + SZ_QK, (0x100000u << GEN_TR) - 1) : (unsigned)(OFF_QK + (L) * SZ_QK))
#define TOK_V(p, L) (GEN_TR * (L) ? TOK_AL(p, OFF_VF + SZ_O, (0x80000u << GEN_TR) - 1) : (unsigned)(OFF_VF + (L) * SZ_O))
__device__ __forceinline__ void start_kgen(const MegaArgs& A, char* smem, const int kg) {
  const int w = A.wv, l = lane_id(), tid = 64 * w + l, c = l & 15, g = l >> 4;
  KGen G;
  unsigned tp = 0u;
#define TEXT_ASK()                                                                                                     \
  {                                                                                                                    \
    const char* pc;                                                                                                    \
    unsigned n;                                                                                                        \
    asm volatile("s_getpc_b64 %0\n.LKG_PC:\n\ts_mov_b32 %1, .LKG_LAST-.LKG_PC" : "=s"(pc), "=s"(n));                    \
    const unsigned o = (unsigned)(tid * 8 + ((blockIdx.x >> 3) & 7)) * IPS;                                            \
    if ((int)o < (int)n) asm volatile("global_load_dword %0, %1, %2" : "=v"(tp) : "v"(o), "s"(pc) : "memory");         \
  }
  if (IPF == 2) TEXT_ASK()
  v4u raw[2] = {};
#pragma unroll
  for (int i = 0; i < TP; ++i)
    if (kg < 32) raw[i] = *reinterpret_cast<const v4u*>(A.x0 + (size_t)(TP * (8 * (kg & 7) + w) + i) * DM + l * 8);
  G.load(A, 8 * kg + w, l);
  {
    const __amdgpu_buffer_rsrc_t rz = MKRS(A.ws + OFF_Z, Z_BYTES);
#pragma unroll
    for (int i = 0; i < 4; ++i) {
      const u64 z = i == 0 && kg < (int)(2 * SZ_ACC / 8192) ? ACC_INIT : 0ull;
      ST128((v4u{(unsigned)z, (unsigned)(z >> 32), (unsigned)z, (unsigned)(z >> 32)}), rz, tid * 16, (i * 64 + kg) * 8192);
    }
  }
  const u64 tag = call_tag(A);
  if (TP && kg < 32) {
    unsigned t = row_stats(raw[0]);
    if (TP > 1) {
      const unsigned t1 = row_stats(raw[1]);
      t = l < 2 ? t : t1;
    }
    if (l < 2 * TP) __hip_atomic_store((gu64*)(A.ws + OFF_GT) + 2 * (L_SEQ + TP * (8 * kg + w)) + l, tagged(tag, (l & 1) ? t : t << 16), RLX_AGENT);
  }
  DRAIN();
  __syncthreads();
  if (tid < 8) __hip_atomic_store((gu64*)(A.ws + OFF_RD) + tid * 64 + kg, tag, RLX_AGENT);
  asm volatile("" : "+v"(G.pa), "+v"(G.pb), "+v"(G.pe));
  G.setup();
  G.st = smem + w * 16640;
  const __amdgpu_buffer_rsrc_t rs = MKRS(A.ws + OFF_KF + (8 * kg + w) * 2048, 386 * 4);
  SST(1);
  unsigned tw = 0u;
#define TLB_ASK()                                                                                                      \
  if (tid < 384) {                                                                                                     \
    const unsigned o = (unsigned)OFF_XA + (unsigned)(tid * 8 + ((blockIdx.x >> 3) & 7)) * 65536u;                      \
    if (o < WS_TOTAL) asm volatile("global_load_dword %0, %1, %2" : "=v"(tw) : "v"(o), "s"(A.ws) : "memory");          \
  }
#define X2_CLEAR() \
  _Pragma("unroll") for (int i = 0; i < 4; ++i) ST128((v4u{0u, 0u, 0u, 0u}), MKRS(A.ws + OFF_X2, SZ_X), tid * 16, (4 * kg + i) * 8192);
  if (XZP == 1) { X2_CLEAR() }
  if (IPF == 1) TEXT_ASK()
  if (TLBW == 1) TLB_ASK()
  unsigned pf[6] = {};
#define PF_ASK(M)                                                                                                                  \
  if (kg < 32) {                                                                                                                   \
    if ((M) & 1) asm volatile("global_load_dword %0, %2, %3\n\tglobal_load_dword %1, %2, %3 offset:128" : "=&v"(pf[0]), "=&v"(pf[1]) : "v"(kg * 131072 + tid * 256), "s"(A.Wo)); \
    if ((M) & 2) asm volatile("global_load_dword %0, %2, %3\n\tglobal_load_dword %1, %2, %3 offset:128" : "=&v"(pf[2]), "=&v"(pf[3]) : "v"(kg * 256 + tid * 8192), "s"(A.Wq)); \
    if ((M) & 4) asm volatile("global_load_dword %0, %2, %3\n\tglobal_load_dword %1, %2, %3 offset:128" : "=&v"(pf[4]), "=&v"(pf[5]) : "v"(kg * 256 + tid * 8192), "s"(A.Wk)); \
  }
  PF_ASK(PFA)
  G.columns(l, 0, 16);
  G.get(G.bh, G.bl, l);
  if (XZP == 2) { X2_CLEAR() }
  PF_ASK(PFB)
  if (TLBW == 2) TLB_ASK()
  (void)G.scale();
  const Cx zb = G.power(256.f);
  Cx b = G.C, z = G.power(2.f);
#pragma unroll 1
  for (int f = 1; f < 9;) {
    char* p = G.st + l * 8;
    const int fe = f == 1 ? 4 : 9;
#pragma unroll 1
    for (; f < fe; ++f) {
      b = cmul(b, zb);
      if (f == 2 || f == 4) z = cmul(z, z);
      Cx y = b;
      p = G.rows(p, y, z, f == 1 ? 8 : f < 4 ? 4 : 2);
    }
    const f32x4 d = G.tile<false>(l);
    const v4u r = {pkh(d.x, d.x), pkh(d.y, d.y), pkh(d.z, d.z), pkh(d.w, d.w)};
    if (f == 4) {
      ST128(r, rs, (g < 2 ? 8 * c + 4 * g : 64 * g + 4 * c) * 4, 0);
    } else {
      ST64((v2u{r.x, r.y}), rs, (256 + 64 * g + 2 * c) * 4, 0);
      ST64((v2u{r.z, r.w}), rs, (288 + 64 * g + 2 * c) * 4, 0);
    }
  }
  SST(2);
  DRAIN();
  asm volatile("" ::"v"(tp), "v"(tw), "v"(pf[0]), "v"(pf[1]), "v"(pf[2]), "v"(pf[3]), "v"(pf[4]), "v"(pf[5]));
  if (l == 0) __hip_atomic_store((gu64*)(A.ws + OFF_KG) + kg * 16 + w, tag, RLX_AGENT);
  if (XZP == 3) { X2_CLEAR() }
  const u64 rv = (XZP & 3) && w == 0 ? ready_look(A) : 0;
  if (TLBW == 3) {
    TLB_ASK()
    DRAIN();
    asm volatile("" ::"v"(tw));
  }
  DRAIN();
  __syncthreads();
  if ((XZP & 3) && w == 0) {
    ready_wait(A, tag, rv);
    if (l < 8) set_flag((unsigned*)(A.ws + OFF_FL) + FL_FZ + l * 64 + kg);
  }
  SST(3);
}

constexpr int C_SM = 131072, C_SO = C_SM + 8192, C_KT = C_SO + 2048, C_X = C_KT + 16384, C_SQ = C_X + 1024;
static_assert(C_SQ + 4096 <= 163840 && C_SQ % 512 == 0, "lds");
__device__ __forceinline__ v4u mk_srd(const void* p, const unsigned bytes) {
  const unsigned long long a = (unsigned long long)p;
  return v4u{(unsigned)a, (unsigned)(a >> 32) & 0xffffu, bytes, 0x00020000u};
}
template <bool ATT>
__device__ __forceinline__ void role_chain_t(const MegaArgs& A, char* smem, const int L, const int h, const int rep) {
  const int w = A.wv;
  char* ws = A.ws;
  unsigned* FL = (unsigned*)(ws + OFF_FL);
  const int l = lane_id(), c = l & 15, g = l >> 4;
  unsigned* FQ = FL + FL_FQ + L * 64 * 32 * 32;
  const int wb = (w + ROT_H) & 7, tb = ROT_T;
  const unsigned sb = lds_base(smem);
  glds16(A.mem0, (unsigned)((c & 7) * DM + 64 * w + 16 * g + 8 * (c >> 3)) * 2, sb + C_SM + w * 1024);
#pragma unroll
  for (int i = 0; i < 16; ++i)
    glds16((const char*)A.Wo + (size_t)(h * DH + 32 * (i >> 2) + 4 * w + (i & 2)) * (DM * 2),
           (unsigned)((i & 1) * 1024 + ((l ^ (((i & 1) | ((w & 3) << 1)) << 1)) << 4)), sb + w * 16384 + i * 1024, WNT & 1);
  const u64 tag = call_tag(A);
  const char* gK = ws + TOK_QK(ws, L) + ((size_t)h * 16 + 2 * w) * 1024;
  const char* gV = ws + TOK_V(ws, L) + ((size_t)h * 8 + 2 * w - 8) * 1024;
  v8u pv, pw;
  {
    const unsigned voff = g * 256 + c * 8, wp = sb + w * 16384 + l * 16;
    pv[0] = (c & 8) ? 0xEEEE : 0x4444;
    pv[1] = sb + C_SM + l * 16;
    const unsigned ra = sb + C_SQ + (g >> 1) * 512 + (c ^ ((g & 1) * 18)) * 8;
    pv[2] = ATT ? ra : voff;
    pv[3] = ATT ? ra ^ 0x108 : wp;
    pv[4] = sb + ((c & 2) ? C_SQ : C_SO) + w * 256 + (((c & 5) | ((c >> 2) & 2)) + 8 * (g >> 1)) * 16 + 8 * ((g ^ (c >> 1)) & 1);
    pv[5] = voff;
    pv[6] = wp;
    pv[7] = sb + C_SO + (l >> 5) * 1024 + w * 256 + (l & 31) * 8;
    const int q = (l >> 2) & 3, p = l & 3;
    const int f = (q & 1) | ((g & 1) << 1) | ((q >> 1) << 2);
#pragma unroll
    for (int i = 0; i < 4; ++i) pw[i] = (4 * i + g) * (HD * 2) + ((c ^ (((g & 1) | (i << 1)) << 1)) << 4) - 1024 * i;
    pw[4] = sb + (g >> 1) * 16384 + (4 * (g & 1) + (q & 1) + 8 * (q >> 1)) * 256 + (w ^ f) * 32 + p * 8;
    pw[5] = sb + (4 * (g >> 1) + 2 * (q >> 1) + (g & 1)) * 16384 + (q & 1) * 1024 + p * 8 + ((4 * wb) ^ f ^ tb) * 32;
    pw[6] = pw[7] = 0u;
  }
  const size_t wo = (size_t)(16 * w) * HD + h * DH;
#define POL 2
#define PGW(w) (POL == 2 ? (w) * 4096 : ((w) & 1) * 4096 + ((w) >> 1) * 16384)
  const v4u rsA = mk_srd(ws + OFF_ACC + L * SZ_ACC + (POL == 1 ? 8192 : 0), SZ_ACC - 8192);
  const v4u rsF = mk_srd(FQ, 64 * 32 * 32 * 4);
  const v4u rsMK = mk_srd(ws + OFF_MEMK + L * SZ_MEM, SZ_MEM);
  const v4u rsMV = mk_srd(ws + OFF_MEMV + L * SZ_MEM, SZ_MEM);
  const v4u pAB = (v4u{0u, 2048u, 3072u, 1024u} ^ (1536u * tb)) + PGW(wb);
  const v4u rsT = mk_srd(FL + FL_FK + L * 32, 64 * 1024 - 128);
  unsigned sc[8];
  sc[0] = h * 128;
  sc[1] = (h * 4 + w - 4) * 512;
  sc[2] = (h * 4 + w - 4) * 512;
  sc[3] = NSEG - 1;
  sc[5] = w;
  sc[6] = sb;
  const char* dbg = ws;
#define CHAIN_OPS                                                                                                          \
  ::"{v[0:7]}"(pv), "{v[24:31]}"(pw), "{s[4:5]}"(A.Wv + wo), "{s[6:7]}"(A.Wq + wo), "{s[8:9]}"(A.Wk + wo), "{s[10:11]}"(tag),         \
      "{s[12:13]}"(ws + OFF_RD + (blockIdx.x & 7) * 512), "{s[14:15]}"(ws + OFF_G + (blockIdx.x & 7) * 256), "{s[16:17]}"(ws + OFF_G + h * 8), \
      "{s18}"(w == 4 && L == 0 ? 255 : 0), "{s19}"(sb + w * 16384), "{s[20:21]}"(ws + WS_TOTAL + (w == 0 ? 534 * 256 + A.vb : 610 * 256) * 8), \
      "{s[40:43]}"(rsA), "{s[44:47]}"(rsF), "{s[48:51]}"(rsMK), "{s[52:55]}"(rsMV), "{s[56:59]}"(pAB), "{s60}"(PGW(w)), "{s61}"(ATT ? PGW(h >> 2) + (h & 3) * 1024 : L), \
      "{s[62:63]}"(gK), "{s[64:65]}"(gV), "{s66}"(0), "{s67}"(w == 4 ? (h >> 3) * 256 + (h & 7) * 8 : 0x40000000), "{s68}"(sc[0]), "{s69}"(sc[1]), "{s70}"(sc[2]),    \
      "{s71}"(sc[3]), "{s73}"(sc[5]), "{s74}"(sc[6]), "{s[36:37]}"(dbg),                   \
      "{s[0:3]}"(rsT)                                                                                                      \
      : "memory", "vcc", "scc", "s99", "v255"
  if (ATT) asm volatile("s_mov_b64 exec, -1\ns_nop 4\n" CHAIN_ASM_ATT ".LKG_LAS2:\n" CHAIN_OPS);
  else asm volatile("s_mov_b64 exec, -1\ns_nop 4\n" CHAIN_ASM_VO CHAIN_OPS);
#undef CHAIN_OPS
}

__device__ __forceinline__ void role_chain(const MegaArgs& A, char* smem, const int L, const int h, const int rep) {
  if (A.wv < 4) role_chain_t<true>(A, smem, L, h, rep);
  else role_chain_t<false>(A, smem, L, h, rep);
}

constexpr int AW_SO = 0, AW_TK = 32768, AW_CNT = AW_TK + 98304;
__device__ __forceinline__ unsigned umin3(unsigned a, unsigned b, unsigned c) {
  return __builtin_elementwise_min(__builtin_elementwise_min(a, b), c);
}
#define PW_FREE(L_, s_, i_) \
  if (l == 0) set_flag(FL + FL_FD + ((s_) * 2 + (L_)) * 32 + (i_))
#define AW_ASM_X \
  "s_mov_b32 s61, 0\n" \
  "s_mov_b32 s67, 1\n" \
  "s_mov_b32 s68, 0\n" \
  "s_mov_b32 s70, 0\n" \
  "s_mov_b32 s73, 0\n" \
  "s_mov_b32 s71, 0x3e0293ee\n" \
  "s_mov_b32 s72, 0xbe0293ee\n" \
  "s_mov_b32 s65, 49152\n" \
  "s_bitcmp0_b32 s58, 2\n" \
  "s_cbranch_scc1 .LCX_OWN_%=\n" \
  "v_swap_b32 v192, v208\n" \
  "v_swap_b32 v193, v209\n" \
  "v_swap_b32 v194, v210\n" \
  "v_swap_b32 v195, v211\n" \
  "v_swap_b32 v196, v212\n" \
  "v_swap_b32 v197, v213\n" \
  "v_swap_b32 v198, v214\n" \
  "v_swap_b32 v199, v215\n" \
  "v_swap_b32 v200, v216\n" \
  "v_swap_b32 v201, v217\n" \
  "v_swap_b32 v202, v218\n" \
  "v_swap_b32 v203, v219\n" \
  "v_swap_b32 v204, v220\n" \
  "v_swap_b32 v205, v221\n" \
  "v_swap_b32 v206, v222\n" \
  "v_swap_b32 v207, v223\n" \
  "v_swap_b32 v224, v240\n" \
  "v_swap_b32 v225, v241\n" \
  "v_swap_b32 v226, v242\n" \
  "v_swap_b32 v227, v243\n" \
  "v_swap_b32 v228, v244\n" \
  "v_swap_b32 v229, v245\n" \
  "v_swap_b32 v230, v246\n" \
  "v_swap_b32 v231, v247\n" \
  "v_swap_b32 v232, v248\n" \
  "v_swap_b32 v233, v249\n" \
  "v_swap_b32 v234, v250\n" \
  "v_swap_b32 v235, v251\n" \
  "v_swap_b32 v236, v252\n" \
  "v_swap_b32 v237, v253\n" \
  "v_swap_b32 v238, v254\n" \
  "v_swap_b32 v239, v255\n" \
  ".LCX_OWN_%=:\n" \
  "v_mov_b32 v38, s58\n" \
  "s_memrealtime s[38:39]\n" \
  "s_waitcnt lgkmcnt(0)\n" \
  "s_mov_b32 s69, s38\n" \
  "s_setprio 3\n" \
  ".p2align 6\n" \
  ".LCX_TOP_%=:\n" \
  "s_mov_b32 s62, 0x200000\n" \
  "s_cmp_ge_u32 s73, s67\n" \
  "s_cbranch_scc1 .LCX_TOK_%=\n" \
  "s_cmp_eq_u32 s61, 0\n" \
  "s_cbranch_scc1 .LCX_SPIN_%=\n" \
  ".LCX_RE_%=:\n" \
  "ds_read_b32 v37, v38\n" \
  "s_add_u32 s62, s62, 1\n" \
  "s_waitcnt lgkmcnt(0)\n" \
  "v_readfirstlane_b32 s73, v37\n" \
  "s_bfe_u32 s64, s73, 0x80008\n" \
  "s_and_b32 s73, s73, 0xff\n" \
  "s_min_u32 s73, s73, s64\n" \
  "s_cmp_ge_u32 s73, s67\n" \
  "s_cbranch_scc1 .LCX_TOK_%=\n" \
  "s_sleep 1\n" \
  "s_cmp_lt_u32 s62, 2097158\n" \
  "s_cbranch_scc1 .LCX_RE_%=\n" \
  ".LCX_SPIN_%=:\n" \
  "s_sleep 2\n" \
  "s_memrealtime s[38:39]\n" \
  "ds_read_b32 v37, v38\n" \
  "s_sub_u32 s62, s62, 1\n" \
  "s_cbranch_scc1 .LCX_END_%=\n" \
  "s_waitcnt lgkmcnt(0)\n" \
  "v_readfirstlane_b32 s73, v37\n" \
  "s_add_u32 s38, s38, 80\n" \
  "s_bfe_u32 s64, s73, 0x80008\n" \
  "s_and_b32 s73, s73, 0xff\n" \
  "s_min_u32 s73, s73, s64\n" \
  "s_cmp_ge_u32 s73, s67\n" \
  "s_cbranch_scc0 .LCX_SPIN_%=\n" \
  "s_cmp_eq_u32 s70, 0\n" \
  "s_cbranch_scc1 .LCX_TOK_%=\n" \
  "s_sub_u32 s63, s38, s69\n" \
  "s_max_i32 s63, s63, 0\n" \
  "s_add_u32 s69, s69, s63\n" \
  ".LCX_TOK_%=:\n" \
  "ds_read_b128 v[48:51], v6 offset:0\n" \
  "ds_read_b128 v[64:67], v0 offset:8192\n" \
  "ds_read_b128 v[80:83], v0 offset:12288\n" \
  "ds_read_b128 v[52:55], v6 offset:1024\n" \
  "ds_read_b128 v[68:71], v0 offset:9216\n" \
  "ds_read_b128 v[84:87], v0 offset:13312\n" \
  "ds_read_b128 v[56:59], v6 offset:2048\n" \
  "ds_read_b128 v[72:75], v0 offset:10240\n" \
  "ds_read_b128 v[88:91], v0 offset:14336\n" \
  "ds_read_b128 v[60:63], v6 offset:3072\n" \
  "ds_read_b128 v[76:79], v0 offset:11264\n" \
  "ds_read_b128 v[92:95], v0 offset:15360\n" \
  "s_waitcnt lgkmcnt(9)\n" \
  "v_mfma_f32_16x16x32_bf16 v[20:23], v[64:67], v[48:51], v[8:11]\n" \
  "v_mfma_f32_16x16x32_bf16 v[24:27], v[80:83], v[48:51], v[12:15]\n" \
  "s_memrealtime s[38:39]\n" \
  "s_waitcnt lgkmcnt(6)\n" \
  "v_mfma_f32_16x16x32_bf16 v[20:23], v[68:71], v[52:55], v[20:23]\n" \
  "v_mfma_f32_16x16x32_bf16 v[24:27], v[84:87], v[52:55], v[24:27]\n" \
  "s_waitcnt lgkmcnt(3)\n" \
  "v_mfma_f32_16x16x32_bf16 v[20:23], v[72:75], v[56:59], v[20:23]\n" \
  "v_mfma_f32_16x16x32_bf16 v[24:27], v[88:91], v[56:59], v[24:27]\n" \
  "s_waitcnt lgkmcnt(0)\n" \
  "v_mfma_f32_16x16x32_bf16 v[20:23], v[76:79], v[60:63], v[20:23]\n" \
  "v_mfma_f32_16x16x32_bf16 v[24:27], v[92:95], v[60:63], v[24:27]\n" \
  "s_sub_u32 s63, s38, s69\n" \
  "s_add_u32 s63, s63, 8\n" \
  "s_or_b32 s63, s63, s68\n" \
  "s_cmp_lt_i32 s63, 0\n" \
  "s_cbranch_scc1 .LCX_D2_%=\n" \
  "s_setprio 3\n" \
  "v_mov_b32 v35, 0\n" \
  "global_load_dwordx4 v[128:131], v5, s[48:49] offset:0 sc1\n" \
  "global_load_dwordx4 v[132:135], v5, s[48:49] offset:1024 sc1\n" \
  "global_load_dwordx4 v[136:139], v3, s[48:49] offset:0 sc1\n" \
  "global_load_dwordx4 v[140:143], v3, s[48:49] offset:512 sc1\n" \
  "global_load_dword v35, v35, s[50:51] sc1\n" \
  "global_load_dword v146, v2, s[52:53] sc1\n" \
  "s_setprio 0\n" \
  "s_mov_b32 s68, -2\n" \
  ".LCX_D2_%=:\n" \
  "ds_read_b128 v[96:99], v0 offset:16384\n" \
  "ds_read_b128 v[100:103], v0 offset:17408\n" \
  "ds_read_b128 v[104:107], v0 offset:18432\n" \
  "ds_read_b128 v[108:111], v0 offset:19456\n" \
  "ds_read_b128 v[112:115], v0 offset:20480\n" \
  "ds_read_b128 v[116:119], v0 offset:21504\n" \
  "ds_read_b128 v[120:123], v0 offset:22528\n" \
  "ds_read_b128 v[124:127], v0 offset:23552\n" \
  "s_add_u32 s67, s67, 1\n" \
  "v_add_u32 v0, s65, v0\n" \
  "v_add_u32 v6, s65, v6\n" \
  "s_sub_u32 s65, 0, s65\n" \
  "s_nop 1\n" \
  "v_max3_f32 v32, v20, v21, v22\n" \
  "v_max3_f32 v33, v23, v24, v25\n" \
  "v_max3_f32 v32, v32, v26, v27\n" \
  "v_max_f32 v32, v32, v33\n" \
  "s_memrealtime s[38:39]\n" \
  "v_mov_b32 v33, v32\n" \
  "s_nop 1\n" \
  "v_permlane16_swap_b32 v32, v33\n" \
  "v_max_f32 v32, v32, v33\n" \
  "v_mov_b32 v33, v32\n" \
  "s_nop 1\n" \
  "v_permlane32_swap_b32 v32, v33\n" \
  "v_max_f32 v32, v32, v33\n" \
  "s_waitcnt lgkmcnt(0)\n" \
  "s_sub_u32 s63, s38, s69\n" \
  "s_add_u32 s63, s63, 8\n" \
  "s_or_b32 s63, s63, s68\n" \
  "s_cmp_lt_i32 s63, 0\n" \
  "s_cbranch_scc1 .LCX_D3_%=\n" \
  "s_setprio 3\n" \
  "v_mov_b32 v35, 0\n" \
  "global_load_dwordx4 v[128:131], v5, s[48:49] offset:0 sc1\n" \
  "global_load_dwordx4 v[132:135], v5, s[48:49] offset:1024 sc1\n" \
  "global_load_dwordx4 v[136:139], v3, s[48:49] offset:0 sc1\n" \
  "global_load_dwordx4 v[140:143], v3, s[48:49] offset:512 sc1\n" \
  "global_load_dword v35, v35, s[50:51] sc1\n" \
  "global_load_dword v146, v2, s[52:53] sc1\n" \
  "s_setprio 0\n" \
  "s_mov_b32 s68, -2\n" \
  ".LCX_D3_%=:\n" \
  "v_mul_f32 v33, s72, v32\n" \
  "v_fma_f32 v20, v20, s71, v33\n" \
  "v_fma_f32 v21, v21, s71, v33\n" \
  "v_fma_f32 v22, v22, s71, v33\n" \
  "v_fma_f32 v23, v23, s71, v33\n" \
  "v_fma_f32 v24, v24, s71, v33\n" \
  "v_fma_f32 v25, v25, s71, v33\n" \
  "v_fma_f32 v26, v26, s71, v33\n" \
  "v_fma_f32 v27, v27, s71, v33\n" \
  "v_exp_f32 v20, v20\n" \
  "v_exp_f32 v21, v21\n" \
  "v_exp_f32 v22, v22\n" \
  "v_exp_f32 v23, v23\n" \
  "v_exp_f32 v24, v24\n" \
  "v_exp_f32 v25, v25\n" \
  "v_exp_f32 v26, v26\n" \
  "v_exp_f32 v27, v27\n" \
  "v_add_f32 v34, v20, v21\n" \
  "v_add_f32 v33, v22, v23\n" \
  "v_add_f32 v36, v24, v25\n" \
  "v_add_f32 v34, v34, v33\n" \
  "v_add_f32 v33, v26, v27\n" \
  "v_add_f32 v34, v34, v36\n" \
  "v_add_f32 v34, v34, v33\n" \
  "v_mov_b64 v[28:29], 0\n" \
  "v_mov_b64 v[30:31], 0\n" \
  "s_waitcnt vmcnt(2) lgkmcnt(0)\n" \
  "s_cmp_gt_i32 s68, -2\n" \
  "s_cbranch_scc1 .LCX_NP_%=\n" \
  "s_waitcnt vmcnt(0)\n" \
  ".LCX_NP_%=:\n" \
  "s_cmp_lg_u32 s68, 0\n" \
  "s_cbranch_scc1 .LCX_TEST_%=\n" \
  ".LCX_WF_%=:\n" \
  "s_memrealtime s[38:39]\n" \
  "s_waitcnt lgkmcnt(0)\n" \
  "s_sub_u32 s63, s69, s38\n" \
  "s_cmp_le_i32 s63, 0\n" \
  "s_cbranch_scc1 .LCX_GO_%=\n" \
  "s_cmp_lt_i32 s63, 12\n" \
  "s_cbranch_scc1 .LCX_WF_%=\n" \
  "s_sleep 3\n" \
  "s_branch .LCX_WF_%=\n" \
  ".LCX_GO_%=:\n" \
  "v_mov_b32 v35, 0\n" \
  "global_load_dwordx4 v[128:131], v5, s[48:49] offset:0 sc1\n" \
  "global_load_dwordx4 v[132:135], v5, s[48:49] offset:1024 sc1\n" \
  "global_load_dwordx4 v[136:139], v3, s[48:49] offset:0 sc1\n" \
  "global_load_dwordx4 v[140:143], v3, s[48:49] offset:512 sc1\n" \
  "global_load_dword v35, v35, s[50:51] sc1\n" \
  "global_load_dword v146, v2, s[52:53] sc1\n" \
  "s_sub_u32 s62, s62, 8\n" \
  "s_cbranch_scc1 .LCX_END_%=\n" \
  "s_waitcnt vmcnt(0)\n" \
  ".LCX_TEST_%=:\n" \
  "s_setprio 3\n" \
  "v_smfmac_f32_16x16x64_bf16 v[28:31], v[128:131], v[48:55], v1\n" \
  "v_smfmac_f32_16x16x64_bf16 v[28:31], v[132:135], v[56:63], v1\n" \
  "v_min3_u32 v33, v128, v129, v130\n" \
  "v_min3_u32 v36, v131, v132, v133\n" \
  "v_min3_u32 v33, v33, v134, v135\n" \
  "v_min3_u32 v36, v36, v136, v137\n" \
  "v_min3_u32 v33, v33, v138, v139\n" \
  "v_min3_u32 v36, v36, v140, v141\n" \
  "v_min3_u32 v33, v33, v142, v143\n" \
  "v_min3_u32 v33, v33, v36, v146\n" \
  "v_cmp_eq_u32 vcc, 0, v33\n" \
  "v_readfirstlane_b32 s64, v35\n" \
  "s_mov_b32 s68, 0\n" \
  "s_cmp_eq_u64 vcc, 0\n" \
  "s_cbranch_scc0 .LCX_FAIL_%=\n" \
  "v_permlane32_swap_b32 v28, v30\n" \
  "v_permlane32_swap_b32 v29, v31\n" \
  "v_mov_b32 v33, s67\n" \
  "ds_write_b32 v38, v33 offset:16\n" \
  "v_add_f32 v28, v28, v30\n" \
  "v_add_f32 v29, v29, v31\n" \
  "v_max3_f32 v36, v28, v29, v32\n" \
  "v_mov_b32 v33, v36\n" \
  "s_cmp_eq_u32 s64, 0\n" \
  "s_cselect_b32 s64, s38, s64\n" \
  "v_permlane16_swap_b32 v36, v33\n" \
  "v_max_f32 v36, v36, v33\n" \
  "v_mov_b32 v33, v36\n" \
  "s_sub_u32 s63, s64, s70\n" \
  "s_mov_b32 s70, s64\n" \
  "v_permlane32_swap_b32 v36, v33\n" \
  "v_max_f32 v36, v36, v33\n" \
  "v_mul_f32 v36, s72, v36\n" \
  "v_fma_f32 v32, v32, s71, v36\n" \
  "v_fma_f32 v28, v28, s71, v36\n" \
  "v_fma_f32 v29, v29, s71, v36\n" \
  "v_exp_f32 v32, v32\n" \
  "v_exp_f32 v28, v28\n" \
  "v_exp_f32 v29, v29\n" \
  "v_fma_f32 v34, v34, v32, v28\n" \
  "v_add_f32 v34, v34, v29\n" \
  "v_mov_b32 v33, v34\n" \
  "s_min_u32 s63, s63, 250\n" \
  "s_add_u32 s69, s64, s63\n" \
  "v_permlane16_swap_b32 v34, v33\n" \
  "v_add_f32 v34, v34, v33\n" \
  "v_mov_b32 v33, v34\n" \
  "s_add_u32 s69, s69, -52\n" \
  "s_add_u32 s48, s48, 0x10000\n" \
  "s_addc_u32 s49, s49, 0\n" \
  "v_permlane32_swap_b32 v34, v33\n" \
  "v_add_f32 v34, v34, v33\n" \
  "v_rcp_f32 v34, v34\n" \
  "s_add_u32 s50, s50, 1024\n" \
  "s_addc_u32 s51, s51, 0\n" \
  "s_add_u32 s52, s52, 256\n" \
  "s_addc_u32 s53, s53, 0\n" \
  "v_mul_f32 v32, v32, v34\n" \
  "v_mul_f32 v20, v20, v32\n" \
  "v_mul_f32 v21, v21, v32\n" \
  "v_mul_f32 v22, v22, v32\n" \
  "v_mul_f32 v23, v23, v32\n" \
  "v_mul_f32 v24, v24, v32\n" \
  "v_mul_f32 v25, v25, v32\n" \
  "v_mul_f32 v26, v26, v32\n" \
  "v_mul_f32 v27, v27, v32\n" \
  "v_cvt_pk_bf16_f32 v40, v20, v21\n" \
  "v_cvt_pk_bf16_f32 v41, v22, v23\n" \
  "v_cvt_pk_bf16_f32 v42, v24, v25\n" \
  "v_cvt_pk_bf16_f32 v43, v26, v27\n" \
  "v_mul_f32 v28, v28, v34\n" \
  "v_mul_f32 v29, v29, v34\n" \
  "v_mfma_f32_16x16x32_bf16 v[64:67], v[96:99], v[40:43], 0\n" \
  "v_cvt_pk_bf16_f32 v44, v28, v29\n" \
  "v_mov_b32 v45, 0\n" \
  "v_mfma_f32_16x16x32_bf16 v[68:71], v[100:103], v[40:43], 0\n" \
  "v_cvt_pk_bf16_f32 v47, v28, v29\n" \
  "v_mov_b32 v46, 0\n" \
  "v_mfma_f32_16x16x32_bf16 v[72:75], v[104:107], v[40:43], 0\n" \
  "v_permlane32_swap_b32 v44, v45\n" \
  "v_mfma_f32_16x16x32_bf16 v[76:79], v[108:111], v[40:43], 0\n" \
  "v_permlane32_swap_b32 v46, v47\n" \
  "v_mfma_f32_16x16x32_bf16 v[80:83], v[112:115], v[40:43], 0\n" \
  "v_xor_b32 v16, 4096, v7\n" \
  "v_mfma_f32_16x16x32_bf16 v[84:87], v[116:119], v[40:43], 0\n" \
  "v_xor_b32 v17, 8192, v7\n" \
  "v_mfma_f32_16x16x32_bf16 v[88:91], v[120:123], v[40:43], 0\n" \
  "v_xor_b32 v18, 12288, v7\n" \
  "v_mfma_f32_16x16x32_bf16 v[92:95], v[124:127], v[40:43], 0\n" \
  "s_memrealtime s[38:39]\n" \
  "ds_read_b32 v37, v38\n" \
  "v_mfma_f32_16x16x16_bf16 v[64:67], v[136:137], v[44:45], v[64:67]\n" \
  "v_mfma_f32_16x16x16_bf16 v[68:71], v[140:141], v[44:45], v[68:71]\n" \
  "v_mfma_f32_16x16x16_bf16 v[72:75], v[136:137], v[46:47], v[72:75]\n" \
  "v_mfma_f32_16x16x16_bf16 v[76:79], v[140:141], v[46:47], v[76:79]\n" \
  "v_mfma_f32_16x16x16_bf16 v[80:83], v[138:139], v[44:45], v[80:83]\n" \
  "v_mfma_f32_16x16x16_bf16 v[84:87], v[142:143], v[44:45], v[84:87]\n" \
  "v_mfma_f32_16x16x16_bf16 v[88:91], v[138:139], v[46:47], v[88:91]\n" \
  "v_mfma_f32_16x16x16_bf16 v[92:95], v[142:143], v[46:47], v[92:95]\n" \
  "s_nop 0\n" \
  "v_cvt_pk_bf16_f32 v160, v64, v65\n" \
  "v_cvt_pk_bf16_f32 v161, v66, v67\n" \
  "v_cvt_pk_bf16_f32 v162, v68, v69\n" \
  "v_cvt_pk_bf16_f32 v163, v70, v71\n" \
  "ds_write_b128 v7, v[160:163] offset:0\n" \
  "v_cvt_pk_bf16_f32 v164, v72, v73\n" \
  "v_cvt_pk_bf16_f32 v165, v74, v75\n" \
  "v_cvt_pk_bf16_f32 v166, v76, v77\n" \
  "v_cvt_pk_bf16_f32 v167, v78, v79\n" \
  "ds_write_b128 v7, v[164:167] offset:1024\n" \
  "v_cvt_pk_bf16_f32 v168, v80, v81\n" \
  "v_cvt_pk_bf16_f32 v169, v82, v83\n" \
  "v_cvt_pk_bf16_f32 v170, v84, v85\n" \
  "v_cvt_pk_bf16_f32 v171, v86, v87\n" \
  "ds_write_b128 v7, v[168:171] offset:2048\n" \
  "v_cvt_pk_bf16_f32 v172, v88, v89\n" \
  "v_cvt_pk_bf16_f32 v173, v90, v91\n" \
  "v_cvt_pk_bf16_f32 v174, v92, v93\n" \
  "v_cvt_pk_bf16_f32 v175, v94, v95\n" \
  "ds_write_b128 v7, v[172:175] offset:3072\n" \
  "v_xor_b32 v7, 0x4000, v7\n" \
  "v_mfma_f32_16x16x32_bf16 v[112:115], v[192:195], v[160:163], 0\n" \
  "v_mfma_f32_16x16x32_bf16 v[116:119], v[224:227], v[160:163], 0\n" \
  "s_waitcnt lgkmcnt(0)\n" \
  "s_barrier\n" \
  "v_mfma_f32_16x16x32_bf16 v[112:115], v[196:199], v[164:167], v[112:115]\n" \
  "ds_read_b128 v[64:67], v16 offset:0\n" \
  "ds_read_b128 v[68:71], v16 offset:1024\n" \
  "ds_read_b128 v[72:75], v16 offset:2048\n" \
  "ds_read_b128 v[76:79], v16 offset:3072\n" \
  "s_sub_u32 s59, s60, 1\n" \
  "s_bfe_u32 s59, s59, 0x10003\n" \
  "v_readfirstlane_b32 s73, v37\n" \
  "s_sub_u32 s63, s38, s69\n" \
  "s_bfe_u32 s64, s73, 0x80010\n" \
  "s_sub_u32 s64, s64, s67\n" \
  "s_or_b32 s63, s63, s64\n" \
  "s_bfe_u32 s64, s73, 0x80008\n" \
  "s_and_b32 s73, s73, 0xff\n" \
  "s_min_u32 s73, s73, s64\n" \
  "s_or_b32 s63, s63, s68\n" \
  "s_cmp_lt_i32 s63, 0\n" \
  "s_cbranch_scc1 .LCX_E4_%=\n" \
  "v_mov_b32 v35, 0\n" \
  "global_load_dwordx4 v[128:131], v5, s[48:49] offset:0 sc1\n" \
  "global_load_dwordx4 v[132:135], v5, s[48:49] offset:1024 sc1\n" \
  "global_load_dwordx4 v[136:139], v3, s[48:49] offset:0 sc1\n" \
  "global_load_dwordx4 v[140:143], v3, s[48:49] offset:512 sc1\n" \
  "global_load_dword v35, v35, s[50:51] sc1\n" \
  "global_load_dword v146, v2, s[52:53] sc1\n" \
  "s_mov_b32 s68, -1\n" \
  ".LCX_E4_%=:\n" \
  "v_mfma_f32_16x16x32_bf16 v[116:119], v[228:231], v[164:167], v[116:119]\n" \
  "v_mfma_f32_16x16x32_bf16 v[112:115], v[200:203], v[168:171], v[112:115]\n" \
  "v_mfma_f32_16x16x32_bf16 v[116:119], v[232:235], v[168:171], v[116:119]\n" \
  "v_mfma_f32_16x16x32_bf16 v[112:115], v[204:207], v[172:175], v[112:115]\n" \
  "v_mfma_f32_16x16x32_bf16 v[116:119], v[236:239], v[172:175], v[116:119]\n" \
  "s_waitcnt lgkmcnt(3)\n" \
  "v_mfma_f32_16x16x32_bf16 v[120:123], v[192:195], v[64:67], 0\n" \
  "v_mfma_f32_16x16x32_bf16 v[124:127], v[224:227], v[64:67], 0\n" \
  "ds_read_b128 v[80:83], v17 offset:0\n" \
  "s_waitcnt lgkmcnt(3)\n" \
  "v_mfma_f32_16x16x32_bf16 v[120:123], v[196:199], v[68:71], v[120:123]\n" \
  "v_mfma_f32_16x16x32_bf16 v[124:127], v[228:231], v[68:71], v[124:127]\n" \
  "ds_read_b128 v[84:87], v17 offset:1024\n" \
  "s_waitcnt lgkmcnt(3)\n" \
  "v_mfma_f32_16x16x32_bf16 v[120:123], v[200:203], v[72:75], v[120:123]\n" \
  "v_mfma_f32_16x16x32_bf16 v[124:127], v[232:235], v[72:75], v[124:127]\n" \
  "ds_read_b128 v[88:91], v17 offset:2048\n" \
  "s_waitcnt lgkmcnt(3)\n" \
  "v_mfma_f32_16x16x32_bf16 v[120:123], v[204:207], v[76:79], v[120:123]\n" \
  "v_mfma_f32_16x16x32_bf16 v[124:127], v[236:239], v[76:79], v[124:127]\n" \
  "ds_read_b128 v[92:95], v17 offset:3072\n" \
  "s_waitcnt lgkmcnt(3)\n" \
  "v_mfma_f32_16x16x32_bf16 v[112:115], v[208:211], v[80:83], v[112:115]\n" \
  "v_mfma_f32_16x16x32_bf16 v[116:119], v[240:243], v[80:83], v[116:119]\n" \
  "ds_read_b128 v[96:99], v18 offset:0\n" \
  "s_waitcnt lgkmcnt(3)\n" \
  "v_mfma_f32_16x16x32_bf16 v[112:115], v[212:215], v[84:87], v[112:115]\n" \
  "v_mfma_f32_16x16x32_bf16 v[116:119], v[244:247], v[84:87], v[116:119]\n" \
  "ds_read_b128 v[100:103], v18 offset:1024\n" \
  "s_waitcnt lgkmcnt(3)\n" \
  "v_mfma_f32_16x16x32_bf16 v[112:115], v[216:219], v[88:91], v[112:115]\n" \
  "v_mfma_f32_16x16x32_bf16 v[116:119], v[248:251], v[88:91], v[116:119]\n" \
  "ds_read_b128 v[104:107], v18 offset:2048\n" \
  "s_waitcnt lgkmcnt(3)\n" \
  "v_mfma_f32_16x16x32_bf16 v[112:115], v[220:223], v[92:95], v[112:115]\n" \
  "v_mfma_f32_16x16x32_bf16 v[116:119], v[252:255], v[92:95], v[116:119]\n" \
  "ds_read_b128 v[108:111], v18 offset:3072\n" \
  "s_waitcnt lgkmcnt(3)\n" \
  "v_mfma_f32_16x16x32_bf16 v[120:123], v[208:211], v[96:99], v[120:123]\n" \
  "v_mfma_f32_16x16x32_bf16 v[124:127], v[240:243], v[96:99], v[124:127]\n" \
  "s_memrealtime s[38:39]\n" \
  "ds_read_b32 v37, v38\n" \
  "s_waitcnt lgkmcnt(3)\n" \
  "v_mfma_f32_16x16x32_bf16 v[120:123], v[212:215], v[100:103], v[120:123]\n" \
  "v_mfma_f32_16x16x32_bf16 v[124:127], v[244:247], v[100:103], v[124:127]\n" \
  "v_cvt_pk_bf16_f32 v20, v112, v113\n" \
  "v_cvt_pk_bf16_f32 v21, v114, v115\n" \
  "v_cvt_pk_bf16_f32 v22, v116, v117\n" \
  "v_cvt_pk_bf16_f32 v23, v118, v119\n" \
  "v_and_or_b32 v20, v20, -2, s59\n" \
  "s_waitcnt lgkmcnt(2)\n" \
  "v_mfma_f32_16x16x32_bf16 v[120:123], v[216:219], v[104:107], v[120:123]\n" \
  "v_mfma_f32_16x16x32_bf16 v[124:127], v[248:251], v[104:107], v[124:127]\n" \
  "buffer_store_dwordx4 v[20:23], v4, s[40:43], s54 offen sc1\n" \
  "s_waitcnt lgkmcnt(0)\n" \
  "v_mfma_f32_16x16x32_bf16 v[120:123], v[220:223], v[108:111], v[120:123]\n" \
  "v_mfma_f32_16x16x32_bf16 v[124:127], v[252:255], v[108:111], v[124:127]\n" \
  "s_xor_b32 s64, s54, 16384\n" \
  "s_mov_b32 s61, 1\n" \
  "s_nop 3\n" \
  "v_cvt_pk_bf16_f32 v24, v120, v121\n" \
  "v_cvt_pk_bf16_f32 v25, v122, v123\n" \
  "v_cvt_pk_bf16_f32 v26, v124, v125\n" \
  "v_cvt_pk_bf16_f32 v27, v126, v127\n" \
  "v_and_or_b32 v24, v24, -2, s59\n" \
  "buffer_store_dwordx4 v[24:27], v4, s[40:43], s64 offen sc1\n" \
  "v_readfirstlane_b32 s73, v37\n" \
  "s_sub_u32 s63, s38, s69\n" \
  "s_bfe_u32 s64, s73, 0x80010\n" \
  "s_sub_u32 s64, s64, s67\n" \
  "s_or_b32 s63, s63, s64\n" \
  "s_bfe_u32 s64, s73, 0x80008\n" \
  "s_and_b32 s73, s73, 0xff\n" \
  "s_min_u32 s73, s73, s64\n" \
  "s_or_b32 s63, s63, s68\n" \
  "s_cmp_lt_i32 s63, 0\n" \
  "s_cbranch_scc1 .LCX_E5_%=\n" \
  "v_mov_b32 v35, 0\n" \
  "global_load_dwordx4 v[128:131], v5, s[48:49] offset:0 sc1\n" \
  "global_load_dwordx4 v[132:135], v5, s[48:49] offset:1024 sc1\n" \
  "global_load_dwordx4 v[136:139], v3, s[48:49] offset:0 sc1\n" \
  "global_load_dwordx4 v[140:143], v3, s[48:49] offset:512 sc1\n" \
  "global_load_dword v35, v35, s[50:51] sc1\n" \
  "global_load_dword v146, v2, s[52:53] sc1\n" \
  "s_mov_b32 s68, -2\n" \
  ".LCX_E5_%=:\n" \
  "s_setprio 0\n" \
  "s_add_u32 s54, s54, 0x80000\n" \
  "s_bitset0_b32 s54, 22\n" \
  "s_sub_u32 s60, s60, 1\n" \
  "s_cmp_lg_u32 s60, 0\n" \
  "s_cbranch_scc1 .LCX_TOP_%=\n" \
  "s_waitcnt vmcnt(0)\n" \
  ".LCX_END_%=:\n" \
  "s_endpgm\n" \
  ".LCX_FAIL_%=:\n" \
  "v_mov_b64 v[28:29], 0\n" \
  "v_mov_b64 v[30:31], 0\n" \
  ".LCX_FAIL2_%=:\n" \
  "s_add_u32 s69, s64, -52\n" \
  "s_cmp_lg_u32 s64, 0\n" \
  "s_cbranch_scc1 .LCX_WF_%=\n" \
  "s_sleep 4\n" \
  "global_load_dword v35, v35, s[50:51] sc1\n" \
  "s_sub_u32 s62, s62, 8\n" \
  "s_cbranch_scc1 .LCX_END_%=\n" \
  "s_waitcnt vmcnt(0)\n" \
  "v_readfirstlane_b32 s64, v35\n" \
  "s_branch .LCX_FAIL2_%=\n"
#define AW_ASM_Y \
  "s_sub_u32 s78, s77, 1\n" \
  "s_mov_b32 s72, 0\n" \
  "s_mov_b32 s67, 0\n" \
  "s_mov_b32 s68, 2\n" \
  "s_mov_b32 s69, 0\n" \
  "s_mov_b32 s65, 49152\n" \
  "s_mov_b32 s66, 16384\n" \
  "v_mov_b32 v5, s58\n" \
  "v_mov_b32 v60, v3\n" \
  "v_add_u32 v3, 4096, v2\n" \
  "s_mov_b32 s62, 0x40000\n" \
  "s_setprio 1\n" \
  "v_cmp_eq_u32 vcc, 0, v4\n" \
  "s_cmp_eq_u64 vcc, 0\n" \
  "s_cbranch_scc1 .LCY_GET_%=\n" \
  "s_branch .LCY_ASK_%=\n" \
  ".p2align 6\n" \
  ".LCY_BAR_%=:\n" \
  "s_barrier\n" \
  "s_memrealtime s[38:39]\n" \
  "ds_read_b128 v[8:11], v0 offset:0\n" \
  "ds_read_b128 v[12:15], v0 offset:1024\n" \
  "ds_read_b128 v[16:19], v0 offset:2048\n" \
  "s_sub_u32 s59, s60, 1\n" \
  "s_bfe_u32 s59, s59, 0x10003\n" \
  "s_add_u32 s68, s68, 1\n" \
  "s_add_u32 s64, s54, 16384\n" \
  "s_cmp_eq_u32 s69, 0\n" \
  "s_cbranch_scc1 .LCY_NI_%=\n" \
  "s_waitcnt vmcnt(3)\n" \
  "s_cmp_lg_u32 s72, 0\n" \
  "s_cbranch_scc1 .LCY_NJ_%=\n" \
  "s_waitcnt vmcnt(0)\n" \
  ".LCY_NJ_%=:\n" \
  "v_mov_b32 v6, s55\n" \
  "s_mov_b64 exec, 1\n" \
  "ds_add_u32 v5, v6\n" \
  "s_mov_b64 exec, -1\n" \
  "s_mov_b32 s69, 0\n" \
  ".LCY_NI_%=:\n" \
  "s_waitcnt lgkmcnt(0)\n" \
  "s_add_u32 s63, s38, 205\n" \
  "s_mov_b32 s72, 0\n" \
  "v_or_b32 v7, 1, s63\n" \
  "s_mov_b64 exec, 1\n" \
  "global_store_dword v2, v7, s[52:53] sc1\n" \
  "s_mov_b64 exec, -1\n" \
  "s_add_u32 s52, s52, 2048\n" \
  "s_addc_u32 s53, s53, 0\n" \
  "v_cmp_eq_u32 vcc, 0, v4\n" \
  "s_cmp_eq_u64 vcc, 0\n" \
  "s_cbranch_scc0 .LCY_NS_%=\n" \
  "v_lshlrev_b32 v6, 16, s55\n" \
  "s_mov_b64 exec, 1\n" \
  "ds_add_u32 v5, v6\n" \
  "s_mov_b64 exec, -1\n" \
  "s_mov_b32 m0, s56\n" \
  "s_add_u32 s71, s56, 4096\n" \
  "global_load_lds_dwordx4 v2, s[48:49] offset:0 sc1\n" \
  "global_load_lds_dwordx4 v2, s[48:49] offset:1024 sc1\n" \
  "global_load_lds_dwordx4 v2, s[48:49] offset:2048 sc1\n" \
  "global_load_lds_dwordx4 v2, s[48:49] offset:3072 sc1\n" \
  ".LCY_NS_%=:\n" \
  "ds_read_b128 v[20:23], v0 offset:3072\n" \
  "v_mfma_f32_16x16x32_bf16 v[32:35], v[64:67], v[8:11], 0\n" \
  "v_mfma_f32_16x16x32_bf16 v[36:39], v[96:99], v[8:11], 0\n" \
  "v_mfma_f32_16x16x32_bf16 v[40:43], v[128:131], v[8:11], 0\n" \
  "v_mfma_f32_16x16x32_bf16 v[44:47], v[160:163], v[8:11], 0\n" \
  "v_mfma_f32_16x16x32_bf16 v[48:51], v[192:195], v[8:11], 0\n" \
  "v_mfma_f32_16x16x32_bf16 v[52:55], v[224:227], v[8:11], 0\n" \
  "ds_read_b128 v[8:11], v0 offset:8192\n" \
  "v_mfma_f32_16x16x32_bf16 v[32:35], v[68:71], v[12:15], v[32:35]\n" \
  "v_mfma_f32_16x16x32_bf16 v[36:39], v[100:103], v[12:15], v[36:39]\n" \
  "v_mfma_f32_16x16x32_bf16 v[40:43], v[132:135], v[12:15], v[40:43]\n" \
  "v_mfma_f32_16x16x32_bf16 v[44:47], v[164:167], v[12:15], v[44:47]\n" \
  "v_mfma_f32_16x16x32_bf16 v[48:51], v[196:199], v[12:15], v[48:51]\n" \
  "v_mfma_f32_16x16x32_bf16 v[52:55], v[228:231], v[12:15], v[52:55]\n" \
  "s_cbranch_vccnz .LCY_D1_%=\n" \
  "s_mov_b32 m0, s71\n" \
  "s_add_u32 s56, s56, s65\n" \
  "global_load_lds_dwordx4 v3, s[48:49] offset:0 sc1\n" \
  ".LCY_D1_%=:\n" \
  "ds_read_b128 v[12:15], v0 offset:9216\n" \
  "v_mfma_f32_16x16x32_bf16 v[32:35], v[72:75], v[16:19], v[32:35]\n" \
  "v_mfma_f32_16x16x32_bf16 v[36:39], v[104:107], v[16:19], v[36:39]\n" \
  "v_mfma_f32_16x16x32_bf16 v[40:43], v[136:139], v[16:19], v[40:43]\n" \
  "v_mfma_f32_16x16x32_bf16 v[44:47], v[168:171], v[16:19], v[44:47]\n" \
  "v_mfma_f32_16x16x32_bf16 v[48:51], v[200:203], v[16:19], v[48:51]\n" \
  "v_mfma_f32_16x16x32_bf16 v[52:55], v[232:235], v[16:19], v[52:55]\n" \
  "s_cbranch_vccnz .LCY_D2_%=\n" \
  "global_load_lds_dwordx4 v3, s[48:49] offset:1024 sc1\n" \
  ".LCY_D2_%=:\n" \
  "ds_read_b128 v[16:19], v0 offset:10240\n" \
  "s_waitcnt lgkmcnt(3)\n" \
  "v_mfma_f32_16x16x32_bf16 v[32:35], v[76:79], v[20:23], v[32:35]\n" \
  "v_mfma_f32_16x16x32_bf16 v[36:39], v[108:111], v[20:23], v[36:39]\n" \
  "v_mfma_f32_16x16x32_bf16 v[40:43], v[140:143], v[20:23], v[40:43]\n" \
  "v_mfma_f32_16x16x32_bf16 v[44:47], v[172:175], v[20:23], v[44:47]\n" \
  "v_mfma_f32_16x16x32_bf16 v[48:51], v[204:207], v[20:23], v[48:51]\n" \
  "v_mfma_f32_16x16x32_bf16 v[52:55], v[236:239], v[20:23], v[52:55]\n" \
  "s_cbranch_vccnz .LCY_D3_%=\n" \
  "global_load_lds_dwordx4 v3, s[48:49] offset:2048 sc1\n" \
  ".LCY_D3_%=:\n" \
  "ds_read_b128 v[20:23], v0 offset:11264\n" \
  "s_waitcnt lgkmcnt(3)\n" \
  "v_mfma_f32_16x16x32_bf16 v[32:35], v[80:83], v[8:11], v[32:35]\n" \
  "v_mfma_f32_16x16x32_bf16 v[36:39], v[112:115], v[8:11], v[36:39]\n" \
  "v_mfma_f32_16x16x32_bf16 v[40:43], v[144:147], v[8:11], v[40:43]\n" \
  "v_mfma_f32_16x16x32_bf16 v[44:47], v[176:179], v[8:11], v[44:47]\n" \
  "v_mfma_f32_16x16x32_bf16 v[48:51], v[208:211], v[8:11], v[48:51]\n" \
  "v_mfma_f32_16x16x32_bf16 v[52:55], v[240:243], v[8:11], v[52:55]\n" \
  "s_cbranch_vccnz .LCY_D4_%=\n" \
  "global_load_lds_dwordx4 v3, s[48:49] offset:3072 sc1\n" \
  "s_add_u32 s48, s48, 0x80000\n" \
  "s_addc_u32 s49, s49, 0\n" \
  ".LCY_D4_%=:\n" \
  "ds_read_b128 v[8:11], v0 offset:4096\n" \
  "s_waitcnt lgkmcnt(3)\n" \
  "v_mfma_f32_16x16x32_bf16 v[32:35], v[84:87], v[12:15], v[32:35]\n" \
  "v_mfma_f32_16x16x32_bf16 v[36:39], v[116:119], v[12:15], v[36:39]\n" \
  "v_mfma_f32_16x16x32_bf16 v[40:43], v[148:151], v[12:15], v[40:43]\n" \
  "v_mfma_f32_16x16x32_bf16 v[44:47], v[180:183], v[12:15], v[44:47]\n" \
  "v_mfma_f32_16x16x32_bf16 v[48:51], v[212:215], v[12:15], v[48:51]\n" \
  "v_mfma_f32_16x16x32_bf16 v[52:55], v[244:247], v[12:15], v[52:55]\n" \
  "s_cbranch_vccnz .LCY_AS_%=\n" \
  "s_mov_b32 m0, s57\n" \
  "s_add_u32 s57, s57, s65\n" \
  "global_load_lds_dwordx4 v2, s[50:51] offset:0 sc1\n" \
  ".LCY_D5_%=:\n" \
  "ds_read_b128 v[12:15], v0 offset:5120\n" \
  "s_waitcnt lgkmcnt(3)\n" \
  "v_mfma_f32_16x16x32_bf16 v[32:35], v[88:91], v[16:19], v[32:35]\n" \
  "v_mfma_f32_16x16x32_bf16 v[36:39], v[120:123], v[16:19], v[36:39]\n" \
  "v_mfma_f32_16x16x32_bf16 v[40:43], v[152:155], v[16:19], v[40:43]\n" \
  "v_mfma_f32_16x16x32_bf16 v[44:47], v[184:187], v[16:19], v[44:47]\n" \
  "v_mfma_f32_16x16x32_bf16 v[48:51], v[216:219], v[16:19], v[48:51]\n" \
  "v_mfma_f32_16x16x32_bf16 v[52:55], v[248:251], v[16:19], v[52:55]\n" \
  "s_cbranch_vccnz .LCY_D6_%=\n" \
  "global_load_lds_dwordx4 v2, s[50:51] offset:1024 sc1\n" \
  "s_sub_u32 s65, 0, s65\n" \
  "s_add_u32 s67, s67, 4096\n" \
  "s_bitset0_b32 s48, s77\n" \
  ".LCY_D6_%=:\n" \
  "ds_read_b128 v[16:19], v0 offset:6144\n" \
  "s_waitcnt lgkmcnt(3)\n" \
  "v_mfma_f32_16x16x32_bf16 v[32:35], v[92:95], v[20:23], v[32:35]\n" \
  "v_mfma_f32_16x16x32_bf16 v[36:39], v[124:127], v[20:23], v[36:39]\n" \
  "v_mfma_f32_16x16x32_bf16 v[40:43], v[156:159], v[20:23], v[40:43]\n" \
  "v_mfma_f32_16x16x32_bf16 v[44:47], v[188:191], v[20:23], v[44:47]\n" \
  "v_mfma_f32_16x16x32_bf16 v[48:51], v[220:223], v[20:23], v[48:51]\n" \
  "v_mfma_f32_16x16x32_bf16 v[52:55], v[252:255], v[20:23], v[52:55]\n" \
  "ds_read_b128 v[20:23], v0 offset:7168\n" \
  "s_waitcnt lgkmcnt(3)\n" \
  "s_nop 1\n" \
  "v_cvt_pk_bf16_f32 v24, v32, v33\n" \
  "v_cvt_pk_bf16_f32 v25, v34, v35\n" \
  "v_cvt_pk_bf16_f32 v26, v36, v37\n" \
  "v_cvt_pk_bf16_f32 v27, v38, v39\n" \
  "s_nop 0\n" \
  "v_mfma_f32_16x16x32_bf16 v[32:35], v[64:67], v[8:11], 0\n" \
  "v_mfma_f32_16x16x32_bf16 v[36:39], v[96:99], v[8:11], 0\n" \
  "v_and_or_b32 v24, v24, -2, s59\n" \
  "buffer_store_dwordx4 v[24:27], v1, s[40:43], s54 offen offset:0 sc1\n" \
  "v_cvt_pk_bf16_f32 v28, v40, v41\n" \
  "v_cvt_pk_bf16_f32 v29, v42, v43\n" \
  "v_cvt_pk_bf16_f32 v30, v44, v45\n" \
  "v_cvt_pk_bf16_f32 v31, v46, v47\n" \
  "s_nop 0\n" \
  "v_mfma_f32_16x16x32_bf16 v[40:43], v[128:131], v[8:11], 0\n" \
  "v_mfma_f32_16x16x32_bf16 v[44:47], v[160:163], v[8:11], 0\n" \
  "v_and_or_b32 v28, v28, -2, s59\n" \
  "buffer_store_dwordx4 v[28:31], v1, s[40:43], s54 offen offset:128 sc1\n" \
  "v_cvt_pk_bf16_f32 v56, v48, v49\n" \
  "v_cvt_pk_bf16_f32 v57, v50, v51\n" \
  "v_cvt_pk_bf16_f32 v58, v52, v53\n" \
  "v_cvt_pk_bf16_f32 v59, v54, v55\n" \
  "s_nop 0\n" \
  "v_mfma_f32_16x16x32_bf16 v[48:51], v[192:195], v[8:11], 0\n" \
  "v_mfma_f32_16x16x32_bf16 v[52:55], v[224:227], v[8:11], 0\n" \
  "v_and_or_b32 v56, v56, -2, s59\n" \
  "buffer_store_dwordx4 v[56:59], v1, s[40:43], s54 offen offset:256 sc1\n" \
  "s_cbranch_vccnz .LCY_AL0_%=\n" \
  ".LCY_AL1_%=:\n" \
  "ds_read_b128 v[8:11], v0 offset:12288\n" \
  "s_waitcnt lgkmcnt(3)\n" \
  "v_mfma_f32_16x16x32_bf16 v[32:35], v[68:71], v[12:15], v[32:35]\n" \
  "v_mfma_f32_16x16x32_bf16 v[36:39], v[100:103], v[12:15], v[36:39]\n" \
  "v_mfma_f32_16x16x32_bf16 v[40:43], v[132:135], v[12:15], v[40:43]\n" \
  "v_mfma_f32_16x16x32_bf16 v[44:47], v[164:167], v[12:15], v[44:47]\n" \
  "v_mfma_f32_16x16x32_bf16 v[48:51], v[196:199], v[12:15], v[48:51]\n" \
  "v_mfma_f32_16x16x32_bf16 v[52:55], v[228:231], v[12:15], v[52:55]\n" \
  "s_cbranch_vccnz .LCY_D7_%=\n" \
  "global_load_lds_dwordx4 v2, s[50:51] offset:2048 sc1\n" \
  "s_sub_u32 s68, s68, 1\n" \
  "s_mov_b32 s69, 1\n" \
  ".LCY_D7_%=:\n" \
  "ds_read_b128 v[12:15], v0 offset:13312\n" \
  "s_waitcnt lgkmcnt(3)\n" \
  "v_mfma_f32_16x16x32_bf16 v[32:35], v[72:75], v[16:19], v[32:35]\n" \
  "v_mfma_f32_16x16x32_bf16 v[36:39], v[104:107], v[16:19], v[36:39]\n" \
  "v_mfma_f32_16x16x32_bf16 v[40:43], v[136:139], v[16:19], v[40:43]\n" \
  "v_mfma_f32_16x16x32_bf16 v[44:47], v[168:171], v[16:19], v[44:47]\n" \
  "v_mfma_f32_16x16x32_bf16 v[48:51], v[200:203], v[16:19], v[48:51]\n" \
  "v_mfma_f32_16x16x32_bf16 v[52:55], v[232:235], v[16:19], v[52:55]\n" \
  "s_cbranch_vccnz .LCY_D8_%=\n" \
  "global_load_lds_dwordx4 v2, s[50:51] offset:3072 sc1\n" \
  "s_add_u32 s50, s50, 0x40000\n" \
  "s_addc_u32 s51, s51, 0\n" \
  "s_bitset0_b32 s50, s78\n" \
  ".LCY_D8_%=:\n" \
  "ds_read_b128 v[16:19], v0 offset:14336\n" \
  "s_waitcnt lgkmcnt(3)\n" \
  "v_mfma_f32_16x16x32_bf16 v[32:35], v[76:79], v[20:23], v[32:35]\n" \
  "v_mfma_f32_16x16x32_bf16 v[36:39], v[108:111], v[20:23], v[36:39]\n" \
  "v_mfma_f32_16x16x32_bf16 v[40:43], v[140:143], v[20:23], v[40:43]\n" \
  "v_mfma_f32_16x16x32_bf16 v[44:47], v[172:175], v[20:23], v[44:47]\n" \
  "v_mfma_f32_16x16x32_bf16 v[48:51], v[204:207], v[20:23], v[48:51]\n" \
  "v_mfma_f32_16x16x32_bf16 v[52:55], v[236:239], v[20:23], v[52:55]\n" \
  "ds_read_b128 v[20:23], v0 offset:15360\n" \
  "s_waitcnt lgkmcnt(3)\n" \
  "v_mfma_f32_16x16x32_bf16 v[32:35], v[80:83], v[8:11], v[32:35]\n" \
  "v_mfma_f32_16x16x32_bf16 v[36:39], v[112:115], v[8:11], v[36:39]\n" \
  "v_mfma_f32_16x16x32_bf16 v[40:43], v[144:147], v[8:11], v[40:43]\n" \
  "v_mfma_f32_16x16x32_bf16 v[44:47], v[176:179], v[8:11], v[44:47]\n" \
  "v_mfma_f32_16x16x32_bf16 v[48:51], v[208:211], v[8:11], v[48:51]\n" \
  "v_mfma_f32_16x16x32_bf16 v[52:55], v[240:243], v[8:11], v[52:55]\n" \
  "s_waitcnt lgkmcnt(2)\n" \
  "v_mfma_f32_16x16x32_bf16 v[32:35], v[84:87], v[12:15], v[32:35]\n" \
  "v_mfma_f32_16x16x32_bf16 v[36:39], v[116:119], v[12:15], v[36:39]\n" \
  "v_mfma_f32_16x16x32_bf16 v[40:43], v[148:151], v[12:15], v[40:43]\n" \
  "v_mfma_f32_16x16x32_bf16 v[44:47], v[180:183], v[12:15], v[44:47]\n" \
  "v_mfma_f32_16x16x32_bf16 v[48:51], v[212:215], v[12:15], v[48:51]\n" \
  "v_mfma_f32_16x16x32_bf16 v[52:55], v[244:247], v[12:15], v[52:55]\n" \
  "s_waitcnt lgkmcnt(1)\n" \
  "v_mfma_f32_16x16x32_bf16 v[32:35], v[88:91], v[16:19], v[32:35]\n" \
  "v_mfma_f32_16x16x32_bf16 v[36:39], v[120:123], v[16:19], v[36:39]\n" \
  "v_mfma_f32_16x16x32_bf16 v[40:43], v[152:155], v[16:19], v[40:43]\n" \
  "v_mfma_f32_16x16x32_bf16 v[44:47], v[184:187], v[16:19], v[44:47]\n" \
  "v_mfma_f32_16x16x32_bf16 v[48:51], v[216:219], v[16:19], v[48:51]\n" \
  "v_mfma_f32_16x16x32_bf16 v[52:55], v[248:251], v[16:19], v[52:55]\n" \
  "s_cbranch_vccnz .LCY_NT_%=\n" \
  "v_mov_b32 v4, 0\n" \
  "buffer_load_dword v4, v60, s[44:47], s67 offen sc1\n" \
  ".LCY_NT_%=:\n" \
  "s_waitcnt lgkmcnt(0)\n" \
  "v_mfma_f32_16x16x32_bf16 v[32:35], v[92:95], v[20:23], v[32:35]\n" \
  "v_mfma_f32_16x16x32_bf16 v[36:39], v[124:127], v[20:23], v[36:39]\n" \
  "v_mfma_f32_16x16x32_bf16 v[40:43], v[156:159], v[20:23], v[40:43]\n" \
  "v_mfma_f32_16x16x32_bf16 v[44:47], v[188:191], v[20:23], v[44:47]\n" \
  "v_mfma_f32_16x16x32_bf16 v[48:51], v[220:223], v[20:23], v[48:51]\n" \
  "v_mfma_f32_16x16x32_bf16 v[52:55], v[252:255], v[20:23], v[52:55]\n" \
  ".LCY_JOIN_%=:\n" \
  "v_add_u32 v0, s66, v0\n" \
  "s_sub_u32 s66, 0, s66\n" \
  "s_add_u32 s54, s54, 0x80000\n" \
  "s_bitset0_b32 s54, 22\n" \
  "s_mov_b32 s62, 0x40000\n" \
  "s_nop 0\n" \
  "v_cvt_pk_bf16_f32 v24, v32, v33\n" \
  "v_cvt_pk_bf16_f32 v25, v34, v35\n" \
  "v_cvt_pk_bf16_f32 v26, v36, v37\n" \
  "v_cvt_pk_bf16_f32 v27, v38, v39\n" \
  "v_and_or_b32 v24, v24, -2, s59\n" \
  "buffer_store_dwordx4 v[24:27], v1, s[40:43], s64 offen offset:0 sc1\n" \
  "v_cvt_pk_bf16_f32 v28, v40, v41\n" \
  "v_cvt_pk_bf16_f32 v29, v42, v43\n" \
  "v_cvt_pk_bf16_f32 v30, v44, v45\n" \
  "v_cvt_pk_bf16_f32 v31, v46, v47\n" \
  "v_and_or_b32 v28, v28, -2, s59\n" \
  "buffer_store_dwordx4 v[28:31], v1, s[40:43], s64 offen offset:128 sc1\n" \
  "v_cvt_pk_bf16_f32 v56, v48, v49\n" \
  "v_cvt_pk_bf16_f32 v57, v50, v51\n" \
  "v_cvt_pk_bf16_f32 v58, v52, v53\n" \
  "v_cvt_pk_bf16_f32 v59, v54, v55\n" \
  "v_and_or_b32 v56, v56, -2, s59\n" \
  "buffer_store_dwordx4 v[56:59], v1, s[40:43], s64 offen offset:256 sc1\n" \
  "s_cbranch_vccnz .LCY_ND_%=\n" \
  "s_cmp_eq_u32 s68, 0\n" \
  "s_cselect_b32 s72, 1, 0\n" \
  "s_cbranch_scc1 .LCY_DEF_%=\n" \
  "s_waitcnt vmcnt(0)\n" \
  "v_mov_b32 v6, s55\n" \
  "s_mov_b64 exec, 1\n" \
  "ds_add_u32 v5, v6\n" \
  "s_mov_b64 exec, -1\n" \
  "s_mov_b32 s69, 0\n" \
  "s_branch .LCY_WC_%=\n" \
  ".LCY_ND_%=:\n" \
  "v_mov_b32 v4, 0\n" \
  "buffer_load_dword v4, v60, s[44:47], s67 offen sc1\n" \
  "s_waitcnt vmcnt(0)\n" \
  ".LCY_WC_%=:\n" \
  "s_branch .LCY_DEF_%=\n" \
  ".LCY_AS_%=:\n" \
  "s_sub_u32 s63, s60, 10\n" \
  "s_cmp_lt_u32 s63, 51\n" \
  "s_cbranch_scc1 .LCY_D5_%=\n" \
  "v_mov_b32 v4, 0\n" \
  "buffer_load_dword v4, v60, s[44:47], s67 offen sc1\n" \
  "s_branch .LCY_D5_%=\n" \
  ".LCY_AL0_%=:\n" \
  "s_sub_u32 s63, s60, 10\n" \
  "s_cmp_lt_u32 s63, 51\n" \
  "s_cbranch_scc1 .LCY_AL1_%=\n" \
  "s_bitcmp1_b32 s67, 18\n" \
  "s_cbranch_scc1 .LCY_AL1_%=\n" \
  "s_branch .LCY_ALT_%=\n" \
  ".LCY_DEF_%=:\n" \
  "s_sub_u32 s60, s60, 1\n" \
  "s_cmp_eq_u32 s60, 0\n" \
  "s_cbranch_scc1 .LCY_END_%=\n" \
  ".LCY_NEED_%=:\n" \
  "s_cmp_eq_u32 s68, 0\n" \
  "s_cbranch_scc1 .LCY_BAR_%=\n" \
  "v_cmp_eq_u32 vcc, 0, v4\n" \
  "s_cmp_eq_u64 vcc, 0\n" \
  "s_cbranch_scc1 .LCY_GET_%=\n" \
  "s_cmp_ge_u32 s67, 0x40000\n" \
  "s_cbranch_scc1 .LCY_BAR_%=\n" \
  "s_sleep 4\n" \
  "ds_read_b32 v6, v5 offset:16\n" \
  "s_waitcnt lgkmcnt(0)\n" \
  "v_readfirstlane_b32 s63, v6\n" \
  "s_add_u32 s63, s63, s60\n" \
  "s_cmp_ge_u32 s63, 66\n" \
  "s_cbranch_scc1 .LCY_BAR_%=\n" \
  ".LCY_ASK_%=:\n" \
  "v_mov_b32 v4, 0\n" \
  "buffer_load_dword v4, v60, s[44:47], s67 offen sc1\n" \
  "s_sub_u32 s62, s62, 1\n" \
  "s_cbranch_scc1 .LCY_END_%=\n" \
  "s_waitcnt vmcnt(0)\n" \
  "s_branch .LCY_NEED_%=\n" \
  ".LCY_GET_%=:\n" \
  "v_lshlrev_b32 v6, 16, s55\n" \
  "s_mov_b64 exec, 1\n" \
  "ds_add_u32 v5, v6\n" \
  "s_mov_b64 exec, -1\n" \
  "s_mov_b32 m0, s56\n" \
  "s_add_u32 s71, s56, 4096\n" \
  "global_load_lds_dwordx4 v2, s[48:49] offset:0 sc1\n" \
  "global_load_lds_dwordx4 v2, s[48:49] offset:1024 sc1\n" \
  "global_load_lds_dwordx4 v2, s[48:49] offset:2048 sc1\n" \
  "global_load_lds_dwordx4 v2, s[48:49] offset:3072 sc1\n" \
  "s_mov_b32 m0, s71\n" \
  "s_add_u32 s56, s56, s65\n" \
  "global_load_lds_dwordx4 v3, s[48:49] offset:0 sc1\n" \
  "global_load_lds_dwordx4 v3, s[48:49] offset:1024 sc1\n" \
  "global_load_lds_dwordx4 v3, s[48:49] offset:2048 sc1\n" \
  "global_load_lds_dwordx4 v3, s[48:49] offset:3072 sc1\n" \
  "s_add_u32 s48, s48, 0x80000\n" \
  "s_addc_u32 s49, s49, 0\n" \
  "s_mov_b32 m0, s57\n" \
  "s_add_u32 s57, s57, s65\n" \
  "global_load_lds_dwordx4 v2, s[50:51] offset:0 sc1\n" \
  "global_load_lds_dwordx4 v2, s[50:51] offset:1024 sc1\n" \
  "s_sub_u32 s65, 0, s65\n" \
  "s_add_u32 s67, s67, 4096\n" \
  "s_bitset0_b32 s48, s77\n" \
  "global_load_lds_dwordx4 v2, s[50:51] offset:2048 sc1\n" \
  "s_sub_u32 s68, s68, 1\n" \
  "s_mov_b32 s69, 1\n" \
  "global_load_lds_dwordx4 v2, s[50:51] offset:3072 sc1\n" \
  "s_add_u32 s50, s50, 0x40000\n" \
  "s_addc_u32 s51, s51, 0\n" \
  "s_bitset0_b32 s50, s78\n" \
  "v_mov_b32 v4, 0\n" \
  "buffer_load_dword v4, v60, s[44:47], s67 offen sc1\n" \
  "s_mov_b32 s61, 0\n" \
  "s_cmp_eq_u32 s68, 0\n" \
  "s_cbranch_scc1 .LCY_BAR_%=\n" \
  "s_waitcnt vmcnt(0)\n" \
  "v_mov_b32 v6, s55\n" \
  "s_mov_b64 exec, 1\n" \
  "ds_add_u32 v5, v6\n" \
  "s_mov_b64 exec, -1\n" \
  "s_mov_b32 s69, 0\n" \
  "s_branch .LCY_NEED_%=\n" \
  ".LCY_END_%=:\n" \
  "s_endpgm\n" \
  ".LCY_ALT_%=:\n" \
  "ds_read_b128 v[8:11], v0 offset:12288\n" \
  "s_waitcnt lgkmcnt(3)\n" \
  "v_mfma_f32_16x16x32_bf16 v[32:35], v[68:71], v[12:15], v[32:35]\n" \
  "v_mfma_f32_16x16x32_bf16 v[36:39], v[100:103], v[12:15], v[36:39]\n" \
  "s_waitcnt vmcnt(3)\n" \
  "v_cmp_eq_u32 vcc, 0, v4\n" \
  "v_mfma_f32_16x16x32_bf16 v[40:43], v[132:135], v[12:15], v[40:43]\n" \
  "s_cbranch_vccnz .LCY_D10_%=\n" \
  "v_lshlrev_b32 v6, 16, s55\n" \
  "s_mov_b64 exec, 1\n" \
  "ds_add_u32 v5, v6\n" \
  "s_mov_b64 exec, -1\n" \
  "s_mov_b32 m0, s56\n" \
  "s_add_u32 s71, s56, 4096\n" \
  "global_load_lds_dwordx4 v2, s[48:49] offset:0 sc1\n" \
  ".LCY_D10_%=:\n" \
  "v_mfma_f32_16x16x32_bf16 v[44:47], v[164:167], v[12:15], v[44:47]\n" \
  "v_mfma_f32_16x16x32_bf16 v[48:51], v[196:199], v[12:15], v[48:51]\n" \
  "v_mfma_f32_16x16x32_bf16 v[52:55], v[228:231], v[12:15], v[52:55]\n" \
  "s_cbranch_vccnz .LCY_D11_%=\n" \
  "global_load_lds_dwordx4 v2, s[48:49] offset:1024 sc1\n" \
  ".LCY_D11_%=:\n" \
  "ds_read_b128 v[12:15], v0 offset:13312\n" \
  "s_waitcnt lgkmcnt(3)\n" \
  "v_mfma_f32_16x16x32_bf16 v[32:35], v[72:75], v[16:19], v[32:35]\n" \
  "v_mfma_f32_16x16x32_bf16 v[36:39], v[104:107], v[16:19], v[36:39]\n" \
  "v_mfma_f32_16x16x32_bf16 v[40:43], v[136:139], v[16:19], v[40:43]\n" \
  "s_cbranch_vccnz .LCY_D12_%=\n" \
  "global_load_lds_dwordx4 v2, s[48:49] offset:2048 sc1\n" \
  ".LCY_D12_%=:\n" \
  "v_mfma_f32_16x16x32_bf16 v[44:47], v[168:171], v[16:19], v[44:47]\n" \
  "v_mfma_f32_16x16x32_bf16 v[48:51], v[200:203], v[16:19], v[48:51]\n" \
  "v_mfma_f32_16x16x32_bf16 v[52:55], v[232:235], v[16:19], v[52:55]\n" \
  "s_cbranch_vccnz .LCY_D13_%=\n" \
  "global_load_lds_dwordx4 v2, s[48:49] offset:3072 sc1\n" \
  ".LCY_D13_%=:\n" \
  "ds_read_b128 v[16:19], v0 offset:14336\n" \
  "s_waitcnt lgkmcnt(3)\n" \
  "v_mfma_f32_16x16x32_bf16 v[32:35], v[76:79], v[20:23], v[32:35]\n" \
  "v_mfma_f32_16x16x32_bf16 v[36:39], v[108:111], v[20:23], v[36:39]\n" \
  "v_mfma_f32_16x16x32_bf16 v[40:43], v[140:143], v[20:23], v[40:43]\n" \
  "s_cbranch_vccnz .LCY_D14_%=\n" \
  "s_mov_b32 m0, s71\n" \
  "s_add_u32 s56, s56, s65\n" \
  "global_load_lds_dwordx4 v3, s[48:49] offset:0 sc1\n" \
  ".LCY_D14_%=:\n" \
  "v_mfma_f32_16x16x32_bf16 v[44:47], v[172:175], v[20:23], v[44:47]\n" \
  "v_mfma_f32_16x16x32_bf16 v[48:51], v[204:207], v[20:23], v[48:51]\n" \
  "v_mfma_f32_16x16x32_bf16 v[52:55], v[236:239], v[20:23], v[52:55]\n" \
  "s_cbranch_vccnz .LCY_D15_%=\n" \
  "global_load_lds_dwordx4 v3, s[48:49] offset:1024 sc1\n" \
  ".LCY_D15_%=:\n" \
  "ds_read_b128 v[20:23], v0 offset:15360\n" \
  "s_waitcnt lgkmcnt(3)\n" \
  "v_mfma_f32_16x16x32_bf16 v[32:35], v[80:83], v[8:11], v[32:35]\n" \
  "v_mfma_f32_16x16x32_bf16 v[36:39], v[112:115], v[8:11], v[36:39]\n" \
  "v_mfma_f32_16x16x32_bf16 v[40:43], v[144:147], v[8:11], v[40:43]\n" \
  "s_cbranch_vccnz .LCY_D16_%=\n" \
  "global_load_lds_dwordx4 v3, s[48:49] offset:2048 sc1\n" \
  ".LCY_D16_%=:\n" \
  "v_mfma_f32_16x16x32_bf16 v[44:47], v[176:179], v[8:11], v[44:47]\n" \
  "v_mfma_f32_16x16x32_bf16 v[48:51], v[208:211], v[8:11], v[48:51]\n" \
  "v_mfma_f32_16x16x32_bf16 v[52:55], v[240:243], v[8:11], v[52:55]\n" \
  "s_cbranch_vccnz .LCY_D17_%=\n" \
  "global_load_lds_dwordx4 v3, s[48:49] offset:3072 sc1\n" \
  "s_add_u32 s48, s48, 0x80000\n" \
  "s_addc_u32 s49, s49, 0\n" \
  ".LCY_D17_%=:\n" \
  "s_waitcnt lgkmcnt(2)\n" \
  "v_mfma_f32_16x16x32_bf16 v[32:35], v[84:87], v[12:15], v[32:35]\n" \
  "v_mfma_f32_16x16x32_bf16 v[36:39], v[116:119], v[12:15], v[36:39]\n" \
  "v_mfma_f32_16x16x32_bf16 v[40:43], v[148:151], v[12:15], v[40:43]\n" \
  "s_cbranch_vccnz .LCY_D18_%=\n" \
  "s_mov_b32 m0, s57\n" \
  "s_add_u32 s57, s57, s65\n" \
  "global_load_lds_dwordx4 v2, s[50:51] offset:0 sc1\n" \
  ".LCY_D18_%=:\n" \
  "v_mfma_f32_16x16x32_bf16 v[44:47], v[180:183], v[12:15], v[44:47]\n" \
  "v_mfma_f32_16x16x32_bf16 v[48:51], v[212:215], v[12:15], v[48:51]\n" \
  "v_mfma_f32_16x16x32_bf16 v[52:55], v[244:247], v[12:15], v[52:55]\n" \
  "s_cbranch_vccnz .LCY_D19_%=\n" \
  "global_load_lds_dwordx4 v2, s[50:51] offset:1024 sc1\n" \
  "s_sub_u32 s65, 0, s65\n" \
  "s_add_u32 s67, s67, 4096\n" \
  "s_bitset0_b32 s48, s77\n" \
  ".LCY_D19_%=:\n" \
  "s_waitcnt lgkmcnt(1)\n" \
  "v_mfma_f32_16x16x32_bf16 v[32:35], v[88:91], v[16:19], v[32:35]\n" \
  "v_mfma_f32_16x16x32_bf16 v[36:39], v[120:123], v[16:19], v[36:39]\n" \
  "v_mfma_f32_16x16x32_bf16 v[40:43], v[152:155], v[16:19], v[40:43]\n" \
  "s_cbranch_vccnz .LCY_D20_%=\n" \
  "global_load_lds_dwordx4 v2, s[50:51] offset:2048 sc1\n" \
  "s_sub_u32 s68, s68, 1\n" \
  "s_mov_b32 s69, 1\n" \
  ".LCY_D20_%=:\n" \
  "v_mfma_f32_16x16x32_bf16 v[44:47], v[184:187], v[16:19], v[44:47]\n" \
  "v_mfma_f32_16x16x32_bf16 v[48:51], v[216:219], v[16:19], v[48:51]\n" \
  "v_mfma_f32_16x16x32_bf16 v[52:55], v[248:251], v[16:19], v[52:55]\n" \
  "s_cbranch_vccnz .LCY_D21_%=\n" \
  "global_load_lds_dwordx4 v2, s[50:51] offset:3072 sc1\n" \
  "s_add_u32 s50, s50, 0x40000\n" \
  "s_addc_u32 s51, s51, 0\n" \
  "s_bitset0_b32 s50, s78\n" \
  "v_mov_b32 v4, 0\n" \
  "buffer_load_dword v4, v60, s[44:47], s67 offen sc1\n" \
  ".LCY_D21_%=:\n" \
  "s_waitcnt lgkmcnt(0)\n" \
  "v_mfma_f32_16x16x32_bf16 v[32:35], v[92:95], v[20:23], v[32:35]\n" \
  "v_mfma_f32_16x16x32_bf16 v[36:39], v[124:127], v[20:23], v[36:39]\n" \
  "v_mfma_f32_16x16x32_bf16 v[40:43], v[156:159], v[20:23], v[40:43]\n" \
  "v_mfma_f32_16x16x32_bf16 v[44:47], v[188:191], v[20:23], v[44:47]\n" \
  "v_mfma_f32_16x16x32_bf16 v[48:51], v[220:223], v[20:23], v[48:51]\n" \
  "v_mfma_f32_16x16x32_bf16 v[52:55], v[252:255], v[20:23], v[52:55]\n" \
  "s_branch .LCY_JOIN_%=\n"
template <bool AT>
__device__ __forceinline__ void role_aw_t(const MegaArgs& A, char* smem, const int L, const int a, const u64 tag) {
  const int w = A.wv;
  const int l = lane_id(), c = l & 15, g = l >> 4;
  const int qt = w & 1, hh = (w >> 1) & 1, h = 2 * a + hh;
  char* ws = A.ws;
  unsigned* FL = (unsigned*)(ws + OFF_FL);
  const unsigned sb = lds_base(smem);
  if (w == 0 && l < 8) reinterpret_cast<unsigned*>(smem + AW_CNT)[l] = 0u;
  constexpr int NP = AT ? 1 : 3;
  const int cp0 = AT ? w : 3 * w - 8;
  v8u wo[2 * NP][4];
  const int dp = (FL_FP - FL_FQ + (L * 16 + a - L * 64 * 32 - h) * 32 + 2 * hh) * 4;
  const unsigned fo = (l & 2) ? 16u + 8 * qt : qt ? 8u * (l & 1) : dp + 4 * (l & 1);
  unsigned nf = 0u;
  {
    const int q = (l >> 2) & 3, p = l & 3, f = q | ((g & 1) << 2);
    const unsigned ra = sb + (4 * g + q) * 256 + (p & 1) * 16;
#define PW_CLEAR(n)                                                                                                         \
  _Pragma("unroll") for (int i = 0; i < 16; ++i)                                                                            \
    ST128((v4u{0u, 0u, 0u, 0u}), MKRS(ws + OFF_PW, SZ_PW), l * 16 + (i & 3) * 1024,                                         \
          ((((n) >> RGB) * 64 + ((n) & ((1 << RGB) - 1))) * 32 + 2 * a + qt) * 16384 + (i >> 2) * 4096);
    if (EF && AT && L && PC0 == 0) { PW_CLEAR(hh) }
#pragma unroll
    for (int r = 0; r < 2; ++r) {
#pragma unroll
      for (int k = 0; k < 2; ++k) {
        const char* base_ = (const char*)A.Wo + ((size_t)((2 * a + r) * DH + 64 * (w & 1) + 32 * k) * DM + 128 * (w >> 1)) * 2;
#pragma unroll
        for (int i = 0; i < 8; ++i) {
          const int fd = g | ((i & 1) << 2);
          glds16(base_, (unsigned)((4 * i + g) * (DM * 2) + ((c ^ (fd << 1)) << 4)), sb + w * 16384 + k * 8192 + i * 1024, WNT & 16);
        }
      }
      if (r == 1 && AT) TOUCH("CX");
      if (r == 1 && !AT) TOUCH("CY");
      SST(4 + 3 * r);
      if (EF && AT && L && PC0 == 1 + r) { PW_CLEAR(hh) }
      if (r == 1) ready_wait(A, tag, ready_look(A));
      if (r == 1 && (PG & (AT ? 1 : 2))) PAGE();
      if (EF && AT && L && PC0 == 1 && r == 0) WAIT_VM(16);
      else WAIT_VM(0);
      if (EF && AT && r == (PC0 != 0) && L) {
        if (r == 0) ready_wait(A, tag, ready_look(A));
        PW_FREE(0, hh, 2 * a + qt);
      }
      RAW_BAR();
      SST(5 + 3 * r);
#pragma unroll
      for (int k2 = 0; k2 < 2; ++k2) {
        if (!AT && r == 1 && k2 == YQ - 1 && !L) nf = __hip_atomic_load((gu32*)((char*)(FL + FL_FQ + h * 32) + fo), RLX_AGENT);
#pragma unroll
        for (int ct = 0; ct < 2 * NP; ++ct) {
          const int cp = cp0 + (ct >> 1);
          const unsigned a_ = ra + (cp >> 2) * 32768 + k2 * 16384 + ((2 * (cp & 3) + (p >> 1)) ^ f) * 32 + (ct & 1) * 8;
          const v2u r0 = trd(a_), r1 = trd(a_ + 4096), r2 = trd(a_ + 8192), r3 = trd(a_ + 12288);
          wo[ct][2 * r + k2] = v8u{r0.x, r0.y, r1.x, r1.y, r2.x, r2.y, r3.x, r3.y};
          asm volatile("" : "+v"(wo[ct][2 * r + k2]));
        }
      }
      RAW_BAR();
      SST(6 + 3 * r);
    }
  }
  WSTAMP2();
  if (__builtin_expect(EF && L, !(PG & 16))) {
    const unsigned t0 = now10();
#pragma unroll 1
    for (int n = 2 + (w >> 1); n < (2 << RGB); n += 4) {
      gate(t0 + PCT + (n - 2) * PCD);
      PW_CLEAR(n)
      DRAIN();
      PW_FREE(n >> RGB, n & ((1 << RGB) - 1), 2 * a + qt);
    }
    if (AWD) gate(t0 + AWD);
  }
  const v4u rsW = mk_srd(ws + OFF_PW, SZ_PW);
  const v4u rsF = mk_srd(FL + FL_FQ + (L * 64 * 32 + h) * 32, (qt ? 0 : dp) + 64 * 4096 - 128);
  const char* p0 = AT ? ws + OFF_MEMK + L * SZ_MEM + h * 2048 : ws + TOK_QK(ws, L) + ((size_t)h * 16 + 8 * qt) * 1024;
  const char* p1 = AT ? (const char*)(FL + FL_FK + L * 32 + (h >> 3) * 64 + (h & 7) * 2) : ws + TOK_V(ws, L) + ((size_t)h * 8 + 4 * qt) * 1024;
  const unsigned* pFW = EF && AT ? FL + FL_FD + L * 32 : FL + FL_FW + (L * 64 * 16 + (a >> 2)) * 32 + (a & 3) + (EF ? (w & 3) * 128 : 0);
  const unsigned tk = sb + AW_TK + hh * 24 * 1024, voff = (c >> 1) * 2048 + (c & 1) * 64 + g * 16;
  const char* dbg = ws + WS_TOTAL + A.vb * 64 * 8;
#define AW_SOPS                                                                                                                 \
  "{s[40:43]}"(rsW), "{s[44:47]}"(rsF), "{s[48:49]}"(p0), "{s[50:51]}"(p1), "{s[52:53]}"(pFW),                                   \
      "{s54}"((L * 64 * 16 + a) * 32768 + cp0 * 128 + (AT ? qt * 16384 : 0)), "{s55}"(1 << (8 * qt)), "{s56}"(tk + qt * 8192), "{s57}"(tk + 16384 + qt * 4096),                  \
      "{s58}"(sb + AW_CNT + 8 + 4 * hh), "{s59}"(sb + AW_CNT), "{s60}"(NSEG), "{s77}"(L ? 19 + GEN_TR : 1), "{s[36:37]}"(dbg)             \
      : "memory", "vcc", "scc", "s80"
  if constexpr (AT) {
    const v8u pv = {tk + l * 16, (c & 8) ? 0xEEEEu : 0x4444u, (l & 31) * 4u, (unsigned)(OFF_MEMV - OFF_MEMK) + c * 32 + (g & 1) * 16 + (g >> 1) * 1024,
                    voff, (unsigned)l * 16, tk + qt * 4096 + l * 16, sb + AW_SO + w * 4096 + l * 16};
    const unsigned neg = 0xff61b1e6u;
    v8u bt;
#pragma unroll
    for (int i = 0; i < 4; ++i) {
      const unsigned d = 4 * g + i > c ? neg : 0u;
      bt[i] = qt ? 0u : d;
      bt[4 + i] = qt ? d : neg;
    }
    asm volatile("s_mov_b64 exec, -1\ns_waitcnt lgkmcnt(0)\ns_nop 4\n" AW_ASM_X ".LKG_LAST:\n"
                 ::"{v[192:199]}"(wo[0][0]), "{v[200:207]}"(wo[0][1]), "{v[208:215]}"(wo[0][2]), "{v[216:223]}"(wo[0][3]),
                 "{v[224:231]}"(wo[1][0]), "{v[232:239]}"(wo[1][1]), "{v[240:247]}"(wo[1][2]), "{v[248:255]}"(wo[1][3]),
                 "{v[0:7]}"(pv), "{v[8:15]}"(bt), AW_SOPS);
  } else {
    const v4u pv = {sb + AW_SO + l * 16, voff, (unsigned)l * 16, fo};
    asm volatile("s_mov_b64 exec, -1\ns_waitcnt vmcnt(0) lgkmcnt(0)\ns_nop 4\n" AW_ASM_Y
                 ::"{v[64:71]}"(wo[0][0]), "{v[72:79]}"(wo[0][1]), "{v[80:87]}"(wo[0][2]), "{v[88:95]}"(wo[0][3]),
                 "{v[96:103]}"(wo[1][0]), "{v[104:111]}"(wo[1][1]), "{v[112:119]}"(wo[1][2]), "{v[120:127]}"(wo[1][3]),
                 "{v[128:135]}"(wo[2][0]), "{v[136:143]}"(wo[2][1]), "{v[144:151]}"(wo[2][2]), "{v[152:159]}"(wo[2][3]),
                 "{v[160:167]}"(wo[3][0]), "{v[168:175]}"(wo[3][1]), "{v[176:183]}"(wo[3][2]), "{v[184:191]}"(wo[3][3]),
                 "{v[192:199]}"(wo[4][0]), "{v[200:207]}"(wo[4][1]), "{v[208:215]}"(wo[4][2]), "{v[216:223]}"(wo[4][3]),
                 "{v[224:231]}"(wo[5][0]), "{v[232:239]}"(wo[5][1]), "{v[240:247]}"(wo[5][2]), "{v[248:255]}"(wo[5][3]),
                 "{v4}"(nf), "{v[0:3]}"(pv), AW_SOPS);
  }
#undef AW_SOPS
}
__device__ __forceinline__ void role_aw(const MegaArgs& A, char* smem, const int L, const int a, const u64 tag) {
  if (A.wv < 4) role_aw_t<true>(A, smem, L, a, tag);
  else role_aw_t<false>(A, smem, L, a, tag);
}

#define P_ASM \
  "s_mov_b32 s68, 0\n" \
  "s_mov_b32 s66, 3\n" \
  "s_mov_b32 s67, 2\n" \
  "s_mov_b32 s72, 0\n" \
  "v_mov_b32 v7, 1\n" \
  "v_mov_b32 v6, s70\n" \
  "v_mov_b32 v5, 0\n" \
  "v_add_u32 v10, 4096, v2\n" \
  ".p2align 6\n" \
  ".LCP_TOP_%=:\n" \
  "ds_read_b128 v[28:31], v1 offset:0\n" \
  "ds_read_b128 v[32:35], v1 offset:1024\n" \
  "s_sub_u32 s73, s64, 1\n" \
  "s_bitset0_b32 s65, s74\n" \
  "s_bfe_u32 s73, s73, 0x10004\n" \
  "s_cmp_gt_u32 s64, s75\n" \
  "s_cselect_b32 s76, 1, 0\n" \
  "v_mov_b32 v11, s76\n" \
  "s_add_u32 s66, s66, 3\n" \
  "s_add_u32 s67, s67, 2\n" \
  "s_mov_b32 s69, 0x400000\n" \
  "v_xor_b32 v0, 0x8000, v0\n" \
  "v_xor_b32 v3, 0x100, v3\n" \
  "v_readfirstlane_b32 s71, v5\n" \
  "s_cmp_ge_u32 s71, s66\n" \
  "s_cbranch_scc1 .LCP_GO_%=\n" \
  "s_cmp_eq_u32 s68, 1\n" \
  "s_cbranch_scc0 .LCP_X1_%=\n" \
  "s_waitcnt vmcnt(0)\n" \
  "s_mov_b64 exec, 1\n" \
  "buffer_store_dword v7, off, s[56:59], s72 sc1\n" \
  "s_mov_b64 exec, -1\n" \
  "s_add_u32 s72, s72, 0x1000\n" \
  "s_mov_b32 s68, 0\n" \
  ".LCP_X1_%=:\n" \
  ".LCP_S2_%=:\n" \
  "s_sleep 1\n" \
  "ds_read_b32 v5, v6 offset:0\n" \
  "s_sub_u32 s69, s69, 1\n" \
  "s_cbranch_scc1 .LCP_END_%=\n" \
  "s_waitcnt lgkmcnt(0)\n" \
  "v_readfirstlane_b32 s71, v5\n" \
  "s_cmp_ge_u32 s71, s66\n" \
  "s_cbranch_scc1 .LCP_GO_%=\n" \
  "s_branch .LCP_S2_%=\n" \
  ".LCP_GO_%=:\n" \
  "ds_read_b128 v[12:15], v0 offset:0\n" \
  "ds_read_b128 v[16:19], v0 offset:2048\n" \
  "ds_read_b128 v[20:23], v0 offset:4096\n" \
  "ds_read_b128 v[24:27], v0 offset:6144\n" \
  "ds_read_b128 v[36:39], v1 offset:2048\n" \
  "s_waitcnt lgkmcnt(4)\n" \
  "v_mfma_f32_16x16x32_bf16 v[48:51], v[64:67], v[12:15], 0\n" \
  "v_mfma_f32_16x16x32_bf16 v[52:55], v[128:131], v[12:15], 0\n" \
  "v_mfma_f32_16x16x32_bf16 v[56:59], v[192:195], v[12:15], 0\n" \
  "v_mfma_f32_16x16x32_bf16 v[60:63], v[28:31], v[12:15], 0\n" \
  "ds_read_b128 v[12:15], v0 offset:8192\n" \
  "ds_read_b128 v[28:31], v1 offset:3072\n" \
  "s_waitcnt lgkmcnt(5)\n" \
  "v_mfma_f32_16x16x32_bf16 v[48:51], v[68:71], v[16:19], v[48:51]\n" \
  "v_mfma_f32_16x16x32_bf16 v[52:55], v[132:135], v[16:19], v[52:55]\n" \
  "v_mfma_f32_16x16x32_bf16 v[56:59], v[196:199], v[16:19], v[56:59]\n" \
  "v_mfma_f32_16x16x32_bf16 v[60:63], v[32:35], v[16:19], v[60:63]\n" \
  "ds_read_b128 v[16:19], v0 offset:10240\n" \
  "ds_read_b128 v[32:35], v1 offset:4096\n" \
  "s_waitcnt lgkmcnt(6)\n" \
  "v_mfma_f32_16x16x32_bf16 v[48:51], v[72:75], v[20:23], v[48:51]\n" \
  "v_mfma_f32_16x16x32_bf16 v[52:55], v[136:139], v[20:23], v[52:55]\n" \
  "v_mfma_f32_16x16x32_bf16 v[56:59], v[200:203], v[20:23], v[56:59]\n" \
  "s_waitcnt lgkmcnt(4)\n" \
  "v_mfma_f32_16x16x32_bf16 v[60:63], v[36:39], v[20:23], v[60:63]\n" \
  "ds_read_b128 v[20:23], v0 offset:12288\n" \
  "ds_read_b128 v[36:39], v1 offset:5120\n" \
  "v_mfma_f32_16x16x32_bf16 v[48:51], v[76:79], v[24:27], v[48:51]\n" \
  "v_mfma_f32_16x16x32_bf16 v[52:55], v[140:143], v[24:27], v[52:55]\n" \
  "v_mfma_f32_16x16x32_bf16 v[56:59], v[204:207], v[24:27], v[56:59]\n" \
  "s_waitcnt lgkmcnt(4)\n" \
  "v_mfma_f32_16x16x32_bf16 v[60:63], v[28:31], v[24:27], v[60:63]\n" \
  "ds_read_b128 v[24:27], v0 offset:14336\n" \
  "ds_read_b128 v[28:31], v1 offset:6144\n" \
  "v_mfma_f32_16x16x32_bf16 v[48:51], v[80:83], v[12:15], v[48:51]\n" \
  "v_mfma_f32_16x16x32_bf16 v[52:55], v[144:147], v[12:15], v[52:55]\n" \
  "v_mfma_f32_16x16x32_bf16 v[56:59], v[208:211], v[12:15], v[56:59]\n" \
  "s_waitcnt lgkmcnt(4)\n" \
  "v_mfma_f32_16x16x32_bf16 v[60:63], v[32:35], v[12:15], v[60:63]\n" \
  "ds_read_b128 v[12:15], v0 offset:16384\n" \
  "ds_read_b128 v[32:35], v1 offset:7168\n" \
  "v_mfma_f32_16x16x32_bf16 v[48:51], v[84:87], v[16:19], v[48:51]\n" \
  "v_mfma_f32_16x16x32_bf16 v[52:55], v[148:151], v[16:19], v[52:55]\n" \
  "v_mfma_f32_16x16x32_bf16 v[56:59], v[212:215], v[16:19], v[56:59]\n" \
  "s_waitcnt lgkmcnt(4)\n" \
  "v_mfma_f32_16x16x32_bf16 v[60:63], v[36:39], v[16:19], v[60:63]\n" \
  "ds_read_b128 v[16:19], v0 offset:18432\n" \
  "ds_read_b128 v[36:39], v1 offset:8192\n" \
  "v_mfma_f32_16x16x32_bf16 v[48:51], v[88:91], v[20:23], v[48:51]\n" \
  "v_mfma_f32_16x16x32_bf16 v[52:55], v[152:155], v[20:23], v[52:55]\n" \
  "v_mfma_f32_16x16x32_bf16 v[56:59], v[216:219], v[20:23], v[56:59]\n" \
  "s_waitcnt lgkmcnt(4)\n" \
  "v_mfma_f32_16x16x32_bf16 v[60:63], v[28:31], v[20:23], v[60:63]\n" \
  "ds_read_b128 v[20:23], v0 offset:20480\n" \
  "ds_read_b128 v[28:31], v1 offset:9216\n" \
  "v_mfma_f32_16x16x32_bf16 v[48:51], v[92:95], v[24:27], v[48:51]\n" \
  "v_mfma_f32_16x16x32_bf16 v[52:55], v[156:159], v[24:27], v[52:55]\n" \
  "v_mfma_f32_16x16x32_bf16 v[56:59], v[220:223], v[24:27], v[56:59]\n" \
  "s_waitcnt lgkmcnt(4)\n" \
  "v_mfma_f32_16x16x32_bf16 v[60:63], v[32:35], v[24:27], v[60:63]\n" \
  "ds_read_b128 v[24:27], v0 offset:22528\n" \
  "ds_read_b128 v[32:35], v1 offset:10240\n" \
  "v_mfma_f32_16x16x32_bf16 v[48:51], v[96:99], v[12:15], v[48:51]\n" \
  "v_mfma_f32_16x16x32_bf16 v[52:55], v[160:163], v[12:15], v[52:55]\n" \
  "v_mfma_f32_16x16x32_bf16 v[56:59], v[224:227], v[12:15], v[56:59]\n" \
  "s_waitcnt lgkmcnt(4)\n" \
  "v_mfma_f32_16x16x32_bf16 v[60:63], v[36:39], v[12:15], v[60:63]\n" \
  "ds_read_b128 v[12:15], v0 offset:24576\n" \
  "ds_read_b128 v[36:39], v1 offset:11264\n" \
  "v_mfma_f32_16x16x32_bf16 v[48:51], v[100:103], v[16:19], v[48:51]\n" \
  "v_mfma_f32_16x16x32_bf16 v[52:55], v[164:167], v[16:19], v[52:55]\n" \
  "v_mfma_f32_16x16x32_bf16 v[56:59], v[228:231], v[16:19], v[56:59]\n" \
  "s_waitcnt lgkmcnt(4)\n" \
  "v_mfma_f32_16x16x32_bf16 v[60:63], v[28:31], v[16:19], v[60:63]\n" \
  "ds_read_b32 v5, v6 offset:16\n" \
  "ds_read_b128 v[16:19], v0 offset:26624\n" \
  "ds_read_b128 v[28:31], v1 offset:12288\n" \
  "v_mfma_f32_16x16x32_bf16 v[48:51], v[104:107], v[20:23], v[48:51]\n" \
  "v_mfma_f32_16x16x32_bf16 v[52:55], v[168:171], v[20:23], v[52:55]\n" \
  "v_mfma_f32_16x16x32_bf16 v[56:59], v[232:235], v[20:23], v[56:59]\n" \
  "s_waitcnt lgkmcnt(5)\n" \
  "v_mfma_f32_16x16x32_bf16 v[60:63], v[32:35], v[20:23], v[60:63]\n" \
  "ds_read_b128 v[20:23], v0 offset:28672\n" \
  "ds_read_b128 v[32:35], v1 offset:13312\n" \
  "v_mfma_f32_16x16x32_bf16 v[48:51], v[108:111], v[24:27], v[48:51]\n" \
  "v_mfma_f32_16x16x32_bf16 v[52:55], v[172:175], v[24:27], v[52:55]\n" \
  "v_mfma_f32_16x16x32_bf16 v[56:59], v[236:239], v[24:27], v[56:59]\n" \
  "s_waitcnt lgkmcnt(5)\n" \
  "v_mfma_f32_16x16x32_bf16 v[60:63], v[36:39], v[24:27], v[60:63]\n" \
  "s_cmp_eq_u32 s68, 1\n" \
  "s_cbranch_scc0 .LCP_X3_%=\n" \
  "s_waitcnt vmcnt(0)\n" \
  "s_mov_b64 exec, 1\n" \
  "buffer_store_dword v7, off, s[56:59], s72 sc1\n" \
  "s_mov_b64 exec, -1\n" \
  "s_add_u32 s72, s72, 0x1000\n" \
  "s_mov_b32 s68, 0\n" \
  ".LCP_X3_%=:\n" \
  "s_waitcnt lgkmcnt(4)\n" \
  "v_readfirstlane_b32 s71, v5\n" \
  "s_cmp_ge_u32 s71, s67\n" \
  "s_cbranch_scc1 .LCP_ST_%=\n" \
  ".LCP_S4_%=:\n" \
  "s_sleep 1\n" \
  "ds_read_b32 v5, v6 offset:16\n" \
  "s_sub_u32 s69, s69, 1\n" \
  "s_cbranch_scc1 .LCP_END_%=\n" \
  "s_waitcnt lgkmcnt(0)\n" \
  "v_readfirstlane_b32 s71, v5\n" \
  "s_cmp_ge_u32 s71, s67\n" \
  "s_cbranch_scc1 .LCP_ST_%=\n" \
  "s_branch .LCP_S4_%=\n" \
  ".LCP_ST_%=:\n" \
  "ds_read_b128 v[24:27], v0 offset:30720\n" \
  "ds_read_b128 v[36:39], v1 offset:14336\n" \
  "ds_read_b128 v[40:43], v4 offset:0\n" \
  "ds_read_b128 v[44:47], v4 offset:64\n" \
  "ds_read2_b32 v[8:9], v3 offset0:0 offset1:32\n" \
  "v_mfma_f32_16x16x32_bf16 v[48:51], v[112:115], v[12:15], v[48:51]\n" \
  "v_mfma_f32_16x16x32_bf16 v[52:55], v[176:179], v[12:15], v[52:55]\n" \
  "v_mfma_f32_16x16x32_bf16 v[56:59], v[240:243], v[12:15], v[56:59]\n" \
  "s_waitcnt lgkmcnt(7)\n" \
  "v_mfma_f32_16x16x32_bf16 v[60:63], v[28:31], v[12:15], v[60:63]\n" \
  "ds_read_b128 v[12:15], v0 offset:256\n" \
  "ds_read_b128 v[28:31], v1 offset:15360\n" \
  "v_mfma_f32_16x16x32_bf16 v[48:51], v[116:119], v[16:19], v[48:51]\n" \
  "v_mfma_f32_16x16x32_bf16 v[52:55], v[180:183], v[16:19], v[52:55]\n" \
  "v_mfma_f32_16x16x32_bf16 v[56:59], v[244:247], v[16:19], v[56:59]\n" \
  "s_waitcnt lgkmcnt(7)\n" \
  "v_mfma_f32_16x16x32_bf16 v[60:63], v[32:35], v[16:19], v[60:63]\n" \
  "ds_read_b128 v[16:19], v0 offset:2304\n" \
  "ds_read_b128 v[32:35], v4 offset:128\n" \
  "v_mfma_f32_16x16x32_bf16 v[48:51], v[120:123], v[20:23], v[48:51]\n" \
  "s_waitcnt lgkmcnt(4)\n" \
  "v_mul_f32 v40, v40, v8\n" \
  "v_mul_f32 v41, v41, v8\n" \
  "v_mfma_f32_16x16x32_bf16 v[52:55], v[184:187], v[20:23], v[52:55]\n" \
  "v_mul_f32 v42, v42, v8\n" \
  "v_mul_f32 v43, v43, v8\n" \
  "v_mfma_f32_16x16x32_bf16 v[56:59], v[248:251], v[20:23], v[56:59]\n" \
  "v_mul_f32 v44, v44, v8\n" \
  "v_mul_f32 v45, v45, v8\n" \
  "v_mfma_f32_16x16x32_bf16 v[60:63], v[36:39], v[20:23], v[60:63]\n" \
  "v_mul_f32 v46, v46, v8\n" \
  "v_mul_f32 v47, v47, v8\n" \
  "ds_read_b128 v[20:23], v0 offset:4352\n" \
  "ds_read_b128 v[36:39], v4 offset:192\n" \
  "v_mfma_f32_16x16x32_bf16 v[48:51], v[124:127], v[24:27], v[48:51]\n" \
  "v_mfma_f32_16x16x32_bf16 v[52:55], v[188:191], v[24:27], v[52:55]\n" \
  "v_mfma_f32_16x16x32_bf16 v[56:59], v[252:255], v[24:27], v[56:59]\n" \
  "s_waitcnt lgkmcnt(2)\n" \
  "v_mul_f32 v32, v32, v8\n" \
  "v_mul_f32 v33, v33, v8\n" \
  "v_mfma_f32_16x16x32_bf16 v[60:63], v[28:31], v[24:27], v[60:63]\n" \
  "v_mul_f32 v34, v34, v8\n" \
  "v_mul_f32 v35, v35, v8\n" \
  "ds_read_b128 v[28:31], v1 offset:0\n" \
  "ds_read_b128 v[24:27], v0 offset:6400\n" \
  "v_fmac_f32 v40, v48, v9\n" \
  "v_fmac_f32 v41, v49, v9\n" \
  "v_fmac_f32 v42, v50, v9\n" \
  "v_fmac_f32 v43, v51, v9\n" \
  "v_mfma_f32_16x16x32_bf16 v[48:51], v[64:67], v[12:15], 0\n" \
  "v_fmac_f32 v44, v52, v9\n" \
  "v_fmac_f32 v45, v53, v9\n" \
  "v_fmac_f32 v46, v54, v9\n" \
  "v_fmac_f32 v47, v55, v9\n" \
  "v_mfma_f32_16x16x32_bf16 v[52:55], v[128:131], v[12:15], 0\n" \
  "v_cvt_pk_bf16_f32 v40, v40, v41\n" \
  "v_cvt_pk_bf16_f32 v41, v42, v43\n" \
  "v_cvt_pk_bf16_f32 v42, v44, v45\n" \
  "v_cvt_pk_bf16_f32 v43, v46, v47\n" \
  "v_bfi_b32 v40, v11, s73, v40\n" \
  "v_fmac_f32 v32, v56, v9\n" \
  "v_fmac_f32 v33, v57, v9\n" \
  "v_fmac_f32 v34, v58, v9\n" \
  "v_fmac_f32 v35, v59, v9\n" \
  "v_mfma_f32_16x16x32_bf16 v[56:59], v[192:195], v[12:15], 0\n" \
  "s_waitcnt lgkmcnt(2)\n" \
  "v_mul_f32 v36, v36, v8\n" \
  "v_mul_f32 v37, v37, v8\n" \
  "v_mul_f32 v38, v38, v8\n" \
  "v_mul_f32 v39, v39, v8\n" \
  "v_fmac_f32 v36, v60, v9\n" \
  "v_fmac_f32 v37, v61, v9\n" \
  "v_fmac_f32 v38, v62, v9\n" \
  "v_fmac_f32 v39, v63, v9\n" \
  "s_waitcnt lgkmcnt(1)\n" \
  "v_mfma_f32_16x16x32_bf16 v[60:63], v[28:31], v[12:15], 0\n" \
  "v_cvt_pk_bf16_f32 v44, v32, v33\n" \
  "v_cvt_pk_bf16_f32 v45, v34, v35\n" \
  "v_cvt_pk_bf16_f32 v46, v36, v37\n" \
  "v_cvt_pk_bf16_f32 v47, v38, v39\n" \
  "v_bfi_b32 v44, v11, s73, v44\n" \
  "ds_read_b128 v[32:35], v1 offset:1024\n" \
  "ds_read_b128 v[36:39], v1 offset:2048\n" \
  "ds_read_b128 v[12:15], v0 offset:8448\n" \
  "ds_read_b128 v[28:31], v1 offset:3072\n" \
  "v_mfma_f32_16x16x32_bf16 v[48:51], v[68:71], v[16:19], v[48:51]\n" \
  "buffer_store_dwordx4 v[40:43], v2, s[40:43], s65 offen offset:0 sc1\n" \
  "v_mfma_f32_16x16x32_bf16 v[52:55], v[132:135], v[16:19], v[52:55]\n" \
  "buffer_store_dwordx4 v[44:47], v2, s[40:43], s65 offen offset:1024 sc1\n" \
  "v_mfma_f32_16x16x32_bf16 v[56:59], v[196:199], v[16:19], v[56:59]\n" \
  "s_waitcnt lgkmcnt(3)\n" \
  "v_mfma_f32_16x16x32_bf16 v[60:63], v[32:35], v[16:19], v[60:63]\n" \
  "ds_read_b128 v[16:19], v0 offset:10496\n" \
  "ds_read_b128 v[32:35], v1 offset:4096\n" \
  "v_mfma_f32_16x16x32_bf16 v[48:51], v[72:75], v[20:23], v[48:51]\n" \
  "v_mfma_f32_16x16x32_bf16 v[52:55], v[136:139], v[20:23], v[52:55]\n" \
  "v_mfma_f32_16x16x32_bf16 v[56:59], v[200:203], v[20:23], v[56:59]\n" \
  "s_waitcnt lgkmcnt(4)\n" \
  "v_mfma_f32_16x16x32_bf16 v[60:63], v[36:39], v[20:23], v[60:63]\n" \
  "ds_read_b128 v[20:23], v0 offset:12544\n" \
  "ds_read_b128 v[36:39], v1 offset:5120\n" \
  "v_mfma_f32_16x16x32_bf16 v[48:51], v[76:79], v[24:27], v[48:51]\n" \
  "v_mfma_f32_16x16x32_bf16 v[52:55], v[140:143], v[24:27], v[52:55]\n" \
  "v_mfma_f32_16x16x32_bf16 v[56:59], v[204:207], v[24:27], v[56:59]\n" \
  "s_waitcnt lgkmcnt(4)\n" \
  "v_mfma_f32_16x16x32_bf16 v[60:63], v[28:31], v[24:27], v[60:63]\n" \
  "ds_read_b128 v[24:27], v0 offset:14592\n" \
  "ds_read_b128 v[28:31], v1 offset:6144\n" \
  "v_mfma_f32_16x16x32_bf16 v[48:51], v[80:83], v[12:15], v[48:51]\n" \
  "v_mfma_f32_16x16x32_bf16 v[52:55], v[144:147], v[12:15], v[52:55]\n" \
  "v_mfma_f32_16x16x32_bf16 v[56:59], v[208:211], v[12:15], v[56:59]\n" \
  "s_waitcnt lgkmcnt(4)\n" \
  "v_mfma_f32_16x16x32_bf16 v[60:63], v[32:35], v[12:15], v[60:63]\n" \
  "ds_read_b128 v[12:15], v0 offset:16640\n" \
  "ds_read_b128 v[32:35], v1 offset:7168\n" \
  "v_mfma_f32_16x16x32_bf16 v[48:51], v[84:87], v[16:19], v[48:51]\n" \
  "v_mfma_f32_16x16x32_bf16 v[52:55], v[148:151], v[16:19], v[52:55]\n" \
  "v_mfma_f32_16x16x32_bf16 v[56:59], v[212:215], v[16:19], v[56:59]\n" \
  "s_waitcnt lgkmcnt(4)\n" \
  "v_mfma_f32_16x16x32_bf16 v[60:63], v[36:39], v[16:19], v[60:63]\n" \
  "ds_read_b128 v[16:19], v0 offset:18688\n" \
  "ds_read_b128 v[36:39], v1 offset:8192\n" \
  "v_mfma_f32_16x16x32_bf16 v[48:51], v[88:91], v[20:23], v[48:51]\n" \
  "v_mfma_f32_16x16x32_bf16 v[52:55], v[152:155], v[20:23], v[52:55]\n" \
  "v_mfma_f32_16x16x32_bf16 v[56:59], v[216:219], v[20:23], v[56:59]\n" \
  "s_waitcnt lgkmcnt(4)\n" \
  "v_mfma_f32_16x16x32_bf16 v[60:63], v[28:31], v[20:23], v[60:63]\n" \
  "ds_read_b128 v[20:23], v0 offset:20736\n" \
  "ds_read_b128 v[28:31], v1 offset:9216\n" \
  "v_mfma_f32_16x16x32_bf16 v[48:51], v[92:95], v[24:27], v[48:51]\n" \
  "v_mfma_f32_16x16x32_bf16 v[52:55], v[156:159], v[24:27], v[52:55]\n" \
  "v_mfma_f32_16x16x32_bf16 v[56:59], v[220:223], v[24:27], v[56:59]\n" \
  "s_waitcnt lgkmcnt(4)\n" \
  "v_mfma_f32_16x16x32_bf16 v[60:63], v[32:35], v[24:27], v[60:63]\n" \
  "ds_read_b128 v[24:27], v0 offset:22784\n" \
  "ds_read_b128 v[32:35], v1 offset:10240\n" \
  "v_mfma_f32_16x16x32_bf16 v[48:51], v[96:99], v[12:15], v[48:51]\n" \
  "v_mfma_f32_16x16x32_bf16 v[52:55], v[160:163], v[12:15], v[52:55]\n" \
  "v_mfma_f32_16x16x32_bf16 v[56:59], v[224:227], v[12:15], v[56:59]\n" \
  "s_waitcnt lgkmcnt(4)\n" \
  "v_mfma_f32_16x16x32_bf16 v[60:63], v[36:39], v[12:15], v[60:63]\n" \
  "ds_read_b128 v[12:15], v0 offset:24832\n" \
  "ds_read_b128 v[36:39], v1 offset:11264\n" \
  "v_mfma_f32_16x16x32_bf16 v[48:51], v[100:103], v[16:19], v[48:51]\n" \
  "v_mfma_f32_16x16x32_bf16 v[52:55], v[164:167], v[16:19], v[52:55]\n" \
  "v_mfma_f32_16x16x32_bf16 v[56:59], v[228:231], v[16:19], v[56:59]\n" \
  "s_waitcnt lgkmcnt(4)\n" \
  "v_mfma_f32_16x16x32_bf16 v[60:63], v[28:31], v[16:19], v[60:63]\n" \
  "ds_read_b128 v[16:19], v0 offset:26880\n" \
  "ds_read_b128 v[28:31], v1 offset:12288\n" \
  "v_mfma_f32_16x16x32_bf16 v[48:51], v[104:107], v[20:23], v[48:51]\n" \
  "v_mfma_f32_16x16x32_bf16 v[52:55], v[168:171], v[20:23], v[52:55]\n" \
  "v_mfma_f32_16x16x32_bf16 v[56:59], v[232:235], v[20:23], v[56:59]\n" \
  "s_waitcnt lgkmcnt(4)\n" \
  "v_mfma_f32_16x16x32_bf16 v[60:63], v[32:35], v[20:23], v[60:63]\n" \
  "ds_read_b128 v[20:23], v0 offset:28928\n" \
  "ds_read_b128 v[32:35], v1 offset:13312\n" \
  "v_mfma_f32_16x16x32_bf16 v[48:51], v[108:111], v[24:27], v[48:51]\n" \
  "v_mfma_f32_16x16x32_bf16 v[52:55], v[172:175], v[24:27], v[52:55]\n" \
  "v_mfma_f32_16x16x32_bf16 v[56:59], v[236:239], v[24:27], v[56:59]\n" \
  "s_waitcnt lgkmcnt(4)\n" \
  "v_mfma_f32_16x16x32_bf16 v[60:63], v[36:39], v[24:27], v[60:63]\n" \
  "ds_read_b128 v[24:27], v0 offset:30976\n" \
  "ds_read_b128 v[36:39], v1 offset:14336\n" \
  "ds_read_b128 v[40:43], v4 offset:0\n" \
  "ds_read_b128 v[44:47], v4 offset:64\n" \
  "ds_read2_b32 v[8:9], v3 offset0:16 offset1:48\n" \
  "v_mfma_f32_16x16x32_bf16 v[48:51], v[112:115], v[12:15], v[48:51]\n" \
  "v_mfma_f32_16x16x32_bf16 v[52:55], v[176:179], v[12:15], v[52:55]\n" \
  "v_mfma_f32_16x16x32_bf16 v[56:59], v[240:243], v[12:15], v[56:59]\n" \
  "s_waitcnt lgkmcnt(7)\n" \
  "v_mfma_f32_16x16x32_bf16 v[60:63], v[28:31], v[12:15], v[60:63]\n" \
  "ds_read_b128 v[28:31], v1 offset:15360\n" \
  "v_mfma_f32_16x16x32_bf16 v[48:51], v[116:119], v[16:19], v[48:51]\n" \
  "v_mfma_f32_16x16x32_bf16 v[52:55], v[180:183], v[16:19], v[52:55]\n" \
  "v_mfma_f32_16x16x32_bf16 v[56:59], v[244:247], v[16:19], v[56:59]\n" \
  "s_waitcnt lgkmcnt(6)\n" \
  "v_mfma_f32_16x16x32_bf16 v[60:63], v[32:35], v[16:19], v[60:63]\n" \
  "ds_read_b128 v[32:35], v4 offset:128\n" \
  "v_mfma_f32_16x16x32_bf16 v[48:51], v[120:123], v[20:23], v[48:51]\n" \
  "s_waitcnt lgkmcnt(2)\n" \
  "v_mul_f32 v40, v40, v8\n" \
  "v_mul_f32 v41, v41, v8\n" \
  "v_mfma_f32_16x16x32_bf16 v[52:55], v[184:187], v[20:23], v[52:55]\n" \
  "v_mul_f32 v42, v42, v8\n" \
  "v_mul_f32 v43, v43, v8\n" \
  "v_mfma_f32_16x16x32_bf16 v[56:59], v[248:251], v[20:23], v[56:59]\n" \
  "v_mul_f32 v44, v44, v8\n" \
  "v_mul_f32 v45, v45, v8\n" \
  "v_mfma_f32_16x16x32_bf16 v[60:63], v[36:39], v[20:23], v[60:63]\n" \
  "v_mul_f32 v46, v46, v8\n" \
  "v_mul_f32 v47, v47, v8\n" \
  "ds_read_b128 v[36:39], v4 offset:192\n" \
  "s_mov_b64 exec, 1\n" \
  "ds_add_u32 v6, v7 offset:8\n" \
  "s_mov_b64 exec, -1\n" \
  "v_xor_b32 v6, 4, v6\n" \
  "v_mfma_f32_16x16x32_bf16 v[48:51], v[124:127], v[24:27], v[48:51]\n" \
  "v_mfma_f32_16x16x32_bf16 v[52:55], v[188:191], v[24:27], v[52:55]\n" \
  "v_mfma_f32_16x16x32_bf16 v[56:59], v[252:255], v[24:27], v[56:59]\n" \
  "s_waitcnt lgkmcnt(2)\n" \
  "v_mul_f32 v32, v32, v8\n" \
  "v_mul_f32 v33, v33, v8\n" \
  "v_mfma_f32_16x16x32_bf16 v[60:63], v[28:31], v[24:27], v[60:63]\n" \
  "v_mul_f32 v34, v34, v8\n" \
  "v_mul_f32 v35, v35, v8\n" \
  "v_fmac_f32 v40, v48, v9\n" \
  "v_fmac_f32 v41, v49, v9\n" \
  "v_fmac_f32 v42, v50, v9\n" \
  "v_fmac_f32 v43, v51, v9\n" \
  "v_fmac_f32 v44, v52, v9\n" \
  "v_fmac_f32 v45, v53, v9\n" \
  "v_fmac_f32 v46, v54, v9\n" \
  "v_fmac_f32 v47, v55, v9\n" \
  "ds_read_b32 v5, v6 offset:0\n" \
  "v_cvt_pk_bf16_f32 v40, v40, v41\n" \
  "v_cvt_pk_bf16_f32 v41, v42, v43\n" \
  "v_cvt_pk_bf16_f32 v42, v44, v45\n" \
  "v_cvt_pk_bf16_f32 v43, v46, v47\n" \
  "v_bfi_b32 v40, v11, s73, v40\n" \
  "buffer_store_dwordx4 v[40:43], v10, s[40:43], s65 offen offset:0 sc1\n" \
  "v_fmac_f32 v32, v56, v9\n" \
  "v_fmac_f32 v33, v57, v9\n" \
  "v_fmac_f32 v34, v58, v9\n" \
  "v_fmac_f32 v35, v59, v9\n" \
  "s_waitcnt lgkmcnt(2)\n" \
  "v_mul_f32 v36, v36, v8\n" \
  "v_mul_f32 v37, v37, v8\n" \
  "v_mul_f32 v38, v38, v8\n" \
  "v_mul_f32 v39, v39, v8\n" \
  "v_fmac_f32 v36, v60, v9\n" \
  "v_fmac_f32 v37, v61, v9\n" \
  "v_fmac_f32 v38, v62, v9\n" \
  "v_fmac_f32 v39, v63, v9\n" \
  "v_cvt_pk_bf16_f32 v44, v32, v33\n" \
  "v_cvt_pk_bf16_f32 v45, v34, v35\n" \
  "v_cvt_pk_bf16_f32 v46, v36, v37\n" \
  "v_cvt_pk_bf16_f32 v47, v38, v39\n" \
  "v_bfi_b32 v44, v11, s73, v44\n" \
  "buffer_store_dwordx4 v[44:47], v10, s[40:43], s65 offen offset:1024 sc1\n" \
  "s_waitcnt lgkmcnt(0)\n" \
  "s_add_u32 s65, s65, 0x80000\n" \
  "s_sub_u32 s64, s64, 1\n" \
  "s_mov_b32 s68, 1\n" \
  "s_cmp_lg_u32 s64, 0\n" \
  "s_cbranch_scc1 .LCP_TOP_%=\n" \
  "s_waitcnt vmcnt(0)\n" \
  "s_mov_b64 exec, 1\n" \
  "buffer_store_dword v7, off, s[56:59], s72 sc1\n" \
  "s_mov_b64 exec, -1\n" \
  "s_add_u32 s72, s72, 0x1000\n" \
  "s_mov_b32 s68, 0\n" \
  ".LCP_END_%=:\n" \
  "s_endpgm\n"
#define V_ASM \
  "s_mov_b32 s68, 0\n" \
  "s_mov_b32 s66, 3\n" \
  "s_mov_b32 s67, 2\n" \
  "s_mov_b32 s72, 0\n" \
  "v_mov_b32 v7, 1\n" \
  "v_mov_b32 v6, s70\n" \
  "v_mov_b32 v5, 0\n" \
  "ds_read_b32 v8, v4 offset:0\n" \
  "ds_read_b32 v9, v4 offset:64\n" \
  "ds_read_b32 v10, v4 offset:128\n" \
  "ds_read_b32 v4, v4 offset:192\n" \
  "s_waitcnt lgkmcnt(0)\n" \
  ".p2align 6\n" \
  ".LCW_TOP_%=:\n" \
  "ds_read_b128 v[28:31], v1 offset:0\n" \
  "ds_read_b128 v[32:35], v1 offset:1024\n" \
  "s_sub_u32 s73, s64, 1\n" \
  "s_bitset0_b32 s65, s74\n" \
  "s_bfe_u32 s73, s73, 0x10004\n" \
  "s_cmp_gt_u32 s64, s75\n" \
  "s_cselect_b32 s76, 1, 0\n" \
  "v_mov_b32 v11, s76\n" \
  "s_add_u32 s66, s66, 3\n" \
  "s_add_u32 s67, s67, 2\n" \
  "s_mov_b32 s69, 0x400000\n" \
  "v_xor_b32 v0, 0x8000, v0\n" \
  "v_xor_b32 v3, 0x100, v3\n" \
  "v_readfirstlane_b32 s71, v5\n" \
  "s_cmp_ge_u32 s71, s66\n" \
  "s_cbranch_scc1 .LCW_GO_%=\n" \
  "s_cmp_eq_u32 s68, 1\n" \
  "s_cbranch_scc0 .LCW_X1_%=\n" \
  "s_waitcnt vmcnt(0)\n" \
  "s_mov_b64 exec, 1\n" \
  "buffer_store_dword v7, off, s[56:59], s72 sc1\n" \
  "s_mov_b64 exec, -1\n" \
  "s_add_u32 s72, s72, 0x1000\n" \
  "s_mov_b32 s68, 0\n" \
  ".LCW_X1_%=:\n" \
  ".LCW_S2_%=:\n" \
  "s_sleep 1\n" \
  "ds_read_b32 v5, v6 offset:0\n" \
  "s_sub_u32 s69, s69, 1\n" \
  "s_cbranch_scc1 .LCW_END_%=\n" \
  "s_waitcnt lgkmcnt(0)\n" \
  "v_readfirstlane_b32 s71, v5\n" \
  "s_cmp_ge_u32 s71, s66\n" \
  "s_cbranch_scc1 .LCW_GO_%=\n" \
  "s_branch .LCW_S2_%=\n" \
  ".LCW_GO_%=:\n" \
  "ds_read_b128 v[12:15], v0 offset:0\n" \
  "ds_read_b128 v[16:19], v0 offset:2048\n" \
  "ds_read_b128 v[20:23], v0 offset:4096\n" \
  "ds_read_b128 v[24:27], v0 offset:6144\n" \
  "ds_read_b128 v[36:39], v1 offset:2048\n" \
  "s_waitcnt lgkmcnt(4)\n" \
  "v_mfma_f32_16x16x32_bf16 v[42:45], v[12:15], v[64:67], 0\n" \
  "v_mfma_f32_16x16x32_bf16 v[48:51], v[12:15], v[128:131], 0\n" \
  "v_mfma_f32_16x16x32_bf16 v[54:57], v[12:15], v[192:195], 0\n" \
  "v_mfma_f32_16x16x32_bf16 v[60:63], v[12:15], v[28:31], 0\n" \
  "ds_read_b128 v[12:15], v0 offset:8192\n" \
  "ds_read_b128 v[28:31], v1 offset:3072\n" \
  "s_waitcnt lgkmcnt(5)\n" \
  "v_mfma_f32_16x16x32_bf16 v[42:45], v[16:19], v[68:71], v[42:45]\n" \
  "v_mfma_f32_16x16x32_bf16 v[48:51], v[16:19], v[132:135], v[48:51]\n" \
  "v_mfma_f32_16x16x32_bf16 v[54:57], v[16:19], v[196:199], v[54:57]\n" \
  "v_mfma_f32_16x16x32_bf16 v[60:63], v[16:19], v[32:35], v[60:63]\n" \
  "ds_read_b128 v[16:19], v0 offset:10240\n" \
  "ds_read_b128 v[32:35], v1 offset:4096\n" \
  "s_waitcnt lgkmcnt(6)\n" \
  "v_mfma_f32_16x16x32_bf16 v[42:45], v[20:23], v[72:75], v[42:45]\n" \
  "v_mfma_f32_16x16x32_bf16 v[48:51], v[20:23], v[136:139], v[48:51]\n" \
  "v_mfma_f32_16x16x32_bf16 v[54:57], v[20:23], v[200:203], v[54:57]\n" \
  "s_waitcnt lgkmcnt(4)\n" \
  "v_mfma_f32_16x16x32_bf16 v[60:63], v[20:23], v[36:39], v[60:63]\n" \
  "ds_read_b128 v[20:23], v0 offset:12288\n" \
  "ds_read_b128 v[36:39], v1 offset:5120\n" \
  "v_mfma_f32_16x16x32_bf16 v[42:45], v[24:27], v[76:79], v[42:45]\n" \
  "v_mfma_f32_16x16x32_bf16 v[48:51], v[24:27], v[140:143], v[48:51]\n" \
  "v_mfma_f32_16x16x32_bf16 v[54:57], v[24:27], v[204:207], v[54:57]\n" \
  "s_waitcnt lgkmcnt(4)\n" \
  "v_mfma_f32_16x16x32_bf16 v[60:63], v[24:27], v[28:31], v[60:63]\n" \
  "ds_read_b128 v[24:27], v0 offset:14336\n" \
  "ds_read_b128 v[28:31], v1 offset:6144\n" \
  "v_mfma_f32_16x16x32_bf16 v[42:45], v[12:15], v[80:83], v[42:45]\n" \
  "v_mfma_f32_16x16x32_bf16 v[48:51], v[12:15], v[144:147], v[48:51]\n" \
  "v_mfma_f32_16x16x32_bf16 v[54:57], v[12:15], v[208:211], v[54:57]\n" \
  "s_waitcnt lgkmcnt(4)\n" \
  "v_mfma_f32_16x16x32_bf16 v[60:63], v[12:15], v[32:35], v[60:63]\n" \
  "ds_read_b128 v[12:15], v0 offset:16384\n" \
  "ds_read_b128 v[32:35], v1 offset:7168\n" \
  "v_mfma_f32_16x16x32_bf16 v[42:45], v[16:19], v[84:87], v[42:45]\n" \
  "v_mfma_f32_16x16x32_bf16 v[48:51], v[16:19], v[148:151], v[48:51]\n" \
  "v_mfma_f32_16x16x32_bf16 v[54:57], v[16:19], v[212:215], v[54:57]\n" \
  "s_waitcnt lgkmcnt(4)\n" \
  "v_mfma_f32_16x16x32_bf16 v[60:63], v[16:19], v[36:39], v[60:63]\n" \
  "ds_read_b128 v[16:19], v0 offset:18432\n" \
  "ds_read_b128 v[36:39], v1 offset:8192\n" \
  "v_mfma_f32_16x16x32_bf16 v[42:45], v[20:23], v[88:91], v[42:45]\n" \
  "v_mfma_f32_16x16x32_bf16 v[48:51], v[20:23], v[152:155], v[48:51]\n" \
  "v_mfma_f32_16x16x32_bf16 v[54:57], v[20:23], v[216:219], v[54:57]\n" \
  "s_waitcnt lgkmcnt(4)\n" \
  "v_mfma_f32_16x16x32_bf16 v[60:63], v[20:23], v[28:31], v[60:63]\n" \
  "ds_read_b128 v[20:23], v0 offset:20480\n" \
  "ds_read_b128 v[28:31], v1 offset:9216\n" \
  "v_mfma_f32_16x16x32_bf16 v[42:45], v[24:27], v[92:95], v[42:45]\n" \
  "v_mfma_f32_16x16x32_bf16 v[48:51], v[24:27], v[156:159], v[48:51]\n" \
  "v_mfma_f32_16x16x32_bf16 v[54:57], v[24:27], v[220:223], v[54:57]\n" \
  "s_waitcnt lgkmcnt(4)\n" \
  "v_mfma_f32_16x16x32_bf16 v[60:63], v[24:27], v[32:35], v[60:63]\n" \
  "ds_read_b128 v[24:27], v0 offset:22528\n" \
  "ds_read_b128 v[32:35], v1 offset:10240\n" \
  "v_mfma_f32_16x16x32_bf16 v[42:45], v[12:15], v[96:99], v[42:45]\n" \
  "v_mfma_f32_16x16x32_bf16 v[48:51], v[12:15], v[160:163], v[48:51]\n" \
  "v_mfma_f32_16x16x32_bf16 v[54:57], v[12:15], v[224:227], v[54:57]\n" \
  "s_waitcnt lgkmcnt(4)\n" \
  "v_mfma_f32_16x16x32_bf16 v[60:63], v[12:15], v[36:39], v[60:63]\n" \
  "ds_read_b128 v[12:15], v0 offset:24576\n" \
  "ds_read_b128 v[36:39], v1 offset:11264\n" \
  "v_mfma_f32_16x16x32_bf16 v[42:45], v[16:19], v[100:103], v[42:45]\n" \
  "v_mfma_f32_16x16x32_bf16 v[48:51], v[16:19], v[164:167], v[48:51]\n" \
  "v_mfma_f32_16x16x32_bf16 v[54:57], v[16:19], v[228:231], v[54:57]\n" \
  "s_waitcnt lgkmcnt(4)\n" \
  "v_mfma_f32_16x16x32_bf16 v[60:63], v[16:19], v[28:31], v[60:63]\n" \
  "ds_read_b128 v[16:19], v0 offset:26624\n" \
  "ds_read_b128 v[28:31], v1 offset:12288\n" \
  "v_mfma_f32_16x16x32_bf16 v[42:45], v[20:23], v[104:107], v[42:45]\n" \
  "v_mfma_f32_16x16x32_bf16 v[48:51], v[20:23], v[168:171], v[48:51]\n" \
  "v_mfma_f32_16x16x32_bf16 v[54:57], v[20:23], v[232:235], v[54:57]\n" \
  "s_waitcnt lgkmcnt(4)\n" \
  "v_mfma_f32_16x16x32_bf16 v[60:63], v[20:23], v[32:35], v[60:63]\n" \
  "ds_read_b128 v[20:23], v0 offset:28672\n" \
  "ds_read_b128 v[32:35], v1 offset:13312\n" \
  "v_mfma_f32_16x16x32_bf16 v[42:45], v[24:27], v[108:111], v[42:45]\n" \
  "v_mfma_f32_16x16x32_bf16 v[48:51], v[24:27], v[172:175], v[48:51]\n" \
  "v_mfma_f32_16x16x32_bf16 v[54:57], v[24:27], v[236:239], v[54:57]\n" \
  "s_waitcnt lgkmcnt(4)\n" \
  "v_mfma_f32_16x16x32_bf16 v[60:63], v[24:27], v[36:39], v[60:63]\n" \
  "s_cmp_eq_u32 s68, 1\n" \
  "s_cbranch_scc0 .LCW_X3_%=\n" \
  "s_waitcnt vmcnt(0)\n" \
  "s_mov_b64 exec, 1\n" \
  "buffer_store_dword v7, off, s[56:59], s72 sc1\n" \
  "s_mov_b64 exec, -1\n" \
  "s_add_u32 s72, s72, 0x1000\n" \
  "s_mov_b32 s68, 0\n" \
  ".LCW_X3_%=:\n" \
  "ds_read_b32 v5, v6 offset:16\n" \
  "ds_read_b128 v[24:27], v0 offset:30720\n" \
  "ds_read_b128 v[36:39], v1 offset:14336\n" \
  "v_mfma_f32_16x16x32_bf16 v[42:45], v[12:15], v[112:115], v[42:45]\n" \
  "v_mfma_f32_16x16x32_bf16 v[48:51], v[12:15], v[176:179], v[48:51]\n" \
  "v_mfma_f32_16x16x32_bf16 v[54:57], v[12:15], v[240:243], v[54:57]\n" \
  "s_waitcnt lgkmcnt(5)\n" \
  "v_mfma_f32_16x16x32_bf16 v[60:63], v[12:15], v[28:31], v[60:63]\n" \
  "ds_read_b128 v[12:15], v0 offset:256\n" \
  "ds_read_b128 v[28:31], v1 offset:15360\n" \
  "v_mfma_f32_16x16x32_bf16 v[42:45], v[16:19], v[116:119], v[42:45]\n" \
  "v_mfma_f32_16x16x32_bf16 v[48:51], v[16:19], v[180:183], v[48:51]\n" \
  "v_mfma_f32_16x16x32_bf16 v[54:57], v[16:19], v[244:247], v[54:57]\n" \
  "s_waitcnt lgkmcnt(5)\n" \
  "v_mfma_f32_16x16x32_bf16 v[60:63], v[16:19], v[32:35], v[60:63]\n" \
  "s_waitcnt lgkmcnt(4)\n" \
  "v_readfirstlane_b32 s71, v5\n" \
  "s_cmp_ge_u32 s71, s67\n" \
  "s_cbranch_scc1 .LCW_ST_%=\n" \
  ".LCW_S4_%=:\n" \
  "s_sleep 1\n" \
  "ds_read_b32 v5, v6 offset:16\n" \
  "s_sub_u32 s69, s69, 1\n" \
  "s_cbranch_scc1 .LCW_END_%=\n" \
  "s_waitcnt lgkmcnt(0)\n" \
  "v_readfirstlane_b32 s71, v5\n" \
  "s_cmp_ge_u32 s71, s67\n" \
  "s_cbranch_scc1 .LCW_ST_%=\n" \
  "s_branch .LCW_S4_%=\n" \
  ".LCW_ST_%=:\n" \
  "ds_read_b128 v[16:19], v0 offset:2304\n" \
  "ds_read_b128 v[32:35], v3 offset:128\n" \
  "v_mfma_f32_16x16x32_bf16 v[42:45], v[20:23], v[120:123], v[42:45]\n" \
  "v_mfma_f32_16x16x32_bf16 v[48:51], v[20:23], v[184:187], v[48:51]\n" \
  "v_mfma_f32_16x16x32_bf16 v[54:57], v[20:23], v[248:251], v[54:57]\n" \
  "s_waitcnt lgkmcnt(4)\n" \
  "v_mfma_f32_16x16x32_bf16 v[60:63], v[20:23], v[36:39], v[60:63]\n" \
  "ds_read_b128 v[20:23], v0 offset:4352\n" \
  "ds_read_b128 v[36:39], v3 offset:0\n" \
  "v_mfma_f32_16x16x32_bf16 v[42:45], v[24:27], v[124:127], v[42:45]\n" \
  "v_mfma_f32_16x16x32_bf16 v[48:51], v[24:27], v[188:191], v[48:51]\n" \
  "v_mfma_f32_16x16x32_bf16 v[54:57], v[24:27], v[252:255], v[54:57]\n" \
  "s_waitcnt lgkmcnt(4)\n" \
  "v_mfma_f32_16x16x32_bf16 v[60:63], v[24:27], v[28:31], v[60:63]\n" \
  "ds_read_b128 v[28:31], v1 offset:0\n" \
  "ds_read_b128 v[24:27], v0 offset:6400\n" \
  "s_waitcnt lgkmcnt(4)\n" \
  "v_mul_f32 v42, v42, v32\n" \
  "v_mul_f32 v43, v43, v33\n" \
  "v_mul_f32 v44, v44, v34\n" \
  "v_mul_f32 v45, v45, v35\n" \
  "s_waitcnt lgkmcnt(2)\n" \
  "v_fmac_f32 v42, v36, v8\n" \
  "v_fmac_f32 v43, v37, v8\n" \
  "v_fmac_f32 v44, v38, v8\n" \
  "v_fmac_f32 v45, v39, v8\n" \
  "v_cvt_pk_bf16_f32 v40, v42, v43\n" \
  "v_cvt_pk_bf16_f32 v41, v44, v45\n" \
  "v_bfi_b32 v40, v11, s73, v40\n" \
  "v_mfma_f32_16x16x32_bf16 v[42:45], v[12:15], v[64:67], 0\n" \
  "v_mul_f32 v48, v48, v32\n" \
  "v_mul_f32 v49, v49, v33\n" \
  "v_mul_f32 v50, v50, v34\n" \
  "v_mul_f32 v51, v51, v35\n" \
  "v_fmac_f32 v48, v36, v9\n" \
  "v_fmac_f32 v49, v37, v9\n" \
  "v_fmac_f32 v50, v38, v9\n" \
  "v_fmac_f32 v51, v39, v9\n" \
  "v_cvt_pk_bf16_f32 v46, v48, v49\n" \
  "v_cvt_pk_bf16_f32 v47, v50, v51\n" \
  "v_bfi_b32 v46, v11, s73, v46\n" \
  "v_mfma_f32_16x16x32_bf16 v[48:51], v[12:15], v[128:131], 0\n" \
  "v_mul_f32 v54, v54, v32\n" \
  "v_mul_f32 v55, v55, v33\n" \
  "v_mul_f32 v56, v56, v34\n" \
  "v_mul_f32 v57, v57, v35\n" \
  "v_fmac_f32 v54, v36, v10\n" \
  "v_fmac_f32 v55, v37, v10\n" \
  "v_fmac_f32 v56, v38, v10\n" \
  "v_fmac_f32 v57, v39, v10\n" \
  "v_cvt_pk_bf16_f32 v52, v54, v55\n" \
  "v_cvt_pk_bf16_f32 v53, v56, v57\n" \
  "v_bfi_b32 v52, v11, s73, v52\n" \
  "v_mfma_f32_16x16x32_bf16 v[54:57], v[12:15], v[192:195], 0\n" \
  "v_mul_f32 v60, v60, v32\n" \
  "v_mul_f32 v61, v61, v33\n" \
  "v_mul_f32 v62, v62, v34\n" \
  "v_mul_f32 v63, v63, v35\n" \
  "v_fmac_f32 v60, v36, v4\n" \
  "v_fmac_f32 v61, v37, v4\n" \
  "v_fmac_f32 v62, v38, v4\n" \
  "v_fmac_f32 v63, v39, v4\n" \
  "v_cvt_pk_bf16_f32 v58, v60, v61\n" \
  "v_cvt_pk_bf16_f32 v59, v62, v63\n" \
  "v_bfi_b32 v58, v11, s73, v58\n" \
  "ds_read_b128 v[32:35], v1 offset:1024\n" \
  "ds_read_b128 v[36:39], v1 offset:2048\n" \
  "s_waitcnt lgkmcnt(3)\n" \
  "v_mfma_f32_16x16x32_bf16 v[60:63], v[12:15], v[28:31], 0\n" \
  "ds_read_b128 v[12:15], v0 offset:8448\n" \
  "ds_read_b128 v[28:31], v1 offset:3072\n" \
  "v_mfma_f32_16x16x32_bf16 v[42:45], v[16:19], v[68:71], v[42:45]\n" \
  "v_mfma_f32_16x16x32_bf16 v[48:51], v[16:19], v[132:135], v[48:51]\n" \
  "v_mfma_f32_16x16x32_bf16 v[54:57], v[16:19], v[196:199], v[54:57]\n" \
  "s_waitcnt lgkmcnt(3)\n" \
  "v_mfma_f32_16x16x32_bf16 v[60:63], v[16:19], v[32:35], v[60:63]\n" \
  "ds_read_b128 v[16:19], v0 offset:10496\n" \
  "ds_read_b128 v[32:35], v1 offset:4096\n" \
  "v_mfma_f32_16x16x32_bf16 v[42:45], v[20:23], v[72:75], v[42:45]\n" \
  "v_mfma_f32_16x16x32_bf16 v[48:51], v[20:23], v[136:139], v[48:51]\n" \
  "v_mfma_f32_16x16x32_bf16 v[54:57], v[20:23], v[200:203], v[54:57]\n" \
  "s_waitcnt lgkmcnt(4)\n" \
  "v_mfma_f32_16x16x32_bf16 v[60:63], v[20:23], v[36:39], v[60:63]\n" \
  "ds_read_b128 v[20:23], v0 offset:12544\n" \
  "ds_read_b128 v[36:39], v1 offset:5120\n" \
  "v_mfma_f32_16x16x32_bf16 v[42:45], v[24:27], v[76:79], v[42:45]\n" \
  "v_mfma_f32_16x16x32_bf16 v[48:51], v[24:27], v[140:143], v[48:51]\n" \
  "v_mfma_f32_16x16x32_bf16 v[54:57], v[24:27], v[204:207], v[54:57]\n" \
  "s_waitcnt lgkmcnt(4)\n" \
  "v_mfma_f32_16x16x32_bf16 v[60:63], v[24:27], v[28:31], v[60:63]\n" \
  "ds_read_b128 v[24:27], v0 offset:14592\n" \
  "ds_read_b128 v[28:31], v1 offset:6144\n" \
  "v_mfma_f32_16x16x32_bf16 v[42:45], v[12:15], v[80:83], v[42:45]\n" \
  "v_mfma_f32_16x16x32_bf16 v[48:51], v[12:15], v[144:147], v[48:51]\n" \
  "v_mfma_f32_16x16x32_bf16 v[54:57], v[12:15], v[208:211], v[54:57]\n" \
  "s_waitcnt lgkmcnt(4)\n" \
  "v_mfma_f32_16x16x32_bf16 v[60:63], v[12:15], v[32:35], v[60:63]\n" \
  "ds_read_b128 v[12:15], v0 offset:16640\n" \
  "ds_read_b128 v[32:35], v1 offset:7168\n" \
  "v_mfma_f32_16x16x32_bf16 v[42:45], v[16:19], v[84:87], v[42:45]\n" \
  "v_mfma_f32_16x16x32_bf16 v[48:51], v[16:19], v[148:151], v[48:51]\n" \
  "v_mfma_f32_16x16x32_bf16 v[54:57], v[16:19], v[212:215], v[54:57]\n" \
  "s_waitcnt lgkmcnt(4)\n" \
  "v_mfma_f32_16x16x32_bf16 v[60:63], v[16:19], v[36:39], v[60:63]\n" \
  "ds_read_b128 v[16:19], v0 offset:18688\n" \
  "ds_read_b128 v[36:39], v1 offset:8192\n" \
  "v_mfma_f32_16x16x32_bf16 v[42:45], v[20:23], v[88:91], v[42:45]\n" \
  "v_mfma_f32_16x16x32_bf16 v[48:51], v[20:23], v[152:155], v[48:51]\n" \
  "v_mfma_f32_16x16x32_bf16 v[54:57], v[20:23], v[216:219], v[54:57]\n" \
  "s_waitcnt lgkmcnt(4)\n" \
  "v_mfma_f32_16x16x32_bf16 v[60:63], v[20:23], v[28:31], v[60:63]\n" \
  "ds_read_b128 v[20:23], v0 offset:20736\n" \
  "ds_read_b128 v[28:31], v1 offset:9216\n" \
  "v_mfma_f32_16x16x32_bf16 v[42:45], v[24:27], v[92:95], v[42:45]\n" \
  "v_mfma_f32_16x16x32_bf16 v[48:51], v[24:27], v[156:159], v[48:51]\n" \
  "v_mfma_f32_16x16x32_bf16 v[54:57], v[24:27], v[220:223], v[54:57]\n" \
  "s_waitcnt lgkmcnt(4)\n" \
  "v_mfma_f32_16x16x32_bf16 v[60:63], v[24:27], v[32:35], v[60:63]\n" \
  "ds_read_b128 v[24:27], v0 offset:22784\n" \
  "ds_read_b128 v[32:35], v1 offset:10240\n" \
  "v_mfma_f32_16x16x32_bf16 v[42:45], v[12:15], v[96:99], v[42:45]\n" \
  "v_mfma_f32_16x16x32_bf16 v[48:51], v[12:15], v[160:163], v[48:51]\n" \
  "v_mfma_f32_16x16x32_bf16 v[54:57], v[12:15], v[224:227], v[54:57]\n" \
  "s_waitcnt lgkmcnt(4)\n" \
  "v_mfma_f32_16x16x32_bf16 v[60:63], v[12:15], v[36:39], v[60:63]\n" \
  "ds_read_b128 v[12:15], v0 offset:24832\n" \
  "ds_read_b128 v[36:39], v1 offset:11264\n" \
  "v_mfma_f32_16x16x32_bf16 v[42:45], v[16:19], v[100:103], v[42:45]\n" \
  "v_mfma_f32_16x16x32_bf16 v[48:51], v[16:19], v[164:167], v[48:51]\n" \
  "v_mfma_f32_16x16x32_bf16 v[54:57], v[16:19], v[228:231], v[54:57]\n" \
  "s_waitcnt lgkmcnt(4)\n" \
  "v_mfma_f32_16x16x32_bf16 v[60:63], v[16:19], v[28:31], v[60:63]\n" \
  "ds_read_b128 v[16:19], v0 offset:26880\n" \
  "ds_read_b128 v[28:31], v1 offset:12288\n" \
  "v_mfma_f32_16x16x32_bf16 v[42:45], v[20:23], v[104:107], v[42:45]\n" \
  "v_mfma_f32_16x16x32_bf16 v[48:51], v[20:23], v[168:171], v[48:51]\n" \
  "v_mfma_f32_16x16x32_bf16 v[54:57], v[20:23], v[232:235], v[54:57]\n" \
  "s_waitcnt lgkmcnt(4)\n" \
  "v_mfma_f32_16x16x32_bf16 v[60:63], v[20:23], v[32:35], v[60:63]\n" \
  "ds_read_b128 v[20:23], v0 offset:28928\n" \
  "ds_read_b128 v[32:35], v1 offset:13312\n" \
  "v_mfma_f32_16x16x32_bf16 v[42:45], v[24:27], v[108:111], v[42:45]\n" \
  "v_mfma_f32_16x16x32_bf16 v[48:51], v[24:27], v[172:175], v[48:51]\n" \
  "v_mfma_f32_16x16x32_bf16 v[54:57], v[24:27], v[236:239], v[54:57]\n" \
  "s_waitcnt lgkmcnt(4)\n" \
  "v_mfma_f32_16x16x32_bf16 v[60:63], v[24:27], v[36:39], v[60:63]\n" \
  "ds_read_b128 v[24:27], v0 offset:30976\n" \
  "ds_read_b128 v[36:39], v1 offset:14336\n" \
  "v_mfma_f32_16x16x32_bf16 v[42:45], v[12:15], v[112:115], v[42:45]\n" \
  "v_mfma_f32_16x16x32_bf16 v[48:51], v[12:15], v[176:179], v[48:51]\n" \
  "v_mfma_f32_16x16x32_bf16 v[54:57], v[12:15], v[240:243], v[54:57]\n" \
  "s_waitcnt lgkmcnt(4)\n" \
  "v_mfma_f32_16x16x32_bf16 v[60:63], v[12:15], v[28:31], v[60:63]\n" \
  "ds_read_b128 v[28:31], v1 offset:15360\n" \
  "v_mfma_f32_16x16x32_bf16 v[42:45], v[16:19], v[116:119], v[42:45]\n" \
  "v_mfma_f32_16x16x32_bf16 v[48:51], v[16:19], v[180:183], v[48:51]\n" \
  "v_mfma_f32_16x16x32_bf16 v[54:57], v[16:19], v[244:247], v[54:57]\n" \
  "s_waitcnt lgkmcnt(3)\n" \
  "v_mfma_f32_16x16x32_bf16 v[60:63], v[16:19], v[32:35], v[60:63]\n" \
  "ds_read_b128 v[32:35], v3 offset:192\n" \
  "v_mfma_f32_16x16x32_bf16 v[42:45], v[20:23], v[120:123], v[42:45]\n" \
  "v_mfma_f32_16x16x32_bf16 v[48:51], v[20:23], v[184:187], v[48:51]\n" \
  "v_mfma_f32_16x16x32_bf16 v[54:57], v[20:23], v[248:251], v[54:57]\n" \
  "s_waitcnt lgkmcnt(2)\n" \
  "v_mfma_f32_16x16x32_bf16 v[60:63], v[20:23], v[36:39], v[60:63]\n" \
  "ds_read_b128 v[36:39], v3 offset:64\n" \
  "s_mov_b64 exec, 1\n" \
  "ds_add_u32 v6, v7 offset:8\n" \
  "s_mov_b64 exec, -1\n" \
  "v_xor_b32 v6, 4, v6\n" \
  "v_mfma_f32_16x16x32_bf16 v[42:45], v[24:27], v[124:127], v[42:45]\n" \
  "v_mfma_f32_16x16x32_bf16 v[48:51], v[24:27], v[188:191], v[48:51]\n" \
  "v_mfma_f32_16x16x32_bf16 v[54:57], v[24:27], v[252:255], v[54:57]\n" \
  "s_waitcnt lgkmcnt(3)\n" \
  "v_mfma_f32_16x16x32_bf16 v[60:63], v[24:27], v[28:31], v[60:63]\n" \
  "s_waitcnt lgkmcnt(2)\n" \
  "v_mul_f32 v42, v42, v32\n" \
  "v_mul_f32 v43, v43, v33\n" \
  "v_mul_f32 v44, v44, v34\n" \
  "v_mul_f32 v45, v45, v35\n" \
  "s_waitcnt lgkmcnt(1)\n" \
  "v_fmac_f32 v42, v36, v8\n" \
  "v_fmac_f32 v43, v37, v8\n" \
  "v_fmac_f32 v44, v38, v8\n" \
  "v_fmac_f32 v45, v39, v8\n" \
  "v_cvt_pk_bf16_f32 v42, v42, v43\n" \
  "v_cvt_pk_bf16_f32 v43, v44, v45\n" \
  "buffer_store_dwordx4 v[40:43], v2, s[40:43], s65 offen offset:0 sc1\n" \
  "v_mul_f32 v48, v48, v32\n" \
  "v_mul_f32 v49, v49, v33\n" \
  "v_mul_f32 v50, v50, v34\n" \
  "v_mul_f32 v51, v51, v35\n" \
  "v_fmac_f32 v48, v36, v9\n" \
  "v_fmac_f32 v49, v37, v9\n" \
  "v_fmac_f32 v50, v38, v9\n" \
  "v_fmac_f32 v51, v39, v9\n" \
  "v_cvt_pk_bf16_f32 v48, v48, v49\n" \
  "v_cvt_pk_bf16_f32 v49, v50, v51\n" \
  "ds_read_b32 v5, v6 offset:0\n" \
  "buffer_store_dwordx4 v[46:49], v2, s[40:43], s65 offen offset:1024 sc1\n" \
  "v_mul_f32 v54, v54, v32\n" \
  "v_mul_f32 v55, v55, v33\n" \
  "v_mul_f32 v56, v56, v34\n" \
  "v_mul_f32 v57, v57, v35\n" \
  "v_fmac_f32 v54, v36, v10\n" \
  "v_fmac_f32 v55, v37, v10\n" \
  "v_fmac_f32 v56, v38, v10\n" \
  "v_fmac_f32 v57, v39, v10\n" \
  "v_cvt_pk_bf16_f32 v54, v54, v55\n" \
  "v_cvt_pk_bf16_f32 v55, v56, v57\n" \
  "buffer_store_dwordx4 v[52:55], v2, s[40:43], s65 offen offset:2048 sc1\n" \
  "v_mul_f32 v60, v60, v32\n" \
  "v_mul_f32 v61, v61, v33\n" \
  "v_mul_f32 v62, v62, v34\n" \
  "v_mul_f32 v63, v63, v35\n" \
  "v_fmac_f32 v60, v36, v4\n" \
  "v_fmac_f32 v61, v37, v4\n" \
  "v_fmac_f32 v62, v38, v4\n" \
  "v_fmac_f32 v63, v39, v4\n" \
  "v_cvt_pk_bf16_f32 v60, v60, v61\n" \
  "v_cvt_pk_bf16_f32 v61, v62, v63\n" \
  "buffer_store_dwordx4 v[58:61], v2, s[40:43], s65 offen offset:3072 sc1\n" \
  "s_waitcnt lgkmcnt(0)\n" \
  "s_add_u32 s65, s65, 0x40000\n" \
  "s_sub_u32 s64, s64, 1\n" \
  "s_mov_b32 s68, 1\n" \
  "s_cmp_lg_u32 s64, 0\n" \
  "s_cbranch_scc1 .LCW_TOP_%=\n" \
  "s_waitcnt vmcnt(0)\n" \
  "s_mov_b64 exec, 1\n" \
  "buffer_store_dword v7, off, s[56:59], s72 sc1\n" \
  "s_mov_b64 exec, -1\n" \
  "s_add_u32 s72, s72, 0x1000\n" \
  "s_mov_b32 s68, 0\n" \
  ".LCW_END_%=:\n" \
  "s_endpgm\n"
constexpr int RG = 2;
#define MARKS(s) __builtin_amdgcn_raw_buffer_load_b32(rm, (c >> 2) * 128 + (c & 3) * 4, (s) * 2048, AUX_SC1)
struct Yes { static constexpr bool value = true; };
struct No { static constexpr bool value = false; };
#define RFL(x) __builtin_amdgcn_readfirstlane(x)
#define HALF(x, i) BF8(((i) ? __builtin_shufflevector(x, x, 4, 5, 6, 7) : __builtin_shufflevector(x, x, 0, 1, 2, 3)))
constexpr int P_W = 65536, P_ST = 131072, P_SS = P_ST + 1024, P_CN = P_SS + 512;
template <bool PR>
__device__ __forceinline__ void role_proj_t(const MegaArgs& A, char* smem, const int L, const int idx, const bool kv) {
  const int w = A.wv;
  const int l = lane_id();
  const int c = l & 15, g = l >> 4;
  char* ws = A.ws;
  unsigned* FL = (unsigned*)(ws + OFF_FL);
  unsigned* TMO = FL + FL_TMO;
  const unsigned sb = lds_base(smem);
  auto cn = (volatile __attribute__((address_space(3))) unsigned*)(unsigned long)(sb + P_CN);
  u64 tag;
  const bool h4 = kv && L;
  const __amdgpu_buffer_rsrc_t rsM = MKRS(ws + OFF_MEMK + L * SZ_MEM, kv && w == 2 ? (4 - L) * SZ_MEM : 0);
  const bool mc = kv && w == 2 && (MCE < 2 || !L);
  constexpr int NMC = MCE == 2 ? 8 : 4;
  const int b0 = RFL(h4 ? (H4S ? 2 : 3) * w : w == 2 ? 0 : HX0),
            b1 = RFL(h4 ? (H4S ? 2 * w + 2 : w == 2 ? 8 : 3 * w + 3 * (w < 2)) : w == 2 ? HX0 : 8);
  const __amdgpu_buffer_rsrc_t rsF = MKRS(FL + FL_FC + L * 64 * 32 * 32, 64 * 4096);
  const int fo = ((blockIdx.x & 3) * 8 + ((H4S != 0 && h4) || w == 3 ? l >> 3 : min(b0 + (l >> 3), b1 - 1))) * 128 + (l & 7) * 4;
#define FC_LOOK(s) __builtin_amdgcn_raw_buffer_load_b32(rsF, fo, (s) * 4096, AUX_SC1)
#define MEM_CLEAR(s)                                                                                                          \
  _Pragma("unroll") for (int i = 0; i < NMC; ++i)                                                                             \
    ST128((v4u{0u, 0u, 0u, 0u}), rsM, l * 16 + (i & 1) * 1024, (s) * 65536 + idx * 2048 + (i >> 1) * (int)SZ_MEM * (MCE == 2 ? 1 : 2));
  const char* Xb = ws + (L ? OFF_X3 : OFF_XA);
  const __amdgpu_buffer_rsrc_t rsS = MKRS(ws + OFF_H + L * (SZ_X / 2), SZ_X / 2);
  unsigned nf = 0u;
  bool early = false;
  v4u t0[16];
#define X_ASK(s) \
  for (int i = b0; i < b1; ++i) glds64c(Xb + RFL((s) * 32768 + i * 4096), l * 16, RFL(sb + ((s) & 1) * 32768 + i * 4096));
#define S_ASK(s, t) _Pragma("unroll") for (int i = 0; i < 16; ++i) t[i] = LD128(rsS, l * 16, (s) * 16384 + i * 1024);
  {
    const int j = w & 3;
    const bool vt = kv && j >= 2;
    constexpr int NT = PR ? 3 : 1;
    v8u wq[NT][8];
    const u16* Wm = (kv ? (PR ? A.Wv : A.Wk) : A.Wq) - (PR ? 128 : 0) * HD;
    const int hm = kv ? idx : 2 * idx + PR;
    const int q = (l >> 2) & 3, p = l & 3, f = q | ((g & 1) << 2);
    unsigned ra[NT];
#pragma unroll
    for (int m = 0; m < NT; ++m) {
      const int ct = PR ? m : 3;
      ra[m] = sb + (j >> 1) * 65536 + (8 * g + q) * 256 + ((4 * (j & 1) + (vt ? ct : (ct & 2) + (p >> 1))) ^ f) * 32 + (vt ? p * 8 : (p & 1) * 16 + (ct & 1) * 8);
    }
#define W_ISSUE(n) wqkv_issue<false>(Wm + (128 * (n) - 256 * ((n) & 1)) * HD, (n) & 1, hm, w, l, sb, (WNT >> (kv ? 1 + PR : 3) & 1) && (L == 0 || (WNT & 32)))
#define W_STAGE(n, WAIT, NEXT)                                                                             \
  {                                                                                                        \
    WAIT;                                                                                                  \
    if (n == 3 && EL == 2 && !PR && L == 0 && w >= 2) nf = FC_LOOK(0);                                     \
    if (n == 3 && EL == 1 && !PR && L == 0 && w >= 2) {                                                    \
      early = __all(nf != 0u);                                                                             \
      if (early) {                                                                                         \
        X_ASK(0)                                                                                           \
        if (w == 3) { S_ASK(0, t0) }                                                                       \
      }                                                                                                    \
      nf = FC_LOOK(early);                                                                                 \
    }                                                                                                      \
    RAW_BAR();                                                                                             \
    if (n == 3 && EL == 3 && !PR && L == 0 && w >= 2) nf = FC_LOOK(0);                                     \
    _Pragma("unroll") for (int m = 0; m < NT; ++m) {                                                       \
      _Pragma("unroll") for (int t = 0; t < 2; ++t) {                                                      \
        const unsigned a_ = ra[m] + ((n) & 1) * 32768 + t * 16384;                                         \
        const v2u r0 = trd(a_), r1 = trd(a_ + 1024), r2 = trd(a_ + 8192), r3 = trd(a_ + 8192 + 1024);      \
        wq[m][2 * (n) + t] = v8u{r0.x, r0.y, r1.x, r1.y, r2.x, r2.y, r3.x, r3.y};                          \
      }                                                                                                    \
    }                                                                                                      \
    _Pragma("unroll") for (int m = 0; m < 2 * NT; ++m) asm volatile("" : "+v"(wq[m >> 1][2 * (n) + (m & 1)])); \
    RAW_BAR();                                                                                             \
    NEXT;                                                                                                  \
    _Pragma("unroll") for (int m = 0; m < 4 * NT; ++m)                                                     \
      cs[m % NT] = MFMA16(HALF(wq[m % NT][2 * (n) + m / NT / 2], (m / NT) & 1), BF8(one), cs[m % NT]);     \
    _Pragma("unroll") for (int m = 0; m < NT; ++m) asm volatile("" : "+v"(cs[m]));                         \
  }
    if (EL && !MC3 && mc) { MEM_CLEAR(0) }
    W_ISSUE(0);
    W_ISSUE(1);
    const bool c1 = GEN_T1 != 0 && kv && !L;
    if (c1) {
      const __amdgpu_buffer_rsrc_t rz = MKRS(ws + RFL((w & 2) ? (int)OFF_VF + idx * 8192 + ((2 * !GEN_F1 + PR) << 18) : (int)OFF_QK + idx * 16384 + 8192 + ((2 * !GEN_F1 + PR) << 19)) + (w & 1) * 4096, 4096);
#pragma unroll
      for (int i = 0; i < 4; ++i) ST128((v4u{0u, 0u, 0u, 0u}), rz, l * 16 + i * 1024, 0);
    }
    if (PR && vt) TOUCH("CW");
    if (PR && !vt) TOUCH("CP");
    tag = call_tag(A);
    if (w == 0 && l < 8) cn[l] = l == 1 ? 3u : l == 5 ? 2u : 0u;
    const v4u one = {0x3f803f80u, 0x3f803f80u, 0x3f803f80u, 0x3f803f80u};
    f32x4 cs[NT] = {};
    u64 rv = 0;
    static_assert(GEN_F1 ? GEN_T1 != 0 : GEN_T1 == 0 || GEN_T1 == 3, "clear");
    W_STAGE(0, if (c1) WAIT_VM(12); else WAIT_VM(8), W_ISSUE(2); if (EL) RD_ASK(rv));
    SST(4);
    W_STAGE(1, if (EL) WAIT_VM(9); else WAIT_VM(8), W_ISSUE(3); if (EL != 0 && MC3 != 0 && mc) { MEM_CLEAR(0) if (MC3 == 2) { MEM_CLEAR(1) } });
    SST(5);
    W_STAGE(2, if (EL != 0 && MC3 == 2 && mc) WAIT_VM(16); else if (EL != 0 && MC3 != 0 && mc) WAIT_VM(12); else WAIT_VM(8), (void)0);
    SST(6);
    asm volatile("" : "+v"(rv));
    if (!EL) ready_wait(A, tag, ready_look(A));
    for (unsigned spins = 0; EL && !__all(rv == tag) && spins < SPIN_CAP; ++spins) {
      __builtin_amdgcn_s_sleep(8);
      RD_ASK(rv);
      asm volatile("s_waitcnt vmcnt(0)" : "+v"(rv)::"memory");
    }
    if (EL == 1 && !PR && L == 0 && w >= 2) nf = FC_LOOK(0);
    if (PG & (PR ? 4 : 8)) PAGE();
    if (GEN_TB == 2 && PR && vt) asm volatile("s_call_b64 vcc, .LTB1CW" ::: "vcc");
    W_STAGE(3, if (EL >= 2 && MC3 == 2 && mc) WAIT_VM(8); else if (EL >= 2 && MC3 == 1 && mc) WAIT_VM(4); else WAIT_VM(0), if (!PR) {
      _Pragma("unroll") for (int ks = 0; ks < 16; ++ks)
        *reinterpret_cast<bf16x8*>(smem + P_W + j * 16384 + ks * 1024 + l * 16) = HALF(wq[0][ks >> 1], ks & 1);
    });
    SST(7);
#undef W_STAGE
#undef W_ISSUE
#pragma unroll
    for (int m = 0; m < NT; ++m) *reinterpret_cast<f32x4*>(smem + P_ST + j * 256 + (PR ? m : 3) * 64 + g * 16) = cs[m];
    RAW_BAR();
    SST(8);
    if constexpr (PR) {
      const v4u rsO = mk_srd(ws + RFL(vt ? TOK_V(ws, L) : TOK_QK(ws, L)), RFL((unsigned)(vt ? SZ_O : SZ_QK) >> (GEN_TR * L ? 6 - GEN_TR : 0)));
      const v4u rsP = mk_srd(FL + RFL(kv ? FL_FQ + (L * 64 * 32 + idx) * 32 + 2 * j : FL_FP + (L * 16 + idx) * 32 + j), 63 * 4096 + 4);
      const int so = RFL(vt ? idx * 8 + 4 * (j & 1) : (kv ? idx * 16 + 8 : (2 * idx + (j >> 1)) * 16) + 2 * (j & 1));
      const v8u pv = {sb + 32768 + g * 512 + c * 16, sb + P_W + j * 16384 + l * 16, (unsigned)l * 16, sb + P_SS + 256 + (vt ? g * 16 : c * 4),
                      sb + P_ST + j * 256 + (vt ? c * 4 : g * 16), 0u, 0u, 0u};
      const char* dbg = nullptr;
#define P_OPS                                                                                                                               \
  ::"{v[64:71]}"(wq[0][0]), "{v[72:79]}"(wq[0][1]), "{v[80:87]}"(wq[0][2]), "{v[88:95]}"(wq[0][3]), "{v[96:103]}"(wq[0][4]),                \
      "{v[104:111]}"(wq[0][5]), "{v[112:119]}"(wq[0][6]), "{v[120:127]}"(wq[0][7]), "{v[128:135]}"(wq[1][0]), "{v[136:143]}"(wq[1][1]),     \
      "{v[144:151]}"(wq[1][2]), "{v[152:159]}"(wq[1][3]), "{v[160:167]}"(wq[1][4]), "{v[168:175]}"(wq[1][5]), "{v[176:183]}"(wq[1][6]),     \
      "{v[184:191]}"(wq[1][7]), "{v[192:199]}"(wq[2][0]), "{v[200:207]}"(wq[2][1]), "{v[208:215]}"(wq[2][2]), "{v[216:223]}"(wq[2][3]),     \
      "{v[224:231]}"(wq[2][4]), "{v[232:239]}"(wq[2][5]), "{v[240:247]}"(wq[2][6]), "{v[248:255]}"(wq[2][7]), "{v[0:7]}"(pv),               \
      "{s[40:43]}"(rsO), "{s[56:59]}"(rsP), "{s64}"(NSEG), "{s65}"(so * 1024), "{s70}"(sb + P_CN), "{s74}"(RFL(L ? 19 + GEN_TR - vt : 0)), "{s75}"(RFL(!kv ? 64 : L ? 0 : 63 - GEN_T1)), "{s[36:37]}"(dbg) \
      : "memory", "vcc", "scc", "s80", "v63"
      if (vt) asm volatile("s_mov_b64 exec, -1\ns_waitcnt vmcnt(0) lgkmcnt(0)\ns_nop 4\n" V_ASM P_OPS);
      else asm volatile("s_mov_b64 exec, -1\ns_waitcnt vmcnt(0) lgkmcnt(0)\ns_nop 4\n" P_ASM P_OPS);
#undef P_OPS
      return;
    }
  }
  __builtin_amdgcn_s_setprio(RPRIO);
  if (h4 || w >= 2) {
    if (!EL && mc) { MEM_CLEAR(0) }
    auto stats = [&](const v4u (&t)[16], const int s) {
      float a = 0.f, b = 0.f, qa = 0.f, qb = 0.f;
#pragma unroll
      for (int i = 0; i < 16; ++i) {
        a = one(a + __uint_as_float(t[i].x));
        b = one(b + __uint_as_float(t[i].z));
        qa = one(qa + __uint_as_float(t[i].y));
        qb = one(qb + __uint_as_float(t[i].w));
      }
      a = xsum32(xsum16(a));
      b = xsum32(xsum16(b));
      float da = 0.f, db = 0.f;
#pragma unroll
      for (int i = 0; i < 16; ++i) {
        const float u = one(__uint_as_float(t[i].x) - a * (1.0f / 64)), v = one(__uint_as_float(t[i].z) - b * (1.0f / 64));
        da = one(da + u * u);
        db = one(db + v * v);
      }
      const float var = xsum32(swap_add16(qa + da * 0.125f, qb + db * 0.125f)) * (1.0f / DM);
      const float ri = 1.0f / bfr1(sqrtf(bfr1(bfr1(var) + 1e-5f)));
      reinterpret_cast<float*>(smem + P_SS)[(s & 1) * 64 + (g >> 1) * 32 + 2 * c + (g & 1)] = g < 2 ? bfr1(((g & 1) ? b : a) * (1.0f / DM)) * (EP ? -ri : 1.0f) : ri;
      asm volatile("" ::: "memory");
      if (l == 0) cn[4 + (s & 1)] = 2 * s + 4;
    };
    static_assert(EL != 1 || MC3 == 2, "clear");
#define K1_NEXT(s)                                                                                                                          \
  if (GEN_F1 != 0 && mc && !L && (s) + 2 <= GEN_T1) {                                                                                       \
    if ((s) == 0 && l < 4) __hip_atomic_store((gu32*)(FL + FL_FQ + idx * 32 + 2 * l + 1), (now10() + KF1) | 1u, RLX_AGENT);                 \
    const __amdgpu_buffer_rsrc_t rz = MKRS(ws + OFF_QK, OFF_PW - OFF_QK);                                                                   \
    if (KW1) gate(now10() + KW1);                                                                                                           \
    _Pragma("unroll") for (int i = 0; i < 16; ++i)                                                                                          \
      ST128((v4u{0u, 0u, 0u, 0u}), rz, l * 16 + (i & 3) * 1024,                                                                             \
            (i < 8 ? idx * 16384 + 8192 + (((s) + 2) << 19) : (int)(OFF_VF - OFF_QK) + idx * 8192 + (((s) + 2) << 18)) + ((i >> 2) & 1) * 4096); \
  }
    if (early) {
      if (w == 3) WAIT_VM(17);
      else WAIT_VM(1);
      if (l == 0) __hip_atomic_fetch_add((__attribute__((address_space(3))) unsigned*)cn, 3u, __ATOMIC_RELAXED, __HIP_MEMORY_SCOPE_WORKGROUP);
      K1_NEXT(0)
      SSX(10, w == 2);
      SSX(9, w == 2);
      if (w == 3) stats(t0, 0);
      SSX(11, w == 3);
    }
    if (L) {
      char* ws = A.ws;
      int w = A.wv, i1 = idx + 32 * kv;
      asm volatile("" : "+s"(ws), "+s"(w), "+s"(i1));
      const int idx = i1 & 31;
      const bool h4 = i1 >= 32, mc = h4 && w == 2;
      const int b0 = h4 ? 3 * w : w == 2 ? 0 : HX0, b1 = h4 ? (w == 2 ? 8 : 3 * w + 3 * (w < 2)) : w == 2 ? HX0 : 8;
      unsigned* FL = (unsigned*)(ws + OFF_FL);
      unsigned* TMO = FL + FL_TMO;
      const char* Xb = ws + OFF_X3;
      const __amdgpu_buffer_rsrc_t rsM = MKRS(ws + OFF_MEMK + SZ_MEM, 3 * SZ_MEM), rsF = MKRS(FL + FL_FC + 64 * 32 * 32, 64 * 4096),
                                   rsS = MKRS(ws + OFF_H + SZ_X / 2, SZ_X / 2);
      unsigned mk;
      int l16 = l * 16;
      asm volatile("" : "+v"(l16));
      const __amdgpu_buffer_rsrc_t rm = MKRS(FL + FL_FW + (blockIdx.x & 3) * 128, 63 * 2048 + 512);
      const __amdgpu_buffer_rsrc_t rsK = MKRS(ws + (w < 2 ? TOK_QK(ws, 1) + idx * 16384 + 8192 : TOK_V(ws, 1) + idx * 8192) + (w & 1) * 4096,
                                              KC && h4 ? (GEN_TR ? (1 << GEN_TR) - 1 : 63) * (w < 2 ? 0x80000 : 0x40000) + 4096 : 0);
#define KV_CLEAR(s) \
  _Pragma("unroll") for (int i = 0; i < 4 * KC; ++i) ST128((v4u{0u, 0u, 0u, 0u}), rsK, l16 + i * 1024, (s) << (w < 2 ? 19 : 18));
      static_assert(KC || !GEN_TL, "clear");
      if (GEN_F0) { KV_CLEAR(0) }
      KV_CLEAR(1)
      if (!wait_n<SLP_V, 32>(FL + FL_FZ + (blockIdx.x & 7) * 64, 64, TMO)) return;
      const __amdgpu_buffer_rsrc_t rf = MKRS(FL + FL_FW + (64 - (1 << GEN_TR)) * 512 + (blockIdx.x & 3) * 128, 65 * 2048);
#define FREE_LOOK(s) __builtin_amdgcn_raw_buffer_load_b32(rf, (c >> 2) * 128 + (c & 3) * 4, (s) * 2048, AUX_SC1)
#pragma unroll 1
      for (unsigned spins = 0;; ++spins) {
        if (spin_timeout(spins, TMO)) return;
        __builtin_amdgcn_s_sleep(32);
        mk = MARKS(0);
        if (__all(mk != 0u)) break;
        asm volatile("" ::: "memory");
      }
      gate(latest(mk, RFL(mk)) + KD + K0 + (h4 ? 0 : P2D));
      auto feed = [&](auto ST_) {
        constexpr bool ST = ST_.value;
        bool due = true;
        int sf = -1;
#pragma unroll 1
        for (int s = 0, n = 0; s < NSEG && n < 4 * NSEG; ++n) {
#pragma unroll 1
          for (unsigned spins = 0; !due; ++spins) {
            if (spin_timeout(spins, TMO)) return;
            if (spins) __builtin_amdgcn_s_sleep(SLP_H);
            due = __all(FC_LOOK(s) != 0u);
            asm volatile("" ::: "memory");
          }
          TS(4, s, 128);
#pragma unroll 1
          for (unsigned spins = 0; cn[2 + (s & 1)] < 4u * (s >> 1); ++spins) {
            if (spins > 64u * SPIN_CAP) return;
            __builtin_amdgcn_s_sleep(2);
          }
          TS(5, s, 128);
          X_ASK(s)
          v4u t[ST ? 16 : 1];
          if (ST) {
            int so = s * 16384;
#pragma unroll
            for (int i = 0; i < 16; ++i) {
              if ((i & 3) == 0) asm volatile("" : "+s"(so));
              t[i] = LD128(rsS, l16 + (i & 3) * 1024, so);
              if ((i & 3) == 3) so += 4096;
              __builtin_amdgcn_sched_barrier(0);
            }
          }
          mk = MARKS(s + 1);
          unsigned z = 1u, fr = 1u;
          if (GEN_TR != 0 && ST) fr = FREE_LOOK(s + 1);
          if (GEN_TR != 0 && ST) WAIT_VM(18);
          else if (ST) WAIT_VM(17);
          else WAIT_VM(1);
#pragma unroll 1
          for (int k = b0; k < b1; k += 3) {
            const unsigned* q = reinterpret_cast<const unsigned*>(smem + (s & 1) * 32768 + k * 4096 + l16 + 4);
            const unsigned* q2 = q + min(b1 - k - 1, 2) * 1024;
#pragma unroll
            for (int i = 0; i < 12; i += 2) z = i < 8 ? umin3(z, q[256 * i], q[256 * i + 256]) : umin3(z, q2[256 * (i - 8)], q2[256 * (i - 7)]);
          }
          if (ST) {
#pragma unroll
            for (int i = 0; i < 16; ++i) z = umin3(z, t[i].y, t[i].w);
          }
          due = __all(z != 0u);
          if (!due) {
            due = KR && sf != s;
            sf = s;
            continue;
          }
          asm volatile("" ::: "memory");
          if (l == 0 && !(ST && h4)) __hip_atomic_fetch_add((__attribute__((address_space(3))) unsigned*)cn + (s & 1), h4 ? 2u : 3u, __ATOMIC_RELAXED, __HIP_MEMORY_SCOPE_WORKGROUP);
          TS(6, s, 128);
          if (GEN_F0 != 0 && h4 && s == 0 && l == 0) __hip_atomic_store((gu32*)(FL + FL_FQ + (64 * 32 + idx) * 32 + 2 * w + 1), (now10() + KF) | 1u, RLX_AGENT);
          if constexpr (ST) {
            stats(t, s);
            TS(7, s, 192);
#pragma unroll 1
            for (unsigned spins = 0; GEN_TR != 0 && s >= (1 << GEN_TR) - 1 && !__all(fr != 0u); ++spins) {
              if (spin_timeout(spins, TMO)) return;
              __builtin_amdgcn_s_sleep(SLP_H);
              fr = FREE_LOOK(s + 1);
              asm volatile("" ::: "memory");
            }
          }
          due = __all(mk != 0u);
          const unsigned td = latest(mk, RFL(mk)) + (h4 ? KD : PD) + (s < 7 - R0 ? K1 : s >= NSEG - 1 - TS1 ? -TL3 : 0);
          if (!ST && mc && s < NSEG - 1 && s) { MEM_CLEAR(s + 1) }
          if (h4 && (!GEN_TR || s < (1 << GEN_TR) - 2)) {
            if (KCT && due) gate(td - KCT);
            KV_CLEAR(s + 2)
          }
          if (due) gate(td);
          ++s;
        }
      };
      static_assert(HX0 == 6, "blocks");
      if (w == 3) feed(Yes{});
      else feed(No{});
      return;
    }
    unsigned tn = now10();
    for (int s = RFL((int)early); s < NSEG; ++s) {
#pragma unroll 1
      for (unsigned spins = 0; !__all(nf != 0u); ++spins) {
        if (spin_timeout(spins, TMO)) return;
        if (spins) __builtin_amdgcn_s_sleep(SLP_H);
        nf = FC_LOOK(s);
        asm volatile("" ::: "memory");
      }
      if (NFW) asm volatile("" ::"v"(nf));
      TS(4, s, 128);
#pragma unroll 1
      for (unsigned spins = 0; cn[2 + (s & 1)] < 4u * (s >> 1); ++spins) {
        if (spins > 64u * SPIN_CAP) return;
        __builtin_amdgcn_s_sleep(2);
      }
      if (s >= PACE0) gate(tn);
      else tn = now10();
      tn += PACE;
      TS(5, s, 128);
      X_ASK(s)
      if (w != 3) {
        nf = FC_LOOK(s + 1);
        if (mc && s < NSEG - 1 && (EL == 0 || MC3 != 2 || s)) {
          MEM_CLEAR(s + 1)
          if (NMC == 8) WAIT_VM(8);
          else WAIT_VM(4);
        } else {
          WAIT_VM(0);
        }
        if (l == 0) __hip_atomic_fetch_add((__attribute__((address_space(3))) unsigned*)cn + (s & 1), 3u, __ATOMIC_RELAXED, __HIP_MEMORY_SCOPE_WORKGROUP);
        K1_NEXT(s)
        TS(6, s, 128);
      } else {
        v4u t[16];
        S_ASK(s, t)
        WAIT_VM(16);
        nf = FC_LOOK(s + 1);
        if (l == 0) __hip_atomic_fetch_add((__attribute__((address_space(3))) unsigned*)cn + (s & 1), 3u, __ATOMIC_RELAXED, __HIP_MEMORY_SCOPE_WORKGROUP);
        stats(t, s);
        TS(7, s, 192);
      }
    }
    return;
  }
  const int RL = !kv, rp = idx & 15, u = RFL(kv ? idx >> 4 : L);
  const __amdgpu_buffer_rsrc_t rsW = MKRS(ws + OFF_PW, SZ_PW);
  const __amdgpu_buffer_rsrc_t rsX = MKRS(ws + (RL ? OFF_X3 : OFF_XA), SZ_X);
  const __amdgpu_buffer_rsrc_t rsY = RL ? MKRS(A.out, SZ_X) : MKRS(ws + OFF_X2, SZ_X);
  const int r = 2 * rp + (g >> 1), yo = r * (DM * 2) + (l & 31) * 16 + u * 512;
  const __amdgpu_buffer_rsrc_t rsC = MKRS(ws + OFF_H, OFF_QK - OFF_H);
#define X_CLEAR(s)                                                                                              \
  {                                                                                                             \
    ST128((v4u{0u, 0u, 0u, 0u}), rsY, yo, (s) * SEG * DM * 2);                                                  \
    if (KD) {                                                                                                   \
      ST128((v4u{0u, 0u, 0u, 0u}), rsC, l * 16, (int)(OFF_X3 - OFF_H) + ((s) * 32 + idx) * 1024);               \
      if (l < 32) ST128((v4u{0u, 0u, 0u, 0u}), rsC, l * 16, (int)(SZ_X / 2) + ((s) * 32 + idx) * 512);          \
    }                                                                                                           \
  }
  if (kv) {
    if (XZD) gate(now10() + XZD);
    if (XZP & 3) {
      if (!wait_n<SLP_V, 32>(FL + FL_FZ + (blockIdx.x & 7) * 64, 64, TMO)) return;
    } else {
      for (int s = w; s < (XZP ? 2 * RG : NSEG); s += RG) X_CLEAR(s)
      DRAIN();
      if (l < 8) set_flag(FL + FL_FZ + l * 64 + 2 * idx + w);
    }
  }
  v4u sel;
#pragma unroll
  for (int e = 0; e < 4; ++e) sel[e] = (g >> 1) == (c >> 3) && ((c & 7) >> 1) == e ? 0x3f80u << (16 * (c & 1)) : 0u;
  unsigned tl = now10();
  int po = (g & 1) * 32768 + (c >> 2) * 128 + (g >> 1) * 64 + (c & 3) * 16;
  asm volatile("" : "+v"(po));
  for (int s = w; s < NSEG; s += RG) {
    v4u xr;
    if (GF_R) {
      gate(tl);
      const gu32* f = (gu32*)(FL + FL_FW) + ((RL * 64 + s) * 4 + (blockIdx.x & 3)) * 4 * 32 + (c >> 2) * 32 + (c & 3);
      unsigned mk;
      for (unsigned spins = 0;; ++spins) {
        tl = now10() + GF_R;
        mk = __hip_atomic_load(f, RLX_AGENT);
        if (__all(mk != 0u) || spin_timeout(spins, TMO)) break;
        if (s < NSEG - RFA) __builtin_amdgcn_s_sleep(SLP_R);
      }
      if (EF) {
        const unsigned tp = latest(mk, tl);
        asm volatile("" ::: "memory");
        xr = LD128(rsX, (l & 31) * 512 + r * 16, s * 32768 + u * 16384);
        gate(tp - (RL ? (s >= NSEG - RLS ? RLEAD : 0) : s >= NSEG - TS1 ? TL1 : s ? 0 : -R0D));
        if (GR2) tl = tp + ((unsigned)(s - GR0) >= NSEG - 12 - GR0 ? GR3 : GR2);
      }
      asm volatile("" ::: "memory");
    } else
    grp_wait<SLP_R, 4, GATE_R>(true, nullptr, 0, FL + FL_FW + ((RL * 64 + s) * 4 + (blockIdx.x & 3)) * 4 * 32, 16, TMO, tl);
    if (s >= NSEG - 6 && !(EF != 0 && GR2 != 0)) tl -= GF_R - (s >= NSEG - 4 ? GF_RT : GF_RU);
    if (!EF) DRAIN();
    TS(0, s, w * 64);
    v4u p[8][2];
    if (!EF) xr = LD128(rsX, (l & 31) * 512 + r * 16, s * 32768 + u * 16384);
    f32x4 ac[2];
    const int sr = EF ? s & ((1 << RGB) - 1) : s;
    const unsigned old = (s >> RGB) & 1;
    for (unsigned spins = 0;; ++spins) {
      int so = (RL * 64 + sr) * 16 * 32768 + rp * 2048 + u * 1024;
#pragma unroll
      for (int k = 0; k < 8; ++k) {
        asm volatile("" : "+s"(so));
#pragma unroll
        for (int b = 0; b < 2; ++b) p[k][b] = __builtin_amdgcn_raw_buffer_load_b128(rsW, po + b * 512, so, RAUX);
        so += 65536;
      }
      __builtin_amdgcn_sched_barrier(0);
      unsigned z[2] = {1u, 1u};
      ac[0] = ac[1] = f32x4{};
#pragma unroll
      for (int k = 0; k < 8; ++k)
#pragma unroll
        for (int b = 0; b < 2; ++b) {
          ac[b] = MFMA16(BF8(sel), BF8(p[k][b]), ac[b]);
          z[b] = __builtin_amdgcn_bitop3_b32(z[b], p[k][b].x, old, 0x60);
          __builtin_amdgcn_sched_barrier(0);
        }
      if (!EF || !RVAL || __all((z[0] & z[1]) != 0u) || spin_timeout(spins, TMO)) break;
      if (SLP_D) __builtin_amdgcn_s_sleep(SLP_D);
      asm volatile("" ::: "memory");
    }
    v4u xo;
    {
      const auto x = __builtin_amdgcn_permlane16_swap(pk2s(ac[0][0], ac[0][1]), pk2s(ac[1][0], ac[1][1]), false, false);
      const auto y = __builtin_amdgcn_permlane16_swap(pk2s(ac[0][2], ac[0][3]), pk2s(ac[1][2], ac[1][3]), false, false);
      const v4u a = {x[0], y[0], x[1], y[1]};
#pragma unroll
      for (int e = 0; e < 4; ++e) {
        xo[e] = pk2s(lo16(xr[e]) + lo16(a[e]), hi16(xr[e]) + hi16(a[e]));
        if (kv) xo[e] = xo[e] ? xo[e] : 0x8000u;
      }
      ST128(xo, rsY, yo, s * SEG * DM * 2);
    }
    TS(1, s, w * 64);
    if (kv) {
      const unsigned p0 = __builtin_amdgcn_readlane(xo[0], 0), p1 = __builtin_amdgcn_readlane(xo[0], 32);
      const float pv = lo16(g < 2 ? p0 : p1);
      float sm = 0.f, q0 = 0.f, q1 = 0.f;
#pragma unroll
      for (int e = 0; e < 4; ++e) {
        const float a = one(lo16(xo[e]) - pv), b = one(hi16(xo[e]) - pv);
        sm = one(sm + one(a + b));
        q0 = one(q0 + a * a);
        q1 = one(q1 + b * b);
      }
      sm = half_sum(sm);
      const float q = fmaxf(one(half_sum(q0 + q1)) - sm * sm * (1.0f / 256), 0.f);
      const u64 st = (u64)__float_as_uint(sm + pv * 256.f) | (u64)(__float_as_uint(q) | 1u) << 32;
      if ((l & 31) < 4) __hip_atomic_store((gu64*)(FL + FL_FR + ((s * 4 + (l & 3)) * 4 + (r >> 3)) * 32 + (r & 7) * 4 + 2 * u), st, RLX_AGENT);
      if (XZP == 4 && s + 2 * RG < NSEG) X_CLEAR(s + 2 * RG)
    }
    if (EF && s + (1 << RGB) < NSEG) PW_FREE(RL, s + (1 << RGB), 2 * rp + u);
    TS(3, s, w * 64);
  }
  if (L == 1 && idx == 0 && w == 0 && l == 0) __hip_atomic_store((gu64*)(ws + OFF_EP), tag, RLX_AGENT);
}
__device__ __forceinline__ void role_proj(const MegaArgs& A, char* smem, const int L, const int idx, const bool kv) {
  if (A.wv < 4) role_proj_t<false>(A, smem, L, idx, kv);
  else role_proj_t<true>(A, smem, L, idx, kv);
}

constexpr int CUN = 2144;
__device__ __forceinline__ v2u part_stats(const v4u o) {
  float v[8];
#pragma unroll
  for (int e = 0; e < 4; ++e) { v[2 * e] = lo16(o[e]); v[2 * e + 1] = hi16(o[e]); }
  const float s = ((v[0] + v[1]) + (v[2] + v[3])) + ((v[4] + v[5]) + (v[6] + v[7])), m = s * 0.125f;
  float q0 = 0.f, q1 = 0.f;
#pragma unroll
  for (int e = 0; e < 4; ++e) {
    const float a = v[e] - m, b = v[4 + e] - m;
    q0 += a * a;
    q1 += b * b;
  }
  return v2u{__float_as_uint(s), __float_as_uint(q0 + q1) | 1u};
}
#define PS_ST(o, voff, soff)                                                                                              \
  {                                                                                                                       \
    const v2u p_ = part_stats(o);                                                                                         \
    if (PSW) {                                                                                                            \
      const v4u q_ = {p_.x, p_.y, (unsigned)__builtin_amdgcn_update_dpp(0, (int)p_.x, 0xb1, 0xf, 0xf, true),              \
                      (unsigned)__builtin_amdgcn_update_dpp(0, (int)p_.y, 0xb1, 0xf, 0xf, true)};                         \
      if (!(l & 1)) ST128(q_, rps, voff, soff);                                                                           \
    } else {                                                                                                              \
      ST64(p_, rps, voff, soff);                                                                                          \
    }                                                                                                                     \
  }
constexpr int CRN = 176;
constexpr int CTN = 528;
constexpr int CV_LDS = (8 * CKN + 8 * 2 * CUN + 64 + 2 * 32 * 8 + 16 * CRN + 64 + 8 * CTN) * 2 + 8 * 1568;
__device__ __forceinline__ void role_conv(const MegaArgs& A, char* smem, const int cv) {
  u16* Kr = reinterpret_cast<u16*>(smem);
  u16* Uc = Kr + 8 * CKN;
  u16* Us = Uc + 8 * CUN + 63;
  u16* To = Uc + 8 * 2 * CUN + 64;
  u16* Sx = To + 32 * 8;
  u16* Rc = Sx + 32 * 8;
  u16* Rs = Rc + 8 * CRN + 63;
  u16* Ta = Rc + 16 * CRN + 64;
  const int w = A.wv;
  const int l = lane_id(), tid = 64 * w + l, c = l & 15, g = l >> 4;
  const int d = 8 * cv + w;
  char* ws = A.ws;
  unsigned* FL = (unsigned*)(ws + OFF_FL);
  unsigned* TMO = FL + FL_TMO;
  u16* kr = Kr + w * CKN;
  const u16* ka = kr + 2080 - 16 * c + 8 * g;
#define KFRAG(D0) (*reinterpret_cast<const f16x8*>(ka - (D0)))
  typedef __attribute__((address_space(3))) const unsigned* lpu;
  typedef __attribute__((address_space(3))) u16* lph;
#define BFRAG(a) __builtin_bit_cast(f16x8, (v4u{((lpu)(unsigned long)(a))[0], ((lpu)(unsigned long)(a))[1], ((lpu)(unsigned long)(a))[2], ((lpu)(unsigned long)(a))[3]}))
  f32x4 acc[8], al[5];
  v4u tx[4];
  const __amdgpu_buffer_rsrc_t rz1 = MKRS(ws + OFF_XA, SZ_X), rps = MKRS(ws + OFF_H, SZ_X);
  u16* uc = Uc + w * CUN;
  const unsigned ub = lds_base(reinterpret_cast<char*>(((c & 1) ? Us : Uc) + w * CUN + c + 8 * g + 32));
  const int t0 = 16 * c + 4 * g;
  float dsk, ysc;
  unsigned mk = 0u;
  const __amdgpu_buffer_rsrc_t rm = MKRS(FL + FL_FW + (blockIdx.x & 3) * 128, 63 * 2048 + 512);
#define SKIP(T) (*reinterpret_cast<const v2u*>(uc + 32 + 256 * (T) + t0))
#define TILE_OUT(dst, a, uu)                                                                   \
  {                                                                                            \
    v2u v_;                                                                                    \
    _Pragma("unroll") for (int e = 0; e < 2; ++e) {                                            \
      const unsigned yy = pk2(a[2 * e] * ysc, a[2 * e + 1] * ysc), u_ = uu[e];                 \
      const unsigned k_ = pk2(h2f((u16)u_) * dsk, h2f((u16)(u_ >> 16)) * dsk);                 \
      v_[e] = pk2(lo16(yy) + lo16(k_), hi16(yy) + hi16(k_));                                   \
    }                                                                                          \
    *reinterpret_cast<v2u*>((dst) + t0) = v_;                                                  \
  }
#define CHUNK_ST(i, src, N) CHUNK_STX(w + 8 * (i), tx[i], src, N)
#define CHUNK_STX(i, X, src, N)                                                                \
  {                                                                                            \
    const unsigned p_ = lds_base((char*)((src) + (c >> 2) * (N) + (tid & ~15) + 4 * (c & 3))); \
    const v2u a0_ = trd(p_), a1_ = trd(p_ + 8 * (N));                                          \
    const v4u a_ = {a0_.x, a0_.y, a1_.x, a1_.y};                                               \
    v4u o;                                                                                     \
    _Pragma("unroll") for (int e = 0; e < 4; ++e) o[e] = pk2(lo16(X[e]) + lo16(a_[e]), hi16(X[e]) + hi16(a_[e])); \
    ST128(o, rz1, (l >> 5) * 32768 + (l & 31) * 16, (2 * (i) * 64 + cv) * 512);                \
    PS_ST(o, (l >> 5) * 16384 + (l & 31) * 8, (2 * (i) * 64 + cv) * 256)                        \
  }
#define CHUNK_FLAG(i) set_flag(FL + FL_FC + ((8 * (i) + l) * 8 + (cv >> 3)) * 32 + (cv & 7))
  {
#define U_VAL(i)                                                                               \
  {                                                                                            \
    const float mb_ = lo16(st[i]), ri_ = 1024.0f / hi16(st[i]);                                 \
    _Pragma("unroll") for (int e = 0; e < 4; ++e) {                                            \
      const unsigned t_ = pk2(lo16(tx[i][e]) - mb_, hi16(tx[i][e]) - mb_);                     \
      const unsigned q_ = pk2(lo16(t_) * ri_, hi16(t_) * ri_);                                 \
      uv[e] = pkh(lo16(q_), hi16(q_));                                                         \
    }                                                                                          \
  }
    unsigned ta[3], k0p, st[4];
    v4u uv;
    v2u kn;
    u64 tag;
    const __amdgpu_buffer_rsrc_t rst = MKRS(ws + OFF_GT, (L_SEQ + 512) * 16);
    {
      KGen G;
      G.load(A, d, l);
      const u16 pk = A.Dskip[d];
      asm volatile("" ::: "memory");
      if (XL == 1) ask_slow(A, 16 * (w - 4) + (cv & 15), ta);
      if (XL) asm volatile("" : "+v"(G.pa), "+v"(G.pb), "+v"(G.pc), "+v"(G.pd), "+v"(G.pe)::"memory");
      tx[0] = *reinterpret_cast<const v4u*>(A.x0 + (size_t)tid * DM + 8 * cv);
      if (XL != 1) ask_slow(A, 16 * (w - 4) + (cv & 15), ta);
      tag = call_tag(A);
      G.setup();
      dsk = bf2f(pk) * (1.0f / 1024);
      G.st = smem + w * 16640;
      SST(0);
      ysc = G.scale();
      const Cx z = G.power(1.f);
      Cx y = cmul(G.C, z);
      G.rows(G.st + l * 8, y, z, 16);
      G.get(G.bh, G.bl, l);
      const float k0 = 2.0f * wave_sum(G.C.r.x + G.C.r.y);
      k0p = l == 0 ? pkh(k0, 0.f) : 0u;
      if (TP) {
        constexpr int R = 64 * TP, U0 = 2 * R + 32, U1 = 3 * R + 111, KN = 4 * R + 192;
        u16* k1 = reinterpret_cast<u16*>(smem + 8 * 16640) + w * KN;
        u16* t1 = reinterpret_cast<u16*>(smem + 8 * 16640) + 8 * KN;
        v4u s1 = {};
        u64 rv = 0;
        if (w < TP) {
          s1 = LD128(rst, tid * 16, (L_SEQ + R * (cv >> 4)) * 16);
          rv = ready_look(A);
        }
        if (l < R / 8 + 4) reinterpret_cast<v4u*>(k1)[l < R / 8 ? l + R / 8 + 4 : l - R / 8] = v4u{k0p, 0u, 0u, 0u};
        k1[U0 + (l < 32 ? l : R + l)] = k1[U1 + (l < 32 ? l : R + l)] = 0;
        G.columns(l, 0, R / 16);
        const f32x4 kt = G.tile<true>(l);
        if (c < R / 16) *reinterpret_cast<v2u*>(k1 + R + 28 - 16 * c - 4 * g) = v2u{pkh(kt.w, kt.z), pkh(kt.y, kt.x)};
        if (w < TP) {
          for (unsigned spins = 0; !__all(has_tag(tag, s1.x, s1.y) && has_tag(tag, s1.z, s1.w)) && spins < SPIN_CAP; ++spins) {
            __builtin_amdgcn_s_sleep(4);
            s1 = LD128(rst, tid * 16, (L_SEQ + R * (cv >> 4)) * 16);
          }
          st[0] = __builtin_amdgcn_perm(s1.w, s1.y, 0x07060302);
          U_VAL(0);
          u16* up = reinterpret_cast<u16*>(smem + 8 * 16640) + 32 + tid;
#pragma unroll
          for (int e = 0; e < 8; ++e) up[e * KN + U0] = up[e * KN + U1] = (u16)(uv[e >> 1] >> (16 * (e & 1)));
        }
        RAW_BAR();
        const unsigned u1 = lds_base(reinterpret_cast<char*>(k1 + ((c & 1) ? U1 : U0) + c + 8 * g));
        f32x4 a = {};
#pragma unroll
        for (int b = 0; b <= R / 32; ++b)
          a = MFMAH(BFRAG(u1 + 64 * b), *reinterpret_cast<const f16x8*>(k1 + R - 16 * (c & (R / 16 - 1)) + 8 * g + 32 * b), a);
        if (c < R / 16) {
          const v2u sk = *reinterpret_cast<const v2u*>(k1 + U0 + 32 + t0);
          TILE_OUT(t1 + w * (R + 16), a, sk);
        }
        RAW_BAR();
        SST(1);
        if (w < TP) CHUNK_ST(0, t1, R + 16);
        G.columns(l, R / 16, TPS);
        if (w < TP) {
          DRAIN();
          ready_wait(A, tag, rv);
          if (l < 8) CHUNK_FLAG(w);
        }
        SST(6);
        G.columns(l, TPS, 16);
      } else {
        G.columns(l, 0, 16);
      }
      const f32x4 kt = G.tile<true>(l);
      kn = v2u{pkh(kt.w, kt.z), pkh(kt.y, kt.x)};
    }
#define U_PUT(i)                                                                               \
  _Pragma("unroll") for (int e = 0; e < 4; ++e) {                                              \
    ((lph)(unsigned long)up0)[(2 * e) * CUN + 512 * (i)] = (u16)uv[e];                         \
    ((lph)(unsigned long)up0)[(2 * e + 1) * CUN + 512 * (i)] = (u16)(uv[e] >> 16);             \
    ((lph)(unsigned long)up1)[(2 * e) * CUN + 512 * (i)] = (u16)uv[e];                         \
    ((lph)(unsigned long)up1)[(2 * e + 1) * CUN + 512 * (i)] = (u16)(uv[e] >> 16);             \
  }
#define U_ROW(i) { U_VAL(i) U_PUT(i) }
    unsigned up0 = lds_base((char*)(Uc + 32 + tid)), up1 = lds_base((char*)(Us + 32 + tid));
    asm volatile("" : "+v"(up0), "+v"(up1), "+v"(kn), "+v"(k0p));
#define ST_GET(I0, I1)                                                                                                        \
  for (unsigned spins = 0; spins < SPIN_CAP; ++spins) {                                                                       \
    bool ok = true;                                                                                                           \
    v4u t_[4];                                                                                                                \
    _Pragma("unroll") for (int i = I0; i < I1; ++i) t_[i] = LD128(rst, (tid + 512 * i) * 16, 0);                              \
    _Pragma("unroll") for (int i = I0; i < I1; ++i) {                                                                         \
      ok = ok && has_tag(tag, t_[i].x, t_[i].y) && has_tag(tag, t_[i].z, t_[i].w);                                            \
      st[i] = __builtin_amdgcn_perm(t_[i].w, t_[i].y, 0x07060302);                                                            \
    }                                                                                                                         \
    if (__all(ok)) break;                                                                                                     \
    __builtin_amdgcn_s_sleep(4);                                                                                              \
    asm volatile("" ::: "memory");                                                                                            \
  }
    if (w < 4) {
      ST_GET(0, 2);
      U_VAL(0);
      asm volatile("" : "+v"(uv));
    }
#pragma unroll
    for (int i = 1; i < 4; ++i) tx[i] = *reinterpret_cast<const v4u*>(A.x0 + (size_t)(tid + 512 * i) * DM + 8 * cv);
    RAW_BAR();
    SST(2);
    if (w < 4) { U_PUT(0) }
    *reinterpret_cast<v2u*>(kr + 2076 - 16 * c - 4 * g) = kn;
    if (l < 38) reinterpret_cast<v4u*>(kr)[l < 32 ? 260 + l : l < 34 ? 194 + l : l - 34] = v4u{k0p, 0u, 0u, 0u};
    (l < 32 ? uc : Us + w * CUN - 32)[l] = 0;
    uc[32 + L_SEQ + l] = Us[w * CUN + 32 + L_SEQ + l] = 0;
    uc[32 + 256 + l] = Us[w * CUN + 32 + 256 + l] = 0;
    RAW_BAR();
    SST(11);
    {
      f32x4 a0 = {}, a1 = {};
      f16x8 bf[9], af[9];
#pragma unroll
      for (int b = 0; b < 9; ++b) {
        af[b] = KFRAG(32 - 32 * b);
        bf[b] = BFRAG(ub + 64 * (b - 1));
      }
      const v2u k0 = SKIP(0);
      __builtin_amdgcn_sched_barrier(0);
#pragma unroll
      for (int b = 0; b < 9; ++b) (b & 1 ? a1 : a0) = MFMAH(bf[b], af[b], (b & 1 ? a1 : a0));
      a0 += a1;
      TILE_OUT(Ta + w * CTN, a0, k0);
    }
    const u64 rv = ready_look(A);
    RAW_BAR();
    SST(3);
    if (w < 4) {
      if (w >= TP) CHUNK_ST(0, Ta, CTN);
      asm volatile("" : "+v"(tx[1]));
      U_ROW(1);
      DRAIN();
      ready_wait(A, tag, rv);
      if (l < 8 && w >= TP) CHUNK_FLAG(w);
      ST_GET(2, 4);
    } else {
      ST_GET(0, 4);
      ready_wait(A, tag, rv);
      asm volatile("" : "+v"(tx[0]), "+v"(tx[1]));
      U_ROW(0);
      U_ROW(1);
    }
    ASKED(ta);
    if (tid < (16 * CRN + 64) / 8) reinterpret_cast<v4u*>(Rc)[tid] = v4u{0u, 0u, 0u, 0u};
    SST(4);
    const gu64* fk = (gu64*)(ws + OFF_KG) + cv * 16 + w;
    u64 kg = __hip_atomic_load(fk, RLX_AGENT);
    U_ROW(2);
    for (unsigned spins = 0; !__all(kg == tag) && spins < SPIN_CAP; ++spins) {
      __builtin_amdgcn_s_sleep(8);
      kg = __hip_atomic_load(fk, RLX_AGENT);
    }
    asm volatile("" : "+v"(tx[3]), "+v"(st[3])::"memory");
    SST(5);
    const unsigned* kd = reinterpret_cast<const unsigned*>(Ta + 8 * CTN) + w * 392;
    glds16c(ws + OFF_KF + d * 2048, l * 16, lds_base((char*)kd));
    if (l < 34) glds16c(ws + OFF_KF + d * 2048 + 1024, l * 16, lds_base((char*)kd) + 1024);
    U_ROW(3);
    DRAIN();
    {
#define MIX(lo, hi) __builtin_amdgcn_perm(hi, lo, 0x07060100)
      const unsigned a0 = kd[384 - l], b0 = kd[383 - l], a1 = kd[320 - l], b1 = kd[319 - l];
      const unsigned a2 = kd[256 - 2 * l], m2 = kd[255 - 2 * l], b2 = kd[254 - 2 * l];
      const unsigned* k3 = kd + 124 - 4 * (l & 31);
      v4u* ko = reinterpret_cast<v4u*>(kr) + l;
      ko[4] = v4u{a0, a0, (l & 1) ? b0 : MIX(a0, b0), b0};
      ko[68] = v4u{a1, a1, (l & 1) ? b1 : MIX(a1, b1), b1};
      ko[132] = v4u{a2, MIX(a2, m2), m2, b2};
      if (l < 32) ko[196] = v4u{k3[4], MIX(k3[3], k3[2]), k3[2], MIX(k3[1], k3[0])};
#undef MIX
    }
    RAW_BAR();
    {
      const f16x8 bm = BFRAG(ub - 64);
      f32x4 a1 = MFMAH(bm, KFRAG(256 + 32), (f32x4{})), a2 = MFMAH(bm, KFRAG(512 + 32), (f32x4{}));
#pragma unroll 2
      for (int r = 0; r < 8; ++r) {
        f16x8 a[3], bf[3];
#pragma unroll
        for (int e = 0; e < 3; ++e) {
          a[e] = KFRAG(256 * e - 32 * r);
          bf[e] = BFRAG(ub + 512 * e + 64 * r);
        }
#pragma unroll
        for (int e = 0; e < 3; ++e) {
          a2 = MFMAH(bf[2 - e], a[e], a2);
          if (e < 2) a1 = MFMAH(bf[1 - e], a[e], a1);
        }
      }
      SST(8);
      const v2u k1 = SKIP(1), k2 = SKIP(2);
      TILE_OUT(Ta + w * CTN + 256, a1, k1);
      TILE_OUT(Ta + w * CTN, a2, k2);
    }
    RAW_BAR();
    SST(10);
    if (w < 4) {
      tx[0] = tx[1];
      tx[1] = tx[2];
      tx[2] = tx[3];
    }
    CHUNK_STX(w < 4 ? w + 8 : w, tx[0], Ta, CTN);
    {
      const f16x8 bm = BFRAG(ub - 64);
#pragma unroll
      for (int T = 3; T < 8; ++T) al[T - 3] = MFMAH(bm, KFRAG(256 * T + 32), (f32x4{}));
    }
    SST(7);
    if (RSK && w >= 4) __builtin_amdgcn_s_sleep(RSK);
#undef U_ROW
#undef U_VAL
#undef U_PUT
#undef ST_GET
  }
  const unsigned rb = lds_base(reinterpret_cast<char*>(((c & 1) ? Rs : Rc) + w * CRN + c + 8 * g));
#pragma unroll
  for (int j = 0; j < 8; ++j) acc[j] = f32x4{};
  const __amdgpu_buffer_rsrc_t rx = MKRS(ws + OFF_X2, SZ_X), rz = MKRS(ws + OFF_X3, SZ_X);
  const __amdgpu_buffer_rsrc_t rf = MKRS(FL + FL_FR, 64 * 16 * 128);
  v2u st = {0u, 0u}, xx = {0u, 0u};
#define X2_LOAD(ss, far)                                                                                                      \
  {                                                                                                                      \
    xx = LD64(rx, (l >> 1) * (DM * 2) + (l & 1) * 8, (far) ? (int)SZ_X : ((ss) * SEG * DM + 8 * cv) * 2);                 \
    st = LD64(rf, l * 8, ((ss) * 4 + (blockIdx.x & 3)) * 512);                                                           \
  }
  unsigned tl = now10();
  static_assert(R0 >= 2 && PP > 8 - R0, "rounds");
  if (CVP && w == 1) __builtin_amdgcn_s_setprio(CVP);
  for (int s = -R0; s < NSEG; ++s) {
    const int q = s & 3, q8 = s & 7;
    const u16* kq = ka + 32 * q8;
    if (w == 1 && s >= 0) {
      gate(tl);
      X2_LOAD(s, 0);
    }
    asm volatile("" ::: "memory");
    if (s < 8 - R0) {
      const int r = s + R0;
      const u16* kl = ka + 32 * r - 256 * 7;
      unsigned uq[4] = {ub + 64 * r, ub + 64 * r + 1024, ub + 64 * r + 2048, ub + 64 * r + 3072};
      asm volatile("" : "+v"(uq[0]), "+v"(uq[1]), "+v"(uq[2]), "+v"(uq[3]));
      f16x8 bf[8], a[8];
#pragma unroll
      for (int e = 7; e >= 0; --e) {
        a[e] = *reinterpret_cast<const f16x8*>(kl + 256 * (7 - e));
        bf[7 - e] = BFRAG(uq[(7 - e) >> 1] + 512 * ((7 - e) & 1));
      }
      __builtin_amdgcn_sched_barrier(0);
#pragma unroll
      for (int e = 7; e >= 0; --e)
#pragma unroll
        for (int T = (e > 3 ? e : 3); T < 8; ++T) al[T - 3] = MFMAH(bf[T - e], a[e], al[T - 3]);
      if (r == 1) {
        DRAIN();
        if (l < 8) CHUNK_FLAG(w < 4 ? w + 8 : w);
      }
      if (s == -1) SST(9);
      if (s == -1 && w == 1) {
        (void)wait_n<SLP_V, 32>(FL + FL_FZ + (blockIdx.x & 7) * 64, 64, TMO);
#pragma unroll 1
        for (unsigned spins = 0; spins < SPIN_CAP; ++spins) {
          mk = MARKS(0);
          if (__all(mk != 0u)) break;
          __builtin_amdgcn_s_sleep(16);
          asm volatile("" ::: "memory");
        }
        tl = latest(mk, tl) + CE + C0D;
      }
      if (s < 0) continue;
    }
    f16x8 a0 = *reinterpret_cast<const f16x8*>(kq - 32), a1 = *reinterpret_cast<const f16x8*>(kq);
    asm volatile("" : "+v"(a0), "+v"(a1));
    if (w == 1) {
#pragma unroll 1
      for (unsigned spins = 0; !__all(umin3(xx.x, xx.y, st.y) != 0u); ++spins) {
        if (spin_timeout(spins, TMO)) break;
        __builtin_amdgcn_s_sleep(SLP_V);
        X2_LOAD(s, __builtin_popcountll(__ballot(st.y != 0u)) < CVT2);
      }
      TS(0, s, 64);
      const float s0 = __uint_as_float(st.x), s1 = dppr<0xb1>(s0), m2 = __uint_as_float(st.y);
      const float var = (m2 + dppr<0xb1>(m2) + (s0 - s1) * (s0 - s1) * (1.0f / DM)) * (1.0f / DM);
      const float mb = bfr1((s0 + s1) * (1.0f / DM)), ri = 1024.0f * __builtin_amdgcn_rcpf(bfr1(__builtin_amdgcn_sqrtf(bfr1(bfr1(var) + 1e-5f))));
      const unsigned t0 = pk2(lo16(xx.x) - mb, hi16(xx.x) - mb), t1 = pk2(lo16(xx.y) - mb, hi16(xx.y) - mb);
      const unsigned b0 = pk2(lo16(t0) * ri, hi16(t0) * ri), b1 = pk2(lo16(t1) * ri, hi16(t1) * ri);
      const unsigned u0 = pkh(lo16(b0), hi16(b0)), u1 = pkh(lo16(b1), hi16(b1));
      u16* rp = Rc + (l & 1) * 4 * CRN + 32 * q + (l >> 1);
#define R_PUT(o) { rp[o] = (u16)u0; rp[(o) + CRN] = (u16)(u0 >> 16); rp[(o) + 2 * CRN] = (u16)u1; rp[(o) + 3 * CRN] = (u16)(u1 >> 16); }
      R_PUT(0) R_PUT(8 * CRN + 63)
      if (q == 0) { R_PUT(128) R_PUT(8 * CRN + 63 + 128) }
#undef R_PUT
      *reinterpret_cast<v2u*>(Sx + l * 4) = xx;
    }
    unsigned ap = rb + 64 * ((q + 3) & 3), ac = rb + 64 * q, au = lds_base((char*)(Rc + w * CRN + 32 * q + c));
    asm volatile("" : "+v"(ap), "+v"(ac), "+v"(au));
    RAW_BAR();
    TS(1, s, 0);
    const f16x8 bp = BFRAG(ap), bc = BFRAG(ac);
    const u16 h0 = ((lph)(unsigned long)au)[0], h1 = ((lph)(unsigned long)au)[16], y0 = Sx[c * 8 + w], y1 = Sx[(16 + c) * 8 + w];
    __builtin_amdgcn_sched_barrier(0);
    const float u0 = h2f(h0), u1 = h2f(h1), x0 = bf2f(y0), x1 = bf2f(y1);
    acc[0] = MFMAH(a0, bp, (q8 == 0 ? acc[1] : acc[0]));
    const f32x4 tmp = MFMAH(a1, bc, acc[0]);
    const unsigned uu = pk2(u0 * dsk, u1 * dsk);
    const unsigned yy = pk2(((q & 1) ? tmp[2] : tmp[0]) * ysc, ((q & 1) ? tmp[3] : tmp[1]) * ysc);
    const unsigned aa = pk2(lo16(yy) + lo16(uu), hi16(yy) + hi16(uu)), oo = pk2(x0 + lo16(aa), x1 + hi16(aa));
    if (g == (q8 >> 1)) {
      To[c * 8 + w] = (u16)oo;
      To[(16 + c) * 8 + w] = (u16)(oo >> 16);
    }
    RAW_BAR();
    if (w == 0 && l < 32) {
      v4u o = *reinterpret_cast<const v4u*>(To + l * 8);
      o.y = o.y ? o.y : 0x8000u;
      ST128A(o, rz, l * 16, (s * 64 + cv) * 512, XAUX);
      PS_ST(o, l * 8, ((64 + s) * 64 + cv) * 256)
    }
    if (w == 1) {
      tl = now10() + GATE_V - 40;
      mk = MARKS(s + 1);
    }
    const int left = 7 - (s >> 3);
#define PUSH(J0, SH)                                                                                              \
  if (left >= (J0)) {                                                                                             \
    _Pragma("unroll") for (int j = (J0); j < (J0) + 2 && j < 8; ++j)                                              \
      acc[j] = MFMAH(*reinterpret_cast<const f16x8*>(kq - 32 - 256 * j), bp, (j + (SH) < 8 ? acc[(j + (SH)) & 7] : f32x4{})); \
  }
    TS(2, s, 0);
    if (w == 0) {
      DRAIN();
      if (l < 4) set_flag(FL + FL_FC + (((64 + s) * 4 + l) * 8 + (cv >> 3)) * 32 + (cv & 7));
    }
    TS(3, s, 0);
    if (q8 == 0) { PUSH(1, 1) PUSH(3, 1) PUSH(5, 1) PUSH(7, 1) }
    else { PUSH(1, 0) PUSH(3, 0) PUSH(5, 0) PUSH(7, 0) }
#undef PUSH
    if (w == 1 && __all(mk != 0u)) tl = latest(mk, tl) + (s >= NSEG - 1 - TS1 ? CE - TL2 : CE);
    if (s == 8 - R0) {
      v2u k[5];
#pragma unroll
      for (int T = 3; T < 8; ++T) k[T - 3] = SKIP(T);
#pragma unroll
      for (int T = 3; T < 8; ++T) TILE_OUT(uc + 32 + 256 * T, al[T - 3], k[T - 3]);
    }
    {
      const int n = 12 + PW * (s - PP), wi = n + ((w - n) & 7), wf = wi - 8 * (PW < 8);
      if (s > PP && wf < 32 && wf >= n - PW && wf < n) {
        DRAIN();
        if (l < 8) CHUNK_FLAG(wf);
      }
      if (s >= PP && wi < 32 && wi < n + PW) {
        CHUNK_STX(wi, tx[1], Uc + 32 + 64 * (wi & 24), CUN);
        tx[1] = tx[2];
        tx[2] = tx[3];
      }
    }
    MSTAMP();
  }
#undef KFRAG
#undef BFRAG
#undef X2_LOAD
#undef TILE_OUT
#undef SKIP
#undef CHUNK_ST
#undef CHUNK_STX
#undef CHUNK_FLAG
}

__device__ __forceinline__ v4u ln_row_packed(const v4u xr) {
  float v[8];
#pragma unroll
  for (int e = 0; e < 4; ++e) { v[2 * e] = lo16(xr[e]); v[2 * e + 1] = hi16(xr[e]); }
  float s = ((v[0] + v[1]) + (v[2] + v[3])) + ((v[4] + v[5]) + (v[6] + v[7]));
  s = wave_sum(s);
  const float mean = s * (1.0f / DM);
  float q = 0.f;
#pragma unroll
  for (int j = 0; j < 8; ++j) { float t = v[j] - mean; q += t * t; }
  q = wave_sum(q);
  const float var = q * (1.0f / DM);
  const float mb = bfr(mean);
  const float sd = bfr(sqrtf(bfr(bfr(var) + 1e-5f)));
  const float ri = 1.0f / sd;
  v4u o;
#pragma unroll
  for (int e = 0; e < 4; ++e) {
    const unsigned t = pk2(v[2 * e] - mb, v[2 * e + 1] - mb);
    const float x0 = lo16(t), x1 = hi16(t);
    float q0 = x0 * ri, q1 = x1 * ri;
    q0 = __builtin_fmaf(__builtin_fmaf(-q0, sd, x0), ri, q0);
    q1 = __builtin_fmaf(__builtin_fmaf(-q1, sd, x1), ri, q1);
    o[e] = pk2(q0, q1);
  }
  return o;
}

__device__ __forceinline__ void ln4_packed(v4u (&x)[4]) {
  f32x2_t v[16];
#pragma unroll
  for (int e = 0; e < 16; ++e) v[e] = f32x2_t{lo16(x[e >> 2][e & 3]), hi16(x[e >> 2][e & 3])};
  const f32x2_t t = (((v[0] + v[1]) + (v[2] + v[3])) + ((v[4] + v[5]) + (v[6] + v[7]))) +
                    (((v[8] + v[9]) + (v[10] + v[11])) + ((v[12] + v[13]) + (v[14] + v[15])));
  const float mean = red_mod4(t.x + t.y) * (1.0f / DM);
  const float mb = bfr1(mean);
  f32x2_t q0 = {0.f, 0.f}, q1 = {0.f, 0.f};
#pragma unroll
  for (int e = 0; e < 16; e += 2) {
    v[e] -= mb;
    v[e + 1] -= mb;
    q0 += v[e] * v[e];
    q1 += v[e + 1] * v[e + 1];
  }
  q0 += q1;
  const float dm = mean - mb;
  const float var = fmaxf(red_mod4(q0.x + q0.y) * (1.0f / DM) - dm * dm, 0.f);
  const float sd = bfr1(sqrtf(bfr1(bfr1(var) + 1e-5f)));
  const float ri = 1.0f / sd;
#pragma unroll
  for (int e = 0; e < 16; ++e) {
    const unsigned t2 = pk2(v[e].x, v[e].y);
    const f32x2_t q = f32x2_t{lo16(t2), hi16(t2)} * ri;
    x[e >> 2][e & 3] = pk2(q.x, q.y);
  }
}

constexpr int MEGA_WGS = 256;
constexpr size_t MEGA_LDS = (size_t)(32 * 4 * 64 * 8 + 8 * SMS + 15 * QS + 128 + 16 * 64 * 8 + 8 * SOS + 512) * 2;
static_assert(MEGA_LDS <= 163840 && MEGA_LDS - 480 >= C_SQ + 4096, "lds");
static_assert(MEGA_LDS >= (size_t)AW_CNT + 128 && MEGA_LDS >= (size_t)P_CN + 512, "lds");
static_assert(MEGA_LDS >= (size_t)CV_LDS, "lds");
static_assert(MEGA_LDS >= (size_t)2 * 32 * SMS * 2 && MEGA_LDS >= 131072, "lds");
static_assert(MEGA_LDS >= (size_t)8 * (16640 + 2 * (704 + 144)), "lds");
__device__ __forceinline__ MegaArgs role_args(const int b, const int wv, const unsigned k0, const unsigned k1) {
  auto p = (const __attribute__((address_space(4))) u64*)__builtin_amdgcn_kernarg_segment_ptr();
  asm volatile("s_waitcnt lgkmcnt(0)" : "+s"(p) : "s"(k0), "s"(k1));
#define KA(i) ((u16*)(__attribute__((address_space(1))) u16*)p[i])
  return MegaArgs{KA(0), KA(1), (char*)KA(2), KA(3), KA(4), KA(5), KA(6), KA(7), KA(8), KA(9), KA(10), KA(11), KA(12), KA(13), b, wv};
#undef KA
}
__global__ __launch_bounds__(512, 2) void mega_kernel(const u16* mem0, const u16* Dskip, char* ws, u16* out, const u16* x0,
                                                      const u16* logAre, const u16* Aim, const u16* Cre, const u16* Cim,
                                                      const u16* logdt, const u16* Wq, const u16* Wk, const u16* Wv,
                                                      const u16* Wo) {
  extern __shared__ __attribute__((aligned(16))) char smem[];
  unsigned k0 = 0u, k1 = 0u;
  if (IPF == 3) {
    unsigned o;
    asm volatile(
        "s_load_dword %0, %3, 0\n\ts_load_dword %1, %3, 64\n\ts_getpc_b64 s[6:7]\n.LKG_PC:\n\ts_bfe_u32 s4, %4, 0x30003\n\t"
        "v_lshl_add_u32 %2, %5, 3, s4\n\tv_lshlrev_b32 %2, 7, %2\n\ts_mov_b32 m0, %6\n\ts_mov_b32 s4, .LKG_LAST-.LKG_PC\n\t"
        "s_max_i32 s4, s4, .LKG_LAS2-.LKG_PC\n\tv_cmp_gt_i32 vcc, s4, %2\n\t"
        "s_and_saveexec_b64 s[4:5], vcc\n\tglobal_load_lds_dword %2, s[6:7]\n\ts_mov_b64 exec, s[4:5]"
        : "=&s"(k0), "=&s"(k1), "=&v"(o)
        : "s"(__builtin_amdgcn_kernarg_segment_ptr()), "s"(blockIdx.x), "v"(threadIdx.x), "s"(lds_base(smem) + (unsigned)MEGA_LDS - 480)
        : "memory", "vcc", "scc", "s4", "s5", "s6", "s7");
  }
  const int b = vbid(), wv = __builtin_amdgcn_readfirstlane(threadIdx.x >> 6);
#define A role_args(b, wv, k0, k1)
  u64 tag = 0;
  if (__builtin_expect((0xde000ff0u >> (b >> 3)) & 1u, 0)) {
    const unsigned t0 = now10();
    if ((b >= 32 && b < 64) || (b >= 200 && b < 232)) start_kgen(A, smem, b < 64 ? b - 32 : b - 168);
    else if (b >= 64 && b < 96) tag = start_stats<4>(A, b - 64);
    else if (b >= 240) (void)start_stats<8>(A, b - 224);
    const unsigned dl = (b >= 32 && b < 64) ? DL_C : (b >= 64 && b < 80) ? DL_A : (b >= 80 && b < 96) ? DL_A2 : b >= 240 ? DL_R2 : ((b >= 96 && b < 104) || b >= 232) ? DL_R : (b >= 200 && b < 232) ? DL_Q : 0u;
    while (now10() - t0 < dl) __builtin_amdgcn_s_sleep(16);
    __builtin_amdgcn_s_waitcnt(0x0f70);
  }
  if ((EXP_OFF & 1) && b >= 64 && b < 80) return;
  if ((EXP_OFF & 8) && b >= 200 && b < 232) return;
  if ((EXP_OFF & 16) && b >= 32 && b < 64) return;
  if ((EXP_OFF & 32) && b >= 80 && b < 96) return;
  if (b < 64) role_chain(A, smem, b >> 5, b & 31, 0);
  else if (b < 96) role_aw(A, smem, (b >> 4) & 1, b & 15, tag);
  else if (b >= 104 && b < 168) role_conv(A, smem, b - 104);
  else {
    const bool kv = b >= 168 && b < 232;
    role_proj(A, smem, b >= (kv ? 200 : 240), kv ? (b - 168) & 31 : b & 15, kv);
  }
#undef A
}

extern "C" void kernel_launch(void* const* d_in, const int* in_sizes, int n_in, void* d_out, int out_size, void* d_ws,
                              size_t ws_size, hipStream_t stream) {
  MegaArgs A;
  A.x0 = (const u16*)d_in[0];
  A.logAre = (const u16*)d_in[1];
  A.Aim = (const u16*)d_in[2];
  A.Cre = (const u16*)d_in[3];
  A.Cim = (const u16*)d_in[4];
  A.logdt = (const u16*)d_in[5];
  A.Dskip = (const u16*)d_in[6];
  A.mem0 = (const u16*)d_in[8];
  A.Wq = (const u16*)d_in[9];
  A.Wk = (const u16*)d_in[10];
  A.Wv = (const u16*)d_in[11];
  A.Wo = (const u16*)d_in[12];
  A.ws = (char*)d_ws;
  A.out = (u16*)d_out;

  (void)hipFuncSetAttribute((const void*)mega_kernel, hipFuncAttributeMaxDynamicSharedMemorySize, (int)MEGA_LDS);
  mega_kernel<<<MEGA_WGS, 512, MEGA_LDS, stream>>>(A.mem0, A.Dskip, A.ws, A.out, A.x0, A.logAre, A.Aim, A.Cre, A.Cim, A.logdt,
                                                   A.Wq, A.Wk, A.Wv, A.Wo);
}
```
